```python
import torch

def _softmax(x, axis=-1):
    return torch.softmax(x, dim=axis)

def _gelu(x):
    return torch.nn.functional.gelu(x, approximate='tanh')

def _silu(x):
    return torch.nn.functional.silu(x)

def _sigmoid(x):
    return torch.sigmoid(x)

def _layernorm(x, axis=-1, eps=1e-05):
    m = x.mean(dim=axis, keepdim=True)
    v = x.var(dim=axis, keepdim=True, unbiased=False)
    return (x - m) / torch.sqrt(v + eps)

def _scatter_add(tgt, idx, src, axis=0):
    return tgt.clone().index_add_(axis, idx, src)

def _segment_sum(src, seg, n):
    out = torch.zeros((n,) + src.shape[1:], dtype=src.dtype)
    return out.index_add_(0, seg, src)

def _pad(x, pw):
    flat = tuple((v for pair in reversed(pw) for v in pair))
    return torch.nn.functional.pad(x, flat)
kernel_name = 'standalone__video_video_swin__vision'

def setup_inputs():

    _w = lambda a: (lambda t: t.to(torch.bfloat16) if t.is_floating_point() else t)(torch.as_tensor(a))
    T = 4
    H = W = 512 // 7 * 7
    C, win = (96, 7)
    return {'x': _w((lambda *s: torch.randn(*s, dtype=torch.bfloat16) * 0.1)(1, T, H, W, C)), 'wqkv_t': _w((lambda *s: torch.randn(*s, dtype=torch.bfloat16) * 0.1)(C, 3 * C)), 'wo_t': _w((lambda *s: torch.randn(*s, dtype=torch.bfloat16) * 0.1)(C, C)), 'wqkv_s': _w((lambda *s: torch.randn(*s, dtype=torch.bfloat16) * 0.1)(C, 3 * C)), 'wo_s': _w((lambda *s: torch.randn(*s, dtype=torch.bfloat16) * 0.05)(C, C))}

def reference(x, wqkv_t, wo_t, wqkv_s, wo_s):
    B, T, H, W, C = x.shape
    win = 7
    scale = C ** (-0.5)
    h = _layernorm(x, axis=-1)
    ht = (lambda x, *d: x.permute(*d))(h, 0, 2, 3, 1, 4).reshape(B * H * W, T, C)
    qkv = ht @ wqkv_t
    q, k, v = (qkv[..., :C].clone(), qkv[..., C:2 * C].clone(), qkv[..., 2 * C:].clone())
    p = _softmax(torch.einsum('btc,bsc->bts', q, k) * scale, axis=-1)
    ot = torch.einsum('bts,bsc->btc', p, v) @ wo_t
    ot = (lambda x, *d: x.permute(*d))(ot.reshape(B, H, W, T, C), 0, 3, 1, 2, 4)
    x = x + ot
    h = _layernorm(x, axis=-1)
    nh, nw = (H // win, W // win)
    hs = h.reshape(B * T, nh, win, nw, win, C)
    hw = (lambda x, *d: x.permute(*d))(hs, 0, 1, 3, 2, 4, 5).reshape(B * T * nh * nw, win * win, C)
    qkv = hw @ wqkv_s
    q, k, v = (qkv[..., :C].clone(), qkv[..., C:2 * C].clone(), qkv[..., 2 * C:].clone())
    p = _softmax(torch.einsum('bqc,bkc->bqk', q, k) * scale, axis=-1)
    os = torch.einsum('bqk,bkc->bqc', p, v) @ wo_s
    os = (lambda x, *d: x.permute(*d))(os.reshape(B * T, nh, nw, win, win, C), 0, 1, 3, 2, 4, 5)
    os = os.reshape(B, T, H, W, C)
    return x + os
```

```cpp
#define CPF 6
#include <hip/hip_runtime.h>
#include <hip/hip_bf16.h>

#define DI __device__ __forceinline__

using u32x2 = __attribute__((ext_vector_type(2))) unsigned;
using u32x4 = __attribute__((ext_vector_type(4))) unsigned;
using u32x8 = __attribute__((ext_vector_type(8))) unsigned;
using u32x16 = __attribute__((ext_vector_type(16))) unsigned;

constexpr int HH = 511, WW = 511, NWX = 73, NWIN = NWX * NWX, GRID = 256;
constexpr int TOKB = 192, ROWB = WW * TOKB;
constexpr unsigned FRAMEB = (unsigned)HH * ROWB, TOTALB = FRAMEB * 4;
constexpr unsigned FS = 9232, L_BUF = 0, L_STG = 4 * FS, REC = 784, NREC = 21, L_EXT = L_STG + 4 * 9216, L_MS = L_EXT + 4 * NREC * REC, L_STA = L_MS + 18432, L_PSC = L_STA + 512,
                   L_STB = L_PSC + 512, L_AUX = L_STB + 512, L_SLW = L_AUX + 1024, L_DUM = L_SLW + 1024, L_FA = L_DUM + 512, LDS_BYTES = 163840;
constexpr unsigned RAWM = 18432, L_GT = L_AUX + 768;
static_assert(L_EXT + 3 * RAWM <= L_MS && ((L_STG >> 10) & 3) == 0 && (L_STG & 1023) < 512 && L_FA + 1024 <= LDS_BYTES && (NWIN + GRID - 1) / GRID <= NREC, "LDS");

#define LP(T, a) ((__attribute__((address_space(3))) T*)(size_t)(unsigned)(a))
#define SB() __builtin_amdgcn_sched_barrier(0)
#define PIN(v) asm volatile("" : "+v"(v))
DI unsigned raw_lane(unsigned lane, unsigned m, unsigned A, unsigned B, unsigned C) {
    const unsigned n = 64 * m + lane, row = (n * 171) >> 11, pc = n - 12 * row;
    return (row >> 3) * (A & 0xffff) + ((row >> 1) & 3) * (B & 0xffff) + (row & 1) * (C & 0xffff) + (pc ^ ((row >> 2) & 3)) * 16 - 1024 * m;
}
DI u32x4 make_srd(unsigned lo, unsigned hi, unsigned bytes) { return u32x4{lo, hi & 0xffffu, bytes, 0x00020000u}; }
DI void raw_dma(unsigned v0, unsigned v1, unsigned v2, const u32x4& srd, unsigned so, unsigned dst) {
    asm volatile("s_mov_b32 m0, %5\n\ts_nop 0\n\tbuffer_load_dwordx4 %0, %3, %4 offen lds\n\tbuffer_load_dwordx4 %1, %3, %4 offen offset:1024 lds\n\tbuffer_load_dwordx4 %2, %3, %4 offen offset:2048 lds"
                 :: "v"(v0), "v"(v1), "v"(v2), "s"(srd), "s"(so), "s"(dst) : "memory");
}

__global__ __launch_bounds__(256, 1) void swin_kernel(const char* __restrict__ x_, const char* __restrict__ wqkv_t, const char* __restrict__ wo_t, const char* __restrict__ wqkv_s,
                                                           const char* __restrict__ wo_s, char* __restrict__ out_, const char* wf) {
    const unsigned k = __builtin_amdgcn_readfirstlane(threadIdx.x >> 6) & 3, lane = threadIdx.x & 63, k1 = k & 1, k2 = k >> 1;
    u32x2 kx, ko, kl, kr, kp;
    const unsigned long long kap = (unsigned long long)__builtin_amdgcn_kernarg_segment_ptr();
    asm volatile("s_load_dwordx2 %0, %4, %5\n\ts_load_dwordx2 %1, %4, %6\n\ts_load_dwordx2 %2, %4, 0x0\n\ts_load_dwordx2 %3, %4, 0x28"
                 : "=&s"(kr), "=&s"(kl), "=&s"(kx), "=&s"(ko) : "s"(kap), "s"(8 + 8 * k), "s"(8 + 16 * k2));
    SB();
    {
        unsigned long long pc; unsigned co = (((blockIdx.x >> 3) * 4 + k) & 7) * 8192 + lane * 128;
        asm volatile("s_getpc_b64 %0\n.Lswin_pc:\n\ts_mov_b32 m0, %2\n\tv_min_u32 %1, .Lswin_fin-.Lswin_pc, %1\n\tglobal_load_lds_dword %1, %0" : "=&s"(pc), "+v"(co) : "s"(L_DUM + 256) : "memory");
    }
    SB();
#define TS(i)
    const unsigned Bl = 1152 + 2304 * k2, Ql = 4608 - 3456 * k2;
    const unsigned Ar = 13824 - 12288 * k1, Br = k1 ? 384 : Bl, Cr = 576 - 384 * k1, Pr = 27648 - 24576 * k1, Qr = k1 ? 6144 : Ql, cr = 192 - 192 * k1, szr = 55296 - 36864 * k1;
    const unsigned ldr = L_STG + k * RAWM + k2 * RAWM, ldl = k1 * RAWM + k2 * (L_MS - k1 * (L_MS - L_EXT + RAWM));
    static_assert(L_STG + 2 * RAWM == L_EXT, "");
    unsigned vr[3];
#pragma unroll
    for (int m = 0; m < 3; ++m) { vr[m] = raw_lane(lane, m, Ar, Br, Cr); PIN(vr[m]); }
    unsigned vW = (((blockIdx.x >> 3) & 31) * 14 + lane) * 128 + (lane >= 14) * 0x40000000u; PIN(vW);
    SB();
    asm volatile("s_waitcnt lgkmcnt(0)" : "+s"(kr), "+s"(kl), "+s"(kx), "+s"(ko));
    const u32x4 srdr = make_srd(kr[0], kr[1], szr);
    asm volatile("s_mov_b32 m0, %2\n\ts_nop 0\n\tbuffer_load_dword %0, %1, 0 offen lds" :: "v"(vW), "s"(srdr), "s"(L_DUM) : "memory");
#define RDMA(n) do { SB(); raw_dma(vr[0], vr[1], vr[2], srdr, cr + (n & 1) * Pr + (n >> 1) * Qr, ldr + n * 3072); SB(); } while (0)
    RDMA(0); RDMA(1);
    TS(0); TS(1);
    const u32x4 srdl = make_srd(kl[0], kl[1], 55296), srdx = make_srd(kx[0], kx[1], TOTALB), srdo = make_srd(ko[0], ko[1], TOTALB);
    const unsigned vb = (blockIdx.x & 7) * (GRID / 8) + (blockIdx.x >> 3);
    const unsigned w0 = (vb * NWIN) / GRID, nwin = ((vb + 1) * NWIN) / GRID - w0, wy0 = w0 / NWX, wx0 = w0 - wy0 * NWX;
    const unsigned base0 = wy0 * 7 * ROWB + wx0 * 7 * TOKB;
    const unsigned i = lane & 15, g = lane >> 4, p = i >> 2, t = i & 3, cl = 384 * k1;
    u32x16 c0, f0; u32x8 c1, sp; u32x4 c2;
    const unsigned stg = L_STG + k * 9216, ext = L_EXT + k * (NREC * REC);
    RDMA(2);
    unsigned vl[3];
#pragma unroll
    for (int m = 0; m < 3; ++m) { vl[m] = raw_lane(lane, m, 13824, Bl, 576); PIN(vl[m]); }
#define LDMA(n) do { SB(); raw_dma(vl[0], vl[1], vl[2], srdl, cl + (n & 1) * 27648 + (n >> 1) * Ql, ldl + n * 3072); SB(); } while (0)
    RDMA(3);
#pragma unroll
    for (int a = 0; a < 2; ++a) {
        const unsigned ra = (8 * p + 4 * a + t) * 192 + ((g ^ ((2 * p + a) & 3)) * 16);
        f0[a] = ldl + ra; f0[2 + a] = ldr + ra; f0[4 + a] = ldr + (8 * g + 4 * a + p) * 192 + ((t ^ ((2 * g + a) & 3)) * 16);
        PIN(f0[a]); PIN(f0[2 + a]); PIN(f0[4 + a]);
    }
    RDMA(4);
#pragma unroll
    for (int a = 0; a < 2; ++a) {
        const unsigned n = 16 * a + 4 * k + p;
        f0[6 + a] = n < nwin ? t * FRAMEB + base0 + n * (7 * TOKB) + (wx0 + n >= NWX ? 7 * ROWB - NWX * 7 * TOKB : 0) + 6 * ROWB + 6 * TOKB + g * 48 : 0x80000000u; PIN(f0[6 + a]);
    }
    f0[8] = i == 0 ? L_AUX + 192 + g * 16 : L_DUM; f0[15] = 0;
    RDMA(5);
    {
        const unsigned sh = 2 - 2 * k2, ml = 64 - 48 * k2 - 6 * 3072, a0 = ldl + g * 256 + (k == 1 ? t * 16 : k1 ? 0 : i * 16);
#pragma unroll
        for (int n = 0; n < 6; ++n) {
            const unsigned z = (32 * (n >> 1) + 4 * (n & 1) + 8 * p + t) >> sh;
            f0[9 + n] = a0 + (z * 3072 + ((z * 43) >> 8) * ml) * k1 + n * 3072 * (1 - k1); PIN(f0[9 + n]);
            if (n == 2) LDMA(0);
        }
    }
    LDMA(1);
#pragma unroll
    for (int m = 0; m < 3; ++m) {
        const unsigned n = 64 * m + lane, tf = (n * 171) >> 13, r = n - 48 * tf, q = (r * 43) >> 9;
        const unsigned c = tf * FRAMEB + ((r - 12 * q) ^ tf) * 16;
#pragma unroll
        for (int u = 0; u < 3; ++u) {
            const unsigned px = 16 * u + 4 * k + q;
            c0[3 * u + m] = c + px * TOKB + ((px * 37) >> 8) * (ROWB - 7 * TOKB) - 1024 * ((3 * u + m) % 4); PIN(c0[3 * u + m]);
        }
        if (m == 1) LDMA(2);
    }
#pragma unroll
    for (int S = 0; S < 3; ++S) c0[9 + S] = stg + t * 768 + p * TOKB + ((3 * g + S) ^ t) * 16;
    c0[12] = L_BUF + t * FS + g * 256 + p * 64 + k * 16;
    c0[13] = L_BUF + k * FS + lane * 16;
    const unsigned vs = g * TOKB + 12 * i, brk = ROWB - 7 * TOKB, u = (p - g) & 3;
    c0[14] = vs; c0[15] = vs + (g >= 3 ? brk : 0);
    PIN(c0);
    LDMA(3);
    c1[0] = vs + (g >= 2 ? brk : 0); c1[1] = vs + (g >= 1 ? brk : 0);
    c1[2] = u < 3 ? L_STA + t * 128 + g * 32 + u * 8 + k * 2 : L_DUM;
    c1[3] = L_STB + t * 128 + (4 * g + k) * 8 + u * 2;
    c1[4] = L_STA + k * 128 + g * 32;
    c1[5] = L_STB + k * 128 + i * 8;
    c1[6] = L_STA + k * 128 + p * 32 + g * 8 + t * 2;
    c1[7] = ext - REC + (i & 1) * 192 + g * 16;
    c2[0] = ext - REC + 384 + 24 * i;
    c2[1] = p == g ? ~0u : 0u;
    c2[2] = L_AUX + (t < 2 ? t : 2) * 192 + g * 16;
    c2[3] = p == g ? L_AUX + 576 + t * 16 : L_AUX + 384;
    PIN(c1); PIN(c2);
    LDMA(4);
    sp[0] = lane * 16; sp[1] = L_MS + (16 * t + 4 * g + p) * 16;
#pragma unroll
    for (int T = 0; T < 2; ++T) {
        const unsigned n = 16 * T + 4 * t + p;
        sp[2 + T] = n < NREC ? ext + n * REC + g * 16 : L_DUM; sp[4 + T] = n < NREC && p == g ? ext + n * REC + 768 : L_DUM;
    }
    sp[6] = ext + g * REC + 384 + 24 * i; sp[7] = g == 0 ? ext + 20 * REC + 384 + 24 * i : L_DUM;
    PIN(sp);
    LDMA(5);
    {
        const unsigned e = 64 * k + lane, y = e - 144, one = (y < 16 ? (y >> 2) & 1 : y >> 5) ? 0x3f800000u : 0x3f80u;
        *LP(unsigned, L_AUX + e * 4) = e < 48 ? 0x3f803f80u : e == 192 ? 0x46800000u : y < 48 && t == (y < 16 ? y >> 3 : (y >> 2) & 3) ? one : 0u;
        *LP(unsigned, L_STA + k * 128 + (lane & 6) * 16 + 24 + (lane & 1) * 4) = 0x3c003c00u;
        *LP(unsigned, L_FA + e * 4) = 0u;
    }
    const u32x8 si = {(unsigned)nwin, base0, (unsigned)wx0, k * FRAMEB, stg, ldl, 0u, 0u};
    asm volatile(
        "s_mov_b32 s78, 0xf0000f\n"
        "s_mov_b32 s79, 0xf0000f00\n"
        "s_mov_b32 s56, 0x22222222\n"
        "s_mov_b32 s57, 0x22222222\n"
        "s_mov_b32 s58, 0x44444444\n"
        "s_mov_b32 s59, 0x44444444\n"
        "s_mov_b32 s60, 0x88888888\n"
        "s_mov_b32 s61, 0x88888888\n"
        "s_mov_b32 s62, 0xffff\n"
        "s_mov_b32 s63, 0x0\n"
        "s_mov_b32 s64, 0xffff0000\n"
        "s_mov_b32 s65, 0x0\n"
        "s_mov_b32 s66, 0x0\n"
        "s_mov_b32 s67, 0xffff\n"
        "s_mov_b32 s68, 0x0\n"
        "s_mov_b32 s69, 0xffff0000\n"
        "s_mov_b32 s70, 0x0\n"
        "s_mov_b32 s71, 0xffffffff\n"
        "s_mov_b32 s72, 0x0\n"
        "s_mov_b32 s73, 0x20000\n"
        "s_mov_b32 s76, 0x3c003c00\n"
        "s_mov_b32 s77, 0x3c003c00\n"
        "s_mov_b64 vcc, s[78:79]\n"
        "s_mov_b32 s80, 0x3c2aaaab\n"
        "s_mov_b32 s81, 0x3727c5ac\n"
        "s_mov_b32 s82, 0x43800000\n"
        "s_mov_b32 s84, 0x80000000\n"
        "s_sub_u32 s85, s48, 1\n"
        "s_mov_b32 s86, 0x80000000\n"
        "s_mov_b32 s89, 0x80000000\n"
        "s_mov_b32 s29, 1\n"
        "s_mov_b32 s31, 0x400000\n"
        "s_mov_b32 s87, s49\n"
        "s_mov_b32 s88, s50\n"
        "s_mov_b32 s54, 0x05040302\n"
        "s_mov_b32 s55, 0x07060302\n"
        "s_mov_b64 s[44:45], s[40:41]\n"
        "s_mov_b32 s46, 0\n"
        "s_mov_b32 s47, s43\n"
        "s_bitcmp1_b32 s52, 10\n"
        "s_cbranch_scc1 FV_%=\n"
        "s_mov_b32 s93, 0x3e16c740\n"
        "v_mov_b32 v44, 0x3f803f80\n"
        "v_mov_b32 v45, 0x3f803f80\n"
        "v_mov_b32 v46, 0x3f803f80\n"
        "v_mov_b32 v47, 0x3f803f80\n"
        "s_waitcnt vmcnt(18)\n"
        "ds_read_b64_tr_b16 a[72:73], v32 offset:0\n"
        "ds_read_b64_tr_b16 a[74:75], v33 offset:0\n"
        "ds_read_b64_tr_b16 a[76:77], v32 offset:6144\n"
        "ds_read_b64_tr_b16 a[78:79], v33 offset:6144\n"
        "ds_read_b64_tr_b16 a[80:81], v32 offset:12288\n"
        "ds_read_b64_tr_b16 a[82:83], v33 offset:12288\n"
        "ds_read_b64_tr_b16 a[84:85], v32 offset:8\n"
        "ds_read_b64_tr_b16 a[86:87], v33 offset:8\n"
        "ds_read_b64_tr_b16 a[88:89], v32 offset:6152\n"
        "ds_read_b64_tr_b16 a[90:91], v33 offset:6152\n"
        "ds_read_b64_tr_b16 a[92:93], v32 offset:12296\n"
        "ds_read_b64_tr_b16 a[94:95], v33 offset:12296\n"
        "ds_read_b64_tr_b16 a[96:97], v32 offset:64\n"
        "ds_read_b64_tr_b16 a[98:99], v33 offset:64\n"
        "ds_read_b64_tr_b16 a[100:101], v32 offset:6208\n"
        "ds_read_b64_tr_b16 a[102:103], v33 offset:6208\n"
        "ds_read_b64_tr_b16 a[104:105], v32 offset:12352\n"
        "ds_read_b64_tr_b16 a[106:107], v33 offset:12352\n"
        "ds_read_b64_tr_b16 a[108:109], v32 offset:72\n"
        "ds_read_b64_tr_b16 a[110:111], v33 offset:72\n"
        "ds_read_b64_tr_b16 a[112:113], v32 offset:6216\n"
        "ds_read_b64_tr_b16 a[114:115], v33 offset:6216\n"
        "ds_read_b64_tr_b16 a[116:117], v32 offset:12360\n"
        "ds_read_b64_tr_b16 a[118:119], v33 offset:12360\n"
        "ds_read_b64_tr_b16 a[120:121], v32 offset:128\n"
        "ds_read_b64_tr_b16 a[122:123], v33 offset:128\n"
        "ds_read_b64_tr_b16 a[124:125], v32 offset:6272\n"
        "ds_read_b64_tr_b16 a[126:127], v33 offset:6272\n"
        "ds_read_b64_tr_b16 a[128:129], v32 offset:12416\n"
        "ds_read_b64_tr_b16 a[130:131], v33 offset:12416\n"
        "ds_read_b64_tr_b16 a[132:133], v32 offset:136\n"
        "ds_read_b64_tr_b16 a[134:135], v33 offset:136\n"
        "ds_read_b64_tr_b16 a[136:137], v32 offset:6280\n"
        "ds_read_b64_tr_b16 a[138:139], v33 offset:6280\n"
        "ds_read_b64_tr_b16 a[140:141], v32 offset:12424\n"
        "ds_read_b64_tr_b16 a[142:143], v33 offset:12424\n"
        "ds_read_b128 a[0:3], v30\n"
        "ds_read_b128 a[4:7], v30 offset:64\n"
        "ds_read_b128 a[8:11], v30 offset:128\n"
        "ds_read_b128 a[12:15], v31\n"
        "ds_read_b128 a[16:19], v31 offset:64\n"
        "ds_read_b128 a[20:23], v31 offset:128\n"
        "ds_read_b128 a[24:27], v30 offset:6144\n"
        "ds_read_b128 a[28:31], v30 offset:6208\n"
        "ds_read_b128 a[32:35], v30 offset:6272\n"
        "ds_read_b128 a[36:39], v31 offset:6144\n"
        "ds_read_b128 a[40:43], v31 offset:6208\n"
        "ds_read_b128 a[44:47], v31 offset:6272\n"
        "ds_read_b128 a[48:51], v30 offset:12288\n"
        "ds_read_b128 a[52:55], v30 offset:12352\n"
        "ds_read_b128 a[56:59], v30 offset:12416\n"
        "ds_read_b128 a[60:63], v31 offset:12288\n"
        "ds_read_b128 a[64:67], v31 offset:12352\n"
        "ds_read_b128 a[68:71], v31 offset:12416\n"
        "s_waitcnt lgkmcnt(15)\n"
        "v_mfma_f32_16x16x32_bf16 v[30:33], a[72:75], v[44:47], 0\n"
        "s_waitcnt vmcnt(12)\n"
        "ds_read_b128 a[144:147], v28\n"
        "ds_read_b128 a[148:151], v28 offset:64\n"
        "v_mfma_f32_16x16x32_bf16 v[30:33], a[76:79], v[44:47], v[30:33]\n"
        "s_waitcnt vmcnt(6)\n"
        "ds_read_b128 a[152:155], v28 offset:128\n"
        "ds_read_b128 a[156:159], v29\n"
        "v_mfma_f32_16x16x32_bf16 v[30:33], a[80:83], v[44:47], v[30:33]\n"
        "s_waitcnt lgkmcnt(15)\n"
        "ds_read_b128 a[160:163], v29 offset:64\n"
        "ds_read_b128 a[164:167], v29 offset:128\n"
        "v_mfma_f32_16x16x32_bf16 v[48:51], a[92:95], v[44:47], 0\n"
        "s_waitcnt lgkmcnt(15)\n"
        "ds_read_b128 a[168:171], v28 offset:6144\n"
        "ds_read_b128 a[172:175], v28 offset:6208\n"
        "v_mfma_f32_16x16x32_bf16 v[48:51], a[88:91], v[44:47], v[48:51]\n"
        "s_waitcnt lgkmcnt(15)\n"
        "ds_read_b128 a[176:179], v28 offset:6272\n"
        "ds_read_b128 a[180:183], v29 offset:6144\n"
        "v_mfma_f32_16x16x32_bf16 v[48:51], a[84:87], v[44:47], v[48:51]\n"
        "s_waitcnt lgkmcnt(15)\n"
        "ds_read_b128 a[184:187], v29 offset:6208\n"
        "ds_read_b128 a[188:191], v29 offset:6272\n"
        "v_mfma_f32_16x16x32_bf16 v[52:55], a[96:99], v[44:47], 0\n"
        "s_waitcnt vmcnt(0)\n"
        "buffer_load_dwordx4 v[162:165], v34, s[36:39], 0 offen\n"
        "v_mul_f32 v30, 0xbc2aaaab, v30\n"
        "v_mfma_f32_16x16x32_bf16 v[52:55], a[100:103], v[44:47], v[52:55]\n"
        "s_waitcnt lgkmcnt(12)\n"
        "ds_read_b128 a[192:195], v28 offset:12288\n"
        "ds_read_b128 a[196:199], v28 offset:12352\n"
        "v_mfma_f32_16x16x32_bf16 v[52:55], a[104:107], v[44:47], v[52:55]\n"
        "s_barrier\n"
        "ds_read_b128 a[200:203], v28 offset:12416\n"
        "v_mul_f32 v31, 0xbc2aaaab, v31\n"
        "v_mfma_f32_16x16x32_bf16 v[56:59], a[116:119], v[44:47], 0\n"
        "s_waitcnt lgkmcnt(15)\n"
        "v_mul_f32 v32, 0xbc2aaaab, v32\n"
        "v_mul_f32 v33, 0xbc2aaaab, v33\n"
        "v_mfma_f32_16x16x32_bf16 v[56:59], a[112:115], v[44:47], v[56:59]\n"
        "s_waitcnt lgkmcnt(12)\n"
        "ds_read_b128 a[204:207], v29 offset:12288\n"
        "ds_read_b128 a[208:211], v29 offset:12352\n"
        "v_mfma_f32_16x16x32_bf16 v[56:59], a[108:111], v[44:47], v[56:59]\n"
        "s_waitcnt lgkmcnt(11)\n"
        "ds_read_b128 a[212:215], v29 offset:12416\n"
        "v_cvt_pk_bf16_f32 v28, v30, v31\n"
        "v_mfma_f32_16x16x32_bf16 v[60:63], a[120:123], v[44:47], 0\n"
        "s_waitcnt lgkmcnt(9)\n"
        "v_cvt_pk_bf16_f32 v29, v32, v33\n"
        "v_mul_f32 v48, 0xbc2aaaab, v48\n"
        "v_mfma_f32_16x16x32_bf16 v[60:63], a[124:127], v[44:47], v[60:63]\n"
        "s_waitcnt lgkmcnt(6)\n"
        "buffer_load_dwordx4 v[168:171], v34, s[36:39], 0 offen offset:16\n"
        "v_mul_f32 v49, 0xbc2aaaab, v49\n"
        "v_mfma_f32_16x16x32_bf16 v[60:63], a[128:131], v[44:47], v[60:63]\n"
        "s_waitcnt lgkmcnt(6)\n"
        "v_mul_f32 v50, 0xbc2aaaab, v50\n"
        "v_mul_f32 v51, 0xbc2aaaab, v51\n"
        "v_mfma_f32_16x16x32_bf16 v[64:67], a[140:143], v[44:47], 0\n"
        "s_waitcnt lgkmcnt(6)\n"
        "v_cvt_pk_bf16_f32 v30, v48, v49\n"
        "v_cvt_pk_bf16_f32 v31, v50, v51\n"
        "v_mfma_f32_16x16x32_bf16 v[64:67], a[136:139], v[44:47], v[64:67]\n"
        "s_waitcnt lgkmcnt(3)\n"
        "v_mul_f32 v52, 0xbc2aaaab, v52\n"
        "v_mul_f32 v53, 0xbc2aaaab, v53\n"
        "v_mfma_f32_16x16x32_bf16 v[64:67], a[132:135], v[44:47], v[64:67]\n"
        "s_waitcnt lgkmcnt(3)\n"
        "v_mul_f32 v54, 0xbc2aaaab, v54\n"
        "v_mul_f32 v55, 0xbc2aaaab, v55\n"
        "v_mfma_f32_16x16x32_bf16 v[44:47], a[144:147], v[28:31], 0\n"
        "s_waitcnt lgkmcnt(3)\n"
        "v_cvt_pk_bf16_f32 v48, v52, v53\n"
        "v_cvt_pk_bf16_f32 v49, v54, v55\n"
        "v_mul_f32 v56, 0xbc2aaaab, v56\n"
        "v_mul_f32 v57, 0xbc2aaaab, v57\n"
        "v_cvt_pk_bf16_f32 v50, v56, v57\n"
        "v_mul_f32 v58, 0xbc2aaaab, v58\n"
        "v_mul_f32 v59, 0xbc2aaaab, v59\n"
        "v_cvt_pk_bf16_f32 v51, v58, v59\n"
        "s_nop 1\n"
        "v_mfma_f32_16x16x32_bf16 v[44:47], a[148:151], v[48:51], v[44:47]\n"
        "s_waitcnt lgkmcnt(0)\n"
        "v_mul_f32 v60, 0xbc2aaaab, v60\n"
        "v_mul_f32 v61, 0xbc2aaaab, v61\n"
        "v_cvt_pk_bf16_f32 v52, v60, v61\n"
        "v_mul_f32 v62, 0xbc2aaaab, v62\n"
        "v_mul_f32 v63, 0xbc2aaaab, v63\n"
        "v_cvt_pk_bf16_f32 v53, v62, v63\n"
        "v_mul_f32 v64, 0xbc2aaaab, v64\n"
        "v_mul_f32 v65, 0xbc2aaaab, v65\n"
        "v_cvt_pk_bf16_f32 v54, v64, v65\n"
        "v_mul_f32 v66, 0xbc2aaaab, v66\n"
        "v_mul_f32 v67, 0xbc2aaaab, v67\n"
        "v_cvt_pk_bf16_f32 v55, v66, v67\n"
        "s_nop 1\n"
        "v_mfma_f32_16x16x32_bf16 v[44:47], a[152:155], v[52:55], v[44:47]\n"
        "s_waitcnt lgkmcnt(0)\n"
        "v_mfma_f32_16x16x32_bf16 v[56:59], a[164:167], v[52:55], 0\n"
        "s_waitcnt lgkmcnt(0)\n"
        "v_mfma_f32_16x16x32_bf16 v[56:59], a[160:163], v[48:51], v[56:59]\n"
        "buffer_load_dwordx4 v[174:177], v34, s[36:39], 0 offen offset:32\n"
        "v_mfma_f32_16x16x32_bf16 v[56:59], a[156:159], v[28:31], v[56:59]\n"
        "v_mfma_f32_16x16x32_bf16 v[60:63], a[168:171], v[28:31], 0\n"
        "v_mfma_f32_16x16x32_bf16 v[60:63], a[172:175], v[48:51], v[60:63]\n"
        "v_mfma_f32_16x16x32_bf16 v[60:63], a[176:179], v[52:55], v[60:63]\n"
        "v_mfma_f32_16x16x32_bf16 v[64:67], a[188:191], v[52:55], 0\n"
        "v_mfma_f32_16x16x32_bf16 v[64:67], a[184:187], v[48:51], v[64:67]\n"
        "v_mfma_f32_16x16x32_bf16 v[64:67], a[180:183], v[28:31], v[64:67]\n"
        "v_mfma_f32_16x16x32_bf16 v[68:71], a[192:195], v[28:31], 0\n"
        "v_mfma_f32_16x16x32_bf16 v[68:71], a[196:199], v[48:51], v[68:71]\n"
        "buffer_load_dwordx4 v[182:185], v35, s[36:39], 0 offen\n"
        "v_mfma_f32_16x16x32_bf16 v[68:71], a[200:203], v[52:55], v[68:71]\n"
        "v_mfma_f32_16x16x32_bf16 v[72:75], a[212:215], v[52:55], 0\n"
        "v_mfma_f32_16x16x32_bf16 v[72:75], a[208:211], v[48:51], v[72:75]\n"
        "v_mfma_f32_16x16x32_bf16 v[72:75], a[204:207], v[28:31], v[72:75]\n"
        "v_mfma_f32_16x16x32_bf16 v[28:31], a[144:147], a[0:3], v[44:47]\n"
        "v_mfma_f32_16x16x32_bf16 v[28:31], a[148:151], a[4:7], v[28:31]\n"
        "v_mfma_f32_16x16x32_bf16 v[28:31], a[152:155], a[8:11], v[28:31]\n"
        "s_nop 0\n"
        "v_mfma_f32_16x16x32_bf16 v[48:51], a[164:167], a[8:11], v[56:59]\n"
        "s_nop 2\n"
        "v_mfma_f32_16x16x32_bf16 v[48:51], a[160:163], a[4:7], v[48:51]\n"
        "s_nop 0\n"
        "buffer_load_dwordx4 v[188:191], v35, s[36:39], 0 offen offset:16\n"
        "v_add_f32 v36, v28, v29\n"
        "v_mfma_f32_16x16x32_bf16 v[48:51], a[156:159], a[0:3], v[48:51]\n"
        "s_nop 0\n"
        "v_add_f32 v36, v36, v30\n"
        "v_add_f32 v36, v36, v31\n"
        "v_mfma_f32_16x16x32_bf16 v[52:55], a[168:171], a[0:3], v[60:63]\n"
        "s_nop 1\n"
        "v_mfma_f32_16x16x32_bf16 v[52:55], a[172:175], a[4:7], v[52:55]\n"
        "s_nop 0\n"
        "v_add_f32 v36, v36, v48\n"
        "v_add_f32 v36, v36, v49\n"
        "v_mfma_f32_16x16x32_bf16 v[52:55], a[176:179], a[8:11], v[52:55]\n"
        "s_nop 0\n"
        "v_add_f32 v36, v36, v50\n"
        "v_add_f32 v36, v36, v51\n"
        "v_mfma_f32_16x16x32_bf16 v[76:79], a[188:191], a[8:11], v[64:67]\n"
        "s_nop 1\n"
        "v_mfma_f32_16x16x32_bf16 v[76:79], a[184:187], a[4:7], v[76:79]\n"
        "s_nop 0\n"
        "v_add_f32 v36, v36, v52\n"
        "v_add_f32 v36, v36, v53\n"
        "v_mfma_f32_16x16x32_bf16 v[76:79], a[180:183], a[0:3], v[76:79]\n"
        "s_nop 0\n"
        "v_add_f32 v36, v36, v54\n"
        "v_add_f32 v36, v36, v55\n"
        "v_mfma_f32_16x16x32_bf16 v[80:83], a[192:195], a[0:3], v[68:71]\n"
        "s_nop 0\n"
        "v_mfma_f32_16x16x32_bf16 v[80:83], a[196:199], a[4:7], v[80:83]\n"
        "s_nop 0\n"
        "buffer_load_dwordx4 v[194:197], v35, s[36:39], 0 offen offset:32\n"
        "v_add_f32 v36, v36, v76\n"
        "v_mfma_f32_16x16x32_bf16 v[80:83], a[200:203], a[8:11], v[80:83]\n"
        "s_nop 0\n"
        "v_add_f32 v36, v36, v77\n"
        "v_add_f32 v36, v36, v78\n"
        "v_mfma_f32_16x16x32_bf16 v[32:35], a[212:215], a[8:11], v[72:75]\n"
        "s_nop 0\n"
        "v_add_f32 v36, v36, v79\n"
        "v_mfma_f32_16x16x32_bf16 v[32:35], a[208:211], a[4:7], v[32:35]\n"
        "s_nop 0\n"
        "v_add_f32 v36, v36, v80\n"
        "v_add_f32 v36, v36, v81\n"
        "v_mfma_f32_16x16x32_bf16 v[32:35], a[204:207], a[0:3], v[32:35]\n"
        "s_nop 0\n"
        "v_add_f32 v36, v36, v82\n"
        "v_add_f32 v36, v36, v83\n"
        "v_mfma_f32_16x16x32_bf16 v[84:87], a[144:147], a[12:15], v[44:47]\n"
        "s_nop 1\n"
        "v_mfma_f32_16x16x32_bf16 v[84:87], a[148:151], a[16:19], v[84:87]\n"
        "s_nop 0\n"
        "v_add_f32 v36, v36, v32\n"
        "v_add_f32 v36, v36, v33\n"
        "v_mfma_f32_16x16x32_bf16 v[84:87], a[152:155], a[20:23], v[84:87]\n"
        "v_add_f32 v36, v36, v34\n"
        "v_add_f32 v36, v36, v35\n"
        "v_mfma_f32_16x16x32_bf16 v[88:91], a[164:167], a[20:23], v[56:59]\n"
        "s_add_u32 m0, s52, 0\n"
        "v_mov_b32 v43, v36\n"
        "v_mfma_f32_16x16x32_bf16 v[88:91], a[160:163], a[16:19], v[88:91]\n"
        "s_nop 0\n"
        "buffer_load_dwordx4 v0, s[36:39], s49 offen lds\n"
        "v_add_f32 v144, v84, v85\n"
        "v_mfma_f32_16x16x32_bf16 v[88:91], a[156:159], a[12:15], v[88:91]\n"
        "v_permlane32_swap_b32 v36, v43\n"
        "v_mfma_f32_16x16x32_bf16 v[92:95], a[168:171], a[12:15], v[60:63]\n"
        "v_add_f32 v36, v36, v43\n"
        "v_mov_b32 v43, v36\n"
        "v_mfma_f32_16x16x32_bf16 v[92:95], a[172:175], a[16:19], v[92:95]\n"
        "s_nop 0\n"
        "v_permlane16_swap_b32 v36, v43\n"
        "v_mfma_f32_16x16x32_bf16 v[92:95], a[176:179], a[20:23], v[92:95]\n"
        "v_add_f32 v36, v36, v43\n"
        "v_add_f32 v144, v144, v86\n"
        "v_mfma_f32_16x16x32_bf16 v[96:99], a[188:191], a[20:23], v[64:67]\n"
        "v_mul_f32 v36, 0xbac909aa, v36\n"
        "v_add_f32 v144, v144, v87\n"
        "v_mfma_f32_16x16x32_bf16 v[96:99], a[184:187], a[16:19], v[96:99]\n"
        "buffer_load_dwordx4 v1, s[36:39], s49 offen offset:1024 lds\n"
        "v_fma_f32 v28, v28, s93, v36\n"
        "v_mfma_f32_16x16x32_bf16 v[96:99], a[180:183], a[12:15], v[96:99]\n"
        "v_fma_f32 v29, v29, s93, v36\n"
        "v_fma_f32 v30, v30, s93, v36\n"
        "v_mfma_f32_16x16x32_bf16 v[100:103], a[192:195], a[12:15], v[68:71]\n"
        "v_fma_f32 v31, v31, s93, v36\n"
        "v_fma_f32 v48, v48, s93, v36\n"
        "v_mfma_f32_16x16x32_bf16 v[100:103], a[196:199], a[16:19], v[100:103]\n"
        "v_fma_f32 v49, v49, s93, v36\n"
        "v_fma_f32 v50, v50, s93, v36\n"
        "v_mfma_f32_16x16x32_bf16 v[100:103], a[200:203], a[20:23], v[100:103]\n"
        "v_fma_f32 v51, v51, s93, v36\n"
        "v_fma_f32 v52, v52, s93, v36\n"
        "v_mfma_f32_16x16x32_bf16 v[104:107], a[212:215], a[20:23], v[72:75]\n"
        "v_fma_f32 v53, v53, s93, v36\n"
        "v_fma_f32 v54, v54, s93, v36\n"
        "v_mfma_f32_16x16x32_bf16 v[104:107], a[208:211], a[16:19], v[104:107]\n"
        "buffer_load_dwordx4 v2, s[36:39], s49 offen offset:2048 lds\n"
        "v_fma_f32 v55, v55, s93, v36\n"
        "v_mfma_f32_16x16x32_bf16 v[104:107], a[204:207], a[12:15], v[104:107]\n"
        "v_fma_f32 v76, v76, s93, v36\n"
        "v_fma_f32 v77, v77, s93, v36\n"
        "v_mfma_f32_16x16x32_bf16 v[108:111], a[144:147], a[24:27], v[44:47]\n"
        "v_fma_f32 v78, v78, s93, v36\n"
        "v_fma_f32 v79, v79, s93, v36\n"
        "v_mfma_f32_16x16x32_bf16 v[108:111], a[148:151], a[28:31], v[108:111]\n"
        "v_fma_f32 v80, v80, s93, v36\n"
        "v_fma_f32 v81, v81, s93, v36\n"
        "v_mfma_f32_16x16x32_bf16 v[108:111], a[152:155], a[32:35], v[108:111]\n"
        "v_fma_f32 v82, v82, s93, v36\n"
        "v_fma_f32 v83, v83, s93, v36\n"
        "v_mfma_f32_16x16x32_bf16 v[112:115], a[164:167], a[32:35], v[56:59]\n"
        "v_fma_f32 v32, v32, s93, v36\n"
        "v_fma_f32 v33, v33, s93, v36\n"
        "v_mfma_f32_16x16x32_bf16 v[112:115], a[160:163], a[28:31], v[112:115]\n"
        "buffer_load_dwordx4 v3, s[36:39], s49 offen offset:3072 lds\n"
        "v_fma_f32 v34, v34, s93, v36\n"
        "v_mfma_f32_16x16x32_bf16 v[112:115], a[156:159], a[24:27], v[112:115]\n"
        "v_fma_f32 v35, v35, s93, v36\n"
        "v_cvt_pk_bf16_f32 v116, v28, v29\n"
        "v_mfma_f32_16x16x32_bf16 v[120:123], a[168:171], a[24:27], v[60:63]\n"
        "v_cvt_pk_bf16_f32 v117, v30, v31\n"
        "v_cvt_pk_bf16_f32 v118, v48, v49\n"
        "v_mfma_f32_16x16x32_bf16 v[120:123], a[172:175], a[28:31], v[120:123]\n"
        "v_cvt_pk_bf16_f32 v119, v50, v51\n"
        "v_cvt_pk_bf16_f32 v28, v52, v53\n"
        "v_mfma_f32_16x16x32_bf16 v[120:123], a[176:179], a[32:35], v[120:123]\n"
        "ds_write_b128 v37, v[116:119]\n"
        "v_mfma_f32_16x16x32_bf16 v[48:51], a[188:191], a[32:35], v[64:67]\n"
        "s_add_u32 m0, s52, 4096\n"
        "v_cvt_pk_bf16_f32 v29, v54, v55\n"
        "v_cvt_pk_bf16_f32 v30, v76, v77\n"
        "v_mfma_f32_16x16x32_bf16 v[48:51], a[184:187], a[28:31], v[48:51]\n"
        "buffer_load_dwordx4 v4, s[36:39], s49 offen lds\n"
        "v_cvt_pk_bf16_f32 v31, v78, v79\n"
        "v_mfma_f32_16x16x32_bf16 v[48:51], a[180:183], a[24:27], v[48:51]\n"
        "ds_write_b128 v37, v[28:31] offset:1024\n"
        "v_mfma_f32_16x16x32_bf16 v[28:31], a[192:195], a[24:27], v[68:71]\n"
        "v_cvt_pk_bf16_f32 v52, v80, v81\n"
        "v_cvt_pk_bf16_f32 v53, v82, v83\n"
        "v_mfma_f32_16x16x32_bf16 v[28:31], a[196:199], a[28:31], v[28:31]\n"
        "v_cvt_pk_bf16_f32 v54, v32, v33\n"
        "v_cvt_pk_bf16_f32 v55, v34, v35\n"
        "v_mfma_f32_16x16x32_bf16 v[28:31], a[200:203], a[32:35], v[28:31]\n"
        "ds_write_b128 v37, v[52:55] offset:2048\n"
        "v_mfma_f32_16x16x32_bf16 v[32:35], a[212:215], a[32:35], v[72:75]\n"
        "v_add_f32 v144, v144, v88\n"
        "v_add_f32 v144, v144, v89\n"
        "v_mfma_f32_16x16x32_bf16 v[32:35], a[208:211], a[28:31], v[32:35]\n"
        "buffer_load_dwordx4 v5, s[36:39], s49 offen offset:1024 lds\n"
        "v_add_f32 v144, v144, v90\n"
        "v_mfma_f32_16x16x32_bf16 v[32:35], a[204:207], a[24:27], v[32:35]\n"
        "v_add_f32 v144, v144, v91\n"
        "v_add_f32 v144, v144, v92\n"
        "v_mfma_f32_16x16x32_bf16 v[52:55], a[144:147], a[36:39], v[44:47]\n"
        "v_add_f32 v144, v144, v93\n"
        "v_add_f32 v144, v144, v94\n"
        "v_mfma_f32_16x16x32_bf16 v[52:55], a[148:151], a[40:43], v[52:55]\n"
        "v_add_f32 v144, v144, v95\n"
        "v_add_f32 v144, v144, v96\n"
        "v_mfma_f32_16x16x32_bf16 v[52:55], a[152:155], a[44:47], v[52:55]\n"
        "v_add_f32 v144, v144, v97\n"
        "v_add_f32 v144, v144, v98\n"
        "v_mfma_f32_16x16x32_bf16 v[76:79], a[164:167], a[44:47], v[56:59]\n"
        "v_add_f32 v144, v144, v99\n"
        "v_add_f32 v144, v144, v100\n"
        "v_mfma_f32_16x16x32_bf16 v[76:79], a[160:163], a[40:43], v[76:79]\n"
        "buffer_load_dwordx4 v6, s[36:39], s49 offen offset:2048 lds\n"
        "v_add_f32 v144, v144, v101\n"
        "v_mfma_f32_16x16x32_bf16 v[76:79], a[156:159], a[36:39], v[76:79]\n"
        "v_add_f32 v144, v144, v102\n"
        "v_add_f32 v144, v144, v103\n"
        "v_mfma_f32_16x16x32_bf16 v[80:83], a[168:171], a[36:39], v[60:63]\n"
        "v_add_f32 v144, v144, v104\n"
        "v_add_f32 v144, v144, v105\n"
        "v_mfma_f32_16x16x32_bf16 v[80:83], a[172:175], a[40:43], v[80:83]\n"
        "v_add_f32 v144, v144, v106\n"
        "v_add_f32 v144, v144, v107\n"
        "v_mfma_f32_16x16x32_bf16 v[80:83], a[176:179], a[44:47], v[80:83]\n"
        "v_mov_b32 v36, v144\n"
        "v_add_f32 v37, v108, v109\n"
        "v_mfma_f32_16x16x32_bf16 v[116:119], a[188:191], a[44:47], v[64:67]\n"
        "v_permlane32_swap_b32 v144, v36\n"
        "v_mfma_f32_16x16x32_bf16 v[116:119], a[184:187], a[40:43], v[116:119]\n"
        "buffer_load_dwordx4 v7, s[36:39], s49 offen offset:3072 lds\n"
        "v_add_f32 v144, v144, v36\n"
        "v_mfma_f32_16x16x32_bf16 v[116:119], a[180:183], a[36:39], v[116:119]\n"
        "v_mov_b32 v36, v144\n"
        "v_add_f32 v37, v37, v110\n"
        "v_mfma_f32_16x16x32_bf16 v[124:127], a[192:195], a[36:39], v[68:71]\n"
        "v_permlane16_swap_b32 v144, v36\n"
        "v_mfma_f32_16x16x32_bf16 v[124:127], a[196:199], a[40:43], v[124:127]\n"
        "v_add_f32 v144, v144, v36\n"
        "v_mul_f32 v144, 0xbac909aa, v144\n"
        "v_mfma_f32_16x16x32_bf16 v[124:127], a[200:203], a[44:47], v[124:127]\n"
        "v_add_f32 v37, v37, v111\n"
        "v_fma_f32 v84, v84, s93, v144\n"
        "v_mfma_f32_16x16x32_bf16 v[128:131], a[212:215], a[44:47], v[72:75]\n"
        "s_add_u32 m0, s52, 8192\n"
        "v_fma_f32 v85, v85, s93, v144\n"
        "v_fma_f32 v86, v86, s93, v144\n"
        "v_mfma_f32_16x16x32_bf16 v[128:131], a[208:211], a[40:43], v[128:131]\n"
        "buffer_load_dwordx4 v8, s[36:39], s49 offen lds\n"
        "v_fma_f32 v87, v87, s93, v144\n"
        "v_mfma_f32_16x16x32_bf16 v[128:131], a[204:207], a[36:39], v[128:131]\n"
        "v_fma_f32 v88, v88, s93, v144\n"
        "v_fma_f32 v89, v89, s93, v144\n"
        "v_mfma_f32_16x16x32_bf16 v[132:135], a[144:147], a[48:51], v[44:47]\n"
        "v_fma_f32 v90, v90, s93, v144\n"
        "v_fma_f32 v91, v91, s93, v144\n"
        "v_mfma_f32_16x16x32_bf16 v[132:135], a[148:151], a[52:55], v[132:135]\n"
        "v_fma_f32 v92, v92, s93, v144\n"
        "v_fma_f32 v93, v93, s93, v144\n"
        "v_mfma_f32_16x16x32_bf16 v[132:135], a[152:155], a[56:59], v[132:135]\n"
        "v_fma_f32 v94, v94, s93, v144\n"
        "v_fma_f32 v95, v95, s93, v144\n"
        "v_mfma_f32_16x16x32_bf16 v[136:139], a[164:167], a[56:59], v[56:59]\n"
        "v_fma_f32 v96, v96, s93, v144\n"
        "v_fma_f32 v97, v97, s93, v144\n"
        "v_mfma_f32_16x16x32_bf16 v[136:139], a[160:163], a[52:55], v[136:139]\n"
        "v_fma_f32 v98, v98, s93, v144\n"
        "v_fma_f32 v99, v99, s93, v144\n"
        "v_mfma_f32_16x16x32_bf16 v[136:139], a[156:159], a[48:51], v[136:139]\n"
        "v_fma_f32 v100, v100, s93, v144\n"
        "v_fma_f32 v101, v101, s93, v144\n"
        "v_mfma_f32_16x16x32_bf16 v[140:143], a[168:171], a[48:51], v[60:63]\n"
        "v_fma_f32 v102, v102, s93, v144\n"
        "v_fma_f32 v103, v103, s93, v144\n"
        "v_mfma_f32_16x16x32_bf16 v[140:143], a[172:175], a[52:55], v[140:143]\n"
        "v_fma_f32 v104, v104, s93, v144\n"
        "v_fma_f32 v105, v105, s93, v144\n"
        "v_mfma_f32_16x16x32_bf16 v[140:143], a[176:179], a[56:59], v[140:143]\n"
        "v_fma_f32 v106, v106, s93, v144\n"
        "v_fma_f32 v107, v107, s93, v144\n"
        "v_mfma_f32_16x16x32_bf16 v[144:147], a[188:191], a[56:59], v[64:67]\n"
        "v_cvt_pk_bf16_f32 v148, v84, v85\n"
        "v_cvt_pk_bf16_f32 v149, v86, v87\n"
        "v_mfma_f32_16x16x32_bf16 v[144:147], a[184:187], a[52:55], v[144:147]\n"
        "v_cvt_pk_bf16_f32 v150, v88, v89\n"
        "v_cvt_pk_bf16_f32 v151, v90, v91\n"
        "v_mfma_f32_16x16x32_bf16 v[144:147], a[180:183], a[48:51], v[144:147]\n"
        "ds_write_b128 v38, v[148:151]\n"
        "v_mfma_f32_16x16x32_bf16 v[84:87], a[192:195], a[48:51], v[68:71]\n"
        "v_cvt_pk_bf16_f32 v88, v92, v93\n"
        "v_cvt_pk_bf16_f32 v89, v94, v95\n"
        "v_mfma_f32_16x16x32_bf16 v[84:87], a[196:199], a[52:55], v[84:87]\n"
        "v_cvt_pk_bf16_f32 v90, v96, v97\n"
        "v_cvt_pk_bf16_f32 v91, v98, v99\n"
        "v_mfma_f32_16x16x32_bf16 v[84:87], a[200:203], a[56:59], v[84:87]\n"
        "ds_write_b128 v38, v[88:91] offset:1024\n"
        "v_mfma_f32_16x16x32_bf16 v[88:91], a[212:215], a[56:59], v[72:75]\n"
        "v_cvt_pk_bf16_f32 v92, v100, v101\n"
        "v_cvt_pk_bf16_f32 v93, v102, v103\n"
        "v_mfma_f32_16x16x32_bf16 v[88:91], a[208:211], a[52:55], v[88:91]\n"
        "v_cvt_pk_bf16_f32 v94, v104, v105\n"
        "v_cvt_pk_bf16_f32 v95, v106, v107\n"
        "v_mfma_f32_16x16x32_bf16 v[88:91], a[204:207], a[48:51], v[88:91]\n"
        "ds_write_b128 v38, v[92:95] offset:2048\n"
        "v_mfma_f32_16x16x32_bf16 v[92:95], a[144:147], a[60:63], v[44:47]\n"
        "v_add_f32 v37, v37, v112\n"
        "v_add_f32 v37, v37, v113\n"
        "v_mfma_f32_16x16x32_bf16 v[92:95], a[148:151], a[64:67], v[92:95]\n"
        "v_add_f32 v37, v37, v114\n"
        "v_add_f32 v37, v37, v115\n"
        "v_mfma_f32_16x16x32_bf16 v[92:95], a[152:155], a[68:71], v[92:95]\n"
        "v_add_f32 v37, v37, v120\n"
        "v_add_f32 v37, v37, v121\n"
        "v_mfma_f32_16x16x32_bf16 v[44:47], a[164:167], a[68:71], v[56:59]\n"
        "v_add_f32 v37, v37, v122\n"
        "v_add_f32 v37, v37, v123\n"
        "v_mfma_f32_16x16x32_bf16 v[44:47], a[160:163], a[64:67], v[44:47]\n"
        "v_add_f32 v37, v37, v48\n"
        "v_add_f32 v37, v37, v49\n"
        "v_mfma_f32_16x16x32_bf16 v[44:47], a[156:159], a[60:63], v[44:47]\n"
        "v_add_f32 v37, v37, v50\n"
        "v_add_f32 v37, v37, v51\n"
        "v_mfma_f32_16x16x32_bf16 v[56:59], a[168:171], a[60:63], v[60:63]\n"
        "v_add_f32 v37, v37, v28\n"
        "v_add_f32 v37, v37, v29\n"
        "v_mfma_f32_16x16x32_bf16 v[56:59], a[172:175], a[64:67], v[56:59]\n"
        "v_add_f32 v37, v37, v30\n"
        "v_add_f32 v37, v37, v31\n"
        "v_mfma_f32_16x16x32_bf16 v[56:59], a[176:179], a[68:71], v[56:59]\n"
        "v_add_f32 v37, v37, v32\n"
        "v_add_f32 v37, v37, v33\n"
        "v_mfma_f32_16x16x32_bf16 v[60:63], a[188:191], a[68:71], v[64:67]\n"
        "v_add_f32 v37, v37, v34\n"
        "v_add_f32 v37, v37, v35\n"
        "v_mfma_f32_16x16x32_bf16 v[60:63], a[184:187], a[64:67], v[60:63]\n"
        "v_mov_b32 v36, v37\n"
        "v_add_f32 v38, v52, v53\n"
        "v_mfma_f32_16x16x32_bf16 v[60:63], a[180:183], a[60:63], v[60:63]\n"
        "v_permlane32_swap_b32 v37, v36\n"
        "v_mfma_f32_16x16x32_bf16 v[64:67], a[192:195], a[60:63], v[68:71]\n"
        "v_add_f32 v37, v37, v36\n"
        "v_mov_b32 v36, v37\n"
        "v_mfma_f32_16x16x32_bf16 v[64:67], a[196:199], a[64:67], v[64:67]\n"
        "s_nop 0\n"
        "v_permlane16_swap_b32 v37, v36\n"
        "v_mfma_f32_16x16x32_bf16 v[64:67], a[200:203], a[68:71], v[64:67]\n"
        "v_add_f32 v37, v37, v36\n"
        "v_add_f32 v38, v38, v54\n"
        "v_mfma_f32_16x16x32_bf16 v[68:71], a[212:215], a[68:71], v[72:75]\n"
        "v_mul_f32 v37, 0xbac909aa, v37\n"
        "v_add_f32 v38, v38, v55\n"
        "v_mfma_f32_16x16x32_bf16 v[68:71], a[208:211], a[64:67], v[68:71]\n"
        "v_fma_f32 v108, v108, s93, v37\n"
        "v_fma_f32 v109, v109, s93, v37\n"
        "v_mfma_f32_16x16x32_bf16 v[68:71], a[204:207], a[60:63], v[68:71]\n"
        "v_fma_f32 v110, v110, s93, v37\n"
        "v_fma_f32 v111, v111, s93, v37\n"
        "v_fma_f32 v112, v112, s93, v37\n"
        "v_fma_f32 v113, v113, s93, v37\n"
        "v_fma_f32 v114, v114, s93, v37\n"
        "v_fma_f32 v115, v115, s93, v37\n"
        "v_fma_f32 v120, v120, s93, v37\n"
        "v_fma_f32 v121, v121, s93, v37\n"
        "v_fma_f32 v122, v122, s93, v37\n"
        "v_fma_f32 v123, v123, s93, v37\n"
        "v_fma_f32 v48, v48, s93, v37\n"
        "v_fma_f32 v49, v49, s93, v37\n"
        "v_fma_f32 v50, v50, s93, v37\n"
        "v_fma_f32 v51, v51, s93, v37\n"
        "v_fma_f32 v28, v28, s93, v37\n"
        "v_fma_f32 v29, v29, s93, v37\n"
        "v_fma_f32 v30, v30, s93, v37\n"
        "v_fma_f32 v31, v31, s93, v37\n"
        "v_fma_f32 v32, v32, s93, v37\n"
        "v_fma_f32 v33, v33, s93, v37\n"
        "v_fma_f32 v34, v34, s93, v37\n"
        "v_fma_f32 v35, v35, s93, v37\n"
        "v_cvt_pk_bf16_f32 v72, v108, v109\n"
        "v_cvt_pk_bf16_f32 v73, v110, v111\n"
        "v_cvt_pk_bf16_f32 v74, v112, v113\n"
        "v_cvt_pk_bf16_f32 v75, v114, v115\n"
        "v_cvt_pk_bf16_f32 v96, v120, v121\n"
        "v_cvt_pk_bf16_f32 v97, v122, v123\n"
        "v_cvt_pk_bf16_f32 v98, v48, v49\n"
        "v_cvt_pk_bf16_f32 v99, v50, v51\n"
        "v_cvt_pk_bf16_f32 v48, v28, v29\n"
        "v_cvt_pk_bf16_f32 v49, v30, v31\n"
        "v_cvt_pk_bf16_f32 v50, v32, v33\n"
        "v_cvt_pk_bf16_f32 v51, v34, v35\n"
        "ds_write_b128 v39, v[72:75]\n"
        "ds_write_b128 v39, v[96:99] offset:1024\n"
        "ds_write_b128 v39, v[48:51] offset:2048\n"
        "v_add_f32 v38, v38, v76\n"
        "v_add_f32 v38, v38, v77\n"
        "v_add_f32 v38, v38, v78\n"
        "v_add_f32 v38, v38, v79\n"
        "v_add_f32 v38, v38, v80\n"
        "v_add_f32 v38, v38, v81\n"
        "v_add_f32 v38, v38, v82\n"
        "v_add_f32 v38, v38, v83\n"
        "v_add_f32 v38, v38, v116\n"
        "v_add_f32 v38, v38, v117\n"
        "v_add_f32 v38, v38, v118\n"
        "v_add_f32 v38, v38, v119\n"
        "v_add_f32 v38, v38, v124\n"
        "v_add_f32 v38, v38, v125\n"
        "v_add_f32 v38, v38, v126\n"
        "v_add_f32 v38, v38, v127\n"
        "v_add_f32 v38, v38, v128\n"
        "v_add_f32 v38, v38, v129\n"
        "v_add_f32 v38, v38, v130\n"
        "v_add_f32 v38, v38, v131\n"
        "v_mov_b32 v28, v38\n"
        "v_add_f32 v43, v132, v133\n"
        "v_add_f32 v43, v43, v134\n"
        "v_permlane32_swap_b32 v38, v28\n"
        "v_add_f32 v38, v38, v28\n"
        "v_mov_b32 v28, v38\n"
        "v_add_f32 v43, v43, v135\n"
        "v_add_f32 v43, v43, v136\n"
        "v_permlane16_swap_b32 v38, v28\n"
        "v_add_f32 v38, v38, v28\n"
        "v_mul_f32 v38, 0xbac909aa, v38\n"
        "v_fma_f32 v52, v52, s93, v38\n"
        "v_fma_f32 v53, v53, s93, v38\n"
        "v_fma_f32 v54, v54, s93, v38\n"
        "v_fma_f32 v55, v55, s93, v38\n"
        "v_fma_f32 v76, v76, s93, v38\n"
        "v_fma_f32 v77, v77, s93, v38\n"
        "v_fma_f32 v78, v78, s93, v38\n"
        "v_fma_f32 v79, v79, s93, v38\n"
        "v_fma_f32 v80, v80, s93, v38\n"
        "v_fma_f32 v81, v81, s93, v38\n"
        "v_fma_f32 v82, v82, s93, v38\n"
        "v_fma_f32 v83, v83, s93, v38\n"
        "v_fma_f32 v116, v116, s93, v38\n"
        "v_fma_f32 v117, v117, s93, v38\n"
        "v_fma_f32 v118, v118, s93, v38\n"
        "v_fma_f32 v119, v119, s93, v38\n"
        "v_fma_f32 v124, v124, s93, v38\n"
        "v_fma_f32 v125, v125, s93, v38\n"
        "v_fma_f32 v126, v126, s93, v38\n"
        "v_fma_f32 v127, v127, s93, v38\n"
        "v_fma_f32 v128, v128, s93, v38\n"
        "v_fma_f32 v129, v129, s93, v38\n"
        "v_fma_f32 v130, v130, s93, v38\n"
        "v_fma_f32 v131, v131, s93, v38\n"
        "v_cvt_pk_bf16_f32 v28, v52, v53\n"
        "v_cvt_pk_bf16_f32 v29, v54, v55\n"
        "v_cvt_pk_bf16_f32 v30, v76, v77\n"
        "v_cvt_pk_bf16_f32 v31, v78, v79\n"
        "v_cvt_pk_bf16_f32 v32, v80, v81\n"
        "v_cvt_pk_bf16_f32 v33, v82, v83\n"
        "v_cvt_pk_bf16_f32 v34, v116, v117\n"
        "v_cvt_pk_bf16_f32 v35, v118, v119\n"
        "v_cvt_pk_bf16_f32 v36, v124, v125\n"
        "v_cvt_pk_bf16_f32 v37, v126, v127\n"
        "v_cvt_pk_bf16_f32 v38, v128, v129\n"
        "v_cvt_pk_bf16_f32 v39, v130, v131\n"
        "ds_write_b128 v40, v[28:31]\n"
        "ds_write_b128 v40, v[32:35] offset:1024\n"
        "ds_write_b128 v40, v[36:39] offset:2048\n"
        "v_add_f32 v43, v43, v137\n"
        "v_add_f32 v43, v43, v138\n"
        "v_add_f32 v43, v43, v139\n"
        "v_add_f32 v43, v43, v140\n"
        "v_add_f32 v43, v43, v141\n"
        "v_add_f32 v43, v43, v142\n"
        "v_add_f32 v43, v43, v143\n"
        "v_add_f32 v43, v43, v144\n"
        "v_add_f32 v43, v43, v145\n"
        "v_add_f32 v43, v43, v146\n"
        "v_add_f32 v43, v43, v147\n"
        "v_add_f32 v43, v43, v84\n"
        "v_add_f32 v43, v43, v85\n"
        "v_add_f32 v43, v43, v86\n"
        "v_add_f32 v43, v43, v87\n"
        "v_add_f32 v43, v43, v88\n"
        "v_add_f32 v43, v43, v89\n"
        "v_add_f32 v43, v43, v90\n"
        "v_add_f32 v43, v43, v91\n"
        "v_mov_b32 v28, v43\n"
        "v_add_f32 v40, v92, v93\n"
        "v_add_f32 v40, v40, v94\n"
        "v_permlane32_swap_b32 v43, v28\n"
        "v_add_f32 v43, v43, v28\n"
        "v_mov_b32 v28, v43\n"
        "v_add_f32 v40, v40, v95\n"
        "v_add_f32 v40, v40, v44\n"
        "v_permlane16_swap_b32 v43, v28\n"
        "v_add_f32 v43, v43, v28\n"
        "v_mul_f32 v43, 0xbac909aa, v43\n"
        "v_fma_f32 v132, v132, s93, v43\n"
        "v_fma_f32 v133, v133, s93, v43\n"
        "v_fma_f32 v134, v134, s93, v43\n"
        "v_fma_f32 v135, v135, s93, v43\n"
        "v_fma_f32 v136, v136, s93, v43\n"
        "v_fma_f32 v137, v137, s93, v43\n"
        "v_fma_f32 v138, v138, s93, v43\n"
        "v_fma_f32 v139, v139, s93, v43\n"
        "v_fma_f32 v140, v140, s93, v43\n"
        "v_fma_f32 v141, v141, s93, v43\n"
        "v_fma_f32 v142, v142, s93, v43\n"
        "v_fma_f32 v143, v143, s93, v43\n"
        "v_fma_f32 v144, v144, s93, v43\n"
        "v_fma_f32 v145, v145, s93, v43\n"
        "v_fma_f32 v146, v146, s93, v43\n"
        "v_fma_f32 v147, v147, s93, v43\n"
        "v_fma_f32 v84, v84, s93, v43\n"
        "v_fma_f32 v85, v85, s93, v43\n"
        "v_fma_f32 v86, v86, s93, v43\n"
        "v_fma_f32 v87, v87, s93, v43\n"
        "v_fma_f32 v88, v88, s93, v43\n"
        "v_fma_f32 v89, v89, s93, v43\n"
        "v_fma_f32 v90, v90, s93, v43\n"
        "v_fma_f32 v91, v91, s93, v43\n"
        "v_cvt_pk_bf16_f32 v28, v132, v133\n"
        "v_cvt_pk_bf16_f32 v29, v134, v135\n"
        "v_cvt_pk_bf16_f32 v30, v136, v137\n"
        "v_cvt_pk_bf16_f32 v31, v138, v139\n"
        "v_cvt_pk_bf16_f32 v32, v140, v141\n"
        "v_cvt_pk_bf16_f32 v33, v142, v143\n"
        "v_cvt_pk_bf16_f32 v34, v144, v145\n"
        "v_cvt_pk_bf16_f32 v35, v146, v147\n"
        "v_cvt_pk_bf16_f32 v36, v84, v85\n"
        "v_cvt_pk_bf16_f32 v37, v86, v87\n"
        "v_cvt_pk_bf16_f32 v38, v88, v89\n"
        "v_cvt_pk_bf16_f32 v39, v90, v91\n"
        "ds_write_b128 v41, v[28:31]\n"
        "ds_write_b128 v41, v[32:35] offset:1024\n"
        "ds_write_b128 v41, v[36:39] offset:2048\n"
        "v_add_f32 v40, v40, v45\n"
        "v_add_f32 v40, v40, v46\n"
        "v_add_f32 v40, v40, v47\n"
        "v_add_f32 v40, v40, v56\n"
        "v_add_f32 v40, v40, v57\n"
        "v_add_f32 v40, v40, v58\n"
        "v_add_f32 v40, v40, v59\n"
        "v_add_f32 v40, v40, v60\n"
        "v_add_f32 v40, v40, v61\n"
        "v_add_f32 v40, v40, v62\n"
        "v_add_f32 v40, v40, v63\n"
        "v_add_f32 v40, v40, v64\n"
        "v_add_f32 v40, v40, v65\n"
        "v_add_f32 v40, v40, v66\n"
        "v_add_f32 v40, v40, v67\n"
        "v_add_f32 v40, v40, v68\n"
        "v_add_f32 v40, v40, v69\n"
        "v_add_f32 v40, v40, v70\n"
        "v_add_f32 v40, v40, v71\n"
        "v_mov_b32 v28, v40\n"
        "s_waitcnt vmcnt(9)\n"
        "s_nop 0\n"
        "v_permlane32_swap_b32 v40, v28\n"
        "v_add_f32 v40, v40, v28\n"
        "v_mov_b32 v28, v40\n"
        "s_nop 1\n"
        "v_permlane16_swap_b32 v40, v28\n"
        "v_add_f32 v40, v40, v28\n"
        "v_mul_f32 v40, 0xbac909aa, v40\n"
        "v_fma_f32 v92, v92, s93, v40\n"
        "v_fma_f32 v93, v93, s93, v40\n"
        "v_fma_f32 v94, v94, s93, v40\n"
        "v_fma_f32 v95, v95, s93, v40\n"
        "v_fma_f32 v44, v44, s93, v40\n"
        "v_fma_f32 v45, v45, s93, v40\n"
        "v_fma_f32 v46, v46, s93, v40\n"
        "v_fma_f32 v47, v47, s93, v40\n"
        "v_fma_f32 v56, v56, s93, v40\n"
        "v_fma_f32 v57, v57, s93, v40\n"
        "v_fma_f32 v58, v58, s93, v40\n"
        "v_fma_f32 v59, v59, s93, v40\n"
        "v_fma_f32 v60, v60, s93, v40\n"
        "v_fma_f32 v61, v61, s93, v40\n"
        "v_fma_f32 v62, v62, s93, v40\n"
        "v_fma_f32 v63, v63, s93, v40\n"
        "v_fma_f32 v64, v64, s93, v40\n"
        "v_fma_f32 v65, v65, s93, v40\n"
        "v_fma_f32 v66, v66, s93, v40\n"
        "v_fma_f32 v67, v67, s93, v40\n"
        "v_fma_f32 v68, v68, s93, v40\n"
        "v_fma_f32 v69, v69, s93, v40\n"
        "v_fma_f32 v70, v70, s93, v40\n"
        "v_fma_f32 v71, v71, s93, v40\n"
        "v_cvt_pk_bf16_f32 v28, v92, v93\n"
        "v_cvt_pk_bf16_f32 v29, v94, v95\n"
        "v_cvt_pk_bf16_f32 v30, v44, v45\n"
        "v_cvt_pk_bf16_f32 v31, v46, v47\n"
        "v_cvt_pk_bf16_f32 v32, v56, v57\n"
        "v_cvt_pk_bf16_f32 v33, v58, v59\n"
        "v_cvt_pk_bf16_f32 v34, v60, v61\n"
        "v_cvt_pk_bf16_f32 v35, v62, v63\n"
        "v_cvt_pk_bf16_f32 v36, v64, v65\n"
        "v_cvt_pk_bf16_f32 v37, v66, v67\n"
        "v_cvt_pk_bf16_f32 v38, v68, v69\n"
        "v_cvt_pk_bf16_f32 v39, v70, v71\n"
        "ds_write_b128 v42, v[28:31]\n"
        "ds_write_b128 v42, v[32:35] offset:1024\n"
        "ds_write_b128 v42, v[36:39] offset:2048\n"
        "s_waitcnt lgkmcnt(0)\n"
        "s_barrier\n"
        "s_branch FE_%=\n"
        "FV_%=:\n"
        "s_bitcmp1_b32 s52, 11\n"
        "s_cbranch_scc1 FG_%=\n"
        "v_mov_b32 v44, 0x3f803f80\n"
        "v_mov_b32 v45, 0x3f803f80\n"
        "v_mov_b32 v46, 0x3f803f80\n"
        "v_mov_b32 v47, 0x3f803f80\n"
        "s_waitcnt vmcnt(18)\n"
        "ds_read_b64_tr_b16 a[72:73], v32 offset:0\n"
        "ds_read_b64_tr_b16 a[74:75], v33 offset:0\n"
        "ds_read_b64_tr_b16 a[76:77], v32 offset:6144\n"
        "ds_read_b64_tr_b16 a[78:79], v33 offset:6144\n"
        "ds_read_b64_tr_b16 a[80:81], v32 offset:12288\n"
        "ds_read_b64_tr_b16 a[82:83], v33 offset:12288\n"
        "ds_read_b64_tr_b16 a[84:85], v32 offset:8\n"
        "ds_read_b64_tr_b16 a[86:87], v33 offset:8\n"
        "ds_read_b64_tr_b16 a[88:89], v32 offset:6152\n"
        "ds_read_b64_tr_b16 a[90:91], v33 offset:6152\n"
        "ds_read_b64_tr_b16 a[92:93], v32 offset:12296\n"
        "ds_read_b64_tr_b16 a[94:95], v33 offset:12296\n"
        "ds_read_b64_tr_b16 a[96:97], v32 offset:64\n"
        "ds_read_b64_tr_b16 a[98:99], v33 offset:64\n"
        "ds_read_b64_tr_b16 a[100:101], v32 offset:6208\n"
        "ds_read_b64_tr_b16 a[102:103], v33 offset:6208\n"
        "ds_read_b64_tr_b16 a[104:105], v32 offset:12352\n"
        "ds_read_b64_tr_b16 a[106:107], v33 offset:12352\n"
        "ds_read_b64_tr_b16 a[108:109], v32 offset:72\n"
        "ds_read_b64_tr_b16 a[110:111], v33 offset:72\n"
        "ds_read_b64_tr_b16 a[112:113], v32 offset:6216\n"
        "ds_read_b64_tr_b16 a[114:115], v33 offset:6216\n"
        "ds_read_b64_tr_b16 a[116:117], v32 offset:12360\n"
        "ds_read_b64_tr_b16 a[118:119], v33 offset:12360\n"
        "ds_read_b64_tr_b16 a[120:121], v32 offset:128\n"
        "ds_read_b64_tr_b16 a[122:123], v33 offset:128\n"
        "ds_read_b64_tr_b16 a[124:125], v32 offset:6272\n"
        "ds_read_b64_tr_b16 a[126:127], v33 offset:6272\n"
        "ds_read_b64_tr_b16 a[128:129], v32 offset:12416\n"
        "ds_read_b64_tr_b16 a[130:131], v33 offset:12416\n"
        "ds_read_b64_tr_b16 a[132:133], v32 offset:136\n"
        "ds_read_b64_tr_b16 a[134:135], v33 offset:136\n"
        "ds_read_b64_tr_b16 a[136:137], v32 offset:6280\n"
        "ds_read_b64_tr_b16 a[138:139], v33 offset:6280\n"
        "ds_read_b64_tr_b16 a[140:141], v32 offset:12424\n"
        "ds_read_b64_tr_b16 a[142:143], v33 offset:12424\n"
        "ds_read_b128 a[0:3], v30\n"
        "ds_read_b128 a[4:7], v30 offset:64\n"
        "ds_read_b128 a[8:11], v30 offset:128\n"
        "ds_read_b128 a[12:15], v31\n"
        "ds_read_b128 a[16:19], v31 offset:64\n"
        "ds_read_b128 a[20:23], v31 offset:128\n"
        "ds_read_b128 a[24:27], v30 offset:6144\n"
        "ds_read_b128 a[28:31], v30 offset:6208\n"
        "ds_read_b128 a[32:35], v30 offset:6272\n"
        "ds_read_b128 a[36:39], v31 offset:6144\n"
        "ds_read_b128 a[40:43], v31 offset:6208\n"
        "ds_read_b128 a[44:47], v31 offset:6272\n"
        "ds_read_b128 a[48:51], v30 offset:12288\n"
        "ds_read_b128 a[52:55], v30 offset:12352\n"
        "ds_read_b128 a[56:59], v30 offset:12416\n"
        "ds_read_b128 a[60:63], v31 offset:12288\n"
        "ds_read_b128 a[64:67], v31 offset:12352\n"
        "ds_read_b128 a[68:71], v31 offset:12416\n"
        "s_waitcnt lgkmcnt(15)\n"
        "s_waitcnt vmcnt(12)\n"
        "ds_read_b128 a[144:147], v28\n"
        "ds_read_b128 a[148:151], v28 offset:64\n"
        "ds_read_b128 a[152:155], v28 offset:128\n"
        "s_waitcnt lgkmcnt(0)\n"
        "v_mfma_f32_16x16x32_bf16 v[30:33], a[144:147], a[72:75], 0\n"
        "s_waitcnt vmcnt(6)\n"
        "ds_read_b128 a[156:159], v29\n"
        "ds_read_b128 a[160:163], v29 offset:64\n"
        "v_mfma_f32_16x16x32_bf16 v[30:33], a[148:151], a[76:79], v[30:33]\n"
        "s_waitcnt vmcnt(0)\n"
        "ds_read_b128 a[164:167], v29 offset:128\n"
        "ds_read_b128 a[168:171], v28 offset:6144\n"
        "v_mfma_f32_16x16x32_bf16 v[30:33], a[152:155], a[80:83], v[30:33]\n"
        "s_waitcnt lgkmcnt(4)\n"
        "ds_read_b128 a[172:175], v28 offset:6208\n"
        "ds_read_b128 a[176:179], v28 offset:6272\n"
        "s_waitcnt lgkmcnt(3)\n"
        "v_mfma_f32_16x16x32_bf16 v[48:51], a[164:167], a[80:83], 0\n"
        "s_barrier\n"
        "ds_read_b128 a[180:183], v29 offset:6144\n"
        "ds_read_b128 a[184:187], v29 offset:6208\n"
        "v_mfma_f32_16x16x32_bf16 v[48:51], a[160:163], a[76:79], v[48:51]\n"
        "s_waitcnt lgkmcnt(2)\n"
        "ds_read_b128 a[188:191], v29 offset:6272\n"
        "ds_read_b128 a[192:195], v28 offset:12288\n"
        "v_mfma_f32_16x16x32_bf16 v[48:51], a[156:159], a[72:75], v[48:51]\n"
        "s_waitcnt lgkmcnt(4)\n"
        "ds_read_b128 a[196:199], v28 offset:12352\n"
        "ds_read_b128 a[200:203], v28 offset:12416\n"
        "v_mfma_f32_16x16x32_bf16 v[52:55], a[168:171], a[72:75], 0\n"
        "s_waitcnt lgkmcnt(6)\n"
        "ds_read_b128 a[204:207], v29 offset:12288\n"
        "ds_read_b128 a[208:211], v29 offset:12352\n"
        "v_mfma_f32_16x16x32_bf16 v[52:55], a[172:175], a[76:79], v[52:55]\n"
        "s_waitcnt lgkmcnt(8)\n"
        "ds_read_b128 a[212:215], v29 offset:12416\n"
        "v_add_f32 v43, v30, v31\n"
        "v_mfma_f32_16x16x32_bf16 v[52:55], a[176:179], a[80:83], v[52:55]\n"
        "s_waitcnt lgkmcnt(9)\n"
        "buffer_load_dwordx4 v[162:165], v34, s[36:39], 0 offen\n"
        "v_add_f32 v43, v43, v32\n"
        "s_waitcnt lgkmcnt(6)\n"
        "v_mfma_f32_16x16x32_bf16 v[56:59], a[188:191], a[80:83], 0\n"
        "s_waitcnt lgkmcnt(6)\n"
        "v_add_f32 v43, v43, v33\n"
        "v_add_f32 v43, v43, v48\n"
        "v_mfma_f32_16x16x32_bf16 v[56:59], a[184:187], a[76:79], v[56:59]\n"
        "s_waitcnt lgkmcnt(3)\n"
        "v_add_f32 v43, v43, v49\n"
        "v_add_f32 v43, v43, v50\n"
        "v_mfma_f32_16x16x32_bf16 v[56:59], a[180:183], a[72:75], v[56:59]\n"
        "s_waitcnt lgkmcnt(3)\n"
        "v_add_f32 v43, v43, v51\n"
        "v_add_f32 v43, v43, v52\n"
        "v_mfma_f32_16x16x32_bf16 v[60:63], a[192:195], a[72:75], 0\n"
        "s_waitcnt lgkmcnt(0)\n"
        "v_add_f32 v43, v43, v53\n"
        "v_add_f32 v43, v43, v54\n"
        "v_mfma_f32_16x16x32_bf16 v[60:63], a[196:199], a[76:79], v[60:63]\n"
        "s_waitcnt lgkmcnt(0)\n"
        "buffer_load_dwordx4 v[168:171], v34, s[36:39], 0 offen offset:16\n"
        "v_add_f32 v43, v43, v55\n"
        "v_mfma_f32_16x16x32_bf16 v[60:63], a[200:203], a[80:83], v[60:63]\n"
        "s_waitcnt lgkmcnt(0)\n"
        "v_add_f32 v43, v43, v56\n"
        "v_add_f32 v43, v43, v57\n"
        "v_mfma_f32_16x16x32_bf16 v[64:67], a[212:215], a[80:83], 0\n"
        "s_waitcnt lgkmcnt(0)\n"
        "v_add_f32 v43, v43, v58\n"
        "v_add_f32 v43, v43, v59\n"
        "v_mfma_f32_16x16x32_bf16 v[64:67], a[208:211], a[76:79], v[64:67]\n"
        "s_waitcnt lgkmcnt(0)\n"
        "v_add_f32 v43, v43, v60\n"
        "v_add_f32 v43, v43, v61\n"
        "v_mfma_f32_16x16x32_bf16 v[64:67], a[204:207], a[72:75], v[64:67]\n"
        "s_waitcnt lgkmcnt(0)\n"
        "v_add_f32 v43, v43, v62\n"
        "v_add_f32 v43, v43, v63\n"
        "v_mfma_f32_16x16x32_bf16 v[68:71], a[0:3], v[44:47], 0\n"
        "s_nop 1\n"
        "v_mfma_f32_16x16x32_bf16 v[68:71], a[4:7], v[44:47], v[68:71]\n"
        "s_nop 0\n"
        "v_add_f32 v43, v43, v64\n"
        "v_add_f32 v43, v43, v65\n"
        "v_mfma_f32_16x16x32_bf16 v[68:71], a[8:11], v[44:47], v[68:71]\n"
        "v_add_f32 v43, v43, v66\n"
        "v_add_f32 v43, v43, v67\n"
        "v_mfma_f32_16x16x32_bf16 v[72:75], a[20:23], v[44:47], 0\n"
        "s_nop 0\n"
        "v_mov_b32 v29, v43\n"
        "v_mfma_f32_16x16x32_bf16 v[72:75], a[16:19], v[44:47], v[72:75]\n"
        "s_nop 0\n"
        "buffer_load_dwordx4 v[174:177], v34, s[36:39], 0 offen offset:32\n"
        "v_cvt_pk_bf16_f32 v28, v68, v69\n"
        "v_mfma_f32_16x16x32_bf16 v[72:75], a[12:15], v[44:47], v[72:75]\n"
        "v_permlane32_swap_b32 v43, v29\n"
        "v_mfma_f32_16x16x32_bf16 v[76:79], a[24:27], v[44:47], 0\n"
        "v_add_f32 v43, v43, v29\n"
        "v_mov_b32 v34, v43\n"
        "v_mfma_f32_16x16x32_bf16 v[76:79], a[28:31], v[44:47], v[76:79]\n"
        "s_nop 0\n"
        "v_permlane16_swap_b32 v43, v34\n"
        "v_mfma_f32_16x16x32_bf16 v[76:79], a[32:35], v[44:47], v[76:79]\n"
        "v_cvt_pk_bf16_f32 v29, v70, v71\n"
        "v_add_f32 v43, v43, v34\n"
        "v_mfma_f32_16x16x32_bf16 v[68:71], a[44:47], v[44:47], 0\n"
        "v_mul_f32 v43, 0xbc2aaaab, v43\n"
        "v_add_f32 v30, v30, v43\n"
        "v_mfma_f32_16x16x32_bf16 v[68:71], a[40:43], v[44:47], v[68:71]\n"
        "v_add_f32 v31, v31, v43\n"
        "v_add_f32 v32, v32, v43\n"
        "v_mfma_f32_16x16x32_bf16 v[68:71], a[36:39], v[44:47], v[68:71]\n"
        "v_add_f32 v33, v33, v43\n"
        "v_add_f32 v48, v48, v43\n"
        "v_mfma_f32_16x16x32_bf16 v[80:83], a[48:51], v[44:47], 0\n"
        "v_add_f32 v49, v49, v43\n"
        "v_add_f32 v50, v50, v43\n"
        "v_mfma_f32_16x16x32_bf16 v[80:83], a[52:55], v[44:47], v[80:83]\n"
        "buffer_load_dwordx4 v[182:185], v35, s[36:39], 0 offen\n"
        "v_add_f32 v51, v51, v43\n"
        "v_mfma_f32_16x16x32_bf16 v[80:83], a[56:59], v[44:47], v[80:83]\n"
        "v_add_f32 v52, v52, v43\n"
        "v_add_f32 v53, v53, v43\n"
        "v_mfma_f32_16x16x32_bf16 v[84:87], a[68:71], v[44:47], 0\n"
        "v_add_f32 v54, v54, v43\n"
        "v_add_f32 v55, v55, v43\n"
        "v_mfma_f32_16x16x32_bf16 v[84:87], a[64:67], v[44:47], v[84:87]\n"
        "v_add_f32 v56, v56, v43\n"
        "v_add_f32 v57, v57, v43\n"
        "v_mfma_f32_16x16x32_bf16 v[84:87], a[60:63], v[44:47], v[84:87]\n"
        "v_add_f32 v58, v58, v43\n"
        "v_add_f32 v59, v59, v43\n"
        "v_mfma_f32_16x16x32_bf16 v[44:47], a[144:147], a[84:87], 0\n"
        "v_add_f32 v60, v60, v43\n"
        "v_add_f32 v61, v61, v43\n"
        "v_mfma_f32_16x16x32_bf16 v[44:47], a[148:151], a[88:91], v[44:47]\n"
        "v_add_f32 v62, v62, v43\n"
        "v_add_f32 v63, v63, v43\n"
        "v_mfma_f32_16x16x32_bf16 v[44:47], a[152:155], a[92:95], v[44:47]\n"
        "v_add_f32 v64, v64, v43\n"
        "v_add_f32 v65, v65, v43\n"
        "v_mfma_f32_16x16x32_bf16 v[88:91], a[164:167], a[92:95], 0\n"
        "v_add_f32 v66, v66, v43\n"
        "v_add_f32 v67, v67, v43\n"
        "v_mfma_f32_16x16x32_bf16 v[88:91], a[160:163], a[88:91], v[88:91]\n"
        "buffer_load_dwordx4 v[188:191], v35, s[36:39], 0 offen offset:16\n"
        "v_cvt_pk_bf16_f32 v92, v30, v31\n"
        "v_mfma_f32_16x16x32_bf16 v[88:91], a[156:159], a[84:87], v[88:91]\n"
        "v_cvt_pk_bf16_f32 v93, v32, v33\n"
        "v_cvt_pk_bf16_f32 v94, v48, v49\n"
        "v_mfma_f32_16x16x32_bf16 v[96:99], a[168:171], a[84:87], 0\n"
        "v_cvt_pk_bf16_f32 v95, v50, v51\n"
        "v_cvt_pk_bf16_f32 v30, v52, v53\n"
        "v_mfma_f32_16x16x32_bf16 v[96:99], a[172:175], a[88:91], v[96:99]\n"
        "ds_write_b128 v37, v[92:95]\n"
        "v_mfma_f32_16x16x32_bf16 v[96:99], a[176:179], a[92:95], v[96:99]\n"
        "v_cvt_pk_bf16_f32 v31, v54, v55\n"
        "v_cvt_pk_bf16_f32 v32, v56, v57\n"
        "v_mfma_f32_16x16x32_bf16 v[48:51], a[188:191], a[92:95], 0\n"
        "v_cvt_pk_bf16_f32 v33, v58, v59\n"
        "v_cvt_pk_bf16_f32 v52, v60, v61\n"
        "v_mfma_f32_16x16x32_bf16 v[48:51], a[184:187], a[88:91], v[48:51]\n"
        "ds_write_b128 v37, v[30:33] offset:1024\n"
        "v_mfma_f32_16x16x32_bf16 v[48:51], a[180:183], a[84:87], v[48:51]\n"
        "v_cvt_pk_bf16_f32 v53, v62, v63\n"
        "v_cvt_pk_bf16_f32 v54, v64, v65\n"
        "v_mfma_f32_16x16x32_bf16 v[56:59], a[192:195], a[84:87], 0\n"
        "v_cvt_pk_bf16_f32 v55, v66, v67\n"
        "v_cvt_pk_bf16_f32 v30, v72, v73\n"
        "v_mfma_f32_16x16x32_bf16 v[56:59], a[196:199], a[88:91], v[56:59]\n"
        "ds_write_b128 v37, v[52:55] offset:2048\n"
        "v_mfma_f32_16x16x32_bf16 v[56:59], a[200:203], a[92:95], v[56:59]\n"
        "buffer_load_dwordx4 v[194:197], v35, s[36:39], 0 offen offset:32\n"
        "v_cvt_pk_bf16_f32 v31, v74, v75\n"
        "v_mfma_f32_16x16x32_bf16 v[32:35], a[212:215], a[92:95], 0\n"
        "v_cvt_pk_bf16_f32 v52, v76, v77\n"
        "v_cvt_pk_bf16_f32 v53, v78, v79\n"
        "v_mfma_f32_16x16x32_bf16 v[32:35], a[208:211], a[88:91], v[32:35]\n"
        "v_cvt_pk_bf16_f32 v54, v68, v69\n"
        "v_cvt_pk_bf16_f32 v55, v70, v71\n"
        "v_mfma_f32_16x16x32_bf16 v[32:35], a[204:207], a[84:87], v[32:35]\n"
        "v_cvt_pk_bf16_f32 v60, v80, v81\n"
        "v_cvt_pk_bf16_f32 v61, v82, v83\n"
        "v_mfma_f32_16x16x32_bf16 v[64:67], a[144:147], v[28:31], 0\n"
        "v_cvt_pk_bf16_f32 v62, v84, v85\n"
        "v_cvt_pk_bf16_f32 v63, v86, v87\n"
        "v_mfma_f32_16x16x32_bf16 v[64:67], a[148:151], v[52:55], v[64:67]\n"
        "v_add_f32 v37, v44, v45\n"
        "v_add_f32 v37, v37, v46\n"
        "v_mfma_f32_16x16x32_bf16 v[64:67], a[152:155], v[60:63], v[64:67]\n"
        "v_add_f32 v37, v37, v47\n"
        "v_add_f32 v37, v37, v88\n"
        "v_mfma_f32_16x16x32_bf16 v[68:71], a[164:167], v[60:63], 0\n"
        "s_add_u32 m0, s52, 0\n"
        "v_add_f32 v37, v37, v89\n"
        "v_add_f32 v37, v37, v90\n"
        "v_mfma_f32_16x16x32_bf16 v[68:71], a[160:163], v[52:55], v[68:71]\n"
        "buffer_load_dwordx4 v0, s[36:39], s49 offen lds\n"
        "v_add_f32 v37, v37, v91\n"
        "v_mfma_f32_16x16x32_bf16 v[68:71], a[156:159], v[28:31], v[68:71]\n"
        "v_add_f32 v37, v37, v96\n"
        "v_add_f32 v37, v37, v97\n"
        "v_mfma_f32_16x16x32_bf16 v[72:75], a[168:171], v[28:31], 0\n"
        "v_add_f32 v37, v37, v98\n"
        "v_add_f32 v37, v37, v99\n"
        "v_mfma_f32_16x16x32_bf16 v[72:75], a[172:175], v[52:55], v[72:75]\n"
        "v_add_f32 v37, v37, v48\n"
        "v_add_f32 v37, v37, v49\n"
        "v_mfma_f32_16x16x32_bf16 v[72:75], a[176:179], v[60:63], v[72:75]\n"
        "v_add_f32 v37, v37, v50\n"
        "v_add_f32 v37, v37, v51\n"
        "v_mfma_f32_16x16x32_bf16 v[76:79], a[188:191], v[60:63], 0\n"
        "v_add_f32 v37, v37, v56\n"
        "v_add_f32 v37, v37, v57\n"
        "v_mfma_f32_16x16x32_bf16 v[76:79], a[184:187], v[52:55], v[76:79]\n"
        "buffer_load_dwordx4 v1, s[36:39], s49 offen offset:1024 lds\n"
        "v_add_f32 v37, v37, v58\n"
        "v_mfma_f32_16x16x32_bf16 v[76:79], a[180:183], v[28:31], v[76:79]\n"
        "v_add_f32 v37, v37, v59\n"
        "v_add_f32 v37, v37, v32\n"
        "v_mfma_f32_16x16x32_bf16 v[80:83], a[192:195], v[28:31], 0\n"
        "v_add_f32 v37, v37, v33\n"
        "v_add_f32 v37, v37, v34\n"
        "v_mfma_f32_16x16x32_bf16 v[80:83], a[196:199], v[52:55], v[80:83]\n"
        "v_add_f32 v37, v37, v35\n"
        "v_mov_b32 v43, v37\n"
        "v_mfma_f32_16x16x32_bf16 v[80:83], a[200:203], v[60:63], v[80:83]\n"
        "s_nop 0\n"
        "v_permlane32_swap_b32 v37, v43\n"
        "v_mfma_f32_16x16x32_bf16 v[84:87], a[212:215], v[60:63], 0\n"
        "v_add_f32 v37, v37, v43\n"
        "v_mov_b32 v43, v37\n"
        "v_mfma_f32_16x16x32_bf16 v[84:87], a[208:211], v[52:55], v[84:87]\n"
        "buffer_load_dwordx4 v2, s[36:39], s49 offen offset:2048 lds\n"
        "v_add_f32 v132, v64, v65\n"
        "v_mfma_f32_16x16x32_bf16 v[84:87], a[204:207], v[28:31], v[84:87]\n"
        "v_permlane16_swap_b32 v37, v43\n"
        "v_mfma_f32_16x16x32_bf16 v[28:31], a[144:147], a[96:99], 0\n"
        "v_add_f32 v37, v37, v43\n"
        "v_add_f32 v132, v132, v66\n"
        "v_mfma_f32_16x16x32_bf16 v[28:31], a[148:151], a[100:103], v[28:31]\n"
        "v_mul_f32 v37, 0xbc2aaaab, v37\n"
        "v_add_f32 v132, v132, v67\n"
        "v_mfma_f32_16x16x32_bf16 v[28:31], a[152:155], a[104:107], v[28:31]\n"
        "v_add_f32 v44, v44, v37\n"
        "v_add_f32 v45, v45, v37\n"
        "v_mfma_f32_16x16x32_bf16 v[52:55], a[164:167], a[104:107], 0\n"
        "v_add_f32 v46, v46, v37\n"
        "v_add_f32 v47, v47, v37\n"
        "v_mfma_f32_16x16x32_bf16 v[52:55], a[160:163], a[100:103], v[52:55]\n"
        "buffer_load_dwordx4 v3, s[36:39], s49 offen offset:3072 lds\n"
        "v_add_f32 v88, v88, v37\n"
        "v_mfma_f32_16x16x32_bf16 v[52:55], a[156:159], a[96:99], v[52:55]\n"
        "v_add_f32 v89, v89, v37\n"
        "v_add_f32 v90, v90, v37\n"
        "v_mfma_f32_16x16x32_bf16 v[60:63], a[168:171], a[96:99], 0\n"
        "v_add_f32 v91, v91, v37\n"
        "v_add_f32 v96, v96, v37\n"
        "v_mfma_f32_16x16x32_bf16 v[60:63], a[172:175], a[100:103], v[60:63]\n"
        "v_add_f32 v97, v97, v37\n"
        "v_add_f32 v98, v98, v37\n"
        "v_mfma_f32_16x16x32_bf16 v[60:63], a[176:179], a[104:107], v[60:63]\n"
        "v_add_f32 v99, v99, v37\n"
        "v_add_f32 v48, v48, v37\n"
        "v_mfma_f32_16x16x32_bf16 v[92:95], a[188:191], a[104:107], 0\n"
        "s_add_u32 m0, s52, 4096\n"
        "v_add_f32 v49, v49, v37\n"
        "v_add_f32 v50, v50, v37\n"
        "v_mfma_f32_16x16x32_bf16 v[92:95], a[184:187], a[100:103], v[92:95]\n"
        "buffer_load_dwordx4 v4, s[36:39], s49 offen lds\n"
        "v_add_f32 v51, v51, v37\n"
        "v_mfma_f32_16x16x32_bf16 v[92:95], a[180:183], a[96:99], v[92:95]\n"
        "v_add_f32 v56, v56, v37\n"
        "v_add_f32 v57, v57, v37\n"
        "v_mfma_f32_16x16x32_bf16 v[100:103], a[192:195], a[96:99], 0\n"
        "v_add_f32 v58, v58, v37\n"
        "v_add_f32 v59, v59, v37\n"
        "v_mfma_f32_16x16x32_bf16 v[100:103], a[196:199], a[100:103], v[100:103]\n"
        "v_add_f32 v32, v32, v37\n"
        "v_add_f32 v33, v33, v37\n"
        "v_mfma_f32_16x16x32_bf16 v[100:103], a[200:203], a[104:107], v[100:103]\n"
        "v_add_f32 v34, v34, v37\n"
        "v_add_f32 v35, v35, v37\n"
        "v_mfma_f32_16x16x32_bf16 v[104:107], a[212:215], a[104:107], 0\n"
        "v_cvt_pk_bf16_f32 v108, v44, v45\n"
        "v_cvt_pk_bf16_f32 v109, v46, v47\n"
        "v_mfma_f32_16x16x32_bf16 v[104:107], a[208:211], a[100:103], v[104:107]\n"
        "buffer_load_dwordx4 v5, s[36:39], s49 offen offset:1024 lds\n"
        "v_cvt_pk_bf16_f32 v110, v88, v89\n"
        "v_mfma_f32_16x16x32_bf16 v[104:107], a[204:207], a[96:99], v[104:107]\n"
        "v_cvt_pk_bf16_f32 v111, v90, v91\n"
        "v_cvt_pk_bf16_f32 v44, v96, v97\n"
        "v_mfma_f32_16x16x32_bf16 v[88:91], a[144:147], a[108:111], 0\n"
        "ds_write_b128 v38, v[108:111]\n"
        "v_mfma_f32_16x16x32_bf16 v[88:91], a[148:151], a[112:115], v[88:91]\n"
        "v_cvt_pk_bf16_f32 v45, v98, v99\n"
        "v_cvt_pk_bf16_f32 v46, v48, v49\n"
        "v_mfma_f32_16x16x32_bf16 v[88:91], a[152:155], a[116:119], v[88:91]\n"
        "v_cvt_pk_bf16_f32 v47, v50, v51\n"
        "v_cvt_pk_bf16_f32 v48, v56, v57\n"
        "v_mfma_f32_16x16x32_bf16 v[96:99], a[164:167], a[116:119], 0\n"
        "ds_write_b128 v38, v[44:47] offset:1024\n"
        "v_mfma_f32_16x16x32_bf16 v[96:99], a[160:163], a[112:115], v[96:99]\n"
        "buffer_load_dwordx4 v6, s[36:39], s49 offen offset:2048 lds\n"
        "v_cvt_pk_bf16_f32 v49, v58, v59\n"
        "v_mfma_f32_16x16x32_bf16 v[96:99], a[156:159], a[108:111], v[96:99]\n"
        "v_cvt_pk_bf16_f32 v50, v32, v33\n"
        "v_cvt_pk_bf16_f32 v51, v34, v35\n"
        "v_mfma_f32_16x16x32_bf16 v[32:35], a[168:171], a[108:111], 0\n"
        "ds_write_b128 v38, v[48:51] offset:2048\n"
        "v_mfma_f32_16x16x32_bf16 v[32:35], a[172:175], a[112:115], v[32:35]\n"
        "v_add_f32 v132, v132, v68\n"
        "v_add_f32 v132, v132, v69\n"
        "v_mfma_f32_16x16x32_bf16 v[32:35], a[176:179], a[116:119], v[32:35]\n"
        "v_add_f32 v132, v132, v70\n"
        "v_add_f32 v132, v132, v71\n"
        "v_mfma_f32_16x16x32_bf16 v[44:47], a[188:191], a[116:119], 0\n"
        "v_add_f32 v132, v132, v72\n"
        "v_add_f32 v132, v132, v73\n"
        "v_mfma_f32_16x16x32_bf16 v[44:47], a[184:187], a[112:115], v[44:47]\n"
        "buffer_load_dwordx4 v7, s[36:39], s49 offen offset:3072 lds\n"
        "v_add_f32 v132, v132, v74\n"
        "v_mfma_f32_16x16x32_bf16 v[44:47], a[180:183], a[108:111], v[44:47]\n"
        "v_add_f32 v132, v132, v75\n"
        "v_add_f32 v132, v132, v76\n"
        "v_mfma_f32_16x16x32_bf16 v[48:51], a[192:195], a[108:111], 0\n"
        "v_add_f32 v132, v132, v77\n"
        "v_add_f32 v132, v132, v78\n"
        "v_mfma_f32_16x16x32_bf16 v[48:51], a[196:199], a[112:115], v[48:51]\n"
        "v_add_f32 v132, v132, v79\n"
        "v_add_f32 v132, v132, v80\n"
        "v_mfma_f32_16x16x32_bf16 v[48:51], a[200:203], a[116:119], v[48:51]\n"
        "v_add_f32 v132, v132, v81\n"
        "v_add_f32 v132, v132, v82\n"
        "v_mfma_f32_16x16x32_bf16 v[56:59], a[212:215], a[116:119], 0\n"
        "s_add_u32 m0, s52, 8192\n"
        "v_add_f32 v132, v132, v83\n"
        "v_add_f32 v132, v132, v84\n"
        "v_mfma_f32_16x16x32_bf16 v[56:59], a[208:211], a[112:115], v[56:59]\n"
        "buffer_load_dwordx4 v8, s[36:39], s49 offen lds\n"
        "v_add_f32 v132, v132, v85\n"
        "v_mfma_f32_16x16x32_bf16 v[56:59], a[204:207], a[108:111], v[56:59]\n"
        "v_add_f32 v132, v132, v86\n"
        "v_add_f32 v132, v132, v87\n"
        "v_mfma_f32_16x16x32_bf16 v[108:111], a[144:147], a[120:123], 0\n"
        "v_mov_b32 v37, v132\n"
        "v_add_f32 v38, v28, v29\n"
        "v_mfma_f32_16x16x32_bf16 v[108:111], a[148:151], a[124:127], v[108:111]\n"
        "v_permlane32_swap_b32 v132, v37\n"
        "v_mfma_f32_16x16x32_bf16 v[108:111], a[152:155], a[128:131], v[108:111]\n"
        "v_add_f32 v132, v132, v37\n"
        "v_mov_b32 v37, v132\n"
        "v_mfma_f32_16x16x32_bf16 v[112:115], a[164:167], a[128:131], 0\n"
        "s_nop 0\n"
        "v_permlane16_swap_b32 v132, v37\n"
        "v_mfma_f32_16x16x32_bf16 v[112:115], a[160:163], a[124:127], v[112:115]\n"
        "v_add_f32 v132, v132, v37\n"
        "v_add_f32 v38, v38, v30\n"
        "v_mfma_f32_16x16x32_bf16 v[112:115], a[156:159], a[120:123], v[112:115]\n"
        "v_mul_f32 v132, 0xbc2aaaab, v132\n"
        "v_add_f32 v38, v38, v31\n"
        "v_mfma_f32_16x16x32_bf16 v[116:119], a[168:171], a[120:123], 0\n"
        "v_add_f32 v64, v64, v132\n"
        "v_add_f32 v65, v65, v132\n"
        "v_mfma_f32_16x16x32_bf16 v[116:119], a[172:175], a[124:127], v[116:119]\n"
        "v_add_f32 v66, v66, v132\n"
        "v_add_f32 v67, v67, v132\n"
        "v_mfma_f32_16x16x32_bf16 v[116:119], a[176:179], a[128:131], v[116:119]\n"
        "v_add_f32 v68, v68, v132\n"
        "v_add_f32 v69, v69, v132\n"
        "v_mfma_f32_16x16x32_bf16 v[120:123], a[188:191], a[128:131], 0\n"
        "v_add_f32 v70, v70, v132\n"
        "v_add_f32 v71, v71, v132\n"
        "v_mfma_f32_16x16x32_bf16 v[120:123], a[184:187], a[124:127], v[120:123]\n"
        "v_add_f32 v72, v72, v132\n"
        "v_add_f32 v73, v73, v132\n"
        "v_mfma_f32_16x16x32_bf16 v[120:123], a[180:183], a[120:123], v[120:123]\n"
        "v_add_f32 v74, v74, v132\n"
        "v_add_f32 v75, v75, v132\n"
        "v_mfma_f32_16x16x32_bf16 v[124:127], a[192:195], a[120:123], 0\n"
        "v_add_f32 v76, v76, v132\n"
        "v_add_f32 v77, v77, v132\n"
        "v_mfma_f32_16x16x32_bf16 v[124:127], a[196:199], a[124:127], v[124:127]\n"
        "v_add_f32 v78, v78, v132\n"
        "v_add_f32 v79, v79, v132\n"
        "v_mfma_f32_16x16x32_bf16 v[124:127], a[200:203], a[128:131], v[124:127]\n"
        "v_add_f32 v80, v80, v132\n"
        "v_add_f32 v81, v81, v132\n"
        "v_mfma_f32_16x16x32_bf16 v[128:131], a[212:215], a[128:131], 0\n"
        "v_add_f32 v82, v82, v132\n"
        "v_add_f32 v83, v83, v132\n"
        "v_mfma_f32_16x16x32_bf16 v[128:131], a[208:211], a[124:127], v[128:131]\n"
        "v_add_f32 v84, v84, v132\n"
        "v_add_f32 v85, v85, v132\n"
        "v_mfma_f32_16x16x32_bf16 v[128:131], a[204:207], a[120:123], v[128:131]\n"
        "v_add_f32 v86, v86, v132\n"
        "v_add_f32 v87, v87, v132\n"
        "v_mfma_f32_16x16x32_bf16 v[132:135], a[144:147], a[132:135], 0\n"
        "v_cvt_pk_bf16_f32 v136, v64, v65\n"
        "v_cvt_pk_bf16_f32 v137, v66, v67\n"
        "v_mfma_f32_16x16x32_bf16 v[132:135], a[148:151], a[136:139], v[132:135]\n"
        "v_cvt_pk_bf16_f32 v138, v68, v69\n"
        "v_cvt_pk_bf16_f32 v139, v70, v71\n"
        "v_mfma_f32_16x16x32_bf16 v[132:135], a[152:155], a[140:143], v[132:135]\n"
        "ds_write_b128 v36, v[136:139]\n"
        "v_mfma_f32_16x16x32_bf16 v[64:67], a[164:167], a[140:143], 0\n"
        "v_cvt_pk_bf16_f32 v68, v72, v73\n"
        "v_cvt_pk_bf16_f32 v69, v74, v75\n"
        "v_mfma_f32_16x16x32_bf16 v[64:67], a[160:163], a[136:139], v[64:67]\n"
        "v_cvt_pk_bf16_f32 v70, v76, v77\n"
        "v_cvt_pk_bf16_f32 v71, v78, v79\n"
        "v_mfma_f32_16x16x32_bf16 v[64:67], a[156:159], a[132:135], v[64:67]\n"
        "ds_write_b128 v36, v[68:71] offset:64\n"
        "v_mfma_f32_16x16x32_bf16 v[68:71], a[168:171], a[132:135], 0\n"
        "v_cvt_pk_bf16_f32 v72, v80, v81\n"
        "v_cvt_pk_bf16_f32 v73, v82, v83\n"
        "v_mfma_f32_16x16x32_bf16 v[68:71], a[172:175], a[136:139], v[68:71]\n"
        "v_cvt_pk_bf16_f32 v74, v84, v85\n"
        "v_cvt_pk_bf16_f32 v75, v86, v87\n"
        "v_mfma_f32_16x16x32_bf16 v[68:71], a[176:179], a[140:143], v[68:71]\n"
        "ds_write_b128 v36, v[72:75] offset:128\n"
        "v_mfma_f32_16x16x32_bf16 v[72:75], a[188:191], a[140:143], 0\n"
        "v_add_f32 v38, v38, v52\n"
        "v_add_f32 v38, v38, v53\n"
        "v_mfma_f32_16x16x32_bf16 v[72:75], a[184:187], a[136:139], v[72:75]\n"
        "v_add_f32 v38, v38, v54\n"
        "v_add_f32 v38, v38, v55\n"
        "v_mfma_f32_16x16x32_bf16 v[72:75], a[180:183], a[132:135], v[72:75]\n"
        "v_add_f32 v38, v38, v60\n"
        "v_add_f32 v38, v38, v61\n"
        "v_mfma_f32_16x16x32_bf16 v[76:79], a[192:195], a[132:135], 0\n"
        "v_add_f32 v38, v38, v62\n"
        "v_add_f32 v38, v38, v63\n"
        "v_mfma_f32_16x16x32_bf16 v[76:79], a[196:199], a[136:139], v[76:79]\n"
        "v_add_f32 v38, v38, v92\n"
        "v_add_f32 v38, v38, v93\n"
        "v_mfma_f32_16x16x32_bf16 v[76:79], a[200:203], a[140:143], v[76:79]\n"
        "v_add_f32 v38, v38, v94\n"
        "v_add_f32 v38, v38, v95\n"
        "v_mfma_f32_16x16x32_bf16 v[80:83], a[212:215], a[140:143], 0\n"
        "v_add_f32 v38, v38, v100\n"
        "v_add_f32 v38, v38, v101\n"
        "v_mfma_f32_16x16x32_bf16 v[80:83], a[208:211], a[136:139], v[80:83]\n"
        "v_add_f32 v38, v38, v102\n"
        "v_add_f32 v38, v38, v103\n"
        "v_mfma_f32_16x16x32_bf16 v[80:83], a[204:207], a[132:135], v[80:83]\n"
        "v_add_f32 v38, v38, v104\n"
        "v_add_f32 v38, v38, v105\n"
        "v_add_f32 v38, v38, v106\n"
        "v_add_f32 v38, v38, v107\n"
        "v_mov_b32 v36, v38\n"
        "v_add_f32 v37, v88, v89\n"
        "v_add_f32 v37, v37, v90\n"
        "v_permlane32_swap_b32 v38, v36\n"
        "v_add_f32 v38, v38, v36\n"
        "v_mov_b32 v36, v38\n"
        "v_add_f32 v37, v37, v91\n"
        "v_add_f32 v37, v37, v96\n"
        "v_permlane16_swap_b32 v38, v36\n"
        "v_add_f32 v38, v38, v36\n"
        "v_mul_f32 v38, 0xbc2aaaab, v38\n"
        "v_add_f32 v28, v28, v38\n"
        "v_add_f32 v29, v29, v38\n"
        "v_add_f32 v30, v30, v38\n"
        "v_add_f32 v31, v31, v38\n"
        "v_add_f32 v52, v52, v38\n"
        "v_add_f32 v53, v53, v38\n"
        "v_add_f32 v54, v54, v38\n"
        "v_add_f32 v55, v55, v38\n"
        "v_add_f32 v60, v60, v38\n"
        "v_add_f32 v61, v61, v38\n"
        "v_add_f32 v62, v62, v38\n"
        "v_add_f32 v63, v63, v38\n"
        "v_add_f32 v92, v92, v38\n"
        "v_add_f32 v93, v93, v38\n"
        "v_add_f32 v94, v94, v38\n"
        "v_add_f32 v95, v95, v38\n"
        "v_add_f32 v100, v100, v38\n"
        "v_add_f32 v101, v101, v38\n"
        "v_add_f32 v102, v102, v38\n"
        "v_add_f32 v103, v103, v38\n"
        "v_add_f32 v104, v104, v38\n"
        "v_add_f32 v105, v105, v38\n"
        "v_add_f32 v106, v106, v38\n"
        "v_add_f32 v107, v107, v38\n"
        "v_cvt_pk_bf16_f32 v84, v28, v29\n"
        "v_cvt_pk_bf16_f32 v85, v30, v31\n"
        "v_cvt_pk_bf16_f32 v86, v52, v53\n"
        "v_cvt_pk_bf16_f32 v87, v54, v55\n"
        "v_cvt_pk_bf16_f32 v28, v60, v61\n"
        "v_cvt_pk_bf16_f32 v29, v62, v63\n"
        "v_cvt_pk_bf16_f32 v30, v92, v93\n"
        "v_cvt_pk_bf16_f32 v31, v94, v95\n"
        "v_cvt_pk_bf16_f32 v52, v100, v101\n"
        "v_cvt_pk_bf16_f32 v53, v102, v103\n"
        "v_cvt_pk_bf16_f32 v54, v104, v105\n"
        "v_cvt_pk_bf16_f32 v55, v106, v107\n"
        "ds_write_b128 v39, v[84:87]\n"
        "ds_write_b128 v39, v[28:31] offset:1024\n"
        "ds_write_b128 v39, v[52:55] offset:2048\n"
        "v_add_f32 v37, v37, v97\n"
        "v_add_f32 v37, v37, v98\n"
        "v_add_f32 v37, v37, v99\n"
        "v_add_f32 v37, v37, v32\n"
        "v_add_f32 v37, v37, v33\n"
        "v_add_f32 v37, v37, v34\n"
        "v_add_f32 v37, v37, v35\n"
        "v_add_f32 v37, v37, v44\n"
        "v_add_f32 v37, v37, v45\n"
        "v_add_f32 v37, v37, v46\n"
        "v_add_f32 v37, v37, v47\n"
        "v_add_f32 v37, v37, v48\n"
        "v_add_f32 v37, v37, v49\n"
        "v_add_f32 v37, v37, v50\n"
        "v_add_f32 v37, v37, v51\n"
        "v_add_f32 v37, v37, v56\n"
        "v_add_f32 v37, v37, v57\n"
        "v_add_f32 v37, v37, v58\n"
        "v_add_f32 v37, v37, v59\n"
        "v_mov_b32 v28, v37\n"
        "v_add_f32 v43, v108, v109\n"
        "v_add_f32 v43, v43, v110\n"
        "v_permlane32_swap_b32 v37, v28\n"
        "v_add_f32 v37, v37, v28\n"
        "v_mov_b32 v28, v37\n"
        "v_add_f32 v43, v43, v111\n"
        "v_add_f32 v43, v43, v112\n"
        "v_permlane16_swap_b32 v37, v28\n"
        "v_add_f32 v37, v37, v28\n"
        "v_mul_f32 v37, 0xbc2aaaab, v37\n"
        "v_add_f32 v88, v88, v37\n"
        "v_add_f32 v89, v89, v37\n"
        "v_add_f32 v90, v90, v37\n"
        "v_add_f32 v91, v91, v37\n"
        "v_add_f32 v96, v96, v37\n"
        "v_add_f32 v97, v97, v37\n"
        "v_add_f32 v98, v98, v37\n"
        "v_add_f32 v99, v99, v37\n"
        "v_add_f32 v32, v32, v37\n"
        "v_add_f32 v33, v33, v37\n"
        "v_add_f32 v34, v34, v37\n"
        "v_add_f32 v35, v35, v37\n"
        "v_add_f32 v44, v44, v37\n"
        "v_add_f32 v45, v45, v37\n"
        "v_add_f32 v46, v46, v37\n"
        "v_add_f32 v47, v47, v37\n"
        "v_add_f32 v48, v48, v37\n"
        "v_add_f32 v49, v49, v37\n"
        "v_add_f32 v50, v50, v37\n"
        "v_add_f32 v51, v51, v37\n"
        "v_add_f32 v56, v56, v37\n"
        "v_add_f32 v57, v57, v37\n"
        "v_add_f32 v58, v58, v37\n"
        "v_add_f32 v59, v59, v37\n"
        "v_cvt_pk_bf16_f32 v28, v88, v89\n"
        "v_cvt_pk_bf16_f32 v29, v90, v91\n"
        "v_cvt_pk_bf16_f32 v30, v96, v97\n"
        "v_cvt_pk_bf16_f32 v31, v98, v99\n"
        "v_cvt_pk_bf16_f32 v36, v32, v33\n"
        "v_cvt_pk_bf16_f32 v37, v34, v35\n"
        "v_cvt_pk_bf16_f32 v38, v44, v45\n"
        "v_cvt_pk_bf16_f32 v39, v46, v47\n"
        "v_cvt_pk_bf16_f32 v32, v48, v49\n"
        "v_cvt_pk_bf16_f32 v33, v50, v51\n"
        "v_cvt_pk_bf16_f32 v34, v56, v57\n"
        "v_cvt_pk_bf16_f32 v35, v58, v59\n"
        "ds_write_b128 v40, v[28:31]\n"
        "ds_write_b128 v40, v[36:39] offset:1024\n"
        "ds_write_b128 v40, v[32:35] offset:2048\n"
        "v_add_f32 v43, v43, v113\n"
        "v_add_f32 v43, v43, v114\n"
        "v_add_f32 v43, v43, v115\n"
        "v_add_f32 v43, v43, v116\n"
        "v_add_f32 v43, v43, v117\n"
        "v_add_f32 v43, v43, v118\n"
        "v_add_f32 v43, v43, v119\n"
        "v_add_f32 v43, v43, v120\n"
        "v_add_f32 v43, v43, v121\n"
        "v_add_f32 v43, v43, v122\n"
        "v_add_f32 v43, v43, v123\n"
        "v_add_f32 v43, v43, v124\n"
        "v_add_f32 v43, v43, v125\n"
        "v_add_f32 v43, v43, v126\n"
        "v_add_f32 v43, v43, v127\n"
        "v_add_f32 v43, v43, v128\n"
        "v_add_f32 v43, v43, v129\n"
        "v_add_f32 v43, v43, v130\n"
        "v_add_f32 v43, v43, v131\n"
        "v_mov_b32 v28, v43\n"
        "v_add_f32 v40, v132, v133\n"
        "v_add_f32 v40, v40, v134\n"
        "v_permlane32_swap_b32 v43, v28\n"
        "v_add_f32 v43, v43, v28\n"
        "v_mov_b32 v28, v43\n"
        "v_add_f32 v40, v40, v135\n"
        "v_add_f32 v40, v40, v64\n"
        "v_permlane16_swap_b32 v43, v28\n"
        "v_add_f32 v43, v43, v28\n"
        "v_mul_f32 v43, 0xbc2aaaab, v43\n"
        "v_add_f32 v108, v108, v43\n"
        "v_add_f32 v109, v109, v43\n"
        "v_add_f32 v110, v110, v43\n"
        "v_add_f32 v111, v111, v43\n"
        "v_add_f32 v112, v112, v43\n"
        "v_add_f32 v113, v113, v43\n"
        "v_add_f32 v114, v114, v43\n"
        "v_add_f32 v115, v115, v43\n"
        "v_add_f32 v116, v116, v43\n"
        "v_add_f32 v117, v117, v43\n"
        "v_add_f32 v118, v118, v43\n"
        "v_add_f32 v119, v119, v43\n"
        "v_add_f32 v120, v120, v43\n"
        "v_add_f32 v121, v121, v43\n"
        "v_add_f32 v122, v122, v43\n"
        "v_add_f32 v123, v123, v43\n"
        "v_add_f32 v124, v124, v43\n"
        "v_add_f32 v125, v125, v43\n"
        "v_add_f32 v126, v126, v43\n"
        "v_add_f32 v127, v127, v43\n"
        "v_add_f32 v128, v128, v43\n"
        "v_add_f32 v129, v129, v43\n"
        "v_add_f32 v130, v130, v43\n"
        "v_add_f32 v131, v131, v43\n"
        "v_cvt_pk_bf16_f32 v28, v108, v109\n"
        "v_cvt_pk_bf16_f32 v29, v110, v111\n"
        "v_cvt_pk_bf16_f32 v30, v112, v113\n"
        "v_cvt_pk_bf16_f32 v31, v114, v115\n"
        "v_cvt_pk_bf16_f32 v32, v116, v117\n"
        "v_cvt_pk_bf16_f32 v33, v118, v119\n"
        "v_cvt_pk_bf16_f32 v34, v120, v121\n"
        "v_cvt_pk_bf16_f32 v35, v122, v123\n"
        "v_cvt_pk_bf16_f32 v36, v124, v125\n"
        "v_cvt_pk_bf16_f32 v37, v126, v127\n"
        "v_cvt_pk_bf16_f32 v38, v128, v129\n"
        "v_cvt_pk_bf16_f32 v39, v130, v131\n"
        "ds_write_b128 v41, v[28:31]\n"
        "ds_write_b128 v41, v[32:35] offset:1024\n"
        "ds_write_b128 v41, v[36:39] offset:2048\n"
        "v_add_f32 v40, v40, v65\n"
        "v_add_f32 v40, v40, v66\n"
        "v_add_f32 v40, v40, v67\n"
        "v_add_f32 v40, v40, v68\n"
        "v_add_f32 v40, v40, v69\n"
        "v_add_f32 v40, v40, v70\n"
        "v_add_f32 v40, v40, v71\n"
        "v_add_f32 v40, v40, v72\n"
        "v_add_f32 v40, v40, v73\n"
        "v_add_f32 v40, v40, v74\n"
        "v_add_f32 v40, v40, v75\n"
        "v_add_f32 v40, v40, v76\n"
        "v_add_f32 v40, v40, v77\n"
        "v_add_f32 v40, v40, v78\n"
        "v_add_f32 v40, v40, v79\n"
        "v_add_f32 v40, v40, v80\n"
        "v_add_f32 v40, v40, v81\n"
        "v_add_f32 v40, v40, v82\n"
        "v_add_f32 v40, v40, v83\n"
        "v_mov_b32 v28, v40\n"
        "s_waitcnt vmcnt(9)\n"
        "s_nop 0\n"
        "v_permlane32_swap_b32 v40, v28\n"
        "v_add_f32 v40, v40, v28\n"
        "v_mov_b32 v28, v40\n"
        "s_nop 1\n"
        "v_permlane16_swap_b32 v40, v28\n"
        "v_add_f32 v40, v40, v28\n"
        "v_mul_f32 v40, 0xbc2aaaab, v40\n"
        "v_add_f32 v132, v132, v40\n"
        "v_add_f32 v133, v133, v40\n"
        "v_add_f32 v134, v134, v40\n"
        "v_add_f32 v135, v135, v40\n"
        "v_add_f32 v64, v64, v40\n"
        "v_add_f32 v65, v65, v40\n"
        "v_add_f32 v66, v66, v40\n"
        "v_add_f32 v67, v67, v40\n"
        "v_add_f32 v68, v68, v40\n"
        "v_add_f32 v69, v69, v40\n"
        "v_add_f32 v70, v70, v40\n"
        "v_add_f32 v71, v71, v40\n"
        "v_add_f32 v72, v72, v40\n"
        "v_add_f32 v73, v73, v40\n"
        "v_add_f32 v74, v74, v40\n"
        "v_add_f32 v75, v75, v40\n"
        "v_add_f32 v76, v76, v40\n"
        "v_add_f32 v77, v77, v40\n"
        "v_add_f32 v78, v78, v40\n"
        "v_add_f32 v79, v79, v40\n"
        "v_add_f32 v80, v80, v40\n"
        "v_add_f32 v81, v81, v40\n"
        "v_add_f32 v82, v82, v40\n"
        "v_add_f32 v83, v83, v40\n"
        "v_cvt_pk_bf16_f32 v28, v132, v133\n"
        "v_cvt_pk_bf16_f32 v29, v134, v135\n"
        "v_cvt_pk_bf16_f32 v30, v64, v65\n"
        "v_cvt_pk_bf16_f32 v31, v66, v67\n"
        "v_cvt_pk_bf16_f32 v32, v68, v69\n"
        "v_cvt_pk_bf16_f32 v33, v70, v71\n"
        "v_cvt_pk_bf16_f32 v34, v72, v73\n"
        "v_cvt_pk_bf16_f32 v35, v74, v75\n"
        "v_cvt_pk_bf16_f32 v36, v76, v77\n"
        "v_cvt_pk_bf16_f32 v37, v78, v79\n"
        "v_cvt_pk_bf16_f32 v38, v80, v81\n"
        "v_cvt_pk_bf16_f32 v39, v82, v83\n"
        "ds_write_b128 v42, v[28:31]\n"
        "ds_write_b128 v42, v[32:35] offset:1024\n"
        "ds_write_b128 v42, v[36:39] offset:2048\n"
        "s_waitcnt lgkmcnt(0)\n"
        "s_barrier\n"
        "s_branch FE_%=\n"
        "FG_%=:\n"
        "s_mov_b32 s93, 0x471c4000\n"
        "v_mov_b32 v44, 0x3f803f80\n"
        "v_mov_b32 v45, 0x3f803f80\n"
        "v_mov_b32 v46, 0x3f803f80\n"
        "v_mov_b32 v47, 0x3f803f80\n"
        "s_waitcnt vmcnt(18)\n"
        "ds_read_b64_tr_b16 a[72:73], v32 offset:0\n"
        "ds_read_b64_tr_b16 a[74:75], v33 offset:0\n"
        "ds_read_b64_tr_b16 a[76:77], v32 offset:6144\n"
        "ds_read_b64_tr_b16 a[78:79], v33 offset:6144\n"
        "ds_read_b64_tr_b16 a[80:81], v32 offset:12288\n"
        "ds_read_b64_tr_b16 a[82:83], v33 offset:12288\n"
        "ds_read_b64_tr_b16 a[84:85], v32 offset:8\n"
        "ds_read_b64_tr_b16 a[86:87], v33 offset:8\n"
        "ds_read_b64_tr_b16 a[88:89], v32 offset:6152\n"
        "ds_read_b64_tr_b16 a[90:91], v33 offset:6152\n"
        "ds_read_b64_tr_b16 a[92:93], v32 offset:12296\n"
        "ds_read_b64_tr_b16 a[94:95], v33 offset:12296\n"
        "ds_read_b64_tr_b16 a[96:97], v32 offset:64\n"
        "ds_read_b64_tr_b16 a[98:99], v33 offset:64\n"
        "ds_read_b64_tr_b16 a[100:101], v32 offset:6208\n"
        "ds_read_b64_tr_b16 a[102:103], v33 offset:6208\n"
        "ds_read_b64_tr_b16 a[104:105], v32 offset:12352\n"
        "ds_read_b64_tr_b16 a[106:107], v33 offset:12352\n"
        "ds_read_b64_tr_b16 a[108:109], v32 offset:72\n"
        "ds_read_b64_tr_b16 a[110:111], v33 offset:72\n"
        "ds_read_b64_tr_b16 a[112:113], v32 offset:6216\n"
        "ds_read_b64_tr_b16 a[114:115], v33 offset:6216\n"
        "ds_read_b64_tr_b16 a[116:117], v32 offset:12360\n"
        "ds_read_b64_tr_b16 a[118:119], v33 offset:12360\n"
        "ds_read_b64_tr_b16 a[120:121], v32 offset:128\n"
        "ds_read_b64_tr_b16 a[122:123], v33 offset:128\n"
        "ds_read_b64_tr_b16 a[124:125], v32 offset:6272\n"
        "ds_read_b64_tr_b16 a[126:127], v33 offset:6272\n"
        "ds_read_b64_tr_b16 a[128:129], v32 offset:12416\n"
        "ds_read_b64_tr_b16 a[130:131], v33 offset:12416\n"
        "ds_read_b64_tr_b16 a[132:133], v32 offset:136\n"
        "ds_read_b64_tr_b16 a[134:135], v33 offset:136\n"
        "ds_read_b64_tr_b16 a[136:137], v32 offset:6280\n"
        "ds_read_b64_tr_b16 a[138:139], v33 offset:6280\n"
        "ds_read_b64_tr_b16 a[140:141], v32 offset:12424\n"
        "ds_read_b64_tr_b16 a[142:143], v33 offset:12424\n"
        "s_waitcnt lgkmcnt(15)\n"
        "s_waitcnt vmcnt(12)\n"
        "ds_read_b128 a[144:147], v28\n"
        "ds_read_b128 a[148:151], v28 offset:64\n"
        "ds_read_b128 a[152:155], v28 offset:128\n"
        "s_waitcnt lgkmcnt(0)\n"
        "v_mfma_f32_16x16x32_bf16 v[30:33], a[144:147], a[72:75], 0\n"
        "s_waitcnt vmcnt(6)\n"
        "ds_read_b128 a[156:159], v29\n"
        "v_mov_b32 v36, 161664\n"
        "v_mfma_f32_16x16x32_bf16 v[30:33], a[148:151], a[76:79], v[30:33]\n"
        "s_waitcnt vmcnt(0)\n"
        "ds_read_b128 a[160:163], v29 offset:64\n"
        "ds_read_b128 a[164:167], v29 offset:128\n"
        "v_mfma_f32_16x16x32_bf16 v[30:33], a[152:155], a[80:83], v[30:33]\n"
        "s_waitcnt lgkmcnt(3)\n"
        "ds_read_b128 a[168:171], v28 offset:6144\n"
        "ds_read_b128 a[172:175], v28 offset:6208\n"
        "s_waitcnt lgkmcnt(2)\n"
        "v_mfma_f32_16x16x32_bf16 v[44:47], a[164:167], a[80:83], 0\n"
        "s_barrier\n"
        "ds_read_b128 a[176:179], v28 offset:6272\n"
        "ds_read_b128 a[180:183], v29 offset:6144\n"
        "v_mfma_f32_16x16x32_bf16 v[44:47], a[160:163], a[76:79], v[44:47]\n"
        "s_waitcnt lgkmcnt(4)\n"
        "ds_read_b128 a[184:187], v29 offset:6208\n"
        "ds_read_b128 a[188:191], v29 offset:6272\n"
        "v_mfma_f32_16x16x32_bf16 v[44:47], a[156:159], a[72:75], v[44:47]\n"
        "s_waitcnt lgkmcnt(6)\n"
        "ds_read_b128 a[192:195], v28 offset:12288\n"
        "ds_read_b128 a[196:199], v28 offset:12352\n"
        "s_waitcnt lgkmcnt(5)\n"
        "v_mfma_f32_16x16x32_bf16 v[48:51], a[168:171], a[72:75], 0\n"
        "s_waitcnt lgkmcnt(5)\n"
        "ds_read_b128 a[200:203], v28 offset:12416\n"
        "ds_read_b128 a[204:207], v29 offset:12288\n"
        "v_mfma_f32_16x16x32_bf16 v[48:51], a[172:175], a[76:79], v[48:51]\n"
        "s_waitcnt lgkmcnt(7)\n"
        "ds_read_b128 a[208:211], v29 offset:12352\n"
        "ds_read_b128 a[212:215], v29 offset:12416\n"
        "v_mfma_f32_16x16x32_bf16 v[48:51], a[176:179], a[80:83], v[48:51]\n"
        "s_waitcnt lgkmcnt(6)\n"
        "buffer_load_dwordx4 v[162:165], v34, s[36:39], 0 offen\n"
        "v_add_f32 v28, v30, v31\n"
        "v_mfma_f32_16x16x32_bf16 v[52:55], a[188:191], a[80:83], 0\n"
        "s_waitcnt lgkmcnt(6)\n"
        "v_add_f32 v28, v28, v32\n"
        "v_add_f32 v28, v28, v33\n"
        "v_mfma_f32_16x16x32_bf16 v[52:55], a[184:187], a[76:79], v[52:55]\n"
        "v_add_f32 v28, v28, v44\n"
        "v_add_f32 v28, v28, v45\n"
        "v_mfma_f32_16x16x32_bf16 v[52:55], a[180:183], a[72:75], v[52:55]\n"
        "v_add_f32 v28, v28, v46\n"
        "v_add_f32 v28, v28, v47\n"
        "s_waitcnt lgkmcnt(3)\n"
        "v_mfma_f32_16x16x32_bf16 v[56:59], a[192:195], a[72:75], 0\n"
        "v_add_f32 v28, v28, v48\n"
        "v_add_f32 v28, v28, v49\n"
        "v_mfma_f32_16x16x32_bf16 v[56:59], a[196:199], a[76:79], v[56:59]\n"
        "s_waitcnt lgkmcnt(0)\n"
        "buffer_load_dwordx4 v[168:171], v34, s[36:39], 0 offen offset:16\n"
        "v_add_f32 v28, v28, v50\n"
        "v_mfma_f32_16x16x32_bf16 v[56:59], a[200:203], a[80:83], v[56:59]\n"
        "v_add_f32 v28, v28, v51\n"
        "v_add_f32 v28, v28, v52\n"
        "v_mfma_f32_16x16x32_bf16 v[60:63], a[212:215], a[80:83], 0\n"
        "v_add_f32 v28, v28, v53\n"
        "v_add_f32 v28, v28, v54\n"
        "v_mfma_f32_16x16x32_bf16 v[60:63], a[208:211], a[76:79], v[60:63]\n"
        "s_nop 0\n"
        "v_add_f32 v28, v28, v55\n"
        "v_add_f32 v28, v28, v56\n"
        "v_mfma_f32_16x16x32_bf16 v[60:63], a[204:207], a[72:75], v[60:63]\n"
        "s_nop 0\n"
        "v_add_f32 v28, v28, v57\n"
        "v_add_f32 v28, v28, v58\n"
        "v_mfma_f32_16x16x32_bf16 v[64:67], a[144:147], a[84:87], 0\n"
        "s_nop 0\n"
        "v_add_f32 v28, v28, v59\n"
        "v_mfma_f32_16x16x32_bf16 v[64:67], a[148:151], a[88:91], v[64:67]\n"
        "s_nop 0\n"
        "v_add_f32 v28, v28, v60\n"
        "v_add_f32 v28, v28, v61\n"
        "v_mfma_f32_16x16x32_bf16 v[64:67], a[152:155], a[92:95], v[64:67]\n"
        "v_add_f32 v28, v28, v62\n"
        "v_add_f32 v28, v28, v63\n"
        "v_mfma_f32_16x16x32_bf16 v[68:71], a[164:167], a[92:95], 0\n"
        "s_nop 0\n"
        "v_mov_b32 v29, v28\n"
        "v_mfma_f32_16x16x32_bf16 v[68:71], a[160:163], a[88:91], v[68:71]\n"
        "s_nop 0\n"
        "buffer_load_dwordx4 v[174:177], v34, s[36:39], 0 offen offset:32\n"
        "v_add_f32 v43, v64, v65\n"
        "v_mfma_f32_16x16x32_bf16 v[68:71], a[156:159], a[84:87], v[68:71]\n"
        "v_permlane32_swap_b32 v28, v29\n"
        "v_mfma_f32_16x16x32_bf16 v[72:75], a[168:171], a[84:87], 0\n"
        "v_add_f32 v28, v28, v29\n"
        "v_mov_b32 v29, v28\n"
        "v_mfma_f32_16x16x32_bf16 v[72:75], a[172:175], a[88:91], v[72:75]\n"
        "s_nop 0\n"
        "v_permlane16_swap_b32 v28, v29\n"
        "v_mfma_f32_16x16x32_bf16 v[72:75], a[176:179], a[92:95], v[72:75]\n"
        "v_add_f32 v28, v28, v29\n"
        "v_add_f32 v43, v43, v66\n"
        "v_mfma_f32_16x16x32_bf16 v[76:79], a[188:191], a[92:95], 0\n"
        "v_mul_f32 v28, 0xbc2aaaab, v28\n"
        "v_add_f32 v43, v43, v67\n"
        "v_mfma_f32_16x16x32_bf16 v[76:79], a[184:187], a[88:91], v[76:79]\n"
        "v_add_f32 v30, v30, v28\n"
        "v_add_f32 v31, v31, v28\n"
        "v_mfma_f32_16x16x32_bf16 v[76:79], a[180:183], a[84:87], v[76:79]\n"
        "v_add_f32 v32, v32, v28\n"
        "v_add_f32 v33, v33, v28\n"
        "v_mfma_f32_16x16x32_bf16 v[80:83], a[192:195], a[84:87], 0\n"
        "v_add_f32 v44, v44, v28\n"
        "v_add_f32 v45, v45, v28\n"
        "v_mfma_f32_16x16x32_bf16 v[80:83], a[196:199], a[88:91], v[80:83]\n"
        "buffer_load_dwordx4 v[182:185], v35, s[36:39], 0 offen\n"
        "v_add_f32 v46, v46, v28\n"
        "v_mfma_f32_16x16x32_bf16 v[80:83], a[200:203], a[92:95], v[80:83]\n"
        "v_add_f32 v47, v47, v28\n"
        "v_add_f32 v48, v48, v28\n"
        "v_mfma_f32_16x16x32_bf16 v[84:87], a[212:215], a[92:95], 0\n"
        "v_add_f32 v49, v49, v28\n"
        "v_add_f32 v50, v50, v28\n"
        "v_mfma_f32_16x16x32_bf16 v[84:87], a[208:211], a[88:91], v[84:87]\n"
        "v_add_f32 v51, v51, v28\n"
        "v_add_f32 v52, v52, v28\n"
        "v_mfma_f32_16x16x32_bf16 v[84:87], a[204:207], a[84:87], v[84:87]\n"
        "v_add_f32 v53, v53, v28\n"
        "v_add_f32 v54, v54, v28\n"
        "v_mfma_f32_16x16x32_bf16 v[88:91], a[144:147], a[96:99], 0\n"
        "v_add_f32 v55, v55, v28\n"
        "v_add_f32 v56, v56, v28\n"
        "v_mfma_f32_16x16x32_bf16 v[88:91], a[148:151], a[100:103], v[88:91]\n"
        "v_add_f32 v57, v57, v28\n"
        "v_add_f32 v58, v58, v28\n"
        "v_mfma_f32_16x16x32_bf16 v[88:91], a[152:155], a[104:107], v[88:91]\n"
        "v_add_f32 v59, v59, v28\n"
        "v_add_f32 v60, v60, v28\n"
        "v_mfma_f32_16x16x32_bf16 v[92:95], a[164:167], a[104:107], 0\n"
        "v_add_f32 v61, v61, v28\n"
        "v_add_f32 v62, v62, v28\n"
        "v_mfma_f32_16x16x32_bf16 v[92:95], a[160:163], a[100:103], v[92:95]\n"
        "buffer_load_dwordx4 v[188:191], v35, s[36:39], 0 offen offset:16\n"
        "v_add_f32 v63, v63, v28\n"
        "v_mfma_f32_16x16x32_bf16 v[92:95], a[156:159], a[96:99], v[92:95]\n"
        "v_cvt_pk_bf16_f32 v28, v30, v31\n"
        "v_cvt_pk_bf16_f32 v29, v32, v33\n"
        "v_mfma_f32_16x16x32_bf16 v[96:99], a[168:171], a[96:99], 0\n"
        "v_cvt_pk_bf16_f32 v30, v44, v45\n"
        "v_cvt_pk_bf16_f32 v31, v46, v47\n"
        "v_mfma_f32_16x16x32_bf16 v[96:99], a[172:175], a[100:103], v[96:99]\n"
        "ds_write_b128 v37, v[28:31]\n"
        "v_mfma_f32_16x16x32_bf16 v[96:99], a[176:179], a[104:107], v[96:99]\n"
        "v_cvt_pk_bf16_f32 v44, v48, v49\n"
        "v_cvt_pk_bf16_f32 v45, v50, v51\n"
        "v_mfma_f32_16x16x32_bf16 v[48:51], a[188:191], a[104:107], 0\n"
        "v_cvt_pk_bf16_f32 v46, v52, v53\n"
        "v_cvt_pk_bf16_f32 v47, v54, v55\n"
        "v_mfma_f32_16x16x32_bf16 v[48:51], a[184:187], a[100:103], v[48:51]\n"
        "ds_write_b128 v37, v[44:47] offset:1024\n"
        "v_mfma_f32_16x16x32_bf16 v[48:51], a[180:183], a[96:99], v[48:51]\n"
        "v_cvt_pk_bf16_f32 v52, v56, v57\n"
        "v_cvt_pk_bf16_f32 v53, v58, v59\n"
        "v_mfma_f32_16x16x32_bf16 v[56:59], a[192:195], a[96:99], 0\n"
        "v_cvt_pk_bf16_f32 v54, v60, v61\n"
        "v_cvt_pk_bf16_f32 v55, v62, v63\n"
        "v_mfma_f32_16x16x32_bf16 v[56:59], a[196:199], a[100:103], v[56:59]\n"
        "ds_write_b128 v37, v[52:55] offset:2048\n"
        "v_mfma_f32_16x16x32_bf16 v[56:59], a[200:203], a[104:107], v[56:59]\n"
        "buffer_load_dwordx4 v[194:197], v35, s[36:39], 0 offen offset:32\n"
        "v_add_f32 v43, v43, v68\n"
        "v_mfma_f32_16x16x32_bf16 v[32:35], a[212:215], a[104:107], 0\n"
        "v_add_f32 v43, v43, v69\n"
        "v_add_f32 v43, v43, v70\n"
        "v_mfma_f32_16x16x32_bf16 v[32:35], a[208:211], a[100:103], v[32:35]\n"
        "v_add_f32 v43, v43, v71\n"
        "v_add_f32 v43, v43, v72\n"
        "v_mfma_f32_16x16x32_bf16 v[32:35], a[204:207], a[96:99], v[32:35]\n"
        "v_add_f32 v43, v43, v73\n"
        "v_add_f32 v43, v43, v74\n"
        "v_mfma_f32_16x16x32_bf16 v[60:63], a[144:147], a[108:111], 0\n"
        "v_add_f32 v43, v43, v75\n"
        "v_add_f32 v43, v43, v76\n"
        "v_mfma_f32_16x16x32_bf16 v[60:63], a[148:151], a[112:115], v[60:63]\n"
        "v_add_f32 v43, v43, v77\n"
        "v_add_f32 v43, v43, v78\n"
        "v_mfma_f32_16x16x32_bf16 v[60:63], a[152:155], a[116:119], v[60:63]\n"
        "v_add_f32 v43, v43, v79\n"
        "v_add_f32 v43, v43, v80\n"
        "v_mfma_f32_16x16x32_bf16 v[100:103], a[164:167], a[116:119], 0\n"
        "s_add_u32 m0, s52, 0\n"
        "v_add_f32 v43, v43, v81\n"
        "v_add_f32 v43, v43, v82\n"
        "v_mfma_f32_16x16x32_bf16 v[100:103], a[160:163], a[112:115], v[100:103]\n"
        "buffer_load_dwordx4 v0, s[36:39], s49 offen lds\n"
        "v_add_f32 v43, v43, v83\n"
        "v_mfma_f32_16x16x32_bf16 v[100:103], a[156:159], a[108:111], v[100:103]\n"
        "v_add_f32 v43, v43, v84\n"
        "v_add_f32 v43, v43, v85\n"
        "v_mfma_f32_16x16x32_bf16 v[104:107], a[168:171], a[108:111], 0\n"
        "v_add_f32 v43, v43, v86\n"
        "v_add_f32 v43, v43, v87\n"
        "v_mfma_f32_16x16x32_bf16 v[104:107], a[172:175], a[112:115], v[104:107]\n"
        "v_mov_b32 v37, v43\n"
        "v_add_f32 v160, v88, v89\n"
        "v_mfma_f32_16x16x32_bf16 v[104:107], a[176:179], a[116:119], v[104:107]\n"
        "v_permlane32_swap_b32 v43, v37\n"
        "v_mfma_f32_16x16x32_bf16 v[108:111], a[188:191], a[116:119], 0\n"
        "v_add_f32 v43, v43, v37\n"
        "v_mov_b32 v37, v43\n"
        "v_mfma_f32_16x16x32_bf16 v[108:111], a[184:187], a[112:115], v[108:111]\n"
        "buffer_load_dwordx4 v1, s[36:39], s49 offen offset:1024 lds\n"
        "v_add_f32 v160, v160, v90\n"
        "v_mfma_f32_16x16x32_bf16 v[108:111], a[180:183], a[108:111], v[108:111]\n"
        "v_permlane16_swap_b32 v43, v37\n"
        "v_mfma_f32_16x16x32_bf16 v[112:115], a[192:195], a[108:111], 0\n"
        "v_add_f32 v43, v43, v37\n"
        "v_mul_f32 v43, 0xbc2aaaab, v43\n"
        "v_mfma_f32_16x16x32_bf16 v[112:115], a[196:199], a[112:115], v[112:115]\n"
        "v_add_f32 v160, v160, v91\n"
        "v_add_f32 v64, v64, v43\n"
        "v_mfma_f32_16x16x32_bf16 v[112:115], a[200:203], a[116:119], v[112:115]\n"
        "v_add_f32 v65, v65, v43\n"
        "v_add_f32 v66, v66, v43\n"
        "v_mfma_f32_16x16x32_bf16 v[116:119], a[212:215], a[116:119], 0\n"
        "v_add_f32 v67, v67, v43\n"
        "v_add_f32 v68, v68, v43\n"
        "v_mfma_f32_16x16x32_bf16 v[116:119], a[208:211], a[112:115], v[116:119]\n"
        "buffer_load_dwordx4 v2, s[36:39], s49 offen offset:2048 lds\n"
        "v_add_f32 v69, v69, v43\n"
        "v_mfma_f32_16x16x32_bf16 v[116:119], a[204:207], a[108:111], v[116:119]\n"
        "v_add_f32 v70, v70, v43\n"
        "v_add_f32 v71, v71, v43\n"
        "v_mfma_f32_16x16x32_bf16 v[120:123], a[144:147], a[120:123], 0\n"
        "v_add_f32 v72, v72, v43\n"
        "v_add_f32 v73, v73, v43\n"
        "v_mfma_f32_16x16x32_bf16 v[120:123], a[148:151], a[124:127], v[120:123]\n"
        "v_add_f32 v74, v74, v43\n"
        "v_add_f32 v75, v75, v43\n"
        "v_mfma_f32_16x16x32_bf16 v[120:123], a[152:155], a[128:131], v[120:123]\n"
        "v_add_f32 v76, v76, v43\n"
        "v_add_f32 v77, v77, v43\n"
        "v_mfma_f32_16x16x32_bf16 v[124:127], a[164:167], a[128:131], 0\n"
        "v_add_f32 v78, v78, v43\n"
        "v_add_f32 v79, v79, v43\n"
        "v_mfma_f32_16x16x32_bf16 v[124:127], a[160:163], a[124:127], v[124:127]\n"
        "buffer_load_dwordx4 v3, s[36:39], s49 offen offset:3072 lds\n"
        "v_add_f32 v80, v80, v43\n"
        "v_mfma_f32_16x16x32_bf16 v[124:127], a[156:159], a[120:123], v[124:127]\n"
        "v_add_f32 v81, v81, v43\n"
        "v_add_f32 v82, v82, v43\n"
        "v_mfma_f32_16x16x32_bf16 v[128:131], a[168:171], a[120:123], 0\n"
        "v_add_f32 v83, v83, v43\n"
        "v_add_f32 v84, v84, v43\n"
        "v_mfma_f32_16x16x32_bf16 v[128:131], a[172:175], a[124:127], v[128:131]\n"
        "v_add_f32 v85, v85, v43\n"
        "v_add_f32 v86, v86, v43\n"
        "v_mfma_f32_16x16x32_bf16 v[128:131], a[176:179], a[128:131], v[128:131]\n"
        "v_add_f32 v87, v87, v43\n"
        "v_cvt_pk_bf16_f32 v132, v64, v65\n"
        "v_mfma_f32_16x16x32_bf16 v[136:139], a[188:191], a[128:131], 0\n"
        "s_add_u32 m0, s52, 4096\n"
        "v_cvt_pk_bf16_f32 v133, v66, v67\n"
        "v_cvt_pk_bf16_f32 v134, v68, v69\n"
        "v_mfma_f32_16x16x32_bf16 v[136:139], a[184:187], a[124:127], v[136:139]\n"
        "buffer_load_dwordx4 v4, s[36:39], s49 offen lds\n"
        "v_cvt_pk_bf16_f32 v135, v70, v71\n"
        "v_mfma_f32_16x16x32_bf16 v[136:139], a[180:183], a[120:123], v[136:139]\n"
        "ds_write_b128 v38, v[132:135]\n"
        "v_mfma_f32_16x16x32_bf16 v[64:67], a[192:195], a[120:123], 0\n"
        "v_cvt_pk_bf16_f32 v68, v72, v73\n"
        "v_cvt_pk_bf16_f32 v69, v74, v75\n"
        "v_mfma_f32_16x16x32_bf16 v[64:67], a[196:199], a[124:127], v[64:67]\n"
        "v_cvt_pk_bf16_f32 v70, v76, v77\n"
        "v_cvt_pk_bf16_f32 v71, v78, v79\n"
        "v_mfma_f32_16x16x32_bf16 v[64:67], a[200:203], a[128:131], v[64:67]\n"
        "ds_write_b128 v38, v[68:71] offset:1024\n"
        "v_mfma_f32_16x16x32_bf16 v[72:75], a[212:215], a[128:131], 0\n"
        "v_cvt_pk_bf16_f32 v76, v80, v81\n"
        "v_cvt_pk_bf16_f32 v77, v82, v83\n"
        "v_mfma_f32_16x16x32_bf16 v[72:75], a[208:211], a[124:127], v[72:75]\n"
        "buffer_load_dwordx4 v5, s[36:39], s49 offen offset:1024 lds\n"
        "v_cvt_pk_bf16_f32 v78, v84, v85\n"
        "v_mfma_f32_16x16x32_bf16 v[72:75], a[204:207], a[120:123], v[72:75]\n"
        "v_cvt_pk_bf16_f32 v79, v86, v87\n"
        "v_add_f32 v160, v160, v92\n"
        "v_mfma_f32_16x16x32_bf16 v[80:83], a[144:147], a[132:135], 0\n"
        "ds_write_b128 v38, v[76:79] offset:2048\n"
        "v_mfma_f32_16x16x32_bf16 v[80:83], a[148:151], a[136:139], v[80:83]\n"
        "v_add_f32 v160, v160, v93\n"
        "v_add_f32 v160, v160, v94\n"
        "v_mfma_f32_16x16x32_bf16 v[80:83], a[152:155], a[140:143], v[80:83]\n"
        "v_add_f32 v160, v160, v95\n"
        "v_add_f32 v160, v160, v96\n"
        "v_mfma_f32_16x16x32_bf16 v[84:87], a[164:167], a[140:143], 0\n"
        "v_add_f32 v160, v160, v97\n"
        "v_add_f32 v160, v160, v98\n"
        "v_mfma_f32_16x16x32_bf16 v[84:87], a[160:163], a[136:139], v[84:87]\n"
        "buffer_load_dwordx4 v6, s[36:39], s49 offen offset:2048 lds\n"
        "v_add_f32 v160, v160, v99\n"
        "v_mfma_f32_16x16x32_bf16 v[84:87], a[156:159], a[132:135], v[84:87]\n"
        "v_add_f32 v160, v160, v48\n"
        "v_add_f32 v160, v160, v49\n"
        "v_mfma_f32_16x16x32_bf16 v[140:143], a[168:171], a[132:135], 0\n"
        "v_add_f32 v160, v160, v50\n"
        "v_add_f32 v160, v160, v51\n"
        "v_mfma_f32_16x16x32_bf16 v[140:143], a[172:175], a[136:139], v[140:143]\n"
        "v_add_f32 v160, v160, v56\n"
        "v_add_f32 v160, v160, v57\n"
        "v_mfma_f32_16x16x32_bf16 v[140:143], a[176:179], a[140:143], v[140:143]\n"
        "v_add_f32 v160, v160, v58\n"
        "v_add_f32 v160, v160, v59\n"
        "v_mfma_f32_16x16x32_bf16 v[144:147], a[188:191], a[140:143], 0\n"
        "v_add_f32 v160, v160, v32\n"
        "v_add_f32 v160, v160, v33\n"
        "v_mfma_f32_16x16x32_bf16 v[144:147], a[184:187], a[136:139], v[144:147]\n"
        "buffer_load_dwordx4 v7, s[36:39], s49 offen offset:3072 lds\n"
        "v_add_f32 v160, v160, v34\n"
        "v_mfma_f32_16x16x32_bf16 v[144:147], a[180:183], a[132:135], v[144:147]\n"
        "v_add_f32 v160, v160, v35\n"
        "v_mov_b32 v37, v160\n"
        "v_mfma_f32_16x16x32_bf16 v[148:151], a[192:195], a[132:135], 0\n"
        "s_nop 0\n"
        "v_permlane32_swap_b32 v160, v37\n"
        "v_mfma_f32_16x16x32_bf16 v[148:151], a[196:199], a[136:139], v[148:151]\n"
        "v_add_f32 v160, v160, v37\n"
        "v_mov_b32 v37, v160\n"
        "v_mfma_f32_16x16x32_bf16 v[148:151], a[200:203], a[140:143], v[148:151]\n"
        "s_nop 0\n"
        "v_permlane16_swap_b32 v160, v37\n"
        "v_mfma_f32_16x16x32_bf16 v[152:155], a[212:215], a[140:143], 0\n"
        "s_add_u32 m0, s52, 8192\n"
        "v_add_f32 v38, v60, v61\n"
        "v_add_f32 v160, v160, v37\n"
        "v_mfma_f32_16x16x32_bf16 v[152:155], a[208:211], a[136:139], v[152:155]\n"
        "buffer_load_dwordx4 v8, s[36:39], s49 offen lds\n"
        "v_add_f32 v38, v38, v62\n"
        "v_mfma_f32_16x16x32_bf16 v[152:155], a[204:207], a[132:135], v[152:155]\n"
        "v_mul_f32 v160, 0xbc2aaaab, v160\n"
        "v_add_f32 v38, v38, v63\n"
        "v_mfma_f32_16x16x32_bf16 v[156:159], v[28:31], v[28:31], 0\n"
        "v_add_f32 v88, v88, v160\n"
        "v_add_f32 v89, v89, v160\n"
        "v_mfma_f32_16x16x32_bf16 v[156:159], v[44:47], v[44:47], v[156:159]\n"
        "v_add_f32 v90, v90, v160\n"
        "v_add_f32 v91, v91, v160\n"
        "v_mfma_f32_16x16x32_bf16 v[156:159], v[52:55], v[52:55], v[156:159]\n"
        "v_add_f32 v92, v92, v160\n"
        "v_add_f32 v93, v93, v160\n"
        "v_mfma_f32_16x16x32_bf16 v[156:159], v[132:135], v[132:135], v[156:159]\n"
        "v_add_f32 v94, v94, v160\n"
        "v_add_f32 v95, v95, v160\n"
        "v_mfma_f32_16x16x32_bf16 v[156:159], v[68:71], v[68:71], v[156:159]\n"
        "v_add_f32 v96, v96, v160\n"
        "v_add_f32 v97, v97, v160\n"
        "v_mfma_f32_16x16x32_bf16 v[156:159], v[76:79], v[76:79], v[156:159]\n"
        "v_add_f32 v98, v98, v160\n"
        "v_add_f32 v99, v99, v160\n"
        "v_cvt_pk_bf16_f32 v28, v88, v89\n"
        "v_cvt_pk_bf16_f32 v29, v90, v91\n"
        "v_cvt_pk_bf16_f32 v30, v92, v93\n"
        "v_cvt_pk_bf16_f32 v31, v94, v95\n"
        "s_nop 1\n"
        "v_mfma_f32_16x16x32_bf16 v[156:159], v[28:31], v[28:31], v[156:159]\n"
        "ds_write_b128 v39, v[28:31]\n"
        "v_cvt_pk_bf16_f32 v28, v96, v97\n"
        "v_cvt_pk_bf16_f32 v29, v98, v99\n"
        "v_add_f32 v48, v48, v160\n"
        "v_add_f32 v49, v49, v160\n"
        "v_cvt_pk_bf16_f32 v30, v48, v49\n"
        "v_add_f32 v50, v50, v160\n"
        "v_add_f32 v51, v51, v160\n"
        "v_cvt_pk_bf16_f32 v31, v50, v51\n"
        "s_nop 1\n"
        "v_mfma_f32_16x16x32_bf16 v[156:159], v[28:31], v[28:31], v[156:159]\n"
        "ds_write_b128 v39, v[28:31] offset:1024\n"
        "v_add_f32 v56, v56, v160\n"
        "v_add_f32 v57, v57, v160\n"
        "v_cvt_pk_bf16_f32 v28, v56, v57\n"
        "v_add_f32 v58, v58, v160\n"
        "v_add_f32 v59, v59, v160\n"
        "v_cvt_pk_bf16_f32 v29, v58, v59\n"
        "v_add_f32 v32, v32, v160\n"
        "v_add_f32 v33, v33, v160\n"
        "v_cvt_pk_bf16_f32 v30, v32, v33\n"
        "v_add_f32 v34, v34, v160\n"
        "v_add_f32 v35, v35, v160\n"
        "v_cvt_pk_bf16_f32 v31, v34, v35\n"
        "s_nop 1\n"
        "v_mfma_f32_16x16x32_bf16 v[156:159], v[28:31], v[28:31], v[156:159]\n"
        "ds_write_b128 v39, v[28:31] offset:2048\n"
        "v_add_f32 v38, v38, v100\n"
        "v_add_f32 v38, v38, v101\n"
        "v_add_f32 v38, v38, v102\n"
        "v_add_f32 v38, v38, v103\n"
        "v_add_f32 v38, v38, v104\n"
        "v_add_f32 v38, v38, v105\n"
        "v_add_f32 v38, v38, v106\n"
        "v_add_f32 v38, v38, v107\n"
        "v_add_f32 v38, v38, v108\n"
        "v_add_f32 v38, v38, v109\n"
        "v_add_f32 v38, v38, v110\n"
        "v_add_f32 v38, v38, v111\n"
        "v_add_f32 v38, v38, v112\n"
        "v_add_f32 v38, v38, v113\n"
        "v_add_f32 v38, v38, v114\n"
        "v_add_f32 v38, v38, v115\n"
        "v_add_f32 v38, v38, v116\n"
        "v_add_f32 v38, v38, v117\n"
        "v_add_f32 v38, v38, v118\n"
        "v_add_f32 v38, v38, v119\n"
        "v_mov_b32 v28, v38\n"
        "s_nop 1\n"
        "v_permlane32_swap_b32 v38, v28\n"
        "v_add_f32 v38, v38, v28\n"
        "v_mov_b32 v28, v38\n"
        "s_nop 1\n"
        "v_permlane16_swap_b32 v38, v28\n"
        "v_add_f32 v38, v38, v28\n"
        "v_mul_f32 v38, 0xbc2aaaab, v38\n"
        "v_add_f32 v60, v60, v38\n"
        "v_add_f32 v61, v61, v38\n"
        "v_cvt_pk_bf16_f32 v28, v60, v61\n"
        "v_add_f32 v62, v62, v38\n"
        "v_add_f32 v63, v63, v38\n"
        "v_cvt_pk_bf16_f32 v29, v62, v63\n"
        "v_add_f32 v100, v100, v38\n"
        "v_add_f32 v101, v101, v38\n"
        "v_cvt_pk_bf16_f32 v30, v100, v101\n"
        "v_add_f32 v102, v102, v38\n"
        "v_add_f32 v103, v103, v38\n"
        "v_cvt_pk_bf16_f32 v31, v102, v103\n"
        "s_nop 1\n"
        "v_mfma_f32_16x16x32_bf16 v[156:159], v[28:31], v[28:31], v[156:159]\n"
        "ds_write_b128 v40, v[28:31]\n"
        "v_add_f32 v104, v104, v38\n"
        "v_add_f32 v105, v105, v38\n"
        "v_cvt_pk_bf16_f32 v28, v104, v105\n"
        "v_add_f32 v106, v106, v38\n"
        "v_add_f32 v107, v107, v38\n"
        "v_cvt_pk_bf16_f32 v29, v106, v107\n"
        "v_add_f32 v108, v108, v38\n"
        "v_add_f32 v109, v109, v38\n"
        "v_cvt_pk_bf16_f32 v30, v108, v109\n"
        "v_add_f32 v110, v110, v38\n"
        "v_add_f32 v111, v111, v38\n"
        "v_cvt_pk_bf16_f32 v31, v110, v111\n"
        "s_nop 1\n"
        "v_mfma_f32_16x16x32_bf16 v[156:159], v[28:31], v[28:31], v[156:159]\n"
        "ds_write_b128 v40, v[28:31] offset:1024\n"
        "v_add_f32 v112, v112, v38\n"
        "v_add_f32 v113, v113, v38\n"
        "v_cvt_pk_bf16_f32 v28, v112, v113\n"
        "v_add_f32 v114, v114, v38\n"
        "v_add_f32 v115, v115, v38\n"
        "v_cvt_pk_bf16_f32 v29, v114, v115\n"
        "v_add_f32 v116, v116, v38\n"
        "v_add_f32 v117, v117, v38\n"
        "v_cvt_pk_bf16_f32 v30, v116, v117\n"
        "v_add_f32 v118, v118, v38\n"
        "v_add_f32 v119, v119, v38\n"
        "v_cvt_pk_bf16_f32 v31, v118, v119\n"
        "s_nop 1\n"
        "v_mfma_f32_16x16x32_bf16 v[156:159], v[28:31], v[28:31], v[156:159]\n"
        "ds_write_b128 v40, v[28:31] offset:2048\n"
        "v_add_f32 v32, v120, v121\n"
        "v_add_f32 v32, v32, v122\n"
        "v_add_f32 v32, v32, v123\n"
        "v_add_f32 v32, v32, v124\n"
        "v_add_f32 v32, v32, v125\n"
        "v_add_f32 v32, v32, v126\n"
        "v_add_f32 v32, v32, v127\n"
        "v_add_f32 v32, v32, v128\n"
        "v_add_f32 v32, v32, v129\n"
        "v_add_f32 v32, v32, v130\n"
        "v_add_f32 v32, v32, v131\n"
        "v_add_f32 v32, v32, v136\n"
        "v_add_f32 v32, v32, v137\n"
        "v_add_f32 v32, v32, v138\n"
        "v_add_f32 v32, v32, v139\n"
        "v_add_f32 v32, v32, v64\n"
        "v_add_f32 v32, v32, v65\n"
        "v_add_f32 v32, v32, v66\n"
        "v_add_f32 v32, v32, v67\n"
        "v_add_f32 v32, v32, v72\n"
        "v_add_f32 v32, v32, v73\n"
        "v_add_f32 v32, v32, v74\n"
        "v_add_f32 v32, v32, v75\n"
        "v_mov_b32 v28, v32\n"
        "s_nop 1\n"
        "v_permlane32_swap_b32 v32, v28\n"
        "v_add_f32 v32, v32, v28\n"
        "v_mov_b32 v28, v32\n"
        "s_nop 1\n"
        "v_permlane16_swap_b32 v32, v28\n"
        "v_add_f32 v32, v32, v28\n"
        "v_mul_f32 v32, 0xbc2aaaab, v32\n"
        "v_add_f32 v120, v120, v32\n"
        "v_add_f32 v121, v121, v32\n"
        "v_cvt_pk_bf16_f32 v28, v120, v121\n"
        "v_add_f32 v122, v122, v32\n"
        "v_add_f32 v123, v123, v32\n"
        "v_cvt_pk_bf16_f32 v29, v122, v123\n"
        "v_add_f32 v124, v124, v32\n"
        "v_add_f32 v125, v125, v32\n"
        "v_cvt_pk_bf16_f32 v30, v124, v125\n"
        "v_add_f32 v126, v126, v32\n"
        "v_add_f32 v127, v127, v32\n"
        "v_cvt_pk_bf16_f32 v31, v126, v127\n"
        "s_nop 1\n"
        "v_mfma_f32_16x16x32_bf16 v[156:159], v[28:31], v[28:31], v[156:159]\n"
        "ds_write_b128 v41, v[28:31]\n"
        "v_add_f32 v128, v128, v32\n"
        "v_add_f32 v129, v129, v32\n"
        "v_cvt_pk_bf16_f32 v28, v128, v129\n"
        "v_add_f32 v130, v130, v32\n"
        "v_add_f32 v131, v131, v32\n"
        "v_cvt_pk_bf16_f32 v29, v130, v131\n"
        "v_add_f32 v136, v136, v32\n"
        "v_add_f32 v137, v137, v32\n"
        "v_cvt_pk_bf16_f32 v30, v136, v137\n"
        "v_add_f32 v138, v138, v32\n"
        "v_add_f32 v139, v139, v32\n"
        "v_cvt_pk_bf16_f32 v31, v138, v139\n"
        "s_nop 1\n"
        "v_mfma_f32_16x16x32_bf16 v[156:159], v[28:31], v[28:31], v[156:159]\n"
        "ds_write_b128 v41, v[28:31] offset:1024\n"
        "v_add_f32 v64, v64, v32\n"
        "v_add_f32 v65, v65, v32\n"
        "v_cvt_pk_bf16_f32 v28, v64, v65\n"
        "v_add_f32 v66, v66, v32\n"
        "v_add_f32 v67, v67, v32\n"
        "v_cvt_pk_bf16_f32 v29, v66, v67\n"
        "v_add_f32 v72, v72, v32\n"
        "v_add_f32 v73, v73, v32\n"
        "v_cvt_pk_bf16_f32 v30, v72, v73\n"
        "v_add_f32 v74, v74, v32\n"
        "v_add_f32 v75, v75, v32\n"
        "v_cvt_pk_bf16_f32 v31, v74, v75\n"
        "s_nop 1\n"
        "v_mfma_f32_16x16x32_bf16 v[156:159], v[28:31], v[28:31], v[156:159]\n"
        "ds_write_b128 v41, v[28:31] offset:2048\n"
        "v_add_f32 v32, v80, v81\n"
        "v_add_f32 v32, v32, v82\n"
        "v_add_f32 v32, v32, v83\n"
        "v_add_f32 v32, v32, v84\n"
        "v_add_f32 v32, v32, v85\n"
        "v_add_f32 v32, v32, v86\n"
        "v_add_f32 v32, v32, v87\n"
        "v_add_f32 v32, v32, v140\n"
        "v_add_f32 v32, v32, v141\n"
        "v_add_f32 v32, v32, v142\n"
        "v_add_f32 v32, v32, v143\n"
        "v_add_f32 v32, v32, v144\n"
        "v_add_f32 v32, v32, v145\n"
        "v_add_f32 v32, v32, v146\n"
        "v_add_f32 v32, v32, v147\n"
        "v_add_f32 v32, v32, v148\n"
        "v_add_f32 v32, v32, v149\n"
        "v_add_f32 v32, v32, v150\n"
        "v_add_f32 v32, v32, v151\n"
        "v_add_f32 v32, v32, v152\n"
        "v_add_f32 v32, v32, v153\n"
        "v_add_f32 v32, v32, v154\n"
        "v_add_f32 v32, v32, v155\n"
        "v_mov_b32 v28, v32\n"
        "s_nop 1\n"
        "v_permlane32_swap_b32 v32, v28\n"
        "v_add_f32 v32, v32, v28\n"
        "v_mov_b32 v28, v32\n"
        "s_nop 1\n"
        "v_permlane16_swap_b32 v32, v28\n"
        "v_add_f32 v32, v32, v28\n"
        "v_mul_f32 v32, 0xbc2aaaab, v32\n"
        "v_add_f32 v80, v80, v32\n"
        "v_add_f32 v81, v81, v32\n"
        "v_cvt_pk_bf16_f32 v28, v80, v81\n"
        "v_add_f32 v82, v82, v32\n"
        "v_add_f32 v83, v83, v32\n"
        "v_cvt_pk_bf16_f32 v29, v82, v83\n"
        "v_add_f32 v84, v84, v32\n"
        "v_add_f32 v85, v85, v32\n"
        "v_cvt_pk_bf16_f32 v30, v84, v85\n"
        "v_add_f32 v86, v86, v32\n"
        "v_add_f32 v87, v87, v32\n"
        "v_cvt_pk_bf16_f32 v31, v86, v87\n"
        "s_nop 1\n"
        "v_mfma_f32_16x16x32_bf16 v[156:159], v[28:31], v[28:31], v[156:159]\n"
        "ds_write_b128 v42, v[28:31]\n"
        "v_add_f32 v140, v140, v32\n"
        "v_add_f32 v141, v141, v32\n"
        "v_cvt_pk_bf16_f32 v28, v140, v141\n"
        "v_add_f32 v142, v142, v32\n"
        "v_add_f32 v143, v143, v32\n"
        "v_cvt_pk_bf16_f32 v29, v142, v143\n"
        "v_add_f32 v144, v144, v32\n"
        "v_add_f32 v145, v145, v32\n"
        "v_cvt_pk_bf16_f32 v30, v144, v145\n"
        "v_add_f32 v146, v146, v32\n"
        "v_add_f32 v147, v147, v32\n"
        "v_cvt_pk_bf16_f32 v31, v146, v147\n"
        "s_nop 1\n"
        "v_mfma_f32_16x16x32_bf16 v[156:159], v[28:31], v[28:31], v[156:159]\n"
        "ds_write_b128 v42, v[28:31] offset:1024\n"
        "v_add_f32 v148, v148, v32\n"
        "v_add_f32 v149, v149, v32\n"
        "v_cvt_pk_bf16_f32 v28, v148, v149\n"
        "v_add_f32 v150, v150, v32\n"
        "v_add_f32 v151, v151, v32\n"
        "v_cvt_pk_bf16_f32 v29, v150, v151\n"
        "v_add_f32 v152, v152, v32\n"
        "v_add_f32 v153, v153, v32\n"
        "v_cvt_pk_bf16_f32 v30, v152, v153\n"
        "v_add_f32 v154, v154, v32\n"
        "v_add_f32 v155, v155, v32\n"
        "v_cvt_pk_bf16_f32 v31, v154, v155\n"
        "s_nop 1\n"
        "v_mfma_f32_16x16x32_bf16 v[156:159], v[28:31], v[28:31], v[156:159]\n"
        "s_nop 0\n"
        "ds_write_b128 v42, v[28:31] offset:2048\n"
        "s_waitcnt vmcnt(9)\n"
        "s_nop 4\n"
        "v_max3_f32 v28, v156, v157, v158\n"
        "v_max_f32 v28, v28, v159\n"
        "v_cmp_nge_f32_e64 s[74:75], s93, v28\n"
        "v_mov_b32 v28, 160384\n"
        "s_nop 0\n"
        "v_cndmask_b32_e64 v28, v36, v28, s[74:75]\n"
        "v_mov_b32 v36, 0\n"
        "ds_write_b32 v28, v36\n"
        "s_waitcnt lgkmcnt(0)\n"
        "s_barrier\n"
        "FE_%=:\n"
        "v_mfma_f32_16x16x32_bf16 v[70:73], v[162:165], v[162:165], 0\n"
        "ds_read_b128 v[86:89], v26\n"
        "ds_read_b128 v[82:85], v26 offset:64\n"
        "v_mov_b64 v[90:91], s[76:77]\n"
        "v_mfma_f32_16x16x32_bf16 v[70:73], v[168:171], v[168:171], v[70:73]\n"
        "ds_read_b128 v[78:81], v26 offset:128\n"
        "v_mov_b64 v[92:93], s[76:77]\n"
        "v_add_u32 v251, 73792, v200\n"
        "v_mfma_f32_16x16x32_bf16 v[70:73], v[174:177], v[174:177], v[70:73]\n"
        "ds_read_b128 a[0:3], v200\n"
        "ds_read_b128 a[4:7], v200 offset:1024\n"
        "v_add_u32 v250, 139648, v200\n"
        "s_waitcnt lgkmcnt(2)\n"
        "v_mfma_f32_16x16x32_bf16 v[110:113], v[78:81], v[174:177], 0\n"
        "ds_read_b128 a[8:11], v200 offset:2048\n"
        "ds_read_b128 a[12:15], v200 offset:3072\n"
        "v_mov_b32 v254, 160384\n"
        "v_mfma_f32_16x16x32_bf16 v[110:113], v[82:85], v[168:171], v[110:113]\n"
        "ds_read_b128 a[16:19], v200 offset:4096\n"
        "ds_read_b128 a[20:23], v200 offset:5120\n"
        "v_mfma_f32_16x16x32_bf16 v[110:113], v[86:89], v[162:165], v[110:113]\n"
        "ds_read_b128 a[24:27], v200 offset:6144\n"
        "ds_read_b128 a[28:31], v200 offset:7168\n"
        "v_mfma_f32_16x16x32_bf16 v[102:105], v[182:185], v[182:185], 0\n"
        "ds_read_b128 a[32:35], v200 offset:8192\n"
        "ds_read_b128 a[36:39], v200 offset:9216\n"
        "v_mfma_f32_16x16x32_bf16 v[102:105], v[188:191], v[188:191], v[102:105]\n"
        "ds_read_b128 a[40:43], v200 offset:10240\n"
        "ds_read_b128 a[44:47], v200 offset:11264\n"
        "v_mfma_f32_16x16x32_bf16 v[102:105], v[194:197], v[194:197], v[102:105]\n"
        "ds_read_b128 a[48:51], v200 offset:12288\n"
        "ds_read_b128 a[52:55], v200 offset:13312\n"
        "v_mfma_f32_16x16x32_bf16 v[64:67], v[78:81], v[194:197], 0\n"
        "ds_read_b128 a[56:59], v200 offset:14336\n"
        "ds_read_b128 a[60:63], v200 offset:15360\n"
        "v_mfma_f32_16x16x32_bf16 v[64:67], v[82:85], v[188:191], v[64:67]\n"
        "ds_read_b128 a[64:67], v200 offset:16384\n"
        "ds_read_b128 a[68:71], v200 offset:17408\n"
        "v_mfma_f32_16x16x32_bf16 v[64:67], v[86:89], v[182:185], v[64:67]\n"
        "ds_read_b128 a[72:75], v200 offset:18432\n"
        "ds_read_b128 a[76:79], v200 offset:19456\n"
        "v_cndmask_b32_e64 v255, v70, v71, s[56:57]\n"
        "s_waitcnt lgkmcnt(15)\n"
        "v_mfma_f32_16x16x32_bf16 v[112:115], a[0:3], v[162:165], 0\n"
        "ds_read_b128 a[80:83], v200 offset:20480\n"
        "ds_read_b128 a[84:87], v200 offset:21504\n"
        "v_cndmask_b32_e64 v255, v255, v72, s[58:59]\n"
        "v_mfma_f32_16x16x32_bf16 v[112:115], a[4:7], v[168:171], v[112:115]\n"
        "s_waitcnt lgkmcnt(15)\n"
        "ds_read_b128 a[88:91], v200 offset:22528\n"
        "ds_read_b128 a[92:95], v200 offset:23552\n"
        "v_cndmask_b32_e64 v255, v255, v73, s[60:61]\n"
        "v_mfma_f32_16x16x32_bf16 v[112:115], a[8:11], v[174:177], v[112:115]\n"
        "ds_read_b128 a[96:99], v200 offset:24576\n"
        "ds_read_b128 a[100:103], v200 offset:25600\n"
        "v_cndmask_b32_e64 v253, v102, v103, s[56:57]\n"
        "v_mfma_f32_16x16x32_bf16 v[70:73], a[20:23], v[174:177], 0\n"
        "s_waitcnt lgkmcnt(15)\n"
        "ds_read_b128 a[104:107], v200 offset:26624\n"
        "ds_read_b128 a[108:111], v200 offset:27648\n"
        "v_cndmask_b32_e64 v253, v253, v104, s[58:59]\n"
        "v_mfma_f32_16x16x32_bf16 v[70:73], a[16:19], v[168:171], v[70:73]\n"
        "s_waitcnt lgkmcnt(15)\n"
        "ds_read_b128 a[112:115], v200 offset:28672\n"
        "ds_read_b128 a[116:119], v200 offset:29696\n"
        "v_cndmask_b32_e64 v253, v253, v105, s[60:61]\n"
        "v_mfma_f32_16x16x32_bf16 v[70:73], a[12:15], v[162:165], v[70:73]\n"
        "s_waitcnt lgkmcnt(15)\n"
        "ds_read_b128 a[120:123], v200 offset:30720\n"
        "ds_read_b128 a[124:127], v200 offset:31744\n"
        "v_cndmask_b32_dpp v255, v253, v255, vcc row_ror:4 row_mask:0xf bank_mask:0xf\n"
        "v_mfma_f32_16x16x32_bf16 v[106:109], a[24:27], v[162:165], 0\n"
        "s_waitcnt lgkmcnt(14)\n"
        "ds_read_b128 a[128:131], v200 offset:32768\n"
        "ds_read_b128 a[132:135], v200 offset:33792\n"
        "v_mul_f32 v255, s80, v255\n"
        "v_mfma_f32_16x16x32_bf16 v[106:109], a[28:31], v[168:171], v[106:109]\n"
        "ds_read_b128 a[136:139], v200 offset:34816\n"
        "ds_read_b128 a[140:143], v200 offset:35840\n"
        "v_cndmask_b32_dpp v252, v64, v110, vcc row_ror:4 row_mask:0xf bank_mask:0xf\n"
        "v_mfma_f32_16x16x32_bf16 v[106:109], a[32:35], v[174:177], v[106:109]\n"
        "v_cndmask_b32_dpp v253, v65, v111, vcc row_ror:4 row_mask:0xf bank_mask:0xf\n"
        "v_mul_f32 v249, s80, v252\n"
        "v_fma_f32 v249, -v249, v249, v255\n"
        "v_mfma_f32_16x16x32_bf16 v[102:105], a[44:47], v[174:177], 0\n"
        "v_max_f32 v249, 0, v249\n"
        "v_add_f32 v249, s81, v249\n"
        "v_cvt_pk_bf16_f32 v110, v112, v113\n"
        "v_mfma_f32_16x16x32_bf16 v[102:105], a[40:43], v[168:171], v[102:105]\n"
        "v_rsq_f32 v255, v249\n"
        "v_cvt_pk_bf16_f32 v111, v114, v115\n"
        "v_mfma_f32_16x16x32_bf16 v[102:105], a[36:39], v[162:165], v[102:105]\n"
        "v_cvt_pk_bf16_f32 v112, v70, v71\n"
        "v_cvt_pk_bf16_f32 v113, v72, v73\n"
        "v_cvt_pk_bf16_f32 v70, v106, v107\n"
        "v_mfma_f32_16x16x32_bf16 v[64:67], a[48:51], v[162:165], 0\n"
        "s_nop 0\n"
        "v_cvt_pk_bf16_f32 v71, v108, v109\n"
        "v_mfma_f32_16x16x32_bf16 v[64:67], a[52:55], v[168:171], v[64:67]\n"
        "s_nop 0\n"
        "v_cvt_pk_bf16_f32 v72, v102, v103\n"
        "v_cvt_pk_bf16_f32 v73, v104, v105\n"
        "v_mfma_f32_16x16x32_bf16 v[64:67], a[56:59], v[174:177], v[64:67]\n"
        "s_nop 1\n"
        "v_mfma_f32_16x16x32_bf16 v[102:105], a[68:71], v[174:177], 0\n"
        "s_nop 2\n"
        "v_mfma_f32_16x16x32_bf16 v[102:105], a[64:67], v[168:171], v[102:105]\n"
        "s_nop 0\n"
        "v_cvt_pk_bf16_f32 v106, v64, v65\n"
        "v_cvt_pk_bf16_f32 v107, v66, v67\n"
        "v_mfma_f32_16x16x32_bf16 v[102:105], a[60:63], v[162:165], v[102:105]\n"
        "s_nop 1\n"
        "v_mfma_f32_16x16x32_bf16 v[64:67], a[0:3], v[182:185], 0\n"
        "s_nop 2\n"
        "v_mfma_f32_16x16x32_bf16 v[64:67], a[4:7], v[188:191], v[64:67]\n"
        "s_nop 0\n"
        "v_cvt_pk_bf16_f32 v108, v102, v103\n"
        "v_cvt_pk_bf16_f32 v109, v104, v105\n"
        "v_mfma_f32_16x16x32_bf16 v[64:67], a[8:11], v[194:197], v[64:67]\n"
        "v_mfma_f32_16x16x32_bf16 v[120:123], v[162:165], v[110:113], 0\n"
        "s_nop 0\n"
        "s_waitcnt lgkmcnt(0)\n"
        "s_barrier\n"
        "v_mfma_f32_16x16x32_bf16 v[120:123], v[168:171], v[70:73], v[120:123]\n"
        "s_nop 0\n"
        "ds_read_b128 a[144:147], v250\n"
        "ds_read_b128 a[148:151], v250 offset:1024\n"
        "v_cvt_pk_bf16_f32 v102, v64, v65\n"
        "v_mfma_f32_16x16x32_bf16 v[120:123], v[174:177], v[106:109], v[120:123]\n"
        "ds_read_b128 a[152:155], v250 offset:2048\n"
        "ds_read_b128 a[156:159], v250 offset:3072\n"
        "v_cvt_pk_bf16_f32 v103, v66, v67\n"
        "v_mfma_f32_16x16x32_bf16 v[64:67], a[20:23], v[194:197], 0\n"
        "ds_read_b128 a[160:163], v250 offset:4096\n"
        "ds_read_b128 a[164:167], v250 offset:5120\n"
        "v_mfma_f32_16x16x32_bf16 v[64:67], a[16:19], v[188:191], v[64:67]\n"
        "ds_read_b128 a[168:171], v250 offset:6144\n"
        "ds_read_b128 a[172:175], v250 offset:7168\n"
        "v_mfma_f32_16x16x32_bf16 v[64:67], a[12:15], v[182:185], v[64:67]\n"
        "ds_read_b128 a[176:179], v250 offset:8192\n"
        "ds_read_b128 a[180:183], v250 offset:9216\n"
        "v_mfma_f32_16x16x32_bf16 v[70:73], a[24:27], v[182:185], 0\n"
        "ds_read_b128 a[184:187], v250 offset:10240\n"
        "ds_read_b128 a[188:191], v250 offset:11264\n"
        "v_mfma_f32_16x16x32_bf16 v[70:73], a[28:31], v[188:191], v[70:73]\n"
        "ds_read_b128 a[192:195], v250 offset:12288\n"
        "ds_read_b128 a[196:199], v250 offset:13312\n"
        "v_cvt_pk_bf16_f32 v104, v64, v65\n"
        "v_mfma_f32_16x16x32_bf16 v[70:73], a[32:35], v[194:197], v[70:73]\n"
        "ds_read_b128 a[200:203], v250 offset:14336\n"
        "v_cvt_pk_bf16_f32 v105, v66, v67\n"
        "v_mfma_f32_16x16x32_bf16 v[106:109], a[44:47], v[194:197], 0\n"
        "ds_read_b128 a[204:207], v250 offset:15360\n"
        "ds_read_b128 a[208:211], v250 offset:16384\n"
        "v_mfma_f32_16x16x32_bf16 v[106:109], a[40:43], v[188:191], v[106:109]\n"
        "s_nop 0\n"
        "ds_read_b128 a[212:215], v250 offset:17408\n"
        "v_cvt_pk_bf16_f32 v110, v70, v71\n"
        "v_cvt_pk_bf16_f32 v111, v72, v73\n"
        "v_mfma_f32_16x16x32_bf16 v[106:109], a[36:39], v[182:185], v[106:109]\n"
        "s_nop 0\n"
        "ds_read_b128 a[216:219], v251\n"
        "v_mfma_f32_16x16x32_bf16 v[64:67], a[48:51], v[182:185], 0\n"
        "s_nop 0\n"
        "ds_read_b128 a[220:223], v251 offset:1024\n"
        "v_mfma_f32_16x16x32_bf16 v[64:67], a[52:55], v[188:191], v[64:67]\n"
        "s_nop 0\n"
        "ds_read_b128 a[224:227], v251 offset:2048\n"
        "v_cvt_pk_bf16_f32 v112, v106, v107\n"
        "v_cvt_pk_bf16_f32 v113, v108, v109\n"
        "v_mfma_f32_16x16x32_bf16 v[64:67], a[56:59], v[194:197], v[64:67]\n"
        "s_nop 0\n"
        "ds_read_b128 a[228:231], v251 offset:3072\n"
        "v_mfma_f32_16x16x32_bf16 v[70:73], a[68:71], v[194:197], 0\n"
        "s_nop 0\n"
        "ds_read_b128 a[232:235], v251 offset:4096\n"
        "v_mfma_f32_16x16x32_bf16 v[70:73], a[64:67], v[188:191], v[70:73]\n"
        "s_nop 0\n"
        "ds_read_b128 a[236:239], v251 offset:5120\n"
        "v_cvt_pk_bf16_f32 v106, v64, v65\n"
        "v_cvt_pk_bf16_f32 v107, v66, v67\n"
        "v_mfma_f32_16x16x32_bf16 v[70:73], a[60:63], v[182:185], v[70:73]\n"
        "s_nop 0\n"
        "ds_read_b128 a[240:243], v251 offset:6144\n"
        "v_mfma_f32_16x16x32_bf16 v[64:67], v[162:165], a[72:75], 0\n"
        "s_nop 0\n"
        "ds_read_b128 a[244:247], v251 offset:7168\n"
        "v_mfma_f32_16x16x32_bf16 v[64:67], v[168:171], a[76:79], v[64:67]\n"
        "s_nop 0\n"
        "ds_read_b128 a[248:251], v251 offset:8192\n"
        "v_cvt_pk_bf16_f32 v108, v70, v71\n"
        "v_cvt_pk_bf16_f32 v109, v72, v73\n"
        "v_mfma_f32_16x16x32_bf16 v[64:67], v[174:177], a[80:83], v[64:67]\n"
        "s_nop 0\n"
        "ds_read_b128 a[252:255], v251 offset:9216\n"
        "v_mfma_f32_16x16x32_bf16 v[70:73], v[174:177], a[92:95], 0\n"
        "s_nop 0\n"
        "ds_read_b128 v[28:31], v251 offset:10240\n"
        "v_mfma_f32_16x16x32_bf16 v[70:73], v[168:171], a[88:91], v[70:73]\n"
        "s_nop 0\n"
        "ds_read_b128 v[32:35], v251 offset:11264\n"
        "v_cvt_pk_bf16_f32 v60, v64, v65\n"
        "v_cvt_pk_bf16_f32 v61, v66, v67\n"
        "v_mfma_f32_16x16x32_bf16 v[70:73], v[162:165], a[84:87], v[70:73]\n"
        "s_nop 0\n"
        "ds_read_b128 v[36:39], v251 offset:12288\n"
        "v_mfma_f32_16x16x32_bf16 v[126:129], v[182:185], v[102:105], 0\n"
        "s_nop 0\n"
        "ds_read_b128 v[40:43], v251 offset:13312\n"
        "v_mfma_f32_16x16x32_bf16 v[126:129], v[188:191], v[110:113], v[126:129]\n"
        "s_nop 0\n"
        "ds_read_b128 v[44:47], v251 offset:14336\n"
        "v_cvt_pk_bf16_f32 v64, v70, v71\n"
        "v_cvt_pk_bf16_f32 v65, v72, v73\n"
        "v_mfma_f32_16x16x32_bf16 v[126:129], v[194:197], v[106:109], v[126:129]\n"
        "s_waitcnt vmcnt(0)\n"
        "ds_read_b128 v[48:51], v251 offset:15360\n"
        "v_mfma_f32_16x16x32_bf16 v[70:73], v[174:177], a[104:107], 0\n"
        "ds_read_b128 v[52:55], v251 offset:16384\n"
        "ds_read_b128 v[104:107], v9\n"
        "v_mfma_f32_16x16x32_bf16 v[70:73], v[168:171], a[100:103], v[70:73]\n"
        "ds_read_b128 v[56:59], v251 offset:17408\n"
        "ds_read_b128 v[110:113], v10\n"
        "v_cndmask_b32_dpp v246, v126, v120, vcc row_ror:4 row_mask:0xf bank_mask:0xf\n"
        "v_mfma_f32_16x16x32_bf16 v[70:73], v[162:165], a[96:99], v[70:73]\n"
        "ds_read_b128 v[116:119], v11\n"
        "v_cndmask_b32_dpp v251, v127, v121, vcc row_ror:4 row_mask:0xf bank_mask:0xf\n"
        "v_cndmask_b32_dpp v248, v128, v122, vcc row_ror:4 row_mask:0xf bank_mask:0xf\n"
        "v_mfma_f32_16x16x32_bf16 v[212:215], v[162:165], a[108:111], 0\n"
        "ds_read_b128 v[124:127], v9 offset:3072\n"
        "v_cndmask_b32_dpp v250, v129, v123, vcc row_ror:4 row_mask:0xf bank_mask:0xf\n"
        "v_mul_f32_dpp v246, v255, v246 quad_perm:[0,0,0,0] row_mask:0xf bank_mask:0xf\n"
        "v_mfma_f32_16x16x32_bf16 v[212:215], v[168:171], a[112:115], v[212:215]\n"
        "ds_read_b128 v[130:133], v10 offset:3072\n"
        "v_mul_f32_dpp v251, v255, v251 quad_perm:[1,1,1,1] row_mask:0xf bank_mask:0xf\n"
        "v_mul_f32_dpp v248, v255, v248 quad_perm:[2,2,2,2] row_mask:0xf bank_mask:0xf\n"
        "v_mfma_f32_16x16x32_bf16 v[212:215], v[174:177], a[116:119], v[212:215]\n"
        "ds_read_b128 v[136:139], v11 offset:3072\n"
        "v_mul_f32_dpp v250, v255, v250 quad_perm:[3,3,3,3] row_mask:0xf bank_mask:0xf\n"
        "v_max3_f32 v249, v246, v251, v248\n"
        "v_mfma_f32_16x16x32_bf16 v[140:143], v[174:177], a[128:131], 0\n"
        "ds_read_b128 v[144:147], v9 offset:6144\n"
        "v_max_f32 v249, v249, v250\n"
        "v_mul_f32 v249, v249, v255\n"
        "v_mfma_f32_16x16x32_bf16 v[140:143], v[168:171], a[124:127], v[140:143]\n"
        "ds_read_b128 v[150:153], v10 offset:6144\n"
        "v_fma_f32 v246, v246, v255, -v249\n"
        "v_fma_f32 v251, v251, v255, -v249\n"
        "v_mfma_f32_16x16x32_bf16 v[140:143], v[162:165], a[120:123], v[140:143]\n"
        "ds_read_b128 v[156:159], v11 offset:6144\n"
        "v_fma_f32 v248, v248, v255, -v249\n"
        "v_fma_f32 v250, v250, v255, -v249\n"
        "v_mfma_f32_16x16x32_bf16 v[120:123], v[162:165], a[132:135], 0\n"
        "s_waitcnt lgkmcnt(6)\n"
        "v_exp_f32 v246, v246\n"
        "v_mfma_f32_16x16x32_bf16 v[120:123], v[168:171], a[136:139], v[120:123]\n"
        "v_exp_f32 v251, v251\n"
        "v_cvt_pk_bf16_f32 v66, v70, v71\n"
        "v_mfma_f32_16x16x32_bf16 v[120:123], v[174:177], a[140:143], v[120:123]\n"
        "v_exp_f32 v248, v248\n"
        "v_add_f32 v249, v246, v251\n"
        "v_mfma_f32_16x16x32_bf16 v[224:227], v[104:107], v[104:107], 0\n"
        "s_mov_b32 s26, s89\n"
        "v_exp_f32 v250, v250\n"
        "v_cvt_pk_bf16_f32 v67, v72, v73\n"
        "v_mfma_f32_16x16x32_bf16 v[224:227], v[110:113], v[110:113], v[224:227]\n"
        "s_mov_b32 s30, s29\n"
        "v_add_f32 v247, v248, v250\n"
        "v_add_f32 v249, v249, v247\n"
        "v_mfma_f32_16x16x32_bf16 v[224:227], v[116:119], v[116:119], v[224:227]\n"
        "s_add_u32 s88, s88, 1\n"
        "v_rcp_f32 v249, v249\n"
        "v_mfma_f32_16x16x32_bf16 v[228:231], v[78:81], v[116:119], 0\n"
        "s_add_u32 s89, s86, s51\n"
        "v_mul_f32 v246, v246, v249\n"
        "v_mul_f32 v251, v251, v249\n"
        "v_mul_f32 v248, v248, v249\n"
        "v_mfma_f32_16x16x32_bf16 v[228:231], v[82:85], v[110:113], v[228:231]\n"
        "s_add_u32 s29, s29, 1\n"
        "v_mul_f32 v250, v250, v249\n"
        "v_mul_f32_dpp v246, v255, v246 quad_perm:[0,0,0,0] row_mask:0xf bank_mask:0xf\n"
        "v_mul_f32_dpp v251, v255, v251 quad_perm:[1,1,1,1] row_mask:0xf bank_mask:0xf\n"
        "v_mfma_f32_16x16x32_bf16 v[228:231], v[86:89], v[104:107], v[228:231]\n"
        "s_cmp_ge_u32 s88, 73\n"
        "v_mul_f32_dpp v248, v255, v248 quad_perm:[2,2,2,2] row_mask:0xf bank_mask:0xf\n"
        "v_mul_f32_dpp v250, v255, v250 quad_perm:[3,3,3,3] row_mask:0xf bank_mask:0xf\n"
        "v_cvt_pk_bf16_f32 v255, v246, v251\n"
        "v_mfma_f32_16x16x32_bf16 v[208:211], a[0:3], v[104:107], 0\n"
        "s_mov_b32 s86, s87\n"
        "v_cvt_pk_bf16_f32 v247, v248, v250\n"
        "v_and_b32 v160, v25, v255\n"
        "v_and_b32 v161, v25, v247\n"
        "v_mfma_f32_16x16x32_bf16 v[208:211], a[4:7], v[110:113], v[208:211]\n"
        "s_waitcnt lgkmcnt(3)\n"
        "v_mov_b64 v[166:167], v[160:161]\n"
        "v_mov_b64 v[172:173], v[160:161]\n"
        "v_mov_b64 v[178:179], v[160:161]\n"
        "v_mfma_f32_16x16x32_bf16 v[208:211], a[8:11], v[116:119], v[208:211]\n"
        "s_cselect_b32 s92, 590016, 0\n"
        "v_mul_f32_dpp v249, v253, v246 quad_perm:[0,0,0,0] row_mask:0xf bank_mask:0xf\n"
        "v_and_b32_dpp v180, v255, v25 row_ror:12 row_mask:0xf bank_mask:0xf\n"
        "v_and_b32_dpp v181, v247, v25 row_ror:12 row_mask:0xf bank_mask:0xf\n"
        "v_mfma_f32_16x16x32_bf16 v[220:223], a[20:23], v[116:119], 0\n"
        "s_cselect_b32 s88, 0, s88\n"
        "ds_read_b64 v[62:63], v27\n"
        "ds_read_b64 v[68:69], v27\n"
        "v_mov_b64 v[186:187], v[180:181]\n"
        "v_mfma_f32_16x16x32_bf16 v[220:223], a[16:19], v[110:113], v[220:223]\n"
        "s_max_u32 s92, s92, 1344\n"
        "ds_read_b64 v[74:75], v27\n"
        "v_mov_b64 v[192:193], v[180:181]\n"
        "v_mov_b64 v[198:199], v[180:181]\n"
        "v_mfma_f32_16x16x32_bf16 v[220:223], a[12:15], v[104:107], v[220:223]\n"
        "s_add_u32 s87, s87, s92\n"
        "ds_read_b32 v254, v254\n"
        "s_cmp_gt_i32 s85, 0\n"
        "v_fmac_f32_dpp v249, v253, v251 quad_perm:[1,1,1,1] row_mask:0xf bank_mask:0xf\n"
        "v_mfma_f32_16x16x32_bf16 v[216:219], a[24:27], v[104:107], 0\n"
        "s_cselect_b32 s94, s87, s84\n"
        "v_cvt_pk_bf16_f32 v70, v212, v213\n"
        "v_cvt_pk_bf16_f32 v71, v214, v215\n"
        "v_fmac_f32_dpp v249, v253, v248 quad_perm:[2,2,2,2] row_mask:0xf bank_mask:0xf\n"
        "v_mfma_f32_16x16x32_bf16 v[216:219], a[28:31], v[110:113], v[216:219]\n"
        "s_add_u32 m0, s52, 0\n"
        "v_fmac_f32_dpp v249, v253, v250 quad_perm:[3,3,3,3] row_mask:0xf bank_mask:0xf\n"
        "v_add_f32 v249, v249, v252\n"
        "v_mul_f32 v253, s80, v249\n"
        "v_mfma_f32_16x16x32_bf16 v[216:219], a[32:35], v[116:119], v[216:219]\n"
        "s_waitcnt lgkmcnt(4)\n"
        "buffer_load_dwordx4 v0, s[36:39], s94 offen lds\n"
        "v_cvt_pk_bf16_f32 v72, v140, v141\n"
        "v_cvt_pk_bf16_f32 v73, v142, v143\n"
        "v_mfma_f32_16x16x32_bf16 v[140:143], a[44:47], v[116:119], 0\n"
        "s_sub_u32 s85, s85, 1\n"
        "v_cvt_pk_bf16_f32 v76, v120, v121\n"
        "v_cvt_pk_bf16_f32 v77, v122, v123\n"
        "v_mfma_f32_16x16x32_bf16 v[140:143], a[40:43], v[110:113], v[140:143]\n"
        "v_cndmask_b32_e64 v255, v224, v225, s[56:57]\n"
        "v_cndmask_b32_e64 v255, v255, v226, s[58:59]\n"
        "v_mfma_f32_16x16x32_bf16 v[140:143], a[36:39], v[104:107], v[140:143]\n"
        "s_waitcnt lgkmcnt(1)\n"
        "v_cndmask_b32_e64 v255, v255, v227, s[60:61]\n"
        "v_cvt_pk_bf16_f32 v224, v208, v209\n"
        "v_mfma_f32_16x16x32_bf16 v[212:215], a[48:51], v[104:107], 0\n"
        "s_waitcnt lgkmcnt(0)\n"
        "buffer_load_dwordx4 v1, s[36:39], s94 offen offset:1024 lds\n"
        "v_readfirstlane_b32 s83, v254\n"
        "v_cvt_pk_bf16_f32 v225, v210, v211\n"
        "v_mfma_f32_16x16x32_bf16 v[212:215], a[52:55], v[110:113], v[212:215]\n"
        "v_cvt_pk_bf16_f32 v226, v220, v221\n"
        "v_cvt_pk_bf16_f32 v227, v222, v223\n"
        "v_mfma_f32_16x16x32_bf16 v[212:215], a[56:59], v[116:119], v[212:215]\n"
        "v_cvt_pk_bf16_f32 v208, v216, v217\n"
        "v_cvt_pk_bf16_f32 v209, v218, v219\n"
        "v_mfma_f32_16x16x32_bf16 v[120:123], a[68:71], v[116:119], 0\n"
        "v_cvt_pk_bf16_f32 v210, v140, v141\n"
        "v_cvt_pk_bf16_f32 v211, v142, v143\n"
        "v_mfma_f32_16x16x32_bf16 v[120:123], a[64:67], v[110:113], v[120:123]\n"
        "s_nop 0\n"
        "buffer_load_dwordx4 v2, s[36:39], s94 offen offset:2048 lds\n"
        "v_cvt_pk_bf16_f32 v140, v212, v213\n"
        "v_mfma_f32_16x16x32_bf16 v[120:123], a[60:63], v[104:107], v[120:123]\n"
        "s_nop 0\n"
        "v_cvt_pk_bf16_f32 v141, v214, v215\n"
        "v_mfma_f32_16x16x32_bf16 v[212:215], v[60:63], v[160:163], 0\n"
        "s_nop 2\n"
        "v_mfma_f32_16x16x32_bf16 v[160:163], v[62:65], v[164:167], 0\n"
        "s_nop 0\n"
        "v_cvt_pk_bf16_f32 v142, v120, v121\n"
        "v_cvt_pk_bf16_f32 v143, v122, v123\n"
        "v_mfma_f32_16x16x32_bf16 v[216:219], v[182:185], a[72:75], 0\n"
        "buffer_load_dwordx4 v3, s[36:39], s94 offen offset:3072 lds\n"
        "v_cvt_pk_bf16_f32 v120, v212, v213\n"
        "v_cvt_pk_bf16_f32 v212, v214, v215\n"
        "v_mfma_f32_16x16x32_bf16 v[216:219], v[188:191], a[76:79], v[216:219]\n"
        "v_cvt_pk_bf16_f32 v220, v160, v161\n"
        "v_cvt_pk_bf16_f32 v121, v162, v163\n"
        "v_mfma_f32_16x16x32_bf16 v[216:219], v[194:197], a[80:83], v[216:219]\n"
        "s_add_u32 m0, s52, 4096\n"
        "v_mfma_f32_16x16x32_bf16 v[160:163], v[194:197], a[92:95], 0\n"
        "s_nop 2\n"
        "v_mfma_f32_16x16x32_bf16 v[160:163], v[188:191], a[88:91], v[160:163]\n"
        "s_nop 0\n"
        "buffer_load_dwordx4 v4, s[36:39], s94 offen lds\n"
        "v_cvt_pk_bf16_f32 v60, v216, v217\n"
        "v_cvt_pk_bf16_f32 v61, v218, v219\n"
        "v_mfma_f32_16x16x32_bf16 v[160:163], v[182:185], a[84:87], v[160:163]\n"
        "s_nop 1\n"
        "v_mfma_f32_16x16x32_bf16 v[214:217], v[66:69], v[166:169], 0\n"
        "s_nop 2\n"
        "v_mfma_f32_16x16x32_bf16 v[164:167], v[68:71], v[170:173], 0\n"
        "s_nop 0\n"
        "v_cvt_pk_bf16_f32 v64, v160, v161\n"
        "v_cvt_pk_bf16_f32 v65, v162, v163\n"
        "v_mfma_f32_16x16x32_bf16 v[160:163], v[182:185], a[96:99], 0\n"
        "buffer_load_dwordx4 v5, s[36:39], s94 offen offset:1024 lds\n"
        "v_cvt_pk_bf16_f32 v213, v214, v215\n"
        "v_cvt_pk_bf16_f32 v221, v216, v217\n"
        "v_mfma_f32_16x16x32_bf16 v[160:163], v[188:191], a[100:103], v[160:163]\n"
        "v_cvt_pk_bf16_f32 v122, v164, v165\n"
        "v_cvt_pk_bf16_f32 v214, v166, v167\n"
        "v_mfma_f32_16x16x32_bf16 v[160:163], v[194:197], a[104:107], v[160:163]\n"
        "s_nop 0\n"
        "v_mfma_f32_16x16x32_bf16 v[164:167], v[194:197], a[116:119], 0\n"
        "s_nop 2\n"
        "v_mfma_f32_16x16x32_bf16 v[164:167], v[188:191], a[112:115], v[164:167]\n"
        "s_nop 0\n"
        "buffer_load_dwordx4 v6, s[36:39], s94 offen offset:2048 lds\n"
        "v_cvt_pk_bf16_f32 v66, v160, v161\n"
        "v_mfma_f32_16x16x32_bf16 v[164:167], v[182:185], a[108:111], v[164:167]\n"
        "s_nop 0\n"
        "v_cvt_pk_bf16_f32 v67, v162, v163\n"
        "v_mfma_f32_16x16x32_bf16 v[168:171], v[72:75], v[172:175], 0\n"
        "s_nop 2\n"
        "v_mfma_f32_16x16x32_bf16 v[160:163], v[74:77], v[176:179], 0\n"
        "s_nop 0\n"
        "v_cvt_pk_bf16_f32 v70, v164, v165\n"
        "v_cvt_pk_bf16_f32 v71, v166, v167\n"
        "v_mfma_f32_16x16x32_bf16 v[164:167], v[182:185], a[120:123], 0\n"
        "buffer_load_dwordx4 v7, s[36:39], s94 offen offset:3072 lds\n"
        "v_cvt_pk_bf16_f32 v222, v168, v169\n"
        "v_cvt_pk_bf16_f32 v123, v170, v171\n"
        "v_mfma_f32_16x16x32_bf16 v[164:167], v[188:191], a[124:127], v[164:167]\n"
        "ds_write_b128 v12, v[120:123]\n"
        "v_cvt_pk_bf16_f32 v215, v160, v161\n"
        "v_mfma_f32_16x16x32_bf16 v[164:167], v[194:197], a[128:131], v[164:167]\n"
        "s_add_u32 m0, s52, 8192\n"
        "ds_write_b128 v12, v[212:215] offset:1024\n"
        "v_cvt_pk_bf16_f32 v223, v162, v163\n"
        "v_mfma_f32_16x16x32_bf16 v[160:163], v[194:197], a[140:143], 0\n"
        "ds_write_b128 v12, v[220:223] offset:2048\n"
        "v_mfma_f32_16x16x32_bf16 v[160:163], v[188:191], a[136:139], v[160:163]\n"
        "s_nop 0\n"
        "buffer_load_dwordx4 v8, s[36:39], s94 offen lds\n"
        "v_cvt_pk_bf16_f32 v72, v164, v165\n"
        "v_mfma_f32_16x16x32_bf16 v[160:163], v[182:185], a[132:135], v[160:163]\n"
        "s_nop 0\n"
        "v_cvt_pk_bf16_f32 v73, v166, v167\n"
        "v_mfma_f32_16x16x32_bf16 v[164:167], v[120:123], v[120:123], 0\n"
        "s_nop 2\n"
        "v_mfma_f32_16x16x32_bf16 v[164:167], v[212:215], v[212:215], v[164:167]\n"
        "s_nop 0\n"
        "v_cvt_pk_bf16_f32 v76, v160, v161\n"
        "v_cvt_pk_bf16_f32 v77, v162, v163\n"
        "v_mfma_f32_16x16x32_bf16 v[164:167], v[220:223], v[220:223], v[164:167]\n"
        "s_nop 1\n"
        "v_mfma_f32_16x16x32_bf16 v[160:163], v[124:127], v[124:127], 0\n"
        "s_nop 2\n"
        "v_mfma_f32_16x16x32_bf16 v[160:163], v[130:133], v[130:133], v[160:163]\n"
        "s_nop 0\n"
        "v_cndmask_b32_e64 v254, v164, v165, s[56:57]\n"
        "v_cndmask_b32_e64 v254, v254, v166, s[58:59]\n"
        "v_cndmask_b32_e64 v254, v254, v167, s[60:61]\n"
        "v_mfma_f32_16x16x32_bf16 v[160:163], v[136:139], v[136:139], v[160:163]\n"
        "s_nop 1\n"
        "v_mfma_f32_16x16x32_bf16 v[120:123], v[78:81], v[136:139], 0\n"
        "s_nop 2\n"
        "v_mfma_f32_16x16x32_bf16 v[120:123], v[82:85], v[130:133], v[120:123]\n"
        "s_nop 0\n"
        "v_cndmask_b32_e64 v252, v160, v161, s[56:57]\n"
        "v_cndmask_b32_e64 v252, v252, v162, s[58:59]\n"
        "v_mfma_f32_16x16x32_bf16 v[120:123], v[86:89], v[124:127], v[120:123]\n"
        "v_cndmask_b32_e64 v252, v252, v163, s[60:61]\n"
        "v_mfma_f32_16x16x32_bf16 v[164:167], v[60:63], v[180:183], 0\n"
        "s_nop 1\n"
        "v_mfma_f32_16x16x32_bf16 v[170:173], v[62:65], v[184:187], 0\n"
        "s_nop 2\n"
        "v_mfma_f32_16x16x32_bf16 v[160:163], v[66:69], v[186:189], 0\n"
        "s_nop 0\n"
        "v_cvt_pk_bf16_f32 v174, v164, v165\n"
        "v_cvt_pk_bf16_f32 v164, v166, v167\n"
        "v_mfma_f32_16x16x32_bf16 v[166:169], v[68:71], v[190:193], 0\n"
        "v_cvt_pk_bf16_f32 v70, v170, v171\n"
        "v_cvt_pk_bf16_f32 v175, v172, v173\n"
        "v_mfma_f32_16x16x32_bf16 v[170:173], v[72:75], v[192:195], 0\n"
        "s_nop 0\n"
        "v_cvt_pk_bf16_f32 v165, v160, v161\n"
        "v_cvt_pk_bf16_f32 v71, v162, v163\n"
        "v_mfma_f32_16x16x32_bf16 v[64:67], v[74:77], v[196:199], 0\n"
        "s_nop 0\n"
        "v_cvt_pk_bf16_f32 v176, v166, v167\n"
        "v_cvt_pk_bf16_f32 v166, v168, v169\n"
        "v_mfma_f32_16x16x32_bf16 v[186:189], v[104:107], v[224:227], 0\n"
        "v_cvt_pk_bf16_f32 v72, v170, v171\n"
        "v_cvt_pk_bf16_f32 v177, v172, v173\n"
        "v_mfma_f32_16x16x32_bf16 v[186:189], v[110:113], v[208:211], v[186:189]\n"
        "ds_write_b128 v12, v[174:177] offset:3072\n"
        "v_cvt_pk_bf16_f32 v167, v64, v65\n"
        "v_mfma_f32_16x16x32_bf16 v[186:189], v[116:119], v[140:143], v[186:189]\n"
        "ds_write_b128 v12, v[164:167] offset:4096\n"
        "v_cvt_pk_bf16_f32 v73, v66, v67\n"
        "v_mfma_f32_16x16x32_bf16 v[160:163], a[8:11], v[136:139], 0\n"
        "ds_write_b128 v12, v[70:73] offset:5120\n"
        "v_mfma_f32_16x16x32_bf16 v[160:163], a[4:7], v[130:133], v[160:163]\n"
        "v_mfma_f32_16x16x32_bf16 v[160:163], a[0:3], v[124:127], v[160:163]\n"
        "s_nop 1\n"
        "v_mfma_f32_16x16x32_bf16 v[140:143], v[174:177], v[174:177], 0\n"
        "s_nop 2\n"
        "v_mfma_f32_16x16x32_bf16 v[140:143], v[164:167], v[164:167], v[140:143]\n"
        "s_nop 0\n"
        "v_cvt_pk_bf16_f32 v64, v160, v161\n"
        "v_cvt_pk_bf16_f32 v65, v162, v163\n"
        "v_mfma_f32_16x16x32_bf16 v[140:143], v[70:73], v[70:73], v[140:143]\n"
        "s_nop 1\n"
        "v_mfma_f32_16x16x32_bf16 v[70:73], a[20:23], v[136:139], 0\n"
        "s_nop 2\n"
        "v_mfma_f32_16x16x32_bf16 v[70:73], a[16:19], v[130:133], v[70:73]\n"
        "s_nop 0\n"
        "v_cndmask_b32_e64 v251, v140, v141, s[56:57]\n"
        "v_cndmask_b32_e64 v251, v251, v142, s[58:59]\n"
        "v_cndmask_b32_e64 v251, v251, v143, s[60:61]\n"
        "v_mfma_f32_16x16x32_bf16 v[70:73], a[12:15], v[124:127], v[70:73]\n"
        "s_nop 0\n"
        "v_cndmask_b32_dpp v254, v251, v254, vcc row_ror:4 row_mask:0xf bank_mask:0xf\n"
        "v_mul_f32 v254, s80, v254\n"
        "v_fma_f32 v253, -v253, v253, v254\n"
        "v_mfma_f32_16x16x32_bf16 v[140:143], a[24:27], v[124:127], 0\n"
        "v_max_f32 v253, 0, v253\n"
        "v_sub_f32 v254, 0x44800000, v254\n"
        "v_add_f32 v253, s81, v253\n"
        "v_mfma_f32_16x16x32_bf16 v[140:143], a[28:31], v[130:133], v[140:143]\n"
        "v_bfi_b32 v253, s84, v254, v253\n"
        "v_cvt_pk_bf16_f32 v66, v70, v71\n"
        "v_cvt_pk_bf16_f32 v67, v72, v73\n"
        "v_mfma_f32_16x16x32_bf16 v[140:143], a[32:35], v[136:139], v[140:143]\n"
        "s_nop 0\n"
        "v_rsq_f32 v254, v253\n"
        "v_mfma_f32_16x16x32_bf16 v[70:73], a[44:47], v[136:139], 0\n"
        "s_nop 0\n"
        "v_cvt_f16_f32 v254, v254\n"
        "v_mfma_f32_16x16x32_bf16 v[70:73], a[40:43], v[130:133], v[70:73]\n"
        "s_nop 0\n"
        "ds_write_b16 v18, v254\n"
        "v_cvt_pk_bf16_f32 v160, v140, v141\n"
        "v_mfma_f32_16x16x32_bf16 v[70:73], a[36:39], v[124:127], v[70:73]\n"
        "s_nop 0\n"
        "ds_write_b16 v19, v254\n"
        "v_cvt_pk_bf16_f32 v161, v142, v143\n"
        "v_mfma_f32_16x16x32_bf16 v[140:143], a[48:51], v[124:127], 0\n"
        "s_nop 1\n"
        "v_mfma_f32_16x16x32_bf16 v[140:143], a[52:55], v[130:133], v[140:143]\n"
        "s_nop 0\n"
        "v_cvt_pk_bf16_f32 v162, v70, v71\n"
        "v_cvt_pk_bf16_f32 v163, v72, v73\n"
        "v_mfma_f32_16x16x32_bf16 v[140:143], a[56:59], v[136:139], v[140:143]\n"
        "s_nop 1\n"
        "v_mfma_f32_16x16x32_bf16 v[70:73], a[68:71], v[136:139], 0\n"
        "s_nop 2\n"
        "v_mfma_f32_16x16x32_bf16 v[70:73], a[64:67], v[130:133], v[70:73]\n"
        "s_nop 0\n"
        "v_cvt_pk_bf16_f32 v164, v140, v141\n"
        "v_cvt_pk_bf16_f32 v165, v142, v143\n"
        "v_mfma_f32_16x16x32_bf16 v[70:73], a[60:63], v[124:127], v[70:73]\n"
        "s_nop 0\n"
        "v_mfma_f32_16x16x32_bf16 v[140:143], v[144:147], v[144:147], 0\n"
        "s_nop 2\n"
        "v_mfma_f32_16x16x32_bf16 v[140:143], v[150:153], v[150:153], v[140:143]\n"
        "s_waitcnt lgkmcnt(0)\n"
        "s_barrier\n"
        "v_cvt_pk_bf16_f32 v166, v70, v71\n"
        "v_cvt_pk_bf16_f32 v167, v72, v73\n"
        "v_mfma_f32_16x16x32_bf16 v[140:143], v[156:159], v[156:159], v[140:143]\n"
        "s_nop 1\n"
        "v_mfma_f32_16x16x32_bf16 v[168:171], v[78:81], v[156:159], 0\n"
        "s_nop 2\n"
        "v_mfma_f32_16x16x32_bf16 v[168:171], v[82:85], v[150:153], v[168:171]\n"
        "s_nop 0\n"
        "v_cndmask_b32_e64 v254, v140, v141, s[56:57]\n"
        "v_cndmask_b32_e64 v254, v254, v142, s[58:59]\n"
        "v_cndmask_b32_e64 v254, v254, v143, s[60:61]\n"
        "v_mfma_f32_16x16x32_bf16 v[168:171], v[86:89], v[144:147], v[168:171]\n"
        "s_nop 0\n"
        "v_cndmask_b32_dpp v252, v254, v252, vcc row_ror:4 row_mask:0xf bank_mask:0xf\n"
        "v_mfma_f32_16x16x32_bf16 v[70:73], v[124:127], v[64:67], 0\n"
        "s_nop 0\n"
        "v_cndmask_b32_dpp v255, v252, v255, vcc row_ror:4 row_mask:0xf bank_mask:0xf\n"
        "v_mul_f32 v255, s80, v255\n"
        "v_mfma_f32_16x16x32_bf16 v[70:73], v[130:133], v[160:163], v[70:73]\n"
        "s_nop 0\n"
        "v_cndmask_b32_dpp v249, v168, v120, vcc row_ror:4 row_mask:0xf bank_mask:0xf\n"
        "v_cndmask_b32_dpp v246, v169, v121, vcc row_ror:4 row_mask:0xf bank_mask:0xf\n"
        "v_mfma_f32_16x16x32_bf16 v[70:73], v[136:139], v[164:167], v[70:73]\n"
        "v_cndmask_b32_dpp v249, v249, v228, vcc row_ror:4 row_mask:0xf bank_mask:0xf\n"
        "v_cndmask_b32_dpp v246, v246, v229, vcc row_ror:4 row_mask:0xf bank_mask:0xf\n"
        "v_mul_f32 v254, s80, v249\n"
        "v_mfma_f32_16x16x32_bf16 v[120:123], a[8:11], v[156:159], 0\n"
        "v_fma_f32 v254, -v254, v254, v255\n"
        "v_max_f32 v254, 0, v254\n"
        "v_add_f32 v254, s81, v254\n"
        "v_mfma_f32_16x16x32_bf16 v[120:123], a[4:7], v[150:153], v[120:123]\n"
        "v_rsq_f32 v248, v254\n"
        "v_mfma_f32_16x16x32_bf16 v[120:123], a[0:3], v[144:147], v[120:123]\n"
        "s_nop 1\n"
        "v_mfma_f32_16x16x32_bf16 v[64:67], a[12:15], v[144:147], 0\n"
        "s_nop 2\n"
        "v_mfma_f32_16x16x32_bf16 v[64:67], a[16:19], v[150:153], v[64:67]\n"
        "s_nop 0\n"
        "v_cvt_pk_bf16_f32 v170, v120, v121\n"
        "v_cvt_pk_bf16_f32 v171, v122, v123\n"
        "v_mfma_f32_16x16x32_bf16 v[64:67], a[20:23], v[156:159], v[64:67]\n"
        "s_nop 1\n"
        "v_mfma_f32_16x16x32_bf16 v[160:163], a[32:35], v[156:159], 0\n"
        "s_nop 2\n"
        "v_mfma_f32_16x16x32_bf16 v[160:163], a[28:31], v[150:153], v[160:163]\n"
        "s_nop 0\n"
        "v_cvt_pk_bf16_f32 v172, v64, v65\n"
        "v_cvt_pk_bf16_f32 v173, v66, v67\n"
        "v_mfma_f32_16x16x32_bf16 v[160:163], a[24:27], v[144:147], v[160:163]\n"
        "s_nop 1\n"
        "v_mfma_f32_16x16x32_bf16 v[140:143], a[36:39], v[144:147], 0\n"
        "s_nop 2\n"
        "v_mfma_f32_16x16x32_bf16 v[140:143], a[40:43], v[150:153], v[140:143]\n"
        "s_nop 0\n"
        "v_cvt_pk_bf16_f32 v120, v160, v161\n"
        "v_cvt_pk_bf16_f32 v121, v162, v163\n"
        "v_mfma_f32_16x16x32_bf16 v[140:143], a[44:47], v[156:159], v[140:143]\n"
        "s_nop 0\n"
        "v_mfma_f32_16x16x32_bf16 v[64:67], a[56:59], v[156:159], 0\n"
        "s_nop 0\n"
        "ds_read_b128 v[182:185], v13\n"
        "ds_read_b128 v[166:169], v13 offset:1024\n"
        "v_mfma_f32_16x16x32_bf16 v[64:67], a[52:55], v[150:153], v[64:67]\n"
        "s_nop 0\n"
        "ds_read_b128 v[174:177], v13 offset:2048\n"
        "v_cvt_pk_bf16_f32 v122, v140, v141\n"
        "v_cvt_pk_bf16_f32 v123, v142, v143\n"
        "v_mfma_f32_16x16x32_bf16 v[64:67], a[48:51], v[144:147], v[64:67]\n"
        "s_nop 1\n"
        "v_mfma_f32_16x16x32_bf16 v[160:163], a[60:63], v[144:147], 0\n"
        "s_nop 2\n"
        "v_mfma_f32_16x16x32_bf16 v[160:163], a[64:67], v[150:153], v[160:163]\n"
        "s_nop 0\n"
        "v_cvt_pk_bf16_f32 v140, v64, v65\n"
        "v_cvt_pk_bf16_f32 v141, v66, v67\n"
        "v_mfma_f32_16x16x32_bf16 v[160:163], a[68:71], v[156:159], v[160:163]\n"
        "s_nop 1\n"
        "v_mfma_f32_16x16x32_bf16 v[64:67], v[116:119], a[80:83], 0\n"
        "s_nop 2\n"
        "v_mfma_f32_16x16x32_bf16 v[64:67], v[110:113], a[76:79], v[64:67]\n"
        "s_nop 0\n"
        "v_cvt_pk_bf16_f32 v142, v160, v161\n"
        "v_cvt_pk_bf16_f32 v143, v162, v163\n"
        "v_mfma_f32_16x16x32_bf16 v[64:67], v[104:107], a[72:75], v[64:67]\n"
        "s_nop 1\n"
        "v_mfma_f32_16x16x32_bf16 v[178:181], v[104:107], a[84:87], 0\n"
        "s_waitcnt lgkmcnt(0)\n"
        "s_nop 1\n"
        "v_mfma_f32_16x16x32_bf16 v[178:181], v[110:113], a[88:91], v[178:181]\n"
        "s_nop 0\n"
        "v_cvt_pk_bf16_f32 v60, v64, v65\n"
        "v_cvt_pk_bf16_f32 v61, v66, v67\n"
        "v_mfma_f32_16x16x32_bf16 v[178:181], v[116:119], a[92:95], v[178:181]\n"
        "s_nop 1\n"
        "v_mfma_f32_16x16x32_bf16 v[160:163], v[144:147], v[170:173], 0\n"
        "s_nop 2\n"
        "v_mfma_f32_16x16x32_bf16 v[160:163], v[150:153], v[120:123], v[160:163]\n"
        "s_nop 0\n"
        "v_cvt_pk_bf16_f32 v64, v178, v179\n"
        "v_cvt_pk_bf16_f32 v65, v180, v181\n"
        "v_mfma_f32_16x16x32_bf16 v[160:163], v[156:159], v[140:143], v[160:163]\n"
        "s_nop 1\n"
        "v_mfma_f32_16x16x32_bf16 v[120:123], a[152:155], v[174:177], 0\n"
        "s_nop 2\n"
        "v_mfma_f32_16x16x32_bf16 v[120:123], a[148:151], v[166:169], v[120:123]\n"
        "s_nop 0\n"
        "v_cndmask_b32_dpp v247, v160, v70, vcc row_ror:4 row_mask:0xf bank_mask:0xf\n"
        "v_cndmask_b32_dpp v245, v161, v71, vcc row_ror:4 row_mask:0xf bank_mask:0xf\n"
        "v_cndmask_b32_dpp v244, v162, v72, vcc row_ror:4 row_mask:0xf bank_mask:0xf\n"
        "v_mfma_f32_16x16x32_bf16 v[120:123], a[144:147], v[182:185], v[120:123]\n"
        "v_cndmask_b32_dpp v242, v163, v73, vcc row_ror:4 row_mask:0xf bank_mask:0xf\n"
        "v_cndmask_b32_dpp v247, v247, v186, vcc row_ror:4 row_mask:0xf bank_mask:0xf\n"
        "v_cndmask_b32_dpp v245, v245, v187, vcc row_ror:4 row_mask:0xf bank_mask:0xf\n"
        "v_mfma_f32_16x16x32_bf16 v[194:197], a[156:159], v[182:185], 0\n"
        "ds_read_b128 v[170:173], v13 offset:3072\n"
        "ds_read_b128 v[162:165], v13 offset:4096\n"
        "v_cndmask_b32_dpp v244, v244, v188, vcc row_ror:4 row_mask:0xf bank_mask:0xf\n"
        "v_mfma_f32_16x16x32_bf16 v[194:197], a[160:163], v[166:169], v[194:197]\n"
        "ds_read_b128 v[178:181], v13 offset:5120\n"
        "ds_read_b128 v[70:73], v20\n"
        "v_cndmask_b32_dpp v242, v242, v189, vcc row_ror:4 row_mask:0xf bank_mask:0xf\n"
        "v_mfma_f32_16x16x32_bf16 v[194:197], a[164:167], v[174:177], v[194:197]\n"
        "ds_read_b64 v[66:67], v21\n"
        "v_mul_f32_dpp v247, v248, v247 quad_perm:[0,0,0,0] row_mask:0xf bank_mask:0xf\n"
        "v_mul_f32_dpp v245, v248, v245 quad_perm:[1,1,1,1] row_mask:0xf bank_mask:0xf\n"
        "v_mfma_f32_16x16x32_bf16 v[186:189], a[176:179], v[174:177], 0\n"
        "v_mul_f32_dpp v244, v248, v244 quad_perm:[2,2,2,2] row_mask:0xf bank_mask:0xf\n"
        "v_mul_f32_dpp v242, v248, v242 quad_perm:[3,3,3,3] row_mask:0xf bank_mask:0xf\n"
        "v_max3_f32 v255, v247, v245, v244\n"
        "v_mfma_f32_16x16x32_bf16 v[186:189], a[172:175], v[166:169], v[186:189]\n"
        "v_max_f32 v255, v255, v242\n"
        "v_mul_f32 v255, v255, v248\n"
        "v_fma_f32 v247, v247, v248, -v255\n"
        "v_mfma_f32_16x16x32_bf16 v[186:189], a[168:171], v[182:185], v[186:189]\n"
        "v_fma_f32 v245, v245, v248, -v255\n"
        "v_fma_f32 v244, v244, v248, -v255\n"
        "v_fma_f32 v242, v242, v248, -v255\n"
        "v_mfma_f32_16x16x32_bf16 v[220:223], a[180:183], v[182:185], 0\n"
        "v_exp_f32 v247, v247\n"
        "v_mfma_f32_16x16x32_bf16 v[220:223], a[184:187], v[166:169], v[220:223]\n"
        "v_exp_f32 v245, v245\n"
        "v_mfma_f32_16x16x32_bf16 v[220:223], a[188:191], v[174:177], v[220:223]\n"
        "v_exp_f32 v244, v244\n"
        "v_add_f32 v241, v247, v245\n"
        "v_mfma_f32_16x16x32_bf16 v[216:219], a[200:203], v[174:177], 0\n"
        "s_waitcnt lgkmcnt(0)\n"
        "v_cvt_f32_f16_sdwa v254, v66 dst_sel:DWORD dst_unused:UNUSED_PAD src0_sel:WORD_0\n"
        "v_cvt_f32_f16_sdwa v252, v66 dst_sel:DWORD dst_unused:UNUSED_PAD src0_sel:WORD_1\n"
        "v_cvt_f32_f16_sdwa v251, v70 dst_sel:DWORD dst_unused:UNUSED_PAD src0_sel:WORD_0\n"
        "v_mfma_f32_16x16x32_bf16 v[216:219], a[196:199], v[166:169], v[216:219]\n"
        "s_barrier\n"
        "v_cvt_f32_f16_sdwa v199, v70 dst_sel:DWORD dst_unused:UNUSED_PAD src0_sel:WORD_1\n"
        "v_cvt_f32_f16_sdwa v200, v71 dst_sel:DWORD dst_unused:UNUSED_PAD src0_sel:WORD_0\n"
        "v_cvt_f32_f16_sdwa v250, v71 dst_sel:DWORD dst_unused:UNUSED_PAD src0_sel:WORD_1\n"
        "v_mfma_f32_16x16x32_bf16 v[216:219], a[192:195], v[182:185], v[216:219]\n"
        "v_cvt_f32_f16_sdwa v255, v72 dst_sel:DWORD dst_unused:UNUSED_PAD src0_sel:WORD_0\n"
        "v_cvt_f32_f16_sdwa v253, v72 dst_sel:DWORD dst_unused:UNUSED_PAD src0_sel:WORD_1\n"
        "v_mul_f32 v120, v120, v254\n"
        "v_mfma_f32_16x16x32_bf16 v[232:235], a[204:207], v[182:185], 0\n"
        "v_exp_f32 v242, v242\n"
        "v_mul_f32 v121, v121, v254\n"
        "v_mfma_f32_16x16x32_bf16 v[232:235], a[208:211], v[166:169], v[232:235]\n"
        "v_mul_f32 v122, v122, v254\n"
        "v_mul_f32 v123, v123, v254\n"
        "v_add_f32 v243, v244, v242\n"
        "v_mfma_f32_16x16x32_bf16 v[232:235], a[212:215], v[174:177], v[232:235]\n"
        "v_cvt_pk_bf16_f32 v224, v120, v121\n"
        "v_cvt_pk_bf16_f32 v225, v122, v123\n"
        "v_add_f32 v241, v241, v243\n"
        "v_mfma_f32_16x16x32_bf16 v[190:193], a[152:155], v[178:181], 0\n"
        "v_rcp_f32 v241, v241\n"
        "v_mul_f32 v194, v194, v254\n"
        "v_mfma_f32_16x16x32_bf16 v[190:193], a[148:151], v[162:165], v[190:193]\n"
        "v_mul_f32 v247, v247, v241\n"
        "v_mul_f32 v245, v245, v241\n"
        "v_mul_f32 v244, v244, v241\n"
        "v_mfma_f32_16x16x32_bf16 v[190:193], a[144:147], v[170:173], v[190:193]\n"
        "v_mul_f32 v242, v242, v241\n"
        "v_mul_f32_dpp v247, v248, v247 quad_perm:[0,0,0,0] row_mask:0xf bank_mask:0xf\n"
        "v_mul_f32_dpp v245, v248, v245 quad_perm:[1,1,1,1] row_mask:0xf bank_mask:0xf\n"
        "v_mfma_f32_16x16x32_bf16 v[228:231], a[156:159], v[170:173], 0\n"
        "v_mul_f32_dpp v244, v248, v244 quad_perm:[2,2,2,2] row_mask:0xf bank_mask:0xf\n"
        "v_mul_f32_dpp v242, v248, v242 quad_perm:[3,3,3,3] row_mask:0xf bank_mask:0xf\n"
        "v_cvt_pk_bf16_f32 v248, v247, v245\n"
        "v_mfma_f32_16x16x32_bf16 v[228:231], a[160:163], v[162:165], v[228:231]\n"
        "v_cvt_pk_bf16_f32 v243, v244, v242\n"
        "v_and_b32 v102, v25, v248\n"
        "v_and_b32 v103, v25, v243\n"
        "v_mfma_f32_16x16x32_bf16 v[228:231], a[164:167], v[178:181], v[228:231]\n"
        "v_mov_b64 v[108:109], v[102:103]\n"
        "v_mov_b64 v[114:115], v[102:103]\n"
        "v_mov_b64 v[120:121], v[102:103]\n"
        "v_mfma_f32_16x16x32_bf16 v[70:73], a[176:179], v[178:181], 0\n"
        "v_mul_f32_dpp v241, v246, v247 quad_perm:[0,0,0,0] row_mask:0xf bank_mask:0xf\n"
        "v_and_b32_dpp v122, v248, v25 row_ror:12 row_mask:0xf bank_mask:0xf\n"
        "v_and_b32_dpp v123, v243, v25 row_ror:12 row_mask:0xf bank_mask:0xf\n"
        "v_mfma_f32_16x16x32_bf16 v[70:73], a[172:175], v[162:165], v[70:73]\n"
        "v_and_b32_dpp v142, v248, v25 row_ror:8 row_mask:0xf bank_mask:0xf\n"
        "v_and_b32_dpp v143, v243, v25 row_ror:8 row_mask:0xf bank_mask:0xf\n"
        "v_mul_f32 v195, v195, v254\n"
        "v_mfma_f32_16x16x32_bf16 v[70:73], a[168:171], v[170:173], v[70:73]\n"
        "v_mul_f32 v196, v196, v254\n"
        "v_mul_f32 v197, v197, v254\n"
        "v_mov_b64 v[128:129], v[122:123]\n"
        "v_mfma_f32_16x16x32_bf16 v[236:239], a[180:183], v[170:173], 0\n"
        "v_mov_b64 v[134:135], v[122:123]\n"
        "v_mov_b64 v[140:141], v[122:123]\n"
        "v_mov_b64 v[148:149], v[142:143]\n"
        "v_mfma_f32_16x16x32_bf16 v[236:239], a[184:187], v[162:165], v[236:239]\n"
        "v_mov_b64 v[154:155], v[142:143]\n"
        "v_mov_b64 v[160:161], v[142:143]\n"
        "v_cvt_pk_bf16_f32 v226, v194, v195\n"
        "v_mfma_f32_16x16x32_bf16 v[236:239], a[188:191], v[178:181], v[236:239]\n"
        "v_fmac_f32_dpp v241, v246, v245 quad_perm:[1,1,1,1] row_mask:0xf bank_mask:0xf\n"
        "v_cvt_pk_bf16_f32 v227, v196, v197\n"
        "v_mul_f32 v186, v186, v254\n"
        "v_mfma_f32_16x16x32_bf16 v[208:211], a[200:203], v[178:181], 0\n"
        "ds_write_b128 v202, v[224:227]\n"
        "v_fmac_f32_dpp v241, v246, v244 quad_perm:[2,2,2,2] row_mask:0xf bank_mask:0xf\n"
        "v_mfma_f32_16x16x32_bf16 v[208:211], a[196:199], v[162:165], v[208:211]\n"
        "s_nop 0\n"
        "v_fmac_f32_dpp v241, v246, v242 quad_perm:[3,3,3,3] row_mask:0xf bank_mask:0xf\n"
        "v_add_f32 v241, v241, v249\n"
        "v_mul_f32 v249, s80, v241\n"
        "v_mfma_f32_16x16x32_bf16 v[208:211], a[192:195], v[170:173], v[208:211]\n"
        "v_mul_f32 v187, v187, v254\n"
        "v_mul_f32 v188, v188, v254\n"
        "v_mul_f32 v189, v189, v254\n"
        "v_mfma_f32_16x16x32_bf16 v[212:215], a[204:207], v[170:173], 0\n"
        "v_cvt_pk_bf16_f32 v194, v186, v187\n"
        "v_cvt_pk_bf16_f32 v195, v188, v189\n"
        "v_mul_f32 v220, v220, v254\n"
        "v_mfma_f32_16x16x32_bf16 v[212:215], a[208:211], v[162:165], v[212:215]\n"
        "v_mul_f32 v221, v221, v254\n"
        "v_mul_f32 v222, v222, v254\n"
        "v_mul_f32 v223, v223, v254\n"
        "v_mfma_f32_16x16x32_bf16 v[212:215], a[212:215], v[178:181], v[212:215]\n"
        "v_cvt_pk_bf16_f32 v196, v220, v221\n"
        "v_cvt_pk_bf16_f32 v197, v222, v223\n"
        "v_mul_f32 v216, v216, v254\n"
        "v_mfma_f32_16x16x32_bf16 v[220:223], v[116:119], a[104:107], 0\n"
        "ds_write_b128 v202, v[194:197] offset:64\n"
        "v_mul_f32 v217, v217, v254\n"
        "v_mfma_f32_16x16x32_bf16 v[220:223], v[110:113], a[100:103], v[220:223]\n"
        "v_mul_f32 v218, v218, v254\n"
        "v_mul_f32 v219, v219, v254\n"
        "v_cvt_pk_bf16_f32 v240, v216, v217\n"
        "v_mfma_f32_16x16x32_bf16 v[220:223], v[104:107], a[96:99], v[220:223]\n"
        "v_cvt_pk_bf16_f32 v241, v218, v219\n"
        "v_mul_f32 v232, v232, v254\n"
        "v_mul_f32 v233, v233, v254\n"
        "v_mfma_f32_16x16x32_bf16 v[244:247], v[104:107], a[108:111], 0\n"
        "v_mul_f32 v234, v234, v254\n"
        "v_mul_f32 v235, v235, v254\n"
        "v_cvt_pk_bf16_f32 v242, v232, v233\n"
        "v_mfma_f32_16x16x32_bf16 v[244:247], v[110:113], a[112:115], v[244:247]\n"
        "v_cvt_pk_bf16_f32 v243, v234, v235\n"
        "v_mul_f32 v190, v190, v252\n"
        "v_mul_f32 v191, v191, v252\n"
        "v_mfma_f32_16x16x32_bf16 v[244:247], v[116:119], a[116:119], v[244:247]\n"
        "ds_write_b128 v202, v[240:243] offset:128\n"
        "v_mul_f32 v192, v192, v252\n"
        "v_mfma_f32_16x16x32_bf16 v[216:219], v[60:63], v[102:105], 0\n"
        "v_mul_f32 v193, v193, v252\n"
        "v_cvt_pk_bf16_f32 v186, v190, v191\n"
        "v_cvt_pk_bf16_f32 v187, v192, v193\n"
        "v_mfma_f32_16x16x32_bf16 v[232:235], v[62:65], v[106:109], 0\n"
        "v_mul_f32 v228, v228, v252\n"
        "v_mul_f32 v229, v229, v252\n"
        "v_mul_f32 v230, v230, v252\n"
        "v_mfma_f32_16x16x32_bf16 v[190:193], v[182:185], v[224:227], 0\n"
        "v_mul_f32 v231, v231, v252\n"
        "v_cvt_pk_bf16_f32 v188, v228, v229\n"
        "v_cvt_pk_bf16_f32 v189, v230, v231\n"
        "v_mfma_f32_16x16x32_bf16 v[190:193], v[166:169], v[194:197], v[190:193]\n"
        "ds_write_b128 v203, v[186:189]\n"
        "v_mul_f32 v70, v70, v252\n"
        "v_mfma_f32_16x16x32_bf16 v[190:193], v[174:177], v[240:243], v[190:193]\n"
        "v_mul_f32 v71, v71, v252\n"
        "v_mul_f32 v72, v72, v252\n"
        "v_mul_f32 v73, v73, v252\n"
        "v_mfma_f32_16x16x32_bf16 v[224:227], v[116:119], a[128:131], 0\n"
        "v_cvt_pk_bf16_f32 v228, v70, v71\n"
        "v_cvt_pk_bf16_f32 v229, v72, v73\n"
        "v_mul_f32 v236, v236, v252\n"
        "v_mfma_f32_16x16x32_bf16 v[224:227], v[110:113], a[124:127], v[224:227]\n"
        "v_mul_f32 v237, v237, v252\n"
        "v_mul_f32 v238, v238, v252\n"
        "v_mul_f32 v239, v239, v252\n"
        "v_mfma_f32_16x16x32_bf16 v[224:227], v[104:107], a[120:123], v[224:227]\n"
        "v_cvt_pk_bf16_f32 v230, v236, v237\n"
        "v_cvt_pk_bf16_f32 v231, v238, v239\n"
        "v_mul_f32 v208, v208, v252\n"
        "v_mfma_f32_16x16x32_bf16 v[194:197], v[104:107], a[132:135], 0\n"
        "ds_write_b128 v203, v[228:231] offset:64\n"
        "v_mul_f32 v209, v209, v252\n"
        "v_mfma_f32_16x16x32_bf16 v[194:197], v[110:113], a[136:139], v[194:197]\n"
        "v_mul_f32 v210, v210, v252\n"
        "v_mul_f32 v211, v211, v252\n"
        "v_cvt_pk_bf16_f32 v64, v208, v209\n"
        "v_mfma_f32_16x16x32_bf16 v[194:197], v[116:119], a[140:143], v[194:197]\n"
        "v_cvt_pk_bf16_f32 v65, v210, v211\n"
        "v_mul_f32 v212, v212, v252\n"
        "v_mul_f32 v213, v213, v252\n"
        "v_cvt_pk_bf16_f32 v66, v220, v221\n"
        "v_cvt_pk_bf16_f32 v67, v222, v223\n"
        "s_nop 1\n"
        "v_mfma_f32_16x16x32_bf16 v[102:105], v[66:69], v[108:111], 0\n"
        "v_mul_f32 v214, v214, v252\n"
        "v_mul_f32 v215, v215, v252\n"
        "v_cvt_pk_bf16_f32 v66, v212, v213\n"
        "v_cvt_pk_bf16_f32 v70, v244, v245\n"
        "v_cvt_pk_bf16_f32 v71, v246, v247\n"
        "s_nop 1\n"
        "v_mfma_f32_16x16x32_bf16 v[220:223], v[68:71], v[112:115], 0\n"
        "v_cvt_pk_bf16_f32 v67, v214, v215\n"
        "v_cvt_pk_bf16_f32 v208, v216, v217\n"
        "v_cvt_pk_bf16_f32 v212, v218, v219\n"
        "v_mfma_f32_16x16x32_bf16 v[106:109], v[170:173], v[186:189], 0\n"
        "ds_write_b128 v203, v[64:67] offset:128\n"
        "v_cvt_pk_bf16_f32 v186, v232, v233\n"
        "v_mfma_f32_16x16x32_bf16 v[106:109], v[162:165], v[228:231], v[106:109]\n"
        "v_cvt_pk_bf16_f32 v209, v234, v235\n"
        "v_cndmask_b32_e64 v248, v190, v191, s[56:57]\n"
        "v_cndmask_b32_e64 v248, v248, v192, s[58:59]\n"
        "v_mfma_f32_16x16x32_bf16 v[106:109], v[178:181], v[64:67], v[106:109]\n"
        "ds_read_b128 v[94:97], v27 offset:64\n"
        "ds_read_b128 v[98:101], v27 offset:128\n"
        "v_cndmask_b32_e64 v248, v248, v193, s[60:61]\n"
        "v_mfma_f32_16x16x32_bf16 v[64:67], v[136:139], a[80:83], 0\n"
        "v_mul_f32 v248, v248, v254\n"
        "v_cvt_pk_bf16_f32 v72, v224, v225\n"
        "v_cvt_pk_bf16_f32 v73, v226, v227\n"
        "v_mfma_f32_16x16x32_bf16 v[64:67], v[130:133], a[76:79], v[64:67]\n"
        "v_exp_f32 v248, v248\n"
        "v_cvt_pk_bf16_f32 v76, v194, v195\n"
        "v_mfma_f32_16x16x32_bf16 v[64:67], v[124:127], a[72:75], v[64:67]\n"
        "ds_write_b32 v204, v248\n"
        "v_cvt_pk_bf16_f32 v77, v196, v197\n"
        "v_mfma_f32_16x16x32_bf16 v[110:113], v[124:127], a[84:87], 0\n"
        "v_cvt_pk_bf16_f32 v213, v102, v103\n"
        "v_cvt_pk_bf16_f32 v187, v104, v105\n"
        "v_cvt_pk_bf16_f32 v210, v220, v221\n"
        "v_mfma_f32_16x16x32_bf16 v[110:113], v[130:133], a[88:91], v[110:113]\n"
        "v_cvt_pk_bf16_f32 v214, v222, v223\n"
        "v_cndmask_b32_e64 v248, v106, v107, s[56:57]\n"
        "v_cndmask_b32_e64 v248, v248, v108, s[58:59]\n"
        "v_mfma_f32_16x16x32_bf16 v[110:113], v[136:139], a[92:95], v[110:113]\n"
        "v_cndmask_b32_e64 v248, v248, v109, s[60:61]\n"
        "v_mul_f32 v248, v248, v252\n"
        "v_cvt_pk_bf16_f32 v60, v64, v65\n"
        "v_mfma_f32_16x16x32_bf16 v[102:105], v[72:75], v[114:117], 0\n"
        "s_waitcnt lgkmcnt(1)\n"
        "v_exp_f32 v248, v248\n"
        "v_cvt_pk_bf16_f32 v61, v66, v67\n"
        "v_mfma_f32_16x16x32_bf16 v[70:73], v[74:77], v[118:121], 0\n"
        "ds_write_b32 v205, v248\n"
        "v_cvt_pk_bf16_f32 v64, v110, v111\n"
        "v_mfma_f32_16x16x32_bf16 v[114:117], v[174:177], a[224:227], 0\n"
        "v_cvt_pk_bf16_f32 v65, v112, v113\n"
        "v_cvt_pk_bf16_f32 v188, v102, v103\n"
        "v_cvt_pk_bf16_f32 v211, v104, v105\n"
        "v_mfma_f32_16x16x32_bf16 v[114:117], v[166:169], a[220:223], v[114:117]\n"
        "ds_write_b128 v12, v[208:211]\n"
        "v_cvt_pk_bf16_f32 v215, v70, v71\n"
        "v_mfma_f32_16x16x32_bf16 v[114:117], v[182:185], a[216:219], v[114:117]\n"
        "s_nop 0\n"
        "ds_write_b128 v12, v[212:215] offset:1024\n"
        "v_cvt_pk_bf16_f32 v189, v72, v73\n"
        "v_mfma_f32_16x16x32_bf16 v[102:105], v[182:185], a[228:231], 0\n"
        "s_nop 0\n"
        "ds_write_b128 v12, v[186:189] offset:2048\n"
        "v_mfma_f32_16x16x32_bf16 v[102:105], v[166:169], a[232:235], v[102:105]\n"
        "s_nop 0\n"
        "v_mul_f32 v114, v114, v251\n"
        "v_mul_f32 v115, v115, v199\n"
        "v_mul_f32 v116, v116, v200\n"
        "v_mfma_f32_16x16x32_bf16 v[102:105], v[174:177], a[236:239], v[102:105]\n"
        "s_nop 0\n"
        "v_mul_f32 v117, v117, v250\n"
        "v_mfma_f32_16x16x32_bf16 v[70:73], v[182:185], v[94:97], 0\n"
        "s_nop 2\n"
        "v_mfma_f32_16x16x32_bf16 v[118:121], v[182:185], v[98:101], 0\n"
        "s_nop 0\n"
        "v_mul_f32 v102, v102, v251\n"
        "v_mul_f32 v103, v103, v199\n"
        "v_mul_f32 v104, v104, v200\n"
        "v_mfma_f32_16x16x32_bf16 v[106:109], v[136:139], a[104:107], 0\n"
        "v_mul_f32 v105, v105, v250\n"
        "v_cvt_pk_f16_f32 v114, v114, v102\n"
        "v_cvt_pk_f16_f32 v115, v115, v103\n"
        "v_mfma_f32_16x16x32_bf16 v[106:109], v[130:133], a[100:103], v[106:109]\n"
        "v_cvt_pk_f16_f32 v116, v116, v104\n"
        "v_cvt_pk_f16_f32 v117, v117, v105\n"
        "v_perm_b32 v76, v114, v70, s54\n"
        "v_mfma_f32_16x16x32_bf16 v[106:109], v[124:127], a[96:99], v[106:109]\n"
        "v_perm_b32 v66, v115, v71, s54\n"
        "v_perm_b32 v70, v116, v72, s54\n"
        "v_perm_b32 v72, v117, v73, s54\n"
        "v_mfma_f32_16x16x32_bf16 v[110:113], v[124:127], a[108:111], 0\n"
        "v_perm_b32 v77, v114, v118, s55\n"
        "v_perm_b32 v67, v115, v119, s55\n"
        "v_perm_b32 v71, v116, v120, s55\n"
        "v_mfma_f32_16x16x32_bf16 v[110:113], v[130:133], a[112:115], v[110:113]\n"
        "ds_write_b64 v206, v[76:77]\n"
        "v_perm_b32 v73, v117, v121, s55\n"
        "v_mfma_f32_16x16x32_bf16 v[110:113], v[136:139], a[116:119], v[110:113]\n"
        "ds_write_b64 v206, v[66:67] offset:3136\n"
        "v_cvt_pk_bf16_f32 v66, v106, v107\n"
        "v_mfma_f32_16x16x32_bf16 v[118:121], v[60:63], v[122:125], 0\n"
        "ds_write_b64 v206, v[70:71] offset:6272\n"
        "v_cvt_pk_bf16_f32 v67, v108, v109\n"
        "v_mfma_f32_16x16x32_bf16 v[102:105], v[62:65], v[126:129], 0\n"
        "s_nop 0\n"
        "ds_write_b64 v206, v[72:73] offset:9408\n"
        "v_cvt_pk_bf16_f32 v70, v110, v111\n"
        "v_mfma_f32_16x16x32_bf16 v[190:193], v[174:177], a[248:251], 0\n"
        "v_cvt_pk_bf16_f32 v71, v112, v113\n"
        "v_cvt_pk_bf16_f32 v116, v118, v119\n"
        "v_cvt_pk_bf16_f32 v112, v120, v121\n"
        "v_mfma_f32_16x16x32_bf16 v[190:193], v[166:169], a[244:247], v[190:193]\n"
        "v_cvt_pk_bf16_f32 v106, v102, v103\n"
        "v_cvt_pk_bf16_f32 v117, v104, v105\n"
        "v_mfma_f32_16x16x32_bf16 v[190:193], v[182:185], a[240:243], v[190:193]\n"
        "s_nop 1\n"
        "v_mfma_f32_16x16x32_bf16 v[194:197], v[182:185], a[252:255], 0\n"
        "s_nop 2\n"
        "v_mfma_f32_16x16x32_bf16 v[194:197], v[166:169], v[28:31], v[194:197]\n"
        "s_nop 0\n"
        "v_mul_f32 v190, v190, v251\n"
        "v_mul_f32 v191, v191, v199\n"
        "v_mul_f32 v192, v192, v200\n"
        "v_mfma_f32_16x16x32_bf16 v[194:197], v[174:177], v[32:35], v[194:197]\n"
        "s_nop 0\n"
        "v_mul_f32 v193, v193, v250\n"
        "v_mfma_f32_16x16x32_bf16 v[102:105], v[166:169], v[94:97], 0\n"
        "s_nop 2\n"
        "v_mfma_f32_16x16x32_bf16 v[108:111], v[166:169], v[98:101], 0\n"
        "s_nop 0\n"
        "v_mul_f32 v194, v194, v251\n"
        "v_mul_f32 v195, v195, v199\n"
        "v_mul_f32 v196, v196, v200\n"
        "v_mfma_f32_16x16x32_bf16 v[118:121], v[208:211], v[208:211], 0\n"
        "v_mul_f32 v197, v197, v250\n"
        "v_cvt_pk_f16_f32 v190, v190, v194\n"
        "v_cvt_pk_f16_f32 v191, v191, v195\n"
        "v_mfma_f32_16x16x32_bf16 v[118:121], v[212:215], v[212:215], v[118:121]\n"
        "v_cvt_pk_f16_f32 v192, v192, v196\n"
        "v_cvt_pk_f16_f32 v193, v193, v197\n"
        "v_perm_b32 v76, v190, v102, s54\n"
        "v_mfma_f32_16x16x32_bf16 v[118:121], v[186:189], v[186:189], v[118:121]\n"
        "v_perm_b32 v60, v191, v103, s54\n"
        "v_perm_b32 v64, v192, v104, s54\n"
        "v_perm_b32 v72, v193, v105, s54\n"
        "v_mfma_f32_16x16x32_bf16 v[102:105], v[136:139], a[128:131], 0\n"
        "v_perm_b32 v77, v190, v108, s55\n"
        "v_perm_b32 v61, v191, v109, s55\n"
        "v_perm_b32 v65, v192, v110, s55\n"
        "v_mfma_f32_16x16x32_bf16 v[102:105], v[130:133], a[124:127], v[102:105]\n"
        "ds_write_b64 v206, v[76:77] offset:8\n"
        "v_perm_b32 v73, v193, v111, s55\n"
        "v_mfma_f32_16x16x32_bf16 v[102:105], v[124:127], a[120:123], v[102:105]\n"
        "ds_write_b64 v206, v[60:61] offset:3144\n"
        "v_cndmask_b32_e64 v248, v118, v119, s[56:57]\n"
        "v_mfma_f32_16x16x32_bf16 v[108:111], v[124:127], a[132:135], 0\n"
        "ds_write_b64 v206, v[64:65] offset:6280\n"
        "v_cndmask_b32_e64 v248, v248, v120, s[58:59]\n"
        "v_mfma_f32_16x16x32_bf16 v[108:111], v[130:133], a[136:139], v[108:111]\n"
        "ds_write_b64 v206, v[72:73] offset:9416\n"
        "v_cndmask_b32_e64 v248, v248, v121, s[60:61]\n"
        "v_mfma_f32_16x16x32_bf16 v[108:111], v[136:139], a[140:143], v[108:111]\n"
        "s_nop 0\n"
        "v_cvt_pk_bf16_f32 v72, v102, v103\n"
        "v_cvt_pk_bf16_f32 v73, v104, v105\n"
        "v_mfma_f32_16x16x32_bf16 v[102:105], v[66:69], v[128:131], 0\n"
        "s_nop 1\n"
        "v_mfma_f32_16x16x32_bf16 v[64:67], v[68:71], v[132:135], 0\n"
        "s_nop 0\n"
        "v_cvt_pk_bf16_f32 v76, v108, v109\n"
        "v_cvt_pk_bf16_f32 v77, v110, v111\n"
        "v_mfma_f32_16x16x32_bf16 v[108:111], v[174:177], v[44:47], 0\n"
        "s_nop 0\n"
        "v_cvt_pk_bf16_f32 v113, v102, v103\n"
        "v_cvt_pk_bf16_f32 v107, v104, v105\n"
        "v_mfma_f32_16x16x32_bf16 v[108:111], v[166:169], v[40:43], v[108:111]\n"
        "v_cvt_pk_bf16_f32 v118, v64, v65\n"
        "v_cvt_pk_bf16_f32 v114, v66, v67\n"
        "v_mfma_f32_16x16x32_bf16 v[108:111], v[182:185], v[36:39], v[108:111]\n"
        "s_nop 1\n"
        "v_mfma_f32_16x16x32_bf16 v[64:67], v[182:185], v[48:51], 0\n"
        "s_nop 2\n"
        "v_mfma_f32_16x16x32_bf16 v[64:67], v[166:169], v[52:55], v[64:67]\n"
        "s_nop 0\n"
        "v_mul_f32 v108, v108, v251\n"
        "v_mul_f32 v109, v109, v199\n"
        "v_mul_f32 v110, v110, v200\n"
        "v_mfma_f32_16x16x32_bf16 v[64:67], v[174:177], v[56:59], v[64:67]\n"
        "s_nop 0\n"
        "v_mul_f32 v111, v111, v250\n"
        "v_mfma_f32_16x16x32_bf16 v[120:123], v[174:177], v[94:97], 0\n"
        "s_nop 2\n"
        "v_mfma_f32_16x16x32_bf16 v[102:105], v[174:177], v[98:101], 0\n"
        "s_nop 0\n"
        "v_mul_f32 v64, v64, v251\n"
        "v_mul_f32 v65, v65, v199\n"
        "v_mul_f32 v66, v66, v200\n"
        "v_mfma_f32_16x16x32_bf16 v[124:127], v[156:159], a[80:83], 0\n"
        "v_mul_f32 v67, v67, v250\n"
        "v_cvt_pk_f16_f32 v108, v108, v64\n"
        "v_cvt_pk_f16_f32 v109, v109, v65\n"
        "v_mfma_f32_16x16x32_bf16 v[124:127], v[150:153], a[76:79], v[124:127]\n"
        "v_cvt_pk_f16_f32 v110, v110, v66\n"
        "v_cvt_pk_f16_f32 v111, v111, v67\n"
        "v_perm_b32 v60, v108, v120, s54\n"
        "v_mfma_f32_16x16x32_bf16 v[124:127], v[144:147], a[72:75], v[124:127]\n"
        "v_perm_b32 v70, v109, v121, s54\n"
        "v_perm_b32 v66, v110, v122, s54\n"
        "v_perm_b32 v64, v111, v123, s54\n"
        "v_mfma_f32_16x16x32_bf16 v[120:123], v[144:147], a[84:87], 0\n"
        "v_perm_b32 v61, v108, v102, s55\n"
        "v_perm_b32 v71, v109, v103, s55\n"
        "v_perm_b32 v67, v110, v104, s55\n"
        "v_mfma_f32_16x16x32_bf16 v[120:123], v[150:153], a[88:91], v[120:123]\n"
        "ds_write_b64 v206, v[60:61] offset:16\n"
        "v_perm_b32 v65, v111, v105, s55\n"
        "v_mfma_f32_16x16x32_bf16 v[120:123], v[156:159], a[92:95], v[120:123]\n"
        "ds_write_b64 v206, v[70:71] offset:3152\n"
        "v_mfma_f32_16x16x32_bf16 v[128:131], v[72:75], v[134:137], 0\n"
        "ds_write_b64 v206, v[66:67] offset:6288\n"
        "v_cvt_pk_bf16_f32 v60, v124, v125\n"
        "v_mfma_f32_16x16x32_bf16 v[70:73], v[74:77], v[138:141], 0\n"
        "ds_write_b64 v206, v[64:65] offset:9424\n"
        "v_cvt_pk_bf16_f32 v61, v126, v127\n"
        "v_mfma_f32_16x16x32_bf16 v[102:105], v[178:181], a[224:227], 0\n"
        "v_cvt_pk_bf16_f32 v64, v120, v121\n"
        "v_cvt_pk_bf16_f32 v65, v122, v123\n"
        "v_cvt_pk_bf16_f32 v108, v128, v129\n"
        "v_mfma_f32_16x16x32_bf16 v[102:105], v[162:165], a[220:223], v[102:105]\n"
        "v_cvt_pk_bf16_f32 v119, v130, v131\n"
        "v_cvt_pk_bf16_f32 v115, v70, v71\n"
        "v_mfma_f32_16x16x32_bf16 v[102:105], v[170:173], a[216:219], v[102:105]\n"
        "ds_write_b128 v12, v[116:119] offset:3072\n"
        "v_cvt_pk_bf16_f32 v109, v72, v73\n"
        "v_mfma_f32_16x16x32_bf16 v[120:123], v[170:173], a[228:231], 0\n"
        "ds_write_b128 v12, v[112:115] offset:4096\n"
        "v_mfma_f32_16x16x32_bf16 v[120:123], v[162:165], a[232:235], v[120:123]\n"
        "ds_write_b128 v12, v[106:109] offset:5120\n"
        "v_mfma_f32_16x16x32_bf16 v[120:123], v[178:181], a[236:239], v[120:123]\n"
        "s_nop 0\n"
        "v_mul_f32 v102, v102, v255\n"
        "v_mul_f32 v103, v103, v253\n"
        "v_mfma_f32_16x16x32_bf16 v[70:73], v[170:173], v[94:97], 0\n"
        "s_nop 1\n"
        "v_mfma_f32_16x16x32_bf16 v[126:129], v[170:173], v[98:101], 0\n"
        "s_nop 0\n"
        "v_mul_f32 v120, v120, v255\n"
        "v_mul_f32 v121, v121, v253\n"
        "v_cvt_pk_f16_f32 v102, v102, v120\n"
        "v_mfma_f32_16x16x32_bf16 v[122:125], v[156:159], a[104:107], 0\n"
        "v_cvt_pk_f16_f32 v103, v103, v121\n"
        "v_perm_b32 v66, v102, v70, s54\n"
        "v_perm_b32 v70, v103, v71, s54\n"
        "v_mfma_f32_16x16x32_bf16 v[122:125], v[150:153], a[100:103], v[122:125]\n"
        "v_perm_b32 v67, v102, v126, s55\n"
        "v_perm_b32 v71, v103, v127, s55\n"
        "v_mfma_f32_16x16x32_bf16 v[122:125], v[144:147], a[96:99], v[122:125]\n"
        "ds_write_b64 v206, v[66:67] offset:12544\n"
        "v_mfma_f32_16x16x32_bf16 v[102:105], v[144:147], a[108:111], 0\n"
        "s_nop 0\n"
        "ds_write_b64 v207, v[70:71]\n"
        "v_mfma_f32_16x16x32_bf16 v[102:105], v[150:153], a[112:115], v[102:105]\n"
        "s_nop 0\n"
        "v_mfma_f32_16x16x32_bf16 v[102:105], v[156:159], a[116:119], v[102:105]\n"
        "s_nop 0\n"
        "v_cvt_pk_bf16_f32 v66, v122, v123\n"
        "v_cvt_pk_bf16_f32 v67, v124, v125\n"
        "v_mfma_f32_16x16x32_bf16 v[120:123], v[60:63], v[142:145], 0\n"
        "s_nop 1\n"
        "v_mfma_f32_16x16x32_bf16 v[124:127], v[62:65], v[146:149], 0\n"
        "s_nop 0\n"
        "v_cvt_pk_bf16_f32 v70, v102, v103\n"
        "v_cvt_pk_bf16_f32 v71, v104, v105\n"
        "v_mfma_f32_16x16x32_bf16 v[102:105], v[178:181], a[248:251], 0\n"
        "s_nop 0\n"
        "v_cvt_pk_bf16_f32 v110, v120, v121\n"
        "v_cvt_pk_bf16_f32 v64, v122, v123\n"
        "v_mfma_f32_16x16x32_bf16 v[102:105], v[162:165], a[244:247], v[102:105]\n"
        "v_cvt_pk_bf16_f32 v132, v124, v125\n"
        "v_cvt_pk_bf16_f32 v111, v126, v127\n"
        "v_mfma_f32_16x16x32_bf16 v[102:105], v[170:173], a[240:243], v[102:105]\n"
        "s_nop 1\n"
        "v_mfma_f32_16x16x32_bf16 v[128:131], v[170:173], a[252:255], 0\n"
        "s_nop 2\n"
        "v_mfma_f32_16x16x32_bf16 v[128:131], v[162:165], v[28:31], v[128:131]\n"
        "s_nop 0\n"
        "v_mul_f32 v102, v102, v255\n"
        "v_mul_f32 v103, v103, v253\n"
        "v_mfma_f32_16x16x32_bf16 v[128:131], v[178:181], v[32:35], v[128:131]\n"
        "s_nop 1\n"
        "v_mfma_f32_16x16x32_bf16 v[134:137], v[162:165], v[94:97], 0\n"
        "s_nop 2\n"
        "v_mfma_f32_16x16x32_bf16 v[120:123], v[162:165], v[98:101], 0\n"
        "s_nop 0\n"
        "v_mul_f32 v128, v128, v255\n"
        "v_mul_f32 v129, v129, v253\n"
        "v_cvt_pk_f16_f32 v102, v102, v128\n"
        "v_mfma_f32_16x16x32_bf16 v[124:127], v[116:119], v[116:119], 0\n"
        "v_cvt_pk_f16_f32 v103, v103, v129\n"
        "v_perm_b32 v60, v102, v134, s54\n"
        "v_perm_b32 v72, v103, v135, s54\n"
        "v_mfma_f32_16x16x32_bf16 v[124:127], v[112:115], v[112:115], v[124:127]\n"
        "v_perm_b32 v61, v102, v120, s55\n"
        "v_perm_b32 v73, v103, v121, s55\n"
        "v_mfma_f32_16x16x32_bf16 v[124:127], v[106:109], v[106:109], v[124:127]\n"
        "ds_write_b64 v206, v[60:61] offset:12552\n"
        "v_mfma_f32_16x16x32_bf16 v[106:109], v[156:159], a[128:131], 0\n"
        "s_nop 0\n"
        "ds_write_b64 v207, v[72:73] offset:8\n"
        "v_mfma_f32_16x16x32_bf16 v[106:109], v[150:153], a[124:127], v[106:109]\n"
        "s_nop 0\n"
        "ds_read_b64_tr_b16 v[118:119], v201\n"
        "ds_read_b64_tr_b16 v[120:121], v201 offset:3072\n"
        "v_cndmask_b32_e64 v251, v124, v125, s[56:57]\n"
        "v_mfma_f32_16x16x32_bf16 v[106:109], v[144:147], a[120:123], v[106:109]\n"
        "ds_read_b64_tr_b16 v[128:129], v201 offset:6144\n"
        "ds_read_b64_tr_b16 v[130:131], v201 offset:9216\n"
        "v_cndmask_b32_e64 v251, v251, v126, s[58:59]\n"
        "v_mfma_f32_16x16x32_bf16 v[112:115], v[144:147], a[132:135], 0\n"
        "ds_read_b64_tr_b16 v[102:103], v201 offset:12288\n"
        "ds_read_b64_tr_b16 v[104:105], v201 offset:15360\n"
        "v_cndmask_b32_e64 v251, v251, v127, s[60:61]\n"
        "v_mfma_f32_16x16x32_bf16 v[112:115], v[150:153], a[136:139], v[112:115]\n"
        "v_cvt_pk_bf16_f32 v72, v106, v107\n"
        "v_cvt_pk_bf16_f32 v73, v108, v109\n"
        "v_mfma_f32_16x16x32_bf16 v[112:115], v[156:159], a[140:143], v[112:115]\n"
        "s_nop 1\n"
        "v_mfma_f32_16x16x32_bf16 v[106:109], v[66:69], v[148:151], 0\n"
        "s_nop 2\n"
        "v_mfma_f32_16x16x32_bf16 v[124:127], v[68:71], v[152:155], 0\n"
        "s_nop 0\n"
        "v_cvt_pk_bf16_f32 v76, v112, v113\n"
        "v_cvt_pk_bf16_f32 v77, v114, v115\n"
        "v_mfma_f32_16x16x32_bf16 v[134:137], v[178:181], v[44:47], 0\n"
        "v_cvt_pk_bf16_f32 v65, v106, v107\n"
        "v_cvt_pk_bf16_f32 v133, v108, v109\n"
        "v_mfma_f32_16x16x32_bf16 v[134:137], v[162:165], v[40:43], v[134:137]\n"
        "ds_read_b64_tr_b16 v[186:187], v201 offset:8\n"
        "ds_read_b64_tr_b16 v[188:189], v201 offset:3080\n"
        "v_cvt_pk_bf16_f32 v112, v124, v125\n"
        "v_mfma_f32_16x16x32_bf16 v[134:137], v[170:173], v[36:39], v[134:137]\n"
        "s_nop 0\n"
        "ds_read_b64_tr_b16 v[122:123], v201 offset:6152\n"
        "ds_read_b64_tr_b16 v[124:125], v201 offset:9224\n"
        "v_cvt_pk_bf16_f32 v66, v126, v127\n"
        "v_mfma_f32_16x16x32_bf16 v[114:117], v[170:173], v[48:51], 0\n"
        "s_waitcnt lgkmcnt(4)\n"
        "ds_read_b64_tr_b16 v[140:141], v201 offset:12296\n"
        "ds_read_b64_tr_b16 v[142:143], v201 offset:15368\n"
        "v_mul_f32 v134, v134, v255\n"
        "v_mfma_f32_16x16x32_bf16 v[114:117], v[162:165], v[52:55], v[114:117]\n"
        "v_mul_f32 v135, v135, v253\n"
        "v_mfma_f32_16x16x32_bf16 v[114:117], v[178:181], v[56:59], v[114:117]\n"
        "s_nop 1\n"
        "v_mfma_f32_16x16x32_bf16 v[136:139], v[178:181], v[94:97], 0\n"
        "s_nop 2\n"
        "v_mfma_f32_16x16x32_bf16 v[106:109], v[178:181], v[98:101], 0\n"
        "s_nop 0\n"
        "v_mul_f32 v114, v114, v255\n"
        "v_mul_f32 v115, v115, v253\n"
        "v_cvt_pk_f16_f32 v134, v134, v114\n"
        "v_mfma_f32_16x16x32_bf16 v[148:151], v[102:105], v[174:177], 0\n"
        "v_cvt_pk_f16_f32 v135, v135, v115\n"
        "v_perm_b32 v70, v134, v136, s54\n"
        "v_perm_b32 v60, v135, v137, s54\n"
        "v_mfma_f32_16x16x32_bf16 v[148:151], v[128:131], v[166:169], v[148:151]\n"
        "v_perm_b32 v71, v134, v106, s55\n"
        "v_perm_b32 v61, v135, v107, s55\n"
        "v_mfma_f32_16x16x32_bf16 v[148:151], v[118:121], v[182:185], v[148:151]\n"
        "ds_read_b64_tr_b16 v[136:137], v201 offset:1024\n"
        "ds_read_b64_tr_b16 v[138:139], v201 offset:4096\n"
        "v_mfma_f32_16x16x32_bf16 v[190:193], v[118:121], v[170:173], 0\n"
        "s_waitcnt lgkmcnt(2)\n"
        "ds_read_b64_tr_b16 v[106:107], v201 offset:7168\n"
        "ds_read_b64_tr_b16 v[108:109], v201 offset:10240\n"
        "v_mfma_f32_16x16x32_bf16 v[190:193], v[128:131], v[162:165], v[190:193]\n"
        "ds_read_b64_tr_b16 v[144:145], v201 offset:13312\n"
        "ds_read_b64_tr_b16 v[146:147], v201 offset:16384\n"
        "v_mul_f32 v148, v148, v254\n"
        "v_mfma_f32_16x16x32_bf16 v[190:193], v[102:105], v[178:181], v[190:193]\n"
        "ds_write_b64 v206, v[70:71] offset:12560\n"
        "v_mul_f32 v149, v149, v254\n"
        "v_mfma_f32_16x16x32_bf16 v[194:197], v[72:75], v[154:157], 0\n"
        "ds_write_b64 v207, v[60:61] offset:16\n"
        "v_mul_f32 v150, v150, v254\n"
        "v_mfma_f32_16x16x32_bf16 v[128:131], v[74:77], v[158:161], 0\n"
        "v_mul_f32 v151, v151, v254\n"
        "v_cvt_pk_bf16_f32 v126, v148, v149\n"
        "v_cvt_pk_bf16_f32 v127, v150, v151\n"
        "v_mfma_f32_16x16x32_bf16 v[114:117], v[140:143], v[174:177], 0\n"
        "ds_read_b64_tr_b16 v[118:119], v201 offset:1032\n"
        "ds_read_b64_tr_b16 v[120:121], v201 offset:4104\n"
        "v_mul_f32 v190, v190, v252\n"
        "v_mfma_f32_16x16x32_bf16 v[114:117], v[122:125], v[166:169], v[114:117]\n"
        "ds_read_b64_tr_b16 v[102:103], v201 offset:7176\n"
        "ds_read_b64_tr_b16 v[104:105], v201 offset:10248\n"
        "v_mul_f32 v191, v191, v252\n"
        "v_mfma_f32_16x16x32_bf16 v[114:117], v[186:189], v[182:185], v[114:117]\n"
        "s_waitcnt lgkmcnt(6)\n"
        "ds_read_b64_tr_b16 v[70:71], v201 offset:13320\n"
        "ds_read_b64_tr_b16 v[72:73], v201 offset:16392\n"
        "v_mul_f32 v192, v192, v252\n"
        "v_mfma_f32_16x16x32_bf16 v[148:151], v[186:189], v[170:173], 0\n"
        "v_mul_f32 v193, v193, v252\n"
        "v_cvt_pk_bf16_f32 v152, v190, v191\n"
        "v_cvt_pk_bf16_f32 v153, v192, v193\n"
        "v_mfma_f32_16x16x32_bf16 v[148:151], v[122:125], v[162:165], v[148:151]\n"
        "v_cvt_pk_bf16_f32 v134, v194, v195\n"
        "v_cvt_pk_bf16_f32 v113, v196, v197\n"
        "v_cvt_pk_bf16_f32 v67, v128, v129\n"
        "v_mfma_f32_16x16x32_bf16 v[148:151], v[140:143], v[178:181], v[148:151]\n"
        "ds_write_b128 v12, v[110:113] offset:6144\n"
        "v_cvt_pk_bf16_f32 v135, v130, v131\n"
        "v_mfma_f32_16x16x32_bf16 v[140:143], v[110:113], v[110:113], 0\n"
        "ds_read_b64_tr_b16 v[122:123], v201 offset:2048\n"
        "ds_read_b64_tr_b16 v[124:125], v201 offset:5120\n"
        "v_mul_f32 v114, v114, v254\n"
        "v_mfma_f32_16x16x32_bf16 v[140:143], v[64:67], v[64:67], v[140:143]\n"
        "ds_read_b64_tr_b16 v[110:111], v201 offset:8192\n"
        "ds_read_b64_tr_b16 v[112:113], v201 offset:11264\n"
        "v_mul_f32 v115, v115, v254\n"
        "v_mfma_f32_16x16x32_bf16 v[140:143], v[132:135], v[132:135], v[140:143]\n"
        "s_waitcnt lgkmcnt(5)\n"
        "ds_read_b64_tr_b16 v[186:187], v201 offset:14336\n"
        "ds_read_b64_tr_b16 v[188:189], v201 offset:17408\n"
        "v_mul_f32 v116, v116, v254\n"
        "v_mfma_f32_16x16x32_bf16 v[156:159], v[144:147], v[174:177], 0\n"
        "ds_write_b128 v12, v[64:67] offset:7168\n"
        "v_mul_f32 v117, v117, v254\n"
        "v_mfma_f32_16x16x32_bf16 v[156:159], v[106:109], v[166:169], v[156:159]\n"
        "ds_write_b128 v12, v[132:135] offset:8192\n"
        "v_cvt_pk_bf16_f32 v128, v114, v115\n"
        "v_mfma_f32_16x16x32_bf16 v[156:159], v[136:139], v[182:185], v[156:159]\n"
        "v_cvt_pk_bf16_f32 v129, v116, v117\n"
        "v_mul_f32 v148, v148, v252\n"
        "v_mul_f32 v149, v149, v252\n"
        "v_mfma_f32_16x16x32_bf16 v[130:133], v[136:139], v[170:173], 0\n"
        "ds_read_b64_tr_b16 v[134:135], v201 offset:2056\n"
        "ds_read_b64_tr_b16 v[136:137], v201 offset:5128\n"
        "v_mul_f32 v150, v150, v252\n"
        "v_mfma_f32_16x16x32_bf16 v[130:133], v[106:109], v[162:165], v[130:133]\n"
        "s_waitcnt lgkmcnt(4)\n"
        "ds_read_b64_tr_b16 v[208:209], v201 offset:8200\n"
        "ds_read_b64_tr_b16 v[210:211], v201 offset:11272\n"
        "v_mul_f32 v151, v151, v252\n"
        "v_mfma_f32_16x16x32_bf16 v[130:133], v[144:147], v[178:181], v[130:133]\n"
        "ds_read_b64_tr_b16 v[204:205], v201 offset:14344\n"
        "ds_read_b64_tr_b16 v[206:207], v201 offset:17416\n"
        "v_cvt_pk_bf16_f32 v154, v148, v149\n"
        "v_mfma_f32_16x16x32_bf16 v[194:197], v[70:73], v[174:177], 0\n"
        "ds_write_b128 v202, v[126:129] offset:192\n"
        "v_cvt_pk_bf16_f32 v155, v150, v151\n"
        "v_mfma_f32_16x16x32_bf16 v[194:197], v[102:105], v[166:169], v[194:197]\n"
        "ds_write_b128 v203, v[152:155] offset:192\n"
        "v_cndmask_b32_e64 v255, v140, v141, s[56:57]\n"
        "v_mfma_f32_16x16x32_bf16 v[194:197], v[118:121], v[182:185], v[194:197]\n"
        "v_cndmask_b32_e64 v255, v255, v142, s[58:59]\n"
        "v_cndmask_b32_e64 v255, v255, v143, s[60:61]\n"
        "v_mul_f32 v156, v156, v254\n"
        "v_mfma_f32_16x16x32_bf16 v[64:67], v[118:121], v[170:173], 0\n"
        "v_cndmask_b32_dpp v251, v255, v251, vcc row_ror:4 row_mask:0xf bank_mask:0xf\n"
        "v_mul_f32 v157, v157, v254\n"
        "v_mul_f32 v158, v158, v254\n"
        "v_mfma_f32_16x16x32_bf16 v[64:67], v[102:105], v[162:165], v[64:67]\n"
        "s_waitcnt lgkmcnt(2)\n"
        "v_cndmask_b32_dpp v248, v251, v248, vcc row_ror:4 row_mask:0xf bank_mask:0xf\n"
        "v_mul_f32 v248, s80, v248\n"
        "v_fma_f32 v249, -v249, v249, v248\n"
        "v_mfma_f32_16x16x32_bf16 v[64:67], v[70:73], v[178:181], v[64:67]\n"
        "v_max_f32 v249, 0, v249\n"
        "v_sub_f32 v248, 0x44800000, v248\n"
        "v_add_f32 v249, s81, v249\n"
        "v_mfma_f32_16x16x32_bf16 v[140:143], v[186:189], v[174:177], 0\n"
        "v_mul_f32 v159, v159, v254\n"
        "v_bfi_b32 v249, s84, v248, v249\n"
        "v_cvt_pk_bf16_f32 v144, v156, v157\n"
        "v_mfma_f32_16x16x32_bf16 v[140:143], v[110:113], v[166:169], v[140:143]\n"
        "s_waitcnt vmcnt(0)\n"
        "v_rsq_f32 v255, v249\n"
        "v_cvt_pk_bf16_f32 v145, v158, v159\n"
        "v_mfma_f32_16x16x32_bf16 v[140:143], v[122:125], v[182:185], v[140:143]\n"
        "ds_read_b128 v[104:107], v9\n"
        "v_cvt_f16_f32 v255, v255\n"
        "v_mul_f32 v130, v130, v252\n"
        "v_mfma_f32_16x16x32_bf16 v[190:193], v[122:125], v[170:173], 0\n"
        "ds_write_b16 v18, v255\n"
        "v_mul_f32 v131, v131, v252\n"
        "v_mfma_f32_16x16x32_bf16 v[190:193], v[110:113], v[162:165], v[190:193]\n"
        "ds_write_b16 v19, v255\n"
        "v_mul_f32 v132, v132, v252\n"
        "v_mfma_f32_16x16x32_bf16 v[190:193], v[186:189], v[178:181], v[190:193]\n"
        "ds_read_b128 v[110:113], v10\n"
        "v_mul_f32 v133, v133, v252\n"
        "v_cvt_pk_bf16_f32 v70, v130, v131\n"
        "v_mfma_f32_16x16x32_bf16 v[120:123], v[204:207], v[174:177], 0\n"
        "ds_read_b128 v[116:119], v11\n"
        "v_cvt_pk_bf16_f32 v71, v132, v133\n"
        "v_mul_f32 v194, v194, v254\n"
        "v_mfma_f32_16x16x32_bf16 v[120:123], v[208:211], v[166:169], v[120:123]\n"
        "ds_read_b128 v[124:127], v9 offset:3072\n"
        "v_mul_f32 v195, v195, v254\n"
        "v_mul_f32 v196, v196, v254\n"
        "v_mfma_f32_16x16x32_bf16 v[120:123], v[134:137], v[182:185], v[120:123]\n"
        "ds_read_b128 v[130:133], v10 offset:3072\n"
        "v_mul_f32 v197, v197, v254\n"
        "v_cvt_pk_bf16_f32 v146, v194, v195\n"
        "v_mfma_f32_16x16x32_bf16 v[166:169], v[134:137], v[170:173], 0\n"
        "ds_read_b128 v[136:139], v11 offset:3072\n"
        "v_cvt_pk_bf16_f32 v147, v196, v197\n"
        "v_mul_f32 v64, v64, v252\n"
        "v_mfma_f32_16x16x32_bf16 v[166:169], v[208:211], v[162:165], v[166:169]\n"
        "ds_write_b128 v202, v[144:147] offset:256\n"
        "v_mul_f32 v65, v65, v252\n"
        "v_mfma_f32_16x16x32_bf16 v[166:169], v[204:207], v[178:181], v[166:169]\n"
        "ds_read_b128 v[144:147], v9 offset:6144\n"
        "v_mul_f32 v66, v66, v252\n"
        "v_mul_f32 v67, v67, v252\n"
        "v_mov_b32 v255, s29\n"
        "ds_write_b16 v22, v255 offset:4096\n"
        "v_cvt_pk_bf16_f32 v72, v64, v65\n"
        "v_cvt_pk_bf16_f32 v73, v66, v67\n"
        "ds_write_b128 v203, v[70:73] offset:256\n"
        "v_mul_f32 v140, v140, v254\n"
        "v_mul_f32 v141, v141, v254\n"
        "v_mul_f32 v142, v142, v254\n"
        "v_mul_f32 v143, v143, v254\n"
        "v_cvt_pk_bf16_f32 v70, v140, v141\n"
        "v_cvt_pk_bf16_f32 v71, v142, v143\n"
        "v_mul_f32 v190, v190, v252\n"
        "ds_read_b128 v[150:153], v10 offset:6144\n"
        "v_mul_f32 v191, v191, v252\n"
        "ds_read_b128 v[156:159], v11 offset:6144\n"
        "v_mul_f32 v192, v192, v252\n"
        "v_mul_f32 v193, v193, v252\n"
        "v_cvt_pk_bf16_f32 v64, v190, v191\n"
        "v_cvt_pk_bf16_f32 v65, v192, v193\n"
        "v_mul_f32 v120, v120, v254\n"
        "v_mul_f32 v121, v121, v254\n"
        "v_mul_f32 v122, v122, v254\n"
        "v_mul_f32 v123, v123, v254\n"
        "v_cvt_pk_bf16_f32 v72, v120, v121\n"
        "v_cvt_pk_bf16_f32 v73, v122, v123\n"
        "ds_write_b128 v202, v[70:73] offset:320\n"
        "v_mul_f32 v166, v166, v252\n"
        "v_mul_f32 v167, v167, v252\n"
        "v_mul_f32 v168, v168, v252\n"
        "v_mul_f32 v169, v169, v252\n"
        "v_cvt_pk_bf16_f32 v66, v166, v167\n"
        "v_cvt_pk_bf16_f32 v67, v168, v169\n"
        "ds_write_b128 v203, v[64:67] offset:320\n"
        "s_waitcnt lgkmcnt(0)\n"
        "s_nop 7\n"
        ".p2align 6\n"
        "TOP_%=:\n"
        "s_waitcnt lgkmcnt(6)\n"
        "v_mfma_f32_16x16x32_bf16 v[64:67], v[104:107], v[104:107], 0\n"
        "s_waitcnt lgkmcnt(3)\n"
        "s_waitcnt lgkmcnt(0)\n"
        "s_mov_b32 s26, s89\n"
        "v_mfma_f32_16x16x32_bf16 v[64:67], v[110:113], v[110:113], v[64:67]\n"
        "s_mov_b32 s30, s29\n"
        "v_add_u32 v23, 784, v23\n"
        "v_add_u32 v24, 784, v24\n"
        "v_mfma_f32_16x16x32_bf16 v[64:67], v[116:119], v[116:119], v[64:67]\n"
        "s_add_u32 s89, s86, s51\n"
        "s_nop 0\n"
        "v_mfma_f32_16x16x32_bf16 v[70:73], v[78:81], v[116:119], 0\n"
        "s_add_u32 s88, s88, 1\n"
        "s_nop 1\n"
        "v_mfma_f32_16x16x32_bf16 v[70:73], v[82:85], v[110:113], v[70:73]\n"
        "s_add_u32 s29, s29, 1\n"
        "v_cndmask_b32_e64 v60, v64, v65, s[56:57]\n"
        "v_cndmask_b32_e64 v60, v60, v66, s[58:59]\n"
        "v_mfma_f32_16x16x32_bf16 v[70:73], v[86:89], v[104:107], v[70:73]\n"
        "s_mov_b32 s86, s87\n"
        "v_cndmask_b32_e64 v60, v60, v67, s[60:61]\n"
        "v_mfma_f32_16x16x32_bf16 v[64:67], a[0:3], v[104:107], 0\n"
        "s_cmp_ge_u32 s88, 73\n"
        "v_mfma_f32_16x16x32_bf16 v[64:67], a[4:7], v[110:113], v[64:67]\n"
        "s_cselect_b32 s92, 590016, 0\n"
        "v_mfma_f32_16x16x32_bf16 v[64:67], a[8:11], v[116:119], v[64:67]\n"
        "s_cselect_b32 s88, 0, s88\n"
        "v_mfma_f32_16x16x32_bf16 v[120:123], a[20:23], v[116:119], 0\n"
        "s_max_u32 s92, s92, 1344\n"
        "s_nop 1\n"
        "v_mfma_f32_16x16x32_bf16 v[120:123], a[16:19], v[110:113], v[120:123]\n"
        "s_add_u32 s87, s87, s92\n"
        "s_cmp_gt_i32 s85, 0\n"
        "v_cvt_pk_bf16_f32 v140, v64, v65\n"
        "v_mfma_f32_16x16x32_bf16 v[120:123], a[12:15], v[104:107], v[120:123]\n"
        "s_cselect_b32 s94, s87, s84\n"
        "v_cvt_pk_bf16_f32 v141, v66, v67\n"
        "v_mfma_f32_16x16x32_bf16 v[64:67], a[24:27], v[104:107], 0\n"
        "s_add_u32 m0, s52, 0\n"
        "s_nop 1\n"
        "v_mfma_f32_16x16x32_bf16 v[64:67], a[28:31], v[110:113], v[64:67]\n"
        "s_sub_u32 s85, s85, 1\n"
        "v_cvt_pk_bf16_f32 v142, v120, v121\n"
        "v_cvt_pk_bf16_f32 v143, v122, v123\n"
        "v_mfma_f32_16x16x32_bf16 v[64:67], a[32:35], v[116:119], v[64:67]\n"
        "s_nop 0\n"
        "buffer_load_dwordx4 v0, s[36:39], s94 offen lds\n"
        "v_mfma_f32_16x16x32_bf16 v[120:123], a[44:47], v[116:119], 0\n"
        "s_nop 2\n"
        "v_mfma_f32_16x16x32_bf16 v[120:123], a[40:43], v[110:113], v[120:123]\n"
        "s_nop 0\n"
        "v_cvt_pk_bf16_f32 v160, v64, v65\n"
        "v_cvt_pk_bf16_f32 v161, v66, v67\n"
        "v_mfma_f32_16x16x32_bf16 v[120:123], a[36:39], v[104:107], v[120:123]\n"
        "s_nop 1\n"
        "v_mfma_f32_16x16x32_bf16 v[64:67], a[48:51], v[104:107], 0\n"
        "s_nop 0\n"
        "buffer_load_dwordx4 v1, s[36:39], s94 offen offset:1024 lds\n"
        "s_nop 0\n"
        "v_mfma_f32_16x16x32_bf16 v[64:67], a[52:55], v[110:113], v[64:67]\n"
        "s_nop 0\n"
        "v_cvt_pk_bf16_f32 v162, v120, v121\n"
        "v_cvt_pk_bf16_f32 v163, v122, v123\n"
        "v_mfma_f32_16x16x32_bf16 v[64:67], a[56:59], v[116:119], v[64:67]\n"
        "s_nop 0\n"
        "v_mfma_f32_16x16x32_bf16 v[120:123], a[68:71], v[116:119], 0\n"
        "s_nop 2\n"
        "v_mfma_f32_16x16x32_bf16 v[120:123], a[64:67], v[110:113], v[120:123]\n"
        "s_nop 0\n"
        "buffer_load_dwordx4 v2, s[36:39], s94 offen offset:2048 lds\n"
        "v_cvt_pk_bf16_f32 v164, v64, v65\n"
        "v_mfma_f32_16x16x32_bf16 v[120:123], a[60:63], v[104:107], v[120:123]\n"
        "s_nop 0\n"
        "v_cvt_pk_bf16_f32 v165, v66, v67\n"
        "v_mfma_f32_16x16x32_bf16 v[64:67], v[124:127], v[124:127], 0\n"
        "s_nop 2\n"
        "v_mfma_f32_16x16x32_bf16 v[64:67], v[130:133], v[130:133], v[64:67]\n"
        "s_nop 0\n"
        "v_cvt_pk_bf16_f32 v166, v120, v121\n"
        "v_cvt_pk_bf16_f32 v167, v122, v123\n"
        "v_mfma_f32_16x16x32_bf16 v[64:67], v[136:139], v[136:139], v[64:67]\n"
        "s_nop 0\n"
        "buffer_load_dwordx4 v3, s[36:39], s94 offen offset:3072 lds\n"
        "v_mfma_f32_16x16x32_bf16 v[120:123], v[78:81], v[136:139], 0\n"
        "s_nop 2\n"
        "v_mfma_f32_16x16x32_bf16 v[120:123], v[82:85], v[130:133], v[120:123]\n"
        "s_add_u32 m0, s52, 4096\n"
        "v_cndmask_b32_e64 v61, v64, v65, s[56:57]\n"
        "v_cndmask_b32_e64 v61, v61, v66, s[58:59]\n"
        "v_mfma_f32_16x16x32_bf16 v[120:123], v[86:89], v[124:127], v[120:123]\n"
        "v_cndmask_b32_e64 v61, v61, v67, s[60:61]\n"
        "v_mfma_f32_16x16x32_bf16 v[64:67], v[104:107], v[140:143], 0\n"
        "buffer_load_dwordx4 v4, s[36:39], s94 offen lds\n"
        "v_mfma_f32_16x16x32_bf16 v[64:67], v[110:113], v[160:163], v[64:67]\n"
        "v_mfma_f32_16x16x32_bf16 v[64:67], v[116:119], v[164:167], v[64:67]\n"
        "v_mfma_f32_16x16x32_bf16 v[140:143], a[8:11], v[136:139], 0\n"
        "v_mfma_f32_16x16x32_bf16 v[140:143], a[4:7], v[130:133], v[140:143]\n"
        "buffer_load_dwordx4 v5, s[36:39], s94 offen offset:1024 lds\n"
        "v_mfma_f32_16x16x32_bf16 v[140:143], a[0:3], v[124:127], v[140:143]\n"
        "s_nop 1\n"
        "v_mfma_f32_16x16x32_bf16 v[160:163], a[12:15], v[124:127], 0\n"
        "s_nop 2\n"
        "v_mfma_f32_16x16x32_bf16 v[160:163], a[16:19], v[130:133], v[160:163]\n"
        "s_nop 0\n"
        "v_cvt_pk_bf16_f32 v164, v140, v141\n"
        "v_cvt_pk_bf16_f32 v165, v142, v143\n"
        "v_mfma_f32_16x16x32_bf16 v[160:163], a[20:23], v[136:139], v[160:163]\n"
        "s_nop 0\n"
        "buffer_load_dwordx4 v6, s[36:39], s94 offen offset:2048 lds\n"
        "v_mfma_f32_16x16x32_bf16 v[140:143], a[32:35], v[136:139], 0\n"
        "s_nop 2\n"
        "v_mfma_f32_16x16x32_bf16 v[140:143], a[28:31], v[130:133], v[140:143]\n"
        "s_nop 0\n"
        "v_cvt_pk_bf16_f32 v166, v160, v161\n"
        "v_cvt_pk_bf16_f32 v167, v162, v163\n"
        "v_mfma_f32_16x16x32_bf16 v[140:143], a[24:27], v[124:127], v[140:143]\n"
        "s_nop 1\n"
        "v_mfma_f32_16x16x32_bf16 v[160:163], a[36:39], v[124:127], 0\n"
        "s_nop 0\n"
        "buffer_load_dwordx4 v7, s[36:39], s94 offen offset:3072 lds\n"
        "s_nop 0\n"
        "v_mfma_f32_16x16x32_bf16 v[160:163], a[40:43], v[130:133], v[160:163]\n"
        "s_nop 0\n"
        "v_cvt_pk_bf16_f32 v168, v140, v141\n"
        "v_cvt_pk_bf16_f32 v169, v142, v143\n"
        "v_mfma_f32_16x16x32_bf16 v[160:163], a[44:47], v[136:139], v[160:163]\n"
        "s_add_u32 m0, s52, 8192\n"
        "v_mfma_f32_16x16x32_bf16 v[140:143], a[56:59], v[136:139], 0\n"
        "s_nop 2\n"
        "v_mfma_f32_16x16x32_bf16 v[140:143], a[52:55], v[130:133], v[140:143]\n"
        "s_nop 0\n"
        "buffer_load_dwordx4 v8, s[36:39], s94 offen lds\n"
        "v_cvt_pk_bf16_f32 v170, v160, v161\n"
        "v_mfma_f32_16x16x32_bf16 v[140:143], a[48:51], v[124:127], v[140:143]\n"
        "s_nop 0\n"
        "v_cvt_pk_bf16_f32 v171, v162, v163\n"
        "v_mfma_f32_16x16x32_bf16 v[160:163], a[60:63], v[124:127], 0\n"
        "s_nop 2\n"
        "v_mfma_f32_16x16x32_bf16 v[160:163], a[64:67], v[130:133], v[160:163]\n"
        "s_nop 0\n"
        "v_cvt_pk_bf16_f32 v172, v140, v141\n"
        "v_cvt_pk_bf16_f32 v173, v142, v143\n"
        "v_mfma_f32_16x16x32_bf16 v[160:163], a[68:71], v[136:139], v[160:163]\n"
        "s_nop 0\n"
        "ds_read_u16 v72, v19 offset:3072\n"
        "v_mfma_f32_16x16x32_bf16 v[140:143], v[144:147], v[144:147], 0\n"
        "s_nop 2\n"
        "v_mfma_f32_16x16x32_bf16 v[140:143], v[150:153], v[150:153], v[140:143]\n"
        "s_nop 0\n"
        "v_cvt_pk_bf16_f32 v174, v160, v161\n"
        "v_cvt_pk_bf16_f32 v175, v162, v163\n"
        "v_mfma_f32_16x16x32_bf16 v[140:143], v[156:159], v[156:159], v[140:143]\n"
        "s_nop 1\n"
        "v_mfma_f32_16x16x32_bf16 v[160:163], v[78:81], v[156:159], 0\n"
        "s_nop 2\n"
        "v_mfma_f32_16x16x32_bf16 v[160:163], v[82:85], v[150:153], v[160:163]\n"
        "s_nop 0\n"
        "v_cndmask_b32_e64 v73, v140, v141, s[56:57]\n"
        "v_cndmask_b32_e64 v73, v73, v142, s[58:59]\n"
        "v_mfma_f32_16x16x32_bf16 v[160:163], v[86:89], v[144:147], v[160:163]\n"
        "s_nop 0\n"
        "v_cndmask_b32_e64 v73, v73, v143, s[60:61]\n"
        "v_mfma_f32_16x16x32_bf16 v[140:143], v[124:127], v[164:167], 0\n"
        "s_nop 0\n"
        "v_cndmask_b32_dpp v61, v73, v61, vcc row_ror:4 row_mask:0xf bank_mask:0xf\n"
        "s_nop 0\n"
        "v_mfma_f32_16x16x32_bf16 v[140:143], v[130:133], v[168:171], v[140:143]\n"
        "s_waitcnt lgkmcnt(0)\n"
        "ds_read_b128 v[162:165], v23\n"
        "ds_read_b128 v[166:169], v23 offset:64\n"
        "v_mfma_f32_16x16x32_bf16 v[140:143], v[136:139], v[172:175], v[140:143]\n"
        "ds_read_b128 v[170:173], v23 offset:128\n"
        "ds_read_b32 v76, v24 offset:360\n"
        "v_mfma_f32_16x16x32_bf16 v[174:177], a[8:11], v[156:159], 0\n"
        "v_cmp_ne_u32_e64 s[24:25], s30, v72\n"
        "v_cndmask_b32_dpp v60, v61, v60, vcc row_ror:4 row_mask:0xf bank_mask:0xf\n"
        "v_mfma_f32_16x16x32_bf16 v[174:177], a[4:7], v[150:153], v[174:177]\n"
        "v_cndmask_b32_dpp v244, v160, v120, vcc row_ror:4 row_mask:0xf bank_mask:0xf\n"
        "v_mul_f32 v60, s80, v60\n"
        "v_mfma_f32_16x16x32_bf16 v[174:177], a[0:3], v[144:147], v[174:177]\n"
        "s_cmp_lg_u64 s[24:25], 0\n"
        "v_cndmask_b32_dpp v244, v244, v70, vcc row_ror:4 row_mask:0xf bank_mask:0xf\n"
        "v_mul_f32 v61, s80, v244\n"
        "v_mfma_f32_16x16x32_bf16 v[178:181], a[12:15], v[144:147], 0\n"
        "s_cbranch_scc1 SP4_%=\n"
        "BK4_%=:\n"
        "v_fma_f32 v61, -v61, v61, v60\n"
        "v_max_f32 v61, 0, v61\n"
        "v_mfma_f32_16x16x32_bf16 v[178:181], a[16:19], v[150:153], v[178:181]\n"
        "v_mov_b32 v60, s29\n"
        "v_cvt_pk_bf16_f32 v182, v174, v175\n"
        "v_mfma_f32_16x16x32_bf16 v[178:181], a[20:23], v[156:159], v[178:181]\n"
        "ds_read_b128 v[194:197], v13\n"
        "v_cvt_pk_bf16_f32 v183, v176, v177\n"
        "v_mfma_f32_16x16x32_bf16 v[174:177], a[32:35], v[156:159], 0\n"
        "ds_read_b128 v[198:201], v13 offset:1024\n"
        "ds_read_b128 v[202:205], v13 offset:2048\n"
        "v_mfma_f32_16x16x32_bf16 v[174:177], a[28:31], v[150:153], v[174:177]\n"
        "s_waitcnt lgkmcnt(3)\n"
        "v_cndmask_b32_e64 v76, 0, v76, s[72:73]\n"
        "v_cvt_pk_bf16_f32 v184, v178, v179\n"
        "v_mfma_f32_16x16x32_bf16 v[174:177], a[24:27], v[144:147], v[174:177]\n"
        "ds_read_b128 v[206:209], v13 offset:3072\n"
        "ds_read_b128 v[210:213], v13 offset:4096\n"
        "v_mfma_f32_16x16x32_bf16 v[214:217], a[36:39], v[144:147], 0\n"
        "ds_read_b128 v[218:221], v13 offset:5120\n"
        "ds_read_b128 v[222:225], v13 offset:6144\n"
        "v_mfma_f32_16x16x32_bf16 v[214:217], a[40:43], v[150:153], v[214:217]\n"
        "ds_read_b128 v[226:229], v13 offset:7168\n"
        "ds_read_b128 v[230:233], v13 offset:8192\n"
        "v_mfma_f32_16x16x32_bf16 v[214:217], a[44:47], v[156:159], v[214:217]\n"
        "ds_read_b128 v[188:191], v20\n"
        "ds_read_b64 v[192:193], v20 offset:16\n"
        "v_mfma_f32_16x16x32_bf16 v[234:237], a[56:59], v[156:159], 0\n"
        "ds_read_u16 v77, v22\n"
        "ds_read_b64 v[242:243], v21\n"
        "v_mfma_f32_16x16x32_bf16 v[234:237], a[52:55], v[150:153], v[234:237]\n"
        "ds_write_b16 v22, v60 offset:4608\n"
        "v_mfma_f32_16x16x32_bf16 v[234:237], a[48:51], v[144:147], v[234:237]\n"
        "s_waitcnt lgkmcnt(11)\n"
        "v_cvt_pk_bf16_f32 v185, v180, v181\n"
        "v_cvt_pk_bf16_f32 v178, v174, v175\n"
        "v_mfma_f32_16x16x32_bf16 v[238:241], a[60:63], v[144:147], 0\n"
        "v_cvt_pk_bf16_f32 v179, v176, v177\n"
        "v_cvt_pk_bf16_f32 v180, v214, v215\n"
        "v_mfma_f32_16x16x32_bf16 v[238:241], a[64:67], v[150:153], v[238:241]\n"
        "v_cvt_pk_bf16_f32 v181, v216, v217\n"
        "v_cvt_pk_bf16_f32 v174, v234, v235\n"
        "v_mfma_f32_16x16x32_bf16 v[238:241], a[68:71], v[156:159], v[238:241]\n"
        "s_waitcnt lgkmcnt(8)\n"
        "v_cvt_pk_bf16_f32 v175, v236, v237\n"
        "v_add_f32 v61, s81, v61\n"
        "v_mfma_f32_16x16x32_bf16 v[214:217], v[162:165], v[194:197], 0\n"
        "s_nop 0\n"
        "v_rsq_f32 v60, v61\n"
        "v_mfma_f32_16x16x32_bf16 v[214:217], v[166:169], v[198:201], v[214:217]\n"
        "s_nop 0\n"
        "v_cvt_pk_bf16_f32 v176, v238, v239\n"
        "v_cvt_pk_bf16_f32 v177, v240, v241\n"
        "v_mfma_f32_16x16x32_bf16 v[214:217], v[170:173], v[202:205], v[214:217]\n"
        "s_waitcnt lgkmcnt(1)\n"
        "v_cndmask_b32_dpp v234, v161, v121, vcc row_ror:4 row_mask:0xf bank_mask:0xf\n"
        "v_mfma_f32_16x16x32_bf16 v[120:123], v[170:173], v[218:221], 0\n"
        "s_nop 0\n"
        "v_cndmask_b32_dpp v234, v234, v71, vcc row_ror:4 row_mask:0xf bank_mask:0xf\n"
        "v_mfma_f32_16x16x32_bf16 v[120:123], v[166:169], v[210:213], v[120:123]\n"
        "v_mfma_f32_16x16x32_bf16 v[120:123], v[162:165], v[206:209], v[120:123]\n"
        "s_nop 1\n"
        "v_mfma_f32_16x16x32_bf16 v[70:73], v[162:165], v[222:225], 0\n"
        "s_nop 2\n"
        "v_mfma_f32_16x16x32_bf16 v[70:73], v[166:169], v[226:229], v[70:73]\n"
        "s_nop 0\n"
        "v_cndmask_b32_e64 v114, v214, v120, s[64:65]\n"
        "v_cndmask_b32_e64 v214, v215, v121, s[64:65]\n"
        "v_mfma_f32_16x16x32_bf16 v[70:73], v[170:173], v[230:233], v[70:73]\n"
        "s_nop 1\n"
        "v_mfma_f32_16x16x32_bf16 v[120:123], v[144:147], v[182:185], 0\n"
        "s_nop 2\n"
        "v_mfma_f32_16x16x32_bf16 v[120:123], v[150:153], v[178:181], v[120:123]\n"
        "s_nop 0\n"
        "v_cndmask_b32_e64 v114, v114, v70, s[70:71]\n"
        "v_cndmask_b32_e64 v214, v214, v71, s[70:71]\n"
        "v_mfma_f32_16x16x32_bf16 v[120:123], v[156:159], v[174:177], v[120:123]\n"
        "v_cvt_pk_f16_f32 v114, v114, v214\n"
        "v_pk_mul_f16 v114, v114, v77 op_sel_hi:[1,0]\n"
        "v_mfma_f32_16x16x32_bf16 v[70:73], a[152:155], v[202:205], 0\n"
        "v_exp_f16_sdwa v114, v114 dst_sel:WORD_0 dst_unused:UNUSED_PRESERVE src0_sel:WORD_0\n"
        "v_mfma_f32_16x16x32_bf16 v[70:73], a[148:151], v[198:201], v[70:73]\n"
        "v_exp_f16_sdwa v114, v114 dst_sel:WORD_1 dst_unused:UNUSED_PRESERVE src0_sel:WORD_1\n"
        "v_mfma_f32_16x16x32_bf16 v[70:73], a[144:147], v[194:197], v[70:73]\n"
        "v_cvt_f32_f16 v128, v114\n"
        "v_cndmask_b32_e64 v128, v128, v76, s[68:69]\n"
        "v_mfma_f32_16x16x32_bf16 v[160:163], a[156:159], v[194:197], 0\n"
        "s_nop 0\n"
        "v_add_f32_dpp v76, v128, v128 row_ror:8 row_mask:0xf bank_mask:0xf\n"
        "v_cndmask_b32_dpp v129, v120, v140, vcc row_ror:4 row_mask:0xf bank_mask:0xf\n"
        "v_mfma_f32_16x16x32_bf16 v[160:163], a[160:163], v[198:201], v[160:163]\n"
        "v_cndmask_b32_dpp v215, v121, v141, vcc row_ror:4 row_mask:0xf bank_mask:0xf\n"
        "v_cndmask_b32_dpp v216, v122, v142, vcc row_ror:4 row_mask:0xf bank_mask:0xf\n"
        "v_mfma_f32_16x16x32_bf16 v[160:163], a[164:167], v[202:205], v[160:163]\n"
        "v_cndmask_b32_dpp v217, v123, v143, vcc row_ror:4 row_mask:0xf bank_mask:0xf\n"
        "v_cndmask_b32_dpp v129, v129, v64, vcc row_ror:4 row_mask:0xf bank_mask:0xf\n"
        "v_mfma_f32_16x16x32_bf16 v[120:123], a[176:179], v[202:205], 0\n"
        "v_cndmask_b32_dpp v215, v215, v65, vcc row_ror:4 row_mask:0xf bank_mask:0xf\n"
        "v_cndmask_b32_dpp v216, v216, v66, vcc row_ror:4 row_mask:0xf bank_mask:0xf\n"
        "v_mfma_f32_16x16x32_bf16 v[120:123], a[172:175], v[198:201], v[120:123]\n"
        "v_cndmask_b32_dpp v217, v217, v67, vcc row_ror:4 row_mask:0xf bank_mask:0xf\n"
        "v_mul_f32_dpp v129, v60, v129 quad_perm:[0,0,0,0] row_mask:0xf bank_mask:0xf\n"
        "v_mfma_f32_16x16x32_bf16 v[120:123], a[168:171], v[194:197], v[120:123]\n"
        "v_mul_f32_dpp v215, v60, v215 quad_perm:[1,1,1,1] row_mask:0xf bank_mask:0xf\n"
        "v_mul_f32_dpp v216, v60, v216 quad_perm:[2,2,2,2] row_mask:0xf bank_mask:0xf\n"
        "v_mfma_f32_16x16x32_bf16 v[64:67], a[180:183], v[194:197], 0\n"
        "v_mul_f32_dpp v217, v60, v217 quad_perm:[3,3,3,3] row_mask:0xf bank_mask:0xf\n"
        "v_max3_f32 v61, v129, v215, v216\n"
        "v_mfma_f32_16x16x32_bf16 v[64:67], a[184:187], v[198:201], v[64:67]\n"
        "v_max_f32 v61, v61, v217\n"
        "v_mul_f32 v61, v61, v60\n"
        "v_mfma_f32_16x16x32_bf16 v[64:67], a[188:191], v[202:205], v[64:67]\n"
        "v_fma_f32 v129, v129, v60, -v61\n"
        "v_fma_f32 v215, v215, v60, -v61\n"
        "v_mfma_f32_16x16x32_bf16 v[140:143], a[200:203], v[202:205], 0\n"
        "v_fma_f32 v216, v216, v60, -v61\n"
        "v_fma_f32 v217, v217, v60, -v61\n"
        "v_mfma_f32_16x16x32_bf16 v[140:143], a[196:199], v[198:201], v[140:143]\n"
        "v_exp_f32 v129, v129\n"
        "v_mfma_f32_16x16x32_bf16 v[140:143], a[192:195], v[194:197], v[140:143]\n"
        "v_cvt_pk_bf16_f32 v164, v70, v71\n"
        "v_cvt_pk_bf16_f32 v165, v72, v73\n"
        "v_mfma_f32_16x16x32_bf16 v[70:73], a[204:207], v[194:197], 0\n"
        "v_cvt_pk_bf16_f32 v166, v160, v161\n"
        "v_cvt_pk_bf16_f32 v167, v162, v163\n"
        "v_mfma_f32_16x16x32_bf16 v[70:73], a[208:211], v[198:201], v[70:73]\n"
        "v_cvt_pk_bf16_f32 v160, v120, v121\n"
        "v_cvt_pk_bf16_f32 v161, v122, v123\n"
        "v_mfma_f32_16x16x32_bf16 v[70:73], a[212:215], v[202:205], v[70:73]\n"
        "v_cvt_pk_bf16_f32 v162, v64, v65\n"
        "v_cvt_pk_bf16_f32 v163, v66, v67\n"
        "v_mfma_f32_16x16x32_bf16 v[64:67], v[116:119], a[80:83], 0\n"
        "s_nop 0\n"
        "v_cvt_pk_bf16_f32 v120, v140, v141\n"
        "v_cvt_pk_bf16_f32 v121, v142, v143\n"
        "v_mfma_f32_16x16x32_bf16 v[64:67], v[110:113], a[76:79], v[64:67]\n"
        "s_nop 0\n"
        "v_cvt_pk_bf16_f32 v122, v70, v71\n"
        "v_cvt_pk_bf16_f32 v123, v72, v73\n"
        "v_mfma_f32_16x16x32_bf16 v[64:67], v[104:107], a[72:75], v[64:67]\n"
        "v_exp_f32 v215, v215\n"
        "v_mfma_f32_16x16x32_bf16 v[70:73], v[104:107], a[84:87], 0\n"
        "v_exp_f32 v216, v216\n"
        "v_mfma_f32_16x16x32_bf16 v[70:73], v[110:113], a[88:91], v[70:73]\n"
        "v_exp_f32 v217, v217\n"
        "v_mfma_f32_16x16x32_bf16 v[70:73], v[116:119], a[92:95], v[70:73]\n"
        "v_add_f32 v61, v129, v215\n"
        "v_add_f32 v102, v216, v217\n"
        "v_mfma_f32_16x16x32_bf16 v[168:171], v[194:197], v[164:167], 0\n"
        "v_add_f32 v61, v61, v102\n"
        "v_add_f32_dpp v76, v76, v76 row_ror:4 row_mask:0xf bank_mask:0xf\n"
        "v_mfma_f32_16x16x32_bf16 v[168:171], v[198:201], v[160:163], v[168:171]\n"
        "v_rcp_f32 v61, v61\n"
        "v_mfma_f32_16x16x32_bf16 v[168:171], v[202:205], v[120:123], v[168:171]\n"
        "v_mul_f32 v129, v129, v61\n"
        "v_mul_f32 v215, v215, v61\n"
        "v_mfma_f32_16x16x32_bf16 v[172:175], v[218:221], v[120:123], 0\n"
        "v_mul_f32 v216, v216, v61\n"
        "v_mul_f32 v217, v217, v61\n"
        "v_mfma_f32_16x16x32_bf16 v[172:175], v[210:213], v[160:163], v[172:175]\n"
        "v_mul_f32_dpp v129, v60, v129 quad_perm:[0,0,0,0] row_mask:0xf bank_mask:0xf\n"
        "v_mul_f32_dpp v215, v60, v215 quad_perm:[1,1,1,1] row_mask:0xf bank_mask:0xf\n"
        "v_mfma_f32_16x16x32_bf16 v[172:175], v[206:209], v[164:167], v[172:175]\n"
        "v_mul_f32_dpp v216, v60, v216 quad_perm:[2,2,2,2] row_mask:0xf bank_mask:0xf\n"
        "v_mul_f32_dpp v217, v60, v217 quad_perm:[3,3,3,3] row_mask:0xf bank_mask:0xf\n"
        "v_mfma_f32_16x16x32_bf16 v[176:179], v[222:225], v[164:167], 0\n"
        "v_cvt_pk_bf16_f32 v134, v129, v215\n"
        "v_cvt_pk_bf16_f32 v135, v216, v217\n"
        "v_mfma_f32_16x16x32_bf16 v[176:179], v[226:229], v[160:163], v[176:179]\n"
        "v_and_b32 v102, v25, v134\n"
        "v_and_b32 v103, v25, v135\n"
        "v_mfma_f32_16x16x32_bf16 v[176:179], v[230:233], v[120:123], v[176:179]\n"
        "v_mov_b64 v[108:109], v[102:103]\n"
        "v_cvt_pk_bf16_f32 v60, v64, v65\n"
        "v_mfma_f32_16x16x32_bf16 v[120:123], v[116:119], a[104:107], 0\n"
        "v_cvt_pk_bf16_f32 v61, v66, v67\n"
        "v_cvt_pk_bf16_f32 v64, v70, v71\n"
        "v_mfma_f32_16x16x32_bf16 v[120:123], v[110:113], a[100:103], v[120:123]\n"
        "v_cvt_pk_bf16_f32 v65, v72, v73\n"
        "v_add_f32_dpp v76, v76, v76 row_ror:2 row_mask:0xf bank_mask:0xf\n"
        "v_mfma_f32_16x16x32_bf16 v[120:123], v[104:107], a[96:99], v[120:123]\n"
        "s_nop 0\n"
        "v_add_f32_dpp v76, v76, v76 row_ror:1 row_mask:0xf bank_mask:0xf\n"
        "v_and_b32 v214, 0xffff0000, v114\n"
        "v_mfma_f32_16x16x32_bf16 v[140:143], v[104:107], a[108:111], 0\n"
        "v_add_f32_dpp v76, v76, v76 row_bcast:15 row_mask:0xa bank_mask:0xf\n"
        "v_mov_b64 v[114:115], v[102:103]\n"
        "v_mfma_f32_16x16x32_bf16 v[140:143], v[110:113], a[112:115], v[140:143]\n"
        "v_add_f32_dpp v76, v76, v76 row_bcast:31 row_mask:0xc bank_mask:0xf\n"
        "v_cvt_pk_bf16_f32 v66, v120, v121\n"
        "v_mfma_f32_16x16x32_bf16 v[140:143], v[116:119], a[116:119], v[140:143]\n"
        "ds_read_u16 v148, v19 offset:3584\n"
        "v_readlane_b32 s95, v76, 63\n"
        "v_mfma_f32_16x16x32_bf16 v[160:163], v[60:63], v[102:105], 0\n"
        "s_nop 0\n"
        "v_rcp_f32 v235, s95\n"
        "v_mfma_f32_16x16x32_bf16 v[164:167], v[62:65], v[106:109], 0\n"
        "s_nop 0\n"
        "v_cvt_pk_bf16_f32 v67, v122, v123\n"
        "v_cvt_pk_bf16_f32 v70, v140, v141\n"
        "v_mfma_f32_16x16x32_bf16 v[180:183], v[116:119], a[128:131], 0\n"
        "v_cvt_pk_bf16_f32 v71, v142, v143\n"
        "v_mul_f32 v128, v128, v235\n"
        "v_mfma_f32_16x16x32_bf16 v[180:183], v[110:113], a[124:127], v[180:183]\n"
        "v_cvt_f16_f32 v128, v128\n"
        "v_mul_f16 v128, v128, v77\n"
        "v_mfma_f32_16x16x32_bf16 v[180:183], v[104:107], a[120:123], v[180:183]\n"
        "ds_write_b16 v22, v128 offset:512\n"
        "v_mfma_f32_16x16x32_bf16 v[184:187], v[104:107], a[132:135], 0\n"
        "v_mov_b64 v[120:121], v[102:103]\n"
        "v_mul_f32_dpp v236, v234, v129 quad_perm:[0,0,0,0] row_mask:0xf bank_mask:0xf\n"
        "v_mfma_f32_16x16x32_bf16 v[184:187], v[110:113], a[136:139], v[184:187]\n"
        "v_and_b32_dpp v122, v134, v25 row_ror:12 row_mask:0xf bank_mask:0xf\n"
        "v_and_b32_dpp v123, v135, v25 row_ror:12 row_mask:0xf bank_mask:0xf\n"
        "v_mfma_f32_16x16x32_bf16 v[184:187], v[116:119], a[140:143], v[184:187]\n"
        "s_waitcnt lgkmcnt(1)\n"
        "v_cmp_ne_u32_e64 s[24:25], s29, v148\n"
        "v_and_b32_dpp v142, v134, v25 row_ror:8 row_mask:0xf bank_mask:0xf\n"
        "v_mfma_f32_16x16x32_bf16 v[102:105], v[66:69], v[108:111], 0\n"
        "v_and_b32_dpp v143, v135, v25 row_ror:8 row_mask:0xf bank_mask:0xf\n"
        "v_cvt_pk_bf16_f32 v72, v180, v181\n"
        "v_mfma_f32_16x16x32_bf16 v[64:67], v[68:71], v[112:115], 0\n"
        "s_cmp_lg_u64 s[24:25], 0\n"
        "v_cvt_pk_bf16_f32 v73, v182, v183\n"
        "v_cvt_pk_bf16_f32 v76, v184, v185\n"
        "v_mfma_f32_16x16x32_bf16 v[106:109], v[136:139], a[80:83], 0\n"
        "s_cbranch_scc1 SP8_%=\n"
        "BK8_%=:\n"
        "v_cvt_pk_bf16_f32 v77, v186, v187\n"
        "v_mov_b64 v[128:129], v[122:123]\n"
        "v_mfma_f32_16x16x32_bf16 v[106:109], v[130:133], a[76:79], v[106:109]\n"
        "v_mov_b64 v[134:135], v[122:123]\n"
        "v_cvt_pk_bf16_f32 v110, v160, v161\n"
        "v_mfma_f32_16x16x32_bf16 v[106:109], v[124:127], a[72:75], v[106:109]\n"
        "v_cvt_pk_bf16_f32 v70, v162, v163\n"
        "v_cvt_pk_bf16_f32 v162, v164, v165\n"
        "v_mfma_f32_16x16x32_bf16 v[180:183], v[124:127], a[84:87], 0\n"
        "v_cvt_pk_bf16_f32 v111, v166, v167\n"
        "v_cvt_pk_bf16_f32 v71, v102, v103\n"
        "v_mfma_f32_16x16x32_bf16 v[180:183], v[130:133], a[88:91], v[180:183]\n"
        "v_cvt_pk_bf16_f32 v163, v104, v105\n"
        "v_cvt_pk_bf16_f32 v112, v64, v65\n"
        "v_mfma_f32_16x16x32_bf16 v[180:183], v[136:139], a[92:95], v[180:183]\n"
        "v_cvt_pk_bf16_f32 v60, v106, v107\n"
        "v_cvt_pk_bf16_f32 v61, v108, v109\n"
        "v_mfma_f32_16x16x32_bf16 v[102:105], v[72:75], v[114:117], 0\n"
        "s_nop 0\n"
        "v_cvt_pk_bf16_f32 v72, v66, v67\n"
        "v_mov_b64 v[140:141], v[122:123]\n"
        "v_mfma_f32_16x16x32_bf16 v[106:109], v[74:77], v[118:121], 0\n"
        "s_nop 0\n"
        "v_cvt_pk_bf16_f32 v64, v180, v181\n"
        "v_cvt_pk_bf16_f32 v65, v182, v183\n"
        "v_mfma_f32_16x16x32_bf16 v[114:117], v[136:139], a[104:107], 0\n"
        "v_cvt_pk_bf16_f32 v113, v104, v105\n"
        "v_cvt_pk_bf16_f32 v164, v102, v103\n"
        "v_mfma_f32_16x16x32_bf16 v[114:117], v[130:133], a[100:103], v[114:117]\n"
        "ds_write_b128 v12, v[110:113]\n"
        "v_mfma_f32_16x16x32_bf16 v[114:117], v[124:127], a[96:99], v[114:117]\n"
        "v_cvt_pk_bf16_f32 v73, v106, v107\n"
        "v_cvt_pk_bf16_f32 v165, v108, v109\n"
        "v_mfma_f32_16x16x32_bf16 v[104:107], v[124:127], a[108:111], 0\n"
        "ds_write_b128 v12, v[70:73] offset:1024\n"
        "v_mfma_f32_16x16x32_bf16 v[104:107], v[130:133], a[112:115], v[104:107]\n"
        "ds_write_b128 v12, v[162:165] offset:2048\n"
        "v_mfma_f32_16x16x32_bf16 v[104:107], v[136:139], a[116:119], v[104:107]\n"
        "v_mov_b64 v[148:149], v[142:143]\n"
        "v_mov_b64 v[154:155], v[142:143]\n"
        "v_mfma_f32_16x16x32_bf16 v[118:121], v[60:63], v[122:125], 0\n"
        "v_fmac_f32_dpp v236, v234, v215 quad_perm:[1,1,1,1] row_mask:0xf bank_mask:0xf\n"
        "v_mov_b64 v[160:161], v[142:143]\n"
        "v_mfma_f32_16x16x32_bf16 v[180:183], v[62:65], v[126:129], 0\n"
        "v_fmac_f32_dpp v236, v234, v216 quad_perm:[2,2,2,2] row_mask:0xf bank_mask:0xf\n"
        "v_cvt_pk_f16_f32 v102, v168, v169\n"
        "v_mfma_f32_16x16x32_bf16 v[166:169], v[110:113], v[110:113], 0\n"
        "v_fmac_f32_dpp v236, v234, v217 quad_perm:[3,3,3,3] row_mask:0xf bank_mask:0xf\n"
        "v_add_f32 v236, v236, v244\n"
        "v_mfma_f32_16x16x32_bf16 v[166:169], v[70:73], v[70:73], v[166:169]\n"
        "v_cvt_pk_f16_f32 v103, v170, v171\n"
        "v_cvt_pk_bf16_f32 v66, v114, v115\n"
        "v_mfma_f32_16x16x32_bf16 v[166:169], v[162:165], v[162:165], v[166:169]\n"
        "v_cvt_pk_bf16_f32 v67, v116, v117\n"
        "v_cvt_pk_bf16_f32 v70, v104, v105\n"
        "v_mfma_f32_16x16x32_bf16 v[108:111], v[136:139], a[128:131], 0\n"
        "v_cvt_pk_bf16_f32 v71, v106, v107\n"
        "v_pk_mul_f16 v102, v102, v242 op_sel_hi:[1,0]\n"
        "v_mfma_f32_16x16x32_bf16 v[108:111], v[130:133], a[124:127], v[108:111]\n"
        "v_pk_mul_f16 v103, v103, v242 op_sel_hi:[1,0]\n"
        "v_pk_mul_f16 v102, v102, v188\n"
        "v_mfma_f32_16x16x32_bf16 v[108:111], v[124:127], a[120:123], v[108:111]\n"
        "v_pk_mul_f16 v103, v103, v189\n"
        "v_cvt_pk_f16_f32 v104, v172, v173\n"
        "v_mfma_f32_16x16x32_bf16 v[112:115], v[124:127], a[132:135], 0\n"
        "v_exp_f16_sdwa v102, v102 dst_sel:WORD_0 dst_unused:UNUSED_PRESERVE src0_sel:WORD_0\n"
        "v_mfma_f32_16x16x32_bf16 v[112:115], v[130:133], a[136:139], v[112:115]\n"
        "v_exp_f16_sdwa v103, v103 dst_sel:WORD_0 dst_unused:UNUSED_PRESERVE src0_sel:WORD_0\n"
        "v_mfma_f32_16x16x32_bf16 v[112:115], v[136:139], a[140:143], v[112:115]\n"
        "v_exp_f16_sdwa v102, v102 dst_sel:WORD_1 dst_unused:UNUSED_PRESERVE src0_sel:WORD_1\n"
        "v_mfma_f32_16x16x32_bf16 v[122:125], v[66:69], v[128:131], 0\n"
        "v_exp_f16_sdwa v103, v103 dst_sel:WORD_1 dst_unused:UNUSED_PRESERVE src0_sel:WORD_1\n"
        "v_mfma_f32_16x16x32_bf16 v[64:67], v[68:71], v[132:135], 0\n"
        "v_cvt_pk_bf16_f32 v72, v108, v109\n"
        "v_cvt_pk_bf16_f32 v73, v110, v111\n"
        "v_mfma_f32_16x16x32_bf16 v[106:109], v[156:159], a[80:83], 0\n"
        "s_nop 0\n"
        "v_cvt_pk_bf16_f32 v76, v112, v113\n"
        "v_cvt_pk_bf16_f32 v77, v114, v115\n"
        "v_mfma_f32_16x16x32_bf16 v[106:109], v[150:153], a[76:79], v[106:109]\n"
        "v_cvt_pk_f16_f32 v105, v174, v175\n"
        "v_cvt_pk_bf16_f32 v110, v118, v119\n"
        "v_mfma_f32_16x16x32_bf16 v[106:109], v[144:147], a[72:75], v[106:109]\n"
        "v_cvt_pk_bf16_f32 v70, v120, v121\n"
        "v_cvt_pk_bf16_f32 v114, v180, v181\n"
        "v_mfma_f32_16x16x32_bf16 v[116:119], v[144:147], a[84:87], 0\n"
        "v_cvt_pk_bf16_f32 v111, v182, v183\n"
        "v_cvt_pk_bf16_f32 v71, v122, v123\n"
        "v_mfma_f32_16x16x32_bf16 v[116:119], v[150:153], a[88:91], v[116:119]\n"
        "v_cvt_pk_bf16_f32 v115, v124, v125\n"
        "v_cvt_pk_bf16_f32 v112, v64, v65\n"
        "v_mfma_f32_16x16x32_bf16 v[116:119], v[156:159], a[92:95], v[116:119]\n"
        "v_cvt_pk_bf16_f32 v60, v106, v107\n"
        "v_cvt_pk_bf16_f32 v61, v108, v109\n"
        "v_mfma_f32_16x16x32_bf16 v[106:109], v[72:75], v[134:137], 0\n"
        "s_nop 0\n"
        "v_cvt_pk_bf16_f32 v72, v66, v67\n"
        "v_pk_mul_f16 v104, v104, v242 op_sel_hi:[1,0]\n"
        "v_mfma_f32_16x16x32_bf16 v[120:123], v[74:77], v[138:141], 0\n"
        "s_nop 0\n"
        "v_cvt_pk_bf16_f32 v64, v116, v117\n"
        "v_cvt_pk_bf16_f32 v65, v118, v119\n"
        "v_mfma_f32_16x16x32_bf16 v[124:127], v[156:159], a[104:107], 0\n"
        "v_cvt_pk_bf16_f32 v113, v108, v109\n"
        "v_cvt_pk_bf16_f32 v116, v106, v107\n"
        "v_mfma_f32_16x16x32_bf16 v[124:127], v[150:153], a[100:103], v[124:127]\n"
        "ds_write_b128 v12, v[110:113] offset:3072\n"
        "v_mfma_f32_16x16x32_bf16 v[124:127], v[144:147], a[96:99], v[124:127]\n"
        "v_cvt_pk_bf16_f32 v73, v120, v121\n"
        "v_cvt_pk_bf16_f32 v117, v122, v123\n"
        "v_mfma_f32_16x16x32_bf16 v[106:109], v[144:147], a[108:111], 0\n"
        "ds_write_b128 v12, v[70:73] offset:4096\n"
        "v_mfma_f32_16x16x32_bf16 v[106:109], v[150:153], a[112:115], v[106:109]\n"
        "ds_write_b128 v12, v[114:117] offset:5120\n"
        "v_mfma_f32_16x16x32_bf16 v[106:109], v[156:159], a[116:119], v[106:109]\n"
        "v_pk_mul_f16 v105, v105, v242 op_sel_hi:[1,0]\n"
        "v_pk_mul_f16 v104, v104, v190\n"
        "v_mfma_f32_16x16x32_bf16 v[118:121], v[60:63], v[142:145], 0\n"
        "v_pk_mul_f16 v105, v105, v191\n"
        "v_cvt_pk_bf16_f32 v66, v124, v125\n"
        "v_mfma_f32_16x16x32_bf16 v[122:125], v[62:65], v[146:149], 0\n"
        "v_exp_f16_sdwa v104, v104 dst_sel:WORD_0 dst_unused:UNUSED_PRESERVE src0_sel:WORD_0\n"
        "v_mfma_f32_16x16x32_bf16 v[128:131], v[110:113], v[110:113], 0\n"
        "v_exp_f16_sdwa v105, v105 dst_sel:WORD_0 dst_unused:UNUSED_PRESERVE src0_sel:WORD_0\n"
        "v_mfma_f32_16x16x32_bf16 v[128:131], v[70:73], v[70:73], v[128:131]\n"
        "v_exp_f16_sdwa v104, v104 dst_sel:WORD_1 dst_unused:UNUSED_PRESERVE src0_sel:WORD_1\n"
        "v_mfma_f32_16x16x32_bf16 v[128:131], v[114:117], v[114:117], v[128:131]\n"
        "v_cvt_pk_bf16_f32 v67, v126, v127\n"
        "v_cvt_pk_bf16_f32 v70, v106, v107\n"
        "v_mfma_f32_16x16x32_bf16 v[110:113], v[156:159], a[128:131], 0\n"
        "v_cvt_pk_bf16_f32 v71, v108, v109\n"
        "v_cvt_pk_f16_f32 v106, v176, v177\n"
        "v_mfma_f32_16x16x32_bf16 v[110:113], v[150:153], a[124:127], v[110:113]\n"
        "v_exp_f16_sdwa v105, v105 dst_sel:WORD_1 dst_unused:UNUSED_PRESERVE src0_sel:WORD_1\n"
        "v_mfma_f32_16x16x32_bf16 v[110:113], v[144:147], a[120:123], v[110:113]\n"
        "v_cvt_pk_f16_f32 v107, v178, v179\n"
        "v_pk_mul_f16 v106, v106, v242 op_sel_hi:[1,0]\n"
        "v_mfma_f32_16x16x32_bf16 v[114:117], v[144:147], a[132:135], 0\n"
        "s_nop 0\n"
        "v_pk_mul_f16 v107, v107, v242 op_sel_hi:[1,0]\n"
        "v_pk_mul_f16 v106, v106, v192\n"
        "v_mfma_f32_16x16x32_bf16 v[114:117], v[150:153], a[136:139], v[114:117]\n"
        "s_nop 0\n"
        "v_cvt_pk_bf16_f32 v72, v110, v111\n"
        "v_cvt_pk_bf16_f32 v73, v112, v113\n"
        "v_mfma_f32_16x16x32_bf16 v[114:117], v[156:159], a[140:143], v[114:117]\n"
        "s_nop 0\n"
        "v_pk_mul_f16 v107, v107, v193\n"
        "v_cvt_pk_bf16_f32 v64, v118, v119\n"
        "v_mfma_f32_16x16x32_bf16 v[110:113], v[66:69], v[148:151], 0\n"
        "s_nop 0\n"
        "v_exp_f16_sdwa v106, v106 dst_sel:WORD_0 dst_unused:UNUSED_PRESERVE src0_sel:WORD_0\n"
        "v_mfma_f32_16x16x32_bf16 v[132:135], v[68:71], v[152:155], 0\n"
        "s_nop 0\n"
        "v_cvt_pk_bf16_f32 v76, v114, v115\n"
        "v_cvt_pk_bf16_f32 v77, v116, v117\n"
        "v_mfma_f32_16x16x32_bf16 v[114:117], a[152:155], v[218:221], 0\n"
        "v_exp_f16_sdwa v107, v107 dst_sel:WORD_0 dst_unused:UNUSED_PRESERVE src0_sel:WORD_0\n"
        "v_mfma_f32_16x16x32_bf16 v[114:117], a[148:151], v[210:213], v[114:117]\n"
        "v_mov_b32 v109, 0\n"
        "v_cndmask_b32_e64 v108, 0, v214, s[62:63]\n"
        "v_mfma_f32_16x16x32_bf16 v[114:117], a[144:147], v[206:209], v[114:117]\n"
        "v_cvt_pk_bf16_f32 v70, v120, v121\n"
        "v_cvt_pk_bf16_f32 v118, v122, v123\n"
        "v_mfma_f32_16x16x32_bf16 v[136:139], v[72:75], v[154:157], 0\n"
        "v_cvt_pk_bf16_f32 v65, v124, v125\n"
        "v_cvt_pk_bf16_f32 v71, v110, v111\n"
        "v_mfma_f32_16x16x32_bf16 v[120:123], v[74:77], v[158:161], 0\n"
        "v_cvt_pk_bf16_f32 v119, v112, v113\n"
        "v_cvt_pk_bf16_f32 v66, v132, v133\n"
        "v_mfma_f32_16x16x32_bf16 v[110:113], a[156:159], v[206:209], 0\n"
        "s_nop 0\n"
        "v_cvt_pk_bf16_f32 v72, v134, v135\n"
        "v_cvt_pk_bf16_f32 v67, v138, v139\n"
        "v_mfma_f32_16x16x32_bf16 v[110:113], a[160:163], v[210:213], v[110:113]\n"
        "ds_write_b128 v12, v[64:67] offset:6144\n"
        "v_mfma_f32_16x16x32_bf16 v[110:113], a[164:167], v[218:221], v[110:113]\n"
        "v_cvt_pk_bf16_f32 v73, v120, v121\n"
        "v_cvt_pk_bf16_f32 v120, v136, v137\n"
        "v_mfma_f32_16x16x32_bf16 v[124:127], a[176:179], v[218:221], 0\n"
        "ds_write_b128 v12, v[70:73] offset:7168\n"
        "v_mfma_f32_16x16x32_bf16 v[124:127], a[172:175], v[210:213], v[124:127]\n"
        "v_cvt_pk_bf16_f32 v121, v122, v123\n"
        "v_cndmask_b32_e64 v60, v166, v167, s[56:57]\n"
        "v_mfma_f32_16x16x32_bf16 v[124:127], a[168:171], v[206:209], v[124:127]\n"
        "ds_write_b128 v12, v[118:121] offset:8192\n"
        "v_mfma_f32_16x16x32_bf16 v[132:135], v[64:67], v[64:67], 0\n"
        "v_exp_f16_sdwa v106, v106 dst_sel:WORD_1 dst_unused:UNUSED_PRESERVE src0_sel:WORD_1\n"
        "v_mfma_f32_16x16x32_bf16 v[132:135], v[70:73], v[70:73], v[132:135]\n"
        "v_exp_f16_sdwa v107, v107 dst_sel:WORD_1 dst_unused:UNUSED_PRESERVE src0_sel:WORD_1\n"
        "v_mfma_f32_16x16x32_bf16 v[132:135], v[118:121], v[118:121], v[132:135]\n"
        "v_cndmask_b32_e64 v60, v60, v168, s[58:59]\n"
        "v_cndmask_b32_e64 v61, v128, v129, s[56:57]\n"
        "v_mfma_f32_16x16x32_bf16 v[64:67], a[188:191], v[218:221], 0\n"
        "v_cndmask_b32_e64 v61, v61, v130, s[58:59]\n"
        "v_cndmask_b32_e64 v61, v61, v131, s[60:61]\n"
        "v_mfma_f32_16x16x32_bf16 v[64:67], a[184:187], v[210:213], v[64:67]\n"
        "v_cvt_pk_bf16_f32 v70, v114, v115\n"
        "v_cvt_pk_bf16_f32 v71, v116, v117\n"
        "v_mfma_f32_16x16x32_bf16 v[64:67], a[180:183], v[206:209], v[64:67]\n"
        "v_cvt_pk_bf16_f32 v72, v110, v111\n"
        "v_cvt_pk_bf16_f32 v73, v112, v113\n"
        "v_mfma_f32_16x16x32_bf16 v[110:113], a[192:195], v[206:209], 0\n"
        "v_cvt_pk_bf16_f32 v114, v124, v125\n"
        "v_cvt_pk_bf16_f32 v115, v126, v127\n"
        "v_mfma_f32_16x16x32_bf16 v[110:113], a[196:199], v[210:213], v[110:113]\n"
        "v_cndmask_b32_e64 v76, v132, v133, s[56:57]\n"
        "v_cndmask_b32_e64 v76, v76, v134, s[58:59]\n"
        "v_mfma_f32_16x16x32_bf16 v[110:113], a[200:203], v[218:221], v[110:113]\n"
        "v_cndmask_b32_e64 v76, v76, v135, s[60:61]\n"
        "v_cvt_pk_bf16_f32 v116, v64, v65\n"
        "v_mfma_f32_16x16x32_bf16 v[118:121], a[212:215], v[218:221], 0\n"
        "s_nop 0\n"
        "v_cvt_pk_bf16_f32 v117, v66, v67\n"
        "v_cndmask_b32_e64 v60, v60, v169, s[60:61]\n"
        "v_mfma_f32_16x16x32_bf16 v[118:121], a[208:211], v[210:213], v[118:121]\n"
        "s_nop 0\n"
        "v_cvt_pk_bf16_f32 v64, v110, v111\n"
        "v_cvt_pk_bf16_f32 v65, v112, v113\n"
        "v_mfma_f32_16x16x32_bf16 v[118:121], a[204:207], v[206:209], v[118:121]\n"
        "v_cndmask_b32_dpp v61, v76, v61, vcc row_ror:4 row_mask:0xf bank_mask:0xf\n"
        "v_mul_f32 v76, s80, v236\n"
        "v_mfma_f32_16x16x32_f16 v[122:125], v[90:93], v[102:105], 0\n"
        "s_nop 0\n"
        "v_cndmask_b32_dpp v60, v61, v60, vcc row_ror:4 row_mask:0xf bank_mask:0xf\n"
        "v_mul_f32 v60, s80, v60\n"
        "v_mfma_f32_16x16x32_f16 v[122:125], v[90:93], v[106:109], v[122:125]\n"
        "s_nop 0\n"
        "v_cvt_pk_bf16_f32 v66, v118, v119\n"
        "v_cvt_pk_bf16_f32 v67, v120, v121\n"
        "v_mfma_f32_16x16x32_bf16 v[118:121], v[194:197], v[70:73], 0\n"
        "v_fma_f32 v76, -v76, v76, v60\n"
        "v_max_f32 v76, 0, v76\n"
        "v_mfma_f32_16x16x32_bf16 v[118:121], v[198:201], v[114:117], v[118:121]\n"
        "v_sub_f32 v60, 0x44800000, v60\n"
        "v_add_f32 v76, s81, v76\n"
        "v_mfma_f32_16x16x32_bf16 v[118:121], v[202:205], v[64:67], v[118:121]\n"
        "v_bfi_b32 v76, s84, v60, v76\n"
        "v_max_f32 v60, s95, v122\n"
        "v_mfma_f32_16x16x32_bf16 v[124:127], v[218:221], v[64:67], 0\n"
        "s_nop 0\n"
        "v_rsq_f32 v61, v76\n"
        "v_mfma_f32_16x16x32_bf16 v[124:127], v[210:213], v[114:117], v[124:127]\n"
        "s_nop 0\n"
        "v_cvt_f16_f32 v61, v61\n"
        "v_cvt_pk_f16_f32 v110, v118, v119\n"
        "v_mfma_f32_16x16x32_bf16 v[124:127], v[206:209], v[70:73], v[124:127]\n"
        "ds_write_b16 v18, v61\n"
        "v_mfma_f32_16x16x32_bf16 v[128:131], v[222:225], v[70:73], 0\n"
        "ds_write_b16 v19, v61\n"
        "v_mfma_f32_16x16x32_bf16 v[128:131], v[226:229], v[114:117], v[128:131]\n"
        "v_rcp_f32 v61, v122\n"
        "v_mfma_f32_16x16x32_bf16 v[128:131], v[230:233], v[64:67], v[128:131]\n"
        "v_mov_b32 v70, s29\n"
        "v_cvt_f16_f32 v76, v61\n"
        "v_mfma_f32_16x16x32_bf16 v[64:67], a[152:155], v[230:233], 0\n"
        "ds_write_b16 v22, v70 offset:4096\n"
        "v_mfma_f32_16x16x32_bf16 v[64:67], a[148:151], v[226:229], v[64:67]\n"
        "v_add_f32 v77, v235, v61\n"
        "v_pk_mul_f16 v102, v102, v76 op_sel_hi:[1,0]\n"
        "v_mfma_f32_16x16x32_bf16 v[64:67], a[144:147], v[222:225], v[64:67]\n"
        "v_pk_mul_f16 v103, v103, v76 op_sel_hi:[1,0]\n"
        "v_pk_mul_f16 v104, v104, v76 op_sel_hi:[1,0]\n"
        "v_mfma_f32_16x16x32_bf16 v[70:73], a[156:159], v[222:225], 0\n"
        "v_pk_mul_f16 v105, v105, v76 op_sel_hi:[1,0]\n"
        "v_pk_mul_f16 v106, v106, v76 op_sel_hi:[1,0]\n"
        "v_mfma_f32_16x16x32_bf16 v[70:73], a[160:163], v[226:229], v[70:73]\n"
        "v_pk_mul_f16 v107, v107, v76 op_sel_hi:[1,0]\n"
        "v_pk_mul_f16 v108, v108, v76 op_sel_hi:[1,0]\n"
        "v_mfma_f32_16x16x32_bf16 v[70:73], a[164:167], v[230:233], v[70:73]\n"
        "v_cvt_pk_bf16_f32 v132, v64, v65\n"
        "v_cvt_pk_bf16_f32 v133, v66, v67\n"
        "v_mfma_f32_16x16x32_bf16 v[64:67], a[176:179], v[230:233], 0\n"
        "s_nop 0\n"
        "v_pk_mul_f16 v102, v102, v188\n"
        "v_pk_mul_f16 v103, v103, v189\n"
        "v_mfma_f32_16x16x32_bf16 v[64:67], a[172:175], v[226:229], v[64:67]\n"
        "s_nop 0\n"
        "v_cvt_pk_bf16_f32 v134, v70, v71\n"
        "v_cvt_pk_bf16_f32 v135, v72, v73\n"
        "v_mfma_f32_16x16x32_bf16 v[64:67], a[168:171], v[222:225], v[64:67]\n"
        "v_pk_mul_f16 v104, v104, v190\n"
        "v_pk_mul_f16 v105, v105, v191\n"
        "v_mfma_f32_16x16x32_bf16 v[70:73], a[180:183], v[222:225], 0\n"
        "s_nop 0\n"
        "v_pk_mul_f16 v106, v106, v192\n"
        "v_pk_mul_f16 v107, v107, v193\n"
        "v_mfma_f32_16x16x32_bf16 v[70:73], a[184:187], v[226:229], v[70:73]\n"
        "s_nop 0\n"
        "v_cvt_pk_bf16_f32 v118, v64, v65\n"
        "v_cvt_pk_bf16_f32 v119, v66, v67\n"
        "v_mfma_f32_16x16x32_bf16 v[70:73], a[188:191], v[230:233], v[70:73]\n"
        "v_cvt_pk_f16_f32 v111, v120, v121\n"
        "v_pk_mul_f16 v110, v110, v242 op_sel:[0,1]\n"
        "v_mfma_f32_16x16x32_bf16 v[64:67], a[200:203], v[230:233], 0\n"
        "s_nop 0\n"
        "v_pk_mul_f16 v111, v111, v242 op_sel:[0,1]\n"
        "v_pk_mul_f16 v110, v110, v188\n"
        "v_mfma_f32_16x16x32_bf16 v[64:67], a[196:199], v[226:229], v[64:67]\n"
        "s_nop 0\n"
        "v_cvt_pk_bf16_f32 v120, v70, v71\n"
        "v_cvt_pk_bf16_f32 v121, v72, v73\n"
        "v_mfma_f32_16x16x32_bf16 v[64:67], a[192:195], v[222:225], v[64:67]\n"
        "s_nop 0\n"
        "v_pk_mul_f16 v111, v111, v189\n"
        "v_cvt_pk_f16_f32 v112, v124, v125\n"
        "v_mfma_f32_16x16x32_bf16 v[70:73], a[204:207], v[222:225], 0\n"
        "s_nop 0\n"
        "v_exp_f16_sdwa v110, v110 dst_sel:WORD_0 dst_unused:UNUSED_PRESERVE src0_sel:WORD_0\n"
        "v_mfma_f32_16x16x32_bf16 v[70:73], a[208:211], v[226:229], v[70:73]\n"
        "s_nop 0\n"
        "v_cvt_pk_bf16_f32 v122, v64, v65\n"
        "v_cvt_pk_bf16_f32 v123, v66, v67\n"
        "v_mfma_f32_16x16x32_bf16 v[70:73], a[212:215], v[230:233], v[70:73]\n"
        "v_exp_f16_sdwa v111, v111 dst_sel:WORD_0 dst_unused:UNUSED_PRESERVE src0_sel:WORD_0\n"
        "v_mfma_f32_16x16x32_bf16 v[64:67], v[202:205], a[224:227], 0\n"
        "v_exp_f16_sdwa v110, v110 dst_sel:WORD_1 dst_unused:UNUSED_PRESERVE src0_sel:WORD_1\n"
        "v_mfma_f32_16x16x32_bf16 v[64:67], v[198:201], a[220:223], v[64:67]\n"
        "s_nop 0\n"
        "v_exp_f16_sdwa v111, v111 dst_sel:WORD_1 dst_unused:UNUSED_PRESERVE src0_sel:WORD_1\n"
        "v_mfma_f32_16x16x32_bf16 v[64:67], v[194:197], a[216:219], v[64:67]\n"
        "s_nop 0\n"
        "v_cvt_pk_bf16_f32 v124, v70, v71\n"
        "v_cvt_pk_bf16_f32 v125, v72, v73\n"
        "v_mfma_f32_16x16x32_bf16 v[70:73], v[206:209], a[216:219], 0\n"
        "v_cvt_pk_f16_f32 v113, v126, v127\n"
        "v_pk_mul_f16 v112, v112, v242 op_sel:[0,1]\n"
        "v_mfma_f32_16x16x32_bf16 v[70:73], v[210:213], a[220:223], v[70:73]\n"
        "v_pk_mul_f16 v113, v113, v242 op_sel:[0,1]\n"
        "v_pk_mul_f16 v112, v112, v190\n"
        "v_mfma_f32_16x16x32_bf16 v[70:73], v[218:221], a[224:227], v[70:73]\n"
        "v_pk_mul_f16 v113, v113, v191\n"
        "v_cvt_pk_f16_f32 v114, v128, v129\n"
        "v_mfma_f32_16x16x32_bf16 v[136:139], v[230:233], a[224:227], 0\n"
        "v_exp_f16_sdwa v112, v112 dst_sel:WORD_0 dst_unused:UNUSED_PRESERVE src0_sel:WORD_0\n"
        "v_mfma_f32_16x16x32_bf16 v[136:139], v[226:229], a[220:223], v[136:139]\n"
        "v_exp_f16_sdwa v113, v113 dst_sel:WORD_0 dst_unused:UNUSED_PRESERVE src0_sel:WORD_0\n"
        "v_mfma_f32_16x16x32_bf16 v[136:139], v[222:225], a[216:219], v[136:139]\n"
        "v_exp_f16_sdwa v112, v112 dst_sel:WORD_1 dst_unused:UNUSED_PRESERVE src0_sel:WORD_1\n"
        "v_mfma_f32_16x16x32_bf16 v[126:129], v[194:197], v[132:135], 0\n"
        "v_exp_f16_sdwa v113, v113 dst_sel:WORD_1 dst_unused:UNUSED_PRESERVE src0_sel:WORD_1\n"
        "v_mfma_f32_16x16x32_bf16 v[126:129], v[198:201], v[118:121], v[126:129]\n"
        "v_cvt_pk_f16_f32 v115, v130, v131\n"
        "v_pk_mul_f16 v114, v114, v242 op_sel:[0,1]\n"
        "v_mfma_f32_16x16x32_bf16 v[126:129], v[202:205], v[122:125], v[126:129]\n"
        "v_pk_mul_f16 v115, v115, v242 op_sel:[0,1]\n"
        "v_pk_mul_f16 v114, v114, v192\n"
        "v_mfma_f32_16x16x32_bf16 v[140:143], v[218:221], v[122:125], 0\n"
        "v_pk_mul_f16 v115, v115, v193\n"
        "v_cvt_pk_f16_f32 v130, v64, v65\n"
        "v_mfma_f32_16x16x32_bf16 v[140:143], v[210:213], v[118:121], v[140:143]\n"
        "v_exp_f16_sdwa v114, v114 dst_sel:WORD_0 dst_unused:UNUSED_PRESERVE src0_sel:WORD_0\n"
        "v_mfma_f32_16x16x32_bf16 v[140:143], v[206:209], v[132:135], v[140:143]\n"
        "v_mov_b32 v117, 0\n"
        "v_cndmask_b32_e64 v116, 0, v214, s[64:65]\n"
        "v_mfma_f32_16x16x32_bf16 v[144:147], v[222:225], v[132:135], 0\n"
        "v_exp_f16_sdwa v115, v115 dst_sel:WORD_0 dst_unused:UNUSED_PRESERVE src0_sel:WORD_0\n"
        "v_mfma_f32_16x16x32_bf16 v[144:147], v[226:229], v[118:121], v[144:147]\n"
        "v_exp_f16_sdwa v114, v114 dst_sel:WORD_1 dst_unused:UNUSED_PRESERVE src0_sel:WORD_1\n"
        "v_mfma_f32_16x16x32_bf16 v[144:147], v[230:233], v[122:125], v[144:147]\n"
        "v_exp_f16_sdwa v115, v115 dst_sel:WORD_1 dst_unused:UNUSED_PRESERVE src0_sel:WORD_1\n"
        "v_mfma_f32_16x16x32_bf16 v[148:151], v[230:233], a[236:239], 0\n"
        "v_cvt_pk_f16_f32 v131, v66, v67\n"
        "v_cvt_pk_f16_f32 v120, v126, v127\n"
        "v_mfma_f32_16x16x32_bf16 v[148:151], v[226:229], a[232:235], v[148:151]\n"
        "v_cvt_pk_f16_f32 v121, v128, v129\n"
        "v_pk_mul_f16 v120, v120, v243 op_sel_hi:[1,0]\n"
        "v_mfma_f32_16x16x32_bf16 v[148:151], v[222:225], a[228:231], v[148:151]\n"
        "v_pk_mul_f16 v121, v121, v243 op_sel_hi:[1,0]\n"
        "v_pk_mul_f16 v120, v120, v188\n"
        "v_mfma_f32_16x16x32_bf16 v[64:67], v[206:209], a[228:231], 0\n"
        "v_pk_mul_f16 v121, v121, v189\n"
        "v_cvt_pk_f16_f32 v122, v140, v141\n"
        "v_mfma_f32_16x16x32_bf16 v[64:67], v[210:213], a[232:235], v[64:67]\n"
        "v_exp_f16_sdwa v120, v120 dst_sel:WORD_0 dst_unused:UNUSED_PRESERVE src0_sel:WORD_0\n"
        "v_mfma_f32_16x16x32_bf16 v[64:67], v[218:221], a[236:239], v[64:67]\n"
        "v_exp_f16_sdwa v121, v121 dst_sel:WORD_0 dst_unused:UNUSED_PRESERVE src0_sel:WORD_0\n"
        "v_mfma_f32_16x16x32_bf16 v[152:155], v[202:205], a[236:239], 0\n"
        "v_exp_f16_sdwa v120, v120 dst_sel:WORD_1 dst_unused:UNUSED_PRESERVE src0_sel:WORD_1\n"
        "v_mfma_f32_16x16x32_bf16 v[152:155], v[198:201], a[232:235], v[152:155]\n"
        "v_exp_f16_sdwa v121, v121 dst_sel:WORD_1 dst_unused:UNUSED_PRESERVE src0_sel:WORD_1\n"
        "v_mfma_f32_16x16x32_bf16 v[152:155], v[194:197], a[228:231], v[152:155]\n"
        "v_cvt_pk_f16_f32 v123, v142, v143\n"
        "v_pk_mul_f16 v122, v122, v243 op_sel_hi:[1,0]\n"
        "v_mfma_f32_16x16x32_bf16 v[158:161], v[194:197], a[240:243], 0\n"
        "v_pk_mul_f16 v123, v123, v243 op_sel_hi:[1,0]\n"
        "v_pk_mul_f16 v122, v122, v190\n"
        "v_mfma_f32_16x16x32_bf16 v[158:161], v[198:201], a[244:247], v[158:161]\n"
        "v_pk_mul_f16 v123, v123, v191\n"
        "v_cvt_pk_f16_f32 v124, v144, v145\n"
        "v_mfma_f32_16x16x32_bf16 v[158:161], v[202:205], a[248:251], v[158:161]\n"
        "v_exp_f16_sdwa v122, v122 dst_sel:WORD_0 dst_unused:UNUSED_PRESERVE src0_sel:WORD_0\n"
        "v_mfma_f32_16x16x32_bf16 v[164:167], v[218:221], a[248:251], 0\n"
        "v_exp_f16_sdwa v123, v123 dst_sel:WORD_0 dst_unused:UNUSED_PRESERVE src0_sel:WORD_0\n"
        "v_mfma_f32_16x16x32_bf16 v[164:167], v[210:213], a[244:247], v[164:167]\n"
        "v_exp_f16_sdwa v122, v122 dst_sel:WORD_1 dst_unused:UNUSED_PRESERVE src0_sel:WORD_1\n"
        "v_mfma_f32_16x16x32_bf16 v[164:167], v[206:209], a[240:243], v[164:167]\n"
        "v_exp_f16_sdwa v123, v123 dst_sel:WORD_1 dst_unused:UNUSED_PRESERVE src0_sel:WORD_1\n"
        "v_mfma_f32_16x16x32_bf16 v[172:175], v[222:225], a[240:243], 0\n"
        "v_cvt_pk_f16_f32 v125, v146, v147\n"
        "v_pk_mul_f16 v124, v124, v243 op_sel_hi:[1,0]\n"
        "v_mfma_f32_16x16x32_bf16 v[172:175], v[226:229], a[244:247], v[172:175]\n"
        "v_mov_b32 v127, 0\n"
        "v_cndmask_b32_e64 v126, 0, v214, s[66:67]\n"
        "v_mfma_f32_16x16x32_bf16 v[172:175], v[230:233], a[248:251], v[172:175]\n"
        "v_pk_mul_f16 v125, v125, v243 op_sel_hi:[1,0]\n"
        "v_pk_mul_f16 v124, v124, v192\n"
        "v_mfma_f32_16x16x32_f16 v[140:143], v[90:93], v[110:113], 0\n"
        "v_pk_mul_f16 v125, v125, v193\n"
        "v_cvt_pk_f16_f32 v132, v70, v71\n"
        "v_mfma_f32_16x16x32_f16 v[140:143], v[90:93], v[114:117], v[140:143]\n"
        "v_exp_f16_sdwa v124, v124 dst_sel:WORD_0 dst_unused:UNUSED_PRESERVE src0_sel:WORD_0\n"
        "v_mfma_f32_16x16x32_bf16 v[180:183], v[230:233], v[32:35], 0\n"
        "v_exp_f16_sdwa v125, v125 dst_sel:WORD_0 dst_unused:UNUSED_PRESERVE src0_sel:WORD_0\n"
        "v_mfma_f32_16x16x32_bf16 v[180:183], v[226:229], v[28:31], v[180:183]\n"
        "v_exp_f16_sdwa v124, v124 dst_sel:WORD_1 dst_unused:UNUSED_PRESERVE src0_sel:WORD_1\n"
        "v_mfma_f32_16x16x32_bf16 v[180:183], v[222:225], a[252:255], v[180:183]\n"
        "v_exp_f16_sdwa v125, v125 dst_sel:WORD_1 dst_unused:UNUSED_PRESERVE src0_sel:WORD_1\n"
        "v_mfma_f32_16x16x32_bf16 v[214:217], v[206:209], a[252:255], 0\n"
        "v_cvt_pk_f16_f32 v133, v72, v73\n"
        "v_cvt_pk_f16_f32 v134, v136, v137\n"
        "v_mfma_f32_16x16x32_bf16 v[214:217], v[210:213], v[28:31], v[214:217]\n"
        "v_cvt_pk_f16_f32 v135, v138, v139\n"
        "v_cvt_pk_f16_f32 v144, v148, v149\n"
        "v_mfma_f32_16x16x32_bf16 v[214:217], v[218:221], v[32:35], v[214:217]\n"
        "ds_read2_b32 v[136:137], v24 offset0:0 offset1:0\n"
        "ds_read2_b32 v[146:147], v24 offset0:1 offset1:1\n"
        "v_mfma_f32_16x16x32_bf16 v[70:73], v[202:205], v[32:35], 0\n"
        "ds_read2_b32 v[162:163], v24 offset0:2 offset1:2\n"
        "ds_read2_b32 v[170:171], v24 offset0:3 offset1:3\n"
        "v_mfma_f32_16x16x32_bf16 v[70:73], v[198:201], v[28:31], v[70:73]\n"
        "ds_read2_b32 v[178:179], v24 offset0:4 offset1:4\n"
        "ds_read2_b32 v[186:187], v24 offset0:5 offset1:5\n"
        "v_mfma_f32_16x16x32_bf16 v[70:73], v[194:197], a[252:255], v[70:73]\n"
        "v_cvt_pk_f16_f32 v145, v150, v151\n"
        "v_max_f32 v60, v60, v140\n"
        "v_mfma_f32_16x16x32_bf16 v[148:151], v[194:197], v[36:39], 0\n"
        "v_rcp_f32 v61, v140\n"
        "v_mfma_f32_16x16x32_bf16 v[148:151], v[198:201], v[40:43], v[148:151]\n"
        "v_add_f32 v77, v77, v61\n"
        "v_cvt_pk_f16_f32 v142, v64, v65\n"
        "v_mfma_f32_16x16x32_bf16 v[148:151], v[202:205], v[44:47], v[148:151]\n"
        "v_cvt_pk_f16_f32 v143, v66, v67\n"
        "v_cvt_pk_f16_f32 v140, v152, v153\n"
        "v_mfma_f32_16x16x32_bf16 v[64:67], v[218:221], v[44:47], 0\n"
        "v_cvt_pk_f16_f32 v141, v154, v155\n"
        "v_cvt_pk_f16_f32 v156, v158, v159\n"
        "v_mfma_f32_16x16x32_bf16 v[64:67], v[210:213], v[40:43], v[64:67]\n"
        "v_cvt_pk_f16_f32 v157, v160, v161\n"
        "v_cvt_pk_f16_f32 v158, v164, v165\n"
        "v_mfma_f32_16x16x32_bf16 v[64:67], v[206:209], v[36:39], v[64:67]\n"
        "v_cvt_pk_f16_f32 v159, v166, v167\n"
        "v_cvt_pk_f16_f32 v160, v172, v173\n"
        "v_mfma_f32_16x16x32_bf16 v[152:155], v[222:225], v[36:39], 0\n"
        "s_waitcnt lgkmcnt(0)\n"
        "v_cvt_pk_f16_f32 v161, v174, v175\n"
        "v_cvt_f16_f32 v76, v61\n"
        "v_mfma_f32_16x16x32_bf16 v[152:155], v[226:229], v[40:43], v[152:155]\n"
        "v_pk_mul_f16 v110, v110, v76 op_sel_hi:[1,0]\n"
        "v_pk_mul_f16 v111, v111, v76 op_sel_hi:[1,0]\n"
        "v_mfma_f32_16x16x32_bf16 v[152:155], v[230:233], v[44:47], v[152:155]\n"
        "v_pk_mul_f16 v112, v112, v76 op_sel_hi:[1,0]\n"
        "v_pk_mul_f16 v113, v113, v76 op_sel_hi:[1,0]\n"
        "v_mfma_f32_16x16x32_f16 v[172:175], v[90:93], v[120:123], 0\n"
        "v_pk_mul_f16 v114, v114, v76 op_sel_hi:[1,0]\n"
        "v_pk_mul_f16 v115, v115, v76 op_sel_hi:[1,0]\n"
        "v_mfma_f32_16x16x32_f16 v[172:175], v[90:93], v[124:127], v[172:175]\n"
        "v_pk_mul_f16 v116, v116, v76 op_sel_hi:[1,0]\n"
        "v_cvt_pk_f16_f32 v168, v180, v181\n"
        "v_mfma_f32_16x16x32_bf16 v[234:237], v[230:233], v[56:59], 0\n"
        "s_nop 0\n"
        "v_cvt_pk_f16_f32 v169, v182, v183\n"
        "v_cvt_pk_f16_f32 v166, v214, v215\n"
        "v_mfma_f32_16x16x32_bf16 v[234:237], v[226:229], v[52:55], v[234:237]\n"
        "s_nop 0\n"
        "v_rcp_f32 v61, v172\n"
        "v_mfma_f32_16x16x32_bf16 v[234:237], v[222:225], v[48:51], v[234:237]\n"
        "v_max_f32 v60, v60, v172\n"
        "v_add_f32 v77, v77, v61\n"
        "v_mfma_f32_16x16x32_bf16 v[238:241], v[206:209], v[48:51], 0\n"
        "v_fma_f32 v60, v77, s82, v60\n"
        "v_cmp_nlt_f32_e64 s[74:75], v60, s83\n"
        "v_mfma_f32_16x16x32_bf16 v[238:241], v[210:213], v[52:55], v[238:241]\n"
        "v_cvt_pk_f16_f32 v167, v216, v217\n"
        "v_cvt_pk_f16_f32 v164, v70, v71\n"
        "v_mfma_f32_16x16x32_bf16 v[238:241], v[218:221], v[56:59], v[238:241]\n"
        "s_cmp_lg_u64 s[74:75], 0\n"
        "v_cvt_pk_f16_f32 v165, v72, v73\n"
        "v_cvt_pk_f16_f32 v172, v148, v149\n"
        "v_mfma_f32_16x16x32_bf16 v[70:73], v[202:205], v[56:59], 0\n"
        "s_cbranch_scc1 SLOW_%=\n"
        "v_cvt_pk_f16_f32 v173, v150, v151\n"
        "v_cvt_pk_f16_f32 v174, v64, v65\n"
        "v_mfma_f32_16x16x32_bf16 v[70:73], v[198:201], v[52:55], v[70:73]\n"
        "v_cvt_pk_f16_f32 v175, v66, v67\n"
        "v_cvt_pk_f16_f32 v176, v152, v153\n"
        "v_mfma_f32_16x16x32_bf16 v[70:73], v[194:197], v[48:51], v[70:73]\n"
        "v_cvt_pk_f16_f32 v177, v154, v155\n"
        "v_pk_mul_f16 v110, v110, v188\n"
        "v_mfma_f32_16x16x32_f16 v[64:67], v[102:105], v[130:133], 0\n"
        "v_pk_mul_f16 v111, v111, v189\n"
        "v_cvt_pk_f16_f32 v184, v234, v235\n"
        "v_mfma_f32_16x16x32_f16 v[64:67], v[106:109], v[134:137], v[64:67]\n"
        "v_cvt_pk_f16_f32 v185, v236, v237\n"
        "v_cvt_pk_f16_f32 v182, v238, v239\n"
        "v_mfma_f32_16x16x32_bf16 v[64:67], v[194:197], v[94:97], v[64:67]\n"
        "v_cvt_pk_f16_f32 v183, v240, v241\n"
        "v_cvt_pk_f16_f32 v180, v70, v71\n"
        "v_mfma_f32_16x16x32_bf16 v[148:151], v[194:197], v[98:101], 0\n"
        "v_cvt_pk_f16_f32 v181, v72, v73\n"
        "v_pk_mul_f16 v112, v112, v190\n"
        "v_mfma_f32_16x16x32_f16 v[148:151], v[106:109], v[144:147], v[148:151]\n"
        "v_pk_mul_f16 v113, v113, v191\n"
        "v_pk_mul_f16 v114, v114, v192\n"
        "v_mfma_f32_16x16x32_f16 v[148:151], v[102:105], v[140:143], v[148:151]\n"
        "v_pk_mul_f16 v115, v115, v193\n"
        "v_cvt_f16_f32 v60, v61\n"
        "v_mfma_f32_16x16x32_f16 v[70:73], v[102:105], v[156:159], 0\n"
        "s_nop 0\n"
        "v_pk_mul_f16 v120, v120, v60 op_sel_hi:[1,0]\n"
        "v_pk_mul_f16 v121, v121, v60 op_sel_hi:[1,0]\n"
        "v_mfma_f32_16x16x32_f16 v[70:73], v[106:109], v[160:163], v[70:73]\n"
        "s_nop 0\n"
        "v_cvt_pk_bf16_f32 v152, v64, v148\n"
        "v_cvt_pk_bf16_f32 v194, v65, v149\n"
        "v_mfma_f32_16x16x32_bf16 v[70:73], v[198:201], v[94:97], v[70:73]\n"
        "v_cvt_pk_bf16_f32 v214, v66, v150\n"
        "v_cvt_pk_bf16_f32 v234, v67, v151\n"
        "v_mfma_f32_16x16x32_bf16 v[64:67], v[198:201], v[98:101], 0\n"
        "v_pk_mul_f16 v122, v122, v60 op_sel_hi:[1,0]\n"
        "v_pk_mul_f16 v123, v123, v60 op_sel_hi:[1,0]\n"
        "v_mfma_f32_16x16x32_f16 v[64:67], v[106:109], v[168:171], v[64:67]\n"
        "v_pk_mul_f16 v124, v124, v60 op_sel_hi:[1,0]\n"
        "v_pk_mul_f16 v125, v125, v60 op_sel_hi:[1,0]\n"
        "v_mfma_f32_16x16x32_f16 v[64:67], v[102:105], v[164:167], v[64:67]\n"
        "v_pk_mul_f16 v126, v126, v60 op_sel_hi:[1,0]\n"
        "v_pk_mul_f16 v120, v120, v188\n"
        "v_mfma_f32_16x16x32_f16 v[148:151], v[102:105], v[172:175], 0\n"
        "s_nop 0\n"
        "v_pk_mul_f16 v121, v121, v189\n"
        "v_pk_mul_f16 v122, v122, v190\n"
        "v_mfma_f32_16x16x32_f16 v[148:151], v[106:109], v[176:179], v[148:151]\n"
        "s_nop 0\n"
        "v_cvt_pk_bf16_f32 v153, v70, v64\n"
        "v_cvt_pk_bf16_f32 v195, v71, v65\n"
        "v_mfma_f32_16x16x32_bf16 v[148:151], v[202:205], v[94:97], v[148:151]\n"
        "v_cvt_pk_bf16_f32 v215, v72, v66\n"
        "v_cvt_pk_bf16_f32 v235, v73, v67\n"
        "v_mfma_f32_16x16x32_bf16 v[64:67], v[202:205], v[98:101], 0\n"
        "v_pk_mul_f16 v123, v123, v191\n"
        "v_pk_mul_f16 v124, v124, v192\n"
        "v_mfma_f32_16x16x32_f16 v[64:67], v[106:109], v[184:187], v[64:67]\n"
        "v_pk_mul_f16 v125, v125, v193\n"
        "v_mfma_f32_16x16x32_f16 v[64:67], v[102:105], v[180:183], v[64:67]\n"
        "s_nop 1\n"
        "v_mfma_f32_16x16x32_f16 v[70:73], v[110:113], v[130:133], 0\n"
        "s_nop 2\n"
        "v_mfma_f32_16x16x32_f16 v[70:73], v[114:117], v[134:137], v[70:73]\n"
        "s_add_u32 s90, s89, 0\n"
        "v_cvt_pk_bf16_f32 v154, v148, v64\n"
        "v_cvt_pk_bf16_f32 v196, v149, v65\n"
        "v_mfma_f32_16x16x32_bf16 v[70:73], v[206:209], v[94:97], v[70:73]\n"
        "v_cvt_pk_bf16_f32 v216, v150, v66\n"
        "v_cvt_pk_bf16_f32 v236, v151, v67\n"
        "v_mfma_f32_16x16x32_bf16 v[64:67], v[206:209], v[98:101], 0\n"
        "v_mfma_f32_16x16x32_f16 v[64:67], v[114:117], v[144:147], v[64:67]\n"
        "v_mfma_f32_16x16x32_f16 v[64:67], v[110:113], v[140:143], v[64:67]\n"
        "s_add_u32 s91, s89, 98112\n"
        "v_mfma_f32_16x16x32_f16 v[102:105], v[110:113], v[156:159], 0\n"
        "buffer_store_dwordx3 v[152:154], v14, s[40:43], s90 offen sc1 nt\n"
        "s_nop 2\n"
        "v_mfma_f32_16x16x32_f16 v[102:105], v[114:117], v[160:163], v[102:105]\n"
        "s_nop 0\n"
        "v_cvt_pk_bf16_f32 v106, v70, v64\n"
        "v_cvt_pk_bf16_f32 v148, v71, v65\n"
        "v_mfma_f32_16x16x32_bf16 v[102:105], v[210:213], v[94:97], v[102:105]\n"
        "buffer_store_dwordx3 v[194:196], v15, s[40:43], s90 offen offset:768 sc1 nt\n"
        "v_cvt_pk_bf16_f32 v152, v72, v66\n"
        "v_cvt_pk_bf16_f32 v200, v73, v67\n"
        "v_mfma_f32_16x16x32_bf16 v[64:67], v[210:213], v[98:101], 0\n"
        "v_mfma_f32_16x16x32_f16 v[64:67], v[114:117], v[168:171], v[64:67]\n"
        "buffer_store_dwordx3 v[214:216], v14, s[40:43], s91 offen offset:192 sc1 nt\n"
        "v_mfma_f32_16x16x32_f16 v[64:67], v[110:113], v[164:167], v[64:67]\n"
        "s_nop 0\n"
        "v_mfma_f32_16x16x32_f16 v[70:73], v[110:113], v[172:175], 0\n"
        "buffer_store_dwordx3 v[234:236], v16, s[40:43], s91 offen offset:960 sc1 nt\n"
        "s_nop 2\n"
        "v_mfma_f32_16x16x32_f16 v[70:73], v[114:117], v[176:179], v[70:73]\n"
        "s_nop 0\n"
        "v_cvt_pk_bf16_f32 v107, v102, v64\n"
        "v_cvt_pk_bf16_f32 v149, v103, v65\n"
        "v_mfma_f32_16x16x32_bf16 v[70:73], v[218:221], v[94:97], v[70:73]\n"
        "v_cvt_pk_bf16_f32 v153, v104, v66\n"
        "v_cvt_pk_bf16_f32 v201, v105, v67\n"
        "v_mfma_f32_16x16x32_bf16 v[64:67], v[218:221], v[98:101], 0\n"
        "v_mfma_f32_16x16x32_f16 v[64:67], v[114:117], v[184:187], v[64:67]\n"
        "v_mfma_f32_16x16x32_f16 v[64:67], v[110:113], v[180:183], v[64:67]\n"
        "s_nop 1\n"
        "v_mfma_f32_16x16x32_f16 v[102:105], v[120:123], v[130:133], 0\n"
        "s_nop 2\n"
        "v_mfma_f32_16x16x32_f16 v[102:105], v[124:127], v[134:137], v[102:105]\n"
        "s_add_u32 s90, s89, 196224\n"
        "v_cvt_pk_bf16_f32 v108, v70, v64\n"
        "v_cvt_pk_bf16_f32 v150, v71, v65\n"
        "v_mfma_f32_16x16x32_bf16 v[102:105], v[222:225], v[94:97], v[102:105]\n"
        "v_cvt_pk_bf16_f32 v154, v72, v66\n"
        "v_cvt_pk_bf16_f32 v202, v73, v67\n"
        "v_mfma_f32_16x16x32_bf16 v[64:67], v[222:225], v[98:101], 0\n"
        "ds_read_b128 v[188:191], v20 offset:512\n"
        "v_mfma_f32_16x16x32_f16 v[64:67], v[124:127], v[144:147], v[64:67]\n"
        "v_mfma_f32_16x16x32_f16 v[64:67], v[120:123], v[140:143], v[64:67]\n"
        "s_add_u32 s91, s89, 294336\n"
        "ds_read_b128 v[192:195], v20 offset:528\n"
        "v_mfma_f32_16x16x32_f16 v[70:73], v[120:123], v[156:159], 0\n"
        "buffer_store_dwordx3 v[106:108], v14, s[40:43], s90 offen offset:384 sc1 nt\n"
        "s_nop 1\n"
        "v_mfma_f32_16x16x32_f16 v[70:73], v[124:127], v[160:163], v[70:73]\n"
        "s_nop 0\n"
        "v_cvt_pk_bf16_f32 v204, v102, v64\n"
        "v_cvt_pk_bf16_f32 v208, v103, v65\n"
        "v_mfma_f32_16x16x32_bf16 v[70:73], v[226:229], v[94:97], v[70:73]\n"
        "buffer_store_dwordx3 v[148:150], v17, s[40:43], s90 offen offset:1152 sc1 nt\n"
        "v_cvt_pk_bf16_f32 v212, v104, v66\n"
        "v_cvt_pk_bf16_f32 v216, v105, v67\n"
        "v_mfma_f32_16x16x32_bf16 v[64:67], v[226:229], v[98:101], 0\n"
        "s_add_u32 s90, s89, 392448\n"
        "v_mfma_f32_16x16x32_f16 v[64:67], v[124:127], v[168:171], v[64:67]\n"
        "buffer_store_dwordx3 v[152:154], v14, s[40:43], s91 offen offset:576 sc1 nt\n"
        "v_mfma_f32_16x16x32_f16 v[64:67], v[120:123], v[164:167], v[64:67]\n"
        "s_nop 0\n"
        "v_mfma_f32_16x16x32_f16 v[196:199], v[120:123], v[172:175], 0\n"
        "buffer_store_dwordx3 v[200:202], v14, s[40:43], s90 offen sc1 nt\n"
        "v_lshlrev_b32 v148, 16, v136\n"
        "s_nop 1\n"
        "v_mfma_f32_16x16x32_f16 v[196:199], v[124:127], v[176:179], v[196:199]\n"
        "s_waitcnt vmcnt(8)\n"
        "v_cvt_pk_bf16_f32 v205, v70, v64\n"
        "v_cvt_pk_bf16_f32 v209, v71, v65\n"
        "v_mfma_f32_16x16x32_bf16 v[196:199], v[230:233], v[94:97], v[196:199]\n"
        "s_waitcnt lgkmcnt(0)\n"
        "ds_read_b128 v[104:107], v9\n"
        "v_lshlrev_b32 v64, 16, v146\n"
        "v_mfma_f32_16x16x32_bf16 v[200:203], v[230:233], v[98:101], 0\n"
        "ds_read_b128 v[110:113], v10\n"
        "v_cvt_pk_bf16_f32 v213, v72, v66\n"
        "v_mfma_f32_16x16x32_f16 v[200:203], v[124:127], v[184:187], v[200:203]\n"
        "ds_read_b128 v[116:119], v11\n"
        "v_lshlrev_b32 v70, 16, v162\n"
        "v_mfma_f32_16x16x32_f16 v[200:203], v[120:123], v[180:183], v[200:203]\n"
        "ds_read_b128 v[124:127], v9 offset:3072\n"
        "v_cvt_pk_bf16_f32 v217, v73, v67\n"
        "v_mfma_f32_16x16x32_f16 v[148:151], v[188:191], v[130:133], v[148:151]\n"
        "ds_read_b128 v[130:133], v10 offset:3072\n"
        "v_lshlrev_b32 v120, 16, v170\n"
        "v_mfma_f32_16x16x32_f16 v[148:151], v[192:195], v[134:137], v[148:151]\n"
        "s_nop 0\n"
        "ds_read_b128 v[136:139], v11 offset:3072\n"
        "v_cvt_pk_bf16_f32 v206, v196, v200\n"
        "v_mfma_f32_16x16x32_f16 v[64:67], v[192:195], v[144:147], v[64:67]\n"
        "ds_read_b128 v[144:147], v9 offset:6144\n"
        "v_lshlrev_b32 v196, 16, v178\n"
        "v_mfma_f32_16x16x32_f16 v[64:67], v[188:191], v[140:143], v[64:67]\n"
        "s_add_u32 s91, s89, 490560\n"
        "ds_read_b128 v[150:153], v10 offset:6144\n"
        "v_cvt_pk_bf16_f32 v210, v197, v201\n"
        "v_mfma_f32_16x16x32_f16 v[70:73], v[188:191], v[156:159], v[70:73]\n"
        "ds_read_b128 v[156:159], v11 offset:6144\n"
        "v_lshlrev_b32 v140, 16, v186\n"
        "v_mfma_f32_16x16x32_f16 v[70:73], v[192:195], v[160:163], v[70:73]\n"
        "v_cvt_pk_bf16_f32 v214, v198, v202\n"
        "v_cvt_pk_bf16_f32 v218, v199, v203\n"
        "v_mfma_f32_16x16x32_f16 v[120:123], v[192:195], v[168:171], v[120:123]\n"
        "buffer_store_dwordx3 v[204:206], v15, s[40:43], s90 offen offset:768 sc1 nt\n"
        "v_cvt_pk_bf16_f32 v160, v148, v64\n"
        "v_mfma_f32_16x16x32_f16 v[120:123], v[188:191], v[164:167], v[120:123]\n"
        "s_add_u32 s90, s89, 588672\n"
        "v_mfma_f32_16x16x32_f16 v[196:199], v[188:191], v[172:175], v[196:199]\n"
        "buffer_store_dwordx3 v[208:210], v14, s[40:43], s91 offen offset:192 sc1 nt\n"
        "s_nop 2\n"
        "v_mfma_f32_16x16x32_f16 v[196:199], v[192:195], v[176:179], v[196:199]\n"
        "s_nop 0\n"
        "v_cvt_pk_bf16_f32 v161, v70, v120\n"
        "v_mfma_f32_16x16x32_f16 v[140:143], v[192:195], v[184:187], v[140:143]\n"
        "buffer_store_dwordx3 v[212:214], v16, s[40:43], s91 offen offset:960 sc1 nt\n"
        "v_mfma_f32_16x16x32_f16 v[140:143], v[188:191], v[180:183], v[140:143]\n"
        "s_add_u32 s46, s89, 590016\n"
        "s_nop 1\n"
        "buffer_store_dwordx3 v[216:218], v14, s[40:43], s90 offen offset:384 sc1 nt\n"
        "s_min_u32 s46, s46, s42\n"
        "s_nop 2\n"
        "v_cvt_pk_bf16_f32 v162, v196, v140\n"
        "buffer_store_dwordx3 v[160:162], v17, s[44:47], s90 offen offset:1152 sc1 nt\n"
        "s_cmp_ge_i32 s85, 0\n"
        "s_cbranch_scc1 TOP_%=\n"
        "LAST_%=:\n"
        "s_waitcnt lgkmcnt(0)\n"
        "s_nop 7\n"
        "v_add_u32 v23, 784, v23\n"
        "ds_read_b128 v[64:67], v23\n"
        "ds_read_b128 v[70:73], v23 offset:64\n"
        "ds_read_b128 v[102:105], v23 offset:128\n"
        "v_add_u32 v24, 784, v24\n"
        "ds_read_b32 v76, v24 offset:360\n"
        "s_waitcnt lgkmcnt(0)\n"
        "ds_read_u16 v60, v19 offset:3072\n"
        "s_waitcnt lgkmcnt(0)\n"
        "s_mov_b32 s30, s29\n"
        "v_cmp_ne_u32_e64 s[24:25], s30, v60\n"
        "s_nop 3\n"
        "s_cmp_lg_u64 s[24:25], 0\n"
        "s_cbranch_scc1 SP17_%=\n"
        "BK17_%=:\n"
        "ds_read_b128 v[194:197], v13\n"
        "ds_read_b128 v[198:201], v13 offset:1024\n"
        "ds_read_b128 v[202:205], v13 offset:2048\n"
        "s_waitcnt lgkmcnt(0)\n"
        "v_mfma_f32_16x16x32_bf16 v[106:109], v[64:67], v[194:197], 0\n"
        "s_mov_b32 s26, s89\n"
        "ds_read_b128 v[206:209], v13 offset:3072\n"
        "ds_read_b128 v[210:213], v13 offset:4096\n"
        "v_mfma_f32_16x16x32_bf16 v[106:109], v[70:73], v[198:201], v[106:109]\n"
        "s_add_u32 s89, s86, s51\n"
        "ds_read_b128 v[218:221], v13 offset:5120\n"
        "ds_read_b128 v[222:225], v13 offset:6144\n"
        "v_mfma_f32_16x16x32_bf16 v[106:109], v[102:105], v[202:205], v[106:109]\n"
        "s_add_u32 s29, s29, 1\n"
        "ds_read_b128 v[226:229], v13 offset:7168\n"
        "ds_read_b128 v[230:233], v13 offset:8192\n"
        "s_waitcnt lgkmcnt(3)\n"
        "v_mfma_f32_16x16x32_bf16 v[110:113], v[102:105], v[218:221], 0\n"
        "s_mov_b32 s86, s87\n"
        "ds_read_b128 v[174:177], v20\n"
        "ds_read_b64 v[178:179], v20 offset:16\n"
        "v_mfma_f32_16x16x32_bf16 v[110:113], v[70:73], v[210:213], v[110:113]\n"
        "s_add_u32 s88, s88, 1\n"
        "ds_read_u16 v77, v22\n"
        "ds_read_b64 v[60:61], v21\n"
        "v_mfma_f32_16x16x32_bf16 v[110:113], v[64:67], v[206:209], v[110:113]\n"
        "s_cmp_ge_u32 s88, 73\n"
        "v_cndmask_b32_e64 v76, 0, v76, s[72:73]\n"
        "s_waitcnt lgkmcnt(0)\n"
        "v_mfma_f32_16x16x32_bf16 v[114:117], v[64:67], v[222:225], 0\n"
        "s_cselect_b32 s92, 590016, 0\n"
        "s_nop 0\n"
        "v_mfma_f32_16x16x32_bf16 v[114:117], v[70:73], v[226:229], v[114:117]\n"
        "s_cselect_b32 s88, 0, s88\n"
        "v_cndmask_b32_e64 v108, v106, v110, s[64:65]\n"
        "v_cndmask_b32_e64 v170, v107, v111, s[64:65]\n"
        "v_mfma_f32_16x16x32_bf16 v[114:117], v[102:105], v[230:233], v[114:117]\n"
        "s_max_u32 s92, s92, 1344\n"
        "s_nop 0\n"
        "v_mfma_f32_16x16x32_bf16 v[64:67], a[152:155], v[202:205], 0\n"
        "s_add_u32 s87, s87, s92\n"
        "s_nop 1\n"
        "v_mfma_f32_16x16x32_bf16 v[64:67], a[148:151], v[198:201], v[64:67]\n"
        "s_cmp_gt_i32 s85, 0\n"
        "v_cndmask_b32_e64 v108, v108, v114, s[70:71]\n"
        "v_cndmask_b32_e64 v170, v170, v115, s[70:71]\n"
        "v_mfma_f32_16x16x32_bf16 v[64:67], a[144:147], v[194:197], v[64:67]\n"
        "s_cselect_b32 s94, s87, s84\n"
        "v_cvt_pk_f16_f32 v108, v108, v170\n"
        "v_pk_mul_f16 v108, v108, v77 op_sel_hi:[1,0]\n"
        "v_mfma_f32_16x16x32_bf16 v[70:73], a[156:159], v[194:197], 0\n"
        "s_sub_u32 s85, s85, 1\n"
        "v_exp_f16_sdwa v108, v108 dst_sel:WORD_0 dst_unused:UNUSED_PRESERVE src0_sel:WORD_0\n"
        "v_mfma_f32_16x16x32_bf16 v[70:73], a[160:163], v[198:201], v[70:73]\n"
        "s_nop 0\n"
        "v_cvt_pk_bf16_f32 v112, v64, v65\n"
        "v_cvt_pk_bf16_f32 v113, v66, v67\n"
        "v_mfma_f32_16x16x32_bf16 v[70:73], a[164:167], v[202:205], v[70:73]\n"
        "s_nop 0\n"
        "v_exp_f16_sdwa v108, v108 dst_sel:WORD_1 dst_unused:UNUSED_PRESERVE src0_sel:WORD_1\n"
        "v_mfma_f32_16x16x32_bf16 v[64:67], a[176:179], v[202:205], 0\n"
        "s_nop 0\n"
        "v_cvt_f32_f16 v102, v108\n"
        "v_cndmask_b32_e64 v102, v102, v76, s[68:69]\n"
        "v_mfma_f32_16x16x32_bf16 v[64:67], a[172:175], v[198:201], v[64:67]\n"
        "s_nop 0\n"
        "v_cvt_pk_bf16_f32 v114, v70, v71\n"
        "v_cvt_pk_bf16_f32 v115, v72, v73\n"
        "v_mfma_f32_16x16x32_bf16 v[64:67], a[168:171], v[194:197], v[64:67]\n"
        "s_nop 0\n"
        "v_add_f32_dpp v76, v102, v102 row_ror:8 row_mask:0xf bank_mask:0xf\n"
        "v_and_b32 v170, 0xffff0000, v108\n"
        "v_mfma_f32_16x16x32_bf16 v[70:73], a[180:183], v[194:197], 0\n"
        "s_nop 0\n"
        "v_add_f32_dpp v76, v76, v76 row_ror:4 row_mask:0xf bank_mask:0xf\n"
        "v_mfma_f32_16x16x32_bf16 v[70:73], a[184:187], v[198:201], v[70:73]\n"
        "s_nop 0\n"
        "v_cvt_pk_bf16_f32 v116, v64, v65\n"
        "v_cvt_pk_bf16_f32 v117, v66, v67\n"
        "v_mfma_f32_16x16x32_bf16 v[70:73], a[188:191], v[202:205], v[70:73]\n"
        "s_nop 0\n"
        "v_add_f32_dpp v76, v76, v76 row_ror:2 row_mask:0xf bank_mask:0xf\n"
        "v_mfma_f32_16x16x32_bf16 v[64:67], a[200:203], v[202:205], 0\n"
        "s_nop 0\n"
        "v_add_f32_dpp v76, v76, v76 row_ror:1 row_mask:0xf bank_mask:0xf\n"
        "s_nop 0\n"
        "v_mfma_f32_16x16x32_bf16 v[64:67], a[196:199], v[198:201], v[64:67]\n"
        "s_nop 0\n"
        "v_cvt_pk_bf16_f32 v118, v70, v71\n"
        "v_cvt_pk_bf16_f32 v119, v72, v73\n"
        "v_mfma_f32_16x16x32_bf16 v[64:67], a[192:195], v[194:197], v[64:67]\n"
        "s_nop 0\n"
        "v_add_f32_dpp v76, v76, v76 row_bcast:15 row_mask:0xa bank_mask:0xf\n"
        "v_mfma_f32_16x16x32_bf16 v[70:73], a[204:207], v[194:197], 0\n"
        "s_nop 0\n"
        "v_add_f32_dpp v76, v76, v76 row_bcast:31 row_mask:0xc bank_mask:0xf\n"
        "s_nop 0\n"
        "v_mfma_f32_16x16x32_bf16 v[70:73], a[208:211], v[198:201], v[70:73]\n"
        "s_nop 0\n"
        "v_cvt_pk_bf16_f32 v120, v64, v65\n"
        "v_cvt_pk_bf16_f32 v121, v66, v67\n"
        "v_mfma_f32_16x16x32_bf16 v[70:73], a[212:215], v[202:205], v[70:73]\n"
        "s_nop 0\n"
        "v_readlane_b32 s95, v76, 63\n"
        "s_nop 0\n"
        "v_mfma_f32_16x16x32_bf16 v[64:67], v[202:205], a[224:227], 0\n"
        "s_nop 0\n"
        "v_rcp_f32 v76, s95\n"
        "v_mfma_f32_16x16x32_bf16 v[64:67], v[198:201], a[220:223], v[64:67]\n"
        "s_nop 0\n"
        "v_cvt_pk_bf16_f32 v122, v70, v71\n"
        "v_cvt_pk_bf16_f32 v123, v72, v73\n"
        "v_mfma_f32_16x16x32_bf16 v[64:67], v[194:197], a[216:219], v[64:67]\n"
        "v_mul_f32 v102, v102, v76\n"
        "v_cvt_f16_f32 v102, v102\n"
        "v_mfma_f32_16x16x32_bf16 v[70:73], v[206:209], a[216:219], 0\n"
        "v_mul_f16 v102, v102, v77\n"
        "v_mfma_f32_16x16x32_bf16 v[70:73], v[210:213], a[220:223], v[70:73]\n"
        "ds_write_b16 v22, v102 offset:512\n"
        "v_mfma_f32_16x16x32_bf16 v[70:73], v[218:221], a[224:227], v[70:73]\n"
        "s_nop 0\n"
        "v_cvt_pk_f16_f32 v102, v64, v65\n"
        "v_cvt_pk_f16_f32 v103, v66, v67\n"
        "v_mfma_f32_16x16x32_bf16 v[64:67], v[230:233], a[224:227], 0\n"
        "s_nop 1\n"
        "v_mfma_f32_16x16x32_bf16 v[64:67], v[226:229], a[220:223], v[64:67]\n"
        "s_nop 0\n"
        "v_cvt_pk_f16_f32 v104, v70, v71\n"
        "v_cvt_pk_f16_f32 v105, v72, v73\n"
        "v_mfma_f32_16x16x32_bf16 v[64:67], v[222:225], a[216:219], v[64:67]\n"
        "s_nop 1\n"
        "v_mfma_f32_16x16x32_bf16 v[70:73], v[194:197], v[112:115], 0\n"
        "s_nop 2\n"
        "v_mfma_f32_16x16x32_bf16 v[70:73], v[198:201], v[116:119], v[70:73]\n"
        "s_nop 0\n"
        "v_cvt_pk_f16_f32 v106, v64, v65\n"
        "v_cvt_pk_f16_f32 v107, v66, v67\n"
        "v_mfma_f32_16x16x32_bf16 v[70:73], v[202:205], v[120:123], v[70:73]\n"
        "s_nop 1\n"
        "v_mfma_f32_16x16x32_bf16 v[64:67], v[218:221], v[120:123], 0\n"
        "s_nop 2\n"
        "v_mfma_f32_16x16x32_bf16 v[64:67], v[210:213], v[116:119], v[64:67]\n"
        "s_nop 0\n"
        "v_cvt_pk_f16_f32 v110, v70, v71\n"
        "v_cvt_pk_f16_f32 v111, v72, v73\n"
        "v_mfma_f32_16x16x32_bf16 v[64:67], v[206:209], v[112:115], v[64:67]\n"
        "v_pk_mul_f16 v110, v110, v60 op_sel_hi:[1,0]\n"
        "v_pk_mul_f16 v111, v111, v60 op_sel_hi:[1,0]\n"
        "v_mfma_f32_16x16x32_bf16 v[70:73], v[222:225], v[112:115], 0\n"
        "v_pk_mul_f16 v110, v110, v174\n"
        "v_pk_mul_f16 v111, v111, v175\n"
        "v_mfma_f32_16x16x32_bf16 v[70:73], v[226:229], v[116:119], v[70:73]\n"
        "v_exp_f16_sdwa v110, v110 dst_sel:WORD_0 dst_unused:UNUSED_PRESERVE src0_sel:WORD_0\n"
        "v_mfma_f32_16x16x32_bf16 v[70:73], v[230:233], v[120:123], v[70:73]\n"
        "v_exp_f16_sdwa v111, v111 dst_sel:WORD_0 dst_unused:UNUSED_PRESERVE src0_sel:WORD_0\n"
        "v_mfma_f32_16x16x32_bf16 v[114:117], a[152:155], v[218:221], 0\n"
        "v_exp_f16_sdwa v110, v110 dst_sel:WORD_1 dst_unused:UNUSED_PRESERVE src0_sel:WORD_1\n"
        "v_mfma_f32_16x16x32_bf16 v[114:117], a[148:151], v[210:213], v[114:117]\n"
        "v_exp_f16_sdwa v111, v111 dst_sel:WORD_1 dst_unused:UNUSED_PRESERVE src0_sel:WORD_1\n"
        "v_mfma_f32_16x16x32_bf16 v[114:117], a[144:147], v[206:209], v[114:117]\n"
        "v_cvt_pk_f16_f32 v112, v64, v65\n"
        "v_cvt_pk_f16_f32 v113, v66, v67\n"
        "v_mfma_f32_16x16x32_bf16 v[64:67], a[156:159], v[206:209], 0\n"
        "v_pk_mul_f16 v112, v112, v60 op_sel_hi:[1,0]\n"
        "v_pk_mul_f16 v113, v113, v60 op_sel_hi:[1,0]\n"
        "v_mfma_f32_16x16x32_bf16 v[64:67], a[160:163], v[210:213], v[64:67]\n"
        "v_pk_mul_f16 v112, v112, v176\n"
        "v_pk_mul_f16 v113, v113, v177\n"
        "v_mfma_f32_16x16x32_bf16 v[64:67], a[164:167], v[218:221], v[64:67]\n"
        "s_nop 0\n"
        "v_cvt_pk_bf16_f32 v128, v114, v115\n"
        "v_cvt_pk_bf16_f32 v129, v116, v117\n"
        "v_mfma_f32_16x16x32_bf16 v[114:117], a[176:179], v[218:221], 0\n"
        "s_nop 0\n"
        "v_exp_f16_sdwa v112, v112 dst_sel:WORD_0 dst_unused:UNUSED_PRESERVE src0_sel:WORD_0\n"
        "v_mfma_f32_16x16x32_bf16 v[114:117], a[172:175], v[210:213], v[114:117]\n"
        "s_nop 0\n"
        "v_cvt_pk_bf16_f32 v130, v64, v65\n"
        "v_cvt_pk_bf16_f32 v131, v66, v67\n"
        "v_mfma_f32_16x16x32_bf16 v[114:117], a[168:171], v[206:209], v[114:117]\n"
        "v_exp_f16_sdwa v113, v113 dst_sel:WORD_0 dst_unused:UNUSED_PRESERVE src0_sel:WORD_0\n"
        "v_mfma_f32_16x16x32_bf16 v[64:67], a[180:183], v[206:209], 0\n"
        "v_exp_f16_sdwa v112, v112 dst_sel:WORD_1 dst_unused:UNUSED_PRESERVE src0_sel:WORD_1\n"
        "v_mfma_f32_16x16x32_bf16 v[64:67], a[184:187], v[210:213], v[64:67]\n"
        "s_nop 0\n"
        "v_exp_f16_sdwa v113, v113 dst_sel:WORD_1 dst_unused:UNUSED_PRESERVE src0_sel:WORD_1\n"
        "v_mfma_f32_16x16x32_bf16 v[64:67], a[188:191], v[218:221], v[64:67]\n"
        "s_nop 0\n"
        "v_cvt_pk_bf16_f32 v132, v114, v115\n"
        "v_cvt_pk_bf16_f32 v133, v116, v117\n"
        "v_mfma_f32_16x16x32_bf16 v[116:119], a[200:203], v[218:221], 0\n"
        "v_cvt_pk_f16_f32 v114, v70, v71\n"
        "v_cvt_pk_f16_f32 v115, v72, v73\n"
        "v_mfma_f32_16x16x32_bf16 v[116:119], a[196:199], v[210:213], v[116:119]\n"
        "s_nop 0\n"
        "v_cvt_pk_bf16_f32 v134, v64, v65\n"
        "v_cvt_pk_bf16_f32 v135, v66, v67\n"
        "v_mfma_f32_16x16x32_bf16 v[116:119], a[192:195], v[206:209], v[116:119]\n"
        "v_pk_mul_f16 v114, v114, v60 op_sel_hi:[1,0]\n"
        "v_pk_mul_f16 v115, v115, v60 op_sel_hi:[1,0]\n"
        "v_mfma_f32_16x16x32_bf16 v[64:67], a[204:207], v[206:209], 0\n"
        "s_nop 0\n"
        "v_pk_mul_f16 v114, v114, v178\n"
        "v_pk_mul_f16 v115, v115, v179\n"
        "v_mfma_f32_16x16x32_bf16 v[64:67], a[208:211], v[210:213], v[64:67]\n"
        "s_nop 0\n"
        "v_cvt_pk_bf16_f32 v70, v116, v117\n"
        "v_cvt_pk_bf16_f32 v71, v118, v119\n"
        "v_mfma_f32_16x16x32_bf16 v[64:67], a[212:215], v[218:221], v[64:67]\n"
        "v_exp_f16_sdwa v114, v114 dst_sel:WORD_0 dst_unused:UNUSED_PRESERVE src0_sel:WORD_0\n"
        "v_mfma_f32_16x16x32_bf16 v[116:119], v[230:233], a[236:239], 0\n"
        "v_exp_f16_sdwa v115, v115 dst_sel:WORD_0 dst_unused:UNUSED_PRESERVE src0_sel:WORD_0\n"
        "v_mfma_f32_16x16x32_bf16 v[116:119], v[226:229], a[232:235], v[116:119]\n"
        "s_nop 0\n"
        "v_exp_f16_sdwa v114, v114 dst_sel:WORD_1 dst_unused:UNUSED_PRESERVE src0_sel:WORD_1\n"
        "v_mfma_f32_16x16x32_bf16 v[116:119], v[222:225], a[228:231], v[116:119]\n"
        "s_nop 0\n"
        "v_cvt_pk_bf16_f32 v72, v64, v65\n"
        "v_cvt_pk_bf16_f32 v73, v66, v67\n"
        "v_mfma_f32_16x16x32_bf16 v[64:67], v[206:209], a[228:231], 0\n"
        "s_nop 0\n"
        "v_exp_f16_sdwa v115, v115 dst_sel:WORD_1 dst_unused:UNUSED_PRESERVE src0_sel:WORD_1\n"
        "v_mfma_f32_16x16x32_bf16 v[64:67], v[210:213], a[232:235], v[64:67]\n"
        "s_nop 0\n"
        "v_cvt_pk_f16_f32 v122, v116, v117\n"
        "v_cvt_pk_f16_f32 v123, v118, v119\n"
        "v_mfma_f32_16x16x32_bf16 v[64:67], v[218:221], a[236:239], v[64:67]\n"
        "s_nop 1\n"
        "v_mfma_f32_16x16x32_bf16 v[124:127], v[202:205], a[236:239], 0\n"
        "s_nop 2\n"
        "v_mfma_f32_16x16x32_bf16 v[124:127], v[198:201], a[232:235], v[124:127]\n"
        "s_nop 0\n"
        "v_cvt_pk_f16_f32 v120, v64, v65\n"
        "v_cvt_pk_f16_f32 v121, v66, v67\n"
        "v_mfma_f32_16x16x32_bf16 v[124:127], v[194:197], a[228:231], v[124:127]\n"
        "s_nop 1\n"
        "v_mfma_f32_16x16x32_bf16 v[64:67], v[194:197], v[128:131], 0\n"
        "s_nop 2\n"
        "v_mfma_f32_16x16x32_bf16 v[64:67], v[198:201], v[132:135], v[64:67]\n"
        "s_nop 0\n"
        "v_cvt_pk_f16_f32 v118, v124, v125\n"
        "v_cvt_pk_f16_f32 v119, v126, v127\n"
        "v_mfma_f32_16x16x32_bf16 v[64:67], v[202:205], v[70:73], v[64:67]\n"
        "s_nop 1\n"
        "v_mfma_f32_16x16x32_bf16 v[136:139], v[218:221], v[70:73], 0\n"
        "s_nop 2\n"
        "v_mfma_f32_16x16x32_bf16 v[136:139], v[210:213], v[132:135], v[136:139]\n"
        "s_nop 0\n"
        "v_cvt_pk_f16_f32 v126, v64, v65\n"
        "v_cvt_pk_f16_f32 v127, v66, v67\n"
        "v_mfma_f32_16x16x32_bf16 v[136:139], v[206:209], v[128:131], v[136:139]\n"
        "v_mov_b32 v117, 0\n"
        "v_cndmask_b32_e64 v116, 0, v170, s[62:63]\n"
        "v_mfma_f32_16x16x32_bf16 v[64:67], v[222:225], v[128:131], 0\n"
        "v_pk_mul_f16 v126, v126, v60 op_sel:[0,1]\n"
        "v_pk_mul_f16 v127, v127, v60 op_sel:[0,1]\n"
        "v_mfma_f32_16x16x32_bf16 v[64:67], v[226:229], v[132:135], v[64:67]\n"
        "v_pk_mul_f16 v126, v126, v174\n"
        "v_pk_mul_f16 v127, v127, v175\n"
        "v_mfma_f32_16x16x32_bf16 v[64:67], v[230:233], v[70:73], v[64:67]\n"
        "v_exp_f16_sdwa v126, v126 dst_sel:WORD_0 dst_unused:UNUSED_PRESERVE src0_sel:WORD_0\n"
        "v_mfma_f32_16x16x32_bf16 v[70:73], a[152:155], v[230:233], 0\n"
        "v_exp_f16_sdwa v127, v127 dst_sel:WORD_0 dst_unused:UNUSED_PRESERVE src0_sel:WORD_0\n"
        "v_mfma_f32_16x16x32_bf16 v[70:73], a[148:151], v[226:229], v[70:73]\n"
        "v_exp_f16_sdwa v126, v126 dst_sel:WORD_1 dst_unused:UNUSED_PRESERVE src0_sel:WORD_1\n"
        "v_mfma_f32_16x16x32_bf16 v[70:73], a[144:147], v[222:225], v[70:73]\n"
        "v_exp_f16_sdwa v127, v127 dst_sel:WORD_1 dst_unused:UNUSED_PRESERVE src0_sel:WORD_1\n"
        "v_mfma_f32_16x16x32_bf16 v[132:135], a[156:159], v[222:225], 0\n"
        "v_cvt_pk_f16_f32 v128, v136, v137\n"
        "v_cvt_pk_f16_f32 v129, v138, v139\n"
        "v_mfma_f32_16x16x32_bf16 v[132:135], a[160:163], v[226:229], v[132:135]\n"
        "v_pk_mul_f16 v128, v128, v60 op_sel:[0,1]\n"
        "v_pk_mul_f16 v129, v129, v60 op_sel:[0,1]\n"
        "v_mfma_f32_16x16x32_bf16 v[132:135], a[164:167], v[230:233], v[132:135]\n"
        "v_pk_mul_f16 v128, v128, v176\n"
        "v_pk_mul_f16 v129, v129, v177\n"
        "v_mfma_f32_16x16x32_bf16 v[136:139], a[176:179], v[230:233], 0\n"
        "v_exp_f16_sdwa v128, v128 dst_sel:WORD_0 dst_unused:UNUSED_PRESERVE src0_sel:WORD_0\n"
        "v_mfma_f32_16x16x32_bf16 v[136:139], a[172:175], v[226:229], v[136:139]\n"
        "v_exp_f16_sdwa v129, v129 dst_sel:WORD_0 dst_unused:UNUSED_PRESERVE src0_sel:WORD_0\n"
        "v_mfma_f32_16x16x32_bf16 v[136:139], a[168:171], v[222:225], v[136:139]\n"
        "v_exp_f16_sdwa v128, v128 dst_sel:WORD_1 dst_unused:UNUSED_PRESERVE src0_sel:WORD_1\n"
        "v_mfma_f32_16x16x32_bf16 v[140:143], a[180:183], v[222:225], 0\n"
        "v_exp_f16_sdwa v129, v129 dst_sel:WORD_1 dst_unused:UNUSED_PRESERVE src0_sel:WORD_1\n"
        "v_mfma_f32_16x16x32_bf16 v[140:143], a[184:187], v[226:229], v[140:143]\n"
        "v_cvt_pk_f16_f32 v130, v64, v65\n"
        "v_cvt_pk_f16_f32 v131, v66, v67\n"
        "v_mfma_f32_16x16x32_bf16 v[140:143], a[188:191], v[230:233], v[140:143]\n"
        "v_pk_mul_f16 v130, v130, v60 op_sel:[0,1]\n"
        "v_pk_mul_f16 v131, v131, v60 op_sel:[0,1]\n"
        "v_mfma_f32_16x16x32_bf16 v[64:67], a[200:203], v[230:233], 0\n"
        "v_pk_mul_f16 v130, v130, v178\n"
        "v_pk_mul_f16 v131, v131, v179\n"
        "v_mfma_f32_16x16x32_bf16 v[64:67], a[196:199], v[226:229], v[64:67]\n"
        "v_exp_f16_sdwa v130, v130 dst_sel:WORD_0 dst_unused:UNUSED_PRESERVE src0_sel:WORD_0\n"
        "v_mfma_f32_16x16x32_bf16 v[64:67], a[192:195], v[222:225], v[64:67]\n"
        "v_cvt_pk_bf16_f32 v144, v70, v71\n"
        "v_cvt_pk_bf16_f32 v145, v72, v73\n"
        "v_mfma_f32_16x16x32_bf16 v[70:73], a[204:207], v[222:225], 0\n"
        "v_cvt_pk_bf16_f32 v146, v132, v133\n"
        "v_cvt_pk_bf16_f32 v147, v134, v135\n"
        "v_mfma_f32_16x16x32_bf16 v[70:73], a[208:211], v[226:229], v[70:73]\n"
        "v_cvt_pk_bf16_f32 v148, v136, v137\n"
        "v_cvt_pk_bf16_f32 v149, v138, v139\n"
        "v_mfma_f32_16x16x32_bf16 v[70:73], a[212:215], v[230:233], v[70:73]\n"
        "v_cvt_pk_bf16_f32 v150, v140, v141\n"
        "v_cvt_pk_bf16_f32 v151, v142, v143\n"
        "v_mfma_f32_16x16x32_f16 v[132:135], v[90:93], v[110:113], 0\n"
        "s_nop 0\n"
        "v_cvt_pk_bf16_f32 v136, v64, v65\n"
        "v_cvt_pk_bf16_f32 v137, v66, v67\n"
        "v_mfma_f32_16x16x32_f16 v[132:135], v[90:93], v[114:117], v[132:135]\n"
        "s_nop 0\n"
        "v_cvt_pk_bf16_f32 v138, v70, v71\n"
        "v_cvt_pk_bf16_f32 v139, v72, v73\n"
        "v_mfma_f32_16x16x32_bf16 v[64:67], v[202:205], a[248:251], 0\n"
        "v_exp_f16_sdwa v131, v131 dst_sel:WORD_0 dst_unused:UNUSED_PRESERVE src0_sel:WORD_0\n"
        "v_mfma_f32_16x16x32_bf16 v[64:67], v[198:201], a[244:247], v[64:67]\n"
        "v_exp_f16_sdwa v130, v130 dst_sel:WORD_1 dst_unused:UNUSED_PRESERVE src0_sel:WORD_1\n"
        "v_mfma_f32_16x16x32_bf16 v[64:67], v[194:197], a[240:243], v[64:67]\n"
        "v_exp_f16_sdwa v131, v131 dst_sel:WORD_1 dst_unused:UNUSED_PRESERVE src0_sel:WORD_1\n"
        "v_mfma_f32_16x16x32_bf16 v[70:73], v[206:209], a[240:243], 0\n"
        "v_rcp_f32 v60, v132\n"
        "v_mfma_f32_16x16x32_bf16 v[70:73], v[210:213], a[244:247], v[70:73]\n"
        "v_max_f32 v77, s95, v132\n"
        "v_cvt_f16_f32 v108, v60\n"
        "v_mfma_f32_16x16x32_bf16 v[70:73], v[218:221], a[248:251], v[70:73]\n"
        "v_mov_b32 v133, 0\n"
        "v_cndmask_b32_e64 v132, 0, v170, s[64:65]\n"
        "v_mfma_f32_16x16x32_bf16 v[152:155], v[230:233], a[248:251], 0\n"
        "v_add_f32 v171, v76, v60\n"
        "v_pk_mul_f16 v110, v110, v108 op_sel_hi:[1,0]\n"
        "v_mfma_f32_16x16x32_bf16 v[152:155], v[226:229], a[244:247], v[152:155]\n"
        "v_pk_mul_f16 v111, v111, v108 op_sel_hi:[1,0]\n"
        "v_pk_mul_f16 v112, v112, v108 op_sel_hi:[1,0]\n"
        "v_mfma_f32_16x16x32_bf16 v[152:155], v[222:225], a[240:243], v[152:155]\n"
        "v_pk_mul_f16 v113, v113, v108 op_sel_hi:[1,0]\n"
        "v_pk_mul_f16 v114, v114, v108 op_sel_hi:[1,0]\n"
        "v_mfma_f32_16x16x32_bf16 v[140:143], v[194:197], v[144:147], 0\n"
        "v_pk_mul_f16 v115, v115, v108 op_sel_hi:[1,0]\n"
        "v_pk_mul_f16 v116, v116, v108 op_sel_hi:[1,0]\n"
        "v_mfma_f32_16x16x32_bf16 v[140:143], v[198:201], v[148:151], v[140:143]\n"
        "v_pk_mul_f16 v110, v110, v174\n"
        "v_pk_mul_f16 v111, v111, v175\n"
        "v_mfma_f32_16x16x32_bf16 v[140:143], v[202:205], v[136:139], v[140:143]\n"
        "v_pk_mul_f16 v112, v112, v176\n"
        "v_pk_mul_f16 v113, v113, v177\n"
        "v_mfma_f32_16x16x32_bf16 v[156:159], v[218:221], v[136:139], 0\n"
        "s_nop 0\n"
        "v_pk_mul_f16 v114, v114, v178\n"
        "v_pk_mul_f16 v115, v115, v179\n"
        "v_mfma_f32_16x16x32_bf16 v[156:159], v[210:213], v[148:151], v[156:159]\n"
        "s_nop 0\n"
        "v_cvt_pk_f16_f32 v134, v140, v141\n"
        "v_cvt_pk_f16_f32 v135, v142, v143\n"
        "v_mfma_f32_16x16x32_bf16 v[156:159], v[206:209], v[144:147], v[156:159]\n"
        "v_pk_mul_f16 v134, v134, v61 op_sel_hi:[1,0]\n"
        "v_pk_mul_f16 v135, v135, v61 op_sel_hi:[1,0]\n"
        "v_mfma_f32_16x16x32_bf16 v[160:163], v[222:225], v[144:147], 0\n"
        "v_pk_mul_f16 v134, v134, v174\n"
        "v_pk_mul_f16 v135, v135, v175\n"
        "v_mfma_f32_16x16x32_bf16 v[160:163], v[226:229], v[148:151], v[160:163]\n"
        "v_exp_f16_sdwa v134, v134 dst_sel:WORD_0 dst_unused:UNUSED_PRESERVE src0_sel:WORD_0\n"
        "v_mfma_f32_16x16x32_bf16 v[160:163], v[230:233], v[136:139], v[160:163]\n"
        "v_exp_f16_sdwa v135, v135 dst_sel:WORD_0 dst_unused:UNUSED_PRESERVE src0_sel:WORD_0\n"
        "v_mfma_f32_16x16x32_bf16 v[166:169], v[230:233], v[32:35], 0\n"
        "v_exp_f16_sdwa v134, v134 dst_sel:WORD_1 dst_unused:UNUSED_PRESERVE src0_sel:WORD_1\n"
        "v_mfma_f32_16x16x32_bf16 v[166:169], v[226:229], v[28:31], v[166:169]\n"
        "v_exp_f16_sdwa v135, v135 dst_sel:WORD_1 dst_unused:UNUSED_PRESERVE src0_sel:WORD_1\n"
        "v_mfma_f32_16x16x32_bf16 v[166:169], v[222:225], a[252:255], v[166:169]\n"
        "v_cvt_pk_f16_f32 v136, v156, v157\n"
        "v_cvt_pk_f16_f32 v137, v158, v159\n"
        "v_mfma_f32_16x16x32_bf16 v[180:183], v[206:209], a[252:255], 0\n"
        "v_pk_mul_f16 v136, v136, v61 op_sel_hi:[1,0]\n"
        "v_pk_mul_f16 v137, v137, v61 op_sel_hi:[1,0]\n"
        "v_mfma_f32_16x16x32_bf16 v[180:183], v[210:213], v[28:31], v[180:183]\n"
        "v_pk_mul_f16 v136, v136, v176\n"
        "v_pk_mul_f16 v137, v137, v177\n"
        "v_mfma_f32_16x16x32_bf16 v[180:183], v[218:221], v[32:35], v[180:183]\n"
        "v_exp_f16_sdwa v136, v136 dst_sel:WORD_0 dst_unused:UNUSED_PRESERVE src0_sel:WORD_0\n"
        "v_mfma_f32_16x16x32_bf16 v[184:187], v[202:205], v[32:35], 0\n"
        "v_exp_f16_sdwa v137, v137 dst_sel:WORD_0 dst_unused:UNUSED_PRESERVE src0_sel:WORD_0\n"
        "v_mfma_f32_16x16x32_bf16 v[184:187], v[198:201], v[28:31], v[184:187]\n"
        "ds_read2_b32 v[108:109], v24 offset0:0 offset1:0\n"
        "v_mov_b32 v141, 0\n"
        "v_mfma_f32_16x16x32_bf16 v[184:187], v[194:197], a[252:255], v[184:187]\n"
        "ds_read2_b32 v[124:125], v24 offset0:1 offset1:1\n"
        "ds_read2_b32 v[148:149], v24 offset0:2 offset1:2\n"
        "v_mfma_f32_16x16x32_f16 v[142:145], v[90:93], v[126:129], 0\n"
        "ds_read2_b32 v[156:157], v24 offset0:3 offset1:3\n"
        "ds_read2_b32 v[164:165], v24 offset0:4 offset1:4\n"
        "v_mfma_f32_16x16x32_f16 v[142:145], v[90:93], v[130:133], v[142:145]\n"
        "ds_read2_b32 v[172:173], v24 offset0:5 offset1:5\n"
        "v_cndmask_b32_e64 v140, 0, v170, s[66:67]\n"
        "v_mfma_f32_16x16x32_bf16 v[188:191], v[194:197], v[36:39], 0\n"
        "v_exp_f16_sdwa v136, v136 dst_sel:WORD_1 dst_unused:UNUSED_PRESERVE src0_sel:WORD_1\n"
        "v_mfma_f32_16x16x32_bf16 v[188:191], v[198:201], v[40:43], v[188:191]\n"
        "v_exp_f16_sdwa v137, v137 dst_sel:WORD_1 dst_unused:UNUSED_PRESERVE src0_sel:WORD_1\n"
        "v_mfma_f32_16x16x32_bf16 v[188:191], v[202:205], v[44:47], v[188:191]\n"
        "v_cvt_pk_f16_f32 v138, v160, v161\n"
        "v_cvt_pk_f16_f32 v139, v162, v163\n"
        "v_mfma_f32_16x16x32_bf16 v[214:217], v[218:221], v[44:47], 0\n"
        "v_pk_mul_f16 v138, v138, v61 op_sel_hi:[1,0]\n"
        "v_pk_mul_f16 v139, v139, v61 op_sel_hi:[1,0]\n"
        "v_mfma_f32_16x16x32_bf16 v[214:217], v[210:213], v[40:43], v[214:217]\n"
        "v_pk_mul_f16 v138, v138, v178\n"
        "v_pk_mul_f16 v139, v139, v179\n"
        "v_mfma_f32_16x16x32_bf16 v[214:217], v[206:209], v[36:39], v[214:217]\n"
        "v_exp_f16_sdwa v138, v138 dst_sel:WORD_0 dst_unused:UNUSED_PRESERVE src0_sel:WORD_0\n"
        "v_mfma_f32_16x16x32_bf16 v[234:237], v[222:225], v[36:39], 0\n"
        "v_exp_f16_sdwa v139, v139 dst_sel:WORD_0 dst_unused:UNUSED_PRESERVE src0_sel:WORD_0\n"
        "v_mfma_f32_16x16x32_bf16 v[234:237], v[226:229], v[40:43], v[234:237]\n"
        "s_waitcnt lgkmcnt(0)\n"
        "v_exp_f16_sdwa v138, v138 dst_sel:WORD_1 dst_unused:UNUSED_PRESERVE src0_sel:WORD_1\n"
        "v_mfma_f32_16x16x32_bf16 v[234:237], v[230:233], v[44:47], v[234:237]\n"
        "v_rcp_f32 v60, v142\n"
        "v_mfma_f32_16x16x32_bf16 v[238:241], v[230:233], v[56:59], 0\n"
        "v_max_f32 v77, v77, v142\n"
        "v_add_f32 v171, v171, v60\n"
        "v_mfma_f32_16x16x32_bf16 v[238:241], v[226:229], v[52:55], v[238:241]\n"
        "v_exp_f16_sdwa v139, v139 dst_sel:WORD_1 dst_unused:UNUSED_PRESERVE src0_sel:WORD_1\n"
        "v_mfma_f32_16x16x32_bf16 v[238:241], v[222:225], v[48:51], v[238:241]\n"
        "v_cvt_pk_f16_f32 v142, v64, v65\n"
        "v_cvt_pk_f16_f32 v143, v66, v67\n"
        "v_mfma_f32_16x16x32_bf16 v[64:67], v[206:209], v[48:51], 0\n"
        "v_cvt_pk_f16_f32 v144, v70, v71\n"
        "v_cvt_pk_f16_f32 v145, v72, v73\n"
        "v_mfma_f32_16x16x32_bf16 v[64:67], v[210:213], v[52:55], v[64:67]\n"
        "v_cvt_pk_f16_f32 v146, v152, v153\n"
        "v_cvt_pk_f16_f32 v147, v154, v155\n"
        "v_mfma_f32_16x16x32_bf16 v[64:67], v[218:221], v[56:59], v[64:67]\n"
        "v_cvt_pk_f16_f32 v154, v166, v167\n"
        "v_cvt_pk_f16_f32 v155, v168, v169\n"
        "v_mfma_f32_16x16x32_bf16 v[70:73], v[202:205], v[56:59], 0\n"
        "v_cvt_pk_f16_f32 v152, v180, v181\n"
        "v_cvt_pk_f16_f32 v153, v182, v183\n"
        "v_mfma_f32_16x16x32_bf16 v[70:73], v[198:201], v[52:55], v[70:73]\n"
        "v_cvt_pk_f16_f32 v150, v184, v185\n"
        "v_cvt_pk_f16_f32 v151, v186, v187\n"
        "v_mfma_f32_16x16x32_bf16 v[70:73], v[194:197], v[48:51], v[70:73]\n"
        "v_cvt_f16_f32 v61, v60\n"
        "v_pk_mul_f16 v126, v126, v61 op_sel_hi:[1,0]\n"
        "v_mfma_f32_16x16x32_f16 v[158:161], v[90:93], v[134:137], 0\n"
        "v_pk_mul_f16 v127, v127, v61 op_sel_hi:[1,0]\n"
        "v_pk_mul_f16 v128, v128, v61 op_sel_hi:[1,0]\n"
        "v_mfma_f32_16x16x32_f16 v[158:161], v[90:93], v[138:141], v[158:161]\n"
        "s_nop 0\n"
        "v_pk_mul_f16 v129, v129, v61 op_sel_hi:[1,0]\n"
        "v_pk_mul_f16 v130, v130, v61 op_sel_hi:[1,0]\n"
        "s_nop 4\n"
        "v_max_f32 v77, v77, v158\n"
        "v_rcp_f32 v60, v158\n"
        "s_nop 0\n"
        "v_add_f32 v171, v171, v60\n"
        "v_fma_f32 v77, v171, s82, v77\n"
        "v_cmp_nlt_f32_e64 s[74:75], v77, s83\n"
        "s_nop 3\n"
        "s_cmp_lg_u64 s[74:75], 0\n"
        "s_cbranch_scc1 SLOW_%=\n"
        "v_mfma_f32_16x16x32_f16 v[180:183], v[110:113], v[102:105], 0\n"
        "v_pk_mul_f16 v131, v131, v61 op_sel_hi:[1,0]\n"
        "v_pk_mul_f16 v132, v132, v61 op_sel_hi:[1,0]\n"
        "v_mfma_f32_16x16x32_f16 v[180:183], v[114:117], v[106:109], v[180:183]\n"
        "v_cvt_pk_f16_f32 v158, v188, v189\n"
        "v_cvt_pk_f16_f32 v159, v190, v191\n"
        "v_mfma_f32_16x16x32_bf16 v[180:183], v[194:197], v[94:97], v[180:183]\n"
        "v_cvt_pk_f16_f32 v160, v214, v215\n"
        "v_cvt_pk_f16_f32 v161, v216, v217\n"
        "v_mfma_f32_16x16x32_bf16 v[184:187], v[194:197], v[98:101], 0\n"
        "v_cvt_pk_f16_f32 v162, v234, v235\n"
        "v_cvt_pk_f16_f32 v163, v236, v237\n"
        "v_mfma_f32_16x16x32_f16 v[184:187], v[114:117], v[122:125], v[184:187]\n"
        "v_pk_mul_f16 v126, v126, v174\n"
        "v_pk_mul_f16 v127, v127, v175\n"
        "v_mfma_f32_16x16x32_f16 v[184:187], v[110:113], v[118:121], v[184:187]\n"
        "v_cvt_pk_f16_f32 v170, v238, v239\n"
        "v_cvt_pk_f16_f32 v171, v240, v241\n"
        "v_mfma_f32_16x16x32_f16 v[188:191], v[110:113], v[142:145], 0\n"
        "v_cvt_pk_f16_f32 v168, v64, v65\n"
        "v_cvt_pk_f16_f32 v169, v66, v67\n"
        "v_mfma_f32_16x16x32_f16 v[188:191], v[114:117], v[146:149], v[188:191]\n"
        "v_cvt_pk_f16_f32 v166, v70, v71\n"
        "v_cvt_pk_f16_f32 v167, v72, v73\n"
        "v_mfma_f32_16x16x32_bf16 v[188:191], v[198:201], v[94:97], v[188:191]\n"
        "v_pk_mul_f16 v128, v128, v176\n"
        "v_pk_mul_f16 v129, v129, v177\n"
        "v_mfma_f32_16x16x32_bf16 v[64:67], v[198:201], v[98:101], 0\n"
        "v_pk_mul_f16 v130, v130, v178\n"
        "v_pk_mul_f16 v131, v131, v179\n"
        "v_mfma_f32_16x16x32_f16 v[64:67], v[114:117], v[154:157], v[64:67]\n"
        "v_cvt_pk_bf16_f32 v192, v180, v184\n"
        "v_cvt_pk_bf16_f32 v180, v181, v185\n"
        "v_mfma_f32_16x16x32_f16 v[64:67], v[110:113], v[150:153], v[64:67]\n"
        "v_cvt_f16_f32 v61, v60\n"
        "v_cvt_pk_bf16_f32 v184, v182, v186\n"
        "v_mfma_f32_16x16x32_f16 v[70:73], v[110:113], v[158:161], 0\n"
        "s_nop 0\n"
        "v_pk_mul_f16 v134, v134, v61 op_sel_hi:[1,0]\n"
        "v_cvt_pk_bf16_f32 v196, v183, v187\n"
        "v_mfma_f32_16x16x32_f16 v[70:73], v[114:117], v[162:165], v[70:73]\n"
        "s_nop 0\n"
        "v_cvt_pk_bf16_f32 v193, v188, v64\n"
        "v_cvt_pk_bf16_f32 v181, v189, v65\n"
        "v_mfma_f32_16x16x32_bf16 v[70:73], v[202:205], v[94:97], v[70:73]\n"
        "v_cvt_pk_bf16_f32 v185, v190, v66\n"
        "v_cvt_pk_bf16_f32 v197, v191, v67\n"
        "v_mfma_f32_16x16x32_bf16 v[64:67], v[202:205], v[98:101], 0\n"
        "v_pk_mul_f16 v135, v135, v61 op_sel_hi:[1,0]\n"
        "v_pk_mul_f16 v136, v136, v61 op_sel_hi:[1,0]\n"
        "v_mfma_f32_16x16x32_f16 v[64:67], v[114:117], v[170:173], v[64:67]\n"
        "v_pk_mul_f16 v137, v137, v61 op_sel_hi:[1,0]\n"
        "v_pk_mul_f16 v138, v138, v61 op_sel_hi:[1,0]\n"
        "v_mfma_f32_16x16x32_f16 v[64:67], v[110:113], v[166:169], v[64:67]\n"
        "v_pk_mul_f16 v139, v139, v61 op_sel_hi:[1,0]\n"
        "v_pk_mul_f16 v140, v140, v61 op_sel_hi:[1,0]\n"
        "v_mfma_f32_16x16x32_f16 v[110:113], v[126:129], v[102:105], 0\n"
        "s_nop 0\n"
        "v_pk_mul_f16 v134, v134, v174\n"
        "v_pk_mul_f16 v135, v135, v175\n"
        "v_mfma_f32_16x16x32_f16 v[110:113], v[130:133], v[106:109], v[110:113]\n"
        "s_add_u32 s90, s89, 0\n"
        "v_cvt_pk_bf16_f32 v194, v70, v64\n"
        "v_cvt_pk_bf16_f32 v182, v71, v65\n"
        "v_mfma_f32_16x16x32_bf16 v[110:113], v[206:209], v[94:97], v[110:113]\n"
        "v_cvt_pk_bf16_f32 v186, v72, v66\n"
        "v_cvt_pk_bf16_f32 v198, v73, v67\n"
        "v_mfma_f32_16x16x32_bf16 v[64:67], v[206:209], v[98:101], 0\n"
        "v_pk_mul_f16 v136, v136, v176\n"
        "v_pk_mul_f16 v137, v137, v177\n"
        "v_mfma_f32_16x16x32_f16 v[64:67], v[130:133], v[122:125], v[64:67]\n"
        "v_pk_mul_f16 v138, v138, v178\n"
        "v_pk_mul_f16 v139, v139, v179\n"
        "v_mfma_f32_16x16x32_f16 v[64:67], v[126:129], v[118:121], v[64:67]\n"
        "s_add_u32 s91, s89, 98112\n"
        "v_mfma_f32_16x16x32_f16 v[70:73], v[126:129], v[142:145], 0\n"
        "buffer_store_dwordx3 v[192:194], v14, s[40:43], s90 offen sc1 nt\n"
        "s_nop 2\n"
        "v_mfma_f32_16x16x32_f16 v[70:73], v[130:133], v[146:149], v[70:73]\n"
        "s_nop 0\n"
        "v_cvt_pk_bf16_f32 v174, v110, v64\n"
        "v_cvt_pk_bf16_f32 v178, v111, v65\n"
        "v_mfma_f32_16x16x32_bf16 v[70:73], v[210:213], v[94:97], v[70:73]\n"
        "buffer_store_dwordx3 v[180:182], v15, s[40:43], s90 offen offset:768 sc1 nt\n"
        "v_cvt_pk_bf16_f32 v188, v112, v66\n"
        "v_cvt_pk_bf16_f32 v192, v113, v67\n"
        "v_mfma_f32_16x16x32_bf16 v[64:67], v[210:213], v[98:101], 0\n"
        "v_mfma_f32_16x16x32_f16 v[64:67], v[130:133], v[154:157], v[64:67]\n"
        "buffer_store_dwordx3 v[184:186], v14, s[40:43], s91 offen offset:192 sc1 nt\n"
        "v_mfma_f32_16x16x32_f16 v[64:67], v[126:129], v[150:153], v[64:67]\n"
        "s_nop 0\n"
        "v_mfma_f32_16x16x32_f16 v[110:113], v[126:129], v[158:161], 0\n"
        "buffer_store_dwordx3 v[196:198], v16, s[40:43], s91 offen offset:960 sc1 nt\n"
        "s_nop 2\n"
        "v_mfma_f32_16x16x32_f16 v[110:113], v[130:133], v[162:165], v[110:113]\n"
        "s_nop 0\n"
        "v_cvt_pk_bf16_f32 v175, v70, v64\n"
        "v_cvt_pk_bf16_f32 v179, v71, v65\n"
        "v_mfma_f32_16x16x32_bf16 v[110:113], v[218:221], v[94:97], v[110:113]\n"
        "v_cvt_pk_bf16_f32 v189, v72, v66\n"
        "v_cvt_pk_bf16_f32 v193, v73, v67\n"
        "v_mfma_f32_16x16x32_bf16 v[64:67], v[218:221], v[98:101], 0\n"
        "v_mfma_f32_16x16x32_f16 v[64:67], v[130:133], v[170:173], v[64:67]\n"
        "v_mfma_f32_16x16x32_f16 v[64:67], v[126:129], v[166:169], v[64:67]\n"
        "s_nop 1\n"
        "v_mfma_f32_16x16x32_f16 v[70:73], v[134:137], v[102:105], 0\n"
        "s_nop 2\n"
        "v_mfma_f32_16x16x32_f16 v[70:73], v[138:141], v[106:109], v[70:73]\n"
        "s_add_u32 s90, s89, 196224\n"
        "v_cvt_pk_bf16_f32 v176, v110, v64\n"
        "v_cvt_pk_bf16_f32 v180, v111, v65\n"
        "v_mfma_f32_16x16x32_bf16 v[70:73], v[222:225], v[94:97], v[70:73]\n"
        "v_cvt_pk_bf16_f32 v190, v112, v66\n"
        "v_cvt_pk_bf16_f32 v194, v113, v67\n"
        "v_mfma_f32_16x16x32_bf16 v[64:67], v[222:225], v[98:101], 0\n"
        "ds_read_b128 v[110:113], v20 offset:512\n"
        "v_mfma_f32_16x16x32_f16 v[64:67], v[138:141], v[122:125], v[64:67]\n"
        "v_mfma_f32_16x16x32_f16 v[64:67], v[134:137], v[118:121], v[64:67]\n"
        "s_add_u32 s91, s89, 294336\n"
        "ds_read_b128 v[114:117], v20 offset:528\n"
        "v_mfma_f32_16x16x32_f16 v[126:129], v[134:137], v[142:145], 0\n"
        "buffer_store_dwordx3 v[174:176], v14, s[40:43], s90 offen offset:384 sc1 nt\n"
        "s_nop 1\n"
        "v_mfma_f32_16x16x32_f16 v[126:129], v[138:141], v[146:149], v[126:129]\n"
        "s_nop 0\n"
        "v_cvt_pk_bf16_f32 v182, v70, v64\n"
        "v_cvt_pk_bf16_f32 v186, v71, v65\n"
        "v_mfma_f32_16x16x32_bf16 v[126:129], v[226:229], v[94:97], v[126:129]\n"
        "buffer_store_dwordx3 v[178:180], v17, s[40:43], s90 offen offset:1152 sc1 nt\n"
        "v_cvt_pk_bf16_f32 v196, v72, v66\n"
        "v_cvt_pk_bf16_f32 v200, v73, v67\n"
        "v_mfma_f32_16x16x32_bf16 v[64:67], v[226:229], v[98:101], 0\n"
        "s_add_u32 s90, s89, 392448\n"
        "v_mfma_f32_16x16x32_f16 v[64:67], v[138:141], v[154:157], v[64:67]\n"
        "buffer_store_dwordx3 v[188:190], v14, s[40:43], s91 offen offset:576 sc1 nt\n"
        "v_mfma_f32_16x16x32_f16 v[64:67], v[134:137], v[150:153], v[64:67]\n"
        "s_nop 0\n"
        "v_mfma_f32_16x16x32_f16 v[70:73], v[134:137], v[158:161], 0\n"
        "buffer_store_dwordx3 v[192:194], v14, s[40:43], s90 offen sc1 nt\n"
        "v_lshlrev_b32 v130, 16, v108\n"
        "s_nop 1\n"
        "v_mfma_f32_16x16x32_f16 v[70:73], v[138:141], v[162:165], v[70:73]\n"
        "s_waitcnt lgkmcnt(0)\n"
        "v_cvt_pk_bf16_f32 v183, v126, v64\n"
        "v_cvt_pk_bf16_f32 v187, v127, v65\n"
        "v_mfma_f32_16x16x32_bf16 v[70:73], v[230:233], v[94:97], v[70:73]\n"
        "v_lshlrev_b32 v64, 16, v124\n"
        "v_cvt_pk_bf16_f32 v197, v128, v66\n"
        "v_mfma_f32_16x16x32_bf16 v[174:177], v[230:233], v[98:101], 0\n"
        "v_cvt_pk_bf16_f32 v201, v129, v67\n"
        "v_mfma_f32_16x16x32_f16 v[174:177], v[138:141], v[170:173], v[174:177]\n"
        "v_lshlrev_b32 v126, 16, v148\n"
        "v_mfma_f32_16x16x32_f16 v[174:177], v[134:137], v[166:169], v[174:177]\n"
        "s_nop 1\n"
        "v_mfma_f32_16x16x32_f16 v[130:133], v[110:113], v[102:105], v[130:133]\n"
        "s_nop 0\n"
        "v_lshlrev_b32 v102, 16, v156\n"
        "s_nop 0\n"
        "v_mfma_f32_16x16x32_f16 v[130:133], v[114:117], v[106:109], v[130:133]\n"
        "s_nop 0\n"
        "v_cvt_pk_bf16_f32 v184, v70, v174\n"
        "v_cvt_pk_bf16_f32 v188, v71, v175\n"
        "v_mfma_f32_16x16x32_f16 v[64:67], v[114:117], v[122:125], v[64:67]\n"
        "v_lshlrev_b32 v70, 16, v164\n"
        "v_cvt_pk_bf16_f32 v198, v72, v176\n"
        "v_mfma_f32_16x16x32_f16 v[64:67], v[110:113], v[118:121], v[64:67]\n"
        "s_add_u32 s91, s89, 490560\n"
        "v_cvt_pk_bf16_f32 v202, v73, v177\n"
        "v_mfma_f32_16x16x32_f16 v[126:129], v[110:113], v[142:145], v[126:129]\n"
        "s_nop 0\n"
        "v_lshlrev_b32 v106, 16, v172\n"
        "s_nop 0\n"
        "v_mfma_f32_16x16x32_f16 v[126:129], v[114:117], v[146:149], v[126:129]\n"
        "s_nop 0\n"
        "v_cvt_pk_bf16_f32 v118, v130, v64\n"
        "v_mfma_f32_16x16x32_f16 v[102:105], v[114:117], v[154:157], v[102:105]\n"
        "buffer_store_dwordx3 v[182:184], v15, s[40:43], s90 offen offset:768 sc1 nt\n"
        "v_mfma_f32_16x16x32_f16 v[102:105], v[110:113], v[150:153], v[102:105]\n"
        "s_add_u32 s90, s89, 588672\n"
        "v_mfma_f32_16x16x32_f16 v[70:73], v[110:113], v[158:161], v[70:73]\n"
        "buffer_store_dwordx3 v[186:188], v14, s[40:43], s91 offen offset:192 sc1 nt\n"
        "s_nop 2\n"
        "v_mfma_f32_16x16x32_f16 v[70:73], v[114:117], v[162:165], v[70:73]\n"
        "s_nop 0\n"
        "v_cvt_pk_bf16_f32 v119, v126, v102\n"
        "v_mfma_f32_16x16x32_f16 v[106:109], v[114:117], v[170:173], v[106:109]\n"
        "buffer_store_dwordx3 v[196:198], v16, s[40:43], s91 offen offset:960 sc1 nt\n"
        "v_mfma_f32_16x16x32_f16 v[106:109], v[110:113], v[166:169], v[106:109]\n"
        "s_add_u32 s46, s89, 590016\n"
        "s_nop 1\n"
        "buffer_store_dwordx3 v[200:202], v14, s[40:43], s90 offen offset:384 sc1 nt\n"
        "s_min_u32 s46, s46, s42\n"
        "s_nop 2\n"
        "v_cvt_pk_bf16_f32 v120, v70, v106\n"
        "buffer_store_dwordx3 v[118:120], v17, s[44:47], s90 offen offset:1152 sc1 nt\n"
        "FIN_%=:\n"
        ".Lswin_fin:\n"
        "s_endpgm\n"
        "SLOW_%=:\n"
        "s_waitcnt lgkmcnt(0)\n"
        "s_nop 15\n"
        "v_mbcnt_lo_u32_b32 v60, -1, 0\n"
        "v_mbcnt_hi_u32_b32 v60, -1, v60\n"
        "v_and_b32 v114, 15, v60\n"
        "v_mul_u32_u24 v61, 24, v114\n"
        "v_sub_u32 v64, v24, v61\n"
        "v_lshrrev_b32 v115, 4, v60\n"
        "v_mul_u32_u24 v61, 96, v115\n"
        "v_add_u32 v64, v64, v61\n"
        "ds_read_b64 v[60:61], v64\n"
        "ds_read_b64 v[76:77], v64 offset:24\n"
        "ds_read_b64 v[118:119], v64 offset:48\n"
        "ds_read_b64 v[122:123], v64 offset:72\n"
        "ds_read_b64 v[124:125], v64 offset:8\n"
        "ds_read_b64 v[126:127], v64 offset:32\n"
        "ds_read_b64 v[130:131], v64 offset:56\n"
        "ds_read_b64 v[132:133], v64 offset:80\n"
        "ds_read_b64 v[134:135], v64 offset:16\n"
        "ds_read_b64 v[138:139], v64 offset:40\n"
        "ds_read_b64 v[140:141], v64 offset:64\n"
        "ds_read_b64 v[142:143], v64 offset:88\n"
        "v_mfma_f32_16x16x32_bf16 v[64:67], v[194:197], v[194:197], 0\n"
        "v_mov_b32 v120, 0x05040100\n"
        "v_subrev_u32 v116, 158080, v20\n"
        "v_mfma_f32_16x16x32_bf16 v[64:67], v[198:201], v[198:201], v[64:67]\n"
        "v_mov_b32 v128, 161664\n"
        "v_mov_b32 v70, 0x3f803f80\n"
        "v_mfma_f32_16x16x32_bf16 v[64:67], v[202:205], v[202:205], v[64:67]\n"
        "v_mov_b32 v71, 0x3f803f80\n"
        "v_mov_b32 v72, 0x3f803f80\n"
        "v_mov_b32 v73, 0x3f803f80\n"
        "s_nop 1\n"
        "v_mfma_f32_16x16x32_bf16 v[102:105], v[70:73], v[202:205], 0\n"
        "v_mov_b32 v174, 0xff800000\n"
        "v_mov_b32 v121, 0\n"
        "v_mfma_f32_16x16x32_bf16 v[102:105], v[70:73], v[198:201], v[102:105]\n"
        "s_waitcnt lgkmcnt(11)\n"
        "v_mov_b32 v129, 0\n"
        "v_mov_b32 v137, 0\n"
        "v_mfma_f32_16x16x32_bf16 v[102:105], v[70:73], v[194:197], v[102:105]\n"
        "s_waitcnt lgkmcnt(10)\n"
        "v_mov_b32 v145, 0\n"
        "v_mov_b32 v153, 0\n"
        "v_mfma_f32_16x16x32_bf16 v[106:109], v[206:209], v[206:209], 0\n"
        "s_waitcnt lgkmcnt(9)\n"
        "v_mov_b32 v161, 0\n"
        "v_mov_b32 v169, 0\n"
        "v_mfma_f32_16x16x32_bf16 v[106:109], v[210:213], v[210:213], v[106:109]\n"
        "s_waitcnt lgkmcnt(8)\n"
        "v_mov_b32 v171, 0\n"
        "v_mov_b32 v173, 0\n"
        "v_mfma_f32_16x16x32_bf16 v[106:109], v[218:221], v[218:221], v[106:109]\n"
        "s_waitcnt lgkmcnt(7)\n"
        "v_mov_b32 v175, 0\n"
        "v_and_b32 v116, 384, v116\n"
        "v_mfma_f32_16x16x32_bf16 v[110:113], v[70:73], v[218:221], 0\n"
        "s_waitcnt lgkmcnt(6)\n"
        "v_lshlrev_b32 v116, 1, v116\n"
        "v_add_u32 v116, 160640, v116\n"
        "v_mfma_f32_16x16x32_bf16 v[110:113], v[70:73], v[210:213], v[110:113]\n"
        "s_waitcnt lgkmcnt(5)\n"
        "v_lshlrev_b32 v154, 2, v114\n"
        "v_lshlrev_b32 v155, 4, v115\n"
        "v_mfma_f32_16x16x32_bf16 v[110:113], v[70:73], v[206:209], v[110:113]\n"
        "s_waitcnt lgkmcnt(4)\n"
        "v_add_u32 v154, v154, v116\n"
        "v_add_u32 v155, v155, v116\n"
        "v_mfma_f32_16x16x32_bf16 v[114:117], v[222:225], v[222:225], 0\n"
        "s_waitcnt lgkmcnt(3)\n"
        "v_cndmask_b32 v128, v128, v154, vcc\n"
        "v_cndmask_b32_e64 v103, v64, v65, s[56:57]\n"
        "v_mfma_f32_16x16x32_bf16 v[114:117], v[226:229], v[226:229], v[114:117]\n"
        "s_waitcnt lgkmcnt(2)\n"
        "v_cndmask_b32_e64 v103, v103, v66, s[58:59]\n"
        "v_cndmask_b32_e64 v103, v103, v67, s[60:61]\n"
        "v_mfma_f32_16x16x32_bf16 v[114:117], v[230:233], v[230:233], v[114:117]\n"
        "s_waitcnt lgkmcnt(1)\n"
        "v_mul_f32 v103, s80, v103\n"
        "v_mul_f32 v111, s80, v102\n"
        "v_mfma_f32_16x16x32_bf16 v[64:67], v[70:73], v[230:233], 0\n"
        "s_waitcnt lgkmcnt(0)\n"
        "v_fma_f32 v111, -v111, v111, v103\n"
        "v_max_f32 v111, 0, v111\n"
        "v_mfma_f32_16x16x32_bf16 v[64:67], v[70:73], v[226:229], v[64:67]\n"
        "v_add_f32 v111, s81, v111\n"
        "v_perm_b32 v176, v61, v60, v120\n"
        "v_mfma_f32_16x16x32_bf16 v[64:67], v[70:73], v[222:225], v[64:67]\n"
        "v_rsq_f32 v111, v111\n"
        "v_perm_b32 v177, v77, v76, v120\n"
        "v_perm_b32 v178, v119, v118, v120\n"
        "v_perm_b32 v179, v123, v122, v120\n"
        "s_nop 1\n"
        "v_mfma_f32_16x16x32_bf16 v[102:105], v[176:179], v[176:179], 0\n"
        "ds_write_b32 v128, v111\n"
        "v_perm_b32 v180, v125, v124, v120\n"
        "v_perm_b32 v181, v127, v126, v120\n"
        "v_perm_b32 v182, v131, v130, v120\n"
        "v_perm_b32 v183, v133, v132, v120\n"
        "s_nop 1\n"
        "v_mfma_f32_16x16x32_bf16 v[102:105], v[180:183], v[180:183], v[102:105]\n"
        "v_perm_b32 v184, v135, v134, v120\n"
        "v_perm_b32 v185, v139, v138, v120\n"
        "v_perm_b32 v186, v141, v140, v120\n"
        "v_perm_b32 v187, v143, v142, v120\n"
        "s_nop 1\n"
        "v_mfma_f32_16x16x32_bf16 v[102:105], v[184:187], v[184:187], v[102:105]\n"
        "v_cndmask_b32_e64 v60, v106, v107, s[56:57]\n"
        "v_cndmask_b32_e64 v60, v60, v108, s[58:59]\n"
        "v_mfma_f32_16x16x32_bf16 v[122:125], v[70:73], v[184:187], 0\n"
        "v_cndmask_b32_e64 v60, v60, v109, s[60:61]\n"
        "v_mul_f32 v60, s80, v60\n"
        "v_mfma_f32_16x16x32_bf16 v[122:125], v[70:73], v[180:183], v[122:125]\n"
        "v_mul_f32 v61, s80, v110\n"
        "v_fma_f32 v61, -v61, v61, v60\n"
        "v_mfma_f32_16x16x32_bf16 v[122:125], v[70:73], v[176:179], v[122:125]\n"
        "v_max_f32 v61, 0, v61\n"
        "v_add_f32 v61, s81, v61\n"
        "v_mfma_f32_16x16x32_bf16 v[70:73], v[194:197], a[216:219], 0\n"
        "v_rsq_f32 v61, v61\n"
        "v_mfma_f32_16x16x32_bf16 v[70:73], v[198:201], a[220:223], v[70:73]\n"
        "ds_write_b32 v128, v61 offset:64\n"
        "v_mfma_f32_16x16x32_bf16 v[70:73], v[202:205], a[224:227], v[70:73]\n"
        "v_cndmask_b32_e64 v60, v114, v115, s[56:57]\n"
        "v_cndmask_b32_e64 v60, v60, v116, s[58:59]\n"
        "v_mfma_f32_16x16x32_bf16 v[106:109], v[218:221], a[224:227], 0\n"
        "v_cndmask_b32_e64 v60, v60, v117, s[60:61]\n"
        "v_mul_f32 v60, s80, v60\n"
        "v_mfma_f32_16x16x32_bf16 v[106:109], v[210:213], a[220:223], v[106:109]\n"
        "v_mul_f32 v61, s80, v64\n"
        "v_fma_f32 v61, -v61, v61, v60\n"
        "v_mfma_f32_16x16x32_bf16 v[106:109], v[206:209], a[216:219], v[106:109]\n"
        "v_max_f32 v61, 0, v61\n"
        "v_add_f32 v61, s81, v61\n"
        "v_mfma_f32_16x16x32_bf16 v[64:67], v[222:225], a[216:219], 0\n"
        "v_rsq_f32 v61, v61\n"
        "v_mfma_f32_16x16x32_bf16 v[64:67], v[226:229], a[220:223], v[64:67]\n"
        "ds_write_b32 v128, v61 offset:128\n"
        "v_mfma_f32_16x16x32_bf16 v[64:67], v[230:233], a[224:227], v[64:67]\n"
        "v_cndmask_b32_e64 v60, v102, v103, s[56:57]\n"
        "v_cndmask_b32_e64 v60, v60, v104, s[58:59]\n"
        "v_mfma_f32_16x16x32_bf16 v[110:113], v[184:187], a[224:227], 0\n"
        "v_cndmask_b32_e64 v60, v60, v105, s[60:61]\n"
        "v_mul_f32 v60, s80, v60\n"
        "v_mfma_f32_16x16x32_bf16 v[110:113], v[180:183], a[220:223], v[110:113]\n"
        "v_mul_f32 v61, s80, v122\n"
        "v_fma_f32 v61, -v61, v61, v60\n"
        "v_mfma_f32_16x16x32_bf16 v[110:113], v[176:179], a[216:219], v[110:113]\n"
        "v_max_f32 v61, 0, v61\n"
        "v_add_f32 v61, s81, v61\n"
        "v_mfma_f32_16x16x32_bf16 v[102:105], v[222:225], a[228:231], 0\n"
        "v_rsq_f32 v61, v61\n"
        "v_mfma_f32_16x16x32_bf16 v[102:105], v[226:229], a[232:235], v[102:105]\n"
        "ds_write_b32 v128, v61 offset:192\n"
        "v_mfma_f32_16x16x32_bf16 v[102:105], v[230:233], a[236:239], v[102:105]\n"
        "v_cvt_pk_bf16_f32 v114, v70, v71\n"
        "v_cvt_pk_bf16_f32 v115, v72, v73\n"
        "v_mfma_f32_16x16x32_bf16 v[70:73], v[218:221], a[236:239], 0\n"
        "v_cvt_pk_bf16_f32 v116, v106, v107\n"
        "v_cvt_pk_bf16_f32 v117, v108, v109\n"
        "v_mfma_f32_16x16x32_bf16 v[70:73], v[210:213], a[232:235], v[70:73]\n"
        "v_cvt_pk_bf16_f32 v118, v64, v65\n"
        "v_cvt_pk_bf16_f32 v119, v66, v67\n"
        "v_mfma_f32_16x16x32_bf16 v[70:73], v[206:209], a[228:231], v[70:73]\n"
        "s_nop 0\n"
        "v_cvt_pk_bf16_f32 v120, v110, 0\n"
        "v_cvt_pk_bf16_f32 v126, v102, v103\n"
        "v_mfma_f32_16x16x32_bf16 v[64:67], v[194:197], a[228:231], 0\n"
        "s_nop 0\n"
        "v_cvt_pk_bf16_f32 v127, v104, v105\n"
        "v_mfma_f32_16x16x32_bf16 v[64:67], v[198:201], a[232:235], v[64:67]\n"
        "s_nop 0\n"
        "v_cvt_pk_bf16_f32 v124, v70, v71\n"
        "v_cvt_pk_bf16_f32 v125, v72, v73\n"
        "v_mfma_f32_16x16x32_bf16 v[64:67], v[202:205], a[236:239], v[64:67]\n"
        "s_nop 1\n"
        "v_mfma_f32_16x16x32_bf16 v[70:73], v[184:187], a[236:239], 0\n"
        "s_nop 2\n"
        "v_mfma_f32_16x16x32_bf16 v[70:73], v[180:183], a[232:235], v[70:73]\n"
        "s_nop 0\n"
        "v_cvt_pk_bf16_f32 v122, v64, v65\n"
        "v_cvt_pk_bf16_f32 v123, v66, v67\n"
        "v_mfma_f32_16x16x32_bf16 v[70:73], v[176:179], a[228:231], v[70:73]\n"
        "s_nop 1\n"
        "v_mfma_f32_16x16x32_bf16 v[64:67], v[194:197], a[240:243], 0\n"
        "s_nop 2\n"
        "v_mfma_f32_16x16x32_bf16 v[64:67], v[198:201], a[244:247], v[64:67]\n"
        "s_nop 0\n"
        "v_cvt_pk_bf16_f32 v128, v70, 0\n"
        "v_mfma_f32_16x16x32_bf16 v[64:67], v[202:205], a[248:251], v[64:67]\n"
        "s_nop 1\n"
        "v_mfma_f32_16x16x32_bf16 v[70:73], v[218:221], a[248:251], 0\n"
        "s_nop 2\n"
        "v_mfma_f32_16x16x32_bf16 v[70:73], v[210:213], a[244:247], v[70:73]\n"
        "s_nop 0\n"
        "v_cvt_pk_bf16_f32 v130, v64, v65\n"
        "v_cvt_pk_bf16_f32 v131, v66, v67\n"
        "v_mfma_f32_16x16x32_bf16 v[70:73], v[206:209], a[240:243], v[70:73]\n"
        "s_nop 1\n"
        "v_mfma_f32_16x16x32_bf16 v[64:67], v[222:225], a[240:243], 0\n"
        "s_nop 2\n"
        "v_mfma_f32_16x16x32_bf16 v[64:67], v[226:229], a[244:247], v[64:67]\n"
        "s_nop 0\n"
        "v_cvt_pk_bf16_f32 v132, v70, v71\n"
        "v_cvt_pk_bf16_f32 v133, v72, v73\n"
        "v_mfma_f32_16x16x32_bf16 v[64:67], v[230:233], a[248:251], v[64:67]\n"
        "s_nop 1\n"
        "v_mfma_f32_16x16x32_bf16 v[70:73], v[184:187], a[248:251], 0\n"
        "s_nop 2\n"
        "v_mfma_f32_16x16x32_bf16 v[70:73], v[180:183], a[244:247], v[70:73]\n"
        "s_nop 0\n"
        "v_cvt_pk_bf16_f32 v134, v64, v65\n"
        "v_cvt_pk_bf16_f32 v135, v66, v67\n"
        "v_mfma_f32_16x16x32_bf16 v[70:73], v[176:179], a[240:243], v[70:73]\n"
        "s_nop 1\n"
        "v_mfma_f32_16x16x32_bf16 v[64:67], v[222:225], a[252:255], 0\n"
        "s_nop 2\n"
        "v_mfma_f32_16x16x32_bf16 v[64:67], v[226:229], v[28:31], v[64:67]\n"
        "s_nop 0\n"
        "v_cvt_pk_bf16_f32 v136, v70, 0\n"
        "v_mfma_f32_16x16x32_bf16 v[64:67], v[230:233], v[32:35], v[64:67]\n"
        "s_nop 1\n"
        "v_mfma_f32_16x16x32_bf16 v[70:73], v[218:221], v[32:35], 0\n"
        "s_nop 2\n"
        "v_mfma_f32_16x16x32_bf16 v[70:73], v[210:213], v[28:31], v[70:73]\n"
        "s_nop 0\n"
        "v_cvt_pk_bf16_f32 v142, v64, v65\n"
        "v_cvt_pk_bf16_f32 v143, v66, v67\n"
        "v_mfma_f32_16x16x32_bf16 v[70:73], v[206:209], a[252:255], v[70:73]\n"
        "s_nop 1\n"
        "v_mfma_f32_16x16x32_bf16 v[64:67], v[194:197], a[252:255], 0\n"
        "s_nop 2\n"
        "v_mfma_f32_16x16x32_bf16 v[64:67], v[198:201], v[28:31], v[64:67]\n"
        "s_nop 0\n"
        "v_cvt_pk_bf16_f32 v140, v70, v71\n"
        "v_cvt_pk_bf16_f32 v141, v72, v73\n"
        "v_mfma_f32_16x16x32_bf16 v[64:67], v[202:205], v[32:35], v[64:67]\n"
        "s_nop 1\n"
        "v_mfma_f32_16x16x32_bf16 v[70:73], v[184:187], v[32:35], 0\n"
        "s_nop 2\n"
        "v_mfma_f32_16x16x32_bf16 v[70:73], v[180:183], v[28:31], v[70:73]\n"
        "s_nop 0\n"
        "v_cvt_pk_bf16_f32 v138, v64, v65\n"
        "v_cvt_pk_bf16_f32 v139, v66, v67\n"
        "v_mfma_f32_16x16x32_bf16 v[70:73], v[176:179], a[252:255], v[70:73]\n"
        "s_nop 1\n"
        "v_mfma_f32_16x16x32_bf16 v[64:67], v[194:197], v[36:39], 0\n"
        "s_nop 2\n"
        "v_mfma_f32_16x16x32_bf16 v[64:67], v[198:201], v[40:43], v[64:67]\n"
        "s_nop 0\n"
        "v_cvt_pk_bf16_f32 v144, v70, 0\n"
        "v_mfma_f32_16x16x32_bf16 v[64:67], v[202:205], v[44:47], v[64:67]\n"
        "s_nop 1\n"
        "v_mfma_f32_16x16x32_bf16 v[70:73], v[218:221], v[44:47], 0\n"
        "s_nop 2\n"
        "v_mfma_f32_16x16x32_bf16 v[70:73], v[210:213], v[40:43], v[70:73]\n"
        "s_nop 0\n"
        "v_cvt_pk_bf16_f32 v146, v64, v65\n"
        "v_cvt_pk_bf16_f32 v147, v66, v67\n"
        "v_mfma_f32_16x16x32_bf16 v[70:73], v[206:209], v[36:39], v[70:73]\n"
        "s_nop 1\n"
        "v_mfma_f32_16x16x32_bf16 v[64:67], v[222:225], v[36:39], 0\n"
        "s_nop 2\n"
        "v_mfma_f32_16x16x32_bf16 v[64:67], v[226:229], v[40:43], v[64:67]\n"
        "s_nop 0\n"
        "v_cvt_pk_bf16_f32 v148, v70, v71\n"
        "v_cvt_pk_bf16_f32 v149, v72, v73\n"
        "v_mfma_f32_16x16x32_bf16 v[64:67], v[230:233], v[44:47], v[64:67]\n"
        "s_nop 1\n"
        "v_mfma_f32_16x16x32_bf16 v[70:73], v[184:187], v[44:47], 0\n"
        "s_nop 2\n"
        "v_mfma_f32_16x16x32_bf16 v[70:73], v[180:183], v[40:43], v[70:73]\n"
        "s_nop 0\n"
        "v_cvt_pk_bf16_f32 v150, v64, v65\n"
        "v_cvt_pk_bf16_f32 v151, v66, v67\n"
        "v_mfma_f32_16x16x32_bf16 v[70:73], v[176:179], v[36:39], v[70:73]\n"
        "s_nop 1\n"
        "v_mfma_f32_16x16x32_bf16 v[64:67], v[222:225], v[48:51], 0\n"
        "s_nop 2\n"
        "v_mfma_f32_16x16x32_bf16 v[64:67], v[226:229], v[52:55], v[64:67]\n"
        "s_nop 0\n"
        "v_cvt_pk_bf16_f32 v152, v70, 0\n"
        "v_mfma_f32_16x16x32_bf16 v[64:67], v[230:233], v[56:59], v[64:67]\n"
        "v_mfma_f32_16x16x32_bf16 v[70:73], v[218:221], v[56:59], 0\n"
        "v_mfma_f32_16x16x32_bf16 v[70:73], v[210:213], v[52:55], v[70:73]\n"
        "ds_read_b128 v[102:105], v155\n"
        "ds_read_b128 v[106:109], v155 offset:64\n"
        "v_mfma_f32_16x16x32_bf16 v[70:73], v[206:209], v[48:51], v[70:73]\n"
        "ds_read_b128 v[110:113], v155 offset:128\n"
        "ds_read_b32 v60, v154\n"
        "v_mfma_f32_16x16x32_bf16 v[162:165], v[194:197], v[48:51], 0\n"
        "ds_read_b32 v61, v154 offset:64\n"
        "ds_read_b32 v76, v154 offset:128\n"
        "v_mfma_f32_16x16x32_bf16 v[162:165], v[198:201], v[52:55], v[162:165]\n"
        "ds_read_b32 v77, v154 offset:192\n"
        "v_cvt_pk_bf16_f32 v158, v64, v65\n"
        "v_mfma_f32_16x16x32_bf16 v[162:165], v[202:205], v[56:59], v[162:165]\n"
        "s_nop 0\n"
        "v_cvt_pk_bf16_f32 v159, v66, v67\n"
        "v_cvt_pk_bf16_f32 v156, v70, v71\n"
        "v_mfma_f32_16x16x32_bf16 v[64:67], v[184:187], v[56:59], 0\n"
        "s_nop 0\n"
        "v_cvt_pk_bf16_f32 v157, v72, v73\n"
        "v_mfma_f32_16x16x32_bf16 v[64:67], v[180:183], v[52:55], v[64:67]\n"
        "s_nop 0\n"
        "v_cvt_pk_bf16_f32 v154, v162, v163\n"
        "v_cvt_pk_bf16_f32 v155, v164, v165\n"
        "v_mfma_f32_16x16x32_bf16 v[64:67], v[176:179], v[48:51], v[64:67]\n"
        "s_nop 1\n"
        "v_mfma_f32_16x16x32_bf16 v[70:73], a[144:147], v[194:197], 0\n"
        "s_nop 2\n"
        "v_mfma_f32_16x16x32_bf16 v[70:73], a[148:151], v[198:201], v[70:73]\n"
        "s_nop 0\n"
        "v_cvt_pk_bf16_f32 v160, v64, 0\n"
        "v_mfma_f32_16x16x32_bf16 v[70:73], a[152:155], v[202:205], v[70:73]\n"
        "s_nop 1\n"
        "v_mfma_f32_16x16x32_bf16 v[64:67], a[164:167], v[202:205], 0\n"
        "s_waitcnt lgkmcnt(0)\n"
        "s_nop 1\n"
        "v_mfma_f32_16x16x32_bf16 v[64:67], a[160:163], v[198:201], v[64:67]\n"
        "s_nop 0\n"
        "v_cvt_pk_bf16_f32 v162, v70, v71\n"
        "v_cvt_pk_bf16_f32 v163, v72, v73\n"
        "v_mfma_f32_16x16x32_bf16 v[64:67], a[156:159], v[194:197], v[64:67]\n"
        "s_nop 1\n"
        "v_mfma_f32_16x16x32_bf16 v[70:73], a[168:171], v[194:197], 0\n"
        "s_nop 2\n"
        "v_mfma_f32_16x16x32_bf16 v[70:73], a[172:175], v[198:201], v[70:73]\n"
        "s_nop 0\n"
        "v_cvt_pk_bf16_f32 v164, v64, v65\n"
        "v_cvt_pk_bf16_f32 v165, v66, v67\n"
        "v_mfma_f32_16x16x32_bf16 v[70:73], a[176:179], v[202:205], v[70:73]\n"
        "s_nop 1\n"
        "v_mfma_f32_16x16x32_bf16 v[64:67], a[188:191], v[202:205], 0\n"
        "s_nop 2\n"
        "v_mfma_f32_16x16x32_bf16 v[64:67], a[184:187], v[198:201], v[64:67]\n"
        "s_nop 0\n"
        "v_cvt_pk_bf16_f32 v188, v70, v71\n"
        "v_cvt_pk_bf16_f32 v189, v72, v73\n"
        "v_mfma_f32_16x16x32_bf16 v[64:67], a[180:183], v[194:197], v[64:67]\n"
        "s_nop 1\n"
        "v_mfma_f32_16x16x32_bf16 v[70:73], a[192:195], v[194:197], 0\n"
        "s_nop 2\n"
        "v_mfma_f32_16x16x32_bf16 v[70:73], a[196:199], v[198:201], v[70:73]\n"
        "s_nop 0\n"
        "v_cvt_pk_bf16_f32 v190, v64, v65\n"
        "v_cvt_pk_bf16_f32 v191, v66, v67\n"
        "v_mfma_f32_16x16x32_bf16 v[70:73], a[200:203], v[202:205], v[70:73]\n"
        "s_nop 1\n"
        "v_mfma_f32_16x16x32_bf16 v[64:67], a[212:215], v[202:205], 0\n"
        "s_nop 2\n"
        "v_mfma_f32_16x16x32_bf16 v[64:67], a[208:211], v[198:201], v[64:67]\n"
        "s_nop 0\n"
        "v_cvt_pk_bf16_f32 v214, v70, v71\n"
        "v_cvt_pk_bf16_f32 v215, v72, v73\n"
        "v_mfma_f32_16x16x32_bf16 v[64:67], a[204:207], v[194:197], v[64:67]\n"
        "s_nop 1\n"
        "v_mfma_f32_16x16x32_bf16 v[70:73], v[194:197], v[162:165], 0\n"
        "s_nop 2\n"
        "v_mfma_f32_16x16x32_bf16 v[70:73], v[198:201], v[188:191], v[70:73]\n"
        "s_nop 0\n"
        "v_cvt_pk_bf16_f32 v216, v64, v65\n"
        "v_cvt_pk_bf16_f32 v217, v66, v67\n"
        "s_nop 1\n"
        "v_mfma_f32_16x16x32_bf16 v[70:73], v[202:205], v[214:217], v[70:73]\n"
        "s_nop 1\n"
        "v_mfma_f32_16x16x32_bf16 v[64:67], v[218:221], v[214:217], 0\n"
        "s_nop 2\n"
        "v_mfma_f32_16x16x32_bf16 v[64:67], v[210:213], v[188:191], v[64:67]\n"
        "s_nop 0\n"
        "v_mul_f32 v70, v70, v102\n"
        "v_mul_f32 v71, v71, v103\n"
        "v_mfma_f32_16x16x32_bf16 v[64:67], v[206:209], v[162:165], v[64:67]\n"
        "s_nop 0\n"
        "v_mul_f32 v72, v72, v104\n"
        "v_mul_f32 v73, v73, v105\n"
        "v_mfma_f32_16x16x32_bf16 v[234:237], v[222:225], v[162:165], 0\n"
        "s_nop 0\n"
        "v_max3_f32 v166, v70, v71, v72\n"
        "v_mfma_f32_16x16x32_bf16 v[234:237], v[226:229], v[188:191], v[234:237]\n"
        "s_nop 0\n"
        "v_mul_f32 v64, v64, v106\n"
        "v_mul_f32 v65, v65, v107\n"
        "v_mfma_f32_16x16x32_bf16 v[234:237], v[230:233], v[214:217], v[234:237]\n"
        "v_mul_f32 v66, v66, v108\n"
        "v_mul_f32 v67, v67, v109\n"
        "v_mfma_f32_16x16x32_bf16 v[238:241], v[184:187], v[214:217], 0\n"
        "s_nop 0\n"
        "v_max3_f32 v166, v166, v73, v64\n"
        "v_max3_f32 v166, v166, v65, v66\n"
        "v_mfma_f32_16x16x32_bf16 v[238:241], v[180:183], v[188:191], v[238:241]\n"
        "s_nop 0\n"
        "v_mul_f32 v234, v234, v110\n"
        "v_mul_f32 v235, v235, v111\n"
        "v_mfma_f32_16x16x32_bf16 v[238:241], v[176:179], v[162:165], v[238:241]\n"
        "s_nop 0\n"
        "v_mul_f32 v236, v236, v112\n"
        "v_mul_f32 v237, v237, v113\n"
        "v_max3_f32 v166, v166, v67, v234\n"
        "v_max3_f32 v166, v166, v235, v236\n"
        "s_nop 2\n"
        "v_mul_f32 v238, v238, v77\n"
        "v_cndmask_b32_e64 v238, v174, v238, s[62:63]\n"
        "v_max3_f32 v166, v166, v237, v238\n"
        "v_mov_b32 v162, v166\n"
        "s_nop 1\n"
        "v_permlane32_swap_b32 v166, v162\n"
        "v_max_f32 v166, v166, v162\n"
        "v_mov_b32 v162, v166\n"
        "s_nop 1\n"
        "v_permlane16_swap_b32 v166, v162\n"
        "v_max_f32 v166, v166, v162\n"
        "v_mul_f32 v166, v166, v60\n"
        "v_fma_f32 v70, v70, v60, -v166\n"
        "v_exp_f32 v70, v70\n"
        "v_fma_f32 v71, v71, v60, -v166\n"
        "v_exp_f32 v71, v71\n"
        "s_nop 0\n"
        "v_add_f32 v168, v70, v71\n"
        "v_mul_f32 v70, v70, v102\n"
        "v_fma_f32 v238, v238, v60, -v166\n"
        "v_exp_f32 v238, v238\n"
        "v_fma_f32 v237, v237, v60, -v166\n"
        "v_exp_f32 v237, v237\n"
        "v_fma_f32 v236, v236, v60, -v166\n"
        "v_exp_f32 v236, v236\n"
        "v_fma_f32 v235, v235, v60, -v166\n"
        "v_exp_f32 v235, v235\n"
        "v_fma_f32 v234, v234, v60, -v166\n"
        "v_exp_f32 v234, v234\n"
        "v_fma_f32 v67, v67, v60, -v166\n"
        "v_exp_f32 v67, v67\n"
        "v_fma_f32 v66, v66, v60, -v166\n"
        "v_exp_f32 v66, v66\n"
        "v_fma_f32 v65, v65, v60, -v166\n"
        "v_exp_f32 v65, v65\n"
        "v_fma_f32 v64, v64, v60, -v166\n"
        "v_exp_f32 v64, v64\n"
        "v_fma_f32 v73, v73, v60, -v166\n"
        "v_exp_f32 v73, v73\n"
        "v_fma_f32 v72, v72, v60, -v166\n"
        "v_exp_f32 v72, v72\n"
        "s_nop 0\n"
        "v_add_f32 v168, v168, v72\n"
        "v_add_f32 v168, v168, v73\n"
        "v_add_f32 v168, v168, v64\n"
        "v_add_f32 v168, v168, v65\n"
        "v_add_f32 v168, v168, v66\n"
        "v_add_f32 v168, v168, v67\n"
        "v_add_f32 v168, v168, v234\n"
        "v_add_f32 v168, v168, v235\n"
        "v_add_f32 v168, v168, v236\n"
        "v_add_f32 v168, v168, v237\n"
        "v_add_f32 v168, v168, v238\n"
        "v_mov_b32 v60, v168\n"
        "s_nop 1\n"
        "v_permlane32_swap_b32 v168, v60\n"
        "v_add_f32 v168, v168, v60\n"
        "v_mov_b32 v60, v168\n"
        "s_nop 1\n"
        "v_permlane16_swap_b32 v168, v60\n"
        "v_add_f32 v168, v168, v60\n"
        "v_rcp_f32 v168, v168\n"
        "s_nop 0\n"
        "v_mul_f32 v70, v70, v168\n"
        "v_mul_f32 v71, v71, v103\n"
        "v_mul_f32 v71, v71, v168\n"
        "v_cvt_pk_bf16_f32 v162, v70, v71\n"
        "v_mul_f32 v72, v72, v104\n"
        "v_mul_f32 v72, v72, v168\n"
        "v_mul_f32 v73, v73, v105\n"
        "v_mul_f32 v73, v73, v168\n"
        "v_cvt_pk_bf16_f32 v163, v72, v73\n"
        "v_mul_f32 v64, v64, v106\n"
        "v_mul_f32 v64, v64, v168\n"
        "v_mul_f32 v65, v65, v107\n"
        "v_mul_f32 v65, v65, v168\n"
        "v_cvt_pk_bf16_f32 v164, v64, v65\n"
        "v_mul_f32 v66, v66, v108\n"
        "v_mul_f32 v66, v66, v168\n"
        "v_mul_f32 v67, v67, v109\n"
        "v_mul_f32 v67, v67, v168\n"
        "v_cvt_pk_bf16_f32 v165, v66, v67\n"
        "s_nop 1\n"
        "v_mfma_f32_16x16x32_bf16 v[64:67], v[162:165], v[114:117], 0\n"
        "v_mul_f32 v234, v234, v110\n"
        "v_mul_f32 v235, v235, v111\n"
        "v_mul_f32 v234, v234, v168\n"
        "v_mul_f32 v235, v235, v168\n"
        "v_cvt_pk_bf16_f32 v166, v234, v235\n"
        "v_mul_f32 v236, v236, v112\n"
        "v_mul_f32 v236, v236, v168\n"
        "v_mul_f32 v237, v237, v113\n"
        "v_mul_f32 v237, v237, v168\n"
        "v_cvt_pk_bf16_f32 v167, v236, v237\n"
        "v_mul_f32 v238, v238, v77\n"
        "v_mul_f32 v238, v238, v168\n"
        "v_cvt_pk_bf16_f32 v168, v238, 0\n"
        "s_nop 1\n"
        "v_mfma_f32_16x16x32_bf16 v[64:67], v[166:169], v[118:121], v[64:67]\n"
        "v_mfma_f32_16x16x32_bf16 v[64:67], v[194:197], v[94:97], v[64:67]\n"
        "v_mfma_f32_16x16x32_bf16 v[70:73], v[194:197], v[98:101], 0\n"
        "v_mfma_f32_16x16x32_bf16 v[70:73], v[166:169], v[126:129], v[70:73]\n"
        "v_mfma_f32_16x16x32_bf16 v[70:73], v[162:165], v[122:125], v[70:73]\n"
        "s_add_u32 s90, s89, 0\n"
        "s_nop 0\n"
        "v_mfma_f32_16x16x32_bf16 v[188:191], v[162:165], v[130:133], 0\n"
        "s_nop 2\n"
        "v_mfma_f32_16x16x32_bf16 v[188:191], v[166:169], v[134:137], v[188:191]\n"
        "s_nop 0\n"
        "v_cvt_pk_bf16_f32 v214, v64, v70\n"
        "v_cvt_pk_bf16_f32 v234, v65, v71\n"
        "v_mfma_f32_16x16x32_bf16 v[188:191], v[198:201], v[94:97], v[188:191]\n"
        "v_cvt_pk_bf16_f32 v238, v66, v72\n"
        "v_cvt_pk_bf16_f32 v242, v67, v73\n"
        "v_mfma_f32_16x16x32_bf16 v[64:67], v[198:201], v[98:101], 0\n"
        "s_add_u32 s91, s89, 98112\n"
        "v_mfma_f32_16x16x32_bf16 v[64:67], v[166:169], v[142:145], v[64:67]\n"
        "v_mfma_f32_16x16x32_bf16 v[64:67], v[162:165], v[138:141], v[64:67]\n"
        "s_nop 1\n"
        "v_mfma_f32_16x16x32_bf16 v[70:73], v[162:165], v[146:149], 0\n"
        "s_nop 2\n"
        "v_mfma_f32_16x16x32_bf16 v[70:73], v[166:169], v[150:153], v[70:73]\n"
        "s_nop 0\n"
        "v_cvt_pk_bf16_f32 v215, v188, v64\n"
        "v_cvt_pk_bf16_f32 v235, v189, v65\n"
        "v_mfma_f32_16x16x32_bf16 v[70:73], v[202:205], v[94:97], v[70:73]\n"
        "v_cvt_pk_bf16_f32 v239, v190, v66\n"
        "v_cvt_pk_bf16_f32 v243, v191, v67\n"
        "v_mfma_f32_16x16x32_bf16 v[64:67], v[202:205], v[98:101], 0\n"
        "v_mfma_f32_16x16x32_bf16 v[64:67], v[166:169], v[158:161], v[64:67]\n"
        "v_mfma_f32_16x16x32_bf16 v[64:67], v[162:165], v[154:157], v[64:67]\n"
        "s_nop 1\n"
        "v_mfma_f32_16x16x32_bf16 v[162:165], a[144:147], v[206:209], 0\n"
        "s_nop 2\n"
        "v_mfma_f32_16x16x32_bf16 v[162:165], a[148:151], v[210:213], v[162:165]\n"
        "s_nop 0\n"
        "v_cvt_pk_bf16_f32 v216, v70, v64\n"
        "v_cvt_pk_bf16_f32 v236, v71, v65\n"
        "v_mfma_f32_16x16x32_bf16 v[162:165], a[152:155], v[218:221], v[162:165]\n"
        "s_nop 0\n"
        "v_cvt_pk_bf16_f32 v240, v72, v66\n"
        "v_cvt_pk_bf16_f32 v244, v73, v67\n"
        "v_mfma_f32_16x16x32_bf16 v[64:67], a[164:167], v[218:221], 0\n"
        "s_nop 1\n"
        "v_mfma_f32_16x16x32_bf16 v[64:67], a[160:163], v[210:213], v[64:67]\n"
        "s_nop 0\n"
        "v_cvt_pk_bf16_f32 v70, v162, v163\n"
        "v_cvt_pk_bf16_f32 v71, v164, v165\n"
        "v_mfma_f32_16x16x32_bf16 v[64:67], a[156:159], v[206:209], v[64:67]\n"
        "s_nop 0\n"
        "v_mfma_f32_16x16x32_bf16 v[162:165], a[168:171], v[206:209], 0\n"
        "buffer_store_dwordx3 v[214:216], v14, s[40:43], s90 offen sc1 nt\n"
        "s_nop 2\n"
        "v_mfma_f32_16x16x32_bf16 v[162:165], a[172:175], v[210:213], v[162:165]\n"
        "s_nop 0\n"
        "v_cvt_pk_bf16_f32 v72, v64, v65\n"
        "v_cvt_pk_bf16_f32 v73, v66, v67\n"
        "v_mfma_f32_16x16x32_bf16 v[162:165], a[176:179], v[218:221], v[162:165]\n"
        "buffer_store_dwordx3 v[234:236], v15, s[40:43], s90 offen offset:768 sc1 nt\n"
        "s_nop 0\n"
        "v_mfma_f32_16x16x32_bf16 v[64:67], a[188:191], v[218:221], 0\n"
        "s_nop 2\n"
        "v_mfma_f32_16x16x32_bf16 v[64:67], a[184:187], v[210:213], v[64:67]\n"
        "buffer_store_dwordx3 v[238:240], v14, s[40:43], s91 offen offset:192 sc1 nt\n"
        "v_cvt_pk_bf16_f32 v166, v162, v163\n"
        "v_cvt_pk_bf16_f32 v167, v164, v165\n"
        "v_mfma_f32_16x16x32_bf16 v[64:67], a[180:183], v[206:209], v[64:67]\n"
        "s_nop 0\n"
        "v_mfma_f32_16x16x32_bf16 v[162:165], a[192:195], v[206:209], 0\n"
        "buffer_store_dwordx3 v[242:244], v16, s[40:43], s91 offen offset:960 sc1 nt\n"
        "s_nop 2\n"
        "v_mfma_f32_16x16x32_bf16 v[162:165], a[196:199], v[210:213], v[162:165]\n"
        "s_nop 0\n"
        "v_cvt_pk_bf16_f32 v168, v64, v65\n"
        "v_cvt_pk_bf16_f32 v169, v66, v67\n"
        "v_mfma_f32_16x16x32_bf16 v[162:165], a[200:203], v[218:221], v[162:165]\n"
        "s_nop 1\n"
        "v_mfma_f32_16x16x32_bf16 v[64:67], a[212:215], v[218:221], 0\n"
        "s_nop 2\n"
        "v_mfma_f32_16x16x32_bf16 v[64:67], a[208:211], v[210:213], v[64:67]\n"
        "s_nop 0\n"
        "v_cvt_pk_bf16_f32 v188, v162, v163\n"
        "v_cvt_pk_bf16_f32 v189, v164, v165\n"
        "v_mfma_f32_16x16x32_bf16 v[64:67], a[204:207], v[206:209], v[64:67]\n"
        "s_nop 1\n"
        "v_mfma_f32_16x16x32_bf16 v[214:217], v[194:197], v[70:73], 0\n"
        "s_nop 2\n"
        "v_mfma_f32_16x16x32_bf16 v[214:217], v[198:201], v[166:169], v[214:217]\n"
        "s_nop 0\n"
        "v_cvt_pk_bf16_f32 v190, v64, v65\n"
        "v_cvt_pk_bf16_f32 v191, v66, v67\n"
        "s_nop 1\n"
        "v_mfma_f32_16x16x32_bf16 v[214:217], v[202:205], v[188:191], v[214:217]\n"
        "s_nop 1\n"
        "v_mfma_f32_16x16x32_bf16 v[64:67], v[218:221], v[188:191], 0\n"
        "s_nop 2\n"
        "v_mfma_f32_16x16x32_bf16 v[64:67], v[210:213], v[166:169], v[64:67]\n"
        "s_nop 0\n"
        "v_mul_f32 v214, v214, v102\n"
        "v_mul_f32 v215, v215, v103\n"
        "v_mfma_f32_16x16x32_bf16 v[64:67], v[206:209], v[70:73], v[64:67]\n"
        "s_nop 0\n"
        "v_mul_f32 v216, v216, v104\n"
        "v_mul_f32 v217, v217, v105\n"
        "v_mfma_f32_16x16x32_bf16 v[234:237], v[222:225], v[70:73], 0\n"
        "s_nop 0\n"
        "v_max3_f32 v60, v214, v215, v216\n"
        "v_mfma_f32_16x16x32_bf16 v[234:237], v[226:229], v[166:169], v[234:237]\n"
        "s_nop 0\n"
        "v_mul_f32 v64, v64, v106\n"
        "v_mul_f32 v65, v65, v107\n"
        "v_mfma_f32_16x16x32_bf16 v[234:237], v[230:233], v[188:191], v[234:237]\n"
        "v_mul_f32 v66, v66, v108\n"
        "v_mul_f32 v67, v67, v109\n"
        "v_mfma_f32_16x16x32_bf16 v[162:165], v[184:187], v[188:191], 0\n"
        "s_nop 0\n"
        "v_max3_f32 v60, v60, v217, v64\n"
        "v_max3_f32 v60, v60, v65, v66\n"
        "v_mfma_f32_16x16x32_bf16 v[162:165], v[180:183], v[166:169], v[162:165]\n"
        "s_nop 0\n"
        "v_mul_f32 v234, v234, v110\n"
        "v_mul_f32 v235, v235, v111\n"
        "v_mfma_f32_16x16x32_bf16 v[162:165], v[176:179], v[70:73], v[162:165]\n"
        "s_nop 0\n"
        "v_mul_f32 v236, v236, v112\n"
        "v_mul_f32 v237, v237, v113\n"
        "v_max3_f32 v60, v60, v67, v234\n"
        "v_max3_f32 v60, v60, v235, v236\n"
        "s_nop 2\n"
        "v_mul_f32 v162, v162, v77\n"
        "v_cndmask_b32_e64 v162, v174, v162, s[62:63]\n"
        "v_max3_f32 v60, v60, v237, v162\n"
        "v_mov_b32 v70, v60\n"
        "s_nop 1\n"
        "v_permlane32_swap_b32 v60, v70\n"
        "v_max_f32 v60, v60, v70\n"
        "v_mov_b32 v70, v60\n"
        "s_nop 1\n"
        "v_permlane16_swap_b32 v60, v70\n"
        "v_max_f32 v60, v60, v70\n"
        "v_mul_f32 v60, v60, v61\n"
        "v_fma_f32 v214, v214, v61, -v60\n"
        "v_exp_f32 v214, v214\n"
        "v_fma_f32 v215, v215, v61, -v60\n"
        "v_exp_f32 v215, v215\n"
        "s_nop 0\n"
        "v_add_f32 v70, v214, v215\n"
        "v_mul_f32 v214, v214, v102\n"
        "v_fma_f32 v162, v162, v61, -v60\n"
        "v_exp_f32 v162, v162\n"
        "v_fma_f32 v237, v237, v61, -v60\n"
        "v_exp_f32 v237, v237\n"
        "v_fma_f32 v236, v236, v61, -v60\n"
        "v_exp_f32 v236, v236\n"
        "v_fma_f32 v235, v235, v61, -v60\n"
        "v_exp_f32 v235, v235\n"
        "v_fma_f32 v234, v234, v61, -v60\n"
        "v_exp_f32 v234, v234\n"
        "v_fma_f32 v67, v67, v61, -v60\n"
        "v_exp_f32 v67, v67\n"
        "v_fma_f32 v66, v66, v61, -v60\n"
        "v_exp_f32 v66, v66\n"
        "v_fma_f32 v65, v65, v61, -v60\n"
        "v_exp_f32 v65, v65\n"
        "v_fma_f32 v64, v64, v61, -v60\n"
        "v_exp_f32 v64, v64\n"
        "v_fma_f32 v217, v217, v61, -v60\n"
        "v_exp_f32 v217, v217\n"
        "v_fma_f32 v216, v216, v61, -v60\n"
        "v_exp_f32 v216, v216\n"
        "s_nop 0\n"
        "v_add_f32 v70, v70, v216\n"
        "v_add_f32 v70, v70, v217\n"
        "v_add_f32 v70, v70, v64\n"
        "v_add_f32 v70, v70, v65\n"
        "v_add_f32 v70, v70, v66\n"
        "v_add_f32 v70, v70, v67\n"
        "v_add_f32 v70, v70, v234\n"
        "v_add_f32 v70, v70, v235\n"
        "v_add_f32 v70, v70, v236\n"
        "v_add_f32 v70, v70, v237\n"
        "v_add_f32 v70, v70, v162\n"
        "v_mov_b32 v60, v70\n"
        "s_nop 1\n"
        "v_permlane32_swap_b32 v70, v60\n"
        "v_add_f32 v70, v70, v60\n"
        "v_mov_b32 v60, v70\n"
        "s_nop 1\n"
        "v_permlane16_swap_b32 v70, v60\n"
        "v_add_f32 v70, v70, v60\n"
        "v_rcp_f32 v70, v70\n"
        "s_nop 0\n"
        "v_mul_f32 v214, v214, v70\n"
        "v_mul_f32 v215, v215, v103\n"
        "v_mul_f32 v215, v215, v70\n"
        "v_cvt_pk_bf16_f32 v164, v214, v215\n"
        "v_mul_f32 v216, v216, v104\n"
        "v_mul_f32 v216, v216, v70\n"
        "v_mul_f32 v217, v217, v105\n"
        "v_mul_f32 v217, v217, v70\n"
        "v_cvt_pk_bf16_f32 v165, v216, v217\n"
        "v_mul_f32 v64, v64, v106\n"
        "v_mul_f32 v64, v64, v70\n"
        "v_mul_f32 v65, v65, v107\n"
        "v_mul_f32 v65, v65, v70\n"
        "v_cvt_pk_bf16_f32 v166, v64, v65\n"
        "v_mul_f32 v66, v66, v108\n"
        "v_mul_f32 v66, v66, v70\n"
        "v_mul_f32 v67, v67, v109\n"
        "v_mul_f32 v67, v67, v70\n"
        "v_cvt_pk_bf16_f32 v167, v66, v67\n"
        "s_nop 1\n"
        "v_mfma_f32_16x16x32_bf16 v[64:67], v[164:167], v[114:117], 0\n"
        "v_mul_f32 v234, v234, v110\n"
        "v_mul_f32 v235, v235, v111\n"
        "v_mul_f32 v234, v234, v70\n"
        "v_mul_f32 v235, v235, v70\n"
        "v_cvt_pk_bf16_f32 v168, v234, v235\n"
        "v_mul_f32 v236, v236, v112\n"
        "v_mul_f32 v236, v236, v70\n"
        "v_mul_f32 v237, v237, v113\n"
        "v_mul_f32 v237, v237, v70\n"
        "v_cvt_pk_bf16_f32 v169, v236, v237\n"
        "v_mul_f32 v162, v162, v77\n"
        "v_mul_f32 v162, v162, v70\n"
        "v_cvt_pk_bf16_f32 v170, v162, 0\n"
        "s_nop 1\n"
        "v_mfma_f32_16x16x32_bf16 v[64:67], v[168:171], v[118:121], v[64:67]\n"
        "v_mfma_f32_16x16x32_bf16 v[64:67], v[206:209], v[94:97], v[64:67]\n"
        "v_mfma_f32_16x16x32_bf16 v[70:73], v[206:209], v[98:101], 0\n"
        "v_mfma_f32_16x16x32_bf16 v[70:73], v[168:171], v[126:129], v[70:73]\n"
        "v_mfma_f32_16x16x32_bf16 v[70:73], v[164:167], v[122:125], v[70:73]\n"
        "s_add_u32 s90, s89, 196224\n"
        "s_nop 0\n"
        "v_mfma_f32_16x16x32_bf16 v[188:191], v[164:167], v[130:133], 0\n"
        "s_nop 2\n"
        "v_mfma_f32_16x16x32_bf16 v[188:191], v[168:171], v[134:137], v[188:191]\n"
        "s_nop 0\n"
        "v_cvt_pk_bf16_f32 v214, v64, v70\n"
        "v_cvt_pk_bf16_f32 v234, v65, v71\n"
        "v_mfma_f32_16x16x32_bf16 v[188:191], v[210:213], v[94:97], v[188:191]\n"
        "v_cvt_pk_bf16_f32 v238, v66, v72\n"
        "v_cvt_pk_bf16_f32 v242, v67, v73\n"
        "v_mfma_f32_16x16x32_bf16 v[64:67], v[210:213], v[98:101], 0\n"
        "s_add_u32 s91, s89, 294336\n"
        "v_mfma_f32_16x16x32_bf16 v[64:67], v[168:171], v[142:145], v[64:67]\n"
        "v_mfma_f32_16x16x32_bf16 v[64:67], v[164:167], v[138:141], v[64:67]\n"
        "s_nop 1\n"
        "v_mfma_f32_16x16x32_bf16 v[70:73], v[164:167], v[146:149], 0\n"
        "s_nop 2\n"
        "v_mfma_f32_16x16x32_bf16 v[70:73], v[168:171], v[150:153], v[70:73]\n"
        "s_nop 0\n"
        "v_cvt_pk_bf16_f32 v215, v188, v64\n"
        "v_cvt_pk_bf16_f32 v235, v189, v65\n"
        "v_mfma_f32_16x16x32_bf16 v[70:73], v[218:221], v[94:97], v[70:73]\n"
        "v_cvt_pk_bf16_f32 v239, v190, v66\n"
        "v_cvt_pk_bf16_f32 v243, v191, v67\n"
        "v_mfma_f32_16x16x32_bf16 v[64:67], v[218:221], v[98:101], 0\n"
        "v_mfma_f32_16x16x32_bf16 v[64:67], v[168:171], v[158:161], v[64:67]\n"
        "v_mfma_f32_16x16x32_bf16 v[64:67], v[164:167], v[154:157], v[64:67]\n"
        "s_nop 1\n"
        "v_mfma_f32_16x16x32_bf16 v[162:165], a[144:147], v[222:225], 0\n"
        "s_nop 2\n"
        "v_mfma_f32_16x16x32_bf16 v[162:165], a[148:151], v[226:229], v[162:165]\n"
        "s_nop 0\n"
        "v_cvt_pk_bf16_f32 v216, v70, v64\n"
        "v_cvt_pk_bf16_f32 v236, v71, v65\n"
        "v_mfma_f32_16x16x32_bf16 v[162:165], a[152:155], v[230:233], v[162:165]\n"
        "s_nop 0\n"
        "v_cvt_pk_bf16_f32 v240, v72, v66\n"
        "v_cvt_pk_bf16_f32 v244, v73, v67\n"
        "v_mfma_f32_16x16x32_bf16 v[64:67], a[164:167], v[230:233], 0\n"
        "s_nop 1\n"
        "v_mfma_f32_16x16x32_bf16 v[64:67], a[160:163], v[226:229], v[64:67]\n"
        "s_nop 0\n"
        "v_cvt_pk_bf16_f32 v70, v162, v163\n"
        "v_cvt_pk_bf16_f32 v71, v164, v165\n"
        "v_mfma_f32_16x16x32_bf16 v[64:67], a[156:159], v[222:225], v[64:67]\n"
        "s_nop 0\n"
        "v_mfma_f32_16x16x32_bf16 v[162:165], a[168:171], v[222:225], 0\n"
        "buffer_store_dwordx3 v[214:216], v14, s[40:43], s90 offen offset:384 sc1 nt\n"
        "s_nop 2\n"
        "v_mfma_f32_16x16x32_bf16 v[162:165], a[172:175], v[226:229], v[162:165]\n"
        "s_nop 0\n"
        "v_cvt_pk_bf16_f32 v72, v64, v65\n"
        "v_cvt_pk_bf16_f32 v73, v66, v67\n"
        "v_mfma_f32_16x16x32_bf16 v[162:165], a[176:179], v[230:233], v[162:165]\n"
        "buffer_store_dwordx3 v[234:236], v17, s[40:43], s90 offen offset:1152 sc1 nt\n"
        "s_add_u32 s90, s89, 392448\n"
        "v_mfma_f32_16x16x32_bf16 v[64:67], a[188:191], v[230:233], 0\n"
        "s_nop 2\n"
        "v_mfma_f32_16x16x32_bf16 v[64:67], a[184:187], v[226:229], v[64:67]\n"
        "buffer_store_dwordx3 v[238:240], v14, s[40:43], s91 offen offset:576 sc1 nt\n"
        "v_cvt_pk_bf16_f32 v166, v162, v163\n"
        "v_cvt_pk_bf16_f32 v167, v164, v165\n"
        "v_mfma_f32_16x16x32_bf16 v[64:67], a[180:183], v[222:225], v[64:67]\n"
        "s_nop 0\n"
        "v_mfma_f32_16x16x32_bf16 v[162:165], a[192:195], v[222:225], 0\n"
        "buffer_store_dwordx3 v[242:244], v14, s[40:43], s90 offen sc1 nt\n"
        "s_nop 2\n"
        "v_mfma_f32_16x16x32_bf16 v[162:165], a[196:199], v[226:229], v[162:165]\n"
        "s_nop 0\n"
        "v_cvt_pk_bf16_f32 v168, v64, v65\n"
        "v_cvt_pk_bf16_f32 v169, v66, v67\n"
        "v_mfma_f32_16x16x32_bf16 v[162:165], a[200:203], v[230:233], v[162:165]\n"
        "s_nop 1\n"
        "v_mfma_f32_16x16x32_bf16 v[64:67], a[212:215], v[230:233], 0\n"
        "s_nop 2\n"
        "v_mfma_f32_16x16x32_bf16 v[64:67], a[208:211], v[226:229], v[64:67]\n"
        "s_nop 0\n"
        "v_cvt_pk_bf16_f32 v188, v162, v163\n"
        "v_cvt_pk_bf16_f32 v189, v164, v165\n"
        "v_mfma_f32_16x16x32_bf16 v[64:67], a[204:207], v[222:225], v[64:67]\n"
        "s_nop 1\n"
        "v_mfma_f32_16x16x32_bf16 v[162:165], v[194:197], v[70:73], 0\n"
        "s_nop 2\n"
        "v_mfma_f32_16x16x32_bf16 v[162:165], v[198:201], v[166:169], v[162:165]\n"
        "s_nop 0\n"
        "v_cvt_pk_bf16_f32 v190, v64, v65\n"
        "v_cvt_pk_bf16_f32 v191, v66, v67\n"
        "s_nop 1\n"
        "v_mfma_f32_16x16x32_bf16 v[162:165], v[202:205], v[188:191], v[162:165]\n"
        "s_nop 1\n"
        "v_mfma_f32_16x16x32_bf16 v[64:67], v[218:221], v[188:191], 0\n"
        "s_nop 2\n"
        "v_mfma_f32_16x16x32_bf16 v[64:67], v[210:213], v[166:169], v[64:67]\n"
        "s_nop 0\n"
        "v_mul_f32 v162, v162, v102\n"
        "v_mul_f32 v163, v163, v103\n"
        "v_mfma_f32_16x16x32_bf16 v[64:67], v[206:209], v[70:73], v[64:67]\n"
        "s_nop 0\n"
        "v_mul_f32 v164, v164, v104\n"
        "v_mul_f32 v165, v165, v105\n"
        "v_mfma_f32_16x16x32_bf16 v[214:217], v[222:225], v[70:73], 0\n"
        "s_nop 0\n"
        "v_max3_f32 v60, v162, v163, v164\n"
        "v_mfma_f32_16x16x32_bf16 v[214:217], v[226:229], v[166:169], v[214:217]\n"
        "s_nop 0\n"
        "v_mul_f32 v64, v64, v106\n"
        "v_mul_f32 v65, v65, v107\n"
        "v_mfma_f32_16x16x32_bf16 v[214:217], v[230:233], v[188:191], v[214:217]\n"
        "v_mul_f32 v66, v66, v108\n"
        "v_mul_f32 v67, v67, v109\n"
        "v_mfma_f32_16x16x32_bf16 v[234:237], v[184:187], v[188:191], 0\n"
        "s_nop 0\n"
        "v_max3_f32 v60, v60, v165, v64\n"
        "v_max3_f32 v60, v60, v65, v66\n"
        "v_mfma_f32_16x16x32_bf16 v[234:237], v[180:183], v[166:169], v[234:237]\n"
        "s_nop 0\n"
        "v_mul_f32 v214, v214, v110\n"
        "v_mul_f32 v215, v215, v111\n"
        "v_mfma_f32_16x16x32_bf16 v[234:237], v[176:179], v[70:73], v[234:237]\n"
        "s_nop 0\n"
        "v_mul_f32 v216, v216, v112\n"
        "v_mul_f32 v217, v217, v113\n"
        "v_max3_f32 v60, v60, v67, v214\n"
        "v_max3_f32 v60, v60, v215, v216\n"
        "s_nop 2\n"
        "v_mul_f32 v234, v234, v77\n"
        "v_cndmask_b32_e64 v234, v174, v234, s[62:63]\n"
        "v_max3_f32 v60, v60, v217, v234\n"
        "v_mov_b32 v61, v60\n"
        "s_nop 1\n"
        "v_permlane32_swap_b32 v60, v61\n"
        "v_max_f32 v60, v60, v61\n"
        "v_mov_b32 v61, v60\n"
        "s_nop 1\n"
        "v_permlane16_swap_b32 v60, v61\n"
        "v_max_f32 v60, v60, v61\n"
        "v_mul_f32 v60, v60, v76\n"
        "v_fma_f32 v162, v162, v76, -v60\n"
        "v_exp_f32 v162, v162\n"
        "v_fma_f32 v163, v163, v76, -v60\n"
        "v_exp_f32 v163, v163\n"
        "s_nop 0\n"
        "v_add_f32 v61, v162, v163\n"
        "v_mul_f32 v162, v162, v102\n"
        "v_fma_f32 v234, v234, v76, -v60\n"
        "v_exp_f32 v234, v234\n"
        "v_fma_f32 v217, v217, v76, -v60\n"
        "v_exp_f32 v217, v217\n"
        "v_fma_f32 v216, v216, v76, -v60\n"
        "v_exp_f32 v216, v216\n"
        "v_fma_f32 v215, v215, v76, -v60\n"
        "v_exp_f32 v215, v215\n"
        "v_fma_f32 v214, v214, v76, -v60\n"
        "v_exp_f32 v214, v214\n"
        "v_fma_f32 v67, v67, v76, -v60\n"
        "v_exp_f32 v67, v67\n"
        "v_fma_f32 v66, v66, v76, -v60\n"
        "v_exp_f32 v66, v66\n"
        "v_fma_f32 v65, v65, v76, -v60\n"
        "v_exp_f32 v65, v65\n"
        "v_fma_f32 v64, v64, v76, -v60\n"
        "v_exp_f32 v64, v64\n"
        "v_fma_f32 v165, v165, v76, -v60\n"
        "v_exp_f32 v165, v165\n"
        "v_fma_f32 v164, v164, v76, -v60\n"
        "v_exp_f32 v164, v164\n"
        "s_nop 0\n"
        "v_add_f32 v61, v61, v164\n"
        "v_add_f32 v61, v61, v165\n"
        "v_add_f32 v61, v61, v64\n"
        "v_add_f32 v61, v61, v65\n"
        "v_add_f32 v61, v61, v66\n"
        "v_add_f32 v61, v61, v67\n"
        "v_add_f32 v61, v61, v214\n"
        "v_add_f32 v61, v61, v215\n"
        "v_add_f32 v61, v61, v216\n"
        "v_add_f32 v61, v61, v217\n"
        "v_add_f32 v61, v61, v234\n"
        "v_mov_b32 v60, v61\n"
        "s_nop 1\n"
        "v_permlane32_swap_b32 v61, v60\n"
        "v_add_f32 v61, v61, v60\n"
        "v_mov_b32 v60, v61\n"
        "s_nop 1\n"
        "v_permlane16_swap_b32 v61, v60\n"
        "v_add_f32 v61, v61, v60\n"
        "v_rcp_f32 v61, v61\n"
        "s_nop 0\n"
        "v_mul_f32 v162, v162, v61\n"
        "v_mul_f32 v163, v163, v103\n"
        "v_mul_f32 v163, v163, v61\n"
        "v_cvt_pk_bf16_f32 v166, v162, v163\n"
        "v_mul_f32 v164, v164, v104\n"
        "v_mul_f32 v164, v164, v61\n"
        "v_mul_f32 v165, v165, v105\n"
        "v_mul_f32 v165, v165, v61\n"
        "v_cvt_pk_bf16_f32 v167, v164, v165\n"
        "v_mul_f32 v64, v64, v106\n"
        "v_mul_f32 v64, v64, v61\n"
        "v_mul_f32 v65, v65, v107\n"
        "v_mul_f32 v65, v65, v61\n"
        "v_cvt_pk_bf16_f32 v168, v64, v65\n"
        "v_mul_f32 v66, v66, v108\n"
        "v_mul_f32 v66, v66, v61\n"
        "v_mul_f32 v67, v67, v109\n"
        "v_mul_f32 v67, v67, v61\n"
        "v_cvt_pk_bf16_f32 v169, v66, v67\n"
        "s_nop 1\n"
        "v_mfma_f32_16x16x32_bf16 v[64:67], v[166:169], v[114:117], 0\n"
        "v_mul_f32 v214, v214, v110\n"
        "v_mul_f32 v215, v215, v111\n"
        "v_mul_f32 v214, v214, v61\n"
        "v_mul_f32 v215, v215, v61\n"
        "v_cvt_pk_bf16_f32 v170, v214, v215\n"
        "v_mul_f32 v216, v216, v112\n"
        "v_mul_f32 v216, v216, v61\n"
        "v_mul_f32 v217, v217, v113\n"
        "v_mul_f32 v217, v217, v61\n"
        "v_cvt_pk_bf16_f32 v171, v216, v217\n"
        "v_mul_f32 v234, v234, v77\n"
        "v_mul_f32 v234, v234, v61\n"
        "v_cvt_pk_bf16_f32 v172, v234, 0\n"
        "s_nop 1\n"
        "v_mfma_f32_16x16x32_bf16 v[64:67], v[170:173], v[118:121], v[64:67]\n"
        "v_mfma_f32_16x16x32_bf16 v[64:67], v[222:225], v[94:97], v[64:67]\n"
        "v_mfma_f32_16x16x32_bf16 v[70:73], v[222:225], v[98:101], 0\n"
        "v_mfma_f32_16x16x32_bf16 v[70:73], v[170:173], v[126:129], v[70:73]\n"
        "v_mfma_f32_16x16x32_bf16 v[70:73], v[166:169], v[122:125], v[70:73]\n"
        "s_nop 1\n"
        "v_mfma_f32_16x16x32_bf16 v[162:165], v[166:169], v[130:133], 0\n"
        "s_nop 2\n"
        "v_mfma_f32_16x16x32_bf16 v[162:165], v[170:173], v[134:137], v[162:165]\n"
        "s_add_u32 s91, s89, 490560\n"
        "v_cvt_pk_bf16_f32 v188, v64, v70\n"
        "v_cvt_pk_bf16_f32 v214, v65, v71\n"
        "v_mfma_f32_16x16x32_bf16 v[162:165], v[226:229], v[94:97], v[162:165]\n"
        "v_cvt_pk_bf16_f32 v234, v66, v72\n"
        "v_cvt_pk_bf16_f32 v238, v67, v73\n"
        "v_mfma_f32_16x16x32_bf16 v[64:67], v[226:229], v[98:101], 0\n"
        "v_mfma_f32_16x16x32_bf16 v[64:67], v[170:173], v[142:145], v[64:67]\n"
        "v_mfma_f32_16x16x32_bf16 v[64:67], v[166:169], v[138:141], v[64:67]\n"
        "s_nop 1\n"
        "v_mfma_f32_16x16x32_bf16 v[70:73], v[166:169], v[146:149], 0\n"
        "s_nop 2\n"
        "v_mfma_f32_16x16x32_bf16 v[70:73], v[170:173], v[150:153], v[70:73]\n"
        "s_nop 0\n"
        "v_cvt_pk_bf16_f32 v189, v162, v64\n"
        "v_cvt_pk_bf16_f32 v215, v163, v65\n"
        "v_mfma_f32_16x16x32_bf16 v[70:73], v[230:233], v[94:97], v[70:73]\n"
        "v_cvt_pk_bf16_f32 v235, v164, v66\n"
        "v_cvt_pk_bf16_f32 v239, v165, v67\n"
        "v_mfma_f32_16x16x32_bf16 v[64:67], v[230:233], v[98:101], 0\n"
        "v_mfma_f32_16x16x32_bf16 v[64:67], v[170:173], v[158:161], v[64:67]\n"
        "v_mfma_f32_16x16x32_bf16 v[64:67], v[166:169], v[154:157], v[64:67]\n"
        "s_nop 1\n"
        "v_mfma_f32_16x16x32_bf16 v[162:165], a[144:147], v[176:179], 0\n"
        "s_nop 2\n"
        "v_mfma_f32_16x16x32_bf16 v[162:165], a[148:151], v[180:183], v[162:165]\n"
        "s_nop 0\n"
        "v_cvt_pk_bf16_f32 v190, v70, v64\n"
        "v_cvt_pk_bf16_f32 v216, v71, v65\n"
        "v_mfma_f32_16x16x32_bf16 v[162:165], a[152:155], v[184:187], v[162:165]\n"
        "s_nop 0\n"
        "v_cvt_pk_bf16_f32 v236, v72, v66\n"
        "v_cvt_pk_bf16_f32 v240, v73, v67\n"
        "v_mfma_f32_16x16x32_bf16 v[64:67], a[164:167], v[184:187], 0\n"
        "s_nop 1\n"
        "v_mfma_f32_16x16x32_bf16 v[64:67], a[160:163], v[180:183], v[64:67]\n"
        "s_nop 0\n"
        "v_cvt_pk_bf16_f32 v70, v162, v163\n"
        "v_cvt_pk_bf16_f32 v71, v164, v165\n"
        "v_mfma_f32_16x16x32_bf16 v[64:67], a[156:159], v[176:179], v[64:67]\n"
        "s_nop 0\n"
        "v_mfma_f32_16x16x32_bf16 v[162:165], a[168:171], v[176:179], 0\n"
        "buffer_store_dwordx3 v[188:190], v15, s[40:43], s90 offen offset:768 sc1 nt\n"
        "s_add_u32 s90, s89, 588672\n"
        "s_nop 1\n"
        "v_mfma_f32_16x16x32_bf16 v[162:165], a[172:175], v[180:183], v[162:165]\n"
        "s_nop 0\n"
        "v_cvt_pk_bf16_f32 v72, v64, v65\n"
        "v_cvt_pk_bf16_f32 v73, v66, v67\n"
        "v_mfma_f32_16x16x32_bf16 v[162:165], a[176:179], v[184:187], v[162:165]\n"
        "buffer_store_dwordx3 v[214:216], v14, s[40:43], s91 offen offset:192 sc1 nt\n"
        "s_nop 0\n"
        "v_mfma_f32_16x16x32_bf16 v[64:67], a[188:191], v[184:187], 0\n"
        "s_nop 2\n"
        "v_mfma_f32_16x16x32_bf16 v[64:67], a[184:187], v[180:183], v[64:67]\n"
        "buffer_store_dwordx3 v[234:236], v16, s[40:43], s91 offen offset:960 sc1 nt\n"
        "v_cvt_pk_bf16_f32 v166, v162, v163\n"
        "v_cvt_pk_bf16_f32 v167, v164, v165\n"
        "v_mfma_f32_16x16x32_bf16 v[64:67], a[180:183], v[176:179], v[64:67]\n"
        "s_nop 0\n"
        "v_mfma_f32_16x16x32_bf16 v[162:165], a[192:195], v[176:179], 0\n"
        "buffer_store_dwordx3 v[238:240], v14, s[40:43], s90 offen offset:384 sc1 nt\n"
        "s_nop 2\n"
        "v_mfma_f32_16x16x32_bf16 v[162:165], a[196:199], v[180:183], v[162:165]\n"
        "s_nop 0\n"
        "v_cvt_pk_bf16_f32 v168, v64, v65\n"
        "v_cvt_pk_bf16_f32 v169, v66, v67\n"
        "v_mfma_f32_16x16x32_bf16 v[162:165], a[200:203], v[184:187], v[162:165]\n"
        "s_nop 1\n"
        "v_mfma_f32_16x16x32_bf16 v[64:67], a[212:215], v[184:187], 0\n"
        "s_nop 2\n"
        "v_mfma_f32_16x16x32_bf16 v[64:67], a[208:211], v[180:183], v[64:67]\n"
        "s_nop 0\n"
        "v_cvt_pk_bf16_f32 v170, v162, v163\n"
        "v_cvt_pk_bf16_f32 v171, v164, v165\n"
        "v_mfma_f32_16x16x32_bf16 v[64:67], a[204:207], v[176:179], v[64:67]\n"
        "s_nop 1\n"
        "v_mfma_f32_16x16x32_bf16 v[162:165], v[194:197], v[70:73], 0\n"
        "s_nop 2\n"
        "v_mfma_f32_16x16x32_bf16 v[162:165], v[198:201], v[166:169], v[162:165]\n"
        "s_nop 0\n"
        "v_cvt_pk_bf16_f32 v172, v64, v65\n"
        "v_cvt_pk_bf16_f32 v173, v66, v67\n"
        "s_nop 1\n"
        "v_mfma_f32_16x16x32_bf16 v[162:165], v[202:205], v[170:173], v[162:165]\n"
        "s_nop 1\n"
        "v_mfma_f32_16x16x32_bf16 v[64:67], v[218:221], v[170:173], 0\n"
        "s_nop 2\n"
        "v_mfma_f32_16x16x32_bf16 v[64:67], v[210:213], v[166:169], v[64:67]\n"
        "s_nop 0\n"
        "v_mul_f32 v162, v162, v102\n"
        "v_mul_f32 v163, v163, v103\n"
        "v_mfma_f32_16x16x32_bf16 v[64:67], v[206:209], v[70:73], v[64:67]\n"
        "s_nop 0\n"
        "v_mul_f32 v164, v164, v104\n"
        "v_mul_f32 v165, v165, v105\n"
        "v_mfma_f32_16x16x32_bf16 v[188:191], v[222:225], v[70:73], 0\n"
        "s_nop 0\n"
        "v_max3_f32 v60, v162, v163, v164\n"
        "v_mfma_f32_16x16x32_bf16 v[188:191], v[226:229], v[166:169], v[188:191]\n"
        "s_nop 0\n"
        "v_mul_f32 v64, v64, v106\n"
        "v_mul_f32 v65, v65, v107\n"
        "v_mfma_f32_16x16x32_bf16 v[188:191], v[230:233], v[170:173], v[188:191]\n"
        "v_mul_f32 v66, v66, v108\n"
        "v_mul_f32 v67, v67, v109\n"
        "v_mfma_f32_16x16x32_bf16 v[192:195], v[184:187], v[170:173], 0\n"
        "s_nop 0\n"
        "v_max3_f32 v60, v60, v165, v64\n"
        "v_max3_f32 v60, v60, v65, v66\n"
        "v_mfma_f32_16x16x32_bf16 v[192:195], v[180:183], v[166:169], v[192:195]\n"
        "s_nop 0\n"
        "v_mul_f32 v188, v188, v110\n"
        "v_mul_f32 v189, v189, v111\n"
        "v_mfma_f32_16x16x32_bf16 v[192:195], v[176:179], v[70:73], v[192:195]\n"
        "s_nop 0\n"
        "v_mul_f32 v190, v190, v112\n"
        "v_mul_f32 v191, v191, v113\n"
        "v_max3_f32 v60, v60, v67, v188\n"
        "v_max3_f32 v60, v60, v189, v190\n"
        "s_nop 2\n"
        "v_mul_f32 v192, v192, v77\n"
        "v_cndmask_b32_e64 v192, v174, v192, s[62:63]\n"
        "v_max3_f32 v60, v60, v191, v192\n"
        "v_mov_b32 v61, v60\n"
        "s_nop 1\n"
        "v_permlane32_swap_b32 v60, v61\n"
        "v_max_f32 v60, v60, v61\n"
        "v_mov_b32 v61, v60\n"
        "s_nop 1\n"
        "v_permlane16_swap_b32 v60, v61\n"
        "v_max_f32 v60, v60, v61\n"
        "v_mul_f32 v60, v60, v77\n"
        "v_fma_f32 v162, v162, v77, -v60\n"
        "v_exp_f32 v162, v162\n"
        "v_fma_f32 v163, v163, v77, -v60\n"
        "v_exp_f32 v163, v163\n"
        "s_nop 0\n"
        "v_add_f32 v61, v162, v163\n"
        "v_mul_f32 v162, v162, v102\n"
        "v_fma_f32 v192, v192, v77, -v60\n"
        "v_exp_f32 v192, v192\n"
        "v_fma_f32 v191, v191, v77, -v60\n"
        "v_exp_f32 v191, v191\n"
        "v_fma_f32 v190, v190, v77, -v60\n"
        "v_exp_f32 v190, v190\n"
        "v_fma_f32 v189, v189, v77, -v60\n"
        "v_exp_f32 v189, v189\n"
        "v_fma_f32 v188, v188, v77, -v60\n"
        "v_exp_f32 v188, v188\n"
        "v_fma_f32 v67, v67, v77, -v60\n"
        "v_exp_f32 v67, v67\n"
        "v_fma_f32 v66, v66, v77, -v60\n"
        "v_exp_f32 v66, v66\n"
        "v_fma_f32 v65, v65, v77, -v60\n"
        "v_exp_f32 v65, v65\n"
        "v_fma_f32 v64, v64, v77, -v60\n"
        "v_exp_f32 v64, v64\n"
        "v_fma_f32 v165, v165, v77, -v60\n"
        "v_exp_f32 v165, v165\n"
        "v_fma_f32 v164, v164, v77, -v60\n"
        "v_exp_f32 v164, v164\n"
        "s_nop 0\n"
        "v_add_f32 v61, v61, v164\n"
        "v_add_f32 v61, v61, v165\n"
        "v_add_f32 v61, v61, v64\n"
        "v_add_f32 v61, v61, v65\n"
        "v_add_f32 v61, v61, v66\n"
        "v_add_f32 v61, v61, v67\n"
        "v_add_f32 v61, v61, v188\n"
        "v_add_f32 v61, v61, v189\n"
        "v_add_f32 v61, v61, v190\n"
        "v_add_f32 v61, v61, v191\n"
        "v_add_f32 v61, v61, v192\n"
        "v_mov_b32 v60, v61\n"
        "s_nop 1\n"
        "v_permlane32_swap_b32 v61, v60\n"
        "v_add_f32 v61, v61, v60\n"
        "v_mov_b32 v60, v61\n"
        "s_nop 1\n"
        "v_permlane16_swap_b32 v61, v60\n"
        "v_add_f32 v61, v61, v60\n"
        "v_rcp_f32 v61, v61\n"
        "s_nop 0\n"
        "v_mul_f32 v162, v162, v61\n"
        "v_mul_f32 v163, v163, v103\n"
        "v_mul_f32 v163, v163, v61\n"
        "v_cvt_pk_bf16_f32 v168, v162, v163\n"
        "v_mul_f32 v164, v164, v104\n"
        "v_mul_f32 v164, v164, v61\n"
        "v_mul_f32 v165, v165, v105\n"
        "v_mul_f32 v165, v165, v61\n"
        "v_cvt_pk_bf16_f32 v169, v164, v165\n"
        "v_mul_f32 v64, v64, v106\n"
        "v_mul_f32 v64, v64, v61\n"
        "v_mul_f32 v65, v65, v107\n"
        "v_mul_f32 v65, v65, v61\n"
        "v_cvt_pk_bf16_f32 v170, v64, v65\n"
        "v_mul_f32 v66, v66, v108\n"
        "v_mul_f32 v66, v66, v61\n"
        "v_mul_f32 v67, v67, v109\n"
        "v_mul_f32 v67, v67, v61\n"
        "v_cvt_pk_bf16_f32 v171, v66, v67\n"
        "s_nop 1\n"
        "v_mfma_f32_16x16x32_bf16 v[64:67], v[168:171], v[114:117], 0\n"
        "v_mul_f32 v188, v188, v110\n"
        "v_mul_f32 v189, v189, v111\n"
        "v_mul_f32 v188, v188, v61\n"
        "v_mul_f32 v189, v189, v61\n"
        "v_cvt_pk_bf16_f32 v172, v188, v189\n"
        "v_mul_f32 v190, v190, v112\n"
        "v_mul_f32 v190, v190, v61\n"
        "v_mul_f32 v191, v191, v113\n"
        "v_mul_f32 v191, v191, v61\n"
        "v_cvt_pk_bf16_f32 v173, v190, v191\n"
        "v_mul_f32 v192, v192, v77\n"
        "v_mul_f32 v192, v192, v61\n"
        "v_cvt_pk_bf16_f32 v174, v192, 0\n"
        "s_nop 1\n"
        "v_mfma_f32_16x16x32_bf16 v[64:67], v[172:175], v[118:121], v[64:67]\n"
        "v_mfma_f32_16x16x32_bf16 v[64:67], v[176:179], v[94:97], v[64:67]\n"
        "v_mfma_f32_16x16x32_bf16 v[70:73], v[176:179], v[98:101], 0\n"
        "v_mfma_f32_16x16x32_bf16 v[70:73], v[172:175], v[126:129], v[70:73]\n"
        "v_mfma_f32_16x16x32_bf16 v[70:73], v[168:171], v[122:125], v[70:73]\n"
        "s_nop 1\n"
        "v_mfma_f32_16x16x32_bf16 v[102:105], v[168:171], v[130:133], 0\n"
        "s_nop 2\n"
        "v_mfma_f32_16x16x32_bf16 v[102:105], v[172:175], v[134:137], v[102:105]\n"
        "s_nop 0\n"
        "v_cvt_pk_bf16_f32 v106, v64, v70\n"
        "v_mfma_f32_16x16x32_bf16 v[102:105], v[180:183], v[94:97], v[102:105]\n"
        "v_mfma_f32_16x16x32_bf16 v[64:67], v[180:183], v[98:101], 0\n"
        "v_mfma_f32_16x16x32_bf16 v[64:67], v[172:175], v[142:145], v[64:67]\n"
        "v_mfma_f32_16x16x32_bf16 v[64:67], v[168:171], v[138:141], v[64:67]\n"
        "s_nop 1\n"
        "v_mfma_f32_16x16x32_bf16 v[70:73], v[168:171], v[146:149], 0\n"
        "s_nop 2\n"
        "v_mfma_f32_16x16x32_bf16 v[70:73], v[172:175], v[150:153], v[70:73]\n"
        "s_nop 0\n"
        "v_cvt_pk_bf16_f32 v107, v102, v64\n"
        "v_mfma_f32_16x16x32_bf16 v[70:73], v[184:187], v[94:97], v[70:73]\n"
        "v_mfma_f32_16x16x32_bf16 v[64:67], v[184:187], v[98:101], 0\n"
        "v_mfma_f32_16x16x32_bf16 v[64:67], v[172:175], v[158:161], v[64:67]\n"
        "s_add_u32 s46, s89, 590016\n"
        "v_mfma_f32_16x16x32_bf16 v[64:67], v[168:171], v[154:157], v[64:67]\n"
        "s_nop 2\n"
        "s_min_u32 s46, s46, s42\n"
        "s_nop 3\n"
        "v_cvt_pk_bf16_f32 v108, v70, v64\n"
        "buffer_store_dwordx3 v[106:108], v17, s[44:47], s90 offen offset:1152 sc1 nt\n"
        "s_nop 15\n"
        "s_cmp_lt_i32 s85, -1\n"
        "s_cbranch_scc1 FIN_%=\n"
        "s_mov_b32 s89, 0x80000000\n"
        "s_waitcnt vmcnt(0)\n"
        "ds_read_b128 v[104:107], v9\n"
        "ds_read_b128 v[110:113], v10\n"
        "ds_read_b128 v[116:119], v11\n"
        "ds_read_b128 v[124:127], v9 offset:3072\n"
        "ds_read_b128 v[130:133], v10 offset:3072\n"
        "ds_read_b128 v[136:139], v11 offset:3072\n"
        "ds_read_b128 v[144:147], v9 offset:6144\n"
        "ds_read_b128 v[150:153], v10 offset:6144\n"
        "ds_read_b128 v[156:159], v11 offset:6144\n"
        "s_waitcnt lgkmcnt(0)\n"
        "s_nop 7\n"
        "s_cmp_ge_i32 s85, 0\n"
        "s_cbranch_scc1 TOP_%=\n"
        "s_branch LAST_%=\n"
        "SP4_%=:\n"
        "s_sleep 1\n"
        "ds_read_u16 v72, v19 offset:3072\n"
        "s_waitcnt lgkmcnt(0)\n"
        "v_cmp_ne_u32_e64 s[24:25], s30, v72\n"
        "s_nop 4\n"
        "s_cmp_eq_u64 s[24:25], 0\n"
        "s_cbranch_scc1 BK4_%=\n"
        "s_sub_u32 s31, s31, 1\n"
        "s_cbranch_scc0 SP4_%=\n"
        "s_endpgm\n"
        "SP8_%=:\n"
        "s_sleep 1\n"
        "ds_read_u16 v148, v19 offset:3584\n"
        "s_waitcnt lgkmcnt(0)\n"
        "v_cmp_ne_u32_e64 s[24:25], s29, v148\n"
        "s_nop 4\n"
        "s_cmp_eq_u64 s[24:25], 0\n"
        "s_cbranch_scc1 BK8_%=\n"
        "s_sub_u32 s31, s31, 1\n"
        "s_cbranch_scc0 SP8_%=\n"
        "s_endpgm\n"
        "SP17_%=:\n"
        "s_sleep 1\n"
        "ds_read_u16 v60, v19 offset:3072\n"
        "s_waitcnt lgkmcnt(0)\n"
        "v_cmp_ne_u32_e64 s[24:25], s30, v60\n"
        "s_nop 4\n"
        "s_cmp_eq_u64 s[24:25], 0\n"
        "s_cbranch_scc1 BK17_%=\n"
        "s_sub_u32 s31, s31, 1\n"
        "s_cbranch_scc0 SP17_%=\n"
        "s_endpgm\n"
        :: "{v[0:15]}"(c0), "{v[16:23]}"(c1), "{v[24:27]}"(c2), "{v[200:207]}"(sp), "{v[28:43]}"(f0), "{s[36:39]}"(srdx), "{s[40:43]}"(srdo), "{s[48:55]}"(si)
        : "v44", "v45", "v46", "v47", "v48", "v49", "v50", "v51", "v52", "v53", "v54", "v55", "v56", "v57", "v58", "v59", "v60", "v61", "v62", "v63", "v64", "v65", "v66", "v67", "v68", "v69", "v70", "v71", "v72", "v73", "v74", "v75", "v76", "v77", "v78", "v79", "v80", "v81", "v82", "v83", "v84", "v85", "v86", "v87", "v88", "v89", "v90", "v91", "v92", "v93", "v94", "v95", "v96", "v97", "v98", "v99", "v100", "v101", "v102", "v103", "v104", "v105", "v106", "v107", "v108", "v109", "v110", "v111", "v112", "v113", "v114", "v115", "v116", "v117", "v118", "v119", "v120", "v121", "v122", "v123", "v124", "v125", "v126", "v127", "v128", "v129", "v130", "v131", "v132", "v133", "v134", "v135", "v136", "v137", "v138", "v139", "v140", "v141", "v142", "v143", "v144", "v145", "v146", "v147", "v148", "v149", "v150", "v151", "v152", "v153", "v154", "v155", "v156", "v157", "v158", "v159", "v160", "v161", "v162", "v163", "v164", "v165", "v166", "v167", "v168", "v169", "v170", "v171", "v172", "v173", "v174", "v175", "v176", "v177", "v178", "v179", "v180", "v181", "v182", "v183", "v184", "v185", "v186", "v187", "v188", "v189", "v190", "v191", "v192", "v193", "v194", "v195", "v196", "v197", "v198", "v199", "v208", "v209", "v210", "v211", "v212", "v213", "v214", "v215", "v216", "v217", "v218", "v219", "v220", "v221", "v222", "v223", "v224", "v225", "v226", "v227", "v228", "v229", "v230", "v231", "v232", "v233", "v234", "v235", "v236", "v237", "v238", "v239", "v240", "v241", "v242", "v243", "v244", "v245", "v246", "v247", "v248", "v249", "v250", "v251", "v252", "v253", "v254", "v255", "a0", "a1", "a2", "a3", "a4", "a5", "a6", "a7", "a8", "a9", "a10", "a11", "a12", "a13", "a14", "a15", "a16", "a17", "a18", "a19", "a20", "a21", "a22", "a23", "a24", "a25", "a26", "a27", "a28", "a29", "a30", "a31", "a32", "a33", "a34", "a35", "a36", "a37", "a38", "a39", "a40", "a41", "a42", "a43", "a44", "a45", "a46", "a47", "a48", "a49", "a50", "a51", "a52", "a53", "a54", "a55", "a56", "a57", "a58", "a59", "a60", "a61", "a62", "a63", "a64", "a65", "a66", "a67", "a68", "a69", "a70", "a71", "a72", "a73", "a74", "a75", "a76", "a77", "a78", "a79", "a80", "a81", "a82", "a83", "a84", "a85", "a86", "a87", "a88", "a89", "a90", "a91", "a92", "a93", "a94", "a95", "a96", "a97", "a98", "a99", "a100", "a101", "a102", "a103", "a104", "a105", "a106", "a107", "a108", "a109", "a110", "a111", "a112", "a113", "a114", "a115", "a116", "a117", "a118", "a119", "a120", "a121", "a122", "a123", "a124", "a125", "a126", "a127", "a128", "a129", "a130", "a131", "a132", "a133", "a134", "a135", "a136", "a137", "a138", "a139", "a140", "a141", "a142", "a143", "a144", "a145", "a146", "a147", "a148", "a149", "a150", "a151", "a152", "a153", "a154", "a155", "a156", "a157", "a158", "a159", "a160", "a161", "a162", "a163", "a164", "a165", "a166", "a167", "a168", "a169", "a170", "a171", "a172", "a173", "a174", "a175", "a176", "a177", "a178", "a179", "a180", "a181", "a182", "a183", "a184", "a185", "a186", "a187", "a188", "a189", "a190", "a191", "a192", "a193", "a194", "a195", "a196", "a197", "a198", "a199", "a200", "a201", "a202", "a203", "a204", "a205", "a206", "a207", "a208", "a209", "a210", "a211", "a212", "a213", "a214", "a215", "a216", "a217", "a218", "a219", "a220", "a221", "a222", "a223", "a224", "a225", "a226", "a227", "a228", "a229", "a230", "a231", "a232", "a233", "a234", "a235", "a236", "a237", "a238", "a239", "a240", "a241", "a242", "a243", "a244", "a245", "a246", "a247", "a248", "a249", "a250", "a251", "a252", "a253", "a254", "a255", "s24", "s25", "s26", "s27", "s28", "s29", "s30", "s31", "s44", "s45", "s46", "s47", "s56", "s57", "s58", "s59", "s60", "s61", "s62", "s63", "s64", "s65", "s66", "s67", "s68", "s69", "s70", "s71", "s72", "s73", "s74", "s75", "s76", "s77", "s78", "s79", "s80", "s81", "s82", "s83", "s84", "s85", "s86", "s87", "s88", "s89", "s90", "s91", "s92", "s93", "s94", "s95", "vcc", "scc", "memory");
}

extern "C" void kernel_launch(void* const* d_in, const int* in_sizes, int n_in, void* d_out, int out_size, void* d_ws, size_t ws_size, hipStream_t stream) {
    (void)in_sizes; (void)n_in; (void)out_size; (void)ws_size;
    (void)hipFuncSetAttribute((const void*)swin_kernel, hipFuncAttributeMaxDynamicSharedMemorySize, LDS_BYTES);
    swin_kernel<<<GRID, 256, LDS_BYTES, stream>>>((const char*)d_in[0], (const char*)d_in[1], (const char*)d_in[2], (const char*)d_in[3], (const char*)d_in[4], (char*)d_out, (const char*)d_ws);
}
```

```cpp
#define NLC 3
#define WSTEP 32
#define CPL 8
#include <hip/hip_runtime.h>
#include <hip/hip_bf16.h>

#define DI __device__ __forceinline__

using u32x2 = __attribute__((ext_vector_type(2))) unsigned;
using u32x4 = __attribute__((ext_vector_type(4))) unsigned;
using u32x8 = __attribute__((ext_vector_type(8))) unsigned;
using u32x16 = __attribute__((ext_vector_type(16))) unsigned;

constexpr int HH = 511, WW = 511, NWX = 73, NWIN = NWX * NWX, GRID = 256;
constexpr int TOKB = 192, ROWB = WW * TOKB;
constexpr unsigned FRAMEB = (unsigned)HH * ROWB, TOTALB = FRAMEB * 4;
constexpr unsigned FS = 9232, L_BUF = 0, L_STG = 4 * FS, REC = 784, NREC = 21, L_EXT = L_STG + 4 * 9216, L_MS = L_EXT + 4 * NREC * REC, L_STA = L_MS + 18432, L_PSC = L_STA + 512,
                   L_STB = L_PSC + 512, L_AUX = L_STB + 512, L_SLW = L_AUX + 1024, L_DUM = L_SLW + 1024, L_FA = L_DUM + 512, LDS_BYTES = 163840;
constexpr unsigned RAWM = 18432, L_GT = L_AUX + 768;
static_assert(L_EXT + 3 * RAWM <= L_MS && ((L_STG >> 10) & 3) == 0 && (L_STG & 1023) < 512 && L_FA + 1024 <= LDS_BYTES && (NWIN + GRID - 1) / GRID <= NREC, "LDS");

#define LP(T, a) ((__attribute__((address_space(3))) T*)(size_t)(unsigned)(a))
#define SB() __builtin_amdgcn_sched_barrier(0)
#define PIN(v) asm volatile("" : "+v"(v))
DI unsigned raw_lane(unsigned lane, unsigned m, unsigned A, unsigned B, unsigned C) {
    const unsigned n = 64 * m + lane, row = (n * 171) >> 11, pc = n - 12 * row;
    return (row >> 3) * (A & 0xffff) + ((row >> 1) & 3) * (B & 0xffff) + (row & 1) * (C & 0xffff) + (pc ^ ((row >> 2) & 3)) * 16 - 1024 * m;
}
DI u32x4 make_srd(unsigned lo, unsigned hi, unsigned bytes) { return u32x4{lo, hi & 0xffffu, bytes, 0x00020000u}; }
DI void raw_dma(unsigned v0, unsigned v1, unsigned v2, const u32x4& srd, unsigned so, unsigned dst) {
    asm volatile("s_mov_b32 m0, %5\n\ts_nop 0\n\tbuffer_load_dwordx4 %0, %3, %4 offen lds\n\tbuffer_load_dwordx4 %1, %3, %4 offen offset:1024 lds\n\tbuffer_load_dwordx4 %2, %3, %4 offen offset:2048 lds"
                 :: "v"(v0), "v"(v1), "v"(v2), "s"(srd), "s"(so), "s"(dst) : "memory");
}

__global__ __launch_bounds__(256, 1) void swin_kernel(const char* __restrict__ x_, const char* __restrict__ wqkv_t, const char* __restrict__ wo_t, const char* __restrict__ wqkv_s,
                                                           const char* __restrict__ wo_s, char* __restrict__ out_, const char* wf) {
    const unsigned k = __builtin_amdgcn_readfirstlane(threadIdx.x >> 6) & 3, lane = threadIdx.x & 63, k1 = k & 1, k2 = k >> 1;
    u32x2 kx, ko, kl, kr, kp;
    const unsigned long long kap = (unsigned long long)__builtin_amdgcn_kernarg_segment_ptr();
    asm volatile("s_load_dwordx2 %0, %4, %5\n\ts_load_dwordx2 %1, %4, %6\n\ts_load_dwordx2 %2, %4, 0x0\n\ts_load_dwordx2 %3, %4, 0x28"
                 : "=&s"(kr), "=&s"(kl), "=&s"(kx), "=&s"(ko) : "s"(kap), "s"(8 + 8 * k), "s"(8 + 16 * k2));
    SB();
    {
        unsigned long long pc; unsigned co = ((((blockIdx.x >> 3) * 4 + k) * CPL + lane / (64 / CPL)) & 511) * 128;
        asm volatile("s_getpc_b64 %0\n.Lswin_pc:\n\ts_mov_b32 m0, %2\n\tv_min_u32 %1, .Lswin_fin-.Lswin_pc, %1\n\tglobal_load_lds_dword %1, %0" : "=&s"(pc), "+v"(co) : "s"(L_DUM + 256) : "memory");
    }
    SB();
#define TS(i)
    const unsigned Bl = 1152 + 2304 * k2, Ql = 4608 - 3456 * k2;
    const unsigned Ar = 13824 - 12288 * k1, Br = k1 ? 384 : Bl, Cr = 576 - 384 * k1, Pr = 27648 - 24576 * k1, Qr = k1 ? 6144 : Ql, cr = 192 - 192 * k1, szr = 55296 - 36864 * k1;
    const unsigned ldr = L_STG + k * RAWM + k2 * RAWM, ldl = k1 * RAWM + k2 * (L_MS - k1 * (L_MS - L_EXT + RAWM));
    static_assert(L_STG + 2 * RAWM == L_EXT, "");
    unsigned vr[3];
#pragma unroll
    for (int m = 0; m < 3; ++m) { vr[m] = raw_lane(lane, m, Ar, Br, Cr); PIN(vr[m]); }
    unsigned vW = (((blockIdx.x >> 3) & 31) * 14 + lane) * 128 + (lane >= 14) * 0x40000000u; PIN(vW);
    SB();
    asm volatile("s_waitcnt lgkmcnt(0)" : "+s"(kr), "+s"(kl), "+s"(kx), "+s"(ko));
    const u32x4 srdr = make_srd(kr[0], kr[1], szr);
    asm volatile("s_mov_b32 m0, %2\n\ts_nop 0\n\tbuffer_load_dword %0, %1, 0 offen lds" :: "v"(vW), "s"(srdr), "s"(L_DUM) : "memory");
#define RDMA(n) do { SB(); raw_dma(vr[0], vr[1], vr[2], srdr, cr + (n & 1) * Pr + (n >> 1) * Qr, ldr + n * 3072); SB(); } while (0)
    RDMA(0); RDMA(1);
    TS(1);
    const u32x4 srdl = make_srd(kl[0], kl[1], 55296), srdx = make_srd(kx[0], kx[1], TOTALB), srdo = make_srd(ko[0], ko[1], TOTALB);
    const unsigned vb = (blockIdx.x & 7) * (GRID / 8) + (blockIdx.x >> 3);
    const unsigned grp = vb / WSTEP, w0 = (grp * NWIN) / (GRID / WSTEP) + vb % WSTEP, nwin = (((grp + 1) * NWIN) / (GRID / WSTEP) - w0 + WSTEP - 1) / WSTEP, wy0 = w0 / NWX, wx0 = w0 - wy0 * NWX;
    const unsigned base0 = wy0 * 7 * ROWB + wx0 * 7 * TOKB;
#define SLOT(n) do { constexpr int a_ = (4 + NLC) * n / 10, b_ = (4 + NLC) * (n + 1) / 10; if (b_ > a_) { if (a_ < 4) RDMA((a_ + 2)); else LDMA((a_ - 4)); } } while (0)
    const unsigned i = lane & 15, g = lane >> 4, p = i >> 2, t = i & 3, cl = 384 * k1;
    u32x16 c0, f0; u32x8 c1, sp; u32x4 c2;
    const unsigned stg = L_STG + k * 9216, ext = L_EXT + k * (NREC * REC);
    unsigned vl[3];
#pragma unroll
    for (int m = 0; m < 3; ++m) { vl[m] = raw_lane(lane, m, 13824, Bl, 576); PIN(vl[m]); }
#define LDMA(n) do { SB(); raw_dma(vl[0], vl[1], vl[2], srdl, cl + (n & 1) * 27648 + (n >> 1) * Ql, ldl + n * 3072); SB(); } while (0)
    SLOT(0); SLOT(1);
#pragma unroll
    for (int a = 0; a < 2; ++a) {
        const unsigned ra = (8 * p + 4 * a + t) * 192 + ((g ^ ((2 * p + a) & 3)) * 16);
        f0[a] = ldl + ra; f0[2 + a] = ldr + ra; f0[4 + a] = ldr + (8 * g + 4 * a + p) * 192 + ((t ^ ((2 * g + a) & 3)) * 16);
        PIN(f0[a]); PIN(f0[2 + a]); PIN(f0[4 + a]);
    }
    SLOT(2);
#pragma unroll
    for (int a = 0; a < 2; ++a) {
        const unsigned n = 16 * a + 4 * k + p;
        const unsigned w = w0 + n * WSTEP;
        f0[6 + a] = n < nwin ? t * FRAMEB + w * (7 * TOKB) + ((w * 57457) >> 22) * (7 * ROWB - NWX * 7 * TOKB) + 6 * ROWB + 6 * TOKB + g * 48 : 0x80000000u; PIN(f0[6 + a]);
    }
    f0[8] = i == 0 ? L_AUX + 192 + g * 16 : L_DUM; f0[15] = 0;
    SLOT(3);
    {
        const unsigned sh = 2 - 2 * k2, ts = 3072 + 16 * k2, ml = 64 - 48 * k2, xm = 64 * (k == 1), a0 = ldl + g * 256 + (k == 1 ? t * 16 : k1 ? 0 : (i ^ ((g & k & 2) << 2)) * 16);
#pragma unroll
        for (int n = 0; n < 6; ++n) {
            const unsigned z = (32 * (n >> 1) + 4 * (n & 1) + 8 * p + t) >> sh;
            const unsigned z6 = (z * 43) >> 8, tl = z - 6 * z6;
            f0[9 + n] = a0 + (tl * ts + ((z6 * ml) ^ ((tl << 5) & xm))) * k1 + n * 3072 * (1 - k1); PIN(f0[9 + n]);
            if (n == 2) SLOT(4);
        }
    }
    SLOT(5);
#pragma unroll
    for (int m = 0; m < 3; ++m) {
        const unsigned n = 64 * m + lane, tf = (n * 171) >> 13, r = n - 48 * tf, q = (r * 43) >> 9;
        const unsigned c = tf * FRAMEB + ((r - 12 * q) ^ tf) * 16;
#pragma unroll
        for (int u = 0; u < 3; ++u) {
            const unsigned px = 16 * u + 4 * k + q;
            c0[3 * u + m] = c + px * TOKB + ((px * 37) >> 8) * (ROWB - 7 * TOKB) - 1024 * ((3 * u + m) % 4); PIN(c0[3 * u + m]);
        }
        if (m == 1) SLOT(6);
    }
#pragma unroll
    for (int S = 0; S < 3; ++S) c0[9 + S] = stg + t * 768 + p * TOKB + ((3 * g + S) ^ t) * 16;
    c0[12] = L_BUF + t * FS + g * 256 + p * 64 + k * 16;
    c0[13] = L_BUF + k * FS + lane * 16;
    const unsigned vs = g * TOKB + 12 * i, brk = ROWB - 7 * TOKB, u = (p - g) & 3;
    c0[14] = vs; c0[15] = vs + (g >= 3 ? brk : 0);
    PIN(c0);
    SLOT(7);
    c1[0] = vs + (g >= 2 ? brk : 0); c1[1] = vs + (g >= 1 ? brk : 0);
    c1[2] = u < 3 ? L_STA + t * 128 + g * 32 + u * 8 + k * 2 : L_DUM;
    c1[3] = L_STB + t * 128 + (4 * g + k) * 8 + u * 2;
    c1[4] = L_STA + k * 128 + g * 32;
    c1[5] = L_STB + k * 128 + i * 8;
    c1[6] = L_STA + k * 128 + p * 32 + g * 8 + t * 2;
    c1[7] = ext - REC + (i & 1) * 192 + g * 16;
    c2[0] = ext - REC + 384 + 24 * i;
    c2[1] = p == g ? ~0u : 0u;
    c2[2] = L_AUX + (t < 2 ? t : 2) * 192 + g * 16;
    c2[3] = p == g ? L_AUX + 576 + t * 16 : L_AUX + 384;
    PIN(c1); PIN(c2);
    SLOT(8);
    sp[0] = lane * 16; sp[1] = L_MS + (16 * t + ((4 * g + p) ^ ((t & 2) << 2))) * 16;
#pragma unroll
    for (int T = 0; T < 2; ++T) {
        const unsigned n = 16 * T + 4 * t + p;
        sp[2 + T] = n < NREC ? ext + n * REC + g * 16 : L_DUM; sp[4 + T] = n < NREC && p == g ? ext + n * REC + 768 : L_DUM;
    }
    sp[6] = ext + g * REC + 384 + 24 * i; sp[7] = g == 0 ? ext + 20 * REC + 384 + 24 * i : L_DUM;
    PIN(sp);
    SLOT(9);
    {
        const unsigned e = 64 * k + lane, y = e - 144, one = (y < 16 ? (y >> 2) & 1 : y >> 5) ? 0x3f800000u : 0x3f80u;
        *LP(unsigned, L_AUX + e * 4) = e < 48 ? 0x3f803f80u : e == 192 ? 0x46800000u : y < 48 && t == (y < 16 ? y >> 3 : (y >> 2) & 3) ? one : 0u;
        *LP(unsigned, L_STA + k * 128 + (lane & 6) * 16 + 24 + (lane & 1) * 4) = 0x3c003c00u;
        *LP(unsigned, L_FA + e * 4) = 0u;
    }
    const u32x4 f1 = {vl[0], vl[1], vl[2], 0u}, sol = {cl + Ql, cl + Ql + 27648, cl + 2 * Ql, cl + 2 * Ql + 27648};
    const u32x8 si = {(unsigned)nwin, base0, (unsigned)wx0, k * FRAMEB, stg, ldl, 0u, 0u};
    asm volatile(
        "s_bitcmp1_b32 s52, 10\n"
        "s_cbranch_scc1 FV_%=\n"
        "s_mov_b32 s24, 0x3e16c740\n"
        "v_mov_b32 v48, 0x3f803f80\n"
        "v_mov_b32 v49, 0x3f803f80\n"
        "v_mov_b32 v50, 0x3f803f80\n"
        "v_mov_b32 v51, 0x3f803f80\n"
        "s_waitcnt vmcnt(9)\n"
        "ds_read_b64_tr_b16 a[72:73], v32 offset:0\n"
        "ds_read_b64_tr_b16 a[74:75], v33 offset:0\n"
        "ds_read_b64_tr_b16 a[76:77], v32 offset:6144\n"
        "ds_read_b64_tr_b16 a[78:79], v33 offset:6144\n"
        "ds_read_b64_tr_b16 a[80:81], v32 offset:12288\n"
        "ds_read_b64_tr_b16 a[82:83], v33 offset:12288\n"
        "s_add_u32 m0, s53, 9216\n"
        "s_nop 0\n"
        "buffer_load_dwordx4 v44, s[16:19], s21 offen lds\n"
        "ds_read_b64_tr_b16 a[84:85], v32 offset:8\n"
        "ds_read_b64_tr_b16 a[86:87], v33 offset:8\n"
        "ds_read_b64_tr_b16 a[88:89], v32 offset:6152\n"
        "ds_read_b64_tr_b16 a[90:91], v33 offset:6152\n"
        "ds_read_b64_tr_b16 a[92:93], v32 offset:12296\n"
        "ds_read_b64_tr_b16 a[94:95], v33 offset:12296\n"
        "buffer_load_dwordx4 v45, s[16:19], s21 offen offset:1024 lds\n"
        "ds_read_b64_tr_b16 a[96:97], v32 offset:64\n"
        "ds_read_b64_tr_b16 a[98:99], v33 offset:64\n"
        "ds_read_b64_tr_b16 a[100:101], v32 offset:6208\n"
        "ds_read_b64_tr_b16 a[102:103], v33 offset:6208\n"
        "ds_read_b64_tr_b16 a[104:105], v32 offset:12352\n"
        "ds_read_b64_tr_b16 a[106:107], v33 offset:12352\n"
        "buffer_load_dwordx4 v46, s[16:19], s21 offen offset:2048 lds\n"
        "ds_read_b64_tr_b16 a[108:109], v32 offset:72\n"
        "ds_read_b64_tr_b16 a[110:111], v33 offset:72\n"
        "ds_read_b64_tr_b16 a[112:113], v32 offset:6216\n"
        "ds_read_b64_tr_b16 a[114:115], v33 offset:6216\n"
        "ds_read_b64_tr_b16 a[116:117], v32 offset:12360\n"
        "ds_read_b64_tr_b16 a[118:119], v33 offset:12360\n"
        "s_add_u32 m0, s53, 12288\n"
        "s_nop 0\n"
        "buffer_load_dwordx4 v44, s[16:19], s22 offen lds\n"
        "ds_read_b64_tr_b16 a[120:121], v32 offset:128\n"
        "ds_read_b64_tr_b16 a[122:123], v33 offset:128\n"
        "ds_read_b64_tr_b16 a[124:125], v32 offset:6272\n"
        "ds_read_b64_tr_b16 a[126:127], v33 offset:6272\n"
        "ds_read_b64_tr_b16 a[128:129], v32 offset:12416\n"
        "ds_read_b64_tr_b16 a[130:131], v33 offset:12416\n"
        "buffer_load_dwordx4 v45, s[16:19], s22 offen offset:1024 lds\n"
        "ds_read_b64_tr_b16 a[132:133], v32 offset:136\n"
        "ds_read_b64_tr_b16 a[134:135], v33 offset:136\n"
        "ds_read_b64_tr_b16 a[136:137], v32 offset:6280\n"
        "ds_read_b64_tr_b16 a[138:139], v33 offset:6280\n"
        "ds_read_b64_tr_b16 a[140:141], v32 offset:12424\n"
        "ds_read_b64_tr_b16 a[142:143], v33 offset:12424\n"
        "buffer_load_dwordx4 v46, s[16:19], s22 offen offset:2048 lds\n"
        "s_waitcnt lgkmcnt(15)\n"
        "v_mfma_f32_16x16x32_bf16 v[52:55], a[72:75], v[48:51], 0\n"
        "s_add_u32 m0, s53, 15360\n"
        "s_waitcnt vmcnt(9)\n"
        "s_waitcnt vmcnt(3)\n"
        "v_mfma_f32_16x16x32_bf16 v[52:55], a[76:79], v[48:51], v[52:55]\n"
        "buffer_load_dwordx4 v44, s[16:19], s23 offen lds\n"
        "v_mfma_f32_16x16x32_bf16 v[52:55], a[80:83], v[48:51], v[52:55]\n"
        "s_waitcnt lgkmcnt(15)\n"
        "v_mfma_f32_16x16x32_bf16 v[56:59], a[92:95], v[48:51], 0\n"
        "s_nop 2\n"
        "v_mfma_f32_16x16x32_bf16 v[56:59], a[88:91], v[48:51], v[56:59]\n"
        "s_waitcnt lgkmcnt(15)\n"
        "buffer_load_dwordx4 v45, s[16:19], s23 offen offset:1024 lds\n"
        "v_mul_f32 v52, 0xbc2aaaab, v52\n"
        "v_mul_f32 v53, 0xbc2aaaab, v53\n"
        "v_mfma_f32_16x16x32_bf16 v[56:59], a[84:87], v[48:51], v[56:59]\n"
        "s_waitcnt lgkmcnt(12)\n"
        "v_mul_f32 v54, 0xbc2aaaab, v54\n"
        "v_mul_f32 v55, 0xbc2aaaab, v55\n"
        "v_cvt_pk_bf16_f32 v44, v52, v53\n"
        "v_mfma_f32_16x16x32_bf16 v[60:63], a[96:99], v[48:51], 0\n"
        "s_nop 0\n"
        "ds_read_b128 a[0:3], v30\n"
        "v_cvt_pk_bf16_f32 v45, v54, v55\n"
        "v_mul_f32 v56, 0xbc2aaaab, v56\n"
        "v_mfma_f32_16x16x32_bf16 v[60:63], a[100:103], v[48:51], v[60:63]\n"
        "s_waitcnt lgkmcnt(7)\n"
        "ds_read_b128 a[4:7], v30 offset:64\n"
        "v_mul_f32 v57, 0xbc2aaaab, v57\n"
        "v_mul_f32 v58, 0xbc2aaaab, v58\n"
        "v_mfma_f32_16x16x32_bf16 v[60:63], a[104:107], v[48:51], v[60:63]\n"
        "s_waitcnt lgkmcnt(2)\n"
        "buffer_load_dwordx4 v46, s[16:19], s23 offen offset:2048 lds\n"
        "v_mul_f32 v59, 0xbc2aaaab, v59\n"
        "v_cvt_pk_bf16_f32 v46, v56, v57\n"
        "v_mfma_f32_16x16x32_bf16 v[52:55], a[116:119], v[48:51], 0\n"
        "s_waitcnt vmcnt(0)\n"
        "ds_read_b128 a[8:11], v30 offset:128\n"
        "v_cvt_pk_bf16_f32 v47, v58, v59\n"
        "v_mul_f32 v60, 0xbc2aaaab, v60\n"
        "v_mfma_f32_16x16x32_bf16 v[52:55], a[112:115], v[48:51], v[52:55]\n"
        "ds_read_b128 a[144:147], v28\n"
        "ds_read_b128 a[148:151], v28 offset:64\n"
        "v_mul_f32 v61, 0xbc2aaaab, v61\n"
        "v_mfma_f32_16x16x32_bf16 v[52:55], a[108:111], v[48:51], v[52:55]\n"
        "ds_read_b128 a[152:155], v28 offset:128\n"
        "v_mul_f32 v62, 0xbc2aaaab, v62\n"
        "v_mul_f32 v63, 0xbc2aaaab, v63\n"
        "v_mfma_f32_16x16x32_bf16 v[56:59], a[120:123], v[48:51], 0\n"
        "ds_read_b128 a[156:159], v29\n"
        "v_cvt_pk_bf16_f32 v64, v60, v61\n"
        "v_cvt_pk_bf16_f32 v65, v62, v63\n"
        "v_mfma_f32_16x16x32_bf16 v[56:59], a[124:127], v[48:51], v[56:59]\n"
        "ds_read_b128 a[160:163], v29 offset:64\n"
        "v_mul_f32 v52, 0xbc2aaaab, v52\n"
        "v_mul_f32 v53, 0xbc2aaaab, v53\n"
        "v_mfma_f32_16x16x32_bf16 v[56:59], a[128:131], v[48:51], v[56:59]\n"
        "ds_read_b128 a[164:167], v29 offset:128\n"
        "v_mul_f32 v54, 0xbc2aaaab, v54\n"
        "v_mul_f32 v55, 0xbc2aaaab, v55\n"
        "v_mfma_f32_16x16x32_bf16 v[60:63], a[140:143], v[48:51], 0\n"
        "ds_read_b128 a[168:171], v28 offset:6144\n"
        "v_cvt_pk_bf16_f32 v66, v52, v53\n"
        "v_cvt_pk_bf16_f32 v67, v54, v55\n"
        "v_mfma_f32_16x16x32_bf16 v[60:63], a[136:139], v[48:51], v[60:63]\n"
        "ds_read_b128 a[172:175], v28 offset:6208\n"
        "v_mul_f32 v56, 0xbc2aaaab, v56\n"
        "v_mul_f32 v57, 0xbc2aaaab, v57\n"
        "v_mfma_f32_16x16x32_bf16 v[60:63], a[132:135], v[48:51], v[60:63]\n"
        "ds_read_b128 a[176:179], v28 offset:6272\n"
        "v_mul_f32 v58, 0xbc2aaaab, v58\n"
        "v_mul_f32 v59, 0xbc2aaaab, v59\n"
        "s_waitcnt lgkmcnt(6)\n"
        "v_mfma_f32_16x16x32_bf16 v[48:51], a[144:147], v[44:47], 0\n"
        "s_waitcnt lgkmcnt(6)\n"
        "ds_read_b128 a[180:183], v29 offset:6144\n"
        "v_cvt_pk_bf16_f32 v52, v56, v57\n"
        "v_cvt_pk_bf16_f32 v53, v58, v59\n"
        "v_mfma_f32_16x16x32_bf16 v[48:51], a[148:151], v[64:67], v[48:51]\n"
        "ds_read_b128 a[184:187], v29 offset:6208\n"
        "v_mul_f32 v60, 0xbc2aaaab, v60\n"
        "v_mul_f32 v61, 0xbc2aaaab, v61\n"
        "v_cvt_pk_bf16_f32 v54, v60, v61\n"
        "v_mul_f32 v62, 0xbc2aaaab, v62\n"
        "v_mul_f32 v63, 0xbc2aaaab, v63\n"
        "v_cvt_pk_bf16_f32 v55, v62, v63\n"
        "s_nop 1\n"
        "v_mfma_f32_16x16x32_bf16 v[48:51], a[152:155], v[52:55], v[48:51]\n"
        "s_waitcnt lgkmcnt(5)\n"
        "ds_read_b128 a[188:191], v29 offset:6272\n"
        "v_mfma_f32_16x16x32_bf16 v[56:59], a[152:155], a[8:11], v[48:51]\n"
        "buffer_load_dwordx4 v[162:165], v34, s[36:39], 0 offen\n"
        "v_mfma_f32_16x16x32_bf16 v[56:59], a[148:151], a[4:7], v[56:59]\n"
        "ds_read_b128 a[192:195], v28 offset:12288\n"
        "ds_read_b128 a[196:199], v28 offset:12352\n"
        "v_mfma_f32_16x16x32_bf16 v[56:59], a[144:147], a[0:3], v[56:59]\n"
        "s_waitcnt lgkmcnt(5)\n"
        "ds_read_b128 a[200:203], v28 offset:12416\n"
        "v_mfma_f32_16x16x32_bf16 v[60:63], a[156:159], v[44:47], 0\n"
        "s_nop 0\n"
        "ds_read_b128 a[204:207], v29 offset:12288\n"
        "v_mfma_f32_16x16x32_bf16 v[60:63], a[160:163], v[64:67], v[60:63]\n"
        "s_nop 0\n"
        "ds_read_b128 a[208:211], v29 offset:12352\n"
        "v_add_f32 v104, v56, v57\n"
        "v_add_f32 v104, v104, v58\n"
        "v_mfma_f32_16x16x32_bf16 v[60:63], a[164:167], v[52:55], v[60:63]\n"
        "ds_read_b128 a[212:215], v29 offset:12416\n"
        "v_add_f32 v104, v104, v59\n"
        "v_mfma_f32_16x16x32_bf16 v[68:71], a[164:167], a[8:11], v[60:63]\n"
        "ds_read_b128 a[12:15], v31\n"
        "v_mfma_f32_16x16x32_bf16 v[68:71], a[160:163], a[4:7], v[68:71]\n"
        "ds_read_b128 a[16:19], v31 offset:64\n"
        "v_mfma_f32_16x16x32_bf16 v[68:71], a[156:159], a[0:3], v[68:71]\n"
        "ds_read_b128 a[20:23], v31 offset:128\n"
        "v_mfma_f32_16x16x32_bf16 v[72:75], a[168:171], v[44:47], 0\n"
        "s_waitcnt lgkmcnt(9)\n"
        "buffer_load_dwordx4 v[168:171], v34, s[36:39], 0 offen offset:16\n"
        "v_mfma_f32_16x16x32_bf16 v[72:75], a[172:175], v[64:67], v[72:75]\n"
        "s_mov_b32 s78, 0xf0000f\n"
        "ds_read_b128 a[24:27], v30 offset:6144\n"
        "ds_read_b128 a[28:31], v30 offset:6208\n"
        "v_add_f32 v104, v104, v68\n"
        "v_mfma_f32_16x16x32_bf16 v[72:75], a[176:179], v[52:55], v[72:75]\n"
        "s_mov_b32 s79, 0xf0000f00\n"
        "ds_read_b128 a[32:35], v30 offset:6272\n"
        "v_add_f32 v104, v104, v69\n"
        "v_add_f32 v104, v104, v70\n"
        "v_mfma_f32_16x16x32_bf16 v[76:79], a[176:179], a[8:11], v[72:75]\n"
        "s_waitcnt lgkmcnt(9)\n"
        "ds_read_b128 a[36:39], v31 offset:6144\n"
        "v_add_f32 v104, v104, v71\n"
        "v_mfma_f32_16x16x32_bf16 v[76:79], a[172:175], a[4:7], v[76:79]\n"
        "s_mov_b32 s56, 0x22222222\n"
        "ds_read_b128 a[40:43], v31 offset:6208\n"
        "v_mfma_f32_16x16x32_bf16 v[76:79], a[168:171], a[0:3], v[76:79]\n"
        "s_mov_b32 s57, 0x22222222\n"
        "ds_read_b128 a[44:47], v31 offset:6272\n"
        "v_mfma_f32_16x16x32_bf16 v[80:83], a[180:183], v[44:47], 0\n"
        "s_waitcnt lgkmcnt(9)\n"
        "ds_read_b128 a[48:51], v30 offset:12288\n"
        "v_mfma_f32_16x16x32_bf16 v[80:83], a[184:187], v[64:67], v[80:83]\n"
        "s_mov_b32 s58, 0x44444444\n"
        "ds_read_b128 a[52:55], v30 offset:12352\n"
        "v_add_f32 v104, v104, v76\n"
        "v_add_f32 v104, v104, v77\n"
        "v_mfma_f32_16x16x32_bf16 v[80:83], a[188:191], v[52:55], v[80:83]\n"
        "s_mov_b32 s59, 0x44444444\n"
        "ds_read_b128 a[56:59], v30 offset:12416\n"
        "v_add_f32 v104, v104, v78\n"
        "v_add_f32 v104, v104, v79\n"
        "v_mfma_f32_16x16x32_bf16 v[84:87], a[188:191], a[8:11], v[80:83]\n"
        "s_mov_b32 s60, 0x88888888\n"
        "buffer_load_dwordx4 v[174:177], v34, s[36:39], 0 offen offset:32\n"
        "v_mfma_f32_16x16x32_bf16 v[84:87], a[184:187], a[4:7], v[84:87]\n"
        "s_waitcnt lgkmcnt(9)\n"
        "ds_read_b128 a[60:63], v31 offset:12288\n"
        "ds_read_b128 a[64:67], v31 offset:12352\n"
        "v_mfma_f32_16x16x32_bf16 v[84:87], a[180:183], a[0:3], v[84:87]\n"
        "s_mov_b32 s61, 0x88888888\n"
        "ds_read_b128 a[68:71], v31 offset:12416\n"
        "v_mfma_f32_16x16x32_bf16 v[28:31], a[192:195], v[44:47], 0\n"
        "s_mov_b32 s62, 0xffff\n"
        "s_nop 1\n"
        "v_mfma_f32_16x16x32_bf16 v[28:31], a[196:199], v[64:67], v[28:31]\n"
        "s_mov_b32 s63, 0x0\n"
        "v_add_f32 v104, v104, v84\n"
        "v_add_f32 v104, v104, v85\n"
        "v_add_f32 v104, v104, v86\n"
        "v_mfma_f32_16x16x32_bf16 v[28:31], a[200:203], v[52:55], v[28:31]\n"
        "s_mov_b32 s64, 0xffff0000\n"
        "v_add_f32 v104, v104, v87\n"
        "v_mfma_f32_16x16x32_bf16 v[88:91], a[200:203], a[8:11], v[28:31]\n"
        "s_mov_b32 s65, 0x0\n"
        "v_mfma_f32_16x16x32_bf16 v[88:91], a[196:199], a[4:7], v[88:91]\n"
        "s_waitcnt lgkmcnt(9)\n"
        "v_mfma_f32_16x16x32_bf16 v[88:91], a[192:195], a[0:3], v[88:91]\n"
        "s_mov_b32 s66, 0x0\n"
        "buffer_load_dwordx4 v[182:185], v35, s[36:39], 0 offen\n"
        "v_mfma_f32_16x16x32_bf16 v[92:95], a[204:207], v[44:47], 0\n"
        "s_mov_b32 s67, 0xffff\n"
        "s_nop 1\n"
        "v_mfma_f32_16x16x32_bf16 v[92:95], a[208:211], v[64:67], v[92:95]\n"
        "s_mov_b32 s68, 0x0\n"
        "v_add_f32 v104, v104, v88\n"
        "v_add_f32 v104, v104, v89\n"
        "v_add_f32 v104, v104, v90\n"
        "v_mfma_f32_16x16x32_bf16 v[92:95], a[212:215], v[52:55], v[92:95]\n"
        "s_mov_b32 s69, 0xffff0000\n"
        "v_add_f32 v104, v104, v91\n"
        "v_mfma_f32_16x16x32_bf16 v[44:47], a[212:215], a[8:11], v[92:95]\n"
        "s_waitcnt lgkmcnt(0)\n"
        "v_mfma_f32_16x16x32_bf16 v[44:47], a[208:211], a[4:7], v[44:47]\n"
        "s_mov_b32 s70, 0x0\n"
        "v_mfma_f32_16x16x32_bf16 v[44:47], a[204:207], a[0:3], v[44:47]\n"
        "s_mov_b32 s71, 0xffffffff\n"
        "s_nop 0\n"
        "v_mfma_f32_16x16x32_bf16 v[52:55], a[144:147], a[12:15], v[48:51]\n"
        "s_mov_b32 s72, 0x0\n"
        "s_nop 1\n"
        "v_mfma_f32_16x16x32_bf16 v[52:55], a[148:151], a[16:19], v[52:55]\n"
        "s_mov_b32 s73, 0x20000\n"
        "v_add_f32 v104, v104, v44\n"
        "v_add_f32 v104, v104, v45\n"
        "v_add_f32 v104, v104, v46\n"
        "v_mfma_f32_16x16x32_bf16 v[52:55], a[152:155], a[20:23], v[52:55]\n"
        "s_barrier\n"
        "buffer_load_dwordx4 v[188:191], v35, s[36:39], 0 offen offset:16\n"
        "v_add_f32 v104, v104, v47\n"
        "v_mov_b32 v105, v104\n"
        "v_mfma_f32_16x16x32_bf16 v[64:67], a[164:167], a[20:23], v[60:63]\n"
        "s_mov_b32 s76, 0x3c003c00\n"
        "v_permlane32_swap_b32 v104, v105\n"
        "v_add_f32 v104, v104, v105\n"
        "v_mfma_f32_16x16x32_bf16 v[64:67], a[160:163], a[16:19], v[64:67]\n"
        "s_mov_b32 s77, 0x3c003c00\n"
        "v_mov_b32 v105, v104\n"
        "v_add_f32 v36, v52, v53\n"
        "v_add_f32 v36, v36, v54\n"
        "v_mfma_f32_16x16x32_bf16 v[64:67], a[156:159], a[12:15], v[64:67]\n"
        "s_mov_b64 vcc, s[78:79]\n"
        "v_permlane16_swap_b32 v104, v105\n"
        "v_add_f32 v104, v104, v105\n"
        "v_mfma_f32_16x16x32_bf16 v[96:99], a[168:171], a[12:15], v[72:75]\n"
        "s_mov_b32 s80, 0x3c2aaaab\n"
        "v_mul_f32 v104, 0xbac909aa, v104\n"
        "v_add_f32 v36, v36, v55\n"
        "v_fma_f32 v56, v56, s24, v104\n"
        "v_mfma_f32_16x16x32_bf16 v[96:99], a[172:175], a[16:19], v[96:99]\n"
        "s_mov_b32 s81, 0x3727c5ac\n"
        "v_fma_f32 v57, v57, s24, v104\n"
        "v_fma_f32 v58, v58, s24, v104\n"
        "v_fma_f32 v59, v59, s24, v104\n"
        "v_mfma_f32_16x16x32_bf16 v[96:99], a[176:179], a[20:23], v[96:99]\n"
        "s_mov_b32 s82, 0x43800000\n"
        "v_fma_f32 v68, v68, s24, v104\n"
        "v_fma_f32 v69, v69, s24, v104\n"
        "v_fma_f32 v70, v70, s24, v104\n"
        "v_mfma_f32_16x16x32_bf16 v[100:103], a[188:191], a[20:23], v[80:83]\n"
        "s_mov_b32 s84, 0x80000000\n"
        "v_fma_f32 v71, v71, s24, v104\n"
        "v_fma_f32 v76, v76, s24, v104\n"
        "v_fma_f32 v77, v77, s24, v104\n"
        "v_mfma_f32_16x16x32_bf16 v[100:103], a[184:187], a[16:19], v[100:103]\n"
        "s_sub_u32 s85, s48, 1\n"
        "v_fma_f32 v78, v78, s24, v104\n"
        "v_fma_f32 v79, v79, s24, v104\n"
        "v_fma_f32 v84, v84, s24, v104\n"
        "v_mfma_f32_16x16x32_bf16 v[100:103], a[180:183], a[12:15], v[100:103]\n"
        "s_mov_b32 s86, 0x80000000\n"
        "buffer_load_dwordx4 v[194:197], v35, s[36:39], 0 offen offset:32\n"
        "v_fma_f32 v85, v85, s24, v104\n"
        "v_fma_f32 v86, v86, s24, v104\n"
        "v_mfma_f32_16x16x32_bf16 v[32:35], a[192:195], a[12:15], v[28:31]\n"
        "s_mov_b32 s89, 0x80000000\n"
        "v_fma_f32 v87, v87, s24, v104\n"
        "v_fma_f32 v88, v88, s24, v104\n"
        "v_fma_f32 v89, v89, s24, v104\n"
        "v_mfma_f32_16x16x32_bf16 v[32:35], a[196:199], a[16:19], v[32:35]\n"
        "s_mov_b32 s29, 1\n"
        "v_fma_f32 v90, v90, s24, v104\n"
        "v_fma_f32 v91, v91, s24, v104\n"
        "v_fma_f32 v44, v44, s24, v104\n"
        "v_mfma_f32_16x16x32_bf16 v[32:35], a[200:203], a[20:23], v[32:35]\n"
        "s_mov_b32 s31, 0x400000\n"
        "v_fma_f32 v45, v45, s24, v104\n"
        "v_fma_f32 v46, v46, s24, v104\n"
        "v_fma_f32 v47, v47, s24, v104\n"
        "v_mfma_f32_16x16x32_bf16 v[104:107], a[212:215], a[20:23], v[92:95]\n"
        "s_mov_b32 s87, s49\n"
        "v_cvt_pk_bf16_f32 v108, v56, v57\n"
        "v_cvt_pk_bf16_f32 v109, v58, v59\n"
        "v_cvt_pk_bf16_f32 v110, v68, v69\n"
        "v_mfma_f32_16x16x32_bf16 v[104:107], a[208:211], a[16:19], v[104:107]\n"
        "s_mov_b32 s88, s50\n"
        "v_cvt_pk_bf16_f32 v111, v70, v71\n"
        "v_cvt_pk_bf16_f32 v56, v76, v77\n"
        "v_cvt_pk_bf16_f32 v57, v78, v79\n"
        "v_mfma_f32_16x16x32_bf16 v[104:107], a[204:207], a[12:15], v[104:107]\n"
        "s_mov_b32 s54, 0x05040302\n"
        "ds_write_b128 v37, v[108:111]\n"
        "v_cvt_pk_bf16_f32 v58, v84, v85\n"
        "v_mfma_f32_16x16x32_bf16 v[68:71], a[144:147], a[24:27], v[48:51]\n"
        "s_mov_b32 s55, 0x07060302\n"
        "v_cvt_pk_bf16_f32 v59, v86, v87\n"
        "v_cvt_pk_bf16_f32 v76, v88, v89\n"
        "v_cvt_pk_bf16_f32 v77, v90, v91\n"
        "v_mfma_f32_16x16x32_bf16 v[68:71], a[148:151], a[28:31], v[68:71]\n"
        "s_add_u32 m0, s52, 0\n"
        "ds_write_b128 v37, v[56:59] offset:1024\n"
        "v_cvt_pk_bf16_f32 v78, v44, v45\n"
        "v_mfma_f32_16x16x32_bf16 v[68:71], a[152:155], a[32:35], v[68:71]\n"
        "s_mov_b64 s[44:45], s[40:41]\n"
        "buffer_load_dwordx4 v0, s[36:39], s49 offen lds\n"
        "v_cvt_pk_bf16_f32 v79, v46, v47\n"
        "v_add_f32 v36, v36, v64\n"
        "v_mfma_f32_16x16x32_bf16 v[44:47], a[164:167], a[32:35], v[60:63]\n"
        "s_mov_b32 s46, 0\n"
        "ds_write_b128 v37, v[76:79] offset:2048\n"
        "v_add_f32 v36, v36, v65\n"
        "v_mfma_f32_16x16x32_bf16 v[44:47], a[160:163], a[28:31], v[44:47]\n"
        "s_mov_b32 s47, s43\n"
        "v_add_f32 v36, v36, v66\n"
        "v_add_f32 v36, v36, v67\n"
        "v_add_f32 v36, v36, v96\n"
        "v_mfma_f32_16x16x32_bf16 v[44:47], a[156:159], a[24:27], v[44:47]\n"
        "s_waitcnt lgkmcnt(3)\n"
        "v_add_f32 v36, v36, v97\n"
        "v_add_f32 v36, v36, v98\n"
        "v_add_f32 v36, v36, v99\n"
        "v_mfma_f32_16x16x32_bf16 v[56:59], a[168:171], a[24:27], v[72:75]\n"
        "s_waitcnt lgkmcnt(3)\n"
        "v_add_f32 v36, v36, v100\n"
        "v_add_f32 v36, v36, v101\n"
        "v_add_f32 v36, v36, v102\n"
        "v_mfma_f32_16x16x32_bf16 v[56:59], a[172:175], a[28:31], v[56:59]\n"
        "s_waitcnt lgkmcnt(3)\n"
        "v_add_f32 v36, v36, v103\n"
        "v_add_f32 v36, v36, v32\n"
        "v_add_f32 v36, v36, v33\n"
        "v_mfma_f32_16x16x32_bf16 v[56:59], a[176:179], a[32:35], v[56:59]\n"
        "buffer_load_dwordx4 v1, s[36:39], s49 offen offset:1024 lds\n"
        "v_add_f32 v36, v36, v34\n"
        "v_add_f32 v36, v36, v35\n"
        "v_mfma_f32_16x16x32_bf16 v[76:79], a[188:191], a[32:35], v[80:83]\n"
        "v_add_f32 v36, v36, v104\n"
        "v_add_f32 v36, v36, v105\n"
        "v_add_f32 v36, v36, v106\n"
        "v_mfma_f32_16x16x32_bf16 v[76:79], a[184:187], a[28:31], v[76:79]\n"
        "v_add_f32 v36, v36, v107\n"
        "v_mov_b32 v37, v36\n"
        "v_add_f32 v128, v68, v69\n"
        "v_mfma_f32_16x16x32_bf16 v[76:79], a[180:183], a[24:27], v[76:79]\n"
        "v_permlane32_swap_b32 v36, v37\n"
        "v_add_f32 v36, v36, v37\n"
        "v_mfma_f32_16x16x32_bf16 v[84:87], a[192:195], a[24:27], v[28:31]\n"
        "v_mov_b32 v37, v36\n"
        "v_add_f32 v128, v128, v70\n"
        "v_add_f32 v128, v128, v71\n"
        "v_mfma_f32_16x16x32_bf16 v[84:87], a[196:199], a[28:31], v[84:87]\n"
        "v_permlane16_swap_b32 v36, v37\n"
        "v_add_f32 v36, v36, v37\n"
        "v_mfma_f32_16x16x32_bf16 v[84:87], a[200:203], a[32:35], v[84:87]\n"
        "buffer_load_dwordx4 v2, s[36:39], s49 offen offset:2048 lds\n"
        "v_mul_f32 v36, 0xbac909aa, v36\n"
        "v_fma_f32 v52, v52, s24, v36\n"
        "v_mfma_f32_16x16x32_bf16 v[88:91], a[212:215], a[32:35], v[92:95]\n"
        "v_fma_f32 v53, v53, s24, v36\n"
        "v_fma_f32 v54, v54, s24, v36\n"
        "v_fma_f32 v55, v55, s24, v36\n"
        "v_mfma_f32_16x16x32_bf16 v[88:91], a[208:211], a[28:31], v[88:91]\n"
        "v_fma_f32 v64, v64, s24, v36\n"
        "v_fma_f32 v65, v65, s24, v36\n"
        "v_fma_f32 v66, v66, s24, v36\n"
        "v_mfma_f32_16x16x32_bf16 v[88:91], a[204:207], a[24:27], v[88:91]\n"
        "v_fma_f32 v67, v67, s24, v36\n"
        "v_fma_f32 v96, v96, s24, v36\n"
        "v_fma_f32 v97, v97, s24, v36\n"
        "v_mfma_f32_16x16x32_bf16 v[108:111], a[144:147], a[36:39], v[48:51]\n"
        "v_fma_f32 v98, v98, s24, v36\n"
        "v_fma_f32 v99, v99, s24, v36\n"
        "v_fma_f32 v100, v100, s24, v36\n"
        "v_mfma_f32_16x16x32_bf16 v[108:111], a[148:151], a[40:43], v[108:111]\n"
        "v_fma_f32 v101, v101, s24, v36\n"
        "v_fma_f32 v102, v102, s24, v36\n"
        "v_fma_f32 v103, v103, s24, v36\n"
        "v_mfma_f32_16x16x32_bf16 v[108:111], a[152:155], a[44:47], v[108:111]\n"
        "buffer_load_dwordx4 v3, s[36:39], s49 offen offset:3072 lds\n"
        "v_fma_f32 v32, v32, s24, v36\n"
        "v_fma_f32 v33, v33, s24, v36\n"
        "v_mfma_f32_16x16x32_bf16 v[112:115], a[164:167], a[44:47], v[60:63]\n"
        "v_fma_f32 v34, v34, s24, v36\n"
        "v_fma_f32 v35, v35, s24, v36\n"
        "v_fma_f32 v104, v104, s24, v36\n"
        "v_mfma_f32_16x16x32_bf16 v[112:115], a[160:163], a[40:43], v[112:115]\n"
        "v_fma_f32 v105, v105, s24, v36\n"
        "v_fma_f32 v106, v106, s24, v36\n"
        "v_fma_f32 v107, v107, s24, v36\n"
        "v_mfma_f32_16x16x32_bf16 v[112:115], a[156:159], a[36:39], v[112:115]\n"
        "v_cvt_pk_bf16_f32 v116, v52, v53\n"
        "v_cvt_pk_bf16_f32 v117, v54, v55\n"
        "v_cvt_pk_bf16_f32 v118, v64, v65\n"
        "v_mfma_f32_16x16x32_bf16 v[52:55], a[168:171], a[36:39], v[72:75]\n"
        "v_cvt_pk_bf16_f32 v119, v66, v67\n"
        "v_cvt_pk_bf16_f32 v64, v96, v97\n"
        "v_cvt_pk_bf16_f32 v65, v98, v99\n"
        "v_mfma_f32_16x16x32_bf16 v[52:55], a[172:175], a[40:43], v[52:55]\n"
        "s_add_u32 m0, s52, 4096\n"
        "ds_write_b128 v38, v[116:119]\n"
        "v_cvt_pk_bf16_f32 v66, v100, v101\n"
        "v_mfma_f32_16x16x32_bf16 v[52:55], a[176:179], a[44:47], v[52:55]\n"
        "buffer_load_dwordx4 v4, s[36:39], s49 offen lds\n"
        "v_cvt_pk_bf16_f32 v67, v102, v103\n"
        "v_cvt_pk_bf16_f32 v96, v32, v33\n"
        "v_mfma_f32_16x16x32_bf16 v[100:103], a[188:191], a[44:47], v[80:83]\n"
        "ds_write_b128 v38, v[64:67] offset:1024\n"
        "v_cvt_pk_bf16_f32 v97, v34, v35\n"
        "v_mfma_f32_16x16x32_bf16 v[100:103], a[184:187], a[40:43], v[100:103]\n"
        "v_cvt_pk_bf16_f32 v98, v104, v105\n"
        "v_cvt_pk_bf16_f32 v99, v106, v107\n"
        "v_add_f32 v128, v128, v44\n"
        "v_mfma_f32_16x16x32_bf16 v[100:103], a[180:183], a[36:39], v[100:103]\n"
        "ds_write_b128 v38, v[96:99] offset:2048\n"
        "v_add_f32 v128, v128, v45\n"
        "v_mfma_f32_16x16x32_bf16 v[32:35], a[192:195], a[36:39], v[28:31]\n"
        "v_add_f32 v128, v128, v46\n"
        "v_add_f32 v128, v128, v47\n"
        "v_add_f32 v128, v128, v56\n"
        "v_mfma_f32_16x16x32_bf16 v[32:35], a[196:199], a[40:43], v[32:35]\n"
        "v_add_f32 v128, v128, v57\n"
        "v_add_f32 v128, v128, v58\n"
        "v_add_f32 v128, v128, v59\n"
        "v_mfma_f32_16x16x32_bf16 v[32:35], a[200:203], a[44:47], v[32:35]\n"
        "buffer_load_dwordx4 v5, s[36:39], s49 offen offset:1024 lds\n"
        "v_add_f32 v128, v128, v76\n"
        "v_add_f32 v128, v128, v77\n"
        "v_mfma_f32_16x16x32_bf16 v[64:67], a[212:215], a[44:47], v[92:95]\n"
        "v_add_f32 v128, v128, v78\n"
        "v_add_f32 v128, v128, v79\n"
        "v_add_f32 v128, v128, v84\n"
        "v_mfma_f32_16x16x32_bf16 v[64:67], a[208:211], a[40:43], v[64:67]\n"
        "v_add_f32 v128, v128, v85\n"
        "v_add_f32 v128, v128, v86\n"
        "v_add_f32 v128, v128, v87\n"
        "v_mfma_f32_16x16x32_bf16 v[64:67], a[204:207], a[36:39], v[64:67]\n"
        "v_add_f32 v128, v128, v88\n"
        "v_add_f32 v128, v128, v89\n"
        "v_add_f32 v128, v128, v90\n"
        "v_mfma_f32_16x16x32_bf16 v[96:99], a[144:147], a[48:51], v[48:51]\n"
        "v_add_f32 v128, v128, v91\n"
        "v_mov_b32 v129, v128\n"
        "v_add_f32 v132, v108, v109\n"
        "v_mfma_f32_16x16x32_bf16 v[96:99], a[148:151], a[52:55], v[96:99]\n"
        "v_permlane32_swap_b32 v128, v129\n"
        "v_add_f32 v128, v128, v129\n"
        "v_mfma_f32_16x16x32_bf16 v[96:99], a[152:155], a[56:59], v[96:99]\n"
        "buffer_load_dwordx4 v6, s[36:39], s49 offen offset:2048 lds\n"
        "v_mov_b32 v129, v128\n"
        "v_add_f32 v132, v132, v110\n"
        "v_mfma_f32_16x16x32_bf16 v[104:107], a[164:167], a[56:59], v[60:63]\n"
        "v_permlane16_swap_b32 v128, v129\n"
        "v_add_f32 v128, v128, v129\n"
        "v_mfma_f32_16x16x32_bf16 v[104:107], a[160:163], a[52:55], v[104:107]\n"
        "v_mul_f32 v128, 0xbac909aa, v128\n"
        "v_add_f32 v132, v132, v111\n"
        "v_fma_f32 v68, v68, s24, v128\n"
        "v_mfma_f32_16x16x32_bf16 v[104:107], a[156:159], a[48:51], v[104:107]\n"
        "v_fma_f32 v69, v69, s24, v128\n"
        "v_fma_f32 v70, v70, s24, v128\n"
        "v_fma_f32 v71, v71, s24, v128\n"
        "v_mfma_f32_16x16x32_bf16 v[116:119], a[168:171], a[48:51], v[72:75]\n"
        "v_fma_f32 v44, v44, s24, v128\n"
        "v_fma_f32 v45, v45, s24, v128\n"
        "v_fma_f32 v46, v46, s24, v128\n"
        "v_mfma_f32_16x16x32_bf16 v[116:119], a[172:175], a[52:55], v[116:119]\n"
        "v_fma_f32 v47, v47, s24, v128\n"
        "v_fma_f32 v56, v56, s24, v128\n"
        "v_fma_f32 v57, v57, s24, v128\n"
        "v_mfma_f32_16x16x32_bf16 v[116:119], a[176:179], a[56:59], v[116:119]\n"
        "buffer_load_dwordx4 v7, s[36:39], s49 offen offset:3072 lds\n"
        "v_fma_f32 v58, v58, s24, v128\n"
        "v_fma_f32 v59, v59, s24, v128\n"
        "v_mfma_f32_16x16x32_bf16 v[120:123], a[188:191], a[56:59], v[80:83]\n"
        "v_fma_f32 v76, v76, s24, v128\n"
        "v_fma_f32 v77, v77, s24, v128\n"
        "v_fma_f32 v78, v78, s24, v128\n"
        "v_mfma_f32_16x16x32_bf16 v[120:123], a[184:187], a[52:55], v[120:123]\n"
        "v_fma_f32 v79, v79, s24, v128\n"
        "v_fma_f32 v84, v84, s24, v128\n"
        "v_fma_f32 v85, v85, s24, v128\n"
        "v_mfma_f32_16x16x32_bf16 v[120:123], a[180:183], a[48:51], v[120:123]\n"
        "v_fma_f32 v86, v86, s24, v128\n"
        "v_fma_f32 v87, v87, s24, v128\n"
        "v_fma_f32 v88, v88, s24, v128\n"
        "v_mfma_f32_16x16x32_bf16 v[124:127], a[192:195], a[48:51], v[28:31]\n"
        "v_fma_f32 v89, v89, s24, v128\n"
        "v_fma_f32 v90, v90, s24, v128\n"
        "v_fma_f32 v91, v91, s24, v128\n"
        "v_mfma_f32_16x16x32_bf16 v[124:127], a[196:199], a[52:55], v[124:127]\n"
        "s_add_u32 m0, s52, 8192\n"
        "v_cvt_pk_bf16_f32 v128, v68, v69\n"
        "v_cvt_pk_bf16_f32 v129, v70, v71\n"
        "v_cvt_pk_bf16_f32 v130, v44, v45\n"
        "v_mfma_f32_16x16x32_bf16 v[124:127], a[200:203], a[56:59], v[124:127]\n"
        "buffer_load_dwordx4 v8, s[36:39], s49 offen lds\n"
        "v_cvt_pk_bf16_f32 v131, v46, v47\n"
        "v_cvt_pk_bf16_f32 v44, v56, v57\n"
        "v_mfma_f32_16x16x32_bf16 v[68:71], a[212:215], a[56:59], v[92:95]\n"
        "ds_write_b128 v39, v[128:131]\n"
        "v_cvt_pk_bf16_f32 v45, v58, v59\n"
        "v_mfma_f32_16x16x32_bf16 v[68:71], a[208:211], a[52:55], v[68:71]\n"
        "v_cvt_pk_bf16_f32 v46, v76, v77\n"
        "v_cvt_pk_bf16_f32 v47, v78, v79\n"
        "v_cvt_pk_bf16_f32 v56, v84, v85\n"
        "v_mfma_f32_16x16x32_bf16 v[68:71], a[204:207], a[48:51], v[68:71]\n"
        "ds_write_b128 v39, v[44:47] offset:1024\n"
        "v_cvt_pk_bf16_f32 v57, v86, v87\n"
        "v_mfma_f32_16x16x32_bf16 v[44:47], a[144:147], a[60:63], v[48:51]\n"
        "v_cvt_pk_bf16_f32 v58, v88, v89\n"
        "v_cvt_pk_bf16_f32 v59, v90, v91\n"
        "v_add_f32 v132, v132, v112\n"
        "v_mfma_f32_16x16x32_bf16 v[44:47], a[148:151], a[64:67], v[44:47]\n"
        "ds_write_b128 v39, v[56:59] offset:2048\n"
        "v_add_f32 v132, v132, v113\n"
        "v_mfma_f32_16x16x32_bf16 v[44:47], a[152:155], a[68:71], v[44:47]\n"
        "v_add_f32 v132, v132, v114\n"
        "v_add_f32 v132, v132, v115\n"
        "v_add_f32 v132, v132, v52\n"
        "v_mfma_f32_16x16x32_bf16 v[36:39], a[164:167], a[68:71], v[60:63]\n"
        "v_add_f32 v132, v132, v53\n"
        "v_add_f32 v132, v132, v54\n"
        "v_add_f32 v132, v132, v55\n"
        "v_mfma_f32_16x16x32_bf16 v[36:39], a[160:163], a[64:67], v[36:39]\n"
        "v_add_f32 v132, v132, v100\n"
        "v_add_f32 v132, v132, v101\n"
        "v_add_f32 v132, v132, v102\n"
        "v_mfma_f32_16x16x32_bf16 v[36:39], a[156:159], a[60:63], v[36:39]\n"
        "v_add_f32 v132, v132, v103\n"
        "v_add_f32 v132, v132, v32\n"
        "v_add_f32 v132, v132, v33\n"
        "v_mfma_f32_16x16x32_bf16 v[48:51], a[168:171], a[60:63], v[72:75]\n"
        "v_add_f32 v132, v132, v34\n"
        "v_add_f32 v132, v132, v35\n"
        "v_add_f32 v132, v132, v64\n"
        "v_mfma_f32_16x16x32_bf16 v[48:51], a[172:175], a[64:67], v[48:51]\n"
        "v_add_f32 v132, v132, v65\n"
        "v_add_f32 v132, v132, v66\n"
        "v_add_f32 v132, v132, v67\n"
        "v_mfma_f32_16x16x32_bf16 v[48:51], a[176:179], a[68:71], v[48:51]\n"
        "v_mov_b32 v133, v132\n"
        "v_add_f32 v72, v96, v97\n"
        "v_add_f32 v72, v72, v98\n"
        "v_mfma_f32_16x16x32_bf16 v[56:59], a[188:191], a[68:71], v[80:83]\n"
        "v_permlane32_swap_b32 v132, v133\n"
        "v_add_f32 v132, v132, v133\n"
        "v_mfma_f32_16x16x32_bf16 v[56:59], a[184:187], a[64:67], v[56:59]\n"
        "v_mov_b32 v133, v132\n"
        "v_add_f32 v72, v72, v99\n"
        "v_add_f32 v72, v72, v104\n"
        "v_mfma_f32_16x16x32_bf16 v[56:59], a[180:183], a[60:63], v[56:59]\n"
        "v_permlane16_swap_b32 v132, v133\n"
        "v_add_f32 v132, v132, v133\n"
        "v_mfma_f32_16x16x32_bf16 v[60:63], a[192:195], a[60:63], v[28:31]\n"
        "v_mul_f32 v132, 0xbac909aa, v132\n"
        "v_add_f32 v72, v72, v105\n"
        "v_add_f32 v72, v72, v106\n"
        "v_mfma_f32_16x16x32_bf16 v[60:63], a[196:199], a[64:67], v[60:63]\n"
        "v_add_f32 v72, v72, v107\n"
        "v_add_f32 v72, v72, v116\n"
        "v_add_f32 v72, v72, v117\n"
        "v_mfma_f32_16x16x32_bf16 v[60:63], a[200:203], a[68:71], v[60:63]\n"
        "v_add_f32 v72, v72, v118\n"
        "v_add_f32 v72, v72, v119\n"
        "v_add_f32 v72, v72, v120\n"
        "v_mfma_f32_16x16x32_bf16 v[28:31], a[212:215], a[68:71], v[92:95]\n"
        "v_add_f32 v72, v72, v121\n"
        "v_add_f32 v72, v72, v122\n"
        "v_add_f32 v72, v72, v123\n"
        "v_mfma_f32_16x16x32_bf16 v[28:31], a[208:211], a[64:67], v[28:31]\n"
        "v_add_f32 v72, v72, v124\n"
        "v_add_f32 v72, v72, v125\n"
        "v_add_f32 v72, v72, v126\n"
        "v_mfma_f32_16x16x32_bf16 v[28:31], a[204:207], a[60:63], v[28:31]\n"
        "v_add_f32 v72, v72, v127\n"
        "v_add_f32 v72, v72, v68\n"
        "v_add_f32 v72, v72, v69\n"
        "v_add_f32 v72, v72, v70\n"
        "v_add_f32 v72, v72, v71\n"
        "v_mov_b32 v73, v72\n"
        "v_add_f32 v84, v44, v45\n"
        "v_add_f32 v84, v84, v46\n"
        "v_permlane32_swap_b32 v72, v73\n"
        "v_add_f32 v72, v72, v73\n"
        "v_mov_b32 v73, v72\n"
        "v_add_f32 v84, v84, v47\n"
        "v_add_f32 v84, v84, v36\n"
        "v_permlane16_swap_b32 v72, v73\n"
        "v_add_f32 v72, v72, v73\n"
        "v_mul_f32 v72, 0xbac909aa, v72\n"
        "v_add_f32 v84, v84, v37\n"
        "v_add_f32 v84, v84, v38\n"
        "v_add_f32 v84, v84, v39\n"
        "v_add_f32 v84, v84, v48\n"
        "v_add_f32 v84, v84, v49\n"
        "v_add_f32 v84, v84, v50\n"
        "v_add_f32 v84, v84, v51\n"
        "v_add_f32 v84, v84, v56\n"
        "v_add_f32 v84, v84, v57\n"
        "v_add_f32 v84, v84, v58\n"
        "v_add_f32 v84, v84, v59\n"
        "v_add_f32 v84, v84, v60\n"
        "v_add_f32 v84, v84, v61\n"
        "v_add_f32 v84, v84, v62\n"
        "v_add_f32 v84, v84, v63\n"
        "v_pk_fma_f32 v[108:109], v[108:109], s[24:25], v[132:133] op_sel_hi:[1,0,0]\n"
        "v_pk_fma_f32 v[110:111], v[110:111], s[24:25], v[132:133] op_sel_hi:[1,0,0]\n"
        "v_pk_fma_f32 v[112:113], v[112:113], s[24:25], v[132:133] op_sel_hi:[1,0,0]\n"
        "v_pk_fma_f32 v[114:115], v[114:115], s[24:25], v[132:133] op_sel_hi:[1,0,0]\n"
        "v_pk_fma_f32 v[52:53], v[52:53], s[24:25], v[132:133] op_sel_hi:[1,0,0]\n"
        "v_pk_fma_f32 v[54:55], v[54:55], s[24:25], v[132:133] op_sel_hi:[1,0,0]\n"
        "v_pk_fma_f32 v[100:101], v[100:101], s[24:25], v[132:133] op_sel_hi:[1,0,0]\n"
        "v_pk_fma_f32 v[102:103], v[102:103], s[24:25], v[132:133] op_sel_hi:[1,0,0]\n"
        "v_pk_fma_f32 v[32:33], v[32:33], s[24:25], v[132:133] op_sel_hi:[1,0,0]\n"
        "v_pk_fma_f32 v[34:35], v[34:35], s[24:25], v[132:133] op_sel_hi:[1,0,0]\n"
        "v_pk_fma_f32 v[64:65], v[64:65], s[24:25], v[132:133] op_sel_hi:[1,0,0]\n"
        "v_pk_fma_f32 v[66:67], v[66:67], s[24:25], v[132:133] op_sel_hi:[1,0,0]\n"
        "v_pk_fma_f32 v[96:97], v[96:97], s[24:25], v[72:73] op_sel_hi:[1,0,0]\n"
        "v_pk_fma_f32 v[98:99], v[98:99], s[24:25], v[72:73] op_sel_hi:[1,0,0]\n"
        "v_pk_fma_f32 v[104:105], v[104:105], s[24:25], v[72:73] op_sel_hi:[1,0,0]\n"
        "v_pk_fma_f32 v[106:107], v[106:107], s[24:25], v[72:73] op_sel_hi:[1,0,0]\n"
        "v_pk_fma_f32 v[116:117], v[116:117], s[24:25], v[72:73] op_sel_hi:[1,0,0]\n"
        "v_pk_fma_f32 v[118:119], v[118:119], s[24:25], v[72:73] op_sel_hi:[1,0,0]\n"
        "v_pk_fma_f32 v[120:121], v[120:121], s[24:25], v[72:73] op_sel_hi:[1,0,0]\n"
        "v_pk_fma_f32 v[122:123], v[122:123], s[24:25], v[72:73] op_sel_hi:[1,0,0]\n"
        "v_pk_fma_f32 v[124:125], v[124:125], s[24:25], v[72:73] op_sel_hi:[1,0,0]\n"
        "v_pk_fma_f32 v[126:127], v[126:127], s[24:25], v[72:73] op_sel_hi:[1,0,0]\n"
        "v_pk_fma_f32 v[68:69], v[68:69], s[24:25], v[72:73] op_sel_hi:[1,0,0]\n"
        "v_pk_fma_f32 v[70:71], v[70:71], s[24:25], v[72:73] op_sel_hi:[1,0,0]\n"
        "v_cvt_pk_bf16_f32 v72, v108, v109\n"
        "v_cvt_pk_bf16_f32 v73, v110, v111\n"
        "v_cvt_pk_bf16_f32 v74, v112, v113\n"
        "v_cvt_pk_bf16_f32 v75, v114, v115\n"
        "v_cvt_pk_bf16_f32 v76, v52, v53\n"
        "v_cvt_pk_bf16_f32 v77, v54, v55\n"
        "v_cvt_pk_bf16_f32 v78, v100, v101\n"
        "v_cvt_pk_bf16_f32 v79, v102, v103\n"
        "v_cvt_pk_bf16_f32 v52, v32, v33\n"
        "v_cvt_pk_bf16_f32 v53, v34, v35\n"
        "v_cvt_pk_bf16_f32 v54, v64, v65\n"
        "v_cvt_pk_bf16_f32 v55, v66, v67\n"
        "v_cvt_pk_bf16_f32 v32, v96, v97\n"
        "v_cvt_pk_bf16_f32 v33, v98, v99\n"
        "v_cvt_pk_bf16_f32 v34, v104, v105\n"
        "v_cvt_pk_bf16_f32 v35, v106, v107\n"
        "v_cvt_pk_bf16_f32 v64, v116, v117\n"
        "v_cvt_pk_bf16_f32 v65, v118, v119\n"
        "v_cvt_pk_bf16_f32 v66, v120, v121\n"
        "v_cvt_pk_bf16_f32 v67, v122, v123\n"
        "v_cvt_pk_bf16_f32 v80, v124, v125\n"
        "v_cvt_pk_bf16_f32 v81, v126, v127\n"
        "v_cvt_pk_bf16_f32 v82, v68, v69\n"
        "v_cvt_pk_bf16_f32 v83, v70, v71\n"
        "v_add_f32 v84, v84, v28\n"
        "ds_write_b128 v40, v[72:75]\n"
        "ds_write_b128 v40, v[76:79] offset:1024\n"
        "ds_write_b128 v40, v[52:55] offset:2048\n"
        "ds_write_b128 v41, v[32:35]\n"
        "ds_write_b128 v41, v[64:67] offset:1024\n"
        "ds_write_b128 v41, v[80:83] offset:2048\n"
        "v_add_f32 v84, v84, v29\n"
        "v_add_f32 v84, v84, v30\n"
        "v_add_f32 v84, v84, v31\n"
        "v_mov_b32 v85, v84\n"
        "s_waitcnt vmcnt(9)\n"
        "s_nop 0\n"
        "v_permlane32_swap_b32 v84, v85\n"
        "v_add_f32 v84, v84, v85\n"
        "v_mov_b32 v85, v84\n"
        "s_nop 1\n"
        "v_permlane16_swap_b32 v84, v85\n"
        "v_add_f32 v84, v84, v85\n"
        "v_mul_f32 v84, 0xbac909aa, v84\n"
        "v_pk_fma_f32 v[44:45], v[44:45], s[24:25], v[84:85] op_sel_hi:[1,0,0]\n"
        "v_pk_fma_f32 v[46:47], v[46:47], s[24:25], v[84:85] op_sel_hi:[1,0,0]\n"
        "v_pk_fma_f32 v[36:37], v[36:37], s[24:25], v[84:85] op_sel_hi:[1,0,0]\n"
        "v_pk_fma_f32 v[38:39], v[38:39], s[24:25], v[84:85] op_sel_hi:[1,0,0]\n"
        "v_pk_fma_f32 v[48:49], v[48:49], s[24:25], v[84:85] op_sel_hi:[1,0,0]\n"
        "v_pk_fma_f32 v[50:51], v[50:51], s[24:25], v[84:85] op_sel_hi:[1,0,0]\n"
        "v_pk_fma_f32 v[56:57], v[56:57], s[24:25], v[84:85] op_sel_hi:[1,0,0]\n"
        "v_pk_fma_f32 v[58:59], v[58:59], s[24:25], v[84:85] op_sel_hi:[1,0,0]\n"
        "v_pk_fma_f32 v[60:61], v[60:61], s[24:25], v[84:85] op_sel_hi:[1,0,0]\n"
        "v_pk_fma_f32 v[62:63], v[62:63], s[24:25], v[84:85] op_sel_hi:[1,0,0]\n"
        "v_pk_fma_f32 v[28:29], v[28:29], s[24:25], v[84:85] op_sel_hi:[1,0,0]\n"
        "v_pk_fma_f32 v[30:31], v[30:31], s[24:25], v[84:85] op_sel_hi:[1,0,0]\n"
        "v_cvt_pk_bf16_f32 v32, v44, v45\n"
        "v_cvt_pk_bf16_f32 v33, v46, v47\n"
        "v_cvt_pk_bf16_f32 v34, v36, v37\n"
        "v_cvt_pk_bf16_f32 v35, v38, v39\n"
        "v_cvt_pk_bf16_f32 v36, v48, v49\n"
        "v_cvt_pk_bf16_f32 v37, v50, v51\n"
        "v_cvt_pk_bf16_f32 v38, v56, v57\n"
        "v_cvt_pk_bf16_f32 v39, v58, v59\n"
        "v_cvt_pk_bf16_f32 v44, v60, v61\n"
        "v_cvt_pk_bf16_f32 v45, v62, v63\n"
        "v_cvt_pk_bf16_f32 v46, v28, v29\n"
        "v_cvt_pk_bf16_f32 v47, v30, v31\n"
        "ds_write_b128 v42, v[32:35]\n"
        "ds_write_b128 v42, v[36:39] offset:1024\n"
        "ds_write_b128 v42, v[44:47] offset:2048\n"
        "s_waitcnt lgkmcnt(0)\n"
        "s_barrier\n"
        "s_branch FE_%=\n"
        "FV_%=:\n"
        "s_bitcmp1_b32 s52, 11\n"
        "s_cbranch_scc1 FG_%=\n"
        "v_mov_b32 v48, 0x3f803f80\n"
        "v_mov_b32 v49, 0x3f803f80\n"
        "v_mov_b32 v50, 0x3f803f80\n"
        "v_mov_b32 v51, 0x3f803f80\n"
        "s_waitcnt vmcnt(9)\n"
        "ds_read_b128 a[0:3], v30\n"
        "ds_read_b128 a[4:7], v30 offset:64\n"
        "ds_read_b128 a[8:11], v30 offset:128\n"
        "s_add_u32 m0, s53, 9216\n"
        "s_nop 0\n"
        "buffer_load_dwordx4 v44, s[16:19], s21 offen lds\n"
        "ds_read_b128 a[12:15], v31\n"
        "ds_read_b128 a[16:19], v31 offset:64\n"
        "ds_read_b128 a[20:23], v31 offset:128\n"
        "buffer_load_dwordx4 v45, s[16:19], s21 offen offset:1024 lds\n"
        "ds_read_b128 a[24:27], v30 offset:6144\n"
        "ds_read_b128 a[28:31], v30 offset:6208\n"
        "ds_read_b128 a[32:35], v30 offset:6272\n"
        "buffer_load_dwordx4 v46, s[16:19], s21 offen offset:2048 lds\n"
        "ds_read_b128 a[36:39], v31 offset:6144\n"
        "ds_read_b128 a[40:43], v31 offset:6208\n"
        "ds_read_b128 a[44:47], v31 offset:6272\n"
        "s_add_u32 m0, s53, 12288\n"
        "s_nop 0\n"
        "buffer_load_dwordx4 v44, s[16:19], s22 offen lds\n"
        "ds_read_b128 a[48:51], v30 offset:12288\n"
        "ds_read_b128 a[52:55], v30 offset:12352\n"
        "ds_read_b128 a[56:59], v30 offset:12416\n"
        "buffer_load_dwordx4 v45, s[16:19], s22 offen offset:1024 lds\n"
        "ds_read_b128 a[60:63], v31 offset:12288\n"
        "ds_read_b128 a[64:67], v31 offset:12352\n"
        "ds_read_b128 a[68:71], v31 offset:12416\n"
        "buffer_load_dwordx4 v46, s[16:19], s22 offen offset:2048 lds\n"
        "ds_read_b64_tr_b16 a[72:73], v32 offset:0\n"
        "ds_read_b64_tr_b16 a[74:75], v33 offset:0\n"
        "ds_read_b64_tr_b16 a[76:77], v32 offset:6144\n"
        "ds_read_b64_tr_b16 a[78:79], v33 offset:6144\n"
        "ds_read_b64_tr_b16 a[80:81], v32 offset:12288\n"
        "ds_read_b64_tr_b16 a[82:83], v33 offset:12288\n"
        "s_waitcnt lgkmcnt(15)\n"
        "v_mfma_f32_16x16x32_bf16 v[52:55], a[0:3], v[48:51], 0\n"
        "s_add_u32 m0, s53, 15360\n"
        "s_waitcnt vmcnt(9)\n"
        "s_waitcnt vmcnt(3)\n"
        "v_mfma_f32_16x16x32_bf16 v[52:55], a[4:7], v[48:51], v[52:55]\n"
        "s_waitcnt lgkmcnt(15)\n"
        "buffer_load_dwordx4 v44, s[16:19], s23 offen lds\n"
        "v_mfma_f32_16x16x32_bf16 v[52:55], a[8:11], v[48:51], v[52:55]\n"
        "s_waitcnt lgkmcnt(15)\n"
        "v_mfma_f32_16x16x32_bf16 v[56:59], a[20:23], v[48:51], 0\n"
        "s_waitcnt lgkmcnt(12)\n"
        "s_nop 1\n"
        "v_mfma_f32_16x16x32_bf16 v[56:59], a[16:19], v[48:51], v[56:59]\n"
        "s_nop 0\n"
        "buffer_load_dwordx4 v45, s[16:19], s23 offen offset:1024 lds\n"
        "v_cvt_pk_bf16_f32 v60, v52, v53\n"
        "v_cvt_pk_bf16_f32 v61, v54, v55\n"
        "v_mfma_f32_16x16x32_bf16 v[56:59], a[12:15], v[48:51], v[56:59]\n"
        "s_waitcnt lgkmcnt(9)\n"
        "v_mfma_f32_16x16x32_bf16 v[52:55], a[24:27], v[48:51], 0\n"
        "s_waitcnt lgkmcnt(6)\n"
        "ds_read_b128 a[144:147], v28\n"
        "s_nop 0\n"
        "v_mfma_f32_16x16x32_bf16 v[52:55], a[28:31], v[48:51], v[52:55]\n"
        "s_waitcnt lgkmcnt(1)\n"
        "ds_read_b128 a[148:151], v28 offset:64\n"
        "v_cvt_pk_bf16_f32 v62, v56, v57\n"
        "v_cvt_pk_bf16_f32 v63, v58, v59\n"
        "v_mfma_f32_16x16x32_bf16 v[52:55], a[32:35], v[48:51], v[52:55]\n"
        "buffer_load_dwordx4 v46, s[16:19], s23 offen offset:2048 lds\n"
        "s_waitcnt vmcnt(0)\n"
        "v_mfma_f32_16x16x32_bf16 v[44:47], a[44:47], v[48:51], 0\n"
        "ds_read_b128 a[152:155], v28 offset:128\n"
        "ds_read_b128 a[156:159], v29\n"
        "v_mfma_f32_16x16x32_bf16 v[44:47], a[40:43], v[48:51], v[44:47]\n"
        "s_nop 0\n"
        "ds_read_b128 a[160:163], v29 offset:64\n"
        "v_cvt_pk_bf16_f32 v56, v52, v53\n"
        "v_cvt_pk_bf16_f32 v57, v54, v55\n"
        "v_mfma_f32_16x16x32_bf16 v[44:47], a[36:39], v[48:51], v[44:47]\n"
        "s_nop 0\n"
        "ds_read_b128 a[164:167], v29 offset:128\n"
        "v_mfma_f32_16x16x32_bf16 v[52:55], a[48:51], v[48:51], 0\n"
        "s_nop 0\n"
        "ds_read_b128 a[168:171], v28 offset:6144\n"
        "v_mfma_f32_16x16x32_bf16 v[52:55], a[52:55], v[48:51], v[52:55]\n"
        "s_nop 0\n"
        "ds_read_b128 a[172:175], v28 offset:6208\n"
        "v_cvt_pk_bf16_f32 v58, v44, v45\n"
        "v_cvt_pk_bf16_f32 v59, v46, v47\n"
        "v_mfma_f32_16x16x32_bf16 v[52:55], a[56:59], v[48:51], v[52:55]\n"
        "s_nop 0\n"
        "ds_read_b128 a[176:179], v28 offset:6272\n"
        "v_mfma_f32_16x16x32_bf16 v[44:47], a[68:71], v[48:51], 0\n"
        "s_nop 0\n"
        "ds_read_b128 a[180:183], v29 offset:6144\n"
        "v_mfma_f32_16x16x32_bf16 v[44:47], a[64:67], v[48:51], v[44:47]\n"
        "s_nop 0\n"
        "ds_read_b128 a[184:187], v29 offset:6208\n"
        "v_cvt_pk_bf16_f32 v64, v52, v53\n"
        "v_cvt_pk_bf16_f32 v65, v54, v55\n"
        "v_mfma_f32_16x16x32_bf16 v[44:47], a[60:63], v[48:51], v[44:47]\n"
        "ds_read_b128 a[188:191], v29 offset:6272\n"
        "s_waitcnt lgkmcnt(9)\n"
        "v_mfma_f32_16x16x32_bf16 v[48:51], a[144:147], a[72:75], 0\n"
        "s_nop 0\n"
        "ds_read_b128 a[192:195], v28 offset:12288\n"
        "v_mfma_f32_16x16x32_bf16 v[48:51], a[148:151], a[76:79], v[48:51]\n"
        "s_nop 0\n"
        "ds_read_b128 a[196:199], v28 offset:12352\n"
        "v_cvt_pk_bf16_f32 v66, v44, v45\n"
        "v_cvt_pk_bf16_f32 v67, v46, v47\n"
        "v_mfma_f32_16x16x32_bf16 v[48:51], a[152:155], a[80:83], v[48:51]\n"
        "s_waitcnt lgkmcnt(8)\n"
        "ds_read_b128 a[200:203], v28 offset:12416\n"
        "v_mfma_f32_16x16x32_bf16 v[44:47], a[152:155], v[64:67], 0\n"
        "buffer_load_dwordx4 v[162:165], v34, s[36:39], 0 offen\n"
        "v_mfma_f32_16x16x32_bf16 v[44:47], a[148:151], v[56:59], v[44:47]\n"
        "s_nop 0\n"
        "ds_read_b128 a[204:207], v29 offset:12288\n"
        "ds_read_b128 a[208:211], v29 offset:12352\n"
        "v_add_f32 v104, v48, v49\n"
        "v_mfma_f32_16x16x32_bf16 v[44:47], a[144:147], v[60:63], v[44:47]\n"
        "s_waitcnt lgkmcnt(8)\n"
        "ds_read_b128 a[212:215], v29 offset:12416\n"
        "v_add_f32 v104, v104, v50\n"
        "v_add_f32 v104, v104, v51\n"
        "v_mfma_f32_16x16x32_bf16 v[28:31], a[156:159], a[72:75], 0\n"
        "ds_read_b64_tr_b16 a[84:85], v32 offset:8\n"
        "v_mfma_f32_16x16x32_bf16 v[28:31], a[160:163], a[76:79], v[28:31]\n"
        "ds_read_b64_tr_b16 a[86:87], v33 offset:8\n"
        "v_add_f32 v108, v44, v45\n"
        "v_add_f32 v108, v108, v46\n"
        "v_mfma_f32_16x16x32_bf16 v[28:31], a[164:167], a[80:83], v[28:31]\n"
        "s_waitcnt lgkmcnt(8)\n"
        "ds_read_b64_tr_b16 a[88:89], v32 offset:6152\n"
        "v_add_f32 v108, v108, v47\n"
        "v_mfma_f32_16x16x32_bf16 v[52:55], a[164:167], v[64:67], 0\n"
        "ds_read_b64_tr_b16 a[90:91], v33 offset:6152\n"
        "v_mfma_f32_16x16x32_bf16 v[52:55], a[160:163], v[56:59], v[52:55]\n"
        "s_nop 0\n"
        "ds_read_b64_tr_b16 a[92:93], v32 offset:12296\n"
        "v_add_f32 v104, v104, v28\n"
        "v_add_f32 v104, v104, v29\n"
        "v_mfma_f32_16x16x32_bf16 v[52:55], a[156:159], v[60:63], v[52:55]\n"
        "ds_read_b64_tr_b16 a[94:95], v33 offset:12296\n"
        "v_add_f32 v104, v104, v30\n"
        "v_add_f32 v104, v104, v31\n"
        "v_mfma_f32_16x16x32_bf16 v[68:71], a[168:171], a[72:75], 0\n"
        "s_waitcnt lgkmcnt(9)\n"
        "buffer_load_dwordx4 v[168:171], v34, s[36:39], 0 offen offset:16\n"
        "v_mfma_f32_16x16x32_bf16 v[68:71], a[172:175], a[76:79], v[68:71]\n"
        "s_mov_b32 s78, 0xf0000f\n"
        "ds_read_b64_tr_b16 a[96:97], v32 offset:64\n"
        "ds_read_b64_tr_b16 a[98:99], v33 offset:64\n"
        "v_add_f32 v108, v108, v52\n"
        "v_mfma_f32_16x16x32_bf16 v[68:71], a[176:179], a[80:83], v[68:71]\n"
        "s_mov_b32 s79, 0xf0000f00\n"
        "ds_read_b64_tr_b16 a[100:101], v32 offset:6208\n"
        "v_add_f32 v108, v108, v53\n"
        "v_add_f32 v108, v108, v54\n"
        "v_mfma_f32_16x16x32_bf16 v[72:75], a[176:179], v[64:67], 0\n"
        "s_mov_b32 s56, 0x22222222\n"
        "ds_read_b64_tr_b16 a[102:103], v33 offset:6208\n"
        "v_add_f32 v108, v108, v55\n"
        "v_add_f32 v104, v104, v68\n"
        "v_mfma_f32_16x16x32_bf16 v[72:75], a[172:175], v[56:59], v[72:75]\n"
        "s_waitcnt lgkmcnt(10)\n"
        "ds_read_b64_tr_b16 a[104:105], v32 offset:12352\n"
        "v_add_f32 v104, v104, v69\n"
        "v_add_f32 v104, v104, v70\n"
        "v_mfma_f32_16x16x32_bf16 v[72:75], a[168:171], v[60:63], v[72:75]\n"
        "s_mov_b32 s57, 0x22222222\n"
        "ds_read_b64_tr_b16 a[106:107], v33 offset:12352\n"
        "v_add_f32 v104, v104, v71\n"
        "v_mfma_f32_16x16x32_bf16 v[76:79], a[180:183], a[72:75], 0\n"
        "s_mov_b32 s58, 0x44444444\n"
        "ds_read_b64_tr_b16 a[108:109], v32 offset:72\n"
        "v_mfma_f32_16x16x32_bf16 v[76:79], a[184:187], a[76:79], v[76:79]\n"
        "s_mov_b32 s59, 0x44444444\n"
        "ds_read_b64_tr_b16 a[110:111], v33 offset:72\n"
        "v_add_f32 v108, v108, v72\n"
        "v_add_f32 v108, v108, v73\n"
        "v_mfma_f32_16x16x32_bf16 v[76:79], a[188:191], a[80:83], v[76:79]\n"
        "s_mov_b32 s60, 0x88888888\n"
        "ds_read_b64_tr_b16 a[112:113], v32 offset:6216\n"
        "v_add_f32 v108, v108, v74\n"
        "v_add_f32 v108, v108, v75\n"
        "v_mfma_f32_16x16x32_bf16 v[80:83], a[188:191], v[64:67], 0\n"
        "s_waitcnt lgkmcnt(9)\n"
        "buffer_load_dwordx4 v[174:177], v34, s[36:39], 0 offen offset:32\n"
        "v_mfma_f32_16x16x32_bf16 v[80:83], a[184:187], v[56:59], v[80:83]\n"
        "s_mov_b32 s61, 0x88888888\n"
        "ds_read_b64_tr_b16 a[114:115], v33 offset:6216\n"
        "ds_read_b64_tr_b16 a[116:117], v32 offset:12360\n"
        "v_add_f32 v104, v104, v76\n"
        "v_mfma_f32_16x16x32_bf16 v[80:83], a[180:183], v[60:63], v[80:83]\n"
        "s_mov_b32 s62, 0xffff\n"
        "ds_read_b64_tr_b16 a[118:119], v33 offset:12360\n"
        "v_add_f32 v104, v104, v77\n"
        "v_add_f32 v104, v104, v78\n"
        "v_mfma_f32_16x16x32_bf16 v[84:87], a[192:195], a[72:75], 0\n"
        "s_mov_b32 s63, 0x0\n"
        "ds_read_b64_tr_b16 a[120:121], v32 offset:128\n"
        "v_add_f32 v104, v104, v79\n"
        "v_add_f32 v108, v108, v80\n"
        "v_mfma_f32_16x16x32_bf16 v[84:87], a[196:199], a[76:79], v[84:87]\n"
        "s_mov_b32 s64, 0xffff0000\n"
        "ds_read_b64_tr_b16 a[122:123], v33 offset:128\n"
        "v_add_f32 v108, v108, v81\n"
        "v_add_f32 v108, v108, v82\n"
        "v_mfma_f32_16x16x32_bf16 v[84:87], a[200:203], a[80:83], v[84:87]\n"
        "s_mov_b32 s65, 0x0\n"
        "ds_read_b64_tr_b16 a[124:125], v32 offset:6272\n"
        "v_add_f32 v108, v108, v83\n"
        "v_mfma_f32_16x16x32_bf16 v[88:91], a[200:203], v[64:67], 0\n"
        "s_mov_b32 s66, 0x0\n"
        "ds_read_b64_tr_b16 a[126:127], v33 offset:6272\n"
        "v_mfma_f32_16x16x32_bf16 v[88:91], a[196:199], v[56:59], v[88:91]\n"
        "s_mov_b32 s67, 0xffff\n"
        "ds_read_b64_tr_b16 a[128:129], v32 offset:12416\n"
        "v_add_f32 v104, v104, v84\n"
        "v_add_f32 v104, v104, v85\n"
        "v_mfma_f32_16x16x32_bf16 v[88:91], a[192:195], v[60:63], v[88:91]\n"
        "s_mov_b32 s68, 0x0\n"
        "ds_read_b64_tr_b16 a[130:131], v33 offset:12416\n"
        "v_add_f32 v104, v104, v86\n"
        "v_add_f32 v104, v104, v87\n"
        "v_mfma_f32_16x16x32_bf16 v[92:95], a[204:207], a[72:75], 0\n"
        "s_waitcnt lgkmcnt(12)\n"
        "buffer_load_dwordx4 v[182:185], v35, s[36:39], 0 offen\n"
        "v_mfma_f32_16x16x32_bf16 v[92:95], a[208:211], a[76:79], v[92:95]\n"
        "s_mov_b32 s69, 0xffff0000\n"
        "ds_read_b64_tr_b16 a[132:133], v32 offset:136\n"
        "ds_read_b64_tr_b16 a[134:135], v33 offset:136\n"
        "v_add_f32 v108, v108, v88\n"
        "v_mfma_f32_16x16x32_bf16 v[92:95], a[212:215], a[80:83], v[92:95]\n"
        "s_mov_b32 s70, 0x0\n"
        "ds_read_b64_tr_b16 a[136:137], v32 offset:6280\n"
        "v_add_f32 v108, v108, v89\n"
        "v_add_f32 v108, v108, v90\n"
        "v_mfma_f32_16x16x32_bf16 v[96:99], a[212:215], v[64:67], 0\n"
        "s_mov_b32 s71, 0xffffffff\n"
        "ds_read_b64_tr_b16 a[138:139], v33 offset:6280\n"
        "v_add_f32 v108, v108, v91\n"
        "v_add_f32 v104, v104, v92\n"
        "v_mfma_f32_16x16x32_bf16 v[96:99], a[208:211], v[56:59], v[96:99]\n"
        "s_mov_b32 s72, 0x0\n"
        "ds_read_b64_tr_b16 a[140:141], v32 offset:12424\n"
        "v_add_f32 v104, v104, v93\n"
        "v_add_f32 v104, v104, v94\n"
        "v_mfma_f32_16x16x32_bf16 v[96:99], a[204:207], v[60:63], v[96:99]\n"
        "s_mov_b32 s73, 0x20000\n"
        "ds_read_b64_tr_b16 a[142:143], v33 offset:12424\n"
        "v_add_f32 v104, v104, v95\n"
        "v_mov_b32 v105, v104\n"
        "v_mfma_f32_16x16x32_bf16 v[56:59], a[144:147], a[84:87], 0\n"
        "s_mov_b32 s76, 0x3c003c00\n"
        "v_permlane32_swap_b32 v104, v105\n"
        "v_add_f32 v104, v104, v105\n"
        "v_mfma_f32_16x16x32_bf16 v[56:59], a[148:151], a[88:91], v[56:59]\n"
        "s_mov_b32 s77, 0x3c003c00\n"
        "v_mov_b32 v105, v104\n"
        "v_add_f32 v108, v108, v96\n"
        "v_add_f32 v108, v108, v97\n"
        "v_mfma_f32_16x16x32_bf16 v[56:59], a[152:155], a[92:95], v[56:59]\n"
        "s_mov_b64 vcc, s[78:79]\n"
        "buffer_load_dwordx4 v[188:191], v35, s[36:39], 0 offen offset:16\n"
        "v_add_f32 v108, v108, v98\n"
        "v_add_f32 v108, v108, v99\n"
        "v_mfma_f32_16x16x32_bf16 v[60:63], a[164:167], a[92:95], 0\n"
        "s_mov_b32 s80, 0x3c2aaaab\n"
        "v_permlane16_swap_b32 v104, v105\n"
        "v_add_f32 v104, v104, v105\n"
        "v_mfma_f32_16x16x32_bf16 v[60:63], a[160:163], a[88:91], v[60:63]\n"
        "s_mov_b32 s81, 0x3727c5ac\n"
        "v_mul_f32 v104, 0xbc2aaaab, v104\n"
        "v_add_f32 v48, v48, v104\n"
        "v_add_f32 v49, v49, v104\n"
        "v_mfma_f32_16x16x32_bf16 v[60:63], a[156:159], a[84:87], v[60:63]\n"
        "s_mov_b32 s82, 0x43800000\n"
        "v_add_f32 v50, v50, v104\n"
        "v_add_f32 v51, v51, v104\n"
        "v_add_f32 v28, v28, v104\n"
        "v_mfma_f32_16x16x32_bf16 v[64:67], a[168:171], a[84:87], 0\n"
        "s_mov_b32 s84, 0x80000000\n"
        "v_add_f32 v29, v29, v104\n"
        "v_add_f32 v30, v30, v104\n"
        "v_add_f32 v31, v31, v104\n"
        "v_mfma_f32_16x16x32_bf16 v[64:67], a[172:175], a[88:91], v[64:67]\n"
        "s_sub_u32 s85, s48, 1\n"
        "v_add_f32 v68, v68, v104\n"
        "v_add_f32 v69, v69, v104\n"
        "v_add_f32 v70, v70, v104\n"
        "v_mfma_f32_16x16x32_bf16 v[64:67], a[176:179], a[92:95], v[64:67]\n"
        "s_waitcnt lgkmcnt(0)\n"
        "v_add_f32 v71, v71, v104\n"
        "v_add_f32 v76, v76, v104\n"
        "v_add_f32 v77, v77, v104\n"
        "v_mfma_f32_16x16x32_bf16 v[100:103], a[188:191], a[92:95], 0\n"
        "s_barrier\n"
        "v_add_f32 v78, v78, v104\n"
        "v_add_f32 v79, v79, v104\n"
        "v_add_f32 v84, v84, v104\n"
        "v_mfma_f32_16x16x32_bf16 v[100:103], a[184:187], a[88:91], v[100:103]\n"
        "s_mov_b32 s86, 0x80000000\n"
        "v_add_f32 v85, v85, v104\n"
        "v_add_f32 v86, v86, v104\n"
        "v_add_f32 v87, v87, v104\n"
        "v_mfma_f32_16x16x32_bf16 v[100:103], a[180:183], a[84:87], v[100:103]\n"
        "s_mov_b32 s89, 0x80000000\n"
        "buffer_load_dwordx4 v[194:197], v35, s[36:39], 0 offen offset:32\n"
        "v_add_f32 v92, v92, v104\n"
        "v_add_f32 v93, v93, v104\n"
        "v_mfma_f32_16x16x32_bf16 v[32:35], a[192:195], a[84:87], 0\n"
        "s_mov_b32 s29, 1\n"
        "v_add_f32 v94, v94, v104\n"
        "v_add_f32 v95, v95, v104\n"
        "v_cvt_pk_bf16_f32 v104, v48, v49\n"
        "v_mfma_f32_16x16x32_bf16 v[32:35], a[196:199], a[88:91], v[32:35]\n"
        "s_mov_b32 s31, 0x400000\n"
        "v_cvt_pk_bf16_f32 v105, v50, v51\n"
        "v_cvt_pk_bf16_f32 v106, v28, v29\n"
        "v_cvt_pk_bf16_f32 v107, v30, v31\n"
        "v_mfma_f32_16x16x32_bf16 v[32:35], a[200:203], a[92:95], v[32:35]\n"
        "s_mov_b32 s87, s49\n"
        "ds_write_b128 v37, v[104:107]\n"
        "v_cvt_pk_bf16_f32 v28, v68, v69\n"
        "v_mfma_f32_16x16x32_bf16 v[48:51], a[212:215], a[92:95], 0\n"
        "s_mov_b32 s88, s50\n"
        "v_cvt_pk_bf16_f32 v29, v70, v71\n"
        "v_cvt_pk_bf16_f32 v30, v76, v77\n"
        "v_cvt_pk_bf16_f32 v31, v78, v79\n"
        "v_mfma_f32_16x16x32_bf16 v[48:51], a[208:211], a[88:91], v[48:51]\n"
        "s_mov_b32 s54, 0x05040302\n"
        "ds_write_b128 v37, v[28:31] offset:1024\n"
        "v_cvt_pk_bf16_f32 v28, v84, v85\n"
        "v_mfma_f32_16x16x32_bf16 v[48:51], a[204:207], a[84:87], v[48:51]\n"
        "s_mov_b32 s55, 0x07060302\n"
        "v_cvt_pk_bf16_f32 v29, v86, v87\n"
        "v_cvt_pk_bf16_f32 v30, v92, v93\n"
        "v_cvt_pk_bf16_f32 v31, v94, v95\n"
        "v_mfma_f32_16x16x32_bf16 v[68:71], a[144:147], a[96:99], 0\n"
        "s_mov_b64 s[44:45], s[40:41]\n"
        "ds_write_b128 v37, v[28:31] offset:2048\n"
        "v_mov_b32 v109, v108\n"
        "v_mfma_f32_16x16x32_bf16 v[68:71], a[148:151], a[100:103], v[68:71]\n"
        "s_add_u32 m0, s52, 0\n"
        "v_permlane32_swap_b32 v108, v109\n"
        "v_add_f32 v108, v108, v109\n"
        "v_mfma_f32_16x16x32_bf16 v[68:71], a[152:155], a[104:107], v[68:71]\n"
        "s_mov_b32 s46, 0\n"
        "buffer_load_dwordx4 v0, s[36:39], s49 offen lds\n"
        "v_mov_b32 v109, v108\n"
        "v_add_f32 v116, v56, v57\n"
        "v_mfma_f32_16x16x32_bf16 v[28:31], a[164:167], a[104:107], 0\n"
        "s_mov_b32 s47, s43\n"
        "v_permlane16_swap_b32 v108, v109\n"
        "v_add_f32 v108, v108, v109\n"
        "v_mfma_f32_16x16x32_bf16 v[28:31], a[160:163], a[100:103], v[28:31]\n"
        "s_waitcnt lgkmcnt(3)\n"
        "v_add_f32 v116, v116, v58\n"
        "v_mul_f32 v108, 0xbc2aaaab, v108\n"
        "v_add_f32 v116, v116, v59\n"
        "v_mfma_f32_16x16x32_bf16 v[28:31], a[156:159], a[96:99], v[28:31]\n"
        "s_waitcnt lgkmcnt(3)\n"
        "v_add_f32 v44, v44, v108\n"
        "v_add_f32 v45, v45, v108\n"
        "v_add_f32 v46, v46, v108\n"
        "v_mfma_f32_16x16x32_bf16 v[76:79], a[168:171], a[96:99], 0\n"
        "s_waitcnt lgkmcnt(3)\n"
        "v_add_f32 v47, v47, v108\n"
        "v_add_f32 v52, v52, v108\n"
        "v_add_f32 v53, v53, v108\n"
        "v_mfma_f32_16x16x32_bf16 v[76:79], a[172:175], a[100:103], v[76:79]\n"
        "v_add_f32 v54, v54, v108\n"
        "v_add_f32 v55, v55, v108\n"
        "v_add_f32 v72, v72, v108\n"
        "v_mfma_f32_16x16x32_bf16 v[76:79], a[176:179], a[104:107], v[76:79]\n"
        "buffer_load_dwordx4 v1, s[36:39], s49 offen offset:1024 lds\n"
        "v_add_f32 v73, v73, v108\n"
        "v_add_f32 v74, v74, v108\n"
        "v_mfma_f32_16x16x32_bf16 v[84:87], a[188:191], a[104:107], 0\n"
        "v_add_f32 v75, v75, v108\n"
        "v_add_f32 v80, v80, v108\n"
        "v_add_f32 v81, v81, v108\n"
        "v_mfma_f32_16x16x32_bf16 v[84:87], a[184:187], a[100:103], v[84:87]\n"
        "v_add_f32 v82, v82, v108\n"
        "v_add_f32 v83, v83, v108\n"
        "v_add_f32 v88, v88, v108\n"
        "v_mfma_f32_16x16x32_bf16 v[84:87], a[180:183], a[96:99], v[84:87]\n"
        "v_add_f32 v89, v89, v108\n"
        "v_add_f32 v90, v90, v108\n"
        "v_add_f32 v91, v91, v108\n"
        "v_mfma_f32_16x16x32_bf16 v[92:95], a[192:195], a[96:99], 0\n"
        "v_add_f32 v96, v96, v108\n"
        "v_add_f32 v97, v97, v108\n"
        "v_add_f32 v98, v98, v108\n"
        "v_mfma_f32_16x16x32_bf16 v[92:95], a[196:199], a[100:103], v[92:95]\n"
        "v_add_f32 v99, v99, v108\n"
        "v_cvt_pk_bf16_f32 v104, v44, v45\n"
        "v_cvt_pk_bf16_f32 v105, v46, v47\n"
        "v_mfma_f32_16x16x32_bf16 v[92:95], a[200:203], a[104:107], v[92:95]\n"
        "buffer_load_dwordx4 v2, s[36:39], s49 offen offset:2048 lds\n"
        "v_cvt_pk_bf16_f32 v106, v52, v53\n"
        "v_cvt_pk_bf16_f32 v107, v54, v55\n"
        "v_mfma_f32_16x16x32_bf16 v[44:47], a[212:215], a[104:107], 0\n"
        "ds_write_b128 v36, v[104:107]\n"
        "v_cvt_pk_bf16_f32 v52, v72, v73\n"
        "v_mfma_f32_16x16x32_bf16 v[44:47], a[208:211], a[100:103], v[44:47]\n"
        "v_cvt_pk_bf16_f32 v53, v74, v75\n"
        "v_cvt_pk_bf16_f32 v54, v80, v81\n"
        "v_cvt_pk_bf16_f32 v55, v82, v83\n"
        "v_mfma_f32_16x16x32_bf16 v[44:47], a[204:207], a[96:99], v[44:47]\n"
        "ds_write_b128 v36, v[52:55] offset:64\n"
        "v_cvt_pk_bf16_f32 v52, v88, v89\n"
        "v_mfma_f32_16x16x32_bf16 v[72:75], a[144:147], a[108:111], 0\n"
        "v_cvt_pk_bf16_f32 v53, v90, v91\n"
        "v_cvt_pk_bf16_f32 v54, v96, v97\n"
        "v_cvt_pk_bf16_f32 v55, v98, v99\n"
        "v_mfma_f32_16x16x32_bf16 v[72:75], a[148:151], a[112:115], v[72:75]\n"
        "ds_write_b128 v36, v[52:55] offset:128\n"
        "v_add_f32 v116, v116, v60\n"
        "v_mfma_f32_16x16x32_bf16 v[72:75], a[152:155], a[116:119], v[72:75]\n"
        "buffer_load_dwordx4 v3, s[36:39], s49 offen offset:3072 lds\n"
        "v_add_f32 v116, v116, v61\n"
        "v_add_f32 v116, v116, v62\n"
        "v_mfma_f32_16x16x32_bf16 v[52:55], a[164:167], a[116:119], 0\n"
        "v_add_f32 v116, v116, v63\n"
        "v_add_f32 v116, v116, v64\n"
        "v_add_f32 v116, v116, v65\n"
        "v_mfma_f32_16x16x32_bf16 v[52:55], a[160:163], a[112:115], v[52:55]\n"
        "v_add_f32 v116, v116, v66\n"
        "v_add_f32 v116, v116, v67\n"
        "v_add_f32 v116, v116, v100\n"
        "v_mfma_f32_16x16x32_bf16 v[52:55], a[156:159], a[108:111], v[52:55]\n"
        "v_add_f32 v116, v116, v101\n"
        "v_add_f32 v116, v116, v102\n"
        "v_add_f32 v116, v116, v103\n"
        "v_mfma_f32_16x16x32_bf16 v[80:83], a[168:171], a[108:111], 0\n"
        "v_add_f32 v116, v116, v32\n"
        "v_add_f32 v116, v116, v33\n"
        "v_add_f32 v116, v116, v34\n"
        "v_mfma_f32_16x16x32_bf16 v[80:83], a[172:175], a[112:115], v[80:83]\n"
        "s_add_u32 m0, s52, 4096\n"
        "v_add_f32 v116, v116, v35\n"
        "v_add_f32 v116, v116, v48\n"
        "v_add_f32 v116, v116, v49\n"
        "v_mfma_f32_16x16x32_bf16 v[80:83], a[176:179], a[116:119], v[80:83]\n"
        "buffer_load_dwordx4 v4, s[36:39], s49 offen lds\n"
        "v_add_f32 v116, v116, v50\n"
        "v_add_f32 v116, v116, v51\n"
        "v_mfma_f32_16x16x32_bf16 v[88:91], a[188:191], a[116:119], 0\n"
        "v_mov_b32 v117, v116\n"
        "v_add_f32 v36, v68, v69\n"
        "v_add_f32 v36, v36, v70\n"
        "v_mfma_f32_16x16x32_bf16 v[88:91], a[184:187], a[112:115], v[88:91]\n"
        "v_permlane32_swap_b32 v116, v117\n"
        "v_add_f32 v116, v116, v117\n"
        "v_mfma_f32_16x16x32_bf16 v[88:91], a[180:183], a[108:111], v[88:91]\n"
        "v_mov_b32 v117, v116\n"
        "v_add_f32 v36, v36, v71\n"
        "v_add_f32 v36, v36, v28\n"
        "v_mfma_f32_16x16x32_bf16 v[96:99], a[192:195], a[108:111], 0\n"
        "v_permlane16_swap_b32 v116, v117\n"
        "v_add_f32 v116, v116, v117\n"
        "v_mfma_f32_16x16x32_bf16 v[96:99], a[196:199], a[112:115], v[96:99]\n"
        "v_mul_f32 v116, 0xbc2aaaab, v116\n"
        "v_add_f32 v56, v56, v116\n"
        "v_add_f32 v57, v57, v116\n"
        "v_mfma_f32_16x16x32_bf16 v[96:99], a[200:203], a[116:119], v[96:99]\n"
        "buffer_load_dwordx4 v5, s[36:39], s49 offen offset:1024 lds\n"
        "v_add_f32 v58, v58, v116\n"
        "v_add_f32 v59, v59, v116\n"
        "v_mfma_f32_16x16x32_bf16 v[104:107], a[212:215], a[116:119], 0\n"
        "v_add_f32 v60, v60, v116\n"
        "v_add_f32 v61, v61, v116\n"
        "v_add_f32 v62, v62, v116\n"
        "v_mfma_f32_16x16x32_bf16 v[104:107], a[208:211], a[112:115], v[104:107]\n"
        "v_add_f32 v63, v63, v116\n"
        "v_add_f32 v64, v64, v116\n"
        "v_add_f32 v65, v65, v116\n"
        "v_mfma_f32_16x16x32_bf16 v[104:107], a[204:207], a[108:111], v[104:107]\n"
        "v_add_f32 v66, v66, v116\n"
        "v_add_f32 v67, v67, v116\n"
        "v_add_f32 v100, v100, v116\n"
        "v_mfma_f32_16x16x32_bf16 v[108:111], a[144:147], a[120:123], 0\n"
        "v_add_f32 v101, v101, v116\n"
        "v_add_f32 v102, v102, v116\n"
        "v_add_f32 v103, v103, v116\n"
        "v_mfma_f32_16x16x32_bf16 v[108:111], a[148:151], a[124:127], v[108:111]\n"
        "v_add_f32 v32, v32, v116\n"
        "v_add_f32 v33, v33, v116\n"
        "v_add_f32 v34, v34, v116\n"
        "v_mfma_f32_16x16x32_bf16 v[108:111], a[152:155], a[128:131], v[108:111]\n"
        "buffer_load_dwordx4 v6, s[36:39], s49 offen offset:2048 lds\n"
        "v_add_f32 v35, v35, v116\n"
        "v_add_f32 v48, v48, v116\n"
        "v_mfma_f32_16x16x32_bf16 v[112:115], a[164:167], a[128:131], 0\n"
        "v_add_f32 v49, v49, v116\n"
        "v_add_f32 v50, v50, v116\n"
        "v_add_f32 v51, v51, v116\n"
        "v_mfma_f32_16x16x32_bf16 v[112:115], a[160:163], a[124:127], v[112:115]\n"
        "v_cvt_pk_bf16_f32 v116, v56, v57\n"
        "v_cvt_pk_bf16_f32 v117, v58, v59\n"
        "v_cvt_pk_bf16_f32 v118, v60, v61\n"
        "v_mfma_f32_16x16x32_bf16 v[112:115], a[156:159], a[120:123], v[112:115]\n"
        "v_cvt_pk_bf16_f32 v119, v62, v63\n"
        "v_cvt_pk_bf16_f32 v56, v64, v65\n"
        "v_cvt_pk_bf16_f32 v57, v66, v67\n"
        "v_mfma_f32_16x16x32_bf16 v[60:63], a[168:171], a[120:123], 0\n"
        "ds_write_b128 v38, v[116:119]\n"
        "v_cvt_pk_bf16_f32 v58, v100, v101\n"
        "v_mfma_f32_16x16x32_bf16 v[60:63], a[172:175], a[124:127], v[60:63]\n"
        "v_cvt_pk_bf16_f32 v59, v102, v103\n"
        "v_cvt_pk_bf16_f32 v64, v32, v33\n"
        "v_cvt_pk_bf16_f32 v65, v34, v35\n"
        "v_mfma_f32_16x16x32_bf16 v[60:63], a[176:179], a[128:131], v[60:63]\n"
        "ds_write_b128 v38, v[56:59] offset:1024\n"
        "v_cvt_pk_bf16_f32 v66, v48, v49\n"
        "v_mfma_f32_16x16x32_bf16 v[32:35], a[188:191], a[128:131], 0\n"
        "buffer_load_dwordx4 v7, s[36:39], s49 offen offset:3072 lds\n"
        "v_cvt_pk_bf16_f32 v67, v50, v51\n"
        "v_add_f32 v36, v36, v29\n"
        "v_mfma_f32_16x16x32_bf16 v[32:35], a[184:187], a[124:127], v[32:35]\n"
        "ds_write_b128 v38, v[64:67] offset:2048\n"
        "v_add_f32 v36, v36, v30\n"
        "v_mfma_f32_16x16x32_bf16 v[32:35], a[180:183], a[120:123], v[32:35]\n"
        "v_add_f32 v36, v36, v31\n"
        "v_add_f32 v36, v36, v76\n"
        "v_add_f32 v36, v36, v77\n"
        "v_mfma_f32_16x16x32_bf16 v[48:51], a[192:195], a[120:123], 0\n"
        "v_add_f32 v36, v36, v78\n"
        "v_add_f32 v36, v36, v79\n"
        "v_add_f32 v36, v36, v84\n"
        "v_mfma_f32_16x16x32_bf16 v[48:51], a[196:199], a[124:127], v[48:51]\n"
        "s_add_u32 m0, s52, 8192\n"
        "v_add_f32 v36, v36, v85\n"
        "v_add_f32 v36, v36, v86\n"
        "v_add_f32 v36, v36, v87\n"
        "v_mfma_f32_16x16x32_bf16 v[48:51], a[200:203], a[128:131], v[48:51]\n"
        "buffer_load_dwordx4 v8, s[36:39], s49 offen lds\n"
        "v_add_f32 v36, v36, v92\n"
        "v_add_f32 v36, v36, v93\n"
        "v_mfma_f32_16x16x32_bf16 v[56:59], a[212:215], a[128:131], 0\n"
        "v_add_f32 v36, v36, v94\n"
        "v_add_f32 v36, v36, v95\n"
        "v_add_f32 v36, v36, v44\n"
        "v_mfma_f32_16x16x32_bf16 v[56:59], a[208:211], a[124:127], v[56:59]\n"
        "v_add_f32 v36, v36, v45\n"
        "v_add_f32 v36, v36, v46\n"
        "v_add_f32 v36, v36, v47\n"
        "v_mfma_f32_16x16x32_bf16 v[56:59], a[204:207], a[120:123], v[56:59]\n"
        "v_mov_b32 v37, v36\n"
        "v_add_f32 v38, v72, v73\n"
        "v_add_f32 v38, v38, v74\n"
        "v_mfma_f32_16x16x32_bf16 v[64:67], a[144:147], a[132:135], 0\n"
        "v_permlane32_swap_b32 v36, v37\n"
        "v_add_f32 v36, v36, v37\n"
        "v_mfma_f32_16x16x32_bf16 v[64:67], a[148:151], a[136:139], v[64:67]\n"
        "v_mov_b32 v37, v36\n"
        "v_add_f32 v38, v38, v75\n"
        "v_add_f32 v38, v38, v52\n"
        "v_mfma_f32_16x16x32_bf16 v[64:67], a[152:155], a[140:143], v[64:67]\n"
        "v_permlane16_swap_b32 v36, v37\n"
        "v_add_f32 v36, v36, v37\n"
        "v_mfma_f32_16x16x32_bf16 v[100:103], a[164:167], a[140:143], 0\n"
        "v_mul_f32 v36, 0xbc2aaaab, v36\n"
        "v_add_f32 v68, v68, v36\n"
        "v_add_f32 v69, v69, v36\n"
        "v_mfma_f32_16x16x32_bf16 v[100:103], a[160:163], a[136:139], v[100:103]\n"
        "v_add_f32 v70, v70, v36\n"
        "v_add_f32 v71, v71, v36\n"
        "v_add_f32 v28, v28, v36\n"
        "v_mfma_f32_16x16x32_bf16 v[100:103], a[156:159], a[132:135], v[100:103]\n"
        "v_add_f32 v29, v29, v36\n"
        "v_add_f32 v30, v30, v36\n"
        "v_add_f32 v31, v31, v36\n"
        "v_mfma_f32_16x16x32_bf16 v[116:119], a[168:171], a[132:135], 0\n"
        "v_add_f32 v76, v76, v36\n"
        "v_add_f32 v77, v77, v36\n"
        "v_add_f32 v78, v78, v36\n"
        "v_mfma_f32_16x16x32_bf16 v[116:119], a[172:175], a[136:139], v[116:119]\n"
        "v_add_f32 v79, v79, v36\n"
        "v_add_f32 v84, v84, v36\n"
        "v_add_f32 v85, v85, v36\n"
        "v_mfma_f32_16x16x32_bf16 v[116:119], a[176:179], a[140:143], v[116:119]\n"
        "v_add_f32 v86, v86, v36\n"
        "v_add_f32 v87, v87, v36\n"
        "v_add_f32 v92, v92, v36\n"
        "v_mfma_f32_16x16x32_bf16 v[120:123], a[188:191], a[140:143], 0\n"
        "v_add_f32 v93, v93, v36\n"
        "v_add_f32 v94, v94, v36\n"
        "v_add_f32 v95, v95, v36\n"
        "v_mfma_f32_16x16x32_bf16 v[120:123], a[184:187], a[136:139], v[120:123]\n"
        "v_add_f32 v44, v44, v36\n"
        "v_add_f32 v45, v45, v36\n"
        "v_add_f32 v46, v46, v36\n"
        "v_mfma_f32_16x16x32_bf16 v[120:123], a[180:183], a[132:135], v[120:123]\n"
        "v_add_f32 v47, v47, v36\n"
        "v_cvt_pk_bf16_f32 v124, v68, v69\n"
        "v_cvt_pk_bf16_f32 v125, v70, v71\n"
        "v_mfma_f32_16x16x32_bf16 v[68:71], a[192:195], a[132:135], 0\n"
        "v_cvt_pk_bf16_f32 v126, v28, v29\n"
        "v_cvt_pk_bf16_f32 v127, v30, v31\n"
        "v_cvt_pk_bf16_f32 v28, v76, v77\n"
        "v_mfma_f32_16x16x32_bf16 v[68:71], a[196:199], a[136:139], v[68:71]\n"
        "ds_write_b128 v39, v[124:127]\n"
        "v_cvt_pk_bf16_f32 v29, v78, v79\n"
        "v_mfma_f32_16x16x32_bf16 v[68:71], a[200:203], a[140:143], v[68:71]\n"
        "v_cvt_pk_bf16_f32 v30, v84, v85\n"
        "v_cvt_pk_bf16_f32 v31, v86, v87\n"
        "v_cvt_pk_bf16_f32 v76, v92, v93\n"
        "v_mfma_f32_16x16x32_bf16 v[84:87], a[212:215], a[140:143], 0\n"
        "ds_write_b128 v39, v[28:31] offset:1024\n"
        "v_cvt_pk_bf16_f32 v77, v94, v95\n"
        "v_mfma_f32_16x16x32_bf16 v[84:87], a[208:211], a[136:139], v[84:87]\n"
        "v_cvt_pk_bf16_f32 v78, v44, v45\n"
        "v_cvt_pk_bf16_f32 v79, v46, v47\n"
        "v_add_f32 v38, v38, v53\n"
        "v_mfma_f32_16x16x32_bf16 v[84:87], a[204:207], a[132:135], v[84:87]\n"
        "ds_write_b128 v39, v[76:79] offset:2048\n"
        "v_add_f32 v38, v38, v54\n"
        "v_add_f32 v38, v38, v55\n"
        "v_add_f32 v38, v38, v80\n"
        "v_add_f32 v38, v38, v81\n"
        "v_add_f32 v38, v38, v82\n"
        "v_add_f32 v38, v38, v83\n"
        "v_add_f32 v38, v38, v88\n"
        "v_add_f32 v38, v38, v89\n"
        "v_add_f32 v38, v38, v90\n"
        "v_add_f32 v38, v38, v91\n"
        "v_add_f32 v38, v38, v96\n"
        "v_add_f32 v38, v38, v97\n"
        "v_add_f32 v38, v38, v98\n"
        "v_add_f32 v38, v38, v99\n"
        "v_add_f32 v38, v38, v104\n"
        "v_add_f32 v38, v38, v105\n"
        "v_add_f32 v38, v38, v106\n"
        "v_add_f32 v38, v38, v107\n"
        "v_mov_b32 v39, v38\n"
        "v_add_f32 v28, v108, v109\n"
        "v_add_f32 v28, v28, v110\n"
        "v_permlane32_swap_b32 v38, v39\n"
        "v_add_f32 v38, v38, v39\n"
        "v_mov_b32 v39, v38\n"
        "v_add_f32 v28, v28, v111\n"
        "v_add_f32 v28, v28, v112\n"
        "v_permlane16_swap_b32 v38, v39\n"
        "v_add_f32 v38, v38, v39\n"
        "v_mul_f32 v38, 0xbc2aaaab, v38\n"
        "v_add_f32 v28, v28, v113\n"
        "v_add_f32 v28, v28, v114\n"
        "v_add_f32 v28, v28, v115\n"
        "v_add_f32 v28, v28, v60\n"
        "v_add_f32 v28, v28, v61\n"
        "v_add_f32 v28, v28, v62\n"
        "v_add_f32 v28, v28, v63\n"
        "v_add_f32 v28, v28, v32\n"
        "v_add_f32 v28, v28, v33\n"
        "v_add_f32 v28, v28, v34\n"
        "v_add_f32 v28, v28, v35\n"
        "v_add_f32 v28, v28, v48\n"
        "v_add_f32 v28, v28, v49\n"
        "v_add_f32 v28, v28, v50\n"
        "v_add_f32 v28, v28, v51\n"
        "v_add_f32 v28, v28, v56\n"
        "v_add_f32 v28, v28, v57\n"
        "v_add_f32 v28, v28, v58\n"
        "v_add_f32 v28, v28, v59\n"
        "v_mov_b32 v29, v28\n"
        "v_add_f32 v76, v64, v65\n"
        "v_add_f32 v76, v76, v66\n"
        "v_permlane32_swap_b32 v28, v29\n"
        "v_add_f32 v28, v28, v29\n"
        "v_mov_b32 v29, v28\n"
        "v_add_f32 v76, v76, v67\n"
        "v_add_f32 v76, v76, v100\n"
        "v_permlane16_swap_b32 v28, v29\n"
        "v_add_f32 v28, v28, v29\n"
        "v_mul_f32 v28, 0xbc2aaaab, v28\n"
        "v_add_f32 v76, v76, v101\n"
        "v_add_f32 v76, v76, v102\n"
        "v_add_f32 v76, v76, v103\n"
        "v_add_f32 v76, v76, v116\n"
        "v_add_f32 v76, v76, v117\n"
        "v_add_f32 v76, v76, v118\n"
        "v_add_f32 v76, v76, v119\n"
        "v_add_f32 v76, v76, v120\n"
        "v_add_f32 v76, v76, v121\n"
        "v_add_f32 v76, v76, v122\n"
        "v_add_f32 v76, v76, v123\n"
        "v_add_f32 v76, v76, v68\n"
        "v_add_f32 v76, v76, v69\n"
        "v_add_f32 v76, v76, v70\n"
        "v_add_f32 v76, v76, v71\n"
        "v_pk_add_f32 v[72:73], v[72:73], v[38:39] op_sel_hi:[1,0]\n"
        "v_pk_add_f32 v[74:75], v[74:75], v[38:39] op_sel_hi:[1,0]\n"
        "v_pk_add_f32 v[52:53], v[52:53], v[38:39] op_sel_hi:[1,0]\n"
        "v_pk_add_f32 v[54:55], v[54:55], v[38:39] op_sel_hi:[1,0]\n"
        "v_pk_add_f32 v[80:81], v[80:81], v[38:39] op_sel_hi:[1,0]\n"
        "v_pk_add_f32 v[82:83], v[82:83], v[38:39] op_sel_hi:[1,0]\n"
        "v_pk_add_f32 v[88:89], v[88:89], v[38:39] op_sel_hi:[1,0]\n"
        "v_pk_add_f32 v[90:91], v[90:91], v[38:39] op_sel_hi:[1,0]\n"
        "v_pk_add_f32 v[96:97], v[96:97], v[38:39] op_sel_hi:[1,0]\n"
        "v_pk_add_f32 v[98:99], v[98:99], v[38:39] op_sel_hi:[1,0]\n"
        "v_pk_add_f32 v[104:105], v[104:105], v[38:39] op_sel_hi:[1,0]\n"
        "v_pk_add_f32 v[106:107], v[106:107], v[38:39] op_sel_hi:[1,0]\n"
        "v_pk_add_f32 v[108:109], v[108:109], v[28:29] op_sel_hi:[1,0]\n"
        "v_pk_add_f32 v[110:111], v[110:111], v[28:29] op_sel_hi:[1,0]\n"
        "v_pk_add_f32 v[112:113], v[112:113], v[28:29] op_sel_hi:[1,0]\n"
        "v_pk_add_f32 v[114:115], v[114:115], v[28:29] op_sel_hi:[1,0]\n"
        "v_pk_add_f32 v[60:61], v[60:61], v[28:29] op_sel_hi:[1,0]\n"
        "v_pk_add_f32 v[62:63], v[62:63], v[28:29] op_sel_hi:[1,0]\n"
        "v_pk_add_f32 v[32:33], v[32:33], v[28:29] op_sel_hi:[1,0]\n"
        "v_pk_add_f32 v[34:35], v[34:35], v[28:29] op_sel_hi:[1,0]\n"
        "v_pk_add_f32 v[48:49], v[48:49], v[28:29] op_sel_hi:[1,0]\n"
        "v_pk_add_f32 v[50:51], v[50:51], v[28:29] op_sel_hi:[1,0]\n"
        "v_pk_add_f32 v[56:57], v[56:57], v[28:29] op_sel_hi:[1,0]\n"
        "v_pk_add_f32 v[58:59], v[58:59], v[28:29] op_sel_hi:[1,0]\n"
        "v_cvt_pk_bf16_f32 v28, v72, v73\n"
        "v_cvt_pk_bf16_f32 v29, v74, v75\n"
        "v_cvt_pk_bf16_f32 v30, v52, v53\n"
        "v_cvt_pk_bf16_f32 v31, v54, v55\n"
        "v_cvt_pk_bf16_f32 v36, v80, v81\n"
        "v_cvt_pk_bf16_f32 v37, v82, v83\n"
        "v_cvt_pk_bf16_f32 v38, v88, v89\n"
        "v_cvt_pk_bf16_f32 v39, v90, v91\n"
        "v_cvt_pk_bf16_f32 v44, v96, v97\n"
        "v_cvt_pk_bf16_f32 v45, v98, v99\n"
        "v_cvt_pk_bf16_f32 v46, v104, v105\n"
        "v_cvt_pk_bf16_f32 v47, v106, v107\n"
        "v_cvt_pk_bf16_f32 v52, v108, v109\n"
        "v_cvt_pk_bf16_f32 v53, v110, v111\n"
        "v_cvt_pk_bf16_f32 v54, v112, v113\n"
        "v_cvt_pk_bf16_f32 v55, v114, v115\n"
        "v_cvt_pk_bf16_f32 v72, v60, v61\n"
        "v_cvt_pk_bf16_f32 v73, v62, v63\n"
        "v_cvt_pk_bf16_f32 v74, v32, v33\n"
        "v_cvt_pk_bf16_f32 v75, v34, v35\n"
        "v_cvt_pk_bf16_f32 v32, v48, v49\n"
        "v_cvt_pk_bf16_f32 v33, v50, v51\n"
        "v_cvt_pk_bf16_f32 v34, v56, v57\n"
        "v_cvt_pk_bf16_f32 v35, v58, v59\n"
        "v_add_f32 v76, v76, v84\n"
        "ds_write_b128 v40, v[28:31]\n"
        "ds_write_b128 v40, v[36:39] offset:1024\n"
        "ds_write_b128 v40, v[44:47] offset:2048\n"
        "ds_write_b128 v41, v[52:55]\n"
        "ds_write_b128 v41, v[72:75] offset:1024\n"
        "ds_write_b128 v41, v[32:35] offset:2048\n"
        "v_add_f32 v76, v76, v85\n"
        "v_add_f32 v76, v76, v86\n"
        "v_add_f32 v76, v76, v87\n"
        "v_mov_b32 v77, v76\n"
        "s_waitcnt vmcnt(9)\n"
        "s_nop 0\n"
        "v_permlane32_swap_b32 v76, v77\n"
        "v_add_f32 v76, v76, v77\n"
        "v_mov_b32 v77, v76\n"
        "s_nop 1\n"
        "v_permlane16_swap_b32 v76, v77\n"
        "v_add_f32 v76, v76, v77\n"
        "v_mul_f32 v76, 0xbc2aaaab, v76\n"
        "v_pk_add_f32 v[64:65], v[64:65], v[76:77] op_sel_hi:[1,0]\n"
        "v_pk_add_f32 v[66:67], v[66:67], v[76:77] op_sel_hi:[1,0]\n"
        "v_pk_add_f32 v[100:101], v[100:101], v[76:77] op_sel_hi:[1,0]\n"
        "v_pk_add_f32 v[102:103], v[102:103], v[76:77] op_sel_hi:[1,0]\n"
        "v_pk_add_f32 v[116:117], v[116:117], v[76:77] op_sel_hi:[1,0]\n"
        "v_pk_add_f32 v[118:119], v[118:119], v[76:77] op_sel_hi:[1,0]\n"
        "v_pk_add_f32 v[120:121], v[120:121], v[76:77] op_sel_hi:[1,0]\n"
        "v_pk_add_f32 v[122:123], v[122:123], v[76:77] op_sel_hi:[1,0]\n"
        "v_pk_add_f32 v[68:69], v[68:69], v[76:77] op_sel_hi:[1,0]\n"
        "v_pk_add_f32 v[70:71], v[70:71], v[76:77] op_sel_hi:[1,0]\n"
        "v_pk_add_f32 v[84:85], v[84:85], v[76:77] op_sel_hi:[1,0]\n"
        "v_pk_add_f32 v[86:87], v[86:87], v[76:77] op_sel_hi:[1,0]\n"
        "v_cvt_pk_bf16_f32 v28, v64, v65\n"
        "v_cvt_pk_bf16_f32 v29, v66, v67\n"
        "v_cvt_pk_bf16_f32 v30, v100, v101\n"
        "v_cvt_pk_bf16_f32 v31, v102, v103\n"
        "v_cvt_pk_bf16_f32 v32, v116, v117\n"
        "v_cvt_pk_bf16_f32 v33, v118, v119\n"
        "v_cvt_pk_bf16_f32 v34, v120, v121\n"
        "v_cvt_pk_bf16_f32 v35, v122, v123\n"
        "v_cvt_pk_bf16_f32 v36, v68, v69\n"
        "v_cvt_pk_bf16_f32 v37, v70, v71\n"
        "v_cvt_pk_bf16_f32 v38, v84, v85\n"
        "v_cvt_pk_bf16_f32 v39, v86, v87\n"
        "ds_write_b128 v42, v[28:31]\n"
        "ds_write_b128 v42, v[32:35] offset:1024\n"
        "ds_write_b128 v42, v[36:39] offset:2048\n"
        "s_waitcnt lgkmcnt(0)\n"
        "s_barrier\n"
        "s_branch FE_%=\n"
        "FG_%=:\n"
        "s_mov_b32 s24, 0x471c4000\n"
        "v_mov_b32 v48, 0x3f803f80\n"
        "v_mov_b32 v49, 0x3f803f80\n"
        "v_mov_b32 v50, 0x3f803f80\n"
        "v_mov_b32 v51, 0x3f803f80\n"
        "s_waitcnt vmcnt(9)\n"
        "ds_read_b64_tr_b16 a[72:73], v32 offset:0\n"
        "ds_read_b64_tr_b16 a[74:75], v33 offset:0\n"
        "ds_read_b64_tr_b16 a[76:77], v32 offset:6144\n"
        "ds_read_b64_tr_b16 a[78:79], v33 offset:6144\n"
        "ds_read_b64_tr_b16 a[80:81], v32 offset:12288\n"
        "ds_read_b64_tr_b16 a[82:83], v33 offset:12288\n"
        "s_add_u32 m0, s53, 9216\n"
        "s_nop 0\n"
        "buffer_load_dwordx4 v44, s[16:19], s21 offen lds\n"
        "ds_read_b64_tr_b16 a[84:85], v32 offset:8\n"
        "ds_read_b64_tr_b16 a[86:87], v33 offset:8\n"
        "ds_read_b64_tr_b16 a[88:89], v32 offset:6152\n"
        "ds_read_b64_tr_b16 a[90:91], v33 offset:6152\n"
        "ds_read_b64_tr_b16 a[92:93], v32 offset:12296\n"
        "ds_read_b64_tr_b16 a[94:95], v33 offset:12296\n"
        "buffer_load_dwordx4 v45, s[16:19], s21 offen offset:1024 lds\n"
        "ds_read_b64_tr_b16 a[96:97], v32 offset:64\n"
        "ds_read_b64_tr_b16 a[98:99], v33 offset:64\n"
        "ds_read_b64_tr_b16 a[100:101], v32 offset:6208\n"
        "ds_read_b64_tr_b16 a[102:103], v33 offset:6208\n"
        "ds_read_b64_tr_b16 a[104:105], v32 offset:12352\n"
        "ds_read_b64_tr_b16 a[106:107], v33 offset:12352\n"
        "buffer_load_dwordx4 v46, s[16:19], s21 offen offset:2048 lds\n"
        "ds_read_b64_tr_b16 a[108:109], v32 offset:72\n"
        "ds_read_b64_tr_b16 a[110:111], v33 offset:72\n"
        "ds_read_b64_tr_b16 a[112:113], v32 offset:6216\n"
        "ds_read_b64_tr_b16 a[114:115], v33 offset:6216\n"
        "ds_read_b64_tr_b16 a[116:117], v32 offset:12360\n"
        "ds_read_b64_tr_b16 a[118:119], v33 offset:12360\n"
        "s_add_u32 m0, s53, 12288\n"
        "s_nop 0\n"
        "buffer_load_dwordx4 v44, s[16:19], s22 offen lds\n"
        "ds_read_b64_tr_b16 a[120:121], v32 offset:128\n"
        "ds_read_b64_tr_b16 a[122:123], v33 offset:128\n"
        "ds_read_b64_tr_b16 a[124:125], v32 offset:6272\n"
        "ds_read_b64_tr_b16 a[126:127], v33 offset:6272\n"
        "ds_read_b64_tr_b16 a[128:129], v32 offset:12416\n"
        "ds_read_b64_tr_b16 a[130:131], v33 offset:12416\n"
        "buffer_load_dwordx4 v45, s[16:19], s22 offen offset:1024 lds\n"
        "ds_read_b64_tr_b16 a[132:133], v32 offset:136\n"
        "ds_read_b64_tr_b16 a[134:135], v33 offset:136\n"
        "ds_read_b64_tr_b16 a[136:137], v32 offset:6280\n"
        "ds_read_b64_tr_b16 a[138:139], v33 offset:6280\n"
        "ds_read_b64_tr_b16 a[140:141], v32 offset:12424\n"
        "ds_read_b64_tr_b16 a[142:143], v33 offset:12424\n"
        "buffer_load_dwordx4 v46, s[16:19], s22 offen offset:2048 lds\n"
        "s_add_u32 m0, s53, 15360\n"
        "s_nop 0\n"
        "buffer_load_dwordx4 v44, s[16:19], s23 offen lds\n"
        "buffer_load_dwordx4 v45, s[16:19], s23 offen offset:1024 lds\n"
        "buffer_load_dwordx4 v46, s[16:19], s23 offen offset:2048 lds\n"
        "s_waitcnt vmcnt(12)\n"
        "ds_read_b128 a[144:147], v28\n"
        "ds_read_b128 a[148:151], v28 offset:64\n"
        "ds_read_b128 a[152:155], v28 offset:128\n"
        "ds_read_b128 a[156:159], v29\n"
        "ds_read_b128 a[160:163], v29 offset:64\n"
        "ds_read_b128 a[164:167], v29 offset:128\n"
        "s_waitcnt lgkmcnt(15)\n"
        "s_waitcnt lgkmcnt(3)\n"
        "v_mfma_f32_16x16x32_bf16 v[30:33], a[144:147], a[72:75], 0\n"
        "s_waitcnt vmcnt(6)\n"
        "ds_read_b128 a[168:171], v28 offset:6144\n"
        "v_mov_b32 v43, 161664\n"
        "v_mfma_f32_16x16x32_bf16 v[30:33], a[148:151], a[76:79], v[30:33]\n"
        "s_waitcnt vmcnt(0)\n"
        "ds_read_b128 a[172:175], v28 offset:6208\n"
        "ds_read_b128 a[176:179], v28 offset:6272\n"
        "v_mfma_f32_16x16x32_bf16 v[30:33], a[152:155], a[80:83], v[30:33]\n"
        "s_waitcnt lgkmcnt(6)\n"
        "ds_read_b128 a[180:183], v29 offset:6144\n"
        "ds_read_b128 a[184:187], v29 offset:6208\n"
        "s_waitcnt lgkmcnt(5)\n"
        "v_mfma_f32_16x16x32_bf16 v[44:47], a[164:167], a[80:83], 0\n"
        "s_waitcnt lgkmcnt(5)\n"
        "ds_read_b128 a[188:191], v29 offset:6272\n"
        "ds_read_b128 a[192:195], v28 offset:12288\n"
        "v_add_f32 v84, v30, v31\n"
        "v_mfma_f32_16x16x32_bf16 v[44:47], a[160:163], a[76:79], v[44:47]\n"
        "s_waitcnt lgkmcnt(7)\n"
        "ds_read_b128 a[196:199], v28 offset:12352\n"
        "ds_read_b128 a[200:203], v28 offset:12416\n"
        "v_add_f32 v84, v84, v32\n"
        "v_mfma_f32_16x16x32_bf16 v[44:47], a[156:159], a[72:75], v[44:47]\n"
        "s_waitcnt lgkmcnt(9)\n"
        "ds_read_b128 a[204:207], v29 offset:12288\n"
        "v_add_f32 v84, v84, v33\n"
        "s_waitcnt lgkmcnt(7)\n"
        "v_mfma_f32_16x16x32_bf16 v[48:51], a[168:171], a[72:75], 0\n"
        "s_waitcnt lgkmcnt(7)\n"
        "ds_read_b128 a[208:211], v29 offset:12352\n"
        "ds_read_b128 a[212:215], v29 offset:12416\n"
        "v_add_f32 v84, v84, v44\n"
        "v_mfma_f32_16x16x32_bf16 v[48:51], a[172:175], a[76:79], v[48:51]\n"
        "s_waitcnt lgkmcnt(9)\n"
        "v_add_f32 v84, v84, v45\n"
        "v_add_f32 v84, v84, v46\n"
        "v_add_f32 v84, v84, v47\n"
        "v_mfma_f32_16x16x32_bf16 v[48:51], a[176:179], a[80:83], v[48:51]\n"
        "s_waitcnt lgkmcnt(6)\n"
        "s_nop 0\n"
        "v_mfma_f32_16x16x32_bf16 v[52:55], a[188:191], a[80:83], 0\n"
        "s_waitcnt lgkmcnt(3)\n"
        "s_nop 1\n"
        "v_mfma_f32_16x16x32_bf16 v[52:55], a[184:187], a[76:79], v[52:55]\n"
        "s_nop 0\n"
        "v_add_f32 v84, v84, v48\n"
        "v_add_f32 v84, v84, v49\n"
        "v_add_f32 v84, v84, v50\n"
        "v_mfma_f32_16x16x32_bf16 v[52:55], a[180:183], a[72:75], v[52:55]\n"
        "s_nop 0\n"
        "v_add_f32 v84, v84, v51\n"
        "v_mfma_f32_16x16x32_bf16 v[56:59], a[192:195], a[72:75], 0\n"
        "s_nop 2\n"
        "v_mfma_f32_16x16x32_bf16 v[56:59], a[196:199], a[76:79], v[56:59]\n"
        "s_nop 0\n"
        "v_add_f32 v84, v84, v52\n"
        "v_add_f32 v84, v84, v53\n"
        "v_add_f32 v84, v84, v54\n"
        "v_mfma_f32_16x16x32_bf16 v[56:59], a[200:203], a[80:83], v[56:59]\n"
        "s_nop 0\n"
        "v_add_f32 v84, v84, v55\n"
        "s_waitcnt lgkmcnt(0)\n"
        "v_mfma_f32_16x16x32_bf16 v[60:63], a[212:215], a[80:83], 0\n"
        "s_nop 1\n"
        "v_mfma_f32_16x16x32_bf16 v[60:63], a[208:211], a[76:79], v[60:63]\n"
        "s_nop 0\n"
        "v_add_f32 v84, v84, v56\n"
        "v_add_f32 v84, v84, v57\n"
        "v_add_f32 v84, v84, v58\n"
        "v_mfma_f32_16x16x32_bf16 v[60:63], a[204:207], a[72:75], v[60:63]\n"
        "s_nop 0\n"
        "v_add_f32 v84, v84, v59\n"
        "v_mfma_f32_16x16x32_bf16 v[64:67], a[144:147], a[84:87], 0\n"
        "s_nop 2\n"
        "v_mfma_f32_16x16x32_bf16 v[64:67], a[148:151], a[88:91], v[64:67]\n"
        "s_nop 0\n"
        "v_add_f32 v84, v84, v60\n"
        "v_add_f32 v84, v84, v61\n"
        "v_add_f32 v84, v84, v62\n"
        "v_mfma_f32_16x16x32_bf16 v[64:67], a[152:155], a[92:95], v[64:67]\n"
        "buffer_load_dwordx4 v[162:165], v34, s[36:39], 0 offen\n"
        "v_add_f32 v84, v84, v63\n"
        "v_mov_b32 v85, v84\n"
        "v_mfma_f32_16x16x32_bf16 v[68:71], a[164:167], a[92:95], 0\n"
        "s_nop 0\n"
        "v_permlane32_swap_b32 v84, v85\n"
        "v_add_f32 v84, v84, v85\n"
        "v_mfma_f32_16x16x32_bf16 v[68:71], a[160:163], a[88:91], v[68:71]\n"
        "v_mov_b32 v85, v84\n"
        "v_add_f32 v36, v64, v65\n"
        "v_add_f32 v36, v36, v66\n"
        "v_mfma_f32_16x16x32_bf16 v[68:71], a[156:159], a[84:87], v[68:71]\n"
        "v_permlane16_swap_b32 v84, v85\n"
        "v_add_f32 v84, v84, v85\n"
        "v_mfma_f32_16x16x32_bf16 v[72:75], a[168:171], a[84:87], 0\n"
        "v_mul_f32 v84, 0xbc2aaaab, v84\n"
        "v_add_f32 v36, v36, v67\n"
        "v_add_f32 v30, v30, v84\n"
        "v_mfma_f32_16x16x32_bf16 v[72:75], a[172:175], a[88:91], v[72:75]\n"
        "v_add_f32 v31, v31, v84\n"
        "v_add_f32 v32, v32, v84\n"
        "v_add_f32 v33, v33, v84\n"
        "v_mfma_f32_16x16x32_bf16 v[72:75], a[176:179], a[92:95], v[72:75]\n"
        "v_add_f32 v44, v44, v84\n"
        "v_add_f32 v45, v45, v84\n"
        "v_add_f32 v46, v46, v84\n"
        "v_mfma_f32_16x16x32_bf16 v[76:79], a[188:191], a[92:95], 0\n"
        "v_add_f32 v47, v47, v84\n"
        "v_add_f32 v48, v48, v84\n"
        "v_add_f32 v49, v49, v84\n"
        "v_mfma_f32_16x16x32_bf16 v[76:79], a[184:187], a[88:91], v[76:79]\n"
        "v_add_f32 v50, v50, v84\n"
        "v_add_f32 v51, v51, v84\n"
        "v_add_f32 v52, v52, v84\n"
        "v_mfma_f32_16x16x32_bf16 v[76:79], a[180:183], a[84:87], v[76:79]\n"
        "buffer_load_dwordx4 v[168:171], v34, s[36:39], 0 offen offset:16\n"
        "v_add_f32 v53, v53, v84\n"
        "v_add_f32 v54, v54, v84\n"
        "v_mfma_f32_16x16x32_bf16 v[80:83], a[192:195], a[84:87], 0\n"
        "s_barrier\n"
        "v_add_f32 v55, v55, v84\n"
        "v_add_f32 v56, v56, v84\n"
        "v_add_f32 v57, v57, v84\n"
        "v_mfma_f32_16x16x32_bf16 v[80:83], a[196:199], a[88:91], v[80:83]\n"
        "s_mov_b32 s78, 0xf0000f\n"
        "v_add_f32 v58, v58, v84\n"
        "v_add_f32 v59, v59, v84\n"
        "v_add_f32 v60, v60, v84\n"
        "v_mfma_f32_16x16x32_bf16 v[80:83], a[200:203], a[92:95], v[80:83]\n"
        "s_mov_b32 s79, 0xf0000f00\n"
        "v_add_f32 v61, v61, v84\n"
        "v_add_f32 v62, v62, v84\n"
        "v_add_f32 v63, v63, v84\n"
        "v_mfma_f32_16x16x32_bf16 v[84:87], a[212:215], a[92:95], 0\n"
        "s_mov_b32 s56, 0x22222222\n"
        "v_cvt_pk_bf16_f32 v28, v30, v31\n"
        "v_cvt_pk_bf16_f32 v29, v32, v33\n"
        "v_cvt_pk_bf16_f32 v30, v44, v45\n"
        "v_mfma_f32_16x16x32_bf16 v[84:87], a[208:211], a[88:91], v[84:87]\n"
        "s_mov_b32 s57, 0x22222222\n"
        "v_cvt_pk_bf16_f32 v31, v46, v47\n"
        "v_cvt_pk_bf16_f32 v44, v48, v49\n"
        "v_cvt_pk_bf16_f32 v45, v50, v51\n"
        "v_mfma_f32_16x16x32_bf16 v[84:87], a[204:207], a[84:87], v[84:87]\n"
        "s_mov_b32 s58, 0x44444444\n"
        "ds_write_b128 v37, v[28:31]\n"
        "v_cvt_pk_bf16_f32 v46, v52, v53\n"
        "v_mfma_f32_16x16x32_bf16 v[48:51], a[144:147], a[96:99], 0\n"
        "s_mov_b32 s59, 0x44444444\n"
        "v_cvt_pk_bf16_f32 v47, v54, v55\n"
        "v_cvt_pk_bf16_f32 v52, v56, v57\n"
        "v_cvt_pk_bf16_f32 v53, v58, v59\n"
        "v_mfma_f32_16x16x32_bf16 v[48:51], a[148:151], a[100:103], v[48:51]\n"
        "s_mov_b32 s60, 0x88888888\n"
        "ds_write_b128 v37, v[44:47] offset:1024\n"
        "v_cvt_pk_bf16_f32 v54, v60, v61\n"
        "v_mfma_f32_16x16x32_bf16 v[48:51], a[152:155], a[104:107], v[48:51]\n"
        "s_mov_b32 s61, 0x88888888\n"
        "buffer_load_dwordx4 v[174:177], v34, s[36:39], 0 offen offset:32\n"
        "v_cvt_pk_bf16_f32 v55, v62, v63\n"
        "v_add_f32 v36, v36, v68\n"
        "v_mfma_f32_16x16x32_bf16 v[56:59], a[164:167], a[104:107], 0\n"
        "s_mov_b32 s62, 0xffff\n"
        "ds_write_b128 v37, v[52:55] offset:2048\n"
        "v_add_f32 v36, v36, v69\n"
        "v_mfma_f32_16x16x32_bf16 v[56:59], a[160:163], a[100:103], v[56:59]\n"
        "s_mov_b32 s63, 0x0\n"
        "v_add_f32 v36, v36, v70\n"
        "v_add_f32 v36, v36, v71\n"
        "v_add_f32 v36, v36, v72\n"
        "v_mfma_f32_16x16x32_bf16 v[56:59], a[156:159], a[96:99], v[56:59]\n"
        "s_mov_b32 s64, 0xffff0000\n"
        "v_add_f32 v36, v36, v73\n"
        "v_add_f32 v36, v36, v74\n"
        "v_add_f32 v36, v36, v75\n"
        "v_mfma_f32_16x16x32_bf16 v[60:63], a[168:171], a[96:99], 0\n"
        "s_mov_b32 s65, 0x0\n"
        "v_add_f32 v36, v36, v76\n"
        "v_add_f32 v36, v36, v77\n"
        "v_add_f32 v36, v36, v78\n"
        "v_mfma_f32_16x16x32_bf16 v[60:63], a[172:175], a[100:103], v[60:63]\n"
        "s_mov_b32 s66, 0x0\n"
        "v_add_f32 v36, v36, v79\n"
        "v_add_f32 v36, v36, v80\n"
        "v_add_f32 v36, v36, v81\n"
        "v_mfma_f32_16x16x32_bf16 v[60:63], a[176:179], a[104:107], v[60:63]\n"
        "s_mov_b32 s67, 0xffff\n"
        "v_add_f32 v36, v36, v82\n"
        "v_add_f32 v36, v36, v83\n"
        "v_add_f32 v36, v36, v84\n"
        "v_mfma_f32_16x16x32_bf16 v[88:91], a[188:191], a[104:107], 0\n"
        "s_mov_b32 s68, 0x0\n"
        "v_add_f32 v36, v36, v85\n"
        "v_add_f32 v36, v36, v86\n"
        "v_add_f32 v36, v36, v87\n"
        "v_mfma_f32_16x16x32_bf16 v[88:91], a[184:187], a[100:103], v[88:91]\n"
        "s_mov_b32 s69, 0xffff0000\n"
        "v_mov_b32 v37, v36\n"
        "v_add_f32 v124, v48, v49\n"
        "v_add_f32 v124, v124, v50\n"
        "v_mfma_f32_16x16x32_bf16 v[88:91], a[180:183], a[96:99], v[88:91]\n"
        "s_mov_b32 s70, 0x0\n"
        "buffer_load_dwordx4 v[182:185], v35, s[36:39], 0 offen\n"
        "v_add_f32 v124, v124, v51\n"
        "v_add_f32 v124, v124, v56\n"
        "v_mfma_f32_16x16x32_bf16 v[92:95], a[192:195], a[96:99], 0\n"
        "s_mov_b32 s71, 0xffffffff\n"
        "v_permlane32_swap_b32 v36, v37\n"
        "v_add_f32 v36, v36, v37\n"
        "v_mfma_f32_16x16x32_bf16 v[92:95], a[196:199], a[100:103], v[92:95]\n"
        "s_mov_b32 s72, 0x0\n"
        "v_mov_b32 v37, v36\n"
        "v_add_f32 v124, v124, v57\n"
        "v_add_f32 v124, v124, v58\n"
        "v_mfma_f32_16x16x32_bf16 v[92:95], a[200:203], a[104:107], v[92:95]\n"
        "s_mov_b32 s73, 0x20000\n"
        "v_permlane16_swap_b32 v36, v37\n"
        "v_add_f32 v36, v36, v37\n"
        "v_mfma_f32_16x16x32_bf16 v[96:99], a[212:215], a[104:107], 0\n"
        "s_mov_b32 s76, 0x3c003c00\n"
        "v_mul_f32 v36, 0xbc2aaaab, v36\n"
        "v_add_f32 v64, v64, v36\n"
        "v_add_f32 v65, v65, v36\n"
        "v_mfma_f32_16x16x32_bf16 v[96:99], a[208:211], a[100:103], v[96:99]\n"
        "s_mov_b32 s77, 0x3c003c00\n"
        "v_add_f32 v66, v66, v36\n"
        "v_add_f32 v67, v67, v36\n"
        "v_add_f32 v68, v68, v36\n"
        "v_mfma_f32_16x16x32_bf16 v[96:99], a[204:207], a[96:99], v[96:99]\n"
        "s_mov_b64 vcc, s[78:79]\n"
        "v_add_f32 v69, v69, v36\n"
        "v_add_f32 v70, v70, v36\n"
        "v_add_f32 v71, v71, v36\n"
        "v_mfma_f32_16x16x32_bf16 v[100:103], a[144:147], a[108:111], 0\n"
        "s_mov_b32 s80, 0x3c2aaaab\n"
        "v_add_f32 v72, v72, v36\n"
        "v_add_f32 v73, v73, v36\n"
        "v_add_f32 v74, v74, v36\n"
        "v_mfma_f32_16x16x32_bf16 v[100:103], a[148:151], a[112:115], v[100:103]\n"
        "s_mov_b32 s81, 0x3727c5ac\n"
        "v_add_f32 v75, v75, v36\n"
        "v_add_f32 v76, v76, v36\n"
        "v_add_f32 v77, v77, v36\n"
        "v_mfma_f32_16x16x32_bf16 v[100:103], a[152:155], a[116:119], v[100:103]\n"
        "s_mov_b32 s82, 0x43800000\n"
        "buffer_load_dwordx4 v[188:191], v35, s[36:39], 0 offen offset:16\n"
        "v_add_f32 v78, v78, v36\n"
        "v_add_f32 v79, v79, v36\n"
        "v_mfma_f32_16x16x32_bf16 v[104:107], a[164:167], a[116:119], 0\n"
        "s_mov_b32 s84, 0x80000000\n"
        "v_add_f32 v80, v80, v36\n"
        "v_add_f32 v81, v81, v36\n"
        "v_add_f32 v82, v82, v36\n"
        "v_mfma_f32_16x16x32_bf16 v[104:107], a[160:163], a[112:115], v[104:107]\n"
        "s_sub_u32 s85, s48, 1\n"
        "v_add_f32 v83, v83, v36\n"
        "v_add_f32 v84, v84, v36\n"
        "v_add_f32 v85, v85, v36\n"
        "v_mfma_f32_16x16x32_bf16 v[104:107], a[156:159], a[108:111], v[104:107]\n"
        "s_mov_b32 s86, 0x80000000\n"
        "v_add_f32 v86, v86, v36\n"
        "v_add_f32 v87, v87, v36\n"
        "v_cvt_pk_bf16_f32 v108, v64, v65\n"
        "v_mfma_f32_16x16x32_bf16 v[112:115], a[168:171], a[108:111], 0\n"
        "s_mov_b32 s89, 0x80000000\n"
        "v_cvt_pk_bf16_f32 v109, v66, v67\n"
        "v_cvt_pk_bf16_f32 v110, v68, v69\n"
        "v_cvt_pk_bf16_f32 v111, v70, v71\n"
        "v_mfma_f32_16x16x32_bf16 v[112:115], a[172:175], a[112:115], v[112:115]\n"
        "s_mov_b32 s29, 1\n"
        "ds_write_b128 v38, v[108:111]\n"
        "v_cvt_pk_bf16_f32 v64, v72, v73\n"
        "v_mfma_f32_16x16x32_bf16 v[112:115], a[176:179], a[116:119], v[112:115]\n"
        "s_mov_b32 s31, 0x400000\n"
        "v_cvt_pk_bf16_f32 v65, v74, v75\n"
        "v_cvt_pk_bf16_f32 v66, v76, v77\n"
        "v_cvt_pk_bf16_f32 v67, v78, v79\n"
        "v_mfma_f32_16x16x32_bf16 v[68:71], a[188:191], a[116:119], 0\n"
        "s_mov_b32 s87, s49\n"
        "v_cvt_pk_bf16_f32 v72, v80, v81\n"
        "v_cvt_pk_bf16_f32 v73, v82, v83\n"
        "v_cvt_pk_bf16_f32 v74, v84, v85\n"
        "v_mfma_f32_16x16x32_bf16 v[68:71], a[184:187], a[112:115], v[68:71]\n"
        "s_mov_b32 s88, s50\n"
        "v_cvt_pk_bf16_f32 v75, v86, v87\n"
        "v_add_f32 v124, v124, v59\n"
        "v_add_f32 v124, v124, v60\n"
        "v_mfma_f32_16x16x32_bf16 v[68:71], a[180:183], a[108:111], v[68:71]\n"
        "s_mov_b32 s54, 0x05040302\n"
        "buffer_load_dwordx4 v[194:197], v35, s[36:39], 0 offen offset:32\n"
        "v_add_f32 v124, v124, v61\n"
        "v_add_f32 v124, v124, v62\n"
        "v_mfma_f32_16x16x32_bf16 v[32:35], a[192:195], a[108:111], 0\n"
        "s_mov_b32 s55, 0x07060302\n"
        "v_add_f32 v124, v124, v63\n"
        "v_add_f32 v124, v124, v88\n"
        "v_add_f32 v124, v124, v89\n"
        "v_mfma_f32_16x16x32_bf16 v[32:35], a[196:199], a[112:115], v[32:35]\n"
        "s_mov_b64 s[44:45], s[40:41]\n"
        "v_add_f32 v124, v124, v90\n"
        "v_add_f32 v124, v124, v91\n"
        "v_add_f32 v124, v124, v92\n"
        "v_mfma_f32_16x16x32_bf16 v[32:35], a[200:203], a[116:119], v[32:35]\n"
        "s_mov_b32 s46, 0\n"
        "v_add_f32 v124, v124, v93\n"
        "v_add_f32 v124, v124, v94\n"
        "v_add_f32 v124, v124, v95\n"
        "v_mfma_f32_16x16x32_bf16 v[76:79], a[212:215], a[116:119], 0\n"
        "s_mov_b32 s47, s43\n"
        "v_add_f32 v124, v124, v96\n"
        "v_add_f32 v124, v124, v97\n"
        "v_add_f32 v124, v124, v98\n"
        "v_mfma_f32_16x16x32_bf16 v[76:79], a[208:211], a[112:115], v[76:79]\n"
        "v_add_f32 v124, v124, v99\n"
        "v_mov_b32 v125, v124\n"
        "v_add_f32 v36, v100, v101\n"
        "v_mfma_f32_16x16x32_bf16 v[76:79], a[204:207], a[108:111], v[76:79]\n"
        "v_permlane32_swap_b32 v124, v125\n"
        "v_add_f32 v124, v124, v125\n"
        "v_mfma_f32_16x16x32_bf16 v[80:83], a[144:147], a[120:123], 0\n"
        "v_mov_b32 v125, v124\n"
        "v_add_f32 v36, v36, v102\n"
        "v_add_f32 v36, v36, v103\n"
        "v_mfma_f32_16x16x32_bf16 v[80:83], a[148:151], a[124:127], v[80:83]\n"
        "s_add_u32 m0, s52, 0\n"
        "v_permlane16_swap_b32 v124, v125\n"
        "v_add_f32 v124, v124, v125\n"
        "v_mfma_f32_16x16x32_bf16 v[80:83], a[152:155], a[128:131], v[80:83]\n"
        "buffer_load_dwordx4 v0, s[36:39], s49 offen lds\n"
        "v_mul_f32 v124, 0xbc2aaaab, v124\n"
        "v_add_f32 v48, v48, v124\n"
        "v_mfma_f32_16x16x32_bf16 v[84:87], a[164:167], a[128:131], 0\n"
        "v_add_f32 v49, v49, v124\n"
        "v_add_f32 v50, v50, v124\n"
        "v_add_f32 v51, v51, v124\n"
        "v_mfma_f32_16x16x32_bf16 v[84:87], a[160:163], a[124:127], v[84:87]\n"
        "v_add_f32 v56, v56, v124\n"
        "v_add_f32 v57, v57, v124\n"
        "v_add_f32 v58, v58, v124\n"
        "v_mfma_f32_16x16x32_bf16 v[84:87], a[156:159], a[120:123], v[84:87]\n"
        "v_add_f32 v59, v59, v124\n"
        "v_add_f32 v60, v60, v124\n"
        "v_add_f32 v61, v61, v124\n"
        "v_mfma_f32_16x16x32_bf16 v[116:119], a[168:171], a[120:123], 0\n"
        "v_add_f32 v62, v62, v124\n"
        "v_add_f32 v63, v63, v124\n"
        "v_add_f32 v88, v88, v124\n"
        "v_mfma_f32_16x16x32_bf16 v[116:119], a[172:175], a[124:127], v[116:119]\n"
        "v_add_f32 v89, v89, v124\n"
        "v_add_f32 v90, v90, v124\n"
        "v_add_f32 v91, v91, v124\n"
        "v_mfma_f32_16x16x32_bf16 v[116:119], a[176:179], a[128:131], v[116:119]\n"
        "buffer_load_dwordx4 v1, s[36:39], s49 offen offset:1024 lds\n"
        "v_add_f32 v92, v92, v124\n"
        "v_add_f32 v93, v93, v124\n"
        "v_mfma_f32_16x16x32_bf16 v[120:123], a[188:191], a[128:131], 0\n"
        "v_add_f32 v94, v94, v124\n"
        "v_add_f32 v95, v95, v124\n"
        "v_add_f32 v96, v96, v124\n"
        "v_mfma_f32_16x16x32_bf16 v[120:123], a[184:187], a[124:127], v[120:123]\n"
        "v_add_f32 v97, v97, v124\n"
        "v_add_f32 v98, v98, v124\n"
        "v_add_f32 v99, v99, v124\n"
        "v_mfma_f32_16x16x32_bf16 v[120:123], a[180:183], a[120:123], v[120:123]\n"
        "v_cvt_pk_bf16_f32 v124, v48, v49\n"
        "v_cvt_pk_bf16_f32 v125, v50, v51\n"
        "v_cvt_pk_bf16_f32 v126, v56, v57\n"
        "v_mfma_f32_16x16x32_bf16 v[48:51], a[192:195], a[120:123], 0\n"
        "v_cvt_pk_bf16_f32 v127, v58, v59\n"
        "v_cvt_pk_bf16_f32 v56, v60, v61\n"
        "v_cvt_pk_bf16_f32 v57, v62, v63\n"
        "v_mfma_f32_16x16x32_bf16 v[48:51], a[196:199], a[124:127], v[48:51]\n"
        "v_cvt_pk_bf16_f32 v58, v88, v89\n"
        "v_cvt_pk_bf16_f32 v59, v90, v91\n"
        "v_cvt_pk_bf16_f32 v60, v92, v93\n"
        "v_mfma_f32_16x16x32_bf16 v[48:51], a[200:203], a[128:131], v[48:51]\n"
        "buffer_load_dwordx4 v2, s[36:39], s49 offen offset:2048 lds\n"
        "v_cvt_pk_bf16_f32 v61, v94, v95\n"
        "v_cvt_pk_bf16_f32 v62, v96, v97\n"
        "v_mfma_f32_16x16x32_bf16 v[88:91], a[212:215], a[128:131], 0\n"
        "v_cvt_pk_bf16_f32 v63, v98, v99\n"
        "v_add_f32 v36, v36, v104\n"
        "v_add_f32 v36, v36, v105\n"
        "v_mfma_f32_16x16x32_bf16 v[88:91], a[208:211], a[124:127], v[88:91]\n"
        "v_add_f32 v36, v36, v106\n"
        "v_add_f32 v36, v36, v107\n"
        "v_add_f32 v36, v36, v112\n"
        "v_mfma_f32_16x16x32_bf16 v[88:91], a[204:207], a[120:123], v[88:91]\n"
        "v_add_f32 v36, v36, v113\n"
        "v_add_f32 v36, v36, v114\n"
        "v_add_f32 v36, v36, v115\n"
        "v_mfma_f32_16x16x32_bf16 v[92:95], a[144:147], a[132:135], 0\n"
        "v_add_f32 v36, v36, v68\n"
        "v_add_f32 v36, v36, v69\n"
        "v_add_f32 v36, v36, v70\n"
        "v_mfma_f32_16x16x32_bf16 v[92:95], a[148:151], a[136:139], v[92:95]\n"
        "v_add_f32 v36, v36, v71\n"
        "v_add_f32 v36, v36, v32\n"
        "v_add_f32 v36, v36, v33\n"
        "v_mfma_f32_16x16x32_bf16 v[92:95], a[152:155], a[140:143], v[92:95]\n"
        "buffer_load_dwordx4 v3, s[36:39], s49 offen offset:3072 lds\n"
        "v_add_f32 v36, v36, v34\n"
        "v_add_f32 v36, v36, v35\n"
        "v_mfma_f32_16x16x32_bf16 v[96:99], a[164:167], a[140:143], 0\n"
        "v_add_f32 v36, v36, v76\n"
        "v_add_f32 v36, v36, v77\n"
        "v_add_f32 v36, v36, v78\n"
        "v_mfma_f32_16x16x32_bf16 v[96:99], a[160:163], a[136:139], v[96:99]\n"
        "v_add_f32 v36, v36, v79\n"
        "v_mov_b32 v37, v36\n"
        "v_add_f32 v148, v80, v81\n"
        "v_mfma_f32_16x16x32_bf16 v[96:99], a[156:159], a[132:135], v[96:99]\n"
        "v_permlane32_swap_b32 v36, v37\n"
        "v_add_f32 v36, v36, v37\n"
        "v_mfma_f32_16x16x32_bf16 v[128:131], a[168:171], a[132:135], 0\n"
        "v_mov_b32 v37, v36\n"
        "v_add_f32 v148, v148, v82\n"
        "v_add_f32 v148, v148, v83\n"
        "v_mfma_f32_16x16x32_bf16 v[128:131], a[172:175], a[136:139], v[128:131]\n"
        "s_add_u32 m0, s52, 4096\n"
        "v_permlane16_swap_b32 v36, v37\n"
        "v_add_f32 v36, v36, v37\n"
        "v_mfma_f32_16x16x32_bf16 v[128:131], a[176:179], a[140:143], v[128:131]\n"
        "buffer_load_dwordx4 v4, s[36:39], s49 offen lds\n"
        "v_mul_f32 v36, 0xbc2aaaab, v36\n"
        "v_add_f32 v148, v148, v84\n"
        "v_mfma_f32_16x16x32_bf16 v[132:135], a[188:191], a[140:143], 0\n"
        "v_add_f32 v148, v148, v85\n"
        "v_add_f32 v148, v148, v86\n"
        "v_add_f32 v148, v148, v87\n"
        "v_mfma_f32_16x16x32_bf16 v[132:135], a[184:187], a[136:139], v[132:135]\n"
        "v_add_f32 v148, v148, v116\n"
        "v_add_f32 v148, v148, v117\n"
        "v_add_f32 v148, v148, v118\n"
        "v_mfma_f32_16x16x32_bf16 v[132:135], a[180:183], a[132:135], v[132:135]\n"
        "v_add_f32 v148, v148, v119\n"
        "v_add_f32 v148, v148, v120\n"
        "v_add_f32 v148, v148, v121\n"
        "v_mfma_f32_16x16x32_bf16 v[136:139], a[192:195], a[132:135], 0\n"
        "v_add_f32 v148, v148, v122\n"
        "v_add_f32 v148, v148, v123\n"
        "v_add_f32 v148, v148, v48\n"
        "v_mfma_f32_16x16x32_bf16 v[136:139], a[196:199], a[136:139], v[136:139]\n"
        "v_add_f32 v148, v148, v49\n"
        "v_add_f32 v148, v148, v50\n"
        "v_add_f32 v148, v148, v51\n"
        "v_mfma_f32_16x16x32_bf16 v[136:139], a[200:203], a[140:143], v[136:139]\n"
        "buffer_load_dwordx4 v5, s[36:39], s49 offen offset:1024 lds\n"
        "v_add_f32 v148, v148, v88\n"
        "v_add_f32 v148, v148, v89\n"
        "v_mfma_f32_16x16x32_bf16 v[140:143], a[212:215], a[140:143], 0\n"
        "v_add_f32 v148, v148, v90\n"
        "v_add_f32 v148, v148, v91\n"
        "v_mov_b32 v149, v148\n"
        "v_mfma_f32_16x16x32_bf16 v[140:143], a[208:211], a[136:139], v[140:143]\n"
        "ds_write_b128 v38, v[64:67] offset:1024\n"
        "v_add_f32 v150, v92, v93\n"
        "v_mfma_f32_16x16x32_bf16 v[140:143], a[204:207], a[132:135], v[140:143]\n"
        "ds_write_b128 v38, v[72:75] offset:2048\n"
        "v_add_f32 v150, v150, v94\n"
        "v_pk_add_f32 v[100:101], v[100:101], v[36:37] op_sel_hi:[1,0]\n"
        "v_cvt_pk_bf16_f32 v144, v100, v101\n"
        "v_pk_add_f32 v[102:103], v[102:103], v[36:37] op_sel_hi:[1,0]\n"
        "v_cvt_pk_bf16_f32 v145, v102, v103\n"
        "v_pk_add_f32 v[104:105], v[104:105], v[36:37] op_sel_hi:[1,0]\n"
        "v_cvt_pk_bf16_f32 v146, v104, v105\n"
        "v_pk_add_f32 v[106:107], v[106:107], v[36:37] op_sel_hi:[1,0]\n"
        "v_cvt_pk_bf16_f32 v147, v106, v107\n"
        "v_pk_add_f32 v[112:113], v[112:113], v[36:37] op_sel_hi:[1,0]\n"
        "v_cvt_pk_bf16_f32 v100, v112, v113\n"
        "v_pk_add_f32 v[114:115], v[114:115], v[36:37] op_sel_hi:[1,0]\n"
        "v_cvt_pk_bf16_f32 v101, v114, v115\n"
        "v_pk_add_f32 v[68:69], v[68:69], v[36:37] op_sel_hi:[1,0]\n"
        "v_cvt_pk_bf16_f32 v102, v68, v69\n"
        "v_pk_add_f32 v[70:71], v[70:71], v[36:37] op_sel_hi:[1,0]\n"
        "v_cvt_pk_bf16_f32 v103, v70, v71\n"
        "v_pk_add_f32 v[32:33], v[32:33], v[36:37] op_sel_hi:[1,0]\n"
        "v_cvt_pk_bf16_f32 v68, v32, v33\n"
        "v_pk_add_f32 v[34:35], v[34:35], v[36:37] op_sel_hi:[1,0]\n"
        "v_cvt_pk_bf16_f32 v69, v34, v35\n"
        "v_pk_add_f32 v[76:77], v[76:77], v[36:37] op_sel_hi:[1,0]\n"
        "v_cvt_pk_bf16_f32 v70, v76, v77\n"
        "v_pk_add_f32 v[78:79], v[78:79], v[36:37] op_sel_hi:[1,0]\n"
        "v_cvt_pk_bf16_f32 v71, v78, v79\n"
        "v_permlane32_swap_b32 v148, v149\n"
        "v_add_f32 v148, v148, v149\n"
        "v_mov_b32 v149, v148\n"
        "s_nop 1\n"
        "v_permlane16_swap_b32 v148, v149\n"
        "v_add_f32 v148, v148, v149\n"
        "v_mul_f32 v148, 0xbc2aaaab, v148\n"
        "v_pk_add_f32 v[80:81], v[80:81], v[148:149] op_sel_hi:[1,0]\n"
        "v_cvt_pk_bf16_f32 v32, v80, v81\n"
        "v_pk_add_f32 v[82:83], v[82:83], v[148:149] op_sel_hi:[1,0]\n"
        "v_cvt_pk_bf16_f32 v33, v82, v83\n"
        "v_pk_add_f32 v[84:85], v[84:85], v[148:149] op_sel_hi:[1,0]\n"
        "v_cvt_pk_bf16_f32 v34, v84, v85\n"
        "v_pk_add_f32 v[86:87], v[86:87], v[148:149] op_sel_hi:[1,0]\n"
        "v_cvt_pk_bf16_f32 v35, v86, v87\n"
        "v_pk_add_f32 v[116:117], v[116:117], v[148:149] op_sel_hi:[1,0]\n"
        "v_cvt_pk_bf16_f32 v76, v116, v117\n"
        "v_pk_add_f32 v[118:119], v[118:119], v[148:149] op_sel_hi:[1,0]\n"
        "v_cvt_pk_bf16_f32 v77, v118, v119\n"
        "v_pk_add_f32 v[120:121], v[120:121], v[148:149] op_sel_hi:[1,0]\n"
        "v_cvt_pk_bf16_f32 v78, v120, v121\n"
        "v_pk_add_f32 v[122:123], v[122:123], v[148:149] op_sel_hi:[1,0]\n"
        "v_cvt_pk_bf16_f32 v79, v122, v123\n"
        "v_pk_add_f32 v[48:49], v[48:49], v[148:149] op_sel_hi:[1,0]\n"
        "v_cvt_pk_bf16_f32 v80, v48, v49\n"
        "v_pk_add_f32 v[50:51], v[50:51], v[148:149] op_sel_hi:[1,0]\n"
        "v_cvt_pk_bf16_f32 v81, v50, v51\n"
        "v_pk_add_f32 v[88:89], v[88:89], v[148:149] op_sel_hi:[1,0]\n"
        "v_cvt_pk_bf16_f32 v82, v88, v89\n"
        "v_pk_add_f32 v[90:91], v[90:91], v[148:149] op_sel_hi:[1,0]\n"
        "v_cvt_pk_bf16_f32 v83, v90, v91\n"
        "v_add_f32 v150, v150, v95\n"
        "v_add_f32 v150, v150, v96\n"
        "v_add_f32 v150, v150, v97\n"
        "v_add_f32 v150, v150, v98\n"
        "v_add_f32 v150, v150, v99\n"
        "v_add_f32 v150, v150, v128\n"
        "v_add_f32 v150, v150, v129\n"
        "v_add_f32 v150, v150, v130\n"
        "v_add_f32 v150, v150, v131\n"
        "v_add_f32 v150, v150, v132\n"
        "v_add_f32 v150, v150, v133\n"
        "v_add_f32 v150, v150, v134\n"
        "v_add_f32 v150, v150, v135\n"
        "v_add_f32 v150, v150, v136\n"
        "v_add_f32 v150, v150, v137\n"
        "v_add_f32 v150, v150, v138\n"
        "v_add_f32 v150, v150, v139\n"
        "v_add_f32 v150, v150, v140\n"
        "v_add_f32 v150, v150, v141\n"
        "v_add_f32 v150, v150, v142\n"
        "v_add_f32 v150, v150, v143\n"
        "v_mov_b32 v151, v150\n"
        "s_nop 1\n"
        "v_permlane32_swap_b32 v150, v151\n"
        "v_add_f32 v150, v150, v151\n"
        "v_mov_b32 v151, v150\n"
        "s_nop 1\n"
        "v_permlane16_swap_b32 v150, v151\n"
        "v_add_f32 v150, v150, v151\n"
        "v_mul_f32 v150, 0xbc2aaaab, v150\n"
        "v_pk_add_f32 v[92:93], v[92:93], v[150:151] op_sel_hi:[1,0]\n"
        "v_cvt_pk_bf16_f32 v48, v92, v93\n"
        "v_pk_add_f32 v[94:95], v[94:95], v[150:151] op_sel_hi:[1,0]\n"
        "v_cvt_pk_bf16_f32 v49, v94, v95\n"
        "v_pk_add_f32 v[96:97], v[96:97], v[150:151] op_sel_hi:[1,0]\n"
        "v_cvt_pk_bf16_f32 v50, v96, v97\n"
        "v_pk_add_f32 v[98:99], v[98:99], v[150:151] op_sel_hi:[1,0]\n"
        "v_cvt_pk_bf16_f32 v51, v98, v99\n"
        "v_pk_add_f32 v[128:129], v[128:129], v[150:151] op_sel_hi:[1,0]\n"
        "v_cvt_pk_bf16_f32 v84, v128, v129\n"
        "v_pk_add_f32 v[130:131], v[130:131], v[150:151] op_sel_hi:[1,0]\n"
        "v_cvt_pk_bf16_f32 v85, v130, v131\n"
        "v_pk_add_f32 v[132:133], v[132:133], v[150:151] op_sel_hi:[1,0]\n"
        "v_cvt_pk_bf16_f32 v86, v132, v133\n"
        "v_pk_add_f32 v[134:135], v[134:135], v[150:151] op_sel_hi:[1,0]\n"
        "v_cvt_pk_bf16_f32 v87, v134, v135\n"
        "v_pk_add_f32 v[136:137], v[136:137], v[150:151] op_sel_hi:[1,0]\n"
        "v_cvt_pk_bf16_f32 v88, v136, v137\n"
        "v_pk_add_f32 v[138:139], v[138:139], v[150:151] op_sel_hi:[1,0]\n"
        "v_cvt_pk_bf16_f32 v89, v138, v139\n"
        "v_pk_add_f32 v[140:141], v[140:141], v[150:151] op_sel_hi:[1,0]\n"
        "v_cvt_pk_bf16_f32 v90, v140, v141\n"
        "v_pk_add_f32 v[142:143], v[142:143], v[150:151] op_sel_hi:[1,0]\n"
        "v_cvt_pk_bf16_f32 v91, v142, v143\n"
        "s_nop 1\n"
        "v_mfma_f32_16x16x32_bf16 v[92:95], v[28:31], v[28:31], 0\n"
        "ds_write_b128 v39, v[124:127]\n"
        "v_mfma_f32_16x16x32_bf16 v[92:95], v[44:47], v[44:47], v[92:95]\n"
        "ds_write_b128 v39, v[56:59] offset:1024\n"
        "v_mfma_f32_16x16x32_bf16 v[92:95], v[52:55], v[52:55], v[92:95]\n"
        "buffer_load_dwordx4 v6, s[36:39], s49 offen offset:2048 lds\n"
        "v_mfma_f32_16x16x32_bf16 v[92:95], v[108:111], v[108:111], v[92:95]\n"
        "ds_write_b128 v39, v[60:63] offset:2048\n"
        "v_mfma_f32_16x16x32_bf16 v[92:95], v[64:67], v[64:67], v[92:95]\n"
        "ds_write_b128 v40, v[144:147]\n"
        "v_mfma_f32_16x16x32_bf16 v[92:95], v[72:75], v[72:75], v[92:95]\n"
        "ds_write_b128 v40, v[100:103] offset:1024\n"
        "v_mfma_f32_16x16x32_bf16 v[92:95], v[124:127], v[124:127], v[92:95]\n"
        "ds_write_b128 v40, v[68:71] offset:2048\n"
        "v_mfma_f32_16x16x32_bf16 v[92:95], v[56:59], v[56:59], v[92:95]\n"
        "ds_write_b128 v41, v[32:35]\n"
        "v_mfma_f32_16x16x32_bf16 v[92:95], v[60:63], v[60:63], v[92:95]\n"
        "buffer_load_dwordx4 v7, s[36:39], s49 offen offset:3072 lds\n"
        "v_mfma_f32_16x16x32_bf16 v[92:95], v[144:147], v[144:147], v[92:95]\n"
        "ds_write_b128 v41, v[76:79] offset:1024\n"
        "v_mfma_f32_16x16x32_bf16 v[92:95], v[100:103], v[100:103], v[92:95]\n"
        "ds_write_b128 v41, v[80:83] offset:2048\n"
        "v_mfma_f32_16x16x32_bf16 v[92:95], v[68:71], v[68:71], v[92:95]\n"
        "ds_write_b128 v42, v[48:51]\n"
        "v_mfma_f32_16x16x32_bf16 v[92:95], v[32:35], v[32:35], v[92:95]\n"
        "ds_write_b128 v42, v[84:87] offset:1024\n"
        "v_mfma_f32_16x16x32_bf16 v[92:95], v[76:79], v[76:79], v[92:95]\n"
        "s_add_u32 m0, s52, 8192\n"
        "ds_write_b128 v42, v[88:91] offset:2048\n"
        "v_mfma_f32_16x16x32_bf16 v[92:95], v[80:83], v[80:83], v[92:95]\n"
        "buffer_load_dwordx4 v8, s[36:39], s49 offen lds\n"
        "v_mfma_f32_16x16x32_bf16 v[92:95], v[48:51], v[48:51], v[92:95]\n"
        "v_mfma_f32_16x16x32_bf16 v[92:95], v[84:87], v[84:87], v[92:95]\n"
        "v_mfma_f32_16x16x32_bf16 v[92:95], v[88:91], v[88:91], v[92:95]\n"
        "s_nop 2\n"
        "s_waitcnt vmcnt(9)\n"
        "s_nop 3\n"
        "v_max3_f32 v28, v92, v93, v94\n"
        "v_max_f32 v28, v28, v95\n"
        "v_cmp_nge_f32_e64 s[74:75], s24, v28\n"
        "v_mov_b32 v28, 160384\n"
        "s_nop 0\n"
        "v_cndmask_b32_e64 v28, v43, v28, s[74:75]\n"
        "v_mov_b32 v43, 0\n"
        "ds_write_b32 v28, v43\n"
        "s_waitcnt lgkmcnt(0)\n"
        "s_barrier\n"
        "FE_%=:\n"
        "ds_read_b128 v[86:89], v26\n"
        "ds_read_b128 v[82:85], v26 offset:64\n"
        "ds_read_b128 v[78:81], v26 offset:128\n"
        "v_xor_b32 v60, 64, v200\n"
        "ds_read_b128 a[0:3], v200\n"
        "ds_read_b128 a[4:7], v200 offset:1024\n"
        "ds_read_b128 a[8:11], v200 offset:2048\n"
        "ds_read_b128 a[12:15], v200 offset:3072\n"
        "ds_read_b128 a[16:19], v200 offset:4096\n"
        "ds_read_b128 a[20:23], v200 offset:5120\n"
        "ds_read_b128 a[24:27], v200 offset:6144\n"
        "ds_read_b128 a[28:31], v200 offset:7168\n"
        "ds_read_b128 a[32:35], v200 offset:8192\n"
        "v_mfma_f32_16x16x32_bf16 v[64:67], v[162:165], v[162:165], 0\n"
        "ds_read_b128 a[36:39], v200 offset:9216\n"
        "v_mov_b64 v[90:91], s[76:77]\n"
        "v_mfma_f32_16x16x32_bf16 v[64:67], v[168:171], v[168:171], v[64:67]\n"
        "ds_read_b128 a[40:43], v200 offset:10240\n"
        "v_mov_b64 v[92:93], s[76:77]\n"
        "v_mfma_f32_16x16x32_bf16 v[64:67], v[174:177], v[174:177], v[64:67]\n"
        "ds_read_b128 a[44:47], v200 offset:11264\n"
        "v_add_u32 v134, 73792, v200\n"
        "v_mfma_f32_16x16x32_bf16 v[70:73], v[182:185], v[182:185], 0\n"
        "ds_read_b128 a[48:51], v200 offset:12288\n"
        "v_lshrrev_b32 v76, 2, v200\n"
        "v_mfma_f32_16x16x32_bf16 v[70:73], v[188:191], v[188:191], v[70:73]\n"
        "ds_read_b128 a[52:55], v200 offset:13312\n"
        "v_mov_b32 v77, 160384\n"
        "v_mfma_f32_16x16x32_bf16 v[70:73], v[194:197], v[194:197], v[70:73]\n"
        "s_waitcnt lgkmcnt(14)\n"
        "ds_read_b128 a[56:59], v200 offset:14336\n"
        "v_cndmask_b32_e64 v61, v64, v65, s[56:57]\n"
        "v_mfma_f32_16x16x32_bf16 v[102:105], v[78:81], v[194:197], 0\n"
        "ds_read_b128 a[60:63], v200 offset:15360\n"
        "v_cndmask_b32_e64 v61, v61, v66, s[58:59]\n"
        "v_mfma_f32_16x16x32_bf16 v[102:105], v[82:85], v[188:191], v[102:105]\n"
        "ds_read_b128 a[64:67], v200 offset:16384\n"
        "v_cndmask_b32_e64 v61, v61, v67, s[60:61]\n"
        "v_mfma_f32_16x16x32_bf16 v[102:105], v[86:89], v[182:185], v[102:105]\n"
        "s_waitcnt lgkmcnt(14)\n"
        "ds_read_b128 a[68:71], v200 offset:17408\n"
        "v_cndmask_b32_e64 v106, v70, v71, s[56:57]\n"
        "v_mfma_f32_16x16x32_bf16 v[64:67], v[86:89], v[162:165], 0\n"
        "s_waitcnt lgkmcnt(12)\n"
        "ds_read_b128 a[72:75], v200 offset:18432\n"
        "v_cndmask_b32_e64 v106, v106, v72, s[58:59]\n"
        "v_mfma_f32_16x16x32_bf16 v[64:67], v[82:85], v[168:171], v[64:67]\n"
        "s_waitcnt lgkmcnt(10)\n"
        "ds_read_b128 a[76:79], v200 offset:19456\n"
        "v_cndmask_b32_e64 v106, v106, v73, s[60:61]\n"
        "v_mfma_f32_16x16x32_bf16 v[64:67], v[78:81], v[174:177], v[64:67]\n"
        "ds_read_b128 a[80:83], v200 offset:20480\n"
        "v_cndmask_b32_dpp v61, v106, v61, vcc row_ror:4 row_mask:0xf bank_mask:0xf\n"
        "v_mfma_f32_16x16x32_bf16 v[70:73], a[8:11], v[174:177], 0\n"
        "ds_read_b128 a[84:87], v200 offset:21504\n"
        "v_mul_f32 v61, s80, v61\n"
        "v_mfma_f32_16x16x32_bf16 v[70:73], a[4:7], v[168:171], v[70:73]\n"
        "s_waitcnt lgkmcnt(10)\n"
        "ds_read_b128 a[88:91], v200 offset:22528\n"
        "v_cndmask_b32_dpp v135, v102, v64, vcc row_ror:4 row_mask:0xf bank_mask:0xf\n"
        "v_mfma_f32_16x16x32_bf16 v[70:73], a[0:3], v[162:165], v[70:73]\n"
        "ds_read_b128 a[92:95], v200 offset:23552\n"
        "v_mul_f32 v64, s80, v135\n"
        "v_mfma_f32_16x16x32_bf16 v[104:107], a[12:15], v[162:165], 0\n"
        "ds_read_b128 a[96:99], v60 offset:24576\n"
        "v_fma_f32 v64, -v64, v64, v61\n"
        "v_mfma_f32_16x16x32_bf16 v[104:107], a[16:19], v[168:171], v[104:107]\n"
        "s_waitcnt lgkmcnt(10)\n"
        "ds_read_b128 a[100:103], v60 offset:25600\n"
        "v_cvt_pk_bf16_f32 v108, v70, v71\n"
        "v_mfma_f32_16x16x32_bf16 v[104:107], a[20:23], v[174:177], v[104:107]\n"
        "ds_read_b128 a[104:107], v60 offset:26624\n"
        "v_cvt_pk_bf16_f32 v109, v72, v73\n"
        "v_mfma_f32_16x16x32_bf16 v[70:73], a[32:35], v[174:177], 0\n"
        "ds_read_b128 a[108:111], v60 offset:27648\n"
        "v_max_f32 v64, 0, v64\n"
        "v_mfma_f32_16x16x32_bf16 v[70:73], a[28:31], v[168:171], v[70:73]\n"
        "s_waitcnt lgkmcnt(10)\n"
        "ds_read_b128 a[112:115], v60 offset:28672\n"
        "v_cvt_pk_bf16_f32 v110, v104, v105\n"
        "v_mfma_f32_16x16x32_bf16 v[70:73], a[24:27], v[162:165], v[70:73]\n"
        "ds_read_b128 a[116:119], v60 offset:29696\n"
        "v_cvt_pk_bf16_f32 v111, v106, v107\n"
        "v_mfma_f32_16x16x32_bf16 v[104:107], a[36:39], v[162:165], 0\n"
        "ds_read_b128 a[120:123], v200 offset:30720\n"
        "v_add_f32 v64, s81, v64\n"
        "v_mfma_f32_16x16x32_bf16 v[104:107], a[40:43], v[168:171], v[104:107]\n"
        "s_nop 0\n"
        "ds_read_b128 a[124:127], v200 offset:31744\n"
        "v_cvt_pk_bf16_f32 v112, v70, v71\n"
        "v_mfma_f32_16x16x32_bf16 v[104:107], a[44:47], v[174:177], v[104:107]\n"
        "ds_read_b128 a[128:131], v200 offset:32768\n"
        "v_cvt_pk_bf16_f32 v113, v72, v73\n"
        "v_mfma_f32_16x16x32_bf16 v[70:73], a[56:59], v[174:177], 0\n"
        "ds_read_b128 a[132:135], v200 offset:33792\n"
        "v_and_b32 v76, 128, v76\n"
        "v_mfma_f32_16x16x32_bf16 v[70:73], a[52:55], v[168:171], v[70:73]\n"
        "s_nop 0\n"
        "ds_read_b128 a[136:139], v200 offset:34816\n"
        "v_cvt_pk_bf16_f32 v114, v104, v105\n"
        "v_mfma_f32_16x16x32_bf16 v[70:73], a[48:51], v[162:165], v[70:73]\n"
        "s_nop 0\n"
        "ds_read_b128 a[140:143], v200 offset:35840\n"
        "v_cvt_pk_bf16_f32 v115, v106, v107\n"
        "v_mfma_f32_16x16x32_bf16 v[104:107], a[60:63], v[162:165], 0\n"
        "s_nop 0\n"
        "v_rsq_f32 v148, v64\n"
        "v_mfma_f32_16x16x32_bf16 v[104:107], a[64:67], v[168:171], v[104:107]\n"
        "s_nop 0\n"
        "v_cvt_pk_bf16_f32 v116, v70, v71\n"
        "v_cvt_pk_bf16_f32 v117, v72, v73\n"
        "v_mfma_f32_16x16x32_bf16 v[104:107], a[68:71], v[174:177], v[104:107]\n"
        "s_nop 0\n"
        "v_xor_b32 v76, v76, v200\n"
        "v_add_u32 v76, 139648, v76\n"
        "v_mfma_f32_16x16x32_bf16 v[70:73], a[8:11], v[194:197], 0\n"
        "s_nop 0\n"
        "v_cndmask_b32_dpp v149, v103, v65, vcc row_ror:4 row_mask:0xf bank_mask:0xf\n"
        "v_mfma_f32_16x16x32_bf16 v[70:73], a[4:7], v[188:191], v[70:73]\n"
        "s_nop 0\n"
        "v_cvt_pk_bf16_f32 v118, v104, v105\n"
        "v_cvt_pk_bf16_f32 v119, v106, v107\n"
        "v_mfma_f32_16x16x32_bf16 v[70:73], a[0:3], v[182:185], v[70:73]\n"
        "s_nop 1\n"
        "v_mfma_f32_16x16x32_bf16 v[120:123], v[162:165], v[108:111], 0\n"
        "s_nop 2\n"
        "v_mfma_f32_16x16x32_bf16 v[120:123], v[168:171], v[112:115], v[120:123]\n"
        "s_nop 0\n"
        "v_cvt_pk_bf16_f32 v64, v70, v71\n"
        "v_cvt_pk_bf16_f32 v65, v72, v73\n"
        "v_mfma_f32_16x16x32_bf16 v[120:123], v[174:177], v[116:119], v[120:123]\n"
        "v_mfma_f32_16x16x32_bf16 v[70:73], a[20:23], v[194:197], 0\n"
        "v_mfma_f32_16x16x32_bf16 v[70:73], a[16:19], v[188:191], v[70:73]\n"
        "s_waitcnt lgkmcnt(0)\n"
        "v_mfma_f32_16x16x32_bf16 v[70:73], a[12:15], v[182:185], v[70:73]\n"
        "s_barrier\n"
        "ds_read_b128 a[144:147], v76\n"
        "v_mfma_f32_16x16x32_bf16 v[102:105], a[24:27], v[182:185], 0\n"
        "s_nop 0\n"
        "ds_read_b128 a[148:151], v76 offset:1024\n"
        "s_nop 0\n"
        "v_mfma_f32_16x16x32_bf16 v[102:105], a[28:31], v[188:191], v[102:105]\n"
        "s_nop 0\n"
        "v_cvt_pk_bf16_f32 v66, v70, v71\n"
        "v_cvt_pk_bf16_f32 v67, v72, v73\n"
        "v_mfma_f32_16x16x32_bf16 v[102:105], a[32:35], v[194:197], v[102:105]\n"
        "s_nop 0\n"
        "v_mfma_f32_16x16x32_bf16 v[70:73], a[44:47], v[194:197], 0\n"
        "s_nop 2\n"
        "v_mfma_f32_16x16x32_bf16 v[70:73], a[40:43], v[188:191], v[70:73]\n"
        "s_nop 0\n"
        "ds_read_b128 a[152:155], v76 offset:2048\n"
        "v_cvt_pk_bf16_f32 v106, v102, v103\n"
        "v_mfma_f32_16x16x32_bf16 v[70:73], a[36:39], v[182:185], v[70:73]\n"
        "s_nop 0\n"
        "v_cvt_pk_bf16_f32 v107, v104, v105\n"
        "v_mfma_f32_16x16x32_bf16 v[102:105], a[48:51], v[182:185], 0\n"
        "s_nop 2\n"
        "v_mfma_f32_16x16x32_bf16 v[102:105], a[52:55], v[188:191], v[102:105]\n"
        "s_nop 0\n"
        "v_cvt_pk_bf16_f32 v108, v70, v71\n"
        "v_cvt_pk_bf16_f32 v109, v72, v73\n"
        "v_mfma_f32_16x16x32_bf16 v[102:105], a[56:59], v[194:197], v[102:105]\n"
        "s_nop 0\n"
        "ds_read_b128 a[156:159], v76 offset:3072\n"
        "v_mfma_f32_16x16x32_bf16 v[70:73], a[68:71], v[194:197], 0\n"
        "s_nop 2\n"
        "v_mfma_f32_16x16x32_bf16 v[70:73], a[64:67], v[188:191], v[70:73]\n"
        "s_nop 0\n"
        "v_cvt_pk_bf16_f32 v110, v102, v103\n"
        "v_cvt_pk_bf16_f32 v111, v104, v105\n"
        "v_mfma_f32_16x16x32_bf16 v[70:73], a[60:63], v[182:185], v[70:73]\n"
        "s_nop 0\n"
        "ds_read_b128 a[160:163], v76 offset:4096\n"
        "v_mfma_f32_16x16x32_bf16 v[102:105], v[162:165], a[72:75], 0\n"
        "s_nop 2\n"
        "v_mfma_f32_16x16x32_bf16 v[102:105], v[168:171], a[76:79], v[102:105]\n"
        "s_nop 0\n"
        "v_cvt_pk_bf16_f32 v112, v70, v71\n"
        "v_cvt_pk_bf16_f32 v113, v72, v73\n"
        "v_mfma_f32_16x16x32_bf16 v[102:105], v[174:177], a[80:83], v[102:105]\n"
        "s_nop 1\n"
        "v_mfma_f32_16x16x32_bf16 v[70:73], v[174:177], a[92:95], 0\n"
        "s_nop 0\n"
        "ds_read_b128 a[164:167], v76 offset:5120\n"
        "s_nop 0\n"
        "v_mfma_f32_16x16x32_bf16 v[70:73], v[168:171], a[88:91], v[70:73]\n"
        "s_nop 0\n"
        "v_cvt_pk_bf16_f32 v60, v102, v103\n"
        "v_cvt_pk_bf16_f32 v61, v104, v105\n"
        "v_mfma_f32_16x16x32_bf16 v[70:73], v[162:165], a[84:87], v[70:73]\n"
        "s_nop 0\n"
        "v_mfma_f32_16x16x32_bf16 v[126:129], v[182:185], v[64:67], 0\n"
        "s_nop 2\n"
        "v_mfma_f32_16x16x32_bf16 v[126:129], v[188:191], v[106:109], v[126:129]\n"
        "s_nop 0\n"
        "ds_read_b128 a[168:171], v76 offset:6144\n"
        "v_cvt_pk_bf16_f32 v64, v70, v71\n"
        "v_mfma_f32_16x16x32_bf16 v[126:129], v[194:197], v[110:113], v[126:129]\n"
        "s_waitcnt vmcnt(0)\n"
        "v_cvt_pk_bf16_f32 v65, v72, v73\n"
        "v_mfma_f32_16x16x32_bf16 v[70:73], v[174:177], a[104:107], 0\n"
        "s_nop 0\n"
        "ds_read_b128 v[104:107], v9\n"
        "v_mfma_f32_16x16x32_bf16 v[70:73], v[168:171], a[100:103], v[70:73]\n"
        "s_nop 0\n"
        "ds_read_b128 v[110:113], v10\n"
        "v_cndmask_b32_dpp v102, v126, v120, vcc row_ror:4 row_mask:0xf bank_mask:0xf\n"
        "v_mfma_f32_16x16x32_bf16 v[70:73], v[162:165], a[96:99], v[70:73]\n"
        "ds_read_b128 v[116:119], v11\n"
        "v_cndmask_b32_dpp v103, v127, v121, vcc row_ror:4 row_mask:0xf bank_mask:0xf\n"
        "v_mfma_f32_16x16x32_bf16 v[140:143], v[162:165], a[108:111], 0\n"
        "ds_read_b128 v[124:127], v9 offset:3072\n"
        "v_cndmask_b32_dpp v108, v128, v122, vcc row_ror:4 row_mask:0xf bank_mask:0xf\n"
        "v_mfma_f32_16x16x32_bf16 v[140:143], v[168:171], a[112:115], v[140:143]\n"
        "ds_read_b128 v[130:133], v10 offset:3072\n"
        "v_cndmask_b32_dpp v109, v129, v123, vcc row_ror:4 row_mask:0xf bank_mask:0xf\n"
        "v_mfma_f32_16x16x32_bf16 v[140:143], v[174:177], a[116:119], v[140:143]\n"
        "ds_read_b128 v[136:139], v11 offset:3072\n"
        "v_mul_f32_dpp v102, v148, v102 quad_perm:[0,0,0,0] row_mask:0xf bank_mask:0xf\n"
        "v_mfma_f32_16x16x32_bf16 v[120:123], v[174:177], a[128:131], 0\n"
        "ds_read_b128 v[144:147], v9 offset:6144\n"
        "v_mul_f32_dpp v103, v148, v103 quad_perm:[1,1,1,1] row_mask:0xf bank_mask:0xf\n"
        "v_mfma_f32_16x16x32_bf16 v[120:123], v[168:171], a[124:127], v[120:123]\n"
        "ds_read_b128 v[150:153], v10 offset:6144\n"
        "v_mul_f32_dpp v108, v148, v108 quad_perm:[2,2,2,2] row_mask:0xf bank_mask:0xf\n"
        "v_mfma_f32_16x16x32_bf16 v[120:123], v[162:165], a[120:123], v[120:123]\n"
        "ds_read_b128 v[156:159], v11 offset:6144\n"
        "v_mul_f32_dpp v109, v148, v109 quad_perm:[3,3,3,3] row_mask:0xf bank_mask:0xf\n"
        "v_mfma_f32_16x16x32_bf16 v[208:211], v[162:165], a[132:135], 0\n"
        "ds_read_b128 a[172:175], v76 offset:7168\n"
        "v_max3_f32 v66, v102, v103, v108\n"
        "v_mfma_f32_16x16x32_bf16 v[208:211], v[168:171], a[136:139], v[208:211]\n"
        "s_waitcnt lgkmcnt(7)\n"
        "ds_read_b128 a[176:179], v76 offset:8192\n"
        "v_max_f32 v66, v66, v109\n"
        "v_mfma_f32_16x16x32_bf16 v[208:211], v[174:177], a[140:143], v[208:211]\n"
        "ds_read_b128 a[180:183], v76 offset:9216\n"
        "v_mul_f32 v66, v66, v148\n"
        "v_mfma_f32_16x16x32_bf16 v[212:215], v[104:107], v[104:107], 0\n"
        "s_mov_b32 s26, s89\n"
        "s_mov_b32 s30, s29\n"
        "s_add_u32 s88, s88, 32\n"
        "v_mfma_f32_16x16x32_bf16 v[212:215], v[110:113], v[110:113], v[212:215]\n"
        "s_add_u32 s89, s86, s51\n"
        "ds_read_b128 a[184:187], v76 offset:10240\n"
        "v_fma_f32 v102, v102, v148, -v66\n"
        "v_mfma_f32_16x16x32_bf16 v[212:215], v[116:119], v[116:119], v[212:215]\n"
        "s_add_u32 s29, s29, 1\n"
        "v_fma_f32 v103, v103, v148, -v66\n"
        "v_fma_f32 v108, v108, v148, -v66\n"
        "v_mfma_f32_16x16x32_bf16 v[216:219], v[78:81], v[116:119], 0\n"
        "s_cmp_ge_u32 s88, 73\n"
        "v_fma_f32 v109, v109, v148, -v66\n"
        "v_cvt_pk_bf16_f32 v66, v70, v71\n"
        "v_mfma_f32_16x16x32_bf16 v[216:219], v[82:85], v[110:113], v[216:219]\n"
        "s_waitcnt lgkmcnt(7)\n"
        "ds_read_b128 a[188:191], v76 offset:11264\n"
        "v_cvt_pk_bf16_f32 v67, v72, v73\n"
        "v_mfma_f32_16x16x32_bf16 v[216:219], v[86:89], v[104:107], v[216:219]\n"
        "s_mov_b32 s86, s87\n"
        "v_exp_f32 v102, v102\n"
        "v_mfma_f32_16x16x32_bf16 v[220:223], a[0:3], v[104:107], 0\n"
        "s_cselect_b32 s92, 588672, 0\n"
        "v_exp_f32 v103, v103\n"
        "v_mfma_f32_16x16x32_bf16 v[220:223], a[4:7], v[110:113], v[220:223]\n"
        "s_cselect_b32 s94, 73, 0\n"
        "s_add_u32 s87, s87, 43008\n"
        "v_add_f32 v114, v102, v103\n"
        "v_mfma_f32_16x16x32_bf16 v[220:223], a[8:11], v[116:119], v[220:223]\n"
        "s_sub_u32 s88, s88, s94\n"
        "ds_read_b128 a[192:195], v76 offset:12288\n"
        "v_cvt_pk_bf16_f32 v70, v140, v141\n"
        "v_mfma_f32_16x16x32_bf16 v[224:227], a[20:23], v[116:119], 0\n"
        "s_add_u32 s87, s87, s92\n"
        "ds_read_b64 v[62:63], v27\n"
        "ds_read_b64 v[68:69], v27\n"
        "v_mfma_f32_16x16x32_bf16 v[224:227], a[16:19], v[110:113], v[224:227]\n"
        "s_cmp_gt_i32 s85, 0\n"
        "ds_read_b64 v[74:75], v27\n"
        "v_cvt_pk_bf16_f32 v71, v142, v143\n"
        "v_mfma_f32_16x16x32_bf16 v[224:227], a[12:15], v[104:107], v[224:227]\n"
        "s_cselect_b32 s94, s87, s84\n"
        "ds_read_b32 v77, v77\n"
        "v_cvt_pk_bf16_f32 v72, v120, v121\n"
        "v_mfma_f32_16x16x32_bf16 v[140:143], a[24:27], v[104:107], 0\n"
        "s_add_u32 m0, s52, 0\n"
        "v_exp_f32 v108, v108\n"
        "v_mfma_f32_16x16x32_bf16 v[140:143], a[28:31], v[110:113], v[140:143]\n"
        "s_waitcnt lgkmcnt(10)\n"
        "v_exp_f32 v109, v109\n"
        "v_mfma_f32_16x16x32_bf16 v[140:143], a[32:35], v[116:119], v[140:143]\n"
        "s_sub_u32 s85, s85, 1\n"
        "buffer_load_dwordx4 v0, s[36:39], s94 offen lds\n"
        "v_add_f32 v73, v108, v109\n"
        "v_mfma_f32_16x16x32_bf16 v[228:231], a[44:47], v[116:119], 0\n"
        "v_add_f32 v114, v114, v73\n"
        "v_cvt_pk_bf16_f32 v73, v122, v123\n"
        "v_mfma_f32_16x16x32_bf16 v[228:231], a[40:43], v[110:113], v[228:231]\n"
        "v_rcp_f32 v114, v114\n"
        "v_mfma_f32_16x16x32_bf16 v[228:231], a[36:39], v[104:107], v[228:231]\n"
        "v_mul_f32 v102, v102, v114\n"
        "v_mul_f32 v103, v103, v114\n"
        "v_mfma_f32_16x16x32_bf16 v[120:123], a[48:51], v[104:107], 0\n"
        "s_waitcnt lgkmcnt(1)\n"
        "v_mul_f32 v108, v108, v114\n"
        "v_mul_f32 v109, v109, v114\n"
        "v_mfma_f32_16x16x32_bf16 v[120:123], a[52:55], v[110:113], v[120:123]\n"
        "s_waitcnt lgkmcnt(0)\n"
        "buffer_load_dwordx4 v1, s[36:39], s94 offen offset:1024 lds\n"
        "v_mul_f32_dpp v102, v148, v102 quad_perm:[0,0,0,0] row_mask:0xf bank_mask:0xf\n"
        "v_mfma_f32_16x16x32_bf16 v[120:123], a[56:59], v[116:119], v[120:123]\n"
        "v_mul_f32_dpp v103, v148, v103 quad_perm:[1,1,1,1] row_mask:0xf bank_mask:0xf\n"
        "v_mul_f32_dpp v108, v148, v108 quad_perm:[2,2,2,2] row_mask:0xf bank_mask:0xf\n"
        "v_mfma_f32_16x16x32_bf16 v[232:235], a[68:71], v[116:119], 0\n"
        "v_mul_f32_dpp v109, v148, v109 quad_perm:[3,3,3,3] row_mask:0xf bank_mask:0xf\n"
        "v_cvt_pk_bf16_f32 v114, v102, v103\n"
        "v_mfma_f32_16x16x32_bf16 v[232:235], a[64:67], v[110:113], v[232:235]\n"
        "v_cvt_pk_bf16_f32 v115, v108, v109\n"
        "v_and_b32 v160, v25, v114\n"
        "v_mfma_f32_16x16x32_bf16 v[232:235], a[60:63], v[104:107], v[232:235]\n"
        "v_and_b32 v161, v25, v115\n"
        "v_mov_b64 v[166:167], v[160:161]\n"
        "s_nop 0\n"
        "v_mfma_f32_16x16x32_bf16 v[236:239], v[60:63], v[160:163], 0\n"
        "buffer_load_dwordx4 v2, s[36:39], s94 offen offset:2048 lds\n"
        "v_mov_b64 v[172:173], v[160:161]\n"
        "v_mfma_f32_16x16x32_bf16 v[240:243], v[62:65], v[164:167], 0\n"
        "ds_read_b128 a[196:199], v76 offset:13312\n"
        "v_readfirstlane_b32 s83, v77\n"
        "v_mfma_f32_16x16x32_bf16 v[162:165], v[182:185], a[72:75], 0\n"
        "ds_read_b128 a[200:203], v76 offset:14336\n"
        "v_mov_b64 v[178:179], v[160:161]\n"
        "v_mfma_f32_16x16x32_bf16 v[162:165], v[188:191], a[76:79], v[162:165]\n"
        "ds_read_b128 a[204:207], v76 offset:15360\n"
        "v_mul_f32_dpp v128, v149, v102 quad_perm:[0,0,0,0] row_mask:0xf bank_mask:0xf\n"
        "v_mfma_f32_16x16x32_bf16 v[162:165], v[194:197], a[80:83], v[162:165]\n"
        "ds_read_b128 a[208:211], v76 offset:16384\n"
        "v_and_b32_dpp v180, v114, v25 row_ror:12 row_mask:0xf bank_mask:0xf\n"
        "v_mfma_f32_16x16x32_bf16 v[244:247], v[194:197], a[92:95], 0\n"
        "buffer_load_dwordx4 v3, s[36:39], s94 offen offset:3072 lds\n"
        "v_and_b32_dpp v181, v115, v25 row_ror:12 row_mask:0xf bank_mask:0xf\n"
        "v_mfma_f32_16x16x32_bf16 v[244:247], v[188:191], a[88:91], v[244:247]\n"
        "ds_read_b128 a[212:215], v76 offset:17408\n"
        "v_cvt_pk_bf16_f32 v76, v208, v209\n"
        "v_mfma_f32_16x16x32_bf16 v[244:247], v[182:185], a[84:87], v[244:247]\n"
        "ds_read_b128 a[216:219], v134\n"
        "v_cvt_pk_bf16_f32 v77, v210, v211\n"
        "v_mfma_f32_16x16x32_bf16 v[208:211], v[66:69], v[166:169], 0\n"
        "s_add_u32 m0, s52, 4096\n"
        "v_mov_b64 v[186:187], v[180:181]\n"
        "v_mov_b64 v[192:193], v[180:181]\n"
        "v_mfma_f32_16x16x32_bf16 v[64:67], v[68:71], v[170:173], 0\n"
        "v_mov_b64 v[198:199], v[180:181]\n"
        "v_cvt_pk_bf16_f32 v166, v236, v237\n"
        "v_mfma_f32_16x16x32_bf16 v[248:251], v[182:185], a[96:99], 0\n"
        "buffer_load_dwordx4 v4, s[36:39], s94 offen lds\n"
        "v_cvt_pk_bf16_f32 v236, v238, v239\n"
        "v_mfma_f32_16x16x32_bf16 v[248:251], v[188:191], a[100:103], v[248:251]\n"
        "ds_read_b128 a[220:223], v134 offset:1024\n"
        "v_cvt_pk_bf16_f32 v70, v240, v241\n"
        "v_mfma_f32_16x16x32_bf16 v[248:251], v[194:197], a[104:107], v[248:251]\n"
        "v_cvt_pk_bf16_f32 v167, v242, v243\n"
        "v_cvt_pk_bf16_f32 v237, v208, v209\n"
        "v_mfma_f32_16x16x32_bf16 v[240:243], v[194:197], a[116:119], 0\n"
        "v_cvt_pk_bf16_f32 v71, v210, v211\n"
        "v_cvt_pk_bf16_f32 v168, v64, v65\n"
        "v_mfma_f32_16x16x32_bf16 v[240:243], v[188:191], a[112:115], v[240:243]\n"
        "ds_read_b128 a[224:227], v134 offset:2048\n"
        "v_cvt_pk_bf16_f32 v238, v66, v67\n"
        "v_mfma_f32_16x16x32_bf16 v[240:243], v[182:185], a[108:111], v[240:243]\n"
        "buffer_load_dwordx4 v5, s[36:39], s94 offen offset:1024 lds\n"
        "v_fmac_f32_dpp v128, v149, v103 quad_perm:[1,1,1,1] row_mask:0xf bank_mask:0xf\n"
        "v_mfma_f32_16x16x32_bf16 v[64:67], v[72:75], v[172:175], 0\n"
        "v_cvt_pk_bf16_f32 v170, v220, v221\n"
        "v_cvt_pk_bf16_f32 v171, v222, v223\n"
        "v_mfma_f32_16x16x32_bf16 v[208:211], v[74:77], v[176:179], 0\n"
        "s_nop 0\n"
        "ds_read_b128 a[228:231], v134 offset:3088\n"
        "v_cvt_pk_bf16_f32 v172, v224, v225\n"
        "v_mfma_f32_16x16x32_bf16 v[174:177], v[182:185], a[120:123], 0\n"
        "s_nop 0\n"
        "v_cvt_pk_bf16_f32 v72, v64, v65\n"
        "v_cvt_pk_bf16_f32 v169, v66, v67\n"
        "v_mfma_f32_16x16x32_bf16 v[174:177], v[188:191], a[124:127], v[174:177]\n"
        "ds_write_b128 v12, v[166:169]\n"
        "v_mfma_f32_16x16x32_bf16 v[174:177], v[194:197], a[128:131], v[174:177]\n"
        "buffer_load_dwordx4 v6, s[36:39], s94 offen offset:2048 lds\n"
        "v_cvt_pk_bf16_f32 v239, v208, v209\n"
        "v_mfma_f32_16x16x32_bf16 v[218:221], v[194:197], a[140:143], 0\n"
        "ds_write_b128 v12, v[236:239] offset:1024\n"
        "v_mfma_f32_16x16x32_bf16 v[218:221], v[188:191], a[136:139], v[218:221]\n"
        "ds_read_b128 a[232:235], v134 offset:4112\n"
        "v_cvt_pk_bf16_f32 v73, v210, v211\n"
        "v_mfma_f32_16x16x32_bf16 v[218:221], v[182:185], a[132:135], v[218:221]\n"
        "ds_write_b128 v12, v[70:73] offset:2048\n"
        "v_mfma_f32_16x16x32_bf16 v[208:211], v[166:169], v[166:169], 0\n"
        "v_cvt_pk_bf16_f32 v173, v226, v227\n"
        "v_cvt_pk_bf16_f32 v166, v140, v141\n"
        "v_mfma_f32_16x16x32_bf16 v[208:211], v[236:239], v[236:239], v[208:211]\n"
        "buffer_load_dwordx4 v7, s[36:39], s94 offen offset:3072 lds\n"
        "v_cvt_pk_bf16_f32 v167, v142, v143\n"
        "v_mfma_f32_16x16x32_bf16 v[208:211], v[70:73], v[70:73], v[208:211]\n"
        "ds_read_b128 a[236:239], v134 offset:5136\n"
        "v_cvt_pk_bf16_f32 v168, v228, v229\n"
        "v_mfma_f32_16x16x32_bf16 v[140:143], v[124:127], v[124:127], 0\n"
        "v_cvt_pk_bf16_f32 v60, v162, v163\n"
        "v_cvt_pk_bf16_f32 v61, v164, v165\n"
        "v_mfma_f32_16x16x32_bf16 v[140:143], v[130:133], v[130:133], v[140:143]\n"
        "s_add_u32 m0, s52, 8192\n"
        "ds_read_b128 a[240:243], v134 offset:6176\n"
        "v_cvt_pk_bf16_f32 v64, v244, v245\n"
        "v_mfma_f32_16x16x32_bf16 v[140:143], v[136:139], v[136:139], v[140:143]\n"
        "v_cvt_pk_bf16_f32 v65, v246, v247\n"
        "v_cvt_pk_bf16_f32 v66, v248, v249\n"
        "v_mfma_f32_16x16x32_bf16 v[160:163], v[78:81], v[136:139], 0\n"
        "buffer_load_dwordx4 v8, s[36:39], s94 offen lds\n"
        "v_cvt_pk_bf16_f32 v67, v250, v251\n"
        "v_mfma_f32_16x16x32_bf16 v[160:163], v[82:85], v[130:133], v[160:163]\n"
        "v_cvt_pk_bf16_f32 v70, v240, v241\n"
        "v_cvt_pk_bf16_f32 v71, v242, v243\n"
        "v_mfma_f32_16x16x32_bf16 v[160:163], v[86:89], v[124:127], v[160:163]\n"
        "ds_read_b128 a[244:247], v134 offset:7200\n"
        "v_cvt_pk_bf16_f32 v72, v174, v175\n"
        "v_mfma_f32_16x16x32_bf16 v[222:225], v[60:63], v[180:183], 0\n"
        "v_cvt_pk_bf16_f32 v73, v176, v177\n"
        "v_cvt_pk_bf16_f32 v76, v218, v219\n"
        "v_mfma_f32_16x16x32_bf16 v[174:177], v[62:65], v[184:187], 0\n"
        "v_cvt_pk_bf16_f32 v77, v220, v221\n"
        "v_cvt_pk_bf16_f32 v169, v230, v231\n"
        "v_mfma_f32_16x16x32_bf16 v[162:165], v[66:69], v[186:189], 0\n"
        "v_cvt_pk_bf16_f32 v64, v120, v121\n"
        "v_cvt_pk_bf16_f32 v65, v122, v123\n"
        "v_mfma_f32_16x16x32_bf16 v[120:123], v[68:71], v[190:193], 0\n"
        "ds_read_b128 a[248:251], v134 offset:8224\n"
        "v_cvt_pk_bf16_f32 v66, v232, v233\n"
        "v_mfma_f32_16x16x32_bf16 v[178:181], v[72:75], v[192:195], 0\n"
        "v_cvt_pk_bf16_f32 v67, v234, v235\n"
        "v_cvt_pk_bf16_f32 v70, v222, v223\n"
        "v_mfma_f32_16x16x32_bf16 v[182:185], v[74:77], v[196:199], 0\n"
        "v_cvt_pk_bf16_f32 v186, v224, v225\n"
        "v_cvt_pk_bf16_f32 v190, v174, v175\n"
        "v_mfma_f32_16x16x32_bf16 v[194:197], v[104:107], v[170:173], 0\n"
        "ds_read_b128 a[252:255], v134 offset:9264\n"
        "v_cvt_pk_bf16_f32 v71, v176, v177\n"
        "v_mfma_f32_16x16x32_bf16 v[194:197], v[110:113], v[166:169], v[194:197]\n"
        "v_cvt_pk_bf16_f32 v187, v162, v163\n"
        "v_cvt_pk_bf16_f32 v191, v164, v165\n"
        "v_mfma_f32_16x16x32_bf16 v[194:197], v[116:119], v[64:67], v[194:197]\n"
        "v_cvt_pk_bf16_f32 v72, v120, v121\n"
        "v_cvt_pk_bf16_f32 v188, v122, v123\n"
        "v_mfma_f32_16x16x32_bf16 v[64:67], a[8:11], v[136:139], 0\n"
        "v_cvt_pk_bf16_f32 v73, v180, v181\n"
        "v_cvt_pk_bf16_f32 v189, v182, v183\n"
        "v_mfma_f32_16x16x32_bf16 v[64:67], a[4:7], v[130:133], v[64:67]\n"
        "ds_write_b128 v12, v[70:73] offset:3072\n"
        "v_mfma_f32_16x16x32_bf16 v[64:67], a[0:3], v[124:127], v[64:67]\n"
        "ds_write_b128 v12, v[186:189] offset:4096\n"
        "v_mfma_f32_16x16x32_bf16 v[120:123], v[70:73], v[70:73], 0\n"
        "v_cvt_pk_bf16_f32 v192, v178, v179\n"
        "v_cvt_pk_bf16_f32 v193, v184, v185\n"
        "v_mfma_f32_16x16x32_bf16 v[120:123], v[186:189], v[186:189], v[120:123]\n"
        "v_fmac_f32_dpp v128, v149, v108 quad_perm:[2,2,2,2] row_mask:0xf bank_mask:0xf\n"
        "v_cndmask_b32_e64 v60, v208, v209, s[56:57]\n"
        "v_mfma_f32_16x16x32_bf16 v[120:123], v[190:193], v[190:193], v[120:123]\n"
        "ds_write_b128 v12, v[190:193] offset:5120\n"
        "v_mfma_f32_16x16x32_bf16 v[70:73], a[20:23], v[136:139], 0\n"
        "v_fmac_f32_dpp v128, v149, v109 quad_perm:[3,3,3,3] row_mask:0xf bank_mask:0xf\n"
        "v_add_f32 v128, v128, v135\n"
        "v_mfma_f32_16x16x32_bf16 v[70:73], a[16:19], v[130:133], v[70:73]\n"
        "v_mul_f32 v61, s80, v128\n"
        "v_cndmask_b32_e64 v60, v60, v210, s[58:59]\n"
        "v_mfma_f32_16x16x32_bf16 v[70:73], a[12:15], v[124:127], v[70:73]\n"
        "ds_read_b128 v[28:31], v134 offset:10288\n"
        "v_cndmask_b32_e64 v60, v60, v211, s[60:61]\n"
        "v_mfma_f32_16x16x32_bf16 v[162:165], a[24:27], v[124:127], 0\n"
        "ds_read_b128 v[32:35], v134 offset:11312\n"
        "v_cvt_pk_bf16_f32 v166, v64, v65\n"
        "v_mfma_f32_16x16x32_bf16 v[162:165], a[28:31], v[130:133], v[162:165]\n"
        "ds_read_b128 v[36:39], v134 offset:12352\n"
        "v_cndmask_b32_e64 v64, v120, v121, s[56:57]\n"
        "v_mfma_f32_16x16x32_bf16 v[162:165], a[32:35], v[136:139], v[162:165]\n"
        "v_cndmask_b32_e64 v64, v64, v122, s[58:59]\n"
        "v_cndmask_b32_e64 v64, v64, v123, s[60:61]\n"
        "v_mfma_f32_16x16x32_bf16 v[120:123], a[44:47], v[136:139], 0\n"
        "ds_read_b128 v[40:43], v134 offset:13376\n"
        "v_cvt_pk_bf16_f32 v167, v66, v67\n"
        "v_mfma_f32_16x16x32_bf16 v[120:123], a[40:43], v[130:133], v[120:123]\n"
        "v_cndmask_b32_dpp v60, v64, v60, vcc row_ror:4 row_mask:0xf bank_mask:0xf\n"
        "v_mul_f32 v60, s80, v60\n"
        "v_mfma_f32_16x16x32_bf16 v[120:123], a[36:39], v[124:127], v[120:123]\n"
        "v_fma_f32 v61, -v61, v61, v60\n"
        "v_max_f32 v61, 0, v61\n"
        "v_mfma_f32_16x16x32_bf16 v[64:67], a[48:51], v[124:127], 0\n"
        "v_sub_f32 v60, 0x44800000, v60\n"
        "v_add_f32 v61, s81, v61\n"
        "v_mfma_f32_16x16x32_bf16 v[64:67], a[52:55], v[130:133], v[64:67]\n"
        "ds_read_b128 v[44:47], v134 offset:14400\n"
        "v_bfi_b32 v61, s84, v60, v61\n"
        "v_mfma_f32_16x16x32_bf16 v[64:67], a[56:59], v[136:139], v[64:67]\n"
        "v_rsq_f32 v60, v61\n"
        "v_mfma_f32_16x16x32_bf16 v[170:173], a[68:71], v[136:139], 0\n"
        "v_cvt_pk_bf16_f32 v168, v70, v71\n"
        "v_cvt_pk_bf16_f32 v169, v72, v73\n"
        "v_mfma_f32_16x16x32_bf16 v[170:173], a[64:67], v[130:133], v[170:173]\n"
        "v_cvt_f16_f32 v60, v60\n"
        "v_cvt_pk_bf16_f32 v70, v162, v163\n"
        "v_mfma_f32_16x16x32_bf16 v[170:173], a[60:63], v[124:127], v[170:173]\n"
        "ds_write_b16 v18, v60\n"
        "v_mfma_f32_16x16x32_bf16 v[174:177], v[144:147], v[144:147], 0\n"
        "ds_write_b16 v19, v60\n"
        "v_mfma_f32_16x16x32_bf16 v[174:177], v[150:153], v[150:153], v[174:177]\n"
        "ds_read_b128 v[48:51], v134 offset:15440\n"
        "v_cvt_pk_bf16_f32 v71, v164, v165\n"
        "v_mfma_f32_16x16x32_bf16 v[174:177], v[156:159], v[156:159], v[174:177]\n"
        "ds_read_b128 v[52:55], v134 offset:16464\n"
        "v_cvt_pk_bf16_f32 v72, v120, v121\n"
        "v_mfma_f32_16x16x32_bf16 v[162:165], v[78:81], v[156:159], 0\n"
        "v_cvt_pk_bf16_f32 v73, v122, v123\n"
        "v_cvt_pk_bf16_f32 v120, v64, v65\n"
        "v_mfma_f32_16x16x32_bf16 v[162:165], v[82:85], v[150:153], v[162:165]\n"
        "v_cvt_pk_bf16_f32 v121, v66, v67\n"
        "v_cvt_pk_bf16_f32 v122, v170, v171\n"
        "v_mfma_f32_16x16x32_bf16 v[162:165], v[86:89], v[144:147], v[162:165]\n"
        "v_cvt_pk_bf16_f32 v123, v172, v173\n"
        "v_cndmask_b32_e64 v60, v212, v213, s[56:57]\n"
        "v_mfma_f32_16x16x32_bf16 v[170:173], v[124:127], v[166:169], 0\n"
        "ds_read_b128 v[56:59], v134 offset:17488\n"
        "v_cndmask_b32_e64 v60, v60, v214, s[58:59]\n"
        "v_mfma_f32_16x16x32_bf16 v[170:173], v[130:133], v[70:73], v[170:173]\n"
        "v_cndmask_b32_e64 v60, v60, v215, s[60:61]\n"
        "v_cndmask_b32_e64 v61, v140, v141, s[56:57]\n"
        "v_mfma_f32_16x16x32_bf16 v[170:173], v[136:139], v[120:123], v[170:173]\n"
        "s_waitcnt lgkmcnt(3)\n"
        "v_cndmask_b32_e64 v61, v61, v142, s[58:59]\n"
        "v_cndmask_b32_e64 v61, v61, v143, s[60:61]\n"
        "v_mfma_f32_16x16x32_bf16 v[64:67], a[8:11], v[156:159], 0\n"
        "s_barrier\n"
        "v_cndmask_b32_e64 v70, v174, v175, s[56:57]\n"
        "v_cndmask_b32_e64 v70, v70, v176, s[58:59]\n"
        "v_mfma_f32_16x16x32_bf16 v[64:67], a[4:7], v[150:153], v[64:67]\n"
        "v_cndmask_b32_e64 v70, v70, v177, s[60:61]\n"
        "v_cndmask_b32_dpp v198, v162, v160, vcc row_ror:4 row_mask:0xf bank_mask:0xf\n"
        "v_mfma_f32_16x16x32_bf16 v[64:67], a[0:3], v[144:147], v[64:67]\n"
        "v_cndmask_b32_dpp v61, v70, v61, vcc row_ror:4 row_mask:0xf bank_mask:0xf\n"
        "v_cndmask_b32_dpp v198, v198, v216, vcc row_ror:4 row_mask:0xf bank_mask:0xf\n"
        "v_mfma_f32_16x16x32_bf16 v[70:73], a[12:15], v[144:147], 0\n"
        "s_nop 0\n"
        "v_cndmask_b32_dpp v60, v61, v60, vcc row_ror:4 row_mask:0xf bank_mask:0xf\n"
        "v_mul_f32 v60, s80, v60\n"
        "v_mfma_f32_16x16x32_bf16 v[70:73], a[16:19], v[150:153], v[70:73]\n"
        "s_nop 0\n"
        "v_cvt_pk_bf16_f32 v120, v64, v65\n"
        "v_cvt_pk_bf16_f32 v121, v66, v67\n"
        "v_mfma_f32_16x16x32_bf16 v[70:73], a[20:23], v[156:159], v[70:73]\n"
        "s_waitcnt lgkmcnt(0)\n"
        "v_mul_f32 v61, s80, v198\n"
        "v_fma_f32 v61, -v61, v61, v60\n"
        "v_mfma_f32_16x16x32_bf16 v[64:67], a[32:35], v[156:159], 0\n"
        "v_max_f32 v61, 0, v61\n"
        "v_add_f32 v61, s81, v61\n"
        "v_mfma_f32_16x16x32_bf16 v[64:67], a[28:31], v[150:153], v[64:67]\n"
        "s_nop 0\n"
        "v_cvt_pk_bf16_f32 v122, v70, v71\n"
        "v_cvt_pk_bf16_f32 v123, v72, v73\n"
        "v_mfma_f32_16x16x32_bf16 v[64:67], a[24:27], v[144:147], v[64:67]\n"
        "s_nop 0\n"
        "v_rsq_f32 v76, v61\n"
        "v_mfma_f32_16x16x32_bf16 v[70:73], a[36:39], v[144:147], 0\n"
        "s_nop 0\n"
        "v_cndmask_b32_dpp v199, v163, v161, vcc row_ror:4 row_mask:0xf bank_mask:0xf\n"
        "s_nop 0\n"
        "v_mfma_f32_16x16x32_bf16 v[70:73], a[40:43], v[150:153], v[70:73]\n"
        "s_nop 0\n"
        "v_cvt_pk_bf16_f32 v140, v64, v65\n"
        "v_cvt_pk_bf16_f32 v141, v66, v67\n"
        "v_mfma_f32_16x16x32_bf16 v[70:73], a[44:47], v[156:159], v[70:73]\n"
        "v_cndmask_b32_dpp v199, v199, v217, vcc row_ror:4 row_mask:0xf bank_mask:0xf\n"
        "v_mfma_f32_16x16x32_bf16 v[64:67], a[56:59], v[156:159], 0\n"
        "s_nop 0\n"
        "ds_read_b128 v[162:165], v13\n"
        "ds_read_b128 v[166:169], v13 offset:1024\n"
        "v_mfma_f32_16x16x32_bf16 v[64:67], a[52:55], v[150:153], v[64:67]\n"
        "s_nop 0\n"
        "ds_read_b128 v[174:177], v13 offset:2048\n"
        "v_cvt_pk_bf16_f32 v142, v70, v71\n"
        "v_mfma_f32_16x16x32_bf16 v[64:67], a[48:51], v[144:147], v[64:67]\n"
        "s_nop 0\n"
        "v_cvt_pk_bf16_f32 v143, v72, v73\n"
        "v_mfma_f32_16x16x32_bf16 v[70:73], a[60:63], v[144:147], 0\n"
        "s_nop 2\n"
        "v_mfma_f32_16x16x32_bf16 v[70:73], a[64:67], v[150:153], v[70:73]\n"
        "s_nop 0\n"
        "v_cvt_pk_bf16_f32 v178, v64, v65\n"
        "v_cvt_pk_bf16_f32 v179, v66, v67\n"
        "v_mfma_f32_16x16x32_bf16 v[70:73], a[68:71], v[156:159], v[70:73]\n"
        "s_nop 1\n"
        "v_mfma_f32_16x16x32_bf16 v[64:67], v[116:119], a[80:83], 0\n"
        "s_nop 2\n"
        "v_mfma_f32_16x16x32_bf16 v[64:67], v[110:113], a[76:79], v[64:67]\n"
        "s_nop 0\n"
        "v_cvt_pk_bf16_f32 v180, v70, v71\n"
        "v_cvt_pk_bf16_f32 v181, v72, v73\n"
        "v_mfma_f32_16x16x32_bf16 v[64:67], v[104:107], a[72:75], v[64:67]\n"
        "s_nop 1\n"
        "v_mfma_f32_16x16x32_bf16 v[70:73], v[104:107], a[84:87], 0\n"
        "s_waitcnt lgkmcnt(0)\n"
        "s_nop 1\n"
        "v_mfma_f32_16x16x32_bf16 v[70:73], v[110:113], a[88:91], v[70:73]\n"
        "s_nop 0\n"
        "v_cvt_pk_bf16_f32 v60, v64, v65\n"
        "v_cvt_pk_bf16_f32 v61, v66, v67\n"
        "v_mfma_f32_16x16x32_bf16 v[70:73], v[116:119], a[92:95], v[70:73]\n"
        "s_nop 1\n"
        "v_mfma_f32_16x16x32_bf16 v[182:185], v[144:147], v[120:123], 0\n"
        "s_nop 2\n"
        "v_mfma_f32_16x16x32_bf16 v[182:185], v[150:153], v[140:143], v[182:185]\n"
        "s_nop 0\n"
        "v_cvt_pk_bf16_f32 v64, v70, v71\n"
        "v_cvt_pk_bf16_f32 v65, v72, v73\n"
        "v_mfma_f32_16x16x32_bf16 v[182:185], v[156:159], v[178:181], v[182:185]\n"
        "s_nop 1\n"
        "v_mfma_f32_16x16x32_bf16 v[70:73], a[152:155], v[174:177], 0\n"
        "s_nop 2\n"
        "v_mfma_f32_16x16x32_bf16 v[70:73], a[148:151], v[166:169], v[70:73]\n"
        "s_nop 0\n"
        "v_cndmask_b32_dpp v148, v182, v170, vcc row_ror:4 row_mask:0xf bank_mask:0xf\n"
        "v_cndmask_b32_dpp v154, v183, v171, vcc row_ror:4 row_mask:0xf bank_mask:0xf\n"
        "v_mfma_f32_16x16x32_bf16 v[70:73], a[144:147], v[162:165], v[70:73]\n"
        "v_cndmask_b32_dpp v160, v184, v172, vcc row_ror:4 row_mask:0xf bank_mask:0xf\n"
        "v_cndmask_b32_dpp v200, v185, v173, vcc row_ror:4 row_mask:0xf bank_mask:0xf\n"
        "v_mfma_f32_16x16x32_bf16 v[120:123], a[156:159], v[162:165], 0\n"
        "ds_read_b128 v[170:173], v13 offset:3072\n"
        "ds_read_b128 v[178:181], v13 offset:4096\n"
        "v_mfma_f32_16x16x32_bf16 v[120:123], a[160:163], v[166:169], v[120:123]\n"
        "ds_read_b128 v[182:185], v13 offset:5120\n"
        "ds_read_b128 v[140:143], v20\n"
        "v_mfma_f32_16x16x32_bf16 v[120:123], a[164:167], v[174:177], v[120:123]\n"
        "ds_read_b64 v[66:67], v21\n"
        "v_cndmask_b32_dpp v148, v148, v194, vcc row_ror:4 row_mask:0xf bank_mask:0xf\n"
        "v_mfma_f32_16x16x32_bf16 v[186:189], a[176:179], v[174:177], 0\n"
        "v_cndmask_b32_dpp v154, v154, v195, vcc row_ror:4 row_mask:0xf bank_mask:0xf\n"
        "v_cndmask_b32_dpp v160, v160, v196, vcc row_ror:4 row_mask:0xf bank_mask:0xf\n"
        "v_mfma_f32_16x16x32_bf16 v[186:189], a[172:175], v[166:169], v[186:189]\n"
        "v_cndmask_b32_dpp v200, v200, v197, vcc row_ror:4 row_mask:0xf bank_mask:0xf\n"
        "v_mul_f32_dpp v148, v76, v148 quad_perm:[0,0,0,0] row_mask:0xf bank_mask:0xf\n"
        "v_mfma_f32_16x16x32_bf16 v[186:189], a[168:171], v[162:165], v[186:189]\n"
        "v_mul_f32_dpp v154, v76, v154 quad_perm:[1,1,1,1] row_mask:0xf bank_mask:0xf\n"
        "v_mul_f32_dpp v160, v76, v160 quad_perm:[2,2,2,2] row_mask:0xf bank_mask:0xf\n"
        "v_mfma_f32_16x16x32_bf16 v[190:193], a[180:183], v[162:165], 0\n"
        "v_mul_f32_dpp v200, v76, v200 quad_perm:[3,3,3,3] row_mask:0xf bank_mask:0xf\n"
        "v_max3_f32 v77, v148, v154, v160\n"
        "v_mfma_f32_16x16x32_bf16 v[190:193], a[184:187], v[166:169], v[190:193]\n"
        "v_max_f32 v77, v77, v200\n"
        "v_mul_f32 v77, v77, v76\n"
        "v_mfma_f32_16x16x32_bf16 v[190:193], a[188:191], v[174:177], v[190:193]\n"
        "v_fma_f32 v148, v148, v76, -v77\n"
        "v_fma_f32 v154, v154, v76, -v77\n"
        "v_mfma_f32_16x16x32_bf16 v[194:197], a[200:203], v[174:177], 0\n"
        "v_fma_f32 v160, v160, v76, -v77\n"
        "v_fma_f32 v200, v200, v76, -v77\n"
        "v_mfma_f32_16x16x32_bf16 v[194:197], a[196:199], v[166:169], v[194:197]\n"
        "s_waitcnt lgkmcnt(0)\n"
        "v_cvt_f32_f16_sdwa v134, v66 dst_sel:DWORD dst_unused:UNUSED_PAD src0_sel:WORD_0\n"
        "v_cvt_f32_f16_sdwa v143, v66 dst_sel:DWORD dst_unused:UNUSED_PAD src0_sel:WORD_1\n"
        "v_mfma_f32_16x16x32_bf16 v[194:197], a[192:195], v[162:165], v[194:197]\n"
        "s_barrier\n"
        "v_lshrrev_b32 v77, 0, v66\n"
        "v_lshrrev_b32 v135, 16, v66\n"
        "v_mfma_f32_16x16x32_bf16 v[208:211], a[204:207], v[162:165], 0\n"
        "v_cvt_f32_f16_sdwa v161, v140 dst_sel:DWORD dst_unused:UNUSED_PAD src0_sel:WORD_0\n"
        "v_cvt_f32_f16_sdwa v244, v140 dst_sel:DWORD dst_unused:UNUSED_PAD src0_sel:WORD_1\n"
        "v_mfma_f32_16x16x32_bf16 v[208:211], a[208:211], v[166:169], v[208:211]\n"
        "v_cvt_f32_f16_sdwa v245, v141 dst_sel:DWORD dst_unused:UNUSED_PAD src0_sel:WORD_0\n"
        "v_cvt_f32_f16_sdwa v246, v141 dst_sel:DWORD dst_unused:UNUSED_PAD src0_sel:WORD_1\n"
        "v_mfma_f32_16x16x32_bf16 v[208:211], a[212:215], v[174:177], v[208:211]\n"
        "v_exp_f32 v148, v148\n"
        "v_mfma_f32_16x16x32_bf16 v[212:215], a[152:155], v[182:185], 0\n"
        "v_exp_f32 v154, v154\n"
        "v_mfma_f32_16x16x32_bf16 v[212:215], a[148:151], v[178:181], v[212:215]\n"
        "v_exp_f32 v160, v160\n"
        "v_mfma_f32_16x16x32_bf16 v[212:215], a[144:147], v[170:173], v[212:215]\n"
        "v_exp_f32 v200, v200\n"
        "v_mfma_f32_16x16x32_bf16 v[216:219], a[156:159], v[170:173], 0\n"
        "v_cvt_pk_bf16_f32 v220, v70, v71\n"
        "v_cvt_pk_bf16_f32 v221, v72, v73\n"
        "v_mfma_f32_16x16x32_bf16 v[216:219], a[160:163], v[178:181], v[216:219]\n"
        "v_add_f32 v66, v148, v154\n"
        "v_add_f32 v67, v160, v200\n"
        "v_mfma_f32_16x16x32_bf16 v[216:219], a[164:167], v[182:185], v[216:219]\n"
        "v_add_f32 v66, v66, v67\n"
        "v_cvt_pk_bf16_f32 v222, v120, v121\n"
        "v_mfma_f32_16x16x32_bf16 v[70:73], a[176:179], v[182:185], 0\n"
        "v_cvt_pk_bf16_f32 v223, v122, v123\n"
        "v_cvt_pk_bf16_f32 v224, v186, v187\n"
        "v_mfma_f32_16x16x32_bf16 v[70:73], a[172:175], v[178:181], v[70:73]\n"
        "ds_write_b128 v202, v[220:223]\n"
        "v_mfma_f32_16x16x32_bf16 v[70:73], a[168:171], v[170:173], v[70:73]\n"
        "v_cvt_pk_bf16_f32 v225, v188, v189\n"
        "v_cvt_pk_bf16_f32 v226, v190, v191\n"
        "v_mfma_f32_16x16x32_bf16 v[186:189], a[180:183], v[170:173], 0\n"
        "v_cvt_pk_bf16_f32 v227, v192, v193\n"
        "v_cvt_pk_bf16_f32 v190, v194, v195\n"
        "v_mfma_f32_16x16x32_bf16 v[186:189], a[184:187], v[178:181], v[186:189]\n"
        "ds_write_b128 v202, v[224:227] offset:64\n"
        "v_mfma_f32_16x16x32_bf16 v[186:189], a[188:191], v[182:185], v[186:189]\n"
        "v_cvt_pk_bf16_f32 v191, v196, v197\n"
        "v_cvt_pk_bf16_f32 v192, v208, v209\n"
        "v_mfma_f32_16x16x32_bf16 v[194:197], a[200:203], v[182:185], 0\n"
        "v_cvt_pk_bf16_f32 v193, v210, v211\n"
        "v_cvt_pk_bf16_f32 v208, v212, v213\n"
        "v_mfma_f32_16x16x32_bf16 v[194:197], a[196:199], v[178:181], v[194:197]\n"
        "ds_write_b128 v202, v[190:193] offset:128\n"
        "v_mfma_f32_16x16x32_bf16 v[194:197], a[192:195], v[170:173], v[194:197]\n"
        "v_rcp_f32 v66, v66\n"
        "v_mfma_f32_16x16x32_bf16 v[228:231], a[204:207], v[170:173], 0\n"
        "v_mul_f32 v148, v148, v66\n"
        "v_mul_f32 v154, v154, v66\n"
        "v_mfma_f32_16x16x32_bf16 v[228:231], a[208:211], v[178:181], v[228:231]\n"
        "v_mul_f32 v160, v160, v66\n"
        "v_mul_f32 v200, v200, v66\n"
        "v_mfma_f32_16x16x32_bf16 v[228:231], a[212:215], v[182:185], v[228:231]\n"
        "v_mul_f32_dpp v148, v76, v148 quad_perm:[0,0,0,0] row_mask:0xf bank_mask:0xf\n"
        "v_mul_f32_dpp v154, v76, v154 quad_perm:[1,1,1,1] row_mask:0xf bank_mask:0xf\n"
        "v_mfma_f32_16x16x32_bf16 v[232:235], v[116:119], a[104:107], 0\n"
        "v_mul_f32_dpp v160, v76, v160 quad_perm:[2,2,2,2] row_mask:0xf bank_mask:0xf\n"
        "v_mul_f32_dpp v200, v76, v200 quad_perm:[3,3,3,3] row_mask:0xf bank_mask:0xf\n"
        "v_mfma_f32_16x16x32_bf16 v[232:235], v[110:113], a[100:103], v[232:235]\n"
        "v_cvt_pk_bf16_f32 v140, v148, v154\n"
        "v_cvt_pk_bf16_f32 v141, v160, v200\n"
        "v_mfma_f32_16x16x32_bf16 v[232:235], v[104:107], a[96:99], v[232:235]\n"
        "v_and_b32 v102, v25, v140\n"
        "v_and_b32 v103, v25, v141\n"
        "v_mfma_f32_16x16x32_bf16 v[236:239], v[104:107], a[108:111], 0\n"
        "v_mov_b64 v[108:109], v[102:103]\n"
        "v_mov_b64 v[114:115], v[102:103]\n"
        "v_mfma_f32_16x16x32_bf16 v[236:239], v[110:113], a[112:115], v[236:239]\n"
        "v_mov_b64 v[120:121], v[102:103]\n"
        "v_cvt_pk_bf16_f32 v209, v214, v215\n"
        "v_mfma_f32_16x16x32_bf16 v[236:239], v[116:119], a[116:119], v[236:239]\n"
        "v_cvt_pk_bf16_f32 v210, v216, v217\n"
        "v_cvt_pk_bf16_f32 v211, v218, v219\n"
        "v_mfma_f32_16x16x32_bf16 v[212:215], v[60:63], v[102:105], 0\n"
        "ds_write_b128 v203, v[208:211]\n"
        "v_mfma_f32_16x16x32_bf16 v[216:219], v[62:65], v[106:109], 0\n"
        "v_cvt_pk_bf16_f32 v64, v70, v71\n"
        "v_cvt_pk_bf16_f32 v65, v72, v73\n"
        "v_mfma_f32_16x16x32_bf16 v[240:243], v[162:165], v[220:223], 0\n"
        "v_cvt_pk_bf16_f32 v66, v186, v187\n"
        "v_cvt_pk_bf16_f32 v67, v188, v189\n"
        "v_mfma_f32_16x16x32_bf16 v[240:243], v[166:169], v[224:227], v[240:243]\n"
        "ds_write_b128 v203, v[64:67] offset:64\n"
        "v_mfma_f32_16x16x32_bf16 v[240:243], v[174:177], v[190:193], v[240:243]\n"
        "v_cvt_pk_bf16_f32 v70, v194, v195\n"
        "v_cvt_pk_bf16_f32 v71, v196, v197\n"
        "v_mfma_f32_16x16x32_bf16 v[186:189], v[170:173], v[208:211], 0\n"
        "v_cvt_pk_bf16_f32 v72, v228, v229\n"
        "v_cvt_pk_bf16_f32 v73, v230, v231\n"
        "v_mfma_f32_16x16x32_bf16 v[186:189], v[178:181], v[64:67], v[186:189]\n"
        "ds_write_b128 v203, v[70:73] offset:128\n"
        "v_mfma_f32_16x16x32_bf16 v[186:189], v[182:185], v[70:73], v[186:189]\n"
        "ds_read_b128 v[94:97], v27 offset:64\n"
        "ds_read_b128 v[98:101], v27 offset:128\n"
        "v_mfma_f32_16x16x32_bf16 v[190:193], v[116:119], a[128:131], 0\n"
        "v_and_b32_dpp v122, v140, v25 row_ror:12 row_mask:0xf bank_mask:0xf\n"
        "v_and_b32_dpp v123, v141, v25 row_ror:12 row_mask:0xf bank_mask:0xf\n"
        "v_mfma_f32_16x16x32_bf16 v[190:193], v[110:113], a[124:127], v[190:193]\n"
        "v_mov_b64 v[128:129], v[122:123]\n"
        "v_cvt_pk_bf16_f32 v66, v232, v233\n"
        "v_mfma_f32_16x16x32_bf16 v[190:193], v[104:107], a[120:123], v[190:193]\n"
        "v_cvt_pk_bf16_f32 v67, v234, v235\n"
        "v_cvt_pk_bf16_f32 v70, v236, v237\n"
        "v_mfma_f32_16x16x32_bf16 v[194:197], v[104:107], a[132:135], 0\n"
        "v_cvt_pk_bf16_f32 v71, v238, v239\n"
        "v_cvt_pk_bf16_f32 v102, v212, v213\n"
        "v_mfma_f32_16x16x32_bf16 v[194:197], v[110:113], a[136:139], v[194:197]\n"
        "v_cvt_pk_bf16_f32 v106, v214, v215\n"
        "v_cvt_pk_bf16_f32 v208, v216, v217\n"
        "v_mfma_f32_16x16x32_bf16 v[194:197], v[116:119], a[140:143], v[194:197]\n"
        "v_cvt_pk_bf16_f32 v103, v218, v219\n"
        "v_cndmask_b32_e64 v76, v240, v241, s[56:57]\n"
        "v_mfma_f32_16x16x32_bf16 v[210:213], v[66:69], v[108:111], 0\n"
        "v_cndmask_b32_e64 v76, v76, v242, s[58:59]\n"
        "v_cndmask_b32_e64 v76, v76, v243, s[60:61]\n"
        "v_mfma_f32_16x16x32_bf16 v[64:67], v[68:71], v[112:115], 0\n"
        "s_waitcnt lgkmcnt(0)\n"
        "v_mul_f32 v76, v76, v134\n"
        "v_mul_f32 v76, v76, v134\n"
        "v_mfma_f32_16x16x32_bf16 v[110:113], v[174:177], a[224:227], 0\n"
        "v_exp_f32 v76, v76\n"
        "v_mfma_f32_16x16x32_bf16 v[110:113], v[166:169], a[220:223], v[110:113]\n"
        "ds_read_b64_tr_b16 v[214:215], v201\n"
        "ds_read_b64_tr_b16 v[216:217], v201 offset:3072\n"
        "v_mfma_f32_16x16x32_bf16 v[110:113], v[162:165], a[216:219], v[110:113]\n"
        "ds_read_b64_tr_b16 v[218:219], v201 offset:6144\n"
        "ds_read_b64_tr_b16 v[220:221], v201 offset:9216\n"
        "v_mfma_f32_16x16x32_bf16 v[222:225], v[162:165], a[228:231], 0\n"
        "ds_read_b64_tr_b16 v[226:227], v201 offset:12288\n"
        "ds_read_b64_tr_b16 v[228:229], v201 offset:15360\n"
        "v_mfma_f32_16x16x32_bf16 v[222:225], v[166:169], a[232:235], v[222:225]\n"
        "ds_write_b64 v204, v[76:77]\n"
        "v_mfma_f32_16x16x32_bf16 v[222:225], v[174:177], a[236:239], v[222:225]\n"
        "v_cndmask_b32_e64 v134, v186, v187, s[56:57]\n"
        "v_cndmask_b32_e64 v134, v134, v188, s[58:59]\n"
        "v_mfma_f32_16x16x32_bf16 v[230:233], v[162:165], v[94:97], 0\n"
        "v_cndmask_b32_e64 v134, v134, v189, s[60:61]\n"
        "v_mul_f32 v134, v134, v143\n"
        "v_mfma_f32_16x16x32_bf16 v[186:189], v[162:165], v[98:101], 0\n"
        "v_mul_f32 v134, v134, v143\n"
        "v_cvt_pk_bf16_f32 v72, v190, v191\n"
        "v_mfma_f32_16x16x32_bf16 v[234:237], v[136:139], a[80:83], 0\n"
        "v_exp_f32 v134, v134\n"
        "v_mfma_f32_16x16x32_bf16 v[234:237], v[130:133], a[76:79], v[234:237]\n"
        "ds_write_b64 v205, v[134:135]\n"
        "v_mfma_f32_16x16x32_bf16 v[234:237], v[124:127], a[72:75], v[234:237]\n"
        "v_cvt_pk_bf16_f32 v73, v192, v193\n"
        "v_cvt_pk_bf16_f32 v76, v194, v195\n"
        "v_mfma_f32_16x16x32_bf16 v[190:193], v[124:127], a[84:87], 0\n"
        "s_waitcnt lgkmcnt(2)\n"
        "v_cvt_pk_bf16_f32 v77, v196, v197\n"
        "v_cvt_pk_bf16_f32 v107, v210, v211\n"
        "v_mfma_f32_16x16x32_bf16 v[190:193], v[130:133], a[88:91], v[190:193]\n"
        "v_cvt_pk_bf16_f32 v209, v212, v213\n"
        "v_cvt_pk_bf16_f32 v104, v64, v65\n"
        "v_mfma_f32_16x16x32_bf16 v[190:193], v[136:139], a[92:95], v[190:193]\n"
        "v_cvt_pk_bf16_f32 v108, v66, v67\n"
        "v_mul_f32 v110, v110, v161\n"
        "v_mfma_f32_16x16x32_bf16 v[194:197], v[72:75], v[114:117], 0\n"
        "v_mul_f32 v111, v111, v244\n"
        "v_mul_f32 v112, v112, v245\n"
        "v_mfma_f32_16x16x32_bf16 v[70:73], v[74:77], v[118:121], 0\n"
        "v_mul_f32 v113, v113, v246\n"
        "v_mul_f32 v222, v222, v161\n"
        "v_mfma_f32_16x16x32_bf16 v[114:117], v[226:229], v[174:177], 0\n"
        "v_mul_f32 v223, v223, v244\n"
        "v_mul_f32 v224, v224, v245\n"
        "v_mfma_f32_16x16x32_bf16 v[114:117], v[218:221], v[166:169], v[114:117]\n"
        "v_mul_f32 v225, v225, v246\n"
        "v_cvt_pk_f16_f32 v110, v110, v222\n"
        "v_mfma_f32_16x16x32_bf16 v[114:117], v[214:217], v[162:165], v[114:117]\n"
        "v_cvt_pk_f16_f32 v111, v111, v223\n"
        "v_cvt_pk_f16_f32 v112, v112, v224\n"
        "v_mfma_f32_16x16x32_bf16 v[118:121], v[214:217], v[170:173], 0\n"
        "v_cvt_pk_f16_f32 v113, v113, v225\n"
        "v_perm_b32 v60, v110, v230, s54\n"
        "v_mfma_f32_16x16x32_bf16 v[118:121], v[218:221], v[178:181], v[118:121]\n"
        "v_perm_b32 v64, v111, v231, s54\n"
        "v_perm_b32 v66, v112, v232, s54\n"
        "v_mfma_f32_16x16x32_bf16 v[118:121], v[226:229], v[182:185], v[118:121]\n"
        "v_perm_b32 v76, v113, v233, s54\n"
        "v_perm_b32 v61, v110, v186, s55\n"
        "v_mfma_f32_16x16x32_bf16 v[212:215], v[136:139], a[104:107], 0\n"
        "ds_write_b64 v206, v[60:61]\n"
        "v_mfma_f32_16x16x32_bf16 v[212:215], v[130:133], a[100:103], v[212:215]\n"
        "v_perm_b32 v65, v111, v187, s55\n"
        "v_perm_b32 v67, v112, v188, s55\n"
        "v_mfma_f32_16x16x32_bf16 v[212:215], v[124:127], a[96:99], v[212:215]\n"
        "ds_write_b64 v206, v[64:65] offset:3136\n"
        "v_mfma_f32_16x16x32_bf16 v[216:219], v[124:127], a[108:111], 0\n"
        "ds_write_b64 v206, v[66:67] offset:6272\n"
        "v_mfma_f32_16x16x32_bf16 v[216:219], v[130:133], a[112:115], v[216:219]\n"
        "v_perm_b32 v77, v113, v189, s55\n"
        "v_cvt_pk_bf16_f32 v105, v196, v197\n"
        "v_mfma_f32_16x16x32_bf16 v[216:219], v[136:139], a[116:119], v[216:219]\n"
        "ds_write_b128 v12, v[102:105]\n"
        "v_cvt_pk_bf16_f32 v60, v234, v235\n"
        "v_cvt_pk_bf16_f32 v61, v236, v237\n"
        "s_nop 1\n"
        "v_mfma_f32_16x16x32_bf16 v[110:113], v[60:63], v[122:125], 0\n"
        "ds_write_b64 v206, v[76:77] offset:9408\n"
        "v_cvt_pk_bf16_f32 v64, v190, v191\n"
        "v_cvt_pk_bf16_f32 v65, v192, v193\n"
        "s_nop 1\n"
        "v_mfma_f32_16x16x32_bf16 v[186:189], v[62:65], v[126:129], 0\n"
        "v_cvt_pk_bf16_f32 v109, v70, v71\n"
        "v_cvt_pk_bf16_f32 v210, v194, v195\n"
        "v_mfma_f32_16x16x32_bf16 v[190:193], v[174:177], a[248:251], 0\n"
        "ds_write_b128 v12, v[106:109] offset:1024\n"
        "v_mfma_f32_16x16x32_bf16 v[190:193], v[166:169], a[244:247], v[190:193]\n"
        "v_cvt_pk_bf16_f32 v211, v72, v73\n"
        "v_cvt_f32_f16_sdwa v204, v142 dst_sel:DWORD dst_unused:UNUSED_PAD src0_sel:WORD_0\n"
        "v_mfma_f32_16x16x32_bf16 v[190:193], v[162:165], a[240:243], v[190:193]\n"
        "ds_write_b128 v12, v[208:211] offset:2048\n"
        "v_mfma_f32_16x16x32_bf16 v[194:197], v[162:165], a[252:255], 0\n"
        "v_cvt_f32_f16_sdwa v205, v142 dst_sel:DWORD dst_unused:UNUSED_PAD src0_sel:WORD_1\n"
        "v_mul_f32_dpp v247, v199, v148 quad_perm:[0,0,0,0] row_mask:0xf bank_mask:0xf\n"
        "v_mfma_f32_16x16x32_bf16 v[194:197], v[166:169], v[28:31], v[194:197]\n"
        "ds_read_b64_tr_b16 v[220:221], v201 offset:8\n"
        "ds_read_b64_tr_b16 v[222:223], v201 offset:3080\n"
        "v_mfma_f32_16x16x32_bf16 v[194:197], v[174:177], v[32:35], v[194:197]\n"
        "ds_read_b64_tr_b16 v[224:225], v201 offset:6152\n"
        "ds_read_b64_tr_b16 v[226:227], v201 offset:9224\n"
        "v_mfma_f32_16x16x32_bf16 v[228:231], v[166:169], v[94:97], 0\n"
        "ds_read_b64_tr_b16 v[232:233], v201 offset:12296\n"
        "ds_read_b64_tr_b16 v[234:235], v201 offset:15368\n"
        "v_mfma_f32_16x16x32_bf16 v[236:239], v[166:169], v[98:101], 0\n"
        "v_and_b32_dpp v142, v140, v25 row_ror:8 row_mask:0xf bank_mask:0xf\n"
        "v_and_b32_dpp v143, v141, v25 row_ror:8 row_mask:0xf bank_mask:0xf\n"
        "v_mfma_f32_16x16x32_bf16 v[240:243], v[102:105], v[102:105], 0\n"
        "v_mov_b64 v[134:135], v[122:123]\n"
        "v_mov_b64 v[140:141], v[122:123]\n"
        "v_mfma_f32_16x16x32_bf16 v[240:243], v[106:109], v[106:109], v[240:243]\n"
        "v_mov_b64 v[148:149], v[142:143]\n"
        "v_fmac_f32_dpp v247, v199, v154 quad_perm:[1,1,1,1] row_mask:0xf bank_mask:0xf\n"
        "v_mfma_f32_16x16x32_bf16 v[240:243], v[208:211], v[208:211], v[240:243]\n"
        "v_mov_b64 v[154:155], v[142:143]\n"
        "v_fmac_f32_dpp v247, v199, v160 quad_perm:[2,2,2,2] row_mask:0xf bank_mask:0xf\n"
        "v_mfma_f32_16x16x32_bf16 v[102:105], v[136:139], a[128:131], 0\n"
        "v_cvt_pk_bf16_f32 v66, v212, v213\n"
        "v_cvt_pk_bf16_f32 v67, v214, v215\n"
        "v_mfma_f32_16x16x32_bf16 v[102:105], v[130:133], a[124:127], v[102:105]\n"
        "v_cvt_pk_bf16_f32 v70, v216, v217\n"
        "v_cvt_pk_bf16_f32 v71, v218, v219\n"
        "v_mfma_f32_16x16x32_bf16 v[102:105], v[124:127], a[120:123], v[102:105]\n"
        "v_mul_f32 v190, v190, v161\n"
        "v_mul_f32 v191, v191, v244\n"
        "v_mfma_f32_16x16x32_bf16 v[106:109], v[124:127], a[132:135], 0\n"
        "s_waitcnt lgkmcnt(0)\n"
        "v_mul_f32 v192, v192, v245\n"
        "v_mul_f32 v193, v193, v246\n"
        "v_mfma_f32_16x16x32_bf16 v[106:109], v[130:133], a[136:139], v[106:109]\n"
        "v_mul_f32 v194, v194, v161\n"
        "v_mul_f32 v195, v195, v244\n"
        "v_mfma_f32_16x16x32_bf16 v[106:109], v[136:139], a[140:143], v[106:109]\n"
        "v_mul_f32 v196, v196, v245\n"
        "v_mul_f32 v197, v197, v246\n"
        "v_mfma_f32_16x16x32_bf16 v[122:125], v[66:69], v[128:131], 0\n"
        "v_cvt_pk_f16_f32 v190, v190, v194\n"
        "v_cvt_pk_f16_f32 v191, v191, v195\n"
        "v_mfma_f32_16x16x32_bf16 v[64:67], v[68:71], v[132:135], 0\n"
        "v_cvt_pk_f16_f32 v192, v192, v196\n"
        "v_cvt_pk_f16_f32 v193, v193, v197\n"
        "v_mfma_f32_16x16x32_bf16 v[126:129], v[232:235], v[174:177], 0\n"
        "v_perm_b32 v60, v190, v228, s54\n"
        "v_perm_b32 v61, v190, v236, s55\n"
        "v_mfma_f32_16x16x32_bf16 v[126:129], v[224:227], v[166:169], v[126:129]\n"
        "ds_write_b64 v206, v[60:61] offset:8\n"
        "v_mfma_f32_16x16x32_bf16 v[126:129], v[220:223], v[162:165], v[126:129]\n"
        "v_perm_b32 v60, v191, v229, s54\n"
        "v_perm_b32 v61, v191, v237, s55\n"
        "v_mfma_f32_16x16x32_bf16 v[130:133], v[220:223], v[170:173], 0\n"
        "ds_write_b64 v206, v[60:61] offset:3144\n"
        "v_mfma_f32_16x16x32_bf16 v[130:133], v[224:227], v[178:181], v[130:133]\n"
        "v_perm_b32 v60, v192, v230, s54\n"
        "v_perm_b32 v61, v192, v238, s55\n"
        "v_mfma_f32_16x16x32_bf16 v[130:133], v[232:235], v[182:185], v[130:133]\n"
        "ds_write_b64 v206, v[60:61] offset:6280\n"
        "v_mfma_f32_16x16x32_bf16 v[194:197], v[156:159], a[80:83], 0\n"
        "v_perm_b32 v60, v193, v231, s54\n"
        "v_perm_b32 v61, v193, v239, s55\n"
        "v_mfma_f32_16x16x32_bf16 v[194:197], v[150:153], a[76:79], v[194:197]\n"
        "ds_write_b64 v206, v[60:61] offset:9416\n"
        "v_mfma_f32_16x16x32_bf16 v[194:197], v[144:147], a[72:75], v[194:197]\n"
        "v_cvt_pk_bf16_f32 v70, v114, v115\n"
        "v_cvt_pk_bf16_f32 v71, v116, v117\n"
        "v_mfma_f32_16x16x32_bf16 v[114:117], v[144:147], a[84:87], 0\n"
        "v_cvt_pk_bf16_f32 v190, v118, v119\n"
        "v_cvt_pk_bf16_f32 v191, v120, v121\n"
        "v_mfma_f32_16x16x32_bf16 v[114:117], v[150:153], a[88:91], v[114:117]\n"
        "v_cvt_pk_bf16_f32 v72, v102, v103\n"
        "v_cvt_pk_bf16_f32 v73, v104, v105\n"
        "v_mfma_f32_16x16x32_bf16 v[114:117], v[156:159], a[92:95], v[114:117]\n"
        "v_cvt_pk_bf16_f32 v76, v106, v107\n"
        "v_cvt_pk_bf16_f32 v77, v108, v109\n"
        "v_mfma_f32_16x16x32_bf16 v[102:105], v[72:75], v[134:137], 0\n"
        "v_cvt_pk_bf16_f32 v72, v126, v127\n"
        "v_cvt_pk_bf16_f32 v73, v128, v129\n"
        "v_mfma_f32_16x16x32_bf16 v[106:109], v[74:77], v[138:141], 0\n"
        "ds_write_b128 v202, v[70:73] offset:192\n"
        "v_mfma_f32_16x16x32_bf16 v[70:73], v[174:177], v[44:47], 0\n"
        "v_cvt_pk_bf16_f32 v118, v110, v111\n"
        "v_cvt_pk_bf16_f32 v110, v112, v113\n"
        "v_mfma_f32_16x16x32_bf16 v[70:73], v[166:169], v[40:43], v[70:73]\n"
        "ds_read_b64_tr_b16 v[126:127], v201 offset:1024\n"
        "ds_read_b64_tr_b16 v[128:129], v201 offset:4096\n"
        "v_mfma_f32_16x16x32_bf16 v[70:73], v[162:165], v[36:39], v[70:73]\n"
        "ds_read_b64_tr_b16 v[134:135], v201 offset:7168\n"
        "ds_read_b64_tr_b16 v[136:137], v201 offset:10240\n"
        "v_mfma_f32_16x16x32_bf16 v[138:141], v[162:165], v[48:51], 0\n"
        "ds_read_b64_tr_b16 v[208:209], v201 offset:13312\n"
        "ds_read_b64_tr_b16 v[210:211], v201 offset:16384\n"
        "v_mfma_f32_16x16x32_bf16 v[138:141], v[166:169], v[52:55], v[138:141]\n"
        "v_cvt_pk_bf16_f32 v192, v130, v131\n"
        "v_cvt_pk_bf16_f32 v193, v132, v133\n"
        "v_mfma_f32_16x16x32_bf16 v[138:141], v[174:177], v[56:59], v[138:141]\n"
        "ds_write_b128 v203, v[190:193] offset:192\n"
        "v_mfma_f32_16x16x32_bf16 v[130:133], v[174:177], v[94:97], 0\n"
        "v_cvt_pk_bf16_f32 v190, v186, v187\n"
        "v_cvt_pk_bf16_f32 v119, v188, v189\n"
        "v_mfma_f32_16x16x32_bf16 v[186:189], v[174:177], v[98:101], 0\n"
        "v_cvt_pk_bf16_f32 v111, v122, v123\n"
        "v_cvt_pk_bf16_f32 v191, v124, v125\n"
        "v_mfma_f32_16x16x32_bf16 v[122:125], v[156:159], a[104:107], 0\n"
        "v_cvt_pk_bf16_f32 v120, v64, v65\n"
        "v_cvt_pk_bf16_f32 v121, v104, v105\n"
        "v_mfma_f32_16x16x32_bf16 v[122:125], v[150:153], a[100:103], v[122:125]\n"
        "ds_write_b128 v12, v[118:121] offset:3072\n"
        "v_mfma_f32_16x16x32_bf16 v[122:125], v[144:147], a[96:99], v[122:125]\n"
        "s_waitcnt lgkmcnt(2)\n"
        "v_cvt_pk_bf16_f32 v112, v66, v67\n"
        "v_cvt_pk_bf16_f32 v60, v194, v195\n"
        "v_mfma_f32_16x16x32_bf16 v[212:215], v[144:147], a[108:111], 0\n"
        "v_cvt_pk_bf16_f32 v61, v196, v197\n"
        "v_cvt_pk_bf16_f32 v113, v106, v107\n"
        "v_mfma_f32_16x16x32_bf16 v[212:215], v[150:153], a[112:115], v[212:215]\n"
        "ds_write_b128 v12, v[110:113] offset:4096\n"
        "v_mfma_f32_16x16x32_bf16 v[212:215], v[156:159], a[116:119], v[212:215]\n"
        "v_cvt_pk_bf16_f32 v64, v114, v115\n"
        "v_cvt_pk_bf16_f32 v65, v116, v117\n"
        "v_mfma_f32_16x16x32_bf16 v[104:107], v[60:63], v[142:145], 0\n"
        "v_cvt_pk_bf16_f32 v192, v102, v103\n"
        "v_cvt_pk_bf16_f32 v193, v108, v109\n"
        "v_mfma_f32_16x16x32_bf16 v[114:117], v[62:65], v[146:149], 0\n"
        "ds_write_b128 v12, v[190:193] offset:5120\n"
        "v_mfma_f32_16x16x32_bf16 v[194:197], v[208:211], v[174:177], 0\n"
        "v_mul_f32 v70, v70, v161\n"
        "v_mul_f32 v71, v71, v244\n"
        "v_mfma_f32_16x16x32_bf16 v[194:197], v[134:137], v[166:169], v[194:197]\n"
        "v_mul_f32 v72, v72, v245\n"
        "v_mul_f32 v73, v73, v246\n"
        "v_mfma_f32_16x16x32_bf16 v[194:197], v[126:129], v[162:165], v[194:197]\n"
        "v_mul_f32 v138, v138, v161\n"
        "v_mul_f32 v139, v139, v244\n"
        "v_mfma_f32_16x16x32_bf16 v[216:219], v[126:129], v[170:173], 0\n"
        "v_mul_f32 v140, v140, v245\n"
        "v_mul_f32 v141, v141, v246\n"
        "v_mfma_f32_16x16x32_bf16 v[216:219], v[134:137], v[178:181], v[216:219]\n"
        "v_cvt_pk_f16_f32 v70, v70, v138\n"
        "v_cvt_pk_f16_f32 v71, v71, v139\n"
        "v_mfma_f32_16x16x32_bf16 v[216:219], v[208:211], v[182:185], v[216:219]\n"
        "v_cvt_pk_f16_f32 v72, v72, v140\n"
        "v_cvt_pk_f16_f32 v73, v73, v141\n"
        "v_mfma_f32_16x16x32_bf16 v[126:129], v[118:121], v[118:121], 0\n"
        "v_perm_b32 v60, v70, v130, s54\n"
        "v_perm_b32 v61, v70, v186, s55\n"
        "v_mfma_f32_16x16x32_bf16 v[126:129], v[110:113], v[110:113], v[126:129]\n"
        "ds_write_b64 v206, v[60:61] offset:16\n"
        "v_mfma_f32_16x16x32_bf16 v[126:129], v[190:193], v[190:193], v[126:129]\n"
        "v_perm_b32 v60, v71, v131, s54\n"
        "v_perm_b32 v61, v71, v187, s55\n"
        "v_mfma_f32_16x16x32_bf16 v[108:111], v[156:159], a[128:131], 0\n"
        "ds_write_b64 v206, v[60:61] offset:3152\n"
        "v_mfma_f32_16x16x32_bf16 v[108:111], v[150:153], a[124:127], v[108:111]\n"
        "ds_read_b64_tr_b16 v[118:119], v201 offset:1032\n"
        "ds_read_b64_tr_b16 v[120:121], v201 offset:4104\n"
        "v_mfma_f32_16x16x32_bf16 v[108:111], v[144:147], a[120:123], v[108:111]\n"
        "ds_read_b64_tr_b16 v[134:135], v201 offset:7176\n"
        "ds_read_b64_tr_b16 v[136:137], v201 offset:10248\n"
        "v_mfma_f32_16x16x32_bf16 v[138:141], v[144:147], a[132:135], 0\n"
        "ds_read_b64_tr_b16 v[144:145], v201 offset:13320\n"
        "ds_read_b64_tr_b16 v[146:147], v201 offset:16392\n"
        "v_mfma_f32_16x16x32_bf16 v[138:141], v[150:153], a[136:139], v[138:141]\n"
        "v_perm_b32 v60, v72, v132, s54\n"
        "v_perm_b32 v61, v72, v188, s55\n"
        "v_mfma_f32_16x16x32_bf16 v[138:141], v[156:159], a[140:143], v[138:141]\n"
        "ds_write_b64 v206, v[60:61] offset:6288\n"
        "v_cvt_pk_bf16_f32 v66, v122, v123\n"
        "v_cvt_pk_bf16_f32 v67, v124, v125\n"
        "s_nop 1\n"
        "v_mfma_f32_16x16x32_bf16 v[122:125], v[66:69], v[148:151], 0\n"
        "v_perm_b32 v60, v73, v133, s54\n"
        "v_perm_b32 v61, v73, v189, s55\n"
        "v_cvt_pk_bf16_f32 v70, v212, v213\n"
        "v_cvt_pk_bf16_f32 v71, v214, v215\n"
        "s_nop 1\n"
        "v_mfma_f32_16x16x32_bf16 v[64:67], v[68:71], v[152:155], 0\n"
        "ds_write_b64 v206, v[60:61] offset:9424\n"
        "v_mfma_f32_16x16x32_bf16 v[70:73], v[182:185], a[224:227], 0\n"
        "v_mov_b64 v[160:161], v[142:143]\n"
        "v_fmac_f32_dpp v247, v199, v200 quad_perm:[3,3,3,3] row_mask:0xf bank_mask:0xf\n"
        "v_mfma_f32_16x16x32_bf16 v[70:73], v[178:181], a[220:223], v[70:73]\n"
        "s_waitcnt lgkmcnt(2)\n"
        "v_add_f32 v247, v247, v198\n"
        "v_mul_f32 v208, s80, v247\n"
        "v_mfma_f32_16x16x32_bf16 v[70:73], v[170:173], a[216:219], v[70:73]\n"
        "v_cndmask_b32_e64 v209, v240, v241, s[56:57]\n"
        "v_cndmask_b32_e64 v209, v209, v242, s[58:59]\n"
        "v_mfma_f32_16x16x32_bf16 v[130:133], v[170:173], a[228:231], 0\n"
        "v_cndmask_b32_e64 v209, v209, v243, s[60:61]\n"
        "v_cvt_pk_bf16_f32 v102, v104, v105\n"
        "v_mfma_f32_16x16x32_bf16 v[130:133], v[178:181], a[232:235], v[130:133]\n"
        "v_cvt_pk_bf16_f32 v112, v106, v107\n"
        "v_cvt_pk_bf16_f32 v106, v114, v115\n"
        "v_mfma_f32_16x16x32_bf16 v[130:133], v[182:185], a[236:239], v[130:133]\n"
        "v_cvt_pk_bf16_f32 v103, v116, v117\n"
        "v_cvt_pk_bf16_f32 v116, v194, v195\n"
        "v_mfma_f32_16x16x32_bf16 v[148:151], v[170:173], v[94:97], 0\n"
        "v_cvt_pk_bf16_f32 v117, v196, v197\n"
        "v_cvt_pk_bf16_f32 v142, v216, v217\n"
        "v_mfma_f32_16x16x32_bf16 v[186:189], v[170:173], v[98:101], 0\n"
        "v_cvt_pk_bf16_f32 v143, v218, v219\n"
        "v_cndmask_b32_e64 v210, v126, v127, s[56:57]\n"
        "v_mfma_f32_16x16x32_bf16 v[190:193], v[144:147], v[174:177], 0\n"
        "v_cndmask_b32_e64 v210, v210, v128, s[58:59]\n"
        "v_cndmask_b32_e64 v210, v210, v129, s[60:61]\n"
        "v_mfma_f32_16x16x32_bf16 v[190:193], v[134:137], v[166:169], v[190:193]\n"
        "v_cvt_pk_bf16_f32 v72, v108, v109\n"
        "v_cvt_pk_bf16_f32 v73, v110, v111\n"
        "v_mfma_f32_16x16x32_bf16 v[190:193], v[118:121], v[162:165], v[190:193]\n"
        "v_cvt_pk_bf16_f32 v76, v138, v139\n"
        "v_cvt_pk_bf16_f32 v77, v140, v141\n"
        "v_mfma_f32_16x16x32_bf16 v[126:129], v[118:121], v[170:173], 0\n"
        "v_cvt_pk_bf16_f32 v113, v122, v123\n"
        "v_cvt_pk_bf16_f32 v107, v124, v125\n"
        "v_mfma_f32_16x16x32_bf16 v[126:129], v[134:137], v[178:181], v[126:129]\n"
        "ds_read_b64_tr_b16 v[120:121], v201 offset:2048\n"
        "ds_read_b64_tr_b16 v[122:123], v201 offset:5120\n"
        "v_mfma_f32_16x16x32_bf16 v[126:129], v[144:147], v[182:185], v[126:129]\n"
        "ds_read_b64_tr_b16 v[132:133], v201 offset:8192\n"
        "ds_read_b64_tr_b16 v[134:135], v201 offset:11264\n"
        "v_mfma_f32_16x16x32_bf16 v[136:139], v[72:75], v[154:157], 0\n"
        "ds_read_b64_tr_b16 v[150:151], v201 offset:14336\n"
        "ds_read_b64_tr_b16 v[152:153], v201 offset:17408\n"
        "v_mfma_f32_16x16x32_bf16 v[108:111], v[74:77], v[158:161], 0\n"
        "v_cvt_pk_bf16_f32 v104, v64, v65\n"
        "v_cvt_pk_bf16_f32 v114, v66, v67\n"
        "v_mfma_f32_16x16x32_bf16 v[64:67], v[182:185], a[248:251], 0\n"
        "v_mul_f32 v70, v70, v204\n"
        "v_mul_f32 v71, v71, v205\n"
        "v_mfma_f32_16x16x32_bf16 v[64:67], v[178:181], a[244:247], v[64:67]\n"
        "v_mul_f32 v130, v130, v204\n"
        "v_mul_f32 v131, v131, v205\n"
        "v_mfma_f32_16x16x32_bf16 v[64:67], v[170:173], a[240:243], v[64:67]\n"
        "v_cvt_pk_f16_f32 v70, v70, v130\n"
        "v_cvt_pk_f16_f32 v71, v71, v131\n"
        "v_mfma_f32_16x16x32_bf16 v[154:157], v[170:173], a[252:255], 0\n"
        "v_perm_b32 v60, v70, v148, s54\n"
        "v_perm_b32 v76, v71, v149, s54\n"
        "v_mfma_f32_16x16x32_bf16 v[154:157], v[178:181], v[28:31], v[154:157]\n"
        "v_perm_b32 v61, v70, v186, s55\n"
        "v_perm_b32 v77, v71, v187, s55\n"
        "v_mfma_f32_16x16x32_bf16 v[154:157], v[182:185], v[32:35], v[154:157]\n"
        "ds_write_b64 v206, v[60:61] offset:12544\n"
        "v_mfma_f32_16x16x32_bf16 v[70:73], v[178:181], v[94:97], 0\n"
        "s_waitcnt lgkmcnt(1)\n"
        "ds_write_b64 v207, v[76:77]\n"
        "v_mfma_f32_16x16x32_bf16 v[146:149], v[178:181], v[98:101], 0\n"
        "v_cvt_pk_bf16_f32 v118, v190, v191\n"
        "v_cvt_pk_bf16_f32 v119, v192, v193\n"
        "v_cvt_pk_bf16_f32 v105, v138, v139\n"
        "s_nop 1\n"
        "v_mfma_f32_16x16x32_bf16 v[138:141], v[102:105], v[102:105], 0\n"
        "ds_write_b128 v202, v[116:119] offset:256\n"
        "v_cvt_pk_bf16_f32 v115, v108, v109\n"
        "s_nop 1\n"
        "v_mfma_f32_16x16x32_bf16 v[138:141], v[112:115], v[112:115], v[138:141]\n"
        "ds_write_b128 v12, v[102:105] offset:6144\n"
        "v_cvt_pk_bf16_f32 v108, v136, v137\n"
        "v_cvt_pk_bf16_f32 v109, v110, v111\n"
        "s_nop 1\n"
        "v_mfma_f32_16x16x32_bf16 v[138:141], v[106:109], v[106:109], v[138:141]\n"
        "ds_read_b64_tr_b16 v[156:157], v201 offset:2056\n"
        "ds_read_b64_tr_b16 v[158:159], v201 offset:5128\n"
        "v_mfma_f32_16x16x32_bf16 v[186:189], v[150:153], v[174:177], 0\n"
        "ds_read_b64_tr_b16 v[190:191], v201 offset:8200\n"
        "ds_read_b64_tr_b16 v[192:193], v201 offset:11272\n"
        "v_mfma_f32_16x16x32_bf16 v[186:189], v[132:135], v[166:169], v[186:189]\n"
        "ds_read_b64_tr_b16 v[194:195], v201 offset:14344\n"
        "ds_read_b64_tr_b16 v[196:197], v201 offset:17416\n"
        "v_mfma_f32_16x16x32_bf16 v[186:189], v[120:123], v[162:165], v[186:189]\n"
        "ds_write_b128 v12, v[112:115] offset:7168\n"
        "v_mfma_f32_16x16x32_bf16 v[198:201], v[120:123], v[170:173], 0\n"
        "ds_write_b128 v12, v[106:109] offset:8192\n"
        "v_mfma_f32_16x16x32_bf16 v[198:201], v[132:135], v[178:181], v[198:201]\n"
        "v_cvt_pk_bf16_f32 v144, v126, v127\n"
        "v_cvt_pk_bf16_f32 v145, v128, v129\n"
        "v_mfma_f32_16x16x32_bf16 v[198:201], v[150:153], v[182:185], v[198:201]\n"
        "ds_write_b128 v203, v[142:145] offset:256\n"
        "v_mfma_f32_16x16x32_bf16 v[102:105], v[182:185], v[44:47], 0\n"
        "s_waitcnt lgkmcnt(3)\n"
        "v_mul_f32 v64, v64, v204\n"
        "v_mul_f32 v65, v65, v205\n"
        "v_mfma_f32_16x16x32_bf16 v[102:105], v[178:181], v[40:43], v[102:105]\n"
        "v_mul_f32 v154, v154, v204\n"
        "v_mul_f32 v155, v155, v205\n"
        "v_mfma_f32_16x16x32_bf16 v[102:105], v[170:173], v[36:39], v[102:105]\n"
        "v_cvt_pk_f16_f32 v64, v64, v154\n"
        "v_cvt_pk_f16_f32 v65, v65, v155\n"
        "v_mfma_f32_16x16x32_bf16 v[114:117], v[170:173], v[48:51], 0\n"
        "s_waitcnt vmcnt(0)\n"
        "v_perm_b32 v60, v64, v70, s54\n"
        "v_perm_b32 v76, v65, v71, s54\n"
        "v_mfma_f32_16x16x32_bf16 v[114:117], v[178:181], v[52:55], v[114:117]\n"
        "s_nop 0\n"
        "ds_read_b128 v[104:107], v9\n"
        "v_perm_b32 v61, v64, v146, s55\n"
        "v_mfma_f32_16x16x32_bf16 v[114:117], v[182:185], v[56:59], v[114:117]\n"
        "ds_write_b64 v206, v[60:61] offset:12552\n"
        "v_mfma_f32_16x16x32_bf16 v[70:73], v[182:185], v[94:97], 0\n"
        "ds_read_b128 v[110:113], v10\n"
        "v_perm_b32 v77, v65, v147, s55\n"
        "v_mfma_f32_16x16x32_bf16 v[64:67], v[182:185], v[98:101], 0\n"
        "ds_write_b64 v207, v[76:77] offset:8\n"
        "v_mfma_f32_16x16x32_bf16 v[120:123], v[194:197], v[174:177], 0\n"
        "s_nop 0\n"
        "ds_read_b128 v[116:119], v11\n"
        "v_cndmask_b32_e64 v60, v138, v139, s[56:57]\n"
        "v_mfma_f32_16x16x32_bf16 v[120:123], v[190:193], v[166:169], v[120:123]\n"
        "ds_read_b128 v[124:127], v9 offset:3072\n"
        "v_cndmask_b32_e64 v60, v60, v140, s[58:59]\n"
        "v_mfma_f32_16x16x32_bf16 v[120:123], v[156:159], v[162:165], v[120:123]\n"
        "ds_read_b128 v[130:133], v10 offset:3072\n"
        "v_cndmask_b32_e64 v60, v60, v141, s[60:61]\n"
        "v_mfma_f32_16x16x32_bf16 v[140:143], v[156:159], v[170:173], 0\n"
        "ds_read_b128 v[136:139], v11 offset:3072\n"
        "v_cndmask_b32_dpp v210, v60, v210, vcc row_ror:4 row_mask:0xf bank_mask:0xf\n"
        "v_mfma_f32_16x16x32_bf16 v[140:143], v[190:193], v[178:181], v[140:143]\n"
        "ds_read_b128 v[144:147], v9 offset:6144\n"
        "v_cndmask_b32_dpp v209, v210, v209, vcc row_ror:4 row_mask:0xf bank_mask:0xf\n"
        "v_mfma_f32_16x16x32_bf16 v[140:143], v[194:197], v[182:185], v[140:143]\n"
        "ds_read_b128 v[150:153], v10 offset:6144\n"
        "v_mul_f32 v209, s80, v209\n"
        "v_mov_b32 v60, s29\n"
        "v_fma_f32 v208, -v208, v208, v209\n"
        "v_max_f32 v208, 0, v208\n"
        "v_sub_f32 v209, 0x44800000, v209\n"
        "v_add_f32 v208, s81, v208\n"
        "v_cvt_pk_bf16_f32 v160, v186, v187\n"
        "v_cvt_pk_bf16_f32 v161, v188, v189\n"
        "v_bfi_b32 v208, s84, v209, v208\n"
        "v_rsq_f32 v61, v208\n"
        "v_cvt_pk_bf16_f32 v164, v198, v199\n"
        "v_cvt_f16_f32 v61, v61\n"
        "ds_write_b16 v18, v61\n"
        "ds_write_b16 v19, v61\n"
        "ds_write_b16 v22, v60 offset:4096\n"
        "v_cvt_pk_bf16_f32 v165, v200, v201\n"
        "v_mul_f32 v102, v102, v204\n"
        "v_mul_f32 v103, v103, v205\n"
        "ds_read_b128 v[156:159], v11 offset:6144\n"
        "v_mul_f32 v114, v114, v204\n"
        "v_mul_f32 v115, v115, v205\n"
        "v_cvt_pk_f16_f32 v102, v102, v114\n"
        "v_cvt_pk_f16_f32 v103, v103, v115\n"
        "v_perm_b32 v60, v102, v70, s54\n"
        "v_perm_b32 v66, v103, v71, s54\n"
        "v_perm_b32 v61, v102, v64, s55\n"
        "v_perm_b32 v67, v103, v65, s55\n"
        "ds_write_b64 v206, v[60:61] offset:12560\n"
        "ds_write_b64 v207, v[66:67] offset:16\n"
        "v_cvt_pk_bf16_f32 v162, v120, v121\n"
        "v_cvt_pk_bf16_f32 v163, v122, v123\n"
        "ds_write_b128 v202, v[160:163] offset:320\n"
        "v_cvt_pk_bf16_f32 v166, v140, v141\n"
        "v_cvt_pk_bf16_f32 v167, v142, v143\n"
        "ds_write_b128 v203, v[164:167] offset:320\n"
        "s_waitcnt lgkmcnt(0)\n"
        "s_nop 7\n"
        ".p2align 6\n"
        "TOP_%=:\n"
        "s_waitcnt lgkmcnt(6)\n"
        "v_mfma_f32_16x16x32_bf16 v[64:67], v[104:107], v[104:107], 0\n"
        "s_mov_b32 s26, s89\n"
        "v_add_u32 v23, 784, v23\n"
        "v_add_u32 v24, 784, v24\n"
        "v_mfma_f32_16x16x32_bf16 v[64:67], v[110:113], v[110:113], v[64:67]\n"
        "s_add_u32 s89, s86, s51\n"
        "v_mfma_f32_16x16x32_bf16 v[64:67], v[116:119], v[116:119], v[64:67]\n"
        "s_mov_b32 s30, s29\n"
        "s_nop 0\n"
        "v_mfma_f32_16x16x32_bf16 v[70:73], v[78:81], v[116:119], 0\n"
        "s_add_u32 s29, s29, 1\n"
        "s_nop 1\n"
        "v_mfma_f32_16x16x32_bf16 v[70:73], v[82:85], v[110:113], v[70:73]\n"
        "s_mov_b32 s86, s87\n"
        "v_cndmask_b32_e64 v76, v64, v65, s[56:57]\n"
        "v_cndmask_b32_e64 v76, v76, v66, s[58:59]\n"
        "v_mfma_f32_16x16x32_bf16 v[70:73], v[86:89], v[104:107], v[70:73]\n"
        "s_add_u32 s88, s88, 32\n"
        "v_cndmask_b32_e64 v76, v76, v67, s[60:61]\n"
        "v_mfma_f32_16x16x32_bf16 v[64:67], a[0:3], v[104:107], 0\n"
        "s_add_u32 s87, s87, 43008\n"
        "v_mfma_f32_16x16x32_bf16 v[64:67], a[4:7], v[110:113], v[64:67]\n"
        "s_cmp_ge_u32 s88, 73\n"
        "v_mfma_f32_16x16x32_bf16 v[64:67], a[8:11], v[116:119], v[64:67]\n"
        "s_cselect_b32 s92, 588672, 0\n"
        "s_nop 0\n"
        "v_mfma_f32_16x16x32_bf16 v[120:123], a[20:23], v[116:119], 0\n"
        "s_cselect_b32 s94, 73, 0\n"
        "s_nop 1\n"
        "v_mfma_f32_16x16x32_bf16 v[120:123], a[16:19], v[110:113], v[120:123]\n"
        "s_sub_u32 s88, s88, s94\n"
        "v_cvt_pk_bf16_f32 v140, v64, v65\n"
        "v_cvt_pk_bf16_f32 v141, v66, v67\n"
        "v_mfma_f32_16x16x32_bf16 v[120:123], a[12:15], v[104:107], v[120:123]\n"
        "s_add_u32 s87, s87, s92\n"
        "s_nop 0\n"
        "v_mfma_f32_16x16x32_bf16 v[64:67], a[24:27], v[104:107], 0\n"
        "s_cmp_gt_i32 s85, 0\n"
        "s_nop 1\n"
        "v_mfma_f32_16x16x32_bf16 v[64:67], a[28:31], v[110:113], v[64:67]\n"
        "s_cselect_b32 s94, s87, s84\n"
        "v_cvt_pk_bf16_f32 v142, v120, v121\n"
        "v_cvt_pk_bf16_f32 v143, v122, v123\n"
        "v_mfma_f32_16x16x32_bf16 v[64:67], a[32:35], v[116:119], v[64:67]\n"
        "s_sub_u32 s85, s85, 1\n"
        "s_nop 0\n"
        "v_mfma_f32_16x16x32_bf16 v[120:123], a[44:47], v[116:119], 0\n"
        "s_waitcnt lgkmcnt(3)\n"
        "s_nop 1\n"
        "v_mfma_f32_16x16x32_bf16 v[120:123], a[40:43], v[110:113], v[120:123]\n"
        "s_waitcnt lgkmcnt(0)\n"
        "v_cvt_pk_bf16_f32 v160, v64, v65\n"
        "v_cvt_pk_bf16_f32 v161, v66, v67\n"
        "v_mfma_f32_16x16x32_bf16 v[120:123], a[36:39], v[104:107], v[120:123]\n"
        "s_nop 1\n"
        "v_mfma_f32_16x16x32_bf16 v[64:67], a[48:51], v[104:107], 0\n"
        "s_nop 2\n"
        "v_mfma_f32_16x16x32_bf16 v[64:67], a[52:55], v[110:113], v[64:67]\n"
        "s_nop 0\n"
        "v_cvt_pk_bf16_f32 v162, v120, v121\n"
        "v_cvt_pk_bf16_f32 v163, v122, v123\n"
        "v_mfma_f32_16x16x32_bf16 v[64:67], a[56:59], v[116:119], v[64:67]\n"
        "s_nop 1\n"
        "v_mfma_f32_16x16x32_bf16 v[120:123], a[68:71], v[116:119], 0\n"
        "s_nop 2\n"
        "v_mfma_f32_16x16x32_bf16 v[120:123], a[64:67], v[110:113], v[120:123]\n"
        "s_nop 0\n"
        "v_cvt_pk_bf16_f32 v164, v64, v65\n"
        "v_cvt_pk_bf16_f32 v165, v66, v67\n"
        "v_mfma_f32_16x16x32_bf16 v[120:123], a[60:63], v[104:107], v[120:123]\n"
        "s_nop 1\n"
        "v_mfma_f32_16x16x32_bf16 v[64:67], v[124:127], v[124:127], 0\n"
        "s_nop 2\n"
        "v_mfma_f32_16x16x32_bf16 v[64:67], v[130:133], v[130:133], v[64:67]\n"
        "s_nop 0\n"
        "v_cvt_pk_bf16_f32 v166, v120, v121\n"
        "v_cvt_pk_bf16_f32 v167, v122, v123\n"
        "v_mfma_f32_16x16x32_bf16 v[64:67], v[136:139], v[136:139], v[64:67]\n"
        "s_nop 1\n"
        "v_mfma_f32_16x16x32_bf16 v[120:123], v[78:81], v[136:139], 0\n"
        "s_nop 2\n"
        "v_mfma_f32_16x16x32_bf16 v[120:123], v[82:85], v[130:133], v[120:123]\n"
        "s_nop 0\n"
        "v_cndmask_b32_e64 v72, v64, v65, s[56:57]\n"
        "v_cndmask_b32_e64 v72, v72, v66, s[58:59]\n"
        "v_mfma_f32_16x16x32_bf16 v[120:123], v[86:89], v[124:127], v[120:123]\n"
        "v_cndmask_b32_e64 v72, v72, v67, s[60:61]\n"
        "v_mfma_f32_16x16x32_bf16 v[64:67], v[104:107], v[140:143], 0\n"
        "v_mfma_f32_16x16x32_bf16 v[64:67], v[110:113], v[160:163], v[64:67]\n"
        "v_mfma_f32_16x16x32_bf16 v[64:67], v[116:119], v[164:167], v[64:67]\n"
        "v_mfma_f32_16x16x32_bf16 v[140:143], a[8:11], v[136:139], 0\n"
        "v_mfma_f32_16x16x32_bf16 v[140:143], a[4:7], v[130:133], v[140:143]\n"
        "v_mfma_f32_16x16x32_bf16 v[140:143], a[0:3], v[124:127], v[140:143]\n"
        "s_nop 1\n"
        "v_mfma_f32_16x16x32_bf16 v[160:163], a[12:15], v[124:127], 0\n"
        "s_nop 2\n"
        "v_mfma_f32_16x16x32_bf16 v[160:163], a[16:19], v[130:133], v[160:163]\n"
        "s_nop 0\n"
        "v_cvt_pk_bf16_f32 v164, v140, v141\n"
        "v_cvt_pk_bf16_f32 v165, v142, v143\n"
        "v_mfma_f32_16x16x32_bf16 v[160:163], a[20:23], v[136:139], v[160:163]\n"
        "s_nop 1\n"
        "v_mfma_f32_16x16x32_bf16 v[140:143], a[32:35], v[136:139], 0\n"
        "s_nop 2\n"
        "v_mfma_f32_16x16x32_bf16 v[140:143], a[28:31], v[130:133], v[140:143]\n"
        "s_nop 0\n"
        "v_cvt_pk_bf16_f32 v166, v160, v161\n"
        "v_cvt_pk_bf16_f32 v167, v162, v163\n"
        "v_mfma_f32_16x16x32_bf16 v[140:143], a[24:27], v[124:127], v[140:143]\n"
        "s_nop 1\n"
        "v_mfma_f32_16x16x32_bf16 v[160:163], a[36:39], v[124:127], 0\n"
        "s_nop 2\n"
        "v_mfma_f32_16x16x32_bf16 v[160:163], a[40:43], v[130:133], v[160:163]\n"
        "s_nop 0\n"
        "v_cvt_pk_bf16_f32 v168, v140, v141\n"
        "v_cvt_pk_bf16_f32 v169, v142, v143\n"
        "v_mfma_f32_16x16x32_bf16 v[160:163], a[44:47], v[136:139], v[160:163]\n"
        "s_nop 1\n"
        "v_mfma_f32_16x16x32_bf16 v[140:143], a[56:59], v[136:139], 0\n"
        "s_nop 2\n"
        "v_mfma_f32_16x16x32_bf16 v[140:143], a[52:55], v[130:133], v[140:143]\n"
        "s_nop 0\n"
        "v_cvt_pk_bf16_f32 v170, v160, v161\n"
        "v_cvt_pk_bf16_f32 v171, v162, v163\n"
        "v_mfma_f32_16x16x32_bf16 v[140:143], a[48:51], v[124:127], v[140:143]\n"
        "s_nop 1\n"
        "v_mfma_f32_16x16x32_bf16 v[160:163], a[60:63], v[124:127], 0\n"
        "s_nop 0\n"
        "ds_read_u16 v73, v19 offset:3072\n"
        "s_nop 0\n"
        "v_mfma_f32_16x16x32_bf16 v[160:163], a[64:67], v[130:133], v[160:163]\n"
        "s_nop 0\n"
        "v_cvt_pk_bf16_f32 v172, v140, v141\n"
        "v_cvt_pk_bf16_f32 v173, v142, v143\n"
        "v_mfma_f32_16x16x32_bf16 v[160:163], a[68:71], v[136:139], v[160:163]\n"
        "s_nop 1\n"
        "v_mfma_f32_16x16x32_bf16 v[140:143], v[144:147], v[144:147], 0\n"
        "s_nop 2\n"
        "v_mfma_f32_16x16x32_bf16 v[140:143], v[150:153], v[150:153], v[140:143]\n"
        "s_nop 0\n"
        "v_cvt_pk_bf16_f32 v174, v160, v161\n"
        "v_cvt_pk_bf16_f32 v175, v162, v163\n"
        "v_mfma_f32_16x16x32_bf16 v[140:143], v[156:159], v[156:159], v[140:143]\n"
        "ds_read_b128 v[160:163], v23\n"
        "ds_read_b128 v[176:179], v23 offset:64\n"
        "v_mfma_f32_16x16x32_bf16 v[180:183], v[78:81], v[156:159], 0\n"
        "s_nop 0\n"
        "ds_read_b128 v[184:187], v23 offset:128\n"
        "ds_read_b64 v[60:61], v24 offset:360\n"
        "v_mfma_f32_16x16x32_bf16 v[180:183], v[82:85], v[150:153], v[180:183]\n"
        "s_nop 0\n"
        "v_cndmask_b32_e64 v77, v140, v141, s[56:57]\n"
        "v_cndmask_b32_e64 v77, v77, v142, s[58:59]\n"
        "v_mfma_f32_16x16x32_bf16 v[180:183], v[86:89], v[144:147], v[180:183]\n"
        "s_waitcnt lgkmcnt(4)\n"
        "v_cmp_ne_u32_e64 s[24:25], s30, v73\n"
        "v_cndmask_b32_e64 v77, v77, v143, s[60:61]\n"
        "v_mfma_f32_16x16x32_bf16 v[140:143], v[124:127], v[164:167], 0\n"
        "s_nop 0\n"
        "v_cndmask_b32_dpp v72, v77, v72, vcc row_ror:4 row_mask:0xf bank_mask:0xf\n"
        "v_mfma_f32_16x16x32_bf16 v[140:143], v[130:133], v[168:171], v[140:143]\n"
        "s_cmp_lg_u64 s[24:25], 0\n"
        "v_cndmask_b32_dpp v76, v72, v76, vcc row_ror:4 row_mask:0xf bank_mask:0xf\n"
        "v_cndmask_b32_dpp v244, v180, v120, vcc row_ror:4 row_mask:0xf bank_mask:0xf\n"
        "v_mfma_f32_16x16x32_bf16 v[140:143], v[136:139], v[172:175], v[140:143]\n"
        "s_cbranch_scc1 SP4_%=\n"
        "BK4_%=:\n"
        "v_mul_f32 v76, s80, v76\n"
        "v_cndmask_b32_dpp v244, v244, v70, vcc row_ror:4 row_mask:0xf bank_mask:0xf\n"
        "v_mfma_f32_16x16x32_bf16 v[164:167], a[8:11], v[156:159], 0\n"
        "v_mul_f32 v70, s80, v244\n"
        "v_fma_f32 v70, -v70, v70, v76\n"
        "v_mfma_f32_16x16x32_bf16 v[164:167], a[4:7], v[150:153], v[164:167]\n"
        "v_max_f32 v70, 0, v70\n"
        "v_add_f32 v70, s81, v70\n"
        "v_mfma_f32_16x16x32_bf16 v[164:167], a[0:3], v[144:147], v[164:167]\n"
        "s_add_u32 m0, s52, 0\n"
        "v_rsq_f32 v76, v70\n"
        "v_mfma_f32_16x16x32_bf16 v[168:171], a[12:15], v[144:147], 0\n"
        "s_waitcnt lgkmcnt(0)\n"
        "ds_read_b128 v[194:197], v13\n"
        "v_readlane_b32 s93, v61, 1\n"
        "v_mfma_f32_16x16x32_bf16 v[168:171], a[16:19], v[150:153], v[168:171]\n"
        "buffer_load_dwordx4 v0, s[36:39], s94 offen lds\n"
        "v_mov_b32 v61, s29\n"
        "v_mfma_f32_16x16x32_bf16 v[168:171], a[20:23], v[156:159], v[168:171]\n"
        "ds_read_b128 v[198:201], v13 offset:1024\n"
        "v_cndmask_b32_e64 v60, 0, v60, s[72:73]\n"
        "v_mfma_f32_16x16x32_bf16 v[172:175], a[32:35], v[156:159], 0\n"
        "ds_read_b128 v[202:205], v13 offset:2048\n"
        "ds_read_b128 v[206:209], v13 offset:3072\n"
        "v_mfma_f32_16x16x32_bf16 v[172:175], a[28:31], v[150:153], v[172:175]\n"
        "ds_read_b128 v[210:213], v13 offset:4096\n"
        "ds_read_b128 v[214:217], v13 offset:5120\n"
        "v_mfma_f32_16x16x32_bf16 v[172:175], a[24:27], v[144:147], v[172:175]\n"
        "ds_read_b128 v[218:221], v13 offset:6144\n"
        "ds_read_b128 v[222:225], v13 offset:7168\n"
        "v_mfma_f32_16x16x32_bf16 v[226:229], a[36:39], v[144:147], 0\n"
        "ds_read_b128 v[230:233], v13 offset:8192\n"
        "ds_read_b128 v[188:191], v20\n"
        "v_mfma_f32_16x16x32_bf16 v[226:229], a[40:43], v[150:153], v[226:229]\n"
        "buffer_load_dwordx4 v1, s[36:39], s94 offen offset:1024 lds\n"
        "v_cvt_pk_bf16_f32 v234, v164, v165\n"
        "v_mfma_f32_16x16x32_bf16 v[226:229], a[44:47], v[156:159], v[226:229]\n"
        "ds_read_b64 v[192:193], v20 offset:16\n"
        "ds_read_u16 v77, v22\n"
        "v_mfma_f32_16x16x32_bf16 v[238:241], a[56:59], v[156:159], 0\n"
        "ds_read_b64 v[242:243], v21\n"
        "v_cvt_pk_bf16_f32 v235, v166, v167\n"
        "v_mfma_f32_16x16x32_bf16 v[238:241], a[52:55], v[150:153], v[238:241]\n"
        "ds_write_b16 v22, v61 offset:4608\n"
        "v_mfma_f32_16x16x32_bf16 v[238:241], a[48:51], v[144:147], v[238:241]\n"
        "s_waitcnt lgkmcnt(11)\n"
        "v_cvt_pk_bf16_f32 v236, v168, v169\n"
        "v_cvt_pk_bf16_f32 v237, v170, v171\n"
        "v_mfma_f32_16x16x32_bf16 v[164:167], a[60:63], v[144:147], 0\n"
        "s_waitcnt lgkmcnt(8)\n"
        "v_cvt_pk_bf16_f32 v168, v172, v173\n"
        "v_cvt_pk_bf16_f32 v169, v174, v175\n"
        "v_mfma_f32_16x16x32_bf16 v[164:167], a[64:67], v[150:153], v[164:167]\n"
        "buffer_load_dwordx4 v2, s[36:39], s94 offen offset:2048 lds\n"
        "v_cvt_pk_bf16_f32 v170, v226, v227\n"
        "v_mfma_f32_16x16x32_bf16 v[164:167], a[68:71], v[156:159], v[164:167]\n"
        "v_cvt_pk_bf16_f32 v171, v228, v229\n"
        "v_cvt_pk_bf16_f32 v172, v238, v239\n"
        "v_mfma_f32_16x16x32_bf16 v[226:229], v[160:163], v[194:197], 0\n"
        "s_nop 0\n"
        "v_cvt_pk_bf16_f32 v173, v240, v241\n"
        "v_cndmask_b32_dpp v238, v181, v121, vcc row_ror:4 row_mask:0xf bank_mask:0xf\n"
        "v_mfma_f32_16x16x32_bf16 v[226:229], v[176:179], v[198:201], v[226:229]\n"
        "s_nop 0\n"
        "v_cvt_pk_bf16_f32 v174, v164, v165\n"
        "v_cvt_pk_bf16_f32 v175, v166, v167\n"
        "v_mfma_f32_16x16x32_bf16 v[226:229], v[184:187], v[202:205], v[226:229]\n"
        "s_waitcnt lgkmcnt(1)\n"
        "v_cndmask_b32_dpp v238, v238, v71, vcc row_ror:4 row_mask:0xf bank_mask:0xf\n"
        "v_mfma_f32_16x16x32_bf16 v[70:73], v[184:187], v[214:217], 0\n"
        "v_mfma_f32_16x16x32_bf16 v[70:73], v[176:179], v[210:213], v[70:73]\n"
        "buffer_load_dwordx4 v3, s[36:39], s94 offen offset:3072 lds\n"
        "v_mfma_f32_16x16x32_bf16 v[70:73], v[160:163], v[206:209], v[70:73]\n"
        "s_nop 1\n"
        "v_mfma_f32_16x16x32_bf16 v[120:123], v[160:163], v[218:221], 0\n"
        "s_nop 2\n"
        "v_mfma_f32_16x16x32_bf16 v[120:123], v[176:179], v[222:225], v[120:123]\n"
        "s_nop 0\n"
        "v_cndmask_b32_e64 v114, v226, v70, s[64:65]\n"
        "v_cndmask_b32_e64 v226, v227, v71, s[64:65]\n"
        "v_mfma_f32_16x16x32_bf16 v[120:123], v[184:187], v[230:233], v[120:123]\n"
        "s_add_u32 m0, s52, 4096\n"
        "v_mfma_f32_16x16x32_bf16 v[70:73], v[144:147], v[234:237], 0\n"
        "s_nop 2\n"
        "v_mfma_f32_16x16x32_bf16 v[70:73], v[150:153], v[168:171], v[70:73]\n"
        "s_nop 0\n"
        "buffer_load_dwordx4 v4, s[36:39], s94 offen lds\n"
        "v_cndmask_b32_e64 v114, v114, v120, s[70:71]\n"
        "v_mfma_f32_16x16x32_bf16 v[70:73], v[156:159], v[172:175], v[70:73]\n"
        "v_cndmask_b32_e64 v226, v226, v121, s[70:71]\n"
        "v_cvt_pk_f16_f32 v114, v114, v226\n"
        "v_mfma_f32_16x16x32_bf16 v[120:123], a[152:155], v[202:205], 0\n"
        "v_pk_mul_f16 v114, v114, v77 op_sel_hi:[1,0]\n"
        "v_pk_mul_f16 v114, v114, s93 op_sel_hi:[1,0]\n"
        "v_mfma_f32_16x16x32_bf16 v[120:123], a[148:151], v[198:201], v[120:123]\n"
        "v_exp_f16_sdwa v114, v114 dst_sel:WORD_0 dst_unused:UNUSED_PRESERVE src0_sel:WORD_0\n"
        "v_mfma_f32_16x16x32_bf16 v[120:123], a[144:147], v[194:197], v[120:123]\n"
        "v_exp_f16_sdwa v114, v114 dst_sel:WORD_1 dst_unused:UNUSED_PRESERVE src0_sel:WORD_1\n"
        "v_mfma_f32_16x16x32_bf16 v[160:163], a[156:159], v[194:197], 0\n"
        "v_cvt_f32_f16 v128, v114\n"
        "v_cndmask_b32_e64 v128, v128, v60, s[68:69]\n"
        "v_mfma_f32_16x16x32_bf16 v[160:163], a[160:163], v[198:201], v[160:163]\n"
        "buffer_load_dwordx4 v5, s[36:39], s94 offen offset:1024 lds\n"
        "v_add_f32_dpp v129, v128, v128 row_ror:8 row_mask:0xf bank_mask:0xf\n"
        "v_mfma_f32_16x16x32_bf16 v[160:163], a[164:167], v[202:205], v[160:163]\n"
        "v_cndmask_b32_dpp v134, v70, v140, vcc row_ror:4 row_mask:0xf bank_mask:0xf\n"
        "v_cndmask_b32_dpp v227, v71, v141, vcc row_ror:4 row_mask:0xf bank_mask:0xf\n"
        "v_mfma_f32_16x16x32_bf16 v[164:167], a[176:179], v[202:205], 0\n"
        "v_cndmask_b32_dpp v228, v72, v142, vcc row_ror:4 row_mask:0xf bank_mask:0xf\n"
        "v_cndmask_b32_dpp v229, v73, v143, vcc row_ror:4 row_mask:0xf bank_mask:0xf\n"
        "v_mfma_f32_16x16x32_bf16 v[164:167], a[172:175], v[198:201], v[164:167]\n"
        "v_cndmask_b32_dpp v134, v134, v64, vcc row_ror:4 row_mask:0xf bank_mask:0xf\n"
        "v_cndmask_b32_dpp v227, v227, v65, vcc row_ror:4 row_mask:0xf bank_mask:0xf\n"
        "v_mfma_f32_16x16x32_bf16 v[164:167], a[168:171], v[194:197], v[164:167]\n"
        "v_cndmask_b32_dpp v228, v228, v66, vcc row_ror:4 row_mask:0xf bank_mask:0xf\n"
        "v_cndmask_b32_dpp v229, v229, v67, vcc row_ror:4 row_mask:0xf bank_mask:0xf\n"
        "v_mfma_f32_16x16x32_bf16 v[64:67], a[180:183], v[194:197], 0\n"
        "v_mul_f32_dpp v134, v76, v134 quad_perm:[0,0,0,0] row_mask:0xf bank_mask:0xf\n"
        "v_mul_f32_dpp v227, v76, v227 quad_perm:[1,1,1,1] row_mask:0xf bank_mask:0xf\n"
        "v_mfma_f32_16x16x32_bf16 v[64:67], a[184:187], v[198:201], v[64:67]\n"
        "buffer_load_dwordx4 v6, s[36:39], s94 offen offset:2048 lds\n"
        "v_mul_f32_dpp v228, v76, v228 quad_perm:[2,2,2,2] row_mask:0xf bank_mask:0xf\n"
        "v_mfma_f32_16x16x32_bf16 v[64:67], a[188:191], v[202:205], v[64:67]\n"
        "v_mul_f32_dpp v229, v76, v229 quad_perm:[3,3,3,3] row_mask:0xf bank_mask:0xf\n"
        "v_max3_f32 v60, v134, v227, v228\n"
        "v_mfma_f32_16x16x32_bf16 v[70:73], a[200:203], v[202:205], 0\n"
        "v_max_f32 v60, v60, v229\n"
        "v_mul_f32 v60, v60, v76\n"
        "v_mfma_f32_16x16x32_bf16 v[70:73], a[196:199], v[198:201], v[70:73]\n"
        "v_fma_f32 v134, v134, v76, -v60\n"
        "v_fma_f32 v227, v227, v76, -v60\n"
        "v_mfma_f32_16x16x32_bf16 v[70:73], a[192:195], v[194:197], v[70:73]\n"
        "v_fma_f32 v228, v228, v76, -v60\n"
        "v_fma_f32 v229, v229, v76, -v60\n"
        "v_mfma_f32_16x16x32_bf16 v[140:143], a[204:207], v[194:197], 0\n"
        "v_exp_f32 v134, v134\n"
        "v_mfma_f32_16x16x32_bf16 v[140:143], a[208:211], v[198:201], v[140:143]\n"
        "buffer_load_dwordx4 v7, s[36:39], s94 offen offset:3072 lds\n"
        "v_cvt_pk_bf16_f32 v168, v120, v121\n"
        "v_mfma_f32_16x16x32_bf16 v[140:143], a[212:215], v[202:205], v[140:143]\n"
        "v_cvt_pk_bf16_f32 v169, v122, v123\n"
        "v_cvt_pk_bf16_f32 v170, v160, v161\n"
        "v_mfma_f32_16x16x32_bf16 v[120:123], v[116:119], a[80:83], 0\n"
        "v_cvt_pk_bf16_f32 v171, v162, v163\n"
        "v_cvt_pk_bf16_f32 v160, v164, v165\n"
        "v_mfma_f32_16x16x32_bf16 v[120:123], v[110:113], a[76:79], v[120:123]\n"
        "v_cvt_pk_bf16_f32 v161, v166, v167\n"
        "v_cvt_pk_bf16_f32 v162, v64, v65\n"
        "v_mfma_f32_16x16x32_bf16 v[120:123], v[104:107], a[72:75], v[120:123]\n"
        "s_add_u32 m0, s52, 8192\n"
        "v_cvt_pk_bf16_f32 v163, v66, v67\n"
        "v_cvt_pk_bf16_f32 v64, v70, v71\n"
        "v_mfma_f32_16x16x32_bf16 v[164:167], v[104:107], a[84:87], 0\n"
        "v_cvt_pk_bf16_f32 v65, v72, v73\n"
        "v_cvt_pk_bf16_f32 v66, v140, v141\n"
        "v_mfma_f32_16x16x32_bf16 v[164:167], v[110:113], a[88:91], v[164:167]\n"
        "buffer_load_dwordx4 v8, s[36:39], s94 offen lds\n"
        "v_cvt_pk_bf16_f32 v67, v142, v143\n"
        "v_mfma_f32_16x16x32_bf16 v[164:167], v[116:119], a[92:95], v[164:167]\n"
        "v_exp_f32 v227, v227\n"
        "v_mfma_f32_16x16x32_bf16 v[172:175], v[194:197], v[168:171], 0\n"
        "v_exp_f32 v228, v228\n"
        "v_mfma_f32_16x16x32_bf16 v[172:175], v[198:201], v[160:163], v[172:175]\n"
        "v_exp_f32 v229, v229\n"
        "v_mfma_f32_16x16x32_bf16 v[172:175], v[202:205], v[64:67], v[172:175]\n"
        "v_add_f32 v60, v134, v227\n"
        "v_add_f32 v61, v228, v229\n"
        "v_mfma_f32_16x16x32_bf16 v[176:179], v[214:217], v[64:67], 0\n"
        "v_add_f32 v60, v60, v61\n"
        "v_add_f32_dpp v129, v129, v129 row_ror:4 row_mask:0xf bank_mask:0xf\n"
        "v_mfma_f32_16x16x32_bf16 v[176:179], v[210:213], v[160:163], v[176:179]\n"
        "v_rcp_f32 v60, v60\n"
        "v_mfma_f32_16x16x32_bf16 v[176:179], v[206:209], v[168:171], v[176:179]\n"
        "v_mul_f32 v134, v134, v60\n"
        "v_mul_f32 v227, v227, v60\n"
        "v_mfma_f32_16x16x32_bf16 v[180:183], v[218:221], v[168:171], 0\n"
        "v_mul_f32 v228, v228, v60\n"
        "v_mul_f32 v229, v229, v60\n"
        "v_mfma_f32_16x16x32_bf16 v[180:183], v[222:225], v[160:163], v[180:183]\n"
        "v_mul_f32_dpp v134, v76, v134 quad_perm:[0,0,0,0] row_mask:0xf bank_mask:0xf\n"
        "v_mul_f32_dpp v227, v76, v227 quad_perm:[1,1,1,1] row_mask:0xf bank_mask:0xf\n"
        "v_mfma_f32_16x16x32_bf16 v[180:183], v[230:233], v[64:67], v[180:183]\n"
        "v_mul_f32_dpp v228, v76, v228 quad_perm:[2,2,2,2] row_mask:0xf bank_mask:0xf\n"
        "v_mul_f32_dpp v229, v76, v229 quad_perm:[3,3,3,3] row_mask:0xf bank_mask:0xf\n"
        "v_mfma_f32_16x16x32_bf16 v[70:73], v[116:119], a[104:107], 0\n"
        "v_cvt_pk_bf16_f32 v76, v134, v227\n"
        "v_cvt_pk_bf16_f32 v135, v228, v229\n"
        "v_mfma_f32_16x16x32_bf16 v[70:73], v[110:113], a[100:103], v[70:73]\n"
        "v_and_b32 v102, v25, v76\n"
        "v_and_b32 v103, v25, v135\n"
        "v_mfma_f32_16x16x32_bf16 v[70:73], v[104:107], a[96:99], v[70:73]\n"
        "v_mov_b64 v[108:109], v[102:103]\n"
        "v_cvt_pk_bf16_f32 v60, v120, v121\n"
        "v_mfma_f32_16x16x32_bf16 v[140:143], v[104:107], a[108:111], 0\n"
        "v_cvt_pk_bf16_f32 v61, v122, v123\n"
        "v_cvt_pk_bf16_f32 v64, v164, v165\n"
        "v_mfma_f32_16x16x32_bf16 v[140:143], v[110:113], a[112:115], v[140:143]\n"
        "v_cvt_pk_bf16_f32 v65, v166, v167\n"
        "v_add_f32_dpp v129, v129, v129 row_ror:2 row_mask:0xf bank_mask:0xf\n"
        "v_mfma_f32_16x16x32_bf16 v[140:143], v[116:119], a[116:119], v[140:143]\n"
        "ds_read_u16 v123, v19 offset:3584\n"
        "v_add_f32_dpp v129, v129, v129 row_ror:1 row_mask:0xf bank_mask:0xf\n"
        "v_mfma_f32_16x16x32_bf16 v[160:163], v[60:63], v[102:105], 0\n"
        "s_nop 0\n"
        "v_add_f32_dpp v129, v129, v129 row_bcast:15 row_mask:0xa bank_mask:0xf\n"
        "v_and_b32 v226, 0xffff0000, v114\n"
        "v_mfma_f32_16x16x32_bf16 v[164:167], v[62:65], v[106:109], 0\n"
        "v_add_f32_dpp v129, v129, v129 row_bcast:31 row_mask:0xc bank_mask:0xf\n"
        "v_mov_b64 v[114:115], v[102:103]\n"
        "v_mfma_f32_16x16x32_bf16 v[168:171], v[116:119], a[128:131], 0\n"
        "v_readlane_b32 s95, v129, 63\n"
        "v_cvt_pk_bf16_f32 v66, v70, v71\n"
        "v_mfma_f32_16x16x32_bf16 v[168:171], v[110:113], a[124:127], v[168:171]\n"
        "v_rcp_f32 v234, s95\n"
        "v_mfma_f32_16x16x32_bf16 v[168:171], v[104:107], a[120:123], v[168:171]\n"
        "v_cvt_pk_bf16_f32 v67, v72, v73\n"
        "v_cvt_pk_bf16_f32 v70, v140, v141\n"
        "v_mfma_f32_16x16x32_bf16 v[184:187], v[104:107], a[132:135], 0\n"
        "v_cvt_pk_bf16_f32 v71, v142, v143\n"
        "v_mul_f32 v128, v128, v234\n"
        "v_mfma_f32_16x16x32_bf16 v[184:187], v[110:113], a[136:139], v[184:187]\n"
        "v_cvt_f16_f32 v128, v128\n"
        "v_mov_b64 v[120:121], v[102:103]\n"
        "v_mfma_f32_16x16x32_bf16 v[184:187], v[116:119], a[140:143], v[184:187]\n"
        "s_waitcnt lgkmcnt(0)\n"
        "v_cmp_ne_u32_e64 s[24:25], s29, v123\n"
        "v_mul_f32_dpp v235, v238, v134 quad_perm:[0,0,0,0] row_mask:0xf bank_mask:0xf\n"
        "v_mfma_f32_16x16x32_bf16 v[102:105], v[66:69], v[108:111], 0\n"
        "v_mul_f16 v128, v128, v77\n"
        "v_and_b32_dpp v122, v76, v25 row_ror:12 row_mask:0xf bank_mask:0xf\n"
        "v_mfma_f32_16x16x32_bf16 v[64:67], v[68:71], v[112:115], 0\n"
        "s_cmp_lg_u64 s[24:25], 0\n"
        "ds_write_b16 v22, v128 offset:512\n"
        "v_mfma_f32_16x16x32_bf16 v[106:109], v[136:139], a[80:83], 0\n"
        "s_cbranch_scc1 SP8_%=\n"
        "BK8_%=:\n"
        "v_and_b32_dpp v123, v135, v25 row_ror:12 row_mask:0xf bank_mask:0xf\n"
        "v_and_b32_dpp v142, v76, v25 row_ror:8 row_mask:0xf bank_mask:0xf\n"
        "v_mfma_f32_16x16x32_bf16 v[106:109], v[130:133], a[76:79], v[106:109]\n"
        "v_and_b32_dpp v143, v135, v25 row_ror:8 row_mask:0xf bank_mask:0xf\n"
        "v_cvt_pk_bf16_f32 v72, v168, v169\n"
        "v_mfma_f32_16x16x32_bf16 v[106:109], v[124:127], a[72:75], v[106:109]\n"
        "v_cvt_pk_bf16_f32 v73, v170, v171\n"
        "v_cvt_pk_bf16_f32 v76, v184, v185\n"
        "v_mfma_f32_16x16x32_bf16 v[110:113], v[124:127], a[84:87], 0\n"
        "v_cvt_pk_bf16_f32 v77, v186, v187\n"
        "v_mov_b64 v[128:129], v[122:123]\n"
        "v_mfma_f32_16x16x32_bf16 v[110:113], v[130:133], a[88:91], v[110:113]\n"
        "v_mov_b64 v[134:135], v[122:123]\n"
        "v_cvt_pk_bf16_f32 v70, v160, v161\n"
        "v_mfma_f32_16x16x32_bf16 v[110:113], v[136:139], a[92:95], v[110:113]\n"
        "v_cvt_pk_bf16_f32 v160, v162, v163\n"
        "v_cvt_pk_bf16_f32 v168, v164, v165\n"
        "v_mfma_f32_16x16x32_bf16 v[184:187], v[72:75], v[114:117], 0\n"
        "v_cvt_pk_bf16_f32 v71, v166, v167\n"
        "v_cvt_pk_bf16_f32 v161, v102, v103\n"
        "v_mfma_f32_16x16x32_bf16 v[114:117], v[74:77], v[118:121], 0\n"
        "v_cvt_pk_bf16_f32 v169, v104, v105\n"
        "v_cvt_pk_bf16_f32 v72, v64, v65\n"
        "v_mfma_f32_16x16x32_bf16 v[118:121], v[136:139], a[104:107], 0\n"
        "v_cvt_pk_bf16_f32 v60, v106, v107\n"
        "v_cvt_pk_bf16_f32 v61, v108, v109\n"
        "v_mfma_f32_16x16x32_bf16 v[118:121], v[130:133], a[100:103], v[118:121]\n"
        "v_cvt_pk_bf16_f32 v64, v110, v111\n"
        "v_cvt_pk_bf16_f32 v65, v112, v113\n"
        "v_mfma_f32_16x16x32_bf16 v[118:121], v[124:127], a[96:99], v[118:121]\n"
        "v_cvt_pk_bf16_f32 v73, v186, v187\n"
        "v_cvt_pk_bf16_f32 v162, v66, v67\n"
        "v_mfma_f32_16x16x32_bf16 v[104:107], v[124:127], a[108:111], 0\n"
        "ds_write_b128 v12, v[70:73]\n"
        "v_mfma_f32_16x16x32_bf16 v[104:107], v[130:133], a[112:115], v[104:107]\n"
        "v_cvt_pk_bf16_f32 v170, v184, v185\n"
        "v_cvt_pk_bf16_f32 v163, v114, v115\n"
        "v_mfma_f32_16x16x32_bf16 v[104:107], v[136:139], a[116:119], v[104:107]\n"
        "ds_write_b128 v12, v[160:163] offset:1024\n"
        "v_mfma_f32_16x16x32_bf16 v[108:111], v[60:63], v[122:125], 0\n"
        "v_cvt_pk_bf16_f32 v171, v116, v117\n"
        "v_mov_b64 v[140:141], v[122:123]\n"
        "v_mfma_f32_16x16x32_bf16 v[112:115], v[62:65], v[126:129], 0\n"
        "ds_write_b128 v12, v[168:171] offset:2048\n"
        "v_mfma_f32_16x16x32_bf16 v[164:167], v[70:73], v[70:73], 0\n"
        "v_mov_b64 v[148:149], v[142:143]\n"
        "v_mov_b64 v[154:155], v[142:143]\n"
        "v_mfma_f32_16x16x32_bf16 v[164:167], v[160:163], v[160:163], v[164:167]\n"
        "v_fmac_f32_dpp v235, v238, v227 quad_perm:[1,1,1,1] row_mask:0xf bank_mask:0xf\n"
        "v_mov_b64 v[160:161], v[142:143]\n"
        "v_mfma_f32_16x16x32_bf16 v[164:167], v[168:171], v[168:171], v[164:167]\n"
        "v_fmac_f32_dpp v235, v238, v228 quad_perm:[2,2,2,2] row_mask:0xf bank_mask:0xf\n"
        "v_cvt_pk_f16_f32 v102, v172, v173\n"
        "v_mfma_f32_16x16x32_bf16 v[168:171], v[136:139], a[128:131], 0\n"
        "v_fmac_f32_dpp v235, v238, v229 quad_perm:[3,3,3,3] row_mask:0xf bank_mask:0xf\n"
        "v_add_f32 v235, v235, v244\n"
        "v_mfma_f32_16x16x32_bf16 v[168:171], v[130:133], a[124:127], v[168:171]\n"
        "v_cvt_pk_f16_f32 v103, v174, v175\n"
        "v_cvt_pk_bf16_f32 v66, v118, v119\n"
        "v_mfma_f32_16x16x32_bf16 v[168:171], v[124:127], a[120:123], v[168:171]\n"
        "v_cvt_pk_bf16_f32 v67, v120, v121\n"
        "v_cvt_pk_bf16_f32 v70, v104, v105\n"
        "v_mfma_f32_16x16x32_bf16 v[116:119], v[124:127], a[132:135], 0\n"
        "v_cvt_pk_bf16_f32 v71, v106, v107\n"
        "v_pk_mul_f16 v102, v102, v242 op_sel_hi:[1,0]\n"
        "v_mfma_f32_16x16x32_bf16 v[116:119], v[130:133], a[136:139], v[116:119]\n"
        "v_pk_mul_f16 v103, v103, v242 op_sel_hi:[1,0]\n"
        "v_pk_mul_f16 v102, v102, v188\n"
        "v_mfma_f32_16x16x32_bf16 v[116:119], v[136:139], a[140:143], v[116:119]\n"
        "v_pk_mul_f16 v103, v103, v189\n"
        "v_cvt_pk_bf16_f32 v72, v168, v169\n"
        "v_mfma_f32_16x16x32_bf16 v[120:123], v[66:69], v[128:131], 0\n"
        "v_exp_f16_sdwa v102, v102 dst_sel:WORD_0 dst_unused:UNUSED_PRESERVE src0_sel:WORD_0\n"
        "v_mfma_f32_16x16x32_bf16 v[64:67], v[68:71], v[132:135], 0\n"
        "v_exp_f16_sdwa v103, v103 dst_sel:WORD_0 dst_unused:UNUSED_PRESERVE src0_sel:WORD_0\n"
        "v_mfma_f32_16x16x32_bf16 v[124:127], v[156:159], a[80:83], 0\n"
        "v_exp_f16_sdwa v102, v102 dst_sel:WORD_1 dst_unused:UNUSED_PRESERVE src0_sel:WORD_1\n"
        "v_mfma_f32_16x16x32_bf16 v[124:127], v[150:153], a[76:79], v[124:127]\n"
        "v_exp_f16_sdwa v103, v103 dst_sel:WORD_1 dst_unused:UNUSED_PRESERVE src0_sel:WORD_1\n"
        "v_mfma_f32_16x16x32_bf16 v[124:127], v[144:147], a[72:75], v[124:127]\n"
        "v_cvt_pk_bf16_f32 v73, v170, v171\n"
        "v_cvt_pk_bf16_f32 v76, v116, v117\n"
        "v_mfma_f32_16x16x32_bf16 v[128:131], v[144:147], a[84:87], 0\n"
        "v_cvt_pk_bf16_f32 v77, v118, v119\n"
        "v_cvt_pk_f16_f32 v104, v176, v177\n"
        "v_mfma_f32_16x16x32_bf16 v[128:131], v[150:153], a[88:91], v[128:131]\n"
        "v_cvt_pk_f16_f32 v105, v178, v179\n"
        "v_cvt_pk_bf16_f32 v70, v108, v109\n"
        "v_mfma_f32_16x16x32_bf16 v[128:131], v[156:159], a[92:95], v[128:131]\n"
        "v_cvt_pk_bf16_f32 v106, v110, v111\n"
        "v_cvt_pk_bf16_f32 v110, v112, v113\n"
        "v_mfma_f32_16x16x32_bf16 v[116:119], v[72:75], v[134:137], 0\n"
        "v_cvt_pk_bf16_f32 v71, v114, v115\n"
        "v_cvt_pk_bf16_f32 v107, v120, v121\n"
        "v_mfma_f32_16x16x32_bf16 v[132:135], v[74:77], v[138:141], 0\n"
        "v_cvt_pk_bf16_f32 v111, v122, v123\n"
        "v_cvt_pk_bf16_f32 v72, v64, v65\n"
        "v_mfma_f32_16x16x32_bf16 v[120:123], v[156:159], a[104:107], 0\n"
        "v_cvt_pk_bf16_f32 v60, v124, v125\n"
        "v_cvt_pk_bf16_f32 v61, v126, v127\n"
        "v_mfma_f32_16x16x32_bf16 v[120:123], v[150:153], a[100:103], v[120:123]\n"
        "v_cvt_pk_bf16_f32 v64, v128, v129\n"
        "v_cvt_pk_bf16_f32 v65, v130, v131\n"
        "v_mfma_f32_16x16x32_bf16 v[120:123], v[144:147], a[96:99], v[120:123]\n"
        "v_cvt_pk_bf16_f32 v73, v118, v119\n"
        "v_cvt_pk_bf16_f32 v108, v66, v67\n"
        "v_mfma_f32_16x16x32_bf16 v[124:127], v[144:147], a[108:111], 0\n"
        "ds_write_b128 v12, v[70:73] offset:3072\n"
        "v_mfma_f32_16x16x32_bf16 v[124:127], v[150:153], a[112:115], v[124:127]\n"
        "v_cvt_pk_bf16_f32 v112, v116, v117\n"
        "v_cvt_pk_bf16_f32 v109, v132, v133\n"
        "v_mfma_f32_16x16x32_bf16 v[124:127], v[156:159], a[116:119], v[124:127]\n"
        "ds_write_b128 v12, v[106:109] offset:4096\n"
        "v_mfma_f32_16x16x32_bf16 v[114:117], v[60:63], v[142:145], 0\n"
        "v_cvt_pk_bf16_f32 v113, v134, v135\n"
        "v_pk_mul_f16 v104, v104, v242 op_sel_hi:[1,0]\n"
        "v_mfma_f32_16x16x32_bf16 v[128:131], v[62:65], v[146:149], 0\n"
        "ds_write_b128 v12, v[110:113] offset:5120\n"
        "v_mfma_f32_16x16x32_bf16 v[132:135], v[70:73], v[70:73], 0\n"
        "v_pk_mul_f16 v105, v105, v242 op_sel_hi:[1,0]\n"
        "v_pk_mul_f16 v104, v104, v190\n"
        "v_mfma_f32_16x16x32_bf16 v[132:135], v[106:109], v[106:109], v[132:135]\n"
        "v_pk_mul_f16 v105, v105, v191\n"
        "v_cvt_pk_bf16_f32 v66, v120, v121\n"
        "v_mfma_f32_16x16x32_bf16 v[132:135], v[110:113], v[110:113], v[132:135]\n"
        "v_exp_f16_sdwa v104, v104 dst_sel:WORD_0 dst_unused:UNUSED_PRESERVE src0_sel:WORD_0\n"
        "v_mfma_f32_16x16x32_bf16 v[108:111], v[156:159], a[128:131], 0\n"
        "v_exp_f16_sdwa v105, v105 dst_sel:WORD_0 dst_unused:UNUSED_PRESERVE src0_sel:WORD_0\n"
        "v_mfma_f32_16x16x32_bf16 v[108:111], v[150:153], a[124:127], v[108:111]\n"
        "v_exp_f16_sdwa v104, v104 dst_sel:WORD_1 dst_unused:UNUSED_PRESERVE src0_sel:WORD_1\n"
        "v_mfma_f32_16x16x32_bf16 v[108:111], v[144:147], a[120:123], v[108:111]\n"
        "v_cvt_pk_bf16_f32 v67, v122, v123\n"
        "v_cvt_pk_bf16_f32 v70, v124, v125\n"
        "v_mfma_f32_16x16x32_bf16 v[118:121], v[144:147], a[132:135], 0\n"
        "v_cvt_pk_bf16_f32 v71, v126, v127\n"
        "v_cvt_pk_f16_f32 v106, v180, v181\n"
        "v_mfma_f32_16x16x32_bf16 v[118:121], v[150:153], a[136:139], v[118:121]\n"
        "v_exp_f16_sdwa v105, v105 dst_sel:WORD_1 dst_unused:UNUSED_PRESERVE src0_sel:WORD_1\n"
        "v_mfma_f32_16x16x32_bf16 v[118:121], v[156:159], a[140:143], v[118:121]\n"
        "v_cvt_pk_f16_f32 v107, v182, v183\n"
        "v_pk_mul_f16 v106, v106, v242 op_sel_hi:[1,0]\n"
        "v_mfma_f32_16x16x32_bf16 v[122:125], v[66:69], v[148:151], 0\n"
        "v_pk_mul_f16 v107, v107, v242 op_sel_hi:[1,0]\n"
        "v_cvt_pk_bf16_f32 v72, v108, v109\n"
        "v_mfma_f32_16x16x32_bf16 v[64:67], v[68:71], v[152:155], 0\n"
        "s_nop 0\n"
        "v_cvt_pk_bf16_f32 v73, v110, v111\n"
        "v_cvt_pk_bf16_f32 v76, v118, v119\n"
        "v_mfma_f32_16x16x32_bf16 v[110:113], a[152:155], v[214:217], 0\n"
        "v_cvt_pk_bf16_f32 v77, v120, v121\n"
        "v_pk_mul_f16 v106, v106, v192\n"
        "v_mfma_f32_16x16x32_bf16 v[110:113], a[148:151], v[210:213], v[110:113]\n"
        "v_mov_b32 v109, 0\n"
        "v_cndmask_b32_e64 v108, 0, v226, s[62:63]\n"
        "v_mfma_f32_16x16x32_bf16 v[110:113], a[144:147], v[206:209], v[110:113]\n"
        "v_pk_mul_f16 v107, v107, v193\n"
        "v_cvt_pk_bf16_f32 v70, v114, v115\n"
        "v_mfma_f32_16x16x32_bf16 v[118:121], v[72:75], v[154:157], 0\n"
        "v_exp_f16_sdwa v106, v106 dst_sel:WORD_0 dst_unused:UNUSED_PRESERVE src0_sel:WORD_0\n"
        "v_mfma_f32_16x16x32_bf16 v[136:139], v[74:77], v[158:161], 0\n"
        "v_exp_f16_sdwa v107, v107 dst_sel:WORD_0 dst_unused:UNUSED_PRESERVE src0_sel:WORD_0\n"
        "v_mfma_f32_16x16x32_bf16 v[140:143], a[156:159], v[206:209], 0\n"
        "v_cvt_pk_bf16_f32 v114, v116, v117\n"
        "v_cvt_pk_bf16_f32 v126, v128, v129\n"
        "v_mfma_f32_16x16x32_bf16 v[140:143], a[160:163], v[210:213], v[140:143]\n"
        "v_cvt_pk_bf16_f32 v71, v130, v131\n"
        "v_cvt_pk_bf16_f32 v115, v122, v123\n"
        "v_mfma_f32_16x16x32_bf16 v[140:143], a[164:167], v[214:217], v[140:143]\n"
        "v_cvt_pk_bf16_f32 v127, v124, v125\n"
        "v_cvt_pk_bf16_f32 v72, v64, v65\n"
        "v_mfma_f32_16x16x32_bf16 v[122:125], a[176:179], v[214:217], 0\n"
        "v_cvt_pk_bf16_f32 v116, v66, v67\n"
        "v_cvt_pk_bf16_f32 v73, v120, v121\n"
        "v_mfma_f32_16x16x32_bf16 v[122:125], a[172:175], v[210:213], v[122:125]\n"
        "ds_write_b128 v12, v[70:73] offset:6144\n"
        "v_mfma_f32_16x16x32_bf16 v[122:125], a[168:171], v[206:209], v[122:125]\n"
        "v_cvt_pk_bf16_f32 v117, v136, v137\n"
        "v_cvt_pk_bf16_f32 v128, v118, v119\n"
        "v_mfma_f32_16x16x32_bf16 v[64:67], v[70:73], v[70:73], 0\n"
        "v_cvt_pk_bf16_f32 v129, v138, v139\n"
        "v_cndmask_b32_e64 v60, v164, v165, s[56:57]\n"
        "v_mfma_f32_16x16x32_bf16 v[64:67], v[114:117], v[114:117], v[64:67]\n"
        "ds_write_b128 v12, v[114:117] offset:7168\n"
        "v_mfma_f32_16x16x32_bf16 v[64:67], v[126:129], v[126:129], v[64:67]\n"
        "v_exp_f16_sdwa v106, v106 dst_sel:WORD_1 dst_unused:UNUSED_PRESERVE src0_sel:WORD_1\n"
        "v_mfma_f32_16x16x32_bf16 v[70:73], a[188:191], v[214:217], 0\n"
        "v_exp_f16_sdwa v107, v107 dst_sel:WORD_1 dst_unused:UNUSED_PRESERVE src0_sel:WORD_1\n"
        "v_mfma_f32_16x16x32_bf16 v[70:73], a[184:187], v[210:213], v[70:73]\n"
        "ds_write_b128 v12, v[126:129] offset:8192\n"
        "v_mfma_f32_16x16x32_bf16 v[70:73], a[180:183], v[206:209], v[70:73]\n"
        "v_cndmask_b32_e64 v60, v60, v166, s[58:59]\n"
        "v_cndmask_b32_e64 v61, v132, v133, s[56:57]\n"
        "v_mfma_f32_16x16x32_bf16 v[114:117], a[192:195], v[206:209], 0\n"
        "v_cndmask_b32_e64 v61, v61, v134, s[58:59]\n"
        "v_cndmask_b32_e64 v61, v61, v135, s[60:61]\n"
        "v_mfma_f32_16x16x32_bf16 v[114:117], a[196:199], v[210:213], v[114:117]\n"
        "v_cvt_pk_bf16_f32 v118, v110, v111\n"
        "v_cvt_pk_bf16_f32 v119, v112, v113\n"
        "v_mfma_f32_16x16x32_bf16 v[114:117], a[200:203], v[214:217], v[114:117]\n"
        "v_cvt_pk_bf16_f32 v120, v140, v141\n"
        "v_cvt_pk_bf16_f32 v121, v142, v143\n"
        "v_mfma_f32_16x16x32_bf16 v[110:113], a[212:215], v[214:217], 0\n"
        "v_cvt_pk_bf16_f32 v126, v122, v123\n"
        "v_cvt_pk_bf16_f32 v127, v124, v125\n"
        "v_mfma_f32_16x16x32_bf16 v[110:113], a[208:211], v[210:213], v[110:113]\n"
        "v_cndmask_b32_e64 v76, v64, v65, s[56:57]\n"
        "v_cndmask_b32_e64 v76, v76, v66, s[58:59]\n"
        "v_mfma_f32_16x16x32_bf16 v[110:113], a[204:207], v[206:209], v[110:113]\n"
        "v_cndmask_b32_e64 v76, v76, v67, s[60:61]\n"
        "v_cvt_pk_bf16_f32 v128, v70, v71\n"
        "v_mfma_f32_16x16x32_f16 v[64:67], v[90:93], v[102:105], 0\n"
        "v_cvt_pk_bf16_f32 v129, v72, v73\n"
        "v_cvt_pk_bf16_f32 v70, v114, v115\n"
        "v_mfma_f32_16x16x32_f16 v[64:67], v[90:93], v[106:109], v[64:67]\n"
        "s_nop 0\n"
        "v_cvt_pk_bf16_f32 v71, v116, v117\n"
        "v_cvt_pk_bf16_f32 v72, v110, v111\n"
        "v_mfma_f32_16x16x32_bf16 v[114:117], v[194:197], v[118:121], 0\n"
        "v_cvt_pk_bf16_f32 v73, v112, v113\n"
        "v_cndmask_b32_e64 v60, v60, v167, s[60:61]\n"
        "v_mfma_f32_16x16x32_bf16 v[114:117], v[198:201], v[126:129], v[114:117]\n"
        "v_cndmask_b32_dpp v61, v76, v61, vcc row_ror:4 row_mask:0xf bank_mask:0xf\n"
        "v_mul_f32 v65, s80, v235\n"
        "v_mfma_f32_16x16x32_bf16 v[114:117], v[202:205], v[70:73], v[114:117]\n"
        "v_cndmask_b32_dpp v60, v61, v60, vcc row_ror:4 row_mask:0xf bank_mask:0xf\n"
        "v_mul_f32 v60, s80, v60\n"
        "v_mfma_f32_16x16x32_bf16 v[122:125], v[214:217], v[70:73], 0\n"
        "v_fma_f32 v65, -v65, v65, v60\n"
        "v_max_f32 v65, 0, v65\n"
        "v_mfma_f32_16x16x32_bf16 v[122:125], v[210:213], v[126:129], v[122:125]\n"
        "v_sub_f32 v60, 0x44800000, v60\n"
        "v_add_f32 v65, s81, v65\n"
        "v_mfma_f32_16x16x32_bf16 v[122:125], v[206:209], v[118:121], v[122:125]\n"
        "v_bfi_b32 v65, s84, v60, v65\n"
        "v_max_f32 v60, s95, v64\n"
        "v_mfma_f32_16x16x32_bf16 v[130:133], v[218:221], v[118:121], 0\n"
        "v_rsq_f32 v61, v65\n"
        "v_mfma_f32_16x16x32_bf16 v[130:133], v[222:225], v[126:129], v[130:133]\n"
        "v_cvt_f16_f32 v61, v61\n"
        "v_cvt_pk_f16_f32 v110, v114, v115\n"
        "v_mfma_f32_16x16x32_bf16 v[130:133], v[230:233], v[70:73], v[130:133]\n"
        "ds_write_b16 v18, v61\n"
        "v_mfma_f32_16x16x32_bf16 v[70:73], a[152:155], v[230:233], 0\n"
        "ds_write_b16 v19, v61\n"
        "v_mfma_f32_16x16x32_bf16 v[70:73], a[148:151], v[222:225], v[70:73]\n"
        "v_mov_b32 v61, s29\n"
        "v_cvt_pk_f16_f32 v111, v116, v117\n"
        "v_mfma_f32_16x16x32_bf16 v[70:73], a[144:147], v[218:221], v[70:73]\n"
        "ds_write_b16 v22, v61 offset:4096\n"
        "v_mfma_f32_16x16x32_bf16 v[112:115], a[156:159], v[218:221], 0\n"
        "v_rcp_f32 v61, v64\n"
        "v_mfma_f32_16x16x32_bf16 v[112:115], a[160:163], v[222:225], v[112:115]\n"
        "v_cvt_f16_f32 v76, v61\n"
        "v_add_f32 v77, v234, v61\n"
        "v_mfma_f32_16x16x32_bf16 v[112:115], a[164:167], v[230:233], v[112:115]\n"
        "v_pk_mul_f16 v102, v102, v76 op_sel_hi:[1,0]\n"
        "v_pk_mul_f16 v103, v103, v76 op_sel_hi:[1,0]\n"
        "v_mfma_f32_16x16x32_bf16 v[64:67], a[176:179], v[230:233], 0\n"
        "v_pk_mul_f16 v104, v104, v76 op_sel_hi:[1,0]\n"
        "v_pk_mul_f16 v105, v105, v76 op_sel_hi:[1,0]\n"
        "v_mfma_f32_16x16x32_bf16 v[64:67], a[172:175], v[222:225], v[64:67]\n"
        "v_pk_mul_f16 v106, v106, v76 op_sel_hi:[1,0]\n"
        "v_pk_mul_f16 v107, v107, v76 op_sel_hi:[1,0]\n"
        "v_mfma_f32_16x16x32_bf16 v[64:67], a[168:171], v[218:221], v[64:67]\n"
        "v_pk_mul_f16 v108, v108, v76 op_sel_hi:[1,0]\n"
        "v_cvt_pk_bf16_f32 v118, v70, v71\n"
        "v_mfma_f32_16x16x32_bf16 v[126:129], a[180:183], v[218:221], 0\n"
        "v_cvt_pk_bf16_f32 v119, v72, v73\n"
        "v_cvt_pk_bf16_f32 v120, v112, v113\n"
        "v_mfma_f32_16x16x32_bf16 v[126:129], a[184:187], v[222:225], v[126:129]\n"
        "s_nop 0\n"
        "v_cvt_pk_bf16_f32 v121, v114, v115\n"
        "v_cvt_pk_bf16_f32 v70, v64, v65\n"
        "v_mfma_f32_16x16x32_bf16 v[126:129], a[188:191], v[230:233], v[126:129]\n"
        "v_cvt_pk_bf16_f32 v71, v66, v67\n"
        "v_pk_mul_f16 v102, v102, v188\n"
        "v_mfma_f32_16x16x32_bf16 v[64:67], a[200:203], v[230:233], 0\n"
        "s_nop 0\n"
        "v_pk_mul_f16 v103, v103, v189\n"
        "v_pk_mul_f16 v104, v104, v190\n"
        "v_mfma_f32_16x16x32_bf16 v[64:67], a[196:199], v[222:225], v[64:67]\n"
        "s_nop 0\n"
        "v_cvt_pk_bf16_f32 v72, v126, v127\n"
        "v_cvt_pk_bf16_f32 v73, v128, v129\n"
        "v_mfma_f32_16x16x32_bf16 v[64:67], a[192:195], v[218:221], v[64:67]\n"
        "v_pk_mul_f16 v105, v105, v191\n"
        "v_pk_mul_f16 v106, v106, v192\n"
        "v_mfma_f32_16x16x32_bf16 v[114:117], a[204:207], v[218:221], 0\n"
        "s_nop 0\n"
        "v_pk_mul_f16 v107, v107, v193\n"
        "v_pk_mul_f16 v110, v110, v242 op_sel:[0,1]\n"
        "v_mfma_f32_16x16x32_bf16 v[114:117], a[208:211], v[222:225], v[114:117]\n"
        "s_nop 0\n"
        "v_cvt_pk_bf16_f32 v126, v64, v65\n"
        "v_cvt_pk_bf16_f32 v127, v66, v67\n"
        "v_mfma_f32_16x16x32_bf16 v[114:117], a[212:215], v[230:233], v[114:117]\n"
        "v_pk_mul_f16 v111, v111, v242 op_sel:[0,1]\n"
        "v_pk_mul_f16 v110, v110, v188\n"
        "v_mfma_f32_16x16x32_bf16 v[64:67], v[202:205], a[224:227], 0\n"
        "s_nop 0\n"
        "v_pk_mul_f16 v111, v111, v189\n"
        "v_cvt_pk_f16_f32 v112, v122, v123\n"
        "v_mfma_f32_16x16x32_bf16 v[64:67], v[198:201], a[220:223], v[64:67]\n"
        "s_nop 0\n"
        "v_cvt_pk_bf16_f32 v128, v114, v115\n"
        "v_cvt_pk_bf16_f32 v129, v116, v117\n"
        "v_mfma_f32_16x16x32_bf16 v[64:67], v[194:197], a[216:219], v[64:67]\n"
        "v_exp_f16_sdwa v110, v110 dst_sel:WORD_0 dst_unused:UNUSED_PRESERVE src0_sel:WORD_0\n"
        "v_mfma_f32_16x16x32_bf16 v[136:139], v[206:209], a[216:219], 0\n"
        "v_exp_f16_sdwa v111, v111 dst_sel:WORD_0 dst_unused:UNUSED_PRESERVE src0_sel:WORD_0\n"
        "v_mfma_f32_16x16x32_bf16 v[136:139], v[210:213], a[220:223], v[136:139]\n"
        "v_exp_f16_sdwa v110, v110 dst_sel:WORD_1 dst_unused:UNUSED_PRESERVE src0_sel:WORD_1\n"
        "v_mfma_f32_16x16x32_bf16 v[136:139], v[214:217], a[224:227], v[136:139]\n"
        "v_exp_f16_sdwa v111, v111 dst_sel:WORD_1 dst_unused:UNUSED_PRESERVE src0_sel:WORD_1\n"
        "v_mfma_f32_16x16x32_bf16 v[140:143], v[230:233], a[224:227], 0\n"
        "v_cvt_pk_f16_f32 v113, v124, v125\n"
        "v_pk_mul_f16 v112, v112, v242 op_sel:[0,1]\n"
        "v_mfma_f32_16x16x32_bf16 v[140:143], v[222:225], a[220:223], v[140:143]\n"
        "v_pk_mul_f16 v113, v113, v242 op_sel:[0,1]\n"
        "v_pk_mul_f16 v112, v112, v190\n"
        "v_mfma_f32_16x16x32_bf16 v[140:143], v[218:221], a[216:219], v[140:143]\n"
        "v_pk_mul_f16 v113, v113, v191\n"
        "v_cvt_pk_f16_f32 v114, v130, v131\n"
        "v_mfma_f32_16x16x32_bf16 v[122:125], v[194:197], v[118:121], 0\n"
        "v_exp_f16_sdwa v112, v112 dst_sel:WORD_0 dst_unused:UNUSED_PRESERVE src0_sel:WORD_0\n"
        "v_mfma_f32_16x16x32_bf16 v[122:125], v[198:201], v[70:73], v[122:125]\n"
        "v_exp_f16_sdwa v113, v113 dst_sel:WORD_0 dst_unused:UNUSED_PRESERVE src0_sel:WORD_0\n"
        "v_mfma_f32_16x16x32_bf16 v[122:125], v[202:205], v[126:129], v[122:125]\n"
        "v_exp_f16_sdwa v112, v112 dst_sel:WORD_1 dst_unused:UNUSED_PRESERVE src0_sel:WORD_1\n"
        "v_mfma_f32_16x16x32_bf16 v[144:147], v[214:217], v[126:129], 0\n"
        "v_exp_f16_sdwa v113, v113 dst_sel:WORD_1 dst_unused:UNUSED_PRESERVE src0_sel:WORD_1\n"
        "v_mfma_f32_16x16x32_bf16 v[144:147], v[210:213], v[70:73], v[144:147]\n"
        "v_cvt_pk_f16_f32 v115, v132, v133\n"
        "v_pk_mul_f16 v114, v114, v242 op_sel:[0,1]\n"
        "v_mfma_f32_16x16x32_bf16 v[144:147], v[206:209], v[118:121], v[144:147]\n"
        "v_mov_b32 v117, 0\n"
        "v_cndmask_b32_e64 v116, 0, v226, s[64:65]\n"
        "v_mfma_f32_16x16x32_bf16 v[132:135], v[218:221], v[118:121], 0\n"
        "v_pk_mul_f16 v115, v115, v242 op_sel:[0,1]\n"
        "v_pk_mul_f16 v114, v114, v192\n"
        "v_mfma_f32_16x16x32_bf16 v[132:135], v[222:225], v[70:73], v[132:135]\n"
        "v_pk_mul_f16 v115, v115, v193\n"
        "v_cvt_pk_f16_f32 v130, v64, v65\n"
        "v_mfma_f32_16x16x32_bf16 v[132:135], v[230:233], v[126:129], v[132:135]\n"
        "v_exp_f16_sdwa v114, v114 dst_sel:WORD_0 dst_unused:UNUSED_PRESERVE src0_sel:WORD_0\n"
        "v_mfma_f32_16x16x32_bf16 v[70:73], v[230:233], a[236:239], 0\n"
        "v_exp_f16_sdwa v115, v115 dst_sel:WORD_0 dst_unused:UNUSED_PRESERVE src0_sel:WORD_0\n"
        "v_mfma_f32_16x16x32_bf16 v[70:73], v[222:225], a[232:235], v[70:73]\n"
        "v_exp_f16_sdwa v114, v114 dst_sel:WORD_1 dst_unused:UNUSED_PRESERVE src0_sel:WORD_1\n"
        "v_mfma_f32_16x16x32_bf16 v[70:73], v[218:221], a[228:231], v[70:73]\n"
        "v_exp_f16_sdwa v115, v115 dst_sel:WORD_1 dst_unused:UNUSED_PRESERVE src0_sel:WORD_1\n"
        "v_mfma_f32_16x16x32_bf16 v[148:151], v[206:209], a[228:231], 0\n"
        "v_cvt_pk_f16_f32 v131, v66, v67\n"
        "v_cvt_pk_f16_f32 v120, v122, v123\n"
        "v_mfma_f32_16x16x32_bf16 v[148:151], v[210:213], a[232:235], v[148:151]\n"
        "v_cvt_pk_f16_f32 v121, v124, v125\n"
        "v_pk_mul_f16 v120, v120, v243 op_sel_hi:[1,0]\n"
        "v_mfma_f32_16x16x32_bf16 v[148:151], v[214:217], a[236:239], v[148:151]\n"
        "v_pk_mul_f16 v121, v121, v243 op_sel_hi:[1,0]\n"
        "v_pk_mul_f16 v120, v120, v188\n"
        "v_mfma_f32_16x16x32_bf16 v[64:67], v[202:205], a[236:239], 0\n"
        "v_pk_mul_f16 v121, v121, v189\n"
        "v_cvt_pk_f16_f32 v122, v144, v145\n"
        "v_mfma_f32_16x16x32_bf16 v[64:67], v[198:201], a[232:235], v[64:67]\n"
        "v_exp_f16_sdwa v120, v120 dst_sel:WORD_0 dst_unused:UNUSED_PRESERVE src0_sel:WORD_0\n"
        "v_mfma_f32_16x16x32_bf16 v[64:67], v[194:197], a[228:231], v[64:67]\n"
        "v_exp_f16_sdwa v121, v121 dst_sel:WORD_0 dst_unused:UNUSED_PRESERVE src0_sel:WORD_0\n"
        "v_mfma_f32_16x16x32_bf16 v[152:155], v[194:197], a[240:243], 0\n"
        "v_exp_f16_sdwa v120, v120 dst_sel:WORD_1 dst_unused:UNUSED_PRESERVE src0_sel:WORD_1\n"
        "v_mfma_f32_16x16x32_bf16 v[152:155], v[198:201], a[244:247], v[152:155]\n"
        "v_exp_f16_sdwa v121, v121 dst_sel:WORD_1 dst_unused:UNUSED_PRESERVE src0_sel:WORD_1\n"
        "v_mfma_f32_16x16x32_bf16 v[152:155], v[202:205], a[248:251], v[152:155]\n"
        "v_cvt_pk_f16_f32 v123, v146, v147\n"
        "v_pk_mul_f16 v122, v122, v243 op_sel_hi:[1,0]\n"
        "v_mfma_f32_16x16x32_bf16 v[164:167], v[214:217], a[248:251], 0\n"
        "v_pk_mul_f16 v123, v123, v243 op_sel_hi:[1,0]\n"
        "v_pk_mul_f16 v122, v122, v190\n"
        "v_mfma_f32_16x16x32_bf16 v[164:167], v[210:213], a[244:247], v[164:167]\n"
        "v_pk_mul_f16 v123, v123, v191\n"
        "v_cvt_pk_f16_f32 v124, v132, v133\n"
        "v_mfma_f32_16x16x32_bf16 v[164:167], v[206:209], a[240:243], v[164:167]\n"
        "v_exp_f16_sdwa v122, v122 dst_sel:WORD_0 dst_unused:UNUSED_PRESERVE src0_sel:WORD_0\n"
        "v_mfma_f32_16x16x32_bf16 v[172:175], v[218:221], a[240:243], 0\n"
        "v_exp_f16_sdwa v123, v123 dst_sel:WORD_0 dst_unused:UNUSED_PRESERVE src0_sel:WORD_0\n"
        "v_mfma_f32_16x16x32_bf16 v[172:175], v[222:225], a[244:247], v[172:175]\n"
        "v_mov_b32 v127, 0\n"
        "v_cndmask_b32_e64 v126, 0, v226, s[66:67]\n"
        "v_mfma_f32_16x16x32_bf16 v[172:175], v[230:233], a[248:251], v[172:175]\n"
        "v_exp_f16_sdwa v122, v122 dst_sel:WORD_1 dst_unused:UNUSED_PRESERVE src0_sel:WORD_1\n"
        "v_mfma_f32_16x16x32_f16 v[156:159], v[90:93], v[110:113], 0\n"
        "v_exp_f16_sdwa v123, v123 dst_sel:WORD_1 dst_unused:UNUSED_PRESERVE src0_sel:WORD_1\n"
        "v_mfma_f32_16x16x32_f16 v[156:159], v[90:93], v[114:117], v[156:159]\n"
        "v_cvt_pk_f16_f32 v125, v134, v135\n"
        "v_pk_mul_f16 v124, v124, v243 op_sel_hi:[1,0]\n"
        "v_mfma_f32_16x16x32_bf16 v[180:183], v[230:233], v[32:35], 0\n"
        "v_pk_mul_f16 v125, v125, v243 op_sel_hi:[1,0]\n"
        "v_pk_mul_f16 v124, v124, v192\n"
        "v_mfma_f32_16x16x32_bf16 v[180:183], v[222:225], v[28:31], v[180:183]\n"
        "v_pk_mul_f16 v125, v125, v193\n"
        "v_cvt_pk_f16_f32 v132, v136, v137\n"
        "v_mfma_f32_16x16x32_bf16 v[180:183], v[218:221], a[252:255], v[180:183]\n"
        "v_exp_f16_sdwa v124, v124 dst_sel:WORD_0 dst_unused:UNUSED_PRESERVE src0_sel:WORD_0\n"
        "v_mfma_f32_16x16x32_bf16 v[226:229], v[206:209], a[252:255], 0\n"
        "v_exp_f16_sdwa v125, v125 dst_sel:WORD_0 dst_unused:UNUSED_PRESERVE src0_sel:WORD_0\n"
        "v_mfma_f32_16x16x32_bf16 v[226:229], v[210:213], v[28:31], v[226:229]\n"
        "v_exp_f16_sdwa v124, v124 dst_sel:WORD_1 dst_unused:UNUSED_PRESERVE src0_sel:WORD_1\n"
        "v_mfma_f32_16x16x32_bf16 v[226:229], v[214:217], v[32:35], v[226:229]\n"
        "ds_read2_b32 v[136:137], v24 offset0:0 offset1:0\n"
        "ds_read2_b32 v[146:147], v24 offset0:1 offset1:1\n"
        "v_mfma_f32_16x16x32_bf16 v[234:237], v[202:205], v[32:35], 0\n"
        "ds_read2_b32 v[162:163], v24 offset0:2 offset1:2\n"
        "ds_read2_b32 v[170:171], v24 offset0:3 offset1:3\n"
        "v_mfma_f32_16x16x32_bf16 v[234:237], v[198:201], v[28:31], v[234:237]\n"
        "ds_read2_b32 v[178:179], v24 offset0:4 offset1:4\n"
        "ds_read2_b32 v[186:187], v24 offset0:5 offset1:5\n"
        "v_mfma_f32_16x16x32_bf16 v[234:237], v[194:197], a[252:255], v[234:237]\n"
        "v_exp_f16_sdwa v125, v125 dst_sel:WORD_1 dst_unused:UNUSED_PRESERVE src0_sel:WORD_1\n"
        "v_mfma_f32_16x16x32_bf16 v[238:241], v[194:197], v[36:39], 0\n"
        "v_cvt_pk_f16_f32 v133, v138, v139\n"
        "v_cvt_pk_f16_f32 v134, v140, v141\n"
        "v_mfma_f32_16x16x32_bf16 v[238:241], v[198:201], v[40:43], v[238:241]\n"
        "v_cvt_pk_f16_f32 v135, v142, v143\n"
        "v_cvt_pk_f16_f32 v144, v70, v71\n"
        "v_mfma_f32_16x16x32_bf16 v[238:241], v[202:205], v[44:47], v[238:241]\n"
        "v_cvt_pk_f16_f32 v145, v72, v73\n"
        "v_max_f32 v60, v60, v156\n"
        "v_mfma_f32_16x16x32_bf16 v[70:73], v[214:217], v[44:47], 0\n"
        "v_rcp_f32 v61, v156\n"
        "v_mfma_f32_16x16x32_bf16 v[70:73], v[210:213], v[40:43], v[70:73]\n"
        "v_add_f32 v77, v77, v61\n"
        "v_cvt_pk_f16_f32 v142, v148, v149\n"
        "v_mfma_f32_16x16x32_bf16 v[70:73], v[206:209], v[36:39], v[70:73]\n"
        "v_cvt_pk_f16_f32 v143, v150, v151\n"
        "v_cvt_pk_f16_f32 v140, v64, v65\n"
        "v_mfma_f32_16x16x32_bf16 v[148:151], v[218:221], v[36:39], 0\n"
        "s_waitcnt lgkmcnt(0)\n"
        "v_cvt_pk_f16_f32 v141, v66, v67\n"
        "v_cvt_pk_f16_f32 v156, v152, v153\n"
        "v_mfma_f32_16x16x32_bf16 v[148:151], v[222:225], v[40:43], v[148:151]\n"
        "v_cvt_pk_f16_f32 v157, v154, v155\n"
        "v_cvt_pk_f16_f32 v158, v164, v165\n"
        "v_mfma_f32_16x16x32_bf16 v[148:151], v[230:233], v[44:47], v[148:151]\n"
        "v_cvt_pk_f16_f32 v159, v166, v167\n"
        "v_cvt_pk_f16_f32 v160, v172, v173\n"
        "v_mfma_f32_16x16x32_f16 v[64:67], v[90:93], v[120:123], 0\n"
        "v_cvt_pk_f16_f32 v161, v174, v175\n"
        "v_cvt_f16_f32 v76, v61\n"
        "v_mfma_f32_16x16x32_f16 v[64:67], v[90:93], v[124:127], v[64:67]\n"
        "v_pk_mul_f16 v110, v110, v76 op_sel_hi:[1,0]\n"
        "v_pk_mul_f16 v111, v111, v76 op_sel_hi:[1,0]\n"
        "v_mfma_f32_16x16x32_bf16 v[152:155], v[230:233], v[56:59], 0\n"
        "s_nop 0\n"
        "v_pk_mul_f16 v112, v112, v76 op_sel_hi:[1,0]\n"
        "v_pk_mul_f16 v113, v113, v76 op_sel_hi:[1,0]\n"
        "v_mfma_f32_16x16x32_bf16 v[152:155], v[222:225], v[52:55], v[152:155]\n"
        "s_nop 0\n"
        "v_rcp_f32 v61, v64\n"
        "v_mfma_f32_16x16x32_bf16 v[152:155], v[218:221], v[48:51], v[152:155]\n"
        "v_max_f32 v60, v60, v64\n"
        "v_add_f32 v77, v77, v61\n"
        "v_mfma_f32_16x16x32_bf16 v[64:67], v[206:209], v[48:51], 0\n"
        "v_fma_f32 v60, v77, s82, v60\n"
        "v_cmp_nlt_f32_e64 s[74:75], v60, s83\n"
        "v_mfma_f32_16x16x32_bf16 v[64:67], v[210:213], v[52:55], v[64:67]\n"
        "v_pk_mul_f16 v114, v114, v76 op_sel_hi:[1,0]\n"
        "v_pk_mul_f16 v115, v115, v76 op_sel_hi:[1,0]\n"
        "v_mfma_f32_16x16x32_bf16 v[64:67], v[214:217], v[56:59], v[64:67]\n"
        "s_cmp_lg_u64 s[74:75], 0\n"
        "v_pk_mul_f16 v116, v116, v76 op_sel_hi:[1,0]\n"
        "v_cvt_pk_f16_f32 v168, v180, v181\n"
        "v_mfma_f32_16x16x32_bf16 v[242:245], v[202:205], v[56:59], 0\n"
        "s_cbranch_scc1 SLOW_%=\n"
        "v_cvt_pk_f16_f32 v169, v182, v183\n"
        "v_cvt_pk_f16_f32 v166, v226, v227\n"
        "v_mfma_f32_16x16x32_bf16 v[242:245], v[198:201], v[52:55], v[242:245]\n"
        "v_cvt_pk_f16_f32 v167, v228, v229\n"
        "v_cvt_pk_f16_f32 v164, v234, v235\n"
        "v_mfma_f32_16x16x32_bf16 v[242:245], v[194:197], v[48:51], v[242:245]\n"
        "v_cvt_pk_f16_f32 v165, v236, v237\n"
        "v_cvt_pk_f16_f32 v172, v238, v239\n"
        "v_mfma_f32_16x16x32_f16 v[226:229], v[102:105], v[130:133], 0\n"
        "v_cvt_pk_f16_f32 v173, v240, v241\n"
        "v_cvt_pk_f16_f32 v174, v70, v71\n"
        "v_mfma_f32_16x16x32_f16 v[226:229], v[106:109], v[134:137], v[226:229]\n"
        "v_cvt_pk_f16_f32 v175, v72, v73\n"
        "v_cvt_pk_f16_f32 v176, v148, v149\n"
        "v_mfma_f32_16x16x32_bf16 v[226:229], v[194:197], v[94:97], v[226:229]\n"
        "v_cvt_pk_f16_f32 v177, v150, v151\n"
        "v_pk_mul_f16 v110, v110, v188\n"
        "v_mfma_f32_16x16x32_bf16 v[70:73], v[194:197], v[98:101], 0\n"
        "v_pk_mul_f16 v111, v111, v189\n"
        "v_cvt_pk_f16_f32 v184, v152, v153\n"
        "v_mfma_f32_16x16x32_f16 v[70:73], v[106:109], v[144:147], v[70:73]\n"
        "v_cvt_pk_f16_f32 v185, v154, v155\n"
        "v_cvt_pk_f16_f32 v182, v64, v65\n"
        "v_mfma_f32_16x16x32_f16 v[70:73], v[102:105], v[140:143], v[70:73]\n"
        "v_cvt_pk_f16_f32 v183, v66, v67\n"
        "v_cvt_pk_f16_f32 v180, v242, v243\n"
        "v_mfma_f32_16x16x32_f16 v[64:67], v[102:105], v[156:159], 0\n"
        "v_cvt_pk_f16_f32 v181, v244, v245\n"
        "v_pk_mul_f16 v112, v112, v190\n"
        "v_mfma_f32_16x16x32_f16 v[64:67], v[106:109], v[160:163], v[64:67]\n"
        "v_pk_mul_f16 v113, v113, v191\n"
        "v_pk_mul_f16 v114, v114, v192\n"
        "v_mfma_f32_16x16x32_bf16 v[64:67], v[198:201], v[94:97], v[64:67]\n"
        "v_pk_mul_f16 v115, v115, v193\n"
        "v_cvt_pk_bf16_f32 v152, v226, v70\n"
        "v_mfma_f32_16x16x32_bf16 v[148:151], v[198:201], v[98:101], 0\n"
        "v_cvt_pk_bf16_f32 v194, v227, v71\n"
        "v_cvt_f16_f32 v60, v61\n"
        "v_mfma_f32_16x16x32_f16 v[148:151], v[106:109], v[168:171], v[148:151]\n"
        "v_cvt_pk_bf16_f32 v198, v228, v72\n"
        "v_pk_mul_f16 v120, v120, v60 op_sel_hi:[1,0]\n"
        "v_mfma_f32_16x16x32_f16 v[148:151], v[102:105], v[164:167], v[148:151]\n"
        "v_cvt_pk_bf16_f32 v226, v229, v73\n"
        "v_pk_mul_f16 v121, v121, v60 op_sel_hi:[1,0]\n"
        "v_mfma_f32_16x16x32_f16 v[70:73], v[102:105], v[172:175], 0\n"
        "s_nop 0\n"
        "v_pk_mul_f16 v122, v122, v60 op_sel_hi:[1,0]\n"
        "v_pk_mul_f16 v123, v123, v60 op_sel_hi:[1,0]\n"
        "v_mfma_f32_16x16x32_f16 v[70:73], v[106:109], v[176:179], v[70:73]\n"
        "s_nop 0\n"
        "v_cvt_pk_bf16_f32 v153, v64, v148\n"
        "v_cvt_pk_bf16_f32 v195, v65, v149\n"
        "v_mfma_f32_16x16x32_bf16 v[70:73], v[202:205], v[94:97], v[70:73]\n"
        "v_cvt_pk_bf16_f32 v199, v66, v150\n"
        "v_cvt_pk_bf16_f32 v227, v67, v151\n"
        "v_mfma_f32_16x16x32_bf16 v[64:67], v[202:205], v[98:101], 0\n"
        "v_pk_mul_f16 v124, v124, v60 op_sel_hi:[1,0]\n"
        "v_pk_mul_f16 v125, v125, v60 op_sel_hi:[1,0]\n"
        "v_mfma_f32_16x16x32_f16 v[64:67], v[106:109], v[184:187], v[64:67]\n"
        "v_pk_mul_f16 v126, v126, v60 op_sel_hi:[1,0]\n"
        "v_pk_mul_f16 v120, v120, v188\n"
        "v_mfma_f32_16x16x32_f16 v[64:67], v[102:105], v[180:183], v[64:67]\n"
        "v_pk_mul_f16 v121, v121, v189\n"
        "v_pk_mul_f16 v122, v122, v190\n"
        "v_mfma_f32_16x16x32_f16 v[102:105], v[110:113], v[130:133], 0\n"
        "s_nop 0\n"
        "v_pk_mul_f16 v123, v123, v191\n"
        "v_pk_mul_f16 v124, v124, v192\n"
        "v_mfma_f32_16x16x32_f16 v[102:105], v[114:117], v[134:137], v[102:105]\n"
        "s_add_u32 s90, s89, 0\n"
        "v_cvt_pk_bf16_f32 v154, v70, v64\n"
        "v_cvt_pk_bf16_f32 v196, v71, v65\n"
        "v_mfma_f32_16x16x32_bf16 v[102:105], v[206:209], v[94:97], v[102:105]\n"
        "v_cvt_pk_bf16_f32 v200, v72, v66\n"
        "v_cvt_pk_bf16_f32 v228, v73, v67\n"
        "v_mfma_f32_16x16x32_bf16 v[64:67], v[206:209], v[98:101], 0\n"
        "v_pk_mul_f16 v125, v125, v193\n"
        "v_mfma_f32_16x16x32_f16 v[64:67], v[114:117], v[144:147], v[64:67]\n"
        "v_mfma_f32_16x16x32_f16 v[64:67], v[110:113], v[140:143], v[64:67]\n"
        "s_add_u32 s91, s89, 98112\n"
        "v_mfma_f32_16x16x32_f16 v[70:73], v[110:113], v[156:159], 0\n"
        "buffer_store_dwordx3 v[152:154], v14, s[40:43], s90 offen sc1 nt\n"
        "s_nop 2\n"
        "v_mfma_f32_16x16x32_f16 v[70:73], v[114:117], v[160:163], v[70:73]\n"
        "s_nop 0\n"
        "v_cvt_pk_bf16_f32 v148, v102, v64\n"
        "v_cvt_pk_bf16_f32 v152, v103, v65\n"
        "v_mfma_f32_16x16x32_bf16 v[70:73], v[210:213], v[94:97], v[70:73]\n"
        "buffer_store_dwordx3 v[194:196], v15, s[40:43], s90 offen offset:768 sc1 nt\n"
        "v_cvt_pk_bf16_f32 v202, v104, v66\n"
        "v_cvt_pk_bf16_f32 v206, v105, v67\n"
        "v_mfma_f32_16x16x32_bf16 v[64:67], v[210:213], v[98:101], 0\n"
        "v_mfma_f32_16x16x32_f16 v[64:67], v[114:117], v[168:171], v[64:67]\n"
        "buffer_store_dwordx3 v[198:200], v14, s[40:43], s91 offen offset:192 sc1 nt\n"
        "v_mfma_f32_16x16x32_f16 v[64:67], v[110:113], v[164:167], v[64:67]\n"
        "s_nop 0\n"
        "v_mfma_f32_16x16x32_f16 v[102:105], v[110:113], v[172:175], 0\n"
        "buffer_store_dwordx3 v[226:228], v16, s[40:43], s91 offen offset:960 sc1 nt\n"
        "s_nop 2\n"
        "v_mfma_f32_16x16x32_f16 v[102:105], v[114:117], v[176:179], v[102:105]\n"
        "s_nop 0\n"
        "v_cvt_pk_bf16_f32 v149, v70, v64\n"
        "v_cvt_pk_bf16_f32 v153, v71, v65\n"
        "v_mfma_f32_16x16x32_bf16 v[102:105], v[214:217], v[94:97], v[102:105]\n"
        "v_cvt_pk_bf16_f32 v203, v72, v66\n"
        "v_cvt_pk_bf16_f32 v207, v73, v67\n"
        "v_mfma_f32_16x16x32_bf16 v[64:67], v[214:217], v[98:101], 0\n"
        "v_mfma_f32_16x16x32_f16 v[64:67], v[114:117], v[184:187], v[64:67]\n"
        "v_mfma_f32_16x16x32_f16 v[64:67], v[110:113], v[180:183], v[64:67]\n"
        "s_nop 1\n"
        "v_mfma_f32_16x16x32_f16 v[70:73], v[120:123], v[130:133], 0\n"
        "s_nop 2\n"
        "v_mfma_f32_16x16x32_f16 v[70:73], v[124:127], v[134:137], v[70:73]\n"
        "s_add_u32 s90, s89, 196224\n"
        "v_cvt_pk_bf16_f32 v150, v102, v64\n"
        "v_cvt_pk_bf16_f32 v154, v103, v65\n"
        "v_mfma_f32_16x16x32_bf16 v[70:73], v[218:221], v[94:97], v[70:73]\n"
        "v_cvt_pk_bf16_f32 v204, v104, v66\n"
        "v_cvt_pk_bf16_f32 v208, v105, v67\n"
        "v_mfma_f32_16x16x32_bf16 v[64:67], v[218:221], v[98:101], 0\n"
        "ds_read_b128 v[188:191], v20 offset:512\n"
        "v_mfma_f32_16x16x32_f16 v[64:67], v[124:127], v[144:147], v[64:67]\n"
        "v_mfma_f32_16x16x32_f16 v[64:67], v[120:123], v[140:143], v[64:67]\n"
        "s_add_u32 s91, s89, 294336\n"
        "ds_read_b128 v[192:195], v20 offset:528\n"
        "v_mfma_f32_16x16x32_f16 v[106:109], v[120:123], v[156:159], 0\n"
        "buffer_store_dwordx3 v[148:150], v14, s[40:43], s90 offen offset:384 sc1 nt\n"
        "s_nop 1\n"
        "v_mfma_f32_16x16x32_f16 v[106:109], v[124:127], v[160:163], v[106:109]\n"
        "s_nop 0\n"
        "v_cvt_pk_bf16_f32 v210, v70, v64\n"
        "v_cvt_pk_bf16_f32 v214, v71, v65\n"
        "v_mfma_f32_16x16x32_bf16 v[106:109], v[222:225], v[94:97], v[106:109]\n"
        "buffer_store_dwordx3 v[152:154], v17, s[40:43], s90 offen offset:1152 sc1 nt\n"
        "v_cvt_pk_bf16_f32 v218, v72, v66\n"
        "v_cvt_pk_bf16_f32 v226, v73, v67\n"
        "v_mfma_f32_16x16x32_bf16 v[64:67], v[222:225], v[98:101], 0\n"
        "s_add_u32 s90, s89, 392448\n"
        "v_mfma_f32_16x16x32_f16 v[64:67], v[124:127], v[168:171], v[64:67]\n"
        "buffer_store_dwordx3 v[202:204], v14, s[40:43], s91 offen offset:576 sc1 nt\n"
        "v_mfma_f32_16x16x32_f16 v[64:67], v[120:123], v[164:167], v[64:67]\n"
        "s_nop 0\n"
        "v_mfma_f32_16x16x32_f16 v[70:73], v[120:123], v[172:175], 0\n"
        "buffer_store_dwordx3 v[206:208], v14, s[40:43], s90 offen sc1 nt\n"
        "v_lshlrev_b32 v148, 16, v136\n"
        "s_nop 1\n"
        "v_mfma_f32_16x16x32_f16 v[70:73], v[124:127], v[176:179], v[70:73]\n"
        "s_waitcnt vmcnt(8)\n"
        "v_cvt_pk_bf16_f32 v211, v106, v64\n"
        "v_cvt_pk_bf16_f32 v215, v107, v65\n"
        "v_mfma_f32_16x16x32_bf16 v[70:73], v[230:233], v[94:97], v[70:73]\n"
        "s_waitcnt lgkmcnt(0)\n"
        "ds_read_b128 v[104:107], v9\n"
        "v_lshlrev_b32 v64, 16, v146\n"
        "v_mfma_f32_16x16x32_bf16 v[196:199], v[230:233], v[98:101], 0\n"
        "ds_read_b128 v[110:113], v10\n"
        "v_cvt_pk_bf16_f32 v219, v108, v66\n"
        "v_mfma_f32_16x16x32_f16 v[196:199], v[124:127], v[184:187], v[196:199]\n"
        "ds_read_b128 v[116:119], v11\n"
        "v_lshlrev_b32 v200, 16, v162\n"
        "v_mfma_f32_16x16x32_f16 v[196:199], v[120:123], v[180:183], v[196:199]\n"
        "ds_read_b128 v[124:127], v9 offset:3072\n"
        "v_cvt_pk_bf16_f32 v227, v109, v67\n"
        "v_mfma_f32_16x16x32_f16 v[148:151], v[188:191], v[130:133], v[148:151]\n"
        "ds_read_b128 v[130:133], v10 offset:3072\n"
        "v_lshlrev_b32 v120, 16, v170\n"
        "v_mfma_f32_16x16x32_f16 v[148:151], v[192:195], v[134:137], v[148:151]\n"
        "s_nop 0\n"
        "ds_read_b128 v[136:139], v11 offset:3072\n"
        "v_cvt_pk_bf16_f32 v212, v70, v196\n"
        "v_mfma_f32_16x16x32_f16 v[64:67], v[192:195], v[144:147], v[64:67]\n"
        "ds_read_b128 v[144:147], v9 offset:6144\n"
        "v_lshlrev_b32 v70, 16, v178\n"
        "v_mfma_f32_16x16x32_f16 v[64:67], v[188:191], v[140:143], v[64:67]\n"
        "s_add_u32 s91, s89, 490560\n"
        "ds_read_b128 v[150:153], v10 offset:6144\n"
        "v_cvt_pk_bf16_f32 v216, v71, v197\n"
        "v_mfma_f32_16x16x32_f16 v[200:203], v[188:191], v[156:159], v[200:203]\n"
        "ds_read_b128 v[156:159], v11 offset:6144\n"
        "v_lshlrev_b32 v140, 16, v186\n"
        "v_mfma_f32_16x16x32_f16 v[200:203], v[192:195], v[160:163], v[200:203]\n"
        "v_cvt_pk_bf16_f32 v220, v72, v198\n"
        "v_cvt_pk_bf16_f32 v228, v73, v199\n"
        "v_mfma_f32_16x16x32_f16 v[120:123], v[192:195], v[168:171], v[120:123]\n"
        "buffer_store_dwordx3 v[210:212], v15, s[40:43], s90 offen offset:768 sc1 nt\n"
        "v_cvt_pk_bf16_f32 v160, v148, v64\n"
        "v_mfma_f32_16x16x32_f16 v[120:123], v[188:191], v[164:167], v[120:123]\n"
        "s_add_u32 s90, s89, 588672\n"
        "v_mfma_f32_16x16x32_f16 v[70:73], v[188:191], v[172:175], v[70:73]\n"
        "buffer_store_dwordx3 v[214:216], v14, s[40:43], s91 offen offset:192 sc1 nt\n"
        "s_nop 2\n"
        "v_mfma_f32_16x16x32_f16 v[70:73], v[192:195], v[176:179], v[70:73]\n"
        "s_nop 0\n"
        "v_cvt_pk_bf16_f32 v161, v200, v120\n"
        "v_mfma_f32_16x16x32_f16 v[140:143], v[192:195], v[184:187], v[140:143]\n"
        "buffer_store_dwordx3 v[218:220], v16, s[40:43], s91 offen offset:960 sc1 nt\n"
        "v_mfma_f32_16x16x32_f16 v[140:143], v[188:191], v[180:183], v[140:143]\n"
        "s_add_u32 s46, s89, 590016\n"
        "s_nop 1\n"
        "buffer_store_dwordx3 v[226:228], v14, s[40:43], s90 offen offset:384 sc1 nt\n"
        "s_min_u32 s46, s46, s42\n"
        "s_nop 2\n"
        "v_cvt_pk_bf16_f32 v162, v70, v140\n"
        "buffer_store_dwordx3 v[160:162], v17, s[44:47], s90 offen offset:1152 sc1 nt\n"
        "s_cmp_ge_i32 s85, 0\n"
        "s_cbranch_scc1 TOP_%=\n"
        "LAST_%=:\n"
        "s_waitcnt lgkmcnt(0)\n"
        "s_nop 7\n"
        "v_add_u32 v23, 784, v23\n"
        "ds_read_b128 v[64:67], v23\n"
        "ds_read_b128 v[70:73], v23 offset:64\n"
        "ds_read_b128 v[102:105], v23 offset:128\n"
        "v_add_u32 v24, 784, v24\n"
        "ds_read_b64 v[60:61], v24 offset:360\n"
        "s_waitcnt lgkmcnt(0)\n"
        "ds_read_u16 v76, v19 offset:3072\n"
        "s_waitcnt lgkmcnt(0)\n"
        "s_mov_b32 s30, s29\n"
        "v_cmp_ne_u32_e64 s[24:25], s30, v76\n"
        "s_nop 3\n"
        "s_cmp_lg_u64 s[24:25], 0\n"
        "s_cbranch_scc1 SP17_%=\n"
        "BK17_%=:\n"
        "ds_read_b128 v[194:197], v13\n"
        "ds_read_b128 v[198:201], v13 offset:1024\n"
        "ds_read_b128 v[202:205], v13 offset:2048\n"
        "s_waitcnt lgkmcnt(0)\n"
        "v_mfma_f32_16x16x32_bf16 v[106:109], v[64:67], v[194:197], 0\n"
        "s_mov_b32 s26, s89\n"
        "ds_read_b128 v[206:209], v13 offset:3072\n"
        "ds_read_b128 v[210:213], v13 offset:4096\n"
        "v_mfma_f32_16x16x32_bf16 v[106:109], v[70:73], v[198:201], v[106:109]\n"
        "s_add_u32 s89, s86, s51\n"
        "ds_read_b128 v[214:217], v13 offset:5120\n"
        "ds_read_b128 v[218:221], v13 offset:6144\n"
        "v_mfma_f32_16x16x32_bf16 v[106:109], v[102:105], v[202:205], v[106:109]\n"
        "s_add_u32 s29, s29, 1\n"
        "ds_read_b128 v[222:225], v13 offset:7168\n"
        "ds_read_b128 v[230:233], v13 offset:8192\n"
        "s_waitcnt lgkmcnt(3)\n"
        "v_mfma_f32_16x16x32_bf16 v[110:113], v[102:105], v[214:217], 0\n"
        "s_mov_b32 s86, s87\n"
        "ds_read_b128 v[174:177], v20\n"
        "ds_read_b64 v[178:179], v20 offset:16\n"
        "v_mfma_f32_16x16x32_bf16 v[110:113], v[70:73], v[210:213], v[110:113]\n"
        "s_add_u32 s88, s88, 32\n"
        "ds_read_u16 v108, v22\n"
        "ds_read_b64 v[76:77], v21\n"
        "v_mfma_f32_16x16x32_bf16 v[110:113], v[64:67], v[206:209], v[110:113]\n"
        "s_add_u32 s87, s87, 43008\n"
        "v_readlane_b32 s93, v61, 1\n"
        "v_cndmask_b32_e64 v60, 0, v60, s[72:73]\n"
        "s_waitcnt lgkmcnt(0)\n"
        "v_mfma_f32_16x16x32_bf16 v[114:117], v[64:67], v[218:221], 0\n"
        "s_cmp_ge_u32 s88, 73\n"
        "v_mfma_f32_16x16x32_bf16 v[114:117], v[70:73], v[222:225], v[114:117]\n"
        "s_cselect_b32 s92, 588672, 0\n"
        "v_cndmask_b32_e64 v61, v106, v110, s[64:65]\n"
        "v_cndmask_b32_e64 v170, v107, v111, s[64:65]\n"
        "v_mfma_f32_16x16x32_bf16 v[114:117], v[102:105], v[230:233], v[114:117]\n"
        "s_cselect_b32 s94, 73, 0\n"
        "s_nop 0\n"
        "v_mfma_f32_16x16x32_bf16 v[64:67], a[152:155], v[202:205], 0\n"
        "s_sub_u32 s88, s88, s94\n"
        "s_nop 1\n"
        "v_mfma_f32_16x16x32_bf16 v[64:67], a[148:151], v[198:201], v[64:67]\n"
        "s_add_u32 s87, s87, s92\n"
        "v_cndmask_b32_e64 v61, v61, v114, s[70:71]\n"
        "v_cndmask_b32_e64 v170, v170, v115, s[70:71]\n"
        "v_mfma_f32_16x16x32_bf16 v[64:67], a[144:147], v[194:197], v[64:67]\n"
        "s_cmp_gt_i32 s85, 0\n"
        "v_cvt_pk_f16_f32 v61, v61, v170\n"
        "v_pk_mul_f16 v61, v61, v108 op_sel_hi:[1,0]\n"
        "v_mfma_f32_16x16x32_bf16 v[70:73], a[156:159], v[194:197], 0\n"
        "s_cselect_b32 s94, s87, s84\n"
        "v_pk_mul_f16 v61, v61, s93 op_sel_hi:[1,0]\n"
        "v_mfma_f32_16x16x32_bf16 v[70:73], a[160:163], v[198:201], v[70:73]\n"
        "s_sub_u32 s85, s85, 1\n"
        "v_cvt_pk_bf16_f32 v112, v64, v65\n"
        "v_cvt_pk_bf16_f32 v113, v66, v67\n"
        "v_mfma_f32_16x16x32_bf16 v[70:73], a[164:167], v[202:205], v[70:73]\n"
        "s_nop 0\n"
        "v_exp_f16_sdwa v61, v61 dst_sel:WORD_0 dst_unused:UNUSED_PRESERVE src0_sel:WORD_0\n"
        "v_mfma_f32_16x16x32_bf16 v[64:67], a[176:179], v[202:205], 0\n"
        "s_nop 0\n"
        "v_exp_f16_sdwa v61, v61 dst_sel:WORD_1 dst_unused:UNUSED_PRESERVE src0_sel:WORD_1\n"
        "v_mfma_f32_16x16x32_bf16 v[64:67], a[172:175], v[198:201], v[64:67]\n"
        "s_nop 0\n"
        "v_cvt_f32_f16 v103, v61\n"
        "v_cvt_pk_bf16_f32 v114, v70, v71\n"
        "v_mfma_f32_16x16x32_bf16 v[64:67], a[168:171], v[194:197], v[64:67]\n"
        "v_cvt_pk_bf16_f32 v115, v72, v73\n"
        "v_cndmask_b32_e64 v103, v103, v60, s[68:69]\n"
        "v_mfma_f32_16x16x32_bf16 v[70:73], a[180:183], v[194:197], 0\n"
        "s_nop 0\n"
        "v_add_f32_dpp v60, v103, v103 row_ror:8 row_mask:0xf bank_mask:0xf\n"
        "v_and_b32 v170, 0xffff0000, v61\n"
        "v_mfma_f32_16x16x32_bf16 v[70:73], a[184:187], v[198:201], v[70:73]\n"
        "s_nop 0\n"
        "v_cvt_pk_bf16_f32 v116, v64, v65\n"
        "v_cvt_pk_bf16_f32 v117, v66, v67\n"
        "v_mfma_f32_16x16x32_bf16 v[70:73], a[188:191], v[202:205], v[70:73]\n"
        "s_nop 0\n"
        "v_add_f32_dpp v60, v60, v60 row_ror:4 row_mask:0xf bank_mask:0xf\n"
        "v_mfma_f32_16x16x32_bf16 v[64:67], a[200:203], v[202:205], 0\n"
        "s_nop 0\n"
        "v_add_f32_dpp v60, v60, v60 row_ror:2 row_mask:0xf bank_mask:0xf\n"
        "s_nop 0\n"
        "v_mfma_f32_16x16x32_bf16 v[64:67], a[196:199], v[198:201], v[64:67]\n"
        "s_nop 0\n"
        "v_cvt_pk_bf16_f32 v118, v70, v71\n"
        "v_cvt_pk_bf16_f32 v119, v72, v73\n"
        "v_mfma_f32_16x16x32_bf16 v[64:67], a[192:195], v[194:197], v[64:67]\n"
        "s_nop 0\n"
        "v_add_f32_dpp v60, v60, v60 row_ror:1 row_mask:0xf bank_mask:0xf\n"
        "v_mfma_f32_16x16x32_bf16 v[70:73], a[204:207], v[194:197], 0\n"
        "s_nop 0\n"
        "v_add_f32_dpp v60, v60, v60 row_bcast:15 row_mask:0xa bank_mask:0xf\n"
        "s_nop 0\n"
        "v_mfma_f32_16x16x32_bf16 v[70:73], a[208:211], v[198:201], v[70:73]\n"
        "s_nop 0\n"
        "v_cvt_pk_bf16_f32 v120, v64, v65\n"
        "v_cvt_pk_bf16_f32 v121, v66, v67\n"
        "v_mfma_f32_16x16x32_bf16 v[70:73], a[212:215], v[202:205], v[70:73]\n"
        "s_nop 0\n"
        "v_add_f32_dpp v60, v60, v60 row_bcast:31 row_mask:0xc bank_mask:0xf\n"
        "v_mfma_f32_16x16x32_bf16 v[64:67], v[202:205], a[224:227], 0\n"
        "s_nop 0\n"
        "v_readlane_b32 s95, v60, 63\n"
        "s_nop 0\n"
        "v_mfma_f32_16x16x32_bf16 v[64:67], v[198:201], a[220:223], v[64:67]\n"
        "s_nop 0\n"
        "v_cvt_pk_bf16_f32 v122, v70, v71\n"
        "v_cvt_pk_bf16_f32 v123, v72, v73\n"
        "v_mfma_f32_16x16x32_bf16 v[64:67], v[194:197], a[216:219], v[64:67]\n"
        "v_rcp_f32 v60, s95\n"
        "v_mfma_f32_16x16x32_bf16 v[70:73], v[206:209], a[216:219], 0\n"
        "s_nop 0\n"
        "v_mul_f32 v103, v103, v60\n"
        "v_cvt_f16_f32 v103, v103\n"
        "v_mfma_f32_16x16x32_bf16 v[70:73], v[210:213], a[220:223], v[70:73]\n"
        "s_nop 0\n"
        "v_mul_f16 v103, v103, v108\n"
        "v_cvt_pk_f16_f32 v102, v64, v65\n"
        "v_mfma_f32_16x16x32_bf16 v[70:73], v[214:217], a[224:227], v[70:73]\n"
        "s_nop 0\n"
        "ds_write_b16 v22, v103 offset:512\n"
        "v_mfma_f32_16x16x32_bf16 v[108:111], v[230:233], a[224:227], 0\n"
        "s_nop 0\n"
        "v_cvt_pk_f16_f32 v103, v66, v67\n"
        "s_nop 0\n"
        "v_mfma_f32_16x16x32_bf16 v[108:111], v[222:225], a[220:223], v[108:111]\n"
        "s_nop 0\n"
        "v_cvt_pk_f16_f32 v104, v70, v71\n"
        "v_cvt_pk_f16_f32 v105, v72, v73\n"
        "v_mfma_f32_16x16x32_bf16 v[108:111], v[218:221], a[216:219], v[108:111]\n"
        "s_nop 1\n"
        "v_mfma_f32_16x16x32_bf16 v[64:67], v[194:197], v[112:115], 0\n"
        "s_nop 2\n"
        "v_mfma_f32_16x16x32_bf16 v[64:67], v[198:201], v[116:119], v[64:67]\n"
        "s_nop 0\n"
        "v_cvt_pk_f16_f32 v106, v108, v109\n"
        "v_cvt_pk_f16_f32 v107, v110, v111\n"
        "v_mfma_f32_16x16x32_bf16 v[64:67], v[202:205], v[120:123], v[64:67]\n"
        "s_nop 1\n"
        "v_mfma_f32_16x16x32_bf16 v[70:73], v[214:217], v[120:123], 0\n"
        "s_nop 2\n"
        "v_mfma_f32_16x16x32_bf16 v[70:73], v[210:213], v[116:119], v[70:73]\n"
        "s_nop 0\n"
        "v_cvt_pk_f16_f32 v110, v64, v65\n"
        "v_cvt_pk_f16_f32 v111, v66, v67\n"
        "v_mfma_f32_16x16x32_bf16 v[70:73], v[206:209], v[112:115], v[70:73]\n"
        "v_pk_mul_f16 v110, v110, v76 op_sel_hi:[1,0]\n"
        "v_pk_mul_f16 v111, v111, v76 op_sel_hi:[1,0]\n"
        "v_mfma_f32_16x16x32_bf16 v[64:67], v[218:221], v[112:115], 0\n"
        "v_pk_mul_f16 v110, v110, v174\n"
        "v_pk_mul_f16 v111, v111, v175\n"
        "v_mfma_f32_16x16x32_bf16 v[64:67], v[222:225], v[116:119], v[64:67]\n"
        "v_exp_f16_sdwa v110, v110 dst_sel:WORD_0 dst_unused:UNUSED_PRESERVE src0_sel:WORD_0\n"
        "v_mfma_f32_16x16x32_bf16 v[64:67], v[230:233], v[120:123], v[64:67]\n"
        "v_exp_f16_sdwa v111, v111 dst_sel:WORD_0 dst_unused:UNUSED_PRESERVE src0_sel:WORD_0\n"
        "v_mfma_f32_16x16x32_bf16 v[114:117], a[152:155], v[214:217], 0\n"
        "v_exp_f16_sdwa v110, v110 dst_sel:WORD_1 dst_unused:UNUSED_PRESERVE src0_sel:WORD_1\n"
        "v_mfma_f32_16x16x32_bf16 v[114:117], a[148:151], v[210:213], v[114:117]\n"
        "v_exp_f16_sdwa v111, v111 dst_sel:WORD_1 dst_unused:UNUSED_PRESERVE src0_sel:WORD_1\n"
        "v_mfma_f32_16x16x32_bf16 v[114:117], a[144:147], v[206:209], v[114:117]\n"
        "v_cvt_pk_f16_f32 v112, v70, v71\n"
        "v_cvt_pk_f16_f32 v113, v72, v73\n"
        "v_mfma_f32_16x16x32_bf16 v[70:73], a[156:159], v[206:209], 0\n"
        "v_pk_mul_f16 v112, v112, v76 op_sel_hi:[1,0]\n"
        "v_pk_mul_f16 v113, v113, v76 op_sel_hi:[1,0]\n"
        "v_mfma_f32_16x16x32_bf16 v[70:73], a[160:163], v[210:213], v[70:73]\n"
        "v_pk_mul_f16 v112, v112, v176\n"
        "v_pk_mul_f16 v113, v113, v177\n"
        "v_mfma_f32_16x16x32_bf16 v[70:73], a[164:167], v[214:217], v[70:73]\n"
        "s_nop 0\n"
        "v_cvt_pk_bf16_f32 v128, v114, v115\n"
        "v_cvt_pk_bf16_f32 v129, v116, v117\n"
        "v_mfma_f32_16x16x32_bf16 v[114:117], a[176:179], v[214:217], 0\n"
        "s_nop 0\n"
        "v_exp_f16_sdwa v112, v112 dst_sel:WORD_0 dst_unused:UNUSED_PRESERVE src0_sel:WORD_0\n"
        "v_mfma_f32_16x16x32_bf16 v[114:117], a[172:175], v[210:213], v[114:117]\n"
        "s_nop 0\n"
        "v_cvt_pk_bf16_f32 v130, v70, v71\n"
        "v_cvt_pk_bf16_f32 v131, v72, v73\n"
        "v_mfma_f32_16x16x32_bf16 v[114:117], a[168:171], v[206:209], v[114:117]\n"
        "v_exp_f16_sdwa v113, v113 dst_sel:WORD_0 dst_unused:UNUSED_PRESERVE src0_sel:WORD_0\n"
        "v_mfma_f32_16x16x32_bf16 v[70:73], a[180:183], v[206:209], 0\n"
        "v_exp_f16_sdwa v112, v112 dst_sel:WORD_1 dst_unused:UNUSED_PRESERVE src0_sel:WORD_1\n"
        "v_mfma_f32_16x16x32_bf16 v[70:73], a[184:187], v[210:213], v[70:73]\n"
        "s_nop 0\n"
        "v_exp_f16_sdwa v113, v113 dst_sel:WORD_1 dst_unused:UNUSED_PRESERVE src0_sel:WORD_1\n"
        "v_mfma_f32_16x16x32_bf16 v[70:73], a[188:191], v[214:217], v[70:73]\n"
        "s_nop 0\n"
        "v_cvt_pk_bf16_f32 v132, v114, v115\n"
        "v_cvt_pk_bf16_f32 v133, v116, v117\n"
        "v_mfma_f32_16x16x32_bf16 v[116:119], a[200:203], v[214:217], 0\n"
        "v_cvt_pk_f16_f32 v114, v64, v65\n"
        "v_cvt_pk_f16_f32 v115, v66, v67\n"
        "v_mfma_f32_16x16x32_bf16 v[116:119], a[196:199], v[210:213], v[116:119]\n"
        "s_nop 0\n"
        "v_cvt_pk_bf16_f32 v134, v70, v71\n"
        "v_cvt_pk_bf16_f32 v135, v72, v73\n"
        "v_mfma_f32_16x16x32_bf16 v[116:119], a[192:195], v[206:209], v[116:119]\n"
        "v_pk_mul_f16 v114, v114, v76 op_sel_hi:[1,0]\n"
        "v_pk_mul_f16 v115, v115, v76 op_sel_hi:[1,0]\n"
        "v_mfma_f32_16x16x32_bf16 v[64:67], a[204:207], v[206:209], 0\n"
        "s_nop 0\n"
        "v_pk_mul_f16 v114, v114, v178\n"
        "v_pk_mul_f16 v115, v115, v179\n"
        "v_mfma_f32_16x16x32_bf16 v[64:67], a[208:211], v[210:213], v[64:67]\n"
        "s_nop 0\n"
        "v_cvt_pk_bf16_f32 v70, v116, v117\n"
        "v_cvt_pk_bf16_f32 v71, v118, v119\n"
        "v_mfma_f32_16x16x32_bf16 v[64:67], a[212:215], v[214:217], v[64:67]\n"
        "v_exp_f16_sdwa v114, v114 dst_sel:WORD_0 dst_unused:UNUSED_PRESERVE src0_sel:WORD_0\n"
        "v_mfma_f32_16x16x32_bf16 v[116:119], v[230:233], a[236:239], 0\n"
        "v_exp_f16_sdwa v115, v115 dst_sel:WORD_0 dst_unused:UNUSED_PRESERVE src0_sel:WORD_0\n"
        "v_mfma_f32_16x16x32_bf16 v[116:119], v[222:225], a[232:235], v[116:119]\n"
        "s_nop 0\n"
        "v_exp_f16_sdwa v114, v114 dst_sel:WORD_1 dst_unused:UNUSED_PRESERVE src0_sel:WORD_1\n"
        "v_mfma_f32_16x16x32_bf16 v[116:119], v[218:221], a[228:231], v[116:119]\n"
        "s_nop 0\n"
        "v_cvt_pk_bf16_f32 v72, v64, v65\n"
        "v_cvt_pk_bf16_f32 v73, v66, v67\n"
        "v_mfma_f32_16x16x32_bf16 v[64:67], v[206:209], a[228:231], 0\n"
        "s_nop 0\n"
        "v_exp_f16_sdwa v115, v115 dst_sel:WORD_1 dst_unused:UNUSED_PRESERVE src0_sel:WORD_1\n"
        "v_mfma_f32_16x16x32_bf16 v[64:67], v[210:213], a[232:235], v[64:67]\n"
        "s_nop 0\n"
        "v_cvt_pk_f16_f32 v122, v116, v117\n"
        "v_cvt_pk_f16_f32 v123, v118, v119\n"
        "v_mfma_f32_16x16x32_bf16 v[64:67], v[214:217], a[236:239], v[64:67]\n"
        "s_nop 1\n"
        "v_mfma_f32_16x16x32_bf16 v[124:127], v[202:205], a[236:239], 0\n"
        "s_nop 2\n"
        "v_mfma_f32_16x16x32_bf16 v[124:127], v[198:201], a[232:235], v[124:127]\n"
        "s_nop 0\n"
        "v_cvt_pk_f16_f32 v120, v64, v65\n"
        "v_cvt_pk_f16_f32 v121, v66, v67\n"
        "v_mfma_f32_16x16x32_bf16 v[124:127], v[194:197], a[228:231], v[124:127]\n"
        "s_nop 1\n"
        "v_mfma_f32_16x16x32_bf16 v[64:67], v[194:197], v[128:131], 0\n"
        "s_nop 2\n"
        "v_mfma_f32_16x16x32_bf16 v[64:67], v[198:201], v[132:135], v[64:67]\n"
        "s_nop 0\n"
        "v_cvt_pk_f16_f32 v118, v124, v125\n"
        "v_cvt_pk_f16_f32 v119, v126, v127\n"
        "v_mfma_f32_16x16x32_bf16 v[64:67], v[202:205], v[70:73], v[64:67]\n"
        "s_nop 1\n"
        "v_mfma_f32_16x16x32_bf16 v[136:139], v[214:217], v[70:73], 0\n"
        "s_nop 2\n"
        "v_mfma_f32_16x16x32_bf16 v[136:139], v[210:213], v[132:135], v[136:139]\n"
        "s_nop 0\n"
        "v_cvt_pk_f16_f32 v126, v64, v65\n"
        "v_cvt_pk_f16_f32 v127, v66, v67\n"
        "v_mfma_f32_16x16x32_bf16 v[136:139], v[206:209], v[128:131], v[136:139]\n"
        "v_mov_b32 v117, 0\n"
        "v_cndmask_b32_e64 v116, 0, v170, s[62:63]\n"
        "v_mfma_f32_16x16x32_bf16 v[64:67], v[218:221], v[128:131], 0\n"
        "v_pk_mul_f16 v126, v126, v76 op_sel:[0,1]\n"
        "v_pk_mul_f16 v127, v127, v76 op_sel:[0,1]\n"
        "v_mfma_f32_16x16x32_bf16 v[64:67], v[222:225], v[132:135], v[64:67]\n"
        "v_pk_mul_f16 v126, v126, v174\n"
        "v_pk_mul_f16 v127, v127, v175\n"
        "v_mfma_f32_16x16x32_bf16 v[64:67], v[230:233], v[70:73], v[64:67]\n"
        "v_exp_f16_sdwa v126, v126 dst_sel:WORD_0 dst_unused:UNUSED_PRESERVE src0_sel:WORD_0\n"
        "v_mfma_f32_16x16x32_bf16 v[70:73], a[152:155], v[230:233], 0\n"
        "v_exp_f16_sdwa v127, v127 dst_sel:WORD_0 dst_unused:UNUSED_PRESERVE src0_sel:WORD_0\n"
        "v_mfma_f32_16x16x32_bf16 v[70:73], a[148:151], v[222:225], v[70:73]\n"
        "v_exp_f16_sdwa v126, v126 dst_sel:WORD_1 dst_unused:UNUSED_PRESERVE src0_sel:WORD_1\n"
        "v_mfma_f32_16x16x32_bf16 v[70:73], a[144:147], v[218:221], v[70:73]\n"
        "v_exp_f16_sdwa v127, v127 dst_sel:WORD_1 dst_unused:UNUSED_PRESERVE src0_sel:WORD_1\n"
        "v_mfma_f32_16x16x32_bf16 v[132:135], a[156:159], v[218:221], 0\n"
        "v_cvt_pk_f16_f32 v128, v136, v137\n"
        "v_cvt_pk_f16_f32 v129, v138, v139\n"
        "v_mfma_f32_16x16x32_bf16 v[132:135], a[160:163], v[222:225], v[132:135]\n"
        "v_pk_mul_f16 v128, v128, v76 op_sel:[0,1]\n"
        "v_pk_mul_f16 v129, v129, v76 op_sel:[0,1]\n"
        "v_mfma_f32_16x16x32_bf16 v[132:135], a[164:167], v[230:233], v[132:135]\n"
        "v_pk_mul_f16 v128, v128, v176\n"
        "v_pk_mul_f16 v129, v129, v177\n"
        "v_mfma_f32_16x16x32_bf16 v[136:139], a[176:179], v[230:233], 0\n"
        "v_exp_f16_sdwa v128, v128 dst_sel:WORD_0 dst_unused:UNUSED_PRESERVE src0_sel:WORD_0\n"
        "v_mfma_f32_16x16x32_bf16 v[136:139], a[172:175], v[222:225], v[136:139]\n"
        "v_exp_f16_sdwa v129, v129 dst_sel:WORD_0 dst_unused:UNUSED_PRESERVE src0_sel:WORD_0\n"
        "v_mfma_f32_16x16x32_bf16 v[136:139], a[168:171], v[218:221], v[136:139]\n"
        "v_exp_f16_sdwa v128, v128 dst_sel:WORD_1 dst_unused:UNUSED_PRESERVE src0_sel:WORD_1\n"
        "v_mfma_f32_16x16x32_bf16 v[140:143], a[180:183], v[218:221], 0\n"
        "v_exp_f16_sdwa v129, v129 dst_sel:WORD_1 dst_unused:UNUSED_PRESERVE src0_sel:WORD_1\n"
        "v_mfma_f32_16x16x32_bf16 v[140:143], a[184:187], v[222:225], v[140:143]\n"
        "v_cvt_pk_f16_f32 v130, v64, v65\n"
        "v_cvt_pk_f16_f32 v131, v66, v67\n"
        "v_mfma_f32_16x16x32_bf16 v[140:143], a[188:191], v[230:233], v[140:143]\n"
        "v_pk_mul_f16 v130, v130, v76 op_sel:[0,1]\n"
        "v_pk_mul_f16 v131, v131, v76 op_sel:[0,1]\n"
        "v_mfma_f32_16x16x32_bf16 v[64:67], a[200:203], v[230:233], 0\n"
        "v_pk_mul_f16 v130, v130, v178\n"
        "v_pk_mul_f16 v131, v131, v179\n"
        "v_mfma_f32_16x16x32_bf16 v[64:67], a[196:199], v[222:225], v[64:67]\n"
        "v_exp_f16_sdwa v130, v130 dst_sel:WORD_0 dst_unused:UNUSED_PRESERVE src0_sel:WORD_0\n"
        "v_mfma_f32_16x16x32_bf16 v[64:67], a[192:195], v[218:221], v[64:67]\n"
        "v_cvt_pk_bf16_f32 v144, v70, v71\n"
        "v_cvt_pk_bf16_f32 v145, v72, v73\n"
        "v_mfma_f32_16x16x32_bf16 v[70:73], a[204:207], v[218:221], 0\n"
        "v_cvt_pk_bf16_f32 v146, v132, v133\n"
        "v_cvt_pk_bf16_f32 v147, v134, v135\n"
        "v_mfma_f32_16x16x32_bf16 v[70:73], a[208:211], v[222:225], v[70:73]\n"
        "v_cvt_pk_bf16_f32 v148, v136, v137\n"
        "v_cvt_pk_bf16_f32 v149, v138, v139\n"
        "v_mfma_f32_16x16x32_bf16 v[70:73], a[212:215], v[230:233], v[70:73]\n"
        "v_cvt_pk_bf16_f32 v150, v140, v141\n"
        "v_cvt_pk_bf16_f32 v151, v142, v143\n"
        "v_mfma_f32_16x16x32_f16 v[132:135], v[90:93], v[110:113], 0\n"
        "s_nop 0\n"
        "v_cvt_pk_bf16_f32 v136, v64, v65\n"
        "v_cvt_pk_bf16_f32 v137, v66, v67\n"
        "v_mfma_f32_16x16x32_f16 v[132:135], v[90:93], v[114:117], v[132:135]\n"
        "s_nop 0\n"
        "v_cvt_pk_bf16_f32 v138, v70, v71\n"
        "v_cvt_pk_bf16_f32 v139, v72, v73\n"
        "v_mfma_f32_16x16x32_bf16 v[64:67], v[202:205], a[248:251], 0\n"
        "v_exp_f16_sdwa v131, v131 dst_sel:WORD_0 dst_unused:UNUSED_PRESERVE src0_sel:WORD_0\n"
        "v_mfma_f32_16x16x32_bf16 v[64:67], v[198:201], a[244:247], v[64:67]\n"
        "v_exp_f16_sdwa v130, v130 dst_sel:WORD_1 dst_unused:UNUSED_PRESERVE src0_sel:WORD_1\n"
        "v_mfma_f32_16x16x32_bf16 v[64:67], v[194:197], a[240:243], v[64:67]\n"
        "v_exp_f16_sdwa v131, v131 dst_sel:WORD_1 dst_unused:UNUSED_PRESERVE src0_sel:WORD_1\n"
        "v_mfma_f32_16x16x32_bf16 v[70:73], v[206:209], a[240:243], 0\n"
        "v_rcp_f32 v61, v132\n"
        "v_mfma_f32_16x16x32_bf16 v[70:73], v[210:213], a[244:247], v[70:73]\n"
        "v_max_f32 v76, s95, v132\n"
        "v_cvt_f16_f32 v108, v61\n"
        "v_mfma_f32_16x16x32_bf16 v[70:73], v[214:217], a[248:251], v[70:73]\n"
        "v_mov_b32 v133, 0\n"
        "v_cndmask_b32_e64 v132, 0, v170, s[64:65]\n"
        "v_mfma_f32_16x16x32_bf16 v[152:155], v[230:233], a[248:251], 0\n"
        "v_add_f32 v171, v60, v61\n"
        "v_pk_mul_f16 v110, v110, v108 op_sel_hi:[1,0]\n"
        "v_mfma_f32_16x16x32_bf16 v[152:155], v[222:225], a[244:247], v[152:155]\n"
        "v_pk_mul_f16 v111, v111, v108 op_sel_hi:[1,0]\n"
        "v_pk_mul_f16 v112, v112, v108 op_sel_hi:[1,0]\n"
        "v_mfma_f32_16x16x32_bf16 v[152:155], v[218:221], a[240:243], v[152:155]\n"
        "v_pk_mul_f16 v113, v113, v108 op_sel_hi:[1,0]\n"
        "v_pk_mul_f16 v114, v114, v108 op_sel_hi:[1,0]\n"
        "v_mfma_f32_16x16x32_bf16 v[140:143], v[194:197], v[144:147], 0\n"
        "v_pk_mul_f16 v115, v115, v108 op_sel_hi:[1,0]\n"
        "v_pk_mul_f16 v116, v116, v108 op_sel_hi:[1,0]\n"
        "v_mfma_f32_16x16x32_bf16 v[140:143], v[198:201], v[148:151], v[140:143]\n"
        "v_pk_mul_f16 v110, v110, v174\n"
        "v_pk_mul_f16 v111, v111, v175\n"
        "v_mfma_f32_16x16x32_bf16 v[140:143], v[202:205], v[136:139], v[140:143]\n"
        "v_pk_mul_f16 v112, v112, v176\n"
        "v_pk_mul_f16 v113, v113, v177\n"
        "v_mfma_f32_16x16x32_bf16 v[156:159], v[214:217], v[136:139], 0\n"
        "s_nop 0\n"
        "v_pk_mul_f16 v114, v114, v178\n"
        "v_pk_mul_f16 v115, v115, v179\n"
        "v_mfma_f32_16x16x32_bf16 v[156:159], v[210:213], v[148:151], v[156:159]\n"
        "s_nop 0\n"
        "v_cvt_pk_f16_f32 v134, v140, v141\n"
        "v_cvt_pk_f16_f32 v135, v142, v143\n"
        "v_mfma_f32_16x16x32_bf16 v[156:159], v[206:209], v[144:147], v[156:159]\n"
        "v_pk_mul_f16 v134, v134, v77 op_sel_hi:[1,0]\n"
        "v_pk_mul_f16 v135, v135, v77 op_sel_hi:[1,0]\n"
        "v_mfma_f32_16x16x32_bf16 v[160:163], v[218:221], v[144:147], 0\n"
        "v_pk_mul_f16 v134, v134, v174\n"
        "v_pk_mul_f16 v135, v135, v175\n"
        "v_mfma_f32_16x16x32_bf16 v[160:163], v[222:225], v[148:151], v[160:163]\n"
        "v_exp_f16_sdwa v134, v134 dst_sel:WORD_0 dst_unused:UNUSED_PRESERVE src0_sel:WORD_0\n"
        "v_mfma_f32_16x16x32_bf16 v[160:163], v[230:233], v[136:139], v[160:163]\n"
        "v_exp_f16_sdwa v135, v135 dst_sel:WORD_0 dst_unused:UNUSED_PRESERVE src0_sel:WORD_0\n"
        "v_mfma_f32_16x16x32_bf16 v[166:169], v[230:233], v[32:35], 0\n"
        "v_exp_f16_sdwa v134, v134 dst_sel:WORD_1 dst_unused:UNUSED_PRESERVE src0_sel:WORD_1\n"
        "v_mfma_f32_16x16x32_bf16 v[166:169], v[222:225], v[28:31], v[166:169]\n"
        "v_exp_f16_sdwa v135, v135 dst_sel:WORD_1 dst_unused:UNUSED_PRESERVE src0_sel:WORD_1\n"
        "v_mfma_f32_16x16x32_bf16 v[166:169], v[218:221], a[252:255], v[166:169]\n"
        "v_cvt_pk_f16_f32 v136, v156, v157\n"
        "v_cvt_pk_f16_f32 v137, v158, v159\n"
        "v_mfma_f32_16x16x32_bf16 v[180:183], v[206:209], a[252:255], 0\n"
        "v_pk_mul_f16 v136, v136, v77 op_sel_hi:[1,0]\n"
        "v_pk_mul_f16 v137, v137, v77 op_sel_hi:[1,0]\n"
        "v_mfma_f32_16x16x32_bf16 v[180:183], v[210:213], v[28:31], v[180:183]\n"
        "v_pk_mul_f16 v136, v136, v176\n"
        "v_pk_mul_f16 v137, v137, v177\n"
        "v_mfma_f32_16x16x32_bf16 v[180:183], v[214:217], v[32:35], v[180:183]\n"
        "v_exp_f16_sdwa v136, v136 dst_sel:WORD_0 dst_unused:UNUSED_PRESERVE src0_sel:WORD_0\n"
        "v_mfma_f32_16x16x32_bf16 v[184:187], v[202:205], v[32:35], 0\n"
        "v_exp_f16_sdwa v137, v137 dst_sel:WORD_0 dst_unused:UNUSED_PRESERVE src0_sel:WORD_0\n"
        "v_mfma_f32_16x16x32_bf16 v[184:187], v[198:201], v[28:31], v[184:187]\n"
        "ds_read2_b32 v[108:109], v24 offset0:0 offset1:0\n"
        "v_mov_b32 v141, 0\n"
        "v_mfma_f32_16x16x32_bf16 v[184:187], v[194:197], a[252:255], v[184:187]\n"
        "ds_read2_b32 v[124:125], v24 offset0:1 offset1:1\n"
        "ds_read2_b32 v[148:149], v24 offset0:2 offset1:2\n"
        "v_mfma_f32_16x16x32_f16 v[142:145], v[90:93], v[126:129], 0\n"
        "ds_read2_b32 v[156:157], v24 offset0:3 offset1:3\n"
        "ds_read2_b32 v[164:165], v24 offset0:4 offset1:4\n"
        "v_mfma_f32_16x16x32_f16 v[142:145], v[90:93], v[130:133], v[142:145]\n"
        "ds_read2_b32 v[172:173], v24 offset0:5 offset1:5\n"
        "v_cndmask_b32_e64 v140, 0, v170, s[66:67]\n"
        "v_mfma_f32_16x16x32_bf16 v[188:191], v[194:197], v[36:39], 0\n"
        "v_exp_f16_sdwa v136, v136 dst_sel:WORD_1 dst_unused:UNUSED_PRESERVE src0_sel:WORD_1\n"
        "v_mfma_f32_16x16x32_bf16 v[188:191], v[198:201], v[40:43], v[188:191]\n"
        "v_exp_f16_sdwa v137, v137 dst_sel:WORD_1 dst_unused:UNUSED_PRESERVE src0_sel:WORD_1\n"
        "v_mfma_f32_16x16x32_bf16 v[188:191], v[202:205], v[44:47], v[188:191]\n"
        "v_cvt_pk_f16_f32 v138, v160, v161\n"
        "v_cvt_pk_f16_f32 v139, v162, v163\n"
        "v_mfma_f32_16x16x32_bf16 v[226:229], v[214:217], v[44:47], 0\n"
        "v_pk_mul_f16 v138, v138, v77 op_sel_hi:[1,0]\n"
        "v_pk_mul_f16 v139, v139, v77 op_sel_hi:[1,0]\n"
        "v_mfma_f32_16x16x32_bf16 v[226:229], v[210:213], v[40:43], v[226:229]\n"
        "v_pk_mul_f16 v138, v138, v178\n"
        "v_pk_mul_f16 v139, v139, v179\n"
        "v_mfma_f32_16x16x32_bf16 v[226:229], v[206:209], v[36:39], v[226:229]\n"
        "v_exp_f16_sdwa v138, v138 dst_sel:WORD_0 dst_unused:UNUSED_PRESERVE src0_sel:WORD_0\n"
        "v_mfma_f32_16x16x32_bf16 v[234:237], v[218:221], v[36:39], 0\n"
        "v_exp_f16_sdwa v139, v139 dst_sel:WORD_0 dst_unused:UNUSED_PRESERVE src0_sel:WORD_0\n"
        "v_mfma_f32_16x16x32_bf16 v[234:237], v[222:225], v[40:43], v[234:237]\n"
        "s_waitcnt lgkmcnt(0)\n"
        "v_exp_f16_sdwa v138, v138 dst_sel:WORD_1 dst_unused:UNUSED_PRESERVE src0_sel:WORD_1\n"
        "v_mfma_f32_16x16x32_bf16 v[234:237], v[230:233], v[44:47], v[234:237]\n"
        "v_rcp_f32 v60, v142\n"
        "v_mfma_f32_16x16x32_bf16 v[238:241], v[230:233], v[56:59], 0\n"
        "v_max_f32 v76, v76, v142\n"
        "v_add_f32 v171, v171, v60\n"
        "v_mfma_f32_16x16x32_bf16 v[238:241], v[222:225], v[52:55], v[238:241]\n"
        "v_exp_f16_sdwa v139, v139 dst_sel:WORD_1 dst_unused:UNUSED_PRESERVE src0_sel:WORD_1\n"
        "v_mfma_f32_16x16x32_bf16 v[238:241], v[218:221], v[48:51], v[238:241]\n"
        "v_cvt_pk_f16_f32 v142, v64, v65\n"
        "v_cvt_pk_f16_f32 v143, v66, v67\n"
        "v_mfma_f32_16x16x32_bf16 v[64:67], v[206:209], v[48:51], 0\n"
        "v_cvt_pk_f16_f32 v144, v70, v71\n"
        "v_cvt_pk_f16_f32 v145, v72, v73\n"
        "v_mfma_f32_16x16x32_bf16 v[64:67], v[210:213], v[52:55], v[64:67]\n"
        "v_cvt_pk_f16_f32 v146, v152, v153\n"
        "v_cvt_pk_f16_f32 v147, v154, v155\n"
        "v_mfma_f32_16x16x32_bf16 v[64:67], v[214:217], v[56:59], v[64:67]\n"
        "v_cvt_pk_f16_f32 v154, v166, v167\n"
        "v_cvt_pk_f16_f32 v155, v168, v169\n"
        "v_mfma_f32_16x16x32_bf16 v[70:73], v[202:205], v[56:59], 0\n"
        "v_cvt_pk_f16_f32 v152, v180, v181\n"
        "v_cvt_pk_f16_f32 v153, v182, v183\n"
        "v_mfma_f32_16x16x32_bf16 v[70:73], v[198:201], v[52:55], v[70:73]\n"
        "v_cvt_pk_f16_f32 v150, v184, v185\n"
        "v_cvt_pk_f16_f32 v151, v186, v187\n"
        "v_mfma_f32_16x16x32_bf16 v[70:73], v[194:197], v[48:51], v[70:73]\n"
        "v_cvt_f16_f32 v61, v60\n"
        "v_pk_mul_f16 v126, v126, v61 op_sel_hi:[1,0]\n"
        "v_mfma_f32_16x16x32_f16 v[158:161], v[90:93], v[134:137], 0\n"
        "v_pk_mul_f16 v127, v127, v61 op_sel_hi:[1,0]\n"
        "v_pk_mul_f16 v128, v128, v61 op_sel_hi:[1,0]\n"
        "v_mfma_f32_16x16x32_f16 v[158:161], v[90:93], v[138:141], v[158:161]\n"
        "s_nop 0\n"
        "v_pk_mul_f16 v129, v129, v61 op_sel_hi:[1,0]\n"
        "v_pk_mul_f16 v130, v130, v61 op_sel_hi:[1,0]\n"
        "s_nop 4\n"
        "v_max_f32 v76, v76, v158\n"
        "v_rcp_f32 v60, v158\n"
        "s_nop 0\n"
        "v_add_f32 v171, v171, v60\n"
        "v_fma_f32 v76, v171, s82, v76\n"
        "v_cmp_nlt_f32_e64 s[74:75], v76, s83\n"
        "s_nop 3\n"
        "s_cmp_lg_u64 s[74:75], 0\n"
        "s_cbranch_scc1 SLOW_%=\n"
        "v_mfma_f32_16x16x32_f16 v[180:183], v[110:113], v[102:105], 0\n"
        "v_pk_mul_f16 v131, v131, v61 op_sel_hi:[1,0]\n"
        "v_pk_mul_f16 v132, v132, v61 op_sel_hi:[1,0]\n"
        "v_mfma_f32_16x16x32_f16 v[180:183], v[114:117], v[106:109], v[180:183]\n"
        "v_cvt_pk_f16_f32 v158, v188, v189\n"
        "v_cvt_pk_f16_f32 v159, v190, v191\n"
        "v_mfma_f32_16x16x32_bf16 v[180:183], v[194:197], v[94:97], v[180:183]\n"
        "v_cvt_pk_f16_f32 v160, v226, v227\n"
        "v_cvt_pk_f16_f32 v161, v228, v229\n"
        "v_mfma_f32_16x16x32_bf16 v[184:187], v[194:197], v[98:101], 0\n"
        "v_cvt_pk_f16_f32 v162, v234, v235\n"
        "v_cvt_pk_f16_f32 v163, v236, v237\n"
        "v_mfma_f32_16x16x32_f16 v[184:187], v[114:117], v[122:125], v[184:187]\n"
        "v_pk_mul_f16 v126, v126, v174\n"
        "v_pk_mul_f16 v127, v127, v175\n"
        "v_mfma_f32_16x16x32_f16 v[184:187], v[110:113], v[118:121], v[184:187]\n"
        "v_cvt_pk_f16_f32 v170, v238, v239\n"
        "v_cvt_pk_f16_f32 v171, v240, v241\n"
        "v_mfma_f32_16x16x32_f16 v[188:191], v[110:113], v[142:145], 0\n"
        "v_cvt_pk_f16_f32 v168, v64, v65\n"
        "v_cvt_pk_f16_f32 v169, v66, v67\n"
        "v_mfma_f32_16x16x32_f16 v[188:191], v[114:117], v[146:149], v[188:191]\n"
        "v_cvt_pk_f16_f32 v166, v70, v71\n"
        "v_cvt_pk_f16_f32 v167, v72, v73\n"
        "v_mfma_f32_16x16x32_bf16 v[188:191], v[198:201], v[94:97], v[188:191]\n"
        "v_pk_mul_f16 v128, v128, v176\n"
        "v_pk_mul_f16 v129, v129, v177\n"
        "v_mfma_f32_16x16x32_bf16 v[64:67], v[198:201], v[98:101], 0\n"
        "v_pk_mul_f16 v130, v130, v178\n"
        "v_pk_mul_f16 v131, v131, v179\n"
        "v_mfma_f32_16x16x32_f16 v[64:67], v[114:117], v[154:157], v[64:67]\n"
        "v_cvt_pk_bf16_f32 v192, v180, v184\n"
        "v_cvt_pk_bf16_f32 v180, v181, v185\n"
        "v_mfma_f32_16x16x32_f16 v[64:67], v[110:113], v[150:153], v[64:67]\n"
        "v_cvt_f16_f32 v61, v60\n"
        "v_cvt_pk_bf16_f32 v184, v182, v186\n"
        "v_mfma_f32_16x16x32_f16 v[70:73], v[110:113], v[158:161], 0\n"
        "s_nop 0\n"
        "v_pk_mul_f16 v134, v134, v61 op_sel_hi:[1,0]\n"
        "v_cvt_pk_bf16_f32 v196, v183, v187\n"
        "v_mfma_f32_16x16x32_f16 v[70:73], v[114:117], v[162:165], v[70:73]\n"
        "s_nop 0\n"
        "v_cvt_pk_bf16_f32 v193, v188, v64\n"
        "v_cvt_pk_bf16_f32 v181, v189, v65\n"
        "v_mfma_f32_16x16x32_bf16 v[70:73], v[202:205], v[94:97], v[70:73]\n"
        "v_cvt_pk_bf16_f32 v185, v190, v66\n"
        "v_cvt_pk_bf16_f32 v197, v191, v67\n"
        "v_mfma_f32_16x16x32_bf16 v[64:67], v[202:205], v[98:101], 0\n"
        "v_pk_mul_f16 v135, v135, v61 op_sel_hi:[1,0]\n"
        "v_pk_mul_f16 v136, v136, v61 op_sel_hi:[1,0]\n"
        "v_mfma_f32_16x16x32_f16 v[64:67], v[114:117], v[170:173], v[64:67]\n"
        "v_pk_mul_f16 v137, v137, v61 op_sel_hi:[1,0]\n"
        "v_pk_mul_f16 v138, v138, v61 op_sel_hi:[1,0]\n"
        "v_mfma_f32_16x16x32_f16 v[64:67], v[110:113], v[166:169], v[64:67]\n"
        "v_pk_mul_f16 v139, v139, v61 op_sel_hi:[1,0]\n"
        "v_pk_mul_f16 v140, v140, v61 op_sel_hi:[1,0]\n"
        "v_mfma_f32_16x16x32_f16 v[110:113], v[126:129], v[102:105], 0\n"
        "s_nop 0\n"
        "v_pk_mul_f16 v134, v134, v174\n"
        "v_pk_mul_f16 v135, v135, v175\n"
        "v_mfma_f32_16x16x32_f16 v[110:113], v[130:133], v[106:109], v[110:113]\n"
        "s_add_u32 s90, s89, 0\n"
        "v_cvt_pk_bf16_f32 v194, v70, v64\n"
        "v_cvt_pk_bf16_f32 v182, v71, v65\n"
        "v_mfma_f32_16x16x32_bf16 v[110:113], v[206:209], v[94:97], v[110:113]\n"
        "v_cvt_pk_bf16_f32 v186, v72, v66\n"
        "v_cvt_pk_bf16_f32 v198, v73, v67\n"
        "v_mfma_f32_16x16x32_bf16 v[64:67], v[206:209], v[98:101], 0\n"
        "v_pk_mul_f16 v136, v136, v176\n"
        "v_pk_mul_f16 v137, v137, v177\n"
        "v_mfma_f32_16x16x32_f16 v[64:67], v[130:133], v[122:125], v[64:67]\n"
        "v_pk_mul_f16 v138, v138, v178\n"
        "v_pk_mul_f16 v139, v139, v179\n"
        "v_mfma_f32_16x16x32_f16 v[64:67], v[126:129], v[118:121], v[64:67]\n"
        "s_add_u32 s91, s89, 98112\n"
        "v_mfma_f32_16x16x32_f16 v[70:73], v[126:129], v[142:145], 0\n"
        "buffer_store_dwordx3 v[192:194], v14, s[40:43], s90 offen sc1 nt\n"
        "s_nop 2\n"
        "v_mfma_f32_16x16x32_f16 v[70:73], v[130:133], v[146:149], v[70:73]\n"
        "s_nop 0\n"
        "v_cvt_pk_bf16_f32 v174, v110, v64\n"
        "v_cvt_pk_bf16_f32 v178, v111, v65\n"
        "v_mfma_f32_16x16x32_bf16 v[70:73], v[210:213], v[94:97], v[70:73]\n"
        "buffer_store_dwordx3 v[180:182], v15, s[40:43], s90 offen offset:768 sc1 nt\n"
        "v_cvt_pk_bf16_f32 v188, v112, v66\n"
        "v_cvt_pk_bf16_f32 v192, v113, v67\n"
        "v_mfma_f32_16x16x32_bf16 v[64:67], v[210:213], v[98:101], 0\n"
        "v_mfma_f32_16x16x32_f16 v[64:67], v[130:133], v[154:157], v[64:67]\n"
        "buffer_store_dwordx3 v[184:186], v14, s[40:43], s91 offen offset:192 sc1 nt\n"
        "v_mfma_f32_16x16x32_f16 v[64:67], v[126:129], v[150:153], v[64:67]\n"
        "s_nop 0\n"
        "v_mfma_f32_16x16x32_f16 v[110:113], v[126:129], v[158:161], 0\n"
        "buffer_store_dwordx3 v[196:198], v16, s[40:43], s91 offen offset:960 sc1 nt\n"
        "s_nop 2\n"
        "v_mfma_f32_16x16x32_f16 v[110:113], v[130:133], v[162:165], v[110:113]\n"
        "s_nop 0\n"
        "v_cvt_pk_bf16_f32 v175, v70, v64\n"
        "v_cvt_pk_bf16_f32 v179, v71, v65\n"
        "v_mfma_f32_16x16x32_bf16 v[110:113], v[214:217], v[94:97], v[110:113]\n"
        "v_cvt_pk_bf16_f32 v189, v72, v66\n"
        "v_cvt_pk_bf16_f32 v193, v73, v67\n"
        "v_mfma_f32_16x16x32_bf16 v[64:67], v[214:217], v[98:101], 0\n"
        "v_mfma_f32_16x16x32_f16 v[64:67], v[130:133], v[170:173], v[64:67]\n"
        "v_mfma_f32_16x16x32_f16 v[64:67], v[126:129], v[166:169], v[64:67]\n"
        "s_nop 1\n"
        "v_mfma_f32_16x16x32_f16 v[70:73], v[134:137], v[102:105], 0\n"
        "s_nop 2\n"
        "v_mfma_f32_16x16x32_f16 v[70:73], v[138:141], v[106:109], v[70:73]\n"
        "s_add_u32 s90, s89, 196224\n"
        "v_cvt_pk_bf16_f32 v176, v110, v64\n"
        "v_cvt_pk_bf16_f32 v180, v111, v65\n"
        "v_mfma_f32_16x16x32_bf16 v[70:73], v[218:221], v[94:97], v[70:73]\n"
        "v_cvt_pk_bf16_f32 v190, v112, v66\n"
        "v_cvt_pk_bf16_f32 v194, v113, v67\n"
        "v_mfma_f32_16x16x32_bf16 v[64:67], v[218:221], v[98:101], 0\n"
        "ds_read_b128 v[110:113], v20 offset:512\n"
        "v_mfma_f32_16x16x32_f16 v[64:67], v[138:141], v[122:125], v[64:67]\n"
        "v_mfma_f32_16x16x32_f16 v[64:67], v[134:137], v[118:121], v[64:67]\n"
        "s_add_u32 s91, s89, 294336\n"
        "ds_read_b128 v[114:117], v20 offset:528\n"
        "v_mfma_f32_16x16x32_f16 v[126:129], v[134:137], v[142:145], 0\n"
        "buffer_store_dwordx3 v[174:176], v14, s[40:43], s90 offen offset:384 sc1 nt\n"
        "s_nop 1\n"
        "v_mfma_f32_16x16x32_f16 v[126:129], v[138:141], v[146:149], v[126:129]\n"
        "s_nop 0\n"
        "v_cvt_pk_bf16_f32 v182, v70, v64\n"
        "v_cvt_pk_bf16_f32 v186, v71, v65\n"
        "v_mfma_f32_16x16x32_bf16 v[126:129], v[222:225], v[94:97], v[126:129]\n"
        "buffer_store_dwordx3 v[178:180], v17, s[40:43], s90 offen offset:1152 sc1 nt\n"
        "v_cvt_pk_bf16_f32 v196, v72, v66\n"
        "v_cvt_pk_bf16_f32 v200, v73, v67\n"
        "v_mfma_f32_16x16x32_bf16 v[64:67], v[222:225], v[98:101], 0\n"
        "s_add_u32 s90, s89, 392448\n"
        "v_mfma_f32_16x16x32_f16 v[64:67], v[138:141], v[154:157], v[64:67]\n"
        "buffer_store_dwordx3 v[188:190], v14, s[40:43], s91 offen offset:576 sc1 nt\n"
        "v_mfma_f32_16x16x32_f16 v[64:67], v[134:137], v[150:153], v[64:67]\n"
        "s_nop 0\n"
        "v_mfma_f32_16x16x32_f16 v[70:73], v[134:137], v[158:161], 0\n"
        "buffer_store_dwordx3 v[192:194], v14, s[40:43], s90 offen sc1 nt\n"
        "v_lshlrev_b32 v130, 16, v108\n"
        "s_nop 1\n"
        "v_mfma_f32_16x16x32_f16 v[70:73], v[138:141], v[162:165], v[70:73]\n"
        "s_waitcnt lgkmcnt(0)\n"
        "v_cvt_pk_bf16_f32 v183, v126, v64\n"
        "v_cvt_pk_bf16_f32 v187, v127, v65\n"
        "v_mfma_f32_16x16x32_bf16 v[70:73], v[230:233], v[94:97], v[70:73]\n"
        "v_lshlrev_b32 v64, 16, v124\n"
        "v_cvt_pk_bf16_f32 v197, v128, v66\n"
        "v_mfma_f32_16x16x32_bf16 v[174:177], v[230:233], v[98:101], 0\n"
        "v_cvt_pk_bf16_f32 v201, v129, v67\n"
        "v_mfma_f32_16x16x32_f16 v[174:177], v[138:141], v[170:173], v[174:177]\n"
        "v_lshlrev_b32 v126, 16, v148\n"
        "v_mfma_f32_16x16x32_f16 v[174:177], v[134:137], v[166:169], v[174:177]\n"
        "s_nop 1\n"
        "v_mfma_f32_16x16x32_f16 v[130:133], v[110:113], v[102:105], v[130:133]\n"
        "s_nop 0\n"
        "v_lshlrev_b32 v102, 16, v156\n"
        "s_nop 0\n"
        "v_mfma_f32_16x16x32_f16 v[130:133], v[114:117], v[106:109], v[130:133]\n"
        "s_nop 0\n"
        "v_cvt_pk_bf16_f32 v184, v70, v174\n"
        "v_cvt_pk_bf16_f32 v188, v71, v175\n"
        "v_mfma_f32_16x16x32_f16 v[64:67], v[114:117], v[122:125], v[64:67]\n"
        "v_lshlrev_b32 v70, 16, v164\n"
        "v_cvt_pk_bf16_f32 v198, v72, v176\n"
        "v_mfma_f32_16x16x32_f16 v[64:67], v[110:113], v[118:121], v[64:67]\n"
        "s_add_u32 s91, s89, 490560\n"
        "v_cvt_pk_bf16_f32 v202, v73, v177\n"
        "v_mfma_f32_16x16x32_f16 v[126:129], v[110:113], v[142:145], v[126:129]\n"
        "s_nop 0\n"
        "v_lshlrev_b32 v106, 16, v172\n"
        "s_nop 0\n"
        "v_mfma_f32_16x16x32_f16 v[126:129], v[114:117], v[146:149], v[126:129]\n"
        "s_nop 0\n"
        "v_cvt_pk_bf16_f32 v118, v130, v64\n"
        "v_mfma_f32_16x16x32_f16 v[102:105], v[114:117], v[154:157], v[102:105]\n"
        "buffer_store_dwordx3 v[182:184], v15, s[40:43], s90 offen offset:768 sc1 nt\n"
        "v_mfma_f32_16x16x32_f16 v[102:105], v[110:113], v[150:153], v[102:105]\n"
        "s_add_u32 s90, s89, 588672\n"
        "v_mfma_f32_16x16x32_f16 v[70:73], v[110:113], v[158:161], v[70:73]\n"
        "buffer_store_dwordx3 v[186:188], v14, s[40:43], s91 offen offset:192 sc1 nt\n"
        "s_nop 2\n"
        "v_mfma_f32_16x16x32_f16 v[70:73], v[114:117], v[162:165], v[70:73]\n"
        "s_nop 0\n"
        "v_cvt_pk_bf16_f32 v119, v126, v102\n"
        "v_mfma_f32_16x16x32_f16 v[106:109], v[114:117], v[170:173], v[106:109]\n"
        "buffer_store_dwordx3 v[196:198], v16, s[40:43], s91 offen offset:960 sc1 nt\n"
        "v_mfma_f32_16x16x32_f16 v[106:109], v[110:113], v[166:169], v[106:109]\n"
        "s_add_u32 s46, s89, 590016\n"
        "s_nop 1\n"
        "buffer_store_dwordx3 v[200:202], v14, s[40:43], s90 offen offset:384 sc1 nt\n"
        "s_min_u32 s46, s46, s42\n"
        "s_nop 2\n"
        "v_cvt_pk_bf16_f32 v120, v70, v106\n"
        "buffer_store_dwordx3 v[118:120], v17, s[44:47], s90 offen offset:1152 sc1 nt\n"
        "FIN_%=:\n"
        ".Lswin_fin:\n"
        "s_endpgm\n"
        "SLOW_%=:\n"
        "s_waitcnt lgkmcnt(0)\n"
        "s_nop 15\n"
        "v_mbcnt_lo_u32_b32 v60, -1, 0\n"
        "v_mbcnt_hi_u32_b32 v60, -1, v60\n"
        "v_and_b32 v114, 15, v60\n"
        "v_mul_u32_u24 v61, 24, v114\n"
        "v_sub_u32 v64, v24, v61\n"
        "v_lshrrev_b32 v115, 4, v60\n"
        "v_mul_u32_u24 v61, 96, v115\n"
        "v_add_u32 v64, v64, v61\n"
        "ds_read_b64 v[60:61], v64\n"
        "ds_read_b64 v[76:77], v64 offset:24\n"
        "ds_read_b64 v[118:119], v64 offset:48\n"
        "ds_read_b64 v[122:123], v64 offset:72\n"
        "ds_read_b64 v[124:125], v64 offset:8\n"
        "ds_read_b64 v[126:127], v64 offset:32\n"
        "ds_read_b64 v[130:131], v64 offset:56\n"
        "ds_read_b64 v[132:133], v64 offset:80\n"
        "ds_read_b64 v[134:135], v64 offset:16\n"
        "ds_read_b64 v[138:139], v64 offset:40\n"
        "ds_read_b64 v[140:141], v64 offset:64\n"
        "ds_read_b64 v[142:143], v64 offset:88\n"
        "v_mfma_f32_16x16x32_bf16 v[64:67], v[194:197], v[194:197], 0\n"
        "v_mov_b32 v120, 0x05040100\n"
        "v_subrev_u32 v116, 158080, v20\n"
        "v_mfma_f32_16x16x32_bf16 v[64:67], v[198:201], v[198:201], v[64:67]\n"
        "v_mov_b32 v128, 161664\n"
        "v_mov_b32 v70, 0x3f803f80\n"
        "v_mfma_f32_16x16x32_bf16 v[64:67], v[202:205], v[202:205], v[64:67]\n"
        "v_mov_b32 v71, 0x3f803f80\n"
        "v_mov_b32 v72, 0x3f803f80\n"
        "v_mov_b32 v73, 0x3f803f80\n"
        "s_nop 1\n"
        "v_mfma_f32_16x16x32_bf16 v[102:105], v[70:73], v[202:205], 0\n"
        "v_mov_b32 v174, 0xff800000\n"
        "v_mov_b32 v121, 0\n"
        "v_mfma_f32_16x16x32_bf16 v[102:105], v[70:73], v[198:201], v[102:105]\n"
        "s_waitcnt lgkmcnt(11)\n"
        "v_mov_b32 v129, 0\n"
        "v_mov_b32 v137, 0\n"
        "v_mfma_f32_16x16x32_bf16 v[102:105], v[70:73], v[194:197], v[102:105]\n"
        "s_waitcnt lgkmcnt(10)\n"
        "v_mov_b32 v145, 0\n"
        "v_mov_b32 v153, 0\n"
        "v_mfma_f32_16x16x32_bf16 v[106:109], v[206:209], v[206:209], 0\n"
        "s_waitcnt lgkmcnt(9)\n"
        "v_mov_b32 v161, 0\n"
        "v_mov_b32 v169, 0\n"
        "v_mfma_f32_16x16x32_bf16 v[106:109], v[210:213], v[210:213], v[106:109]\n"
        "s_waitcnt lgkmcnt(8)\n"
        "v_mov_b32 v171, 0\n"
        "v_mov_b32 v173, 0\n"
        "v_mfma_f32_16x16x32_bf16 v[106:109], v[214:217], v[214:217], v[106:109]\n"
        "s_waitcnt lgkmcnt(7)\n"
        "v_mov_b32 v175, 0\n"
        "v_and_b32 v116, 384, v116\n"
        "v_mfma_f32_16x16x32_bf16 v[110:113], v[70:73], v[214:217], 0\n"
        "s_waitcnt lgkmcnt(6)\n"
        "v_lshlrev_b32 v116, 1, v116\n"
        "v_add_u32 v116, 160640, v116\n"
        "v_mfma_f32_16x16x32_bf16 v[110:113], v[70:73], v[210:213], v[110:113]\n"
        "s_waitcnt lgkmcnt(5)\n"
        "v_lshlrev_b32 v154, 2, v114\n"
        "v_lshlrev_b32 v155, 4, v115\n"
        "v_mfma_f32_16x16x32_bf16 v[110:113], v[70:73], v[206:209], v[110:113]\n"
        "s_waitcnt lgkmcnt(4)\n"
        "v_add_u32 v154, v154, v116\n"
        "v_add_u32 v155, v155, v116\n"
        "v_mfma_f32_16x16x32_bf16 v[114:117], v[218:221], v[218:221], 0\n"
        "s_waitcnt lgkmcnt(3)\n"
        "v_cndmask_b32 v128, v128, v154, vcc\n"
        "v_cndmask_b32_e64 v103, v64, v65, s[56:57]\n"
        "v_mfma_f32_16x16x32_bf16 v[114:117], v[222:225], v[222:225], v[114:117]\n"
        "s_waitcnt lgkmcnt(2)\n"
        "v_cndmask_b32_e64 v103, v103, v66, s[58:59]\n"
        "v_cndmask_b32_e64 v103, v103, v67, s[60:61]\n"
        "v_mfma_f32_16x16x32_bf16 v[114:117], v[230:233], v[230:233], v[114:117]\n"
        "s_waitcnt lgkmcnt(1)\n"
        "v_mul_f32 v103, s80, v103\n"
        "v_mul_f32 v111, s80, v102\n"
        "v_mfma_f32_16x16x32_bf16 v[64:67], v[70:73], v[230:233], 0\n"
        "s_waitcnt lgkmcnt(0)\n"
        "v_fma_f32 v111, -v111, v111, v103\n"
        "v_max_f32 v111, 0, v111\n"
        "v_mfma_f32_16x16x32_bf16 v[64:67], v[70:73], v[222:225], v[64:67]\n"
        "v_add_f32 v111, s81, v111\n"
        "v_perm_b32 v176, v61, v60, v120\n"
        "v_mfma_f32_16x16x32_bf16 v[64:67], v[70:73], v[218:221], v[64:67]\n"
        "v_rsq_f32 v111, v111\n"
        "v_perm_b32 v177, v77, v76, v120\n"
        "v_perm_b32 v178, v119, v118, v120\n"
        "v_perm_b32 v179, v123, v122, v120\n"
        "s_nop 1\n"
        "v_mfma_f32_16x16x32_bf16 v[102:105], v[176:179], v[176:179], 0\n"
        "ds_write_b32 v128, v111\n"
        "v_perm_b32 v180, v125, v124, v120\n"
        "v_perm_b32 v181, v127, v126, v120\n"
        "v_perm_b32 v182, v131, v130, v120\n"
        "v_perm_b32 v183, v133, v132, v120\n"
        "s_nop 1\n"
        "v_mfma_f32_16x16x32_bf16 v[102:105], v[180:183], v[180:183], v[102:105]\n"
        "v_perm_b32 v184, v135, v134, v120\n"
        "v_perm_b32 v185, v139, v138, v120\n"
        "v_perm_b32 v186, v141, v140, v120\n"
        "v_perm_b32 v187, v143, v142, v120\n"
        "s_nop 1\n"
        "v_mfma_f32_16x16x32_bf16 v[102:105], v[184:187], v[184:187], v[102:105]\n"
        "v_cndmask_b32_e64 v60, v106, v107, s[56:57]\n"
        "v_cndmask_b32_e64 v60, v60, v108, s[58:59]\n"
        "v_mfma_f32_16x16x32_bf16 v[122:125], v[70:73], v[184:187], 0\n"
        "v_cndmask_b32_e64 v60, v60, v109, s[60:61]\n"
        "v_mul_f32 v60, s80, v60\n"
        "v_mfma_f32_16x16x32_bf16 v[122:125], v[70:73], v[180:183], v[122:125]\n"
        "v_mul_f32 v61, s80, v110\n"
        "v_fma_f32 v61, -v61, v61, v60\n"
        "v_mfma_f32_16x16x32_bf16 v[122:125], v[70:73], v[176:179], v[122:125]\n"
        "v_max_f32 v61, 0, v61\n"
        "v_add_f32 v61, s81, v61\n"
        "v_mfma_f32_16x16x32_bf16 v[70:73], v[194:197], a[216:219], 0\n"
        "v_rsq_f32 v61, v61\n"
        "v_mfma_f32_16x16x32_bf16 v[70:73], v[198:201], a[220:223], v[70:73]\n"
        "ds_write_b32 v128, v61 offset:64\n"
        "v_mfma_f32_16x16x32_bf16 v[70:73], v[202:205], a[224:227], v[70:73]\n"
        "v_cndmask_b32_e64 v60, v114, v115, s[56:57]\n"
        "v_cndmask_b32_e64 v60, v60, v116, s[58:59]\n"
        "v_mfma_f32_16x16x32_bf16 v[106:109], v[214:217], a[224:227], 0\n"
        "v_cndmask_b32_e64 v60, v60, v117, s[60:61]\n"
        "v_mul_f32 v60, s80, v60\n"
        "v_mfma_f32_16x16x32_bf16 v[106:109], v[210:213], a[220:223], v[106:109]\n"
        "v_mul_f32 v61, s80, v64\n"
        "v_fma_f32 v61, -v61, v61, v60\n"
        "v_mfma_f32_16x16x32_bf16 v[106:109], v[206:209], a[216:219], v[106:109]\n"
        "v_max_f32 v61, 0, v61\n"
        "v_add_f32 v61, s81, v61\n"
        "v_mfma_f32_16x16x32_bf16 v[64:67], v[218:221], a[216:219], 0\n"
        "v_rsq_f32 v61, v61\n"
        "v_mfma_f32_16x16x32_bf16 v[64:67], v[222:225], a[220:223], v[64:67]\n"
        "ds_write_b32 v128, v61 offset:128\n"
        "v_mfma_f32_16x16x32_bf16 v[64:67], v[230:233], a[224:227], v[64:67]\n"
        "v_cndmask_b32_e64 v60, v102, v103, s[56:57]\n"
        "v_cndmask_b32_e64 v60, v60, v104, s[58:59]\n"
        "v_mfma_f32_16x16x32_bf16 v[110:113], v[184:187], a[224:227], 0\n"
        "v_cndmask_b32_e64 v60, v60, v105, s[60:61]\n"
        "v_mul_f32 v60, s80, v60\n"
        "v_mfma_f32_16x16x32_bf16 v[110:113], v[180:183], a[220:223], v[110:113]\n"
        "v_mul_f32 v61, s80, v122\n"
        "v_fma_f32 v61, -v61, v61, v60\n"
        "v_mfma_f32_16x16x32_bf16 v[110:113], v[176:179], a[216:219], v[110:113]\n"
        "v_max_f32 v61, 0, v61\n"
        "v_add_f32 v61, s81, v61\n"
        "v_mfma_f32_16x16x32_bf16 v[102:105], v[218:221], a[228:231], 0\n"
        "v_rsq_f32 v61, v61\n"
        "v_mfma_f32_16x16x32_bf16 v[102:105], v[222:225], a[232:235], v[102:105]\n"
        "ds_write_b32 v128, v61 offset:192\n"
        "v_mfma_f32_16x16x32_bf16 v[102:105], v[230:233], a[236:239], v[102:105]\n"
        "v_cvt_pk_bf16_f32 v114, v70, v71\n"
        "v_cvt_pk_bf16_f32 v115, v72, v73\n"
        "v_mfma_f32_16x16x32_bf16 v[70:73], v[214:217], a[236:239], 0\n"
        "v_cvt_pk_bf16_f32 v116, v106, v107\n"
        "v_cvt_pk_bf16_f32 v117, v108, v109\n"
        "v_mfma_f32_16x16x32_bf16 v[70:73], v[210:213], a[232:235], v[70:73]\n"
        "v_cvt_pk_bf16_f32 v118, v64, v65\n"
        "v_cvt_pk_bf16_f32 v119, v66, v67\n"
        "v_mfma_f32_16x16x32_bf16 v[70:73], v[206:209], a[228:231], v[70:73]\n"
        "s_nop 0\n"
        "v_cvt_pk_bf16_f32 v120, v110, 0\n"
        "v_cvt_pk_bf16_f32 v126, v102, v103\n"
        "v_mfma_f32_16x16x32_bf16 v[64:67], v[194:197], a[228:231], 0\n"
        "s_nop 0\n"
        "v_cvt_pk_bf16_f32 v127, v104, v105\n"
        "v_mfma_f32_16x16x32_bf16 v[64:67], v[198:201], a[232:235], v[64:67]\n"
        "s_nop 0\n"
        "v_cvt_pk_bf16_f32 v124, v70, v71\n"
        "v_cvt_pk_bf16_f32 v125, v72, v73\n"
        "v_mfma_f32_16x16x32_bf16 v[64:67], v[202:205], a[236:239], v[64:67]\n"
        "s_nop 1\n"
        "v_mfma_f32_16x16x32_bf16 v[70:73], v[184:187], a[236:239], 0\n"
        "s_nop 2\n"
        "v_mfma_f32_16x16x32_bf16 v[70:73], v[180:183], a[232:235], v[70:73]\n"
        "s_nop 0\n"
        "v_cvt_pk_bf16_f32 v122, v64, v65\n"
        "v_cvt_pk_bf16_f32 v123, v66, v67\n"
        "v_mfma_f32_16x16x32_bf16 v[70:73], v[176:179], a[228:231], v[70:73]\n"
        "s_nop 1\n"
        "v_mfma_f32_16x16x32_bf16 v[64:67], v[194:197], a[240:243], 0\n"
        "s_nop 2\n"
        "v_mfma_f32_16x16x32_bf16 v[64:67], v[198:201], a[244:247], v[64:67]\n"
        "s_nop 0\n"
        "v_cvt_pk_bf16_f32 v128, v70, 0\n"
        "v_mfma_f32_16x16x32_bf16 v[64:67], v[202:205], a[248:251], v[64:67]\n"
        "s_nop 1\n"
        "v_mfma_f32_16x16x32_bf16 v[70:73], v[214:217], a[248:251], 0\n"
        "s_nop 2\n"
        "v_mfma_f32_16x16x32_bf16 v[70:73], v[210:213], a[244:247], v[70:73]\n"
        "s_nop 0\n"
        "v_cvt_pk_bf16_f32 v130, v64, v65\n"
        "v_cvt_pk_bf16_f32 v131, v66, v67\n"
        "v_mfma_f32_16x16x32_bf16 v[70:73], v[206:209], a[240:243], v[70:73]\n"
        "s_nop 1\n"
        "v_mfma_f32_16x16x32_bf16 v[64:67], v[218:221], a[240:243], 0\n"
        "s_nop 2\n"
        "v_mfma_f32_16x16x32_bf16 v[64:67], v[222:225], a[244:247], v[64:67]\n"
        "s_nop 0\n"
        "v_cvt_pk_bf16_f32 v132, v70, v71\n"
        "v_cvt_pk_bf16_f32 v133, v72, v73\n"
        "v_mfma_f32_16x16x32_bf16 v[64:67], v[230:233], a[248:251], v[64:67]\n"
        "s_nop 1\n"
        "v_mfma_f32_16x16x32_bf16 v[70:73], v[184:187], a[248:251], 0\n"
        "s_nop 2\n"
        "v_mfma_f32_16x16x32_bf16 v[70:73], v[180:183], a[244:247], v[70:73]\n"
        "s_nop 0\n"
        "v_cvt_pk_bf16_f32 v134, v64, v65\n"
        "v_cvt_pk_bf16_f32 v135, v66, v67\n"
        "v_mfma_f32_16x16x32_bf16 v[70:73], v[176:179], a[240:243], v[70:73]\n"
        "s_nop 1\n"
        "v_mfma_f32_16x16x32_bf16 v[64:67], v[218:221], a[252:255], 0\n"
        "s_nop 2\n"
        "v_mfma_f32_16x16x32_bf16 v[64:67], v[222:225], v[28:31], v[64:67]\n"
        "s_nop 0\n"
        "v_cvt_pk_bf16_f32 v136, v70, 0\n"
        "v_mfma_f32_16x16x32_bf16 v[64:67], v[230:233], v[32:35], v[64:67]\n"
        "s_nop 1\n"
        "v_mfma_f32_16x16x32_bf16 v[70:73], v[214:217], v[32:35], 0\n"
        "s_nop 2\n"
        "v_mfma_f32_16x16x32_bf16 v[70:73], v[210:213], v[28:31], v[70:73]\n"
        "s_nop 0\n"
        "v_cvt_pk_bf16_f32 v142, v64, v65\n"
        "v_cvt_pk_bf16_f32 v143, v66, v67\n"
        "v_mfma_f32_16x16x32_bf16 v[70:73], v[206:209], a[252:255], v[70:73]\n"
        "s_nop 1\n"
        "v_mfma_f32_16x16x32_bf16 v[64:67], v[194:197], a[252:255], 0\n"
        "s_nop 2\n"
        "v_mfma_f32_16x16x32_bf16 v[64:67], v[198:201], v[28:31], v[64:67]\n"
        "s_nop 0\n"
        "v_cvt_pk_bf16_f32 v140, v70, v71\n"
        "v_cvt_pk_bf16_f32 v141, v72, v73\n"
        "v_mfma_f32_16x16x32_bf16 v[64:67], v[202:205], v[32:35], v[64:67]\n"
        "s_nop 1\n"
        "v_mfma_f32_16x16x32_bf16 v[70:73], v[184:187], v[32:35], 0\n"
        "s_nop 2\n"
        "v_mfma_f32_16x16x32_bf16 v[70:73], v[180:183], v[28:31], v[70:73]\n"
        "s_nop 0\n"
        "v_cvt_pk_bf16_f32 v138, v64, v65\n"
        "v_cvt_pk_bf16_f32 v139, v66, v67\n"
        "v_mfma_f32_16x16x32_bf16 v[70:73], v[176:179], a[252:255], v[70:73]\n"
        "s_nop 1\n"
        "v_mfma_f32_16x16x32_bf16 v[64:67], v[194:197], v[36:39], 0\n"
        "s_nop 2\n"
        "v_mfma_f32_16x16x32_bf16 v[64:67], v[198:201], v[40:43], v[64:67]\n"
        "s_nop 0\n"
        "v_cvt_pk_bf16_f32 v144, v70, 0\n"
        "v_mfma_f32_16x16x32_bf16 v[64:67], v[202:205], v[44:47], v[64:67]\n"
        "s_nop 1\n"
        "v_mfma_f32_16x16x32_bf16 v[70:73], v[214:217], v[44:47], 0\n"
        "s_nop 2\n"
        "v_mfma_f32_16x16x32_bf16 v[70:73], v[210:213], v[40:43], v[70:73]\n"
        "s_nop 0\n"
        "v_cvt_pk_bf16_f32 v146, v64, v65\n"
        "v_cvt_pk_bf16_f32 v147, v66, v67\n"
        "v_mfma_f32_16x16x32_bf16 v[70:73], v[206:209], v[36:39], v[70:73]\n"
        "s_nop 1\n"
        "v_mfma_f32_16x16x32_bf16 v[64:67], v[218:221], v[36:39], 0\n"
        "s_nop 2\n"
        "v_mfma_f32_16x16x32_bf16 v[64:67], v[222:225], v[40:43], v[64:67]\n"
        "s_nop 0\n"
        "v_cvt_pk_bf16_f32 v148, v70, v71\n"
        "v_cvt_pk_bf16_f32 v149, v72, v73\n"
        "v_mfma_f32_16x16x32_bf16 v[64:67], v[230:233], v[44:47], v[64:67]\n"
        "s_nop 1\n"
        "v_mfma_f32_16x16x32_bf16 v[70:73], v[184:187], v[44:47], 0\n"
        "s_nop 2\n"
        "v_mfma_f32_16x16x32_bf16 v[70:73], v[180:183], v[40:43], v[70:73]\n"
        "s_nop 0\n"
        "v_cvt_pk_bf16_f32 v150, v64, v65\n"
        "v_cvt_pk_bf16_f32 v151, v66, v67\n"
        "v_mfma_f32_16x16x32_bf16 v[70:73], v[176:179], v[36:39], v[70:73]\n"
        "s_nop 1\n"
        "v_mfma_f32_16x16x32_bf16 v[64:67], v[218:221], v[48:51], 0\n"
        "s_nop 2\n"
        "v_mfma_f32_16x16x32_bf16 v[64:67], v[222:225], v[52:55], v[64:67]\n"
        "s_nop 0\n"
        "v_cvt_pk_bf16_f32 v152, v70, 0\n"
        "v_mfma_f32_16x16x32_bf16 v[64:67], v[230:233], v[56:59], v[64:67]\n"
        "v_mfma_f32_16x16x32_bf16 v[70:73], v[214:217], v[56:59], 0\n"
        "v_mfma_f32_16x16x32_bf16 v[70:73], v[210:213], v[52:55], v[70:73]\n"
        "ds_read_b128 v[102:105], v155\n"
        "ds_read_b128 v[106:109], v155 offset:64\n"
        "v_mfma_f32_16x16x32_bf16 v[70:73], v[206:209], v[48:51], v[70:73]\n"
        "ds_read_b128 v[110:113], v155 offset:128\n"
        "ds_read_b32 v60, v154\n"
        "v_mfma_f32_16x16x32_bf16 v[162:165], v[194:197], v[48:51], 0\n"
        "ds_read_b32 v61, v154 offset:64\n"
        "ds_read_b32 v76, v154 offset:128\n"
        "v_mfma_f32_16x16x32_bf16 v[162:165], v[198:201], v[52:55], v[162:165]\n"
        "ds_read_b32 v77, v154 offset:192\n"
        "v_cvt_pk_bf16_f32 v158, v64, v65\n"
        "v_mfma_f32_16x16x32_bf16 v[162:165], v[202:205], v[56:59], v[162:165]\n"
        "s_nop 0\n"
        "v_cvt_pk_bf16_f32 v159, v66, v67\n"
        "v_cvt_pk_bf16_f32 v156, v70, v71\n"
        "v_mfma_f32_16x16x32_bf16 v[64:67], v[184:187], v[56:59], 0\n"
        "s_nop 0\n"
        "v_cvt_pk_bf16_f32 v157, v72, v73\n"
        "v_mfma_f32_16x16x32_bf16 v[64:67], v[180:183], v[52:55], v[64:67]\n"
        "s_nop 0\n"
        "v_cvt_pk_bf16_f32 v154, v162, v163\n"
        "v_cvt_pk_bf16_f32 v155, v164, v165\n"
        "v_mfma_f32_16x16x32_bf16 v[64:67], v[176:179], v[48:51], v[64:67]\n"
        "s_nop 1\n"
        "v_mfma_f32_16x16x32_bf16 v[70:73], a[144:147], v[194:197], 0\n"
        "s_nop 2\n"
        "v_mfma_f32_16x16x32_bf16 v[70:73], a[148:151], v[198:201], v[70:73]\n"
        "s_nop 0\n"
        "v_cvt_pk_bf16_f32 v160, v64, 0\n"
        "v_mfma_f32_16x16x32_bf16 v[70:73], a[152:155], v[202:205], v[70:73]\n"
        "s_nop 1\n"
        "v_mfma_f32_16x16x32_bf16 v[64:67], a[164:167], v[202:205], 0\n"
        "s_waitcnt lgkmcnt(0)\n"
        "s_nop 1\n"
        "v_mfma_f32_16x16x32_bf16 v[64:67], a[160:163], v[198:201], v[64:67]\n"
        "s_nop 0\n"
        "v_cvt_pk_bf16_f32 v162, v70, v71\n"
        "v_cvt_pk_bf16_f32 v163, v72, v73\n"
        "v_mfma_f32_16x16x32_bf16 v[64:67], a[156:159], v[194:197], v[64:67]\n"
        "s_nop 1\n"
        "v_mfma_f32_16x16x32_bf16 v[70:73], a[168:171], v[194:197], 0\n"
        "s_nop 2\n"
        "v_mfma_f32_16x16x32_bf16 v[70:73], a[172:175], v[198:201], v[70:73]\n"
        "s_nop 0\n"
        "v_cvt_pk_bf16_f32 v164, v64, v65\n"
        "v_cvt_pk_bf16_f32 v165, v66, v67\n"
        "v_mfma_f32_16x16x32_bf16 v[70:73], a[176:179], v[202:205], v[70:73]\n"
        "s_nop 1\n"
        "v_mfma_f32_16x16x32_bf16 v[64:67], a[188:191], v[202:205], 0\n"
        "s_nop 2\n"
        "v_mfma_f32_16x16x32_bf16 v[64:67], a[184:187], v[198:201], v[64:67]\n"
        "s_nop 0\n"
        "v_cvt_pk_bf16_f32 v188, v70, v71\n"
        "v_cvt_pk_bf16_f32 v189, v72, v73\n"
        "v_mfma_f32_16x16x32_bf16 v[64:67], a[180:183], v[194:197], v[64:67]\n"
        "s_nop 1\n"
        "v_mfma_f32_16x16x32_bf16 v[70:73], a[192:195], v[194:197], 0\n"
        "s_nop 2\n"
        "v_mfma_f32_16x16x32_bf16 v[70:73], a[196:199], v[198:201], v[70:73]\n"
        "s_nop 0\n"
        "v_cvt_pk_bf16_f32 v190, v64, v65\n"
        "v_cvt_pk_bf16_f32 v191, v66, v67\n"
        "v_mfma_f32_16x16x32_bf16 v[70:73], a[200:203], v[202:205], v[70:73]\n"
        "s_nop 1\n"
        "v_mfma_f32_16x16x32_bf16 v[64:67], a[212:215], v[202:205], 0\n"
        "s_nop 2\n"
        "v_mfma_f32_16x16x32_bf16 v[64:67], a[208:211], v[198:201], v[64:67]\n"
        "s_nop 0\n"
        "v_cvt_pk_bf16_f32 v226, v70, v71\n"
        "v_cvt_pk_bf16_f32 v227, v72, v73\n"
        "v_mfma_f32_16x16x32_bf16 v[64:67], a[204:207], v[194:197], v[64:67]\n"
        "s_nop 1\n"
        "v_mfma_f32_16x16x32_bf16 v[70:73], v[194:197], v[162:165], 0\n"
        "s_nop 2\n"
        "v_mfma_f32_16x16x32_bf16 v[70:73], v[198:201], v[188:191], v[70:73]\n"
        "s_nop 0\n"
        "v_cvt_pk_bf16_f32 v228, v64, v65\n"
        "v_cvt_pk_bf16_f32 v229, v66, v67\n"
        "s_nop 1\n"
        "v_mfma_f32_16x16x32_bf16 v[70:73], v[202:205], v[226:229], v[70:73]\n"
        "s_nop 1\n"
        "v_mfma_f32_16x16x32_bf16 v[64:67], v[214:217], v[226:229], 0\n"
        "s_nop 2\n"
        "v_mfma_f32_16x16x32_bf16 v[64:67], v[210:213], v[188:191], v[64:67]\n"
        "s_nop 0\n"
        "v_mul_f32 v70, v70, v102\n"
        "v_mul_f32 v71, v71, v103\n"
        "v_mfma_f32_16x16x32_bf16 v[64:67], v[206:209], v[162:165], v[64:67]\n"
        "s_nop 0\n"
        "v_mul_f32 v72, v72, v104\n"
        "v_mul_f32 v73, v73, v105\n"
        "v_mfma_f32_16x16x32_bf16 v[234:237], v[218:221], v[162:165], 0\n"
        "s_nop 0\n"
        "v_max3_f32 v166, v70, v71, v72\n"
        "v_mfma_f32_16x16x32_bf16 v[234:237], v[222:225], v[188:191], v[234:237]\n"
        "s_nop 0\n"
        "v_mul_f32 v64, v64, v106\n"
        "v_mul_f32 v65, v65, v107\n"
        "v_mfma_f32_16x16x32_bf16 v[234:237], v[230:233], v[226:229], v[234:237]\n"
        "v_mul_f32 v66, v66, v108\n"
        "v_mul_f32 v67, v67, v109\n"
        "v_mfma_f32_16x16x32_bf16 v[238:241], v[184:187], v[226:229], 0\n"
        "s_nop 0\n"
        "v_max3_f32 v166, v166, v73, v64\n"
        "v_max3_f32 v166, v166, v65, v66\n"
        "v_mfma_f32_16x16x32_bf16 v[238:241], v[180:183], v[188:191], v[238:241]\n"
        "s_nop 0\n"
        "v_mul_f32 v234, v234, v110\n"
        "v_mul_f32 v235, v235, v111\n"
        "v_mfma_f32_16x16x32_bf16 v[238:241], v[176:179], v[162:165], v[238:241]\n"
        "s_nop 0\n"
        "v_mul_f32 v236, v236, v112\n"
        "v_mul_f32 v237, v237, v113\n"
        "v_max3_f32 v166, v166, v67, v234\n"
        "v_max3_f32 v166, v166, v235, v236\n"
        "s_nop 2\n"
        "v_mul_f32 v238, v238, v77\n"
        "v_cndmask_b32_e64 v238, v174, v238, s[62:63]\n"
        "v_max3_f32 v166, v166, v237, v238\n"
        "v_mov_b32 v162, v166\n"
        "s_nop 1\n"
        "v_permlane32_swap_b32 v166, v162\n"
        "v_max_f32 v166, v166, v162\n"
        "v_mov_b32 v162, v166\n"
        "s_nop 1\n"
        "v_permlane16_swap_b32 v166, v162\n"
        "v_max_f32 v166, v166, v162\n"
        "v_mul_f32 v166, v166, v60\n"
        "v_fma_f32 v70, v70, v60, -v166\n"
        "v_exp_f32 v70, v70\n"
        "v_fma_f32 v71, v71, v60, -v166\n"
        "v_exp_f32 v71, v71\n"
        "s_nop 0\n"
        "v_add_f32 v168, v70, v71\n"
        "v_mul_f32 v70, v70, v102\n"
        "v_fma_f32 v238, v238, v60, -v166\n"
        "v_exp_f32 v238, v238\n"
        "v_fma_f32 v237, v237, v60, -v166\n"
        "v_exp_f32 v237, v237\n"
        "v_fma_f32 v236, v236, v60, -v166\n"
        "v_exp_f32 v236, v236\n"
        "v_fma_f32 v235, v235, v60, -v166\n"
        "v_exp_f32 v235, v235\n"
        "v_fma_f32 v234, v234, v60, -v166\n"
        "v_exp_f32 v234, v234\n"
        "v_fma_f32 v67, v67, v60, -v166\n"
        "v_exp_f32 v67, v67\n"
        "v_fma_f32 v66, v66, v60, -v166\n"
        "v_exp_f32 v66, v66\n"
        "v_fma_f32 v65, v65, v60, -v166\n"
        "v_exp_f32 v65, v65\n"
        "v_fma_f32 v64, v64, v60, -v166\n"
        "v_exp_f32 v64, v64\n"
        "v_fma_f32 v73, v73, v60, -v166\n"
        "v_exp_f32 v73, v73\n"
        "v_fma_f32 v72, v72, v60, -v166\n"
        "v_exp_f32 v72, v72\n"
        "s_nop 0\n"
        "v_add_f32 v168, v168, v72\n"
        "v_add_f32 v168, v168, v73\n"
        "v_add_f32 v168, v168, v64\n"
        "v_add_f32 v168, v168, v65\n"
        "v_add_f32 v168, v168, v66\n"
        "v_add_f32 v168, v168, v67\n"
        "v_add_f32 v168, v168, v234\n"
        "v_add_f32 v168, v168, v235\n"
        "v_add_f32 v168, v168, v236\n"
        "v_add_f32 v168, v168, v237\n"
        "v_add_f32 v168, v168, v238\n"
        "v_mov_b32 v60, v168\n"
        "s_nop 1\n"
        "v_permlane32_swap_b32 v168, v60\n"
        "v_add_f32 v168, v168, v60\n"
        "v_mov_b32 v60, v168\n"
        "s_nop 1\n"
        "v_permlane16_swap_b32 v168, v60\n"
        "v_add_f32 v168, v168, v60\n"
        "v_rcp_f32 v168, v168\n"
        "s_nop 0\n"
        "v_mul_f32 v70, v70, v168\n"
        "v_mul_f32 v71, v71, v103\n"
        "v_mul_f32 v71, v71, v168\n"
        "v_cvt_pk_bf16_f32 v162, v70, v71\n"
        "v_mul_f32 v72, v72, v104\n"
        "v_mul_f32 v72, v72, v168\n"
        "v_mul_f32 v73, v73, v105\n"
        "v_mul_f32 v73, v73, v168\n"
        "v_cvt_pk_bf16_f32 v163, v72, v73\n"
        "v_mul_f32 v64, v64, v106\n"
        "v_mul_f32 v64, v64, v168\n"
        "v_mul_f32 v65, v65, v107\n"
        "v_mul_f32 v65, v65, v168\n"
        "v_cvt_pk_bf16_f32 v164, v64, v65\n"
        "v_mul_f32 v66, v66, v108\n"
        "v_mul_f32 v66, v66, v168\n"
        "v_mul_f32 v67, v67, v109\n"
        "v_mul_f32 v67, v67, v168\n"
        "v_cvt_pk_bf16_f32 v165, v66, v67\n"
        "s_nop 1\n"
        "v_mfma_f32_16x16x32_bf16 v[64:67], v[162:165], v[114:117], 0\n"
        "v_mul_f32 v234, v234, v110\n"
        "v_mul_f32 v235, v235, v111\n"
        "v_mul_f32 v234, v234, v168\n"
        "v_mul_f32 v235, v235, v168\n"
        "v_cvt_pk_bf16_f32 v166, v234, v235\n"
        "v_mul_f32 v236, v236, v112\n"
        "v_mul_f32 v236, v236, v168\n"
        "v_mul_f32 v237, v237, v113\n"
        "v_mul_f32 v237, v237, v168\n"
        "v_cvt_pk_bf16_f32 v167, v236, v237\n"
        "v_mul_f32 v238, v238, v77\n"
        "v_mul_f32 v238, v238, v168\n"
        "v_cvt_pk_bf16_f32 v168, v238, 0\n"
        "s_nop 1\n"
        "v_mfma_f32_16x16x32_bf16 v[64:67], v[166:169], v[118:121], v[64:67]\n"
        "v_mfma_f32_16x16x32_bf16 v[64:67], v[194:197], v[94:97], v[64:67]\n"
        "v_mfma_f32_16x16x32_bf16 v[70:73], v[194:197], v[98:101], 0\n"
        "v_mfma_f32_16x16x32_bf16 v[70:73], v[166:169], v[126:129], v[70:73]\n"
        "v_mfma_f32_16x16x32_bf16 v[70:73], v[162:165], v[122:125], v[70:73]\n"
        "s_add_u32 s90, s89, 0\n"
        "s_nop 0\n"
        "v_mfma_f32_16x16x32_bf16 v[188:191], v[162:165], v[130:133], 0\n"
        "s_nop 2\n"
        "v_mfma_f32_16x16x32_bf16 v[188:191], v[166:169], v[134:137], v[188:191]\n"
        "s_nop 0\n"
        "v_cvt_pk_bf16_f32 v226, v64, v70\n"
        "v_cvt_pk_bf16_f32 v234, v65, v71\n"
        "v_mfma_f32_16x16x32_bf16 v[188:191], v[198:201], v[94:97], v[188:191]\n"
        "v_cvt_pk_bf16_f32 v238, v66, v72\n"
        "v_cvt_pk_bf16_f32 v242, v67, v73\n"
        "v_mfma_f32_16x16x32_bf16 v[64:67], v[198:201], v[98:101], 0\n"
        "s_add_u32 s91, s89, 98112\n"
        "v_mfma_f32_16x16x32_bf16 v[64:67], v[166:169], v[142:145], v[64:67]\n"
        "v_mfma_f32_16x16x32_bf16 v[64:67], v[162:165], v[138:141], v[64:67]\n"
        "s_nop 1\n"
        "v_mfma_f32_16x16x32_bf16 v[70:73], v[162:165], v[146:149], 0\n"
        "s_nop 2\n"
        "v_mfma_f32_16x16x32_bf16 v[70:73], v[166:169], v[150:153], v[70:73]\n"
        "s_nop 0\n"
        "v_cvt_pk_bf16_f32 v227, v188, v64\n"
        "v_cvt_pk_bf16_f32 v235, v189, v65\n"
        "v_mfma_f32_16x16x32_bf16 v[70:73], v[202:205], v[94:97], v[70:73]\n"
        "v_cvt_pk_bf16_f32 v239, v190, v66\n"
        "v_cvt_pk_bf16_f32 v243, v191, v67\n"
        "v_mfma_f32_16x16x32_bf16 v[64:67], v[202:205], v[98:101], 0\n"
        "v_mfma_f32_16x16x32_bf16 v[64:67], v[166:169], v[158:161], v[64:67]\n"
        "v_mfma_f32_16x16x32_bf16 v[64:67], v[162:165], v[154:157], v[64:67]\n"
        "s_nop 1\n"
        "v_mfma_f32_16x16x32_bf16 v[162:165], a[144:147], v[206:209], 0\n"
        "s_nop 2\n"
        "v_mfma_f32_16x16x32_bf16 v[162:165], a[148:151], v[210:213], v[162:165]\n"
        "s_nop 0\n"
        "v_cvt_pk_bf16_f32 v228, v70, v64\n"
        "v_cvt_pk_bf16_f32 v236, v71, v65\n"
        "v_mfma_f32_16x16x32_bf16 v[162:165], a[152:155], v[214:217], v[162:165]\n"
        "s_nop 0\n"
        "v_cvt_pk_bf16_f32 v240, v72, v66\n"
        "v_cvt_pk_bf16_f32 v244, v73, v67\n"
        "v_mfma_f32_16x16x32_bf16 v[64:67], a[164:167], v[214:217], 0\n"
        "s_nop 1\n"
        "v_mfma_f32_16x16x32_bf16 v[64:67], a[160:163], v[210:213], v[64:67]\n"
        "s_nop 0\n"
        "v_cvt_pk_bf16_f32 v70, v162, v163\n"
        "v_cvt_pk_bf16_f32 v71, v164, v165\n"
        "v_mfma_f32_16x16x32_bf16 v[64:67], a[156:159], v[206:209], v[64:67]\n"
        "s_nop 0\n"
        "v_mfma_f32_16x16x32_bf16 v[162:165], a[168:171], v[206:209], 0\n"
        "buffer_store_dwordx3 v[226:228], v14, s[40:43], s90 offen sc1 nt\n"
        "s_nop 2\n"
        "v_mfma_f32_16x16x32_bf16 v[162:165], a[172:175], v[210:213], v[162:165]\n"
        "s_nop 0\n"
        "v_cvt_pk_bf16_f32 v72, v64, v65\n"
        "v_cvt_pk_bf16_f32 v73, v66, v67\n"
        "v_mfma_f32_16x16x32_bf16 v[162:165], a[176:179], v[214:217], v[162:165]\n"
        "buffer_store_dwordx3 v[234:236], v15, s[40:43], s90 offen offset:768 sc1 nt\n"
        "s_nop 0\n"
        "v_mfma_f32_16x16x32_bf16 v[64:67], a[188:191], v[214:217], 0\n"
        "s_nop 2\n"
        "v_mfma_f32_16x16x32_bf16 v[64:67], a[184:187], v[210:213], v[64:67]\n"
        "buffer_store_dwordx3 v[238:240], v14, s[40:43], s91 offen offset:192 sc1 nt\n"
        "v_cvt_pk_bf16_f32 v166, v162, v163\n"
        "v_cvt_pk_bf16_f32 v167, v164, v165\n"
        "v_mfma_f32_16x16x32_bf16 v[64:67], a[180:183], v[206:209], v[64:67]\n"
        "s_nop 0\n"
        "v_mfma_f32_16x16x32_bf16 v[162:165], a[192:195], v[206:209], 0\n"
        "buffer_store_dwordx3 v[242:244], v16, s[40:43], s91 offen offset:960 sc1 nt\n"
        "s_nop 2\n"
        "v_mfma_f32_16x16x32_bf16 v[162:165], a[196:199], v[210:213], v[162:165]\n"
        "s_nop 0\n"
        "v_cvt_pk_bf16_f32 v168, v64, v65\n"
        "v_cvt_pk_bf16_f32 v169, v66, v67\n"
        "v_mfma_f32_16x16x32_bf16 v[162:165], a[200:203], v[214:217], v[162:165]\n"
        "s_nop 1\n"
        "v_mfma_f32_16x16x32_bf16 v[64:67], a[212:215], v[214:217], 0\n"
        "s_nop 2\n"
        "v_mfma_f32_16x16x32_bf16 v[64:67], a[208:211], v[210:213], v[64:67]\n"
        "s_nop 0\n"
        "v_cvt_pk_bf16_f32 v188, v162, v163\n"
        "v_cvt_pk_bf16_f32 v189, v164, v165\n"
        "v_mfma_f32_16x16x32_bf16 v[64:67], a[204:207], v[206:209], v[64:67]\n"
        "s_nop 1\n"
        "v_mfma_f32_16x16x32_bf16 v[226:229], v[194:197], v[70:73], 0\n"
        "s_nop 2\n"
        "v_mfma_f32_16x16x32_bf16 v[226:229], v[198:201], v[166:169], v[226:229]\n"
        "s_nop 0\n"
        "v_cvt_pk_bf16_f32 v190, v64, v65\n"
        "v_cvt_pk_bf16_f32 v191, v66, v67\n"
        "s_nop 1\n"
        "v_mfma_f32_16x16x32_bf16 v[226:229], v[202:205], v[188:191], v[226:229]\n"
        "s_nop 1\n"
        "v_mfma_f32_16x16x32_bf16 v[64:67], v[214:217], v[188:191], 0\n"
        "s_nop 2\n"
        "v_mfma_f32_16x16x32_bf16 v[64:67], v[210:213], v[166:169], v[64:67]\n"
        "s_nop 0\n"
        "v_mul_f32 v226, v226, v102\n"
        "v_mul_f32 v227, v227, v103\n"
        "v_mfma_f32_16x16x32_bf16 v[64:67], v[206:209], v[70:73], v[64:67]\n"
        "s_nop 0\n"
        "v_mul_f32 v228, v228, v104\n"
        "v_mul_f32 v229, v229, v105\n"
        "v_mfma_f32_16x16x32_bf16 v[234:237], v[218:221], v[70:73], 0\n"
        "s_nop 0\n"
        "v_max3_f32 v60, v226, v227, v228\n"
        "v_mfma_f32_16x16x32_bf16 v[234:237], v[222:225], v[166:169], v[234:237]\n"
        "s_nop 0\n"
        "v_mul_f32 v64, v64, v106\n"
        "v_mul_f32 v65, v65, v107\n"
        "v_mfma_f32_16x16x32_bf16 v[234:237], v[230:233], v[188:191], v[234:237]\n"
        "v_mul_f32 v66, v66, v108\n"
        "v_mul_f32 v67, v67, v109\n"
        "v_mfma_f32_16x16x32_bf16 v[162:165], v[184:187], v[188:191], 0\n"
        "s_nop 0\n"
        "v_max3_f32 v60, v60, v229, v64\n"
        "v_max3_f32 v60, v60, v65, v66\n"
        "v_mfma_f32_16x16x32_bf16 v[162:165], v[180:183], v[166:169], v[162:165]\n"
        "s_nop 0\n"
        "v_mul_f32 v234, v234, v110\n"
        "v_mul_f32 v235, v235, v111\n"
        "v_mfma_f32_16x16x32_bf16 v[162:165], v[176:179], v[70:73], v[162:165]\n"
        "s_nop 0\n"
        "v_mul_f32 v236, v236, v112\n"
        "v_mul_f32 v237, v237, v113\n"
        "v_max3_f32 v60, v60, v67, v234\n"
        "v_max3_f32 v60, v60, v235, v236\n"
        "s_nop 2\n"
        "v_mul_f32 v162, v162, v77\n"
        "v_cndmask_b32_e64 v162, v174, v162, s[62:63]\n"
        "v_max3_f32 v60, v60, v237, v162\n"
        "v_mov_b32 v70, v60\n"
        "s_nop 1\n"
        "v_permlane32_swap_b32 v60, v70\n"
        "v_max_f32 v60, v60, v70\n"
        "v_mov_b32 v70, v60\n"
        "s_nop 1\n"
        "v_permlane16_swap_b32 v60, v70\n"
        "v_max_f32 v60, v60, v70\n"
        "v_mul_f32 v60, v60, v61\n"
        "v_fma_f32 v226, v226, v61, -v60\n"
        "v_exp_f32 v226, v226\n"
        "v_fma_f32 v227, v227, v61, -v60\n"
        "v_exp_f32 v227, v227\n"
        "s_nop 0\n"
        "v_add_f32 v70, v226, v227\n"
        "v_mul_f32 v226, v226, v102\n"
        "v_fma_f32 v162, v162, v61, -v60\n"
        "v_exp_f32 v162, v162\n"
        "v_fma_f32 v237, v237, v61, -v60\n"
        "v_exp_f32 v237, v237\n"
        "v_fma_f32 v236, v236, v61, -v60\n"
        "v_exp_f32 v236, v236\n"
        "v_fma_f32 v235, v235, v61, -v60\n"
        "v_exp_f32 v235, v235\n"
        "v_fma_f32 v234, v234, v61, -v60\n"
        "v_exp_f32 v234, v234\n"
        "v_fma_f32 v67, v67, v61, -v60\n"
        "v_exp_f32 v67, v67\n"
        "v_fma_f32 v66, v66, v61, -v60\n"
        "v_exp_f32 v66, v66\n"
        "v_fma_f32 v65, v65, v61, -v60\n"
        "v_exp_f32 v65, v65\n"
        "v_fma_f32 v64, v64, v61, -v60\n"
        "v_exp_f32 v64, v64\n"
        "v_fma_f32 v229, v229, v61, -v60\n"
        "v_exp_f32 v229, v229\n"
        "v_fma_f32 v228, v228, v61, -v60\n"
        "v_exp_f32 v228, v228\n"
        "s_nop 0\n"
        "v_add_f32 v70, v70, v228\n"
        "v_add_f32 v70, v70, v229\n"
        "v_add_f32 v70, v70, v64\n"
        "v_add_f32 v70, v70, v65\n"
        "v_add_f32 v70, v70, v66\n"
        "v_add_f32 v70, v70, v67\n"
        "v_add_f32 v70, v70, v234\n"
        "v_add_f32 v70, v70, v235\n"
        "v_add_f32 v70, v70, v236\n"
        "v_add_f32 v70, v70, v237\n"
        "v_add_f32 v70, v70, v162\n"
        "v_mov_b32 v60, v70\n"
        "s_nop 1\n"
        "v_permlane32_swap_b32 v70, v60\n"
        "v_add_f32 v70, v70, v60\n"
        "v_mov_b32 v60, v70\n"
        "s_nop 1\n"
        "v_permlane16_swap_b32 v70, v60\n"
        "v_add_f32 v70, v70, v60\n"
        "v_rcp_f32 v70, v70\n"
        "s_nop 0\n"
        "v_mul_f32 v226, v226, v70\n"
        "v_mul_f32 v227, v227, v103\n"
        "v_mul_f32 v227, v227, v70\n"
        "v_cvt_pk_bf16_f32 v164, v226, v227\n"
        "v_mul_f32 v228, v228, v104\n"
        "v_mul_f32 v228, v228, v70\n"
        "v_mul_f32 v229, v229, v105\n"
        "v_mul_f32 v229, v229, v70\n"
        "v_cvt_pk_bf16_f32 v165, v228, v229\n"
        "v_mul_f32 v64, v64, v106\n"
        "v_mul_f32 v64, v64, v70\n"
        "v_mul_f32 v65, v65, v107\n"
        "v_mul_f32 v65, v65, v70\n"
        "v_cvt_pk_bf16_f32 v166, v64, v65\n"
        "v_mul_f32 v66, v66, v108\n"
        "v_mul_f32 v66, v66, v70\n"
        "v_mul_f32 v67, v67, v109\n"
        "v_mul_f32 v67, v67, v70\n"
        "v_cvt_pk_bf16_f32 v167, v66, v67\n"
        "s_nop 1\n"
        "v_mfma_f32_16x16x32_bf16 v[64:67], v[164:167], v[114:117], 0\n"
        "v_mul_f32 v234, v234, v110\n"
        "v_mul_f32 v235, v235, v111\n"
        "v_mul_f32 v234, v234, v70\n"
        "v_mul_f32 v235, v235, v70\n"
        "v_cvt_pk_bf16_f32 v168, v234, v235\n"
        "v_mul_f32 v236, v236, v112\n"
        "v_mul_f32 v236, v236, v70\n"
        "v_mul_f32 v237, v237, v113\n"
        "v_mul_f32 v237, v237, v70\n"
        "v_cvt_pk_bf16_f32 v169, v236, v237\n"
        "v_mul_f32 v162, v162, v77\n"
        "v_mul_f32 v162, v162, v70\n"
        "v_cvt_pk_bf16_f32 v170, v162, 0\n"
        "s_nop 1\n"
        "v_mfma_f32_16x16x32_bf16 v[64:67], v[168:171], v[118:121], v[64:67]\n"
        "v_mfma_f32_16x16x32_bf16 v[64:67], v[206:209], v[94:97], v[64:67]\n"
        "v_mfma_f32_16x16x32_bf16 v[70:73], v[206:209], v[98:101], 0\n"
        "v_mfma_f32_16x16x32_bf16 v[70:73], v[168:171], v[126:129], v[70:73]\n"
        "v_mfma_f32_16x16x32_bf16 v[70:73], v[164:167], v[122:125], v[70:73]\n"
        "s_add_u32 s90, s89, 196224\n"
        "s_nop 0\n"
        "v_mfma_f32_16x16x32_bf16 v[188:191], v[164:167], v[130:133], 0\n"
        "s_nop 2\n"
        "v_mfma_f32_16x16x32_bf16 v[188:191], v[168:171], v[134:137], v[188:191]\n"
        "s_nop 0\n"
        "v_cvt_pk_bf16_f32 v226, v64, v70\n"
        "v_cvt_pk_bf16_f32 v234, v65, v71\n"
        "v_mfma_f32_16x16x32_bf16 v[188:191], v[210:213], v[94:97], v[188:191]\n"
        "v_cvt_pk_bf16_f32 v238, v66, v72\n"
        "v_cvt_pk_bf16_f32 v242, v67, v73\n"
        "v_mfma_f32_16x16x32_bf16 v[64:67], v[210:213], v[98:101], 0\n"
        "s_add_u32 s91, s89, 294336\n"
        "v_mfma_f32_16x16x32_bf16 v[64:67], v[168:171], v[142:145], v[64:67]\n"
        "v_mfma_f32_16x16x32_bf16 v[64:67], v[164:167], v[138:141], v[64:67]\n"
        "s_nop 1\n"
        "v_mfma_f32_16x16x32_bf16 v[70:73], v[164:167], v[146:149], 0\n"
        "s_nop 2\n"
        "v_mfma_f32_16x16x32_bf16 v[70:73], v[168:171], v[150:153], v[70:73]\n"
        "s_nop 0\n"
        "v_cvt_pk_bf16_f32 v227, v188, v64\n"
        "v_cvt_pk_bf16_f32 v235, v189, v65\n"
        "v_mfma_f32_16x16x32_bf16 v[70:73], v[214:217], v[94:97], v[70:73]\n"
        "v_cvt_pk_bf16_f32 v239, v190, v66\n"
        "v_cvt_pk_bf16_f32 v243, v191, v67\n"
        "v_mfma_f32_16x16x32_bf16 v[64:67], v[214:217], v[98:101], 0\n"
        "v_mfma_f32_16x16x32_bf16 v[64:67], v[168:171], v[158:161], v[64:67]\n"
        "v_mfma_f32_16x16x32_bf16 v[64:67], v[164:167], v[154:157], v[64:67]\n"
        "s_nop 1\n"
        "v_mfma_f32_16x16x32_bf16 v[162:165], a[144:147], v[218:221], 0\n"
        "s_nop 2\n"
        "v_mfma_f32_16x16x32_bf16 v[162:165], a[148:151], v[222:225], v[162:165]\n"
        "s_nop 0\n"
        "v_cvt_pk_bf16_f32 v228, v70, v64\n"
        "v_cvt_pk_bf16_f32 v236, v71, v65\n"
        "v_mfma_f32_16x16x32_bf16 v[162:165], a[152:155], v[230:233], v[162:165]\n"
        "s_nop 0\n"
        "v_cvt_pk_bf16_f32 v240, v72, v66\n"
        "v_cvt_pk_bf16_f32 v244, v73, v67\n"
        "v_mfma_f32_16x16x32_bf16 v[64:67], a[164:167], v[230:233], 0\n"
        "s_nop 1\n"
        "v_mfma_f32_16x16x32_bf16 v[64:67], a[160:163], v[222:225], v[64:67]\n"
        "s_nop 0\n"
        "v_cvt_pk_bf16_f32 v70, v162, v163\n"
        "v_cvt_pk_bf16_f32 v71, v164, v165\n"
        "v_mfma_f32_16x16x32_bf16 v[64:67], a[156:159], v[218:221], v[64:67]\n"
        "s_nop 0\n"
        "v_mfma_f32_16x16x32_bf16 v[162:165], a[168:171], v[218:221], 0\n"
        "buffer_store_dwordx3 v[226:228], v14, s[40:43], s90 offen offset:384 sc1 nt\n"
        "s_nop 2\n"
        "v_mfma_f32_16x16x32_bf16 v[162:165], a[172:175], v[222:225], v[162:165]\n"
        "s_nop 0\n"
        "v_cvt_pk_bf16_f32 v72, v64, v65\n"
        "v_cvt_pk_bf16_f32 v73, v66, v67\n"
        "v_mfma_f32_16x16x32_bf16 v[162:165], a[176:179], v[230:233], v[162:165]\n"
        "buffer_store_dwordx3 v[234:236], v17, s[40:43], s90 offen offset:1152 sc1 nt\n"
        "s_add_u32 s90, s89, 392448\n"
        "v_mfma_f32_16x16x32_bf16 v[64:67], a[188:191], v[230:233], 0\n"
        "s_nop 2\n"
        "v_mfma_f32_16x16x32_bf16 v[64:67], a[184:187], v[222:225], v[64:67]\n"
        "buffer_store_dwordx3 v[238:240], v14, s[40:43], s91 offen offset:576 sc1 nt\n"
        "v_cvt_pk_bf16_f32 v166, v162, v163\n"
        "v_cvt_pk_bf16_f32 v167, v164, v165\n"
        "v_mfma_f32_16x16x32_bf16 v[64:67], a[180:183], v[218:221], v[64:67]\n"
        "s_nop 0\n"
        "v_mfma_f32_16x16x32_bf16 v[162:165], a[192:195], v[218:221], 0\n"
        "buffer_store_dwordx3 v[242:244], v14, s[40:43], s90 offen sc1 nt\n"
        "s_nop 2\n"
        "v_mfma_f32_16x16x32_bf16 v[162:165], a[196:199], v[222:225], v[162:165]\n"
        "s_nop 0\n"
        "v_cvt_pk_bf16_f32 v168, v64, v65\n"
        "v_cvt_pk_bf16_f32 v169, v66, v67\n"
        "v_mfma_f32_16x16x32_bf16 v[162:165], a[200:203], v[230:233], v[162:165]\n"
        "s_nop 1\n"
        "v_mfma_f32_16x16x32_bf16 v[64:67], a[212:215], v[230:233], 0\n"
        "s_nop 2\n"
        "v_mfma_f32_16x16x32_bf16 v[64:67], a[208:211], v[222:225], v[64:67]\n"
        "s_nop 0\n"
        "v_cvt_pk_bf16_f32 v188, v162, v163\n"
        "v_cvt_pk_bf16_f32 v189, v164, v165\n"
        "v_mfma_f32_16x16x32_bf16 v[64:67], a[204:207], v[218:221], v[64:67]\n"
        "s_nop 1\n"
        "v_mfma_f32_16x16x32_bf16 v[162:165], v[194:197], v[70:73], 0\n"
        "s_nop 2\n"
        "v_mfma_f32_16x16x32_bf16 v[162:165], v[198:201], v[166:169], v[162:165]\n"
        "s_nop 0\n"
        "v_cvt_pk_bf16_f32 v190, v64, v65\n"
        "v_cvt_pk_bf16_f32 v191, v66, v67\n"
        "s_nop 1\n"
        "v_mfma_f32_16x16x32_bf16 v[162:165], v[202:205], v[188:191], v[162:165]\n"
        "s_nop 1\n"
        "v_mfma_f32_16x16x32_bf16 v[64:67], v[214:217], v[188:191], 0\n"
        "s_nop 2\n"
        "v_mfma_f32_16x16x32_bf16 v[64:67], v[210:213], v[166:169], v[64:67]\n"
        "s_nop 0\n"
        "v_mul_f32 v162, v162, v102\n"
        "v_mul_f32 v163, v163, v103\n"
        "v_mfma_f32_16x16x32_bf16 v[64:67], v[206:209], v[70:73], v[64:67]\n"
        "s_nop 0\n"
        "v_mul_f32 v164, v164, v104\n"
        "v_mul_f32 v165, v165, v105\n"
        "v_mfma_f32_16x16x32_bf16 v[226:229], v[218:221], v[70:73], 0\n"
        "s_nop 0\n"
        "v_max3_f32 v60, v162, v163, v164\n"
        "v_mfma_f32_16x16x32_bf16 v[226:229], v[222:225], v[166:169], v[226:229]\n"
        "s_nop 0\n"
        "v_mul_f32 v64, v64, v106\n"
        "v_mul_f32 v65, v65, v107\n"
        "v_mfma_f32_16x16x32_bf16 v[226:229], v[230:233], v[188:191], v[226:229]\n"
        "v_mul_f32 v66, v66, v108\n"
        "v_mul_f32 v67, v67, v109\n"
        "v_mfma_f32_16x16x32_bf16 v[234:237], v[184:187], v[188:191], 0\n"
        "s_nop 0\n"
        "v_max3_f32 v60, v60, v165, v64\n"
        "v_max3_f32 v60, v60, v65, v66\n"
        "v_mfma_f32_16x16x32_bf16 v[234:237], v[180:183], v[166:169], v[234:237]\n"
        "s_nop 0\n"
        "v_mul_f32 v226, v226, v110\n"
        "v_mul_f32 v227, v227, v111\n"
        "v_mfma_f32_16x16x32_bf16 v[234:237], v[176:179], v[70:73], v[234:237]\n"
        "s_nop 0\n"
        "v_mul_f32 v228, v228, v112\n"
        "v_mul_f32 v229, v229, v113\n"
        "v_max3_f32 v60, v60, v67, v226\n"
        "v_max3_f32 v60, v60, v227, v228\n"
        "s_nop 2\n"
        "v_mul_f32 v234, v234, v77\n"
        "v_cndmask_b32_e64 v234, v174, v234, s[62:63]\n"
        "v_max3_f32 v60, v60, v229, v234\n"
        "v_mov_b32 v61, v60\n"
        "s_nop 1\n"
        "v_permlane32_swap_b32 v60, v61\n"
        "v_max_f32 v60, v60, v61\n"
        "v_mov_b32 v61, v60\n"
        "s_nop 1\n"
        "v_permlane16_swap_b32 v60, v61\n"
        "v_max_f32 v60, v60, v61\n"
        "v_mul_f32 v60, v60, v76\n"
        "v_fma_f32 v162, v162, v76, -v60\n"
        "v_exp_f32 v162, v162\n"
        "v_fma_f32 v163, v163, v76, -v60\n"
        "v_exp_f32 v163, v163\n"
        "s_nop 0\n"
        "v_add_f32 v61, v162, v163\n"
        "v_mul_f32 v162, v162, v102\n"
        "v_fma_f32 v234, v234, v76, -v60\n"
        "v_exp_f32 v234, v234\n"
        "v_fma_f32 v229, v229, v76, -v60\n"
        "v_exp_f32 v229, v229\n"
        "v_fma_f32 v228, v228, v76, -v60\n"
        "v_exp_f32 v228, v228\n"
        "v_fma_f32 v227, v227, v76, -v60\n"
        "v_exp_f32 v227, v227\n"
        "v_fma_f32 v226, v226, v76, -v60\n"
        "v_exp_f32 v226, v226\n"
        "v_fma_f32 v67, v67, v76, -v60\n"
        "v_exp_f32 v67, v67\n"
        "v_fma_f32 v66, v66, v76, -v60\n"
        "v_exp_f32 v66, v66\n"
        "v_fma_f32 v65, v65, v76, -v60\n"
        "v_exp_f32 v65, v65\n"
        "v_fma_f32 v64, v64, v76, -v60\n"
        "v_exp_f32 v64, v64\n"
        "v_fma_f32 v165, v165, v76, -v60\n"
        "v_exp_f32 v165, v165\n"
        "v_fma_f32 v164, v164, v76, -v60\n"
        "v_exp_f32 v164, v164\n"
        "s_nop 0\n"
        "v_add_f32 v61, v61, v164\n"
        "v_add_f32 v61, v61, v165\n"
        "v_add_f32 v61, v61, v64\n"
        "v_add_f32 v61, v61, v65\n"
        "v_add_f32 v61, v61, v66\n"
        "v_add_f32 v61, v61, v67\n"
        "v_add_f32 v61, v61, v226\n"
        "v_add_f32 v61, v61, v227\n"
        "v_add_f32 v61, v61, v228\n"
        "v_add_f32 v61, v61, v229\n"
        "v_add_f32 v61, v61, v234\n"
        "v_mov_b32 v60, v61\n"
        "s_nop 1\n"
        "v_permlane32_swap_b32 v61, v60\n"
        "v_add_f32 v61, v61, v60\n"
        "v_mov_b32 v60, v61\n"
        "s_nop 1\n"
        "v_permlane16_swap_b32 v61, v60\n"
        "v_add_f32 v61, v61, v60\n"
        "v_rcp_f32 v61, v61\n"
        "s_nop 0\n"
        "v_mul_f32 v162, v162, v61\n"
        "v_mul_f32 v163, v163, v103\n"
        "v_mul_f32 v163, v163, v61\n"
        "v_cvt_pk_bf16_f32 v166, v162, v163\n"
        "v_mul_f32 v164, v164, v104\n"
        "v_mul_f32 v164, v164, v61\n"
        "v_mul_f32 v165, v165, v105\n"
        "v_mul_f32 v165, v165, v61\n"
        "v_cvt_pk_bf16_f32 v167, v164, v165\n"
        "v_mul_f32 v64, v64, v106\n"
        "v_mul_f32 v64, v64, v61\n"
        "v_mul_f32 v65, v65, v107\n"
        "v_mul_f32 v65, v65, v61\n"
        "v_cvt_pk_bf16_f32 v168, v64, v65\n"
        "v_mul_f32 v66, v66, v108\n"
        "v_mul_f32 v66, v66, v61\n"
        "v_mul_f32 v67, v67, v109\n"
        "v_mul_f32 v67, v67, v61\n"
        "v_cvt_pk_bf16_f32 v169, v66, v67\n"
        "s_nop 1\n"
        "v_mfma_f32_16x16x32_bf16 v[64:67], v[166:169], v[114:117], 0\n"
        "v_mul_f32 v226, v226, v110\n"
        "v_mul_f32 v227, v227, v111\n"
        "v_mul_f32 v226, v226, v61\n"
        "v_mul_f32 v227, v227, v61\n"
        "v_cvt_pk_bf16_f32 v170, v226, v227\n"
        "v_mul_f32 v228, v228, v112\n"
        "v_mul_f32 v228, v228, v61\n"
        "v_mul_f32 v229, v229, v113\n"
        "v_mul_f32 v229, v229, v61\n"
        "v_cvt_pk_bf16_f32 v171, v228, v229\n"
        "v_mul_f32 v234, v234, v77\n"
        "v_mul_f32 v234, v234, v61\n"
        "v_cvt_pk_bf16_f32 v172, v234, 0\n"
        "s_nop 1\n"
        "v_mfma_f32_16x16x32_bf16 v[64:67], v[170:173], v[118:121], v[64:67]\n"
        "v_mfma_f32_16x16x32_bf16 v[64:67], v[218:221], v[94:97], v[64:67]\n"
        "v_mfma_f32_16x16x32_bf16 v[70:73], v[218:221], v[98:101], 0\n"
        "v_mfma_f32_16x16x32_bf16 v[70:73], v[170:173], v[126:129], v[70:73]\n"
        "v_mfma_f32_16x16x32_bf16 v[70:73], v[166:169], v[122:125], v[70:73]\n"
        "s_nop 1\n"
        "v_mfma_f32_16x16x32_bf16 v[162:165], v[166:169], v[130:133], 0\n"
        "s_nop 2\n"
        "v_mfma_f32_16x16x32_bf16 v[162:165], v[170:173], v[134:137], v[162:165]\n"
        "s_add_u32 s91, s89, 490560\n"
        "v_cvt_pk_bf16_f32 v188, v64, v70\n"
        "v_cvt_pk_bf16_f32 v226, v65, v71\n"
        "v_mfma_f32_16x16x32_bf16 v[162:165], v[222:225], v[94:97], v[162:165]\n"
        "v_cvt_pk_bf16_f32 v234, v66, v72\n"
        "v_cvt_pk_bf16_f32 v238, v67, v73\n"
        "v_mfma_f32_16x16x32_bf16 v[64:67], v[222:225], v[98:101], 0\n"
        "v_mfma_f32_16x16x32_bf16 v[64:67], v[170:173], v[142:145], v[64:67]\n"
        "v_mfma_f32_16x16x32_bf16 v[64:67], v[166:169], v[138:141], v[64:67]\n"
        "s_nop 1\n"
        "v_mfma_f32_16x16x32_bf16 v[70:73], v[166:169], v[146:149], 0\n"
        "s_nop 2\n"
        "v_mfma_f32_16x16x32_bf16 v[70:73], v[170:173], v[150:153], v[70:73]\n"
        "s_nop 0\n"
        "v_cvt_pk_bf16_f32 v189, v162, v64\n"
        "v_cvt_pk_bf16_f32 v227, v163, v65\n"
        "v_mfma_f32_16x16x32_bf16 v[70:73], v[230:233], v[94:97], v[70:73]\n"
        "v_cvt_pk_bf16_f32 v235, v164, v66\n"
        "v_cvt_pk_bf16_f32 v239, v165, v67\n"
        "v_mfma_f32_16x16x32_bf16 v[64:67], v[230:233], v[98:101], 0\n"
        "v_mfma_f32_16x16x32_bf16 v[64:67], v[170:173], v[158:161], v[64:67]\n"
        "v_mfma_f32_16x16x32_bf16 v[64:67], v[166:169], v[154:157], v[64:67]\n"
        "s_nop 1\n"
        "v_mfma_f32_16x16x32_bf16 v[162:165], a[144:147], v[176:179], 0\n"
        "s_nop 2\n"
        "v_mfma_f32_16x16x32_bf16 v[162:165], a[148:151], v[180:183], v[162:165]\n"
        "s_nop 0\n"
        "v_cvt_pk_bf16_f32 v190, v70, v64\n"
        "v_cvt_pk_bf16_f32 v228, v71, v65\n"
        "v_mfma_f32_16x16x32_bf16 v[162:165], a[152:155], v[184:187], v[162:165]\n"
        "s_nop 0\n"
        "v_cvt_pk_bf16_f32 v236, v72, v66\n"
        "v_cvt_pk_bf16_f32 v240, v73, v67\n"
        "v_mfma_f32_16x16x32_bf16 v[64:67], a[164:167], v[184:187], 0\n"
        "s_nop 1\n"
        "v_mfma_f32_16x16x32_bf16 v[64:67], a[160:163], v[180:183], v[64:67]\n"
        "s_nop 0\n"
        "v_cvt_pk_bf16_f32 v70, v162, v163\n"
        "v_cvt_pk_bf16_f32 v71, v164, v165\n"
        "v_mfma_f32_16x16x32_bf16 v[64:67], a[156:159], v[176:179], v[64:67]\n"
        "s_nop 0\n"
        "v_mfma_f32_16x16x32_bf16 v[162:165], a[168:171], v[176:179], 0\n"
        "buffer_store_dwordx3 v[188:190], v15, s[40:43], s90 offen offset:768 sc1 nt\n"
        "s_add_u32 s90, s89, 588672\n"
        "s_nop 1\n"
        "v_mfma_f32_16x16x32_bf16 v[162:165], a[172:175], v[180:183], v[162:165]\n"
        "s_nop 0\n"
        "v_cvt_pk_bf16_f32 v72, v64, v65\n"
        "v_cvt_pk_bf16_f32 v73, v66, v67\n"
        "v_mfma_f32_16x16x32_bf16 v[162:165], a[176:179], v[184:187], v[162:165]\n"
        "buffer_store_dwordx3 v[226:228], v14, s[40:43], s91 offen offset:192 sc1 nt\n"
        "s_nop 0\n"
        "v_mfma_f32_16x16x32_bf16 v[64:67], a[188:191], v[184:187], 0\n"
        "s_nop 2\n"
        "v_mfma_f32_16x16x32_bf16 v[64:67], a[184:187], v[180:183], v[64:67]\n"
        "buffer_store_dwordx3 v[234:236], v16, s[40:43], s91 offen offset:960 sc1 nt\n"
        "v_cvt_pk_bf16_f32 v166, v162, v163\n"
        "v_cvt_pk_bf16_f32 v167, v164, v165\n"
        "v_mfma_f32_16x16x32_bf16 v[64:67], a[180:183], v[176:179], v[64:67]\n"
        "s_nop 0\n"
        "v_mfma_f32_16x16x32_bf16 v[162:165], a[192:195], v[176:179], 0\n"
        "buffer_store_dwordx3 v[238:240], v14, s[40:43], s90 offen offset:384 sc1 nt\n"
        "s_nop 2\n"
        "v_mfma_f32_16x16x32_bf16 v[162:165], a[196:199], v[180:183], v[162:165]\n"
        "s_nop 0\n"
        "v_cvt_pk_bf16_f32 v168, v64, v65\n"
        "v_cvt_pk_bf16_f32 v169, v66, v67\n"
        "v_mfma_f32_16x16x32_bf16 v[162:165], a[200:203], v[184:187], v[162:165]\n"
        "s_nop 1\n"
        "v_mfma_f32_16x16x32_bf16 v[64:67], a[212:215], v[184:187], 0\n"
        "s_nop 2\n"
        "v_mfma_f32_16x16x32_bf16 v[64:67], a[208:211], v[180:183], v[64:67]\n"
        "s_nop 0\n"
        "v_cvt_pk_bf16_f32 v170, v162, v163\n"
        "v_cvt_pk_bf16_f32 v171, v164, v165\n"
        "v_mfma_f32_16x16x32_bf16 v[64:67], a[204:207], v[176:179], v[64:67]\n"
        "s_nop 1\n"
        "v_mfma_f32_16x16x32_bf16 v[162:165], v[194:197], v[70:73], 0\n"
        "s_nop 2\n"
        "v_mfma_f32_16x16x32_bf16 v[162:165], v[198:201], v[166:169], v[162:165]\n"
        "s_nop 0\n"
        "v_cvt_pk_bf16_f32 v172, v64, v65\n"
        "v_cvt_pk_bf16_f32 v173, v66, v67\n"
        "s_nop 1\n"
        "v_mfma_f32_16x16x32_bf16 v[162:165], v[202:205], v[170:173], v[162:165]\n"
        "s_nop 1\n"
        "v_mfma_f32_16x16x32_bf16 v[64:67], v[214:217], v[170:173], 0\n"
        "s_nop 2\n"
        "v_mfma_f32_16x16x32_bf16 v[64:67], v[210:213], v[166:169], v[64:67]\n"
        "s_nop 0\n"
        "v_mul_f32 v162, v162, v102\n"
        "v_mul_f32 v163, v163, v103\n"
        "v_mfma_f32_16x16x32_bf16 v[64:67], v[206:209], v[70:73], v[64:67]\n"
        "s_nop 0\n"
        "v_mul_f32 v164, v164, v104\n"
        "v_mul_f32 v165, v165, v105\n"
        "v_mfma_f32_16x16x32_bf16 v[188:191], v[218:221], v[70:73], 0\n"
        "s_nop 0\n"
        "v_max3_f32 v60, v162, v163, v164\n"
        "v_mfma_f32_16x16x32_bf16 v[188:191], v[222:225], v[166:169], v[188:191]\n"
        "s_nop 0\n"
        "v_mul_f32 v64, v64, v106\n"
        "v_mul_f32 v65, v65, v107\n"
        "v_mfma_f32_16x16x32_bf16 v[188:191], v[230:233], v[170:173], v[188:191]\n"
        "v_mul_f32 v66, v66, v108\n"
        "v_mul_f32 v67, v67, v109\n"
        "v_mfma_f32_16x16x32_bf16 v[192:195], v[184:187], v[170:173], 0\n"
        "s_nop 0\n"
        "v_max3_f32 v60, v60, v165, v64\n"
        "v_max3_f32 v60, v60, v65, v66\n"
        "v_mfma_f32_16x16x32_bf16 v[192:195], v[180:183], v[166:169], v[192:195]\n"
        "s_nop 0\n"
        "v_mul_f32 v188, v188, v110\n"
        "v_mul_f32 v189, v189, v111\n"
        "v_mfma_f32_16x16x32_bf16 v[192:195], v[176:179], v[70:73], v[192:195]\n"
        "s_nop 0\n"
        "v_mul_f32 v190, v190, v112\n"
        "v_mul_f32 v191, v191, v113\n"
        "v_max3_f32 v60, v60, v67, v188\n"
        "v_max3_f32 v60, v60, v189, v190\n"
        "s_nop 2\n"
        "v_mul_f32 v192, v192, v77\n"
        "v_cndmask_b32_e64 v192, v174, v192, s[62:63]\n"
        "v_max3_f32 v60, v60, v191, v192\n"
        "v_mov_b32 v61, v60\n"
        "s_nop 1\n"
        "v_permlane32_swap_b32 v60, v61\n"
        "v_max_f32 v60, v60, v61\n"
        "v_mov_b32 v61, v60\n"
        "s_nop 1\n"
        "v_permlane16_swap_b32 v60, v61\n"
        "v_max_f32 v60, v60, v61\n"
        "v_mul_f32 v60, v60, v77\n"
        "v_fma_f32 v162, v162, v77, -v60\n"
        "v_exp_f32 v162, v162\n"
        "v_fma_f32 v163, v163, v77, -v60\n"
        "v_exp_f32 v163, v163\n"
        "s_nop 0\n"
        "v_add_f32 v61, v162, v163\n"
        "v_mul_f32 v162, v162, v102\n"
        "v_fma_f32 v192, v192, v77, -v60\n"
        "v_exp_f32 v192, v192\n"
        "v_fma_f32 v191, v191, v77, -v60\n"
        "v_exp_f32 v191, v191\n"
        "v_fma_f32 v190, v190, v77, -v60\n"
        "v_exp_f32 v190, v190\n"
        "v_fma_f32 v189, v189, v77, -v60\n"
        "v_exp_f32 v189, v189\n"
        "v_fma_f32 v188, v188, v77, -v60\n"
        "v_exp_f32 v188, v188\n"
        "v_fma_f32 v67, v67, v77, -v60\n"
        "v_exp_f32 v67, v67\n"
        "v_fma_f32 v66, v66, v77, -v60\n"
        "v_exp_f32 v66, v66\n"
        "v_fma_f32 v65, v65, v77, -v60\n"
        "v_exp_f32 v65, v65\n"
        "v_fma_f32 v64, v64, v77, -v60\n"
        "v_exp_f32 v64, v64\n"
        "v_fma_f32 v165, v165, v77, -v60\n"
        "v_exp_f32 v165, v165\n"
        "v_fma_f32 v164, v164, v77, -v60\n"
        "v_exp_f32 v164, v164\n"
        "s_nop 0\n"
        "v_add_f32 v61, v61, v164\n"
        "v_add_f32 v61, v61, v165\n"
        "v_add_f32 v61, v61, v64\n"
        "v_add_f32 v61, v61, v65\n"
        "v_add_f32 v61, v61, v66\n"
        "v_add_f32 v61, v61, v67\n"
        "v_add_f32 v61, v61, v188\n"
        "v_add_f32 v61, v61, v189\n"
        "v_add_f32 v61, v61, v190\n"
        "v_add_f32 v61, v61, v191\n"
        "v_add_f32 v61, v61, v192\n"
        "v_mov_b32 v60, v61\n"
        "s_nop 1\n"
        "v_permlane32_swap_b32 v61, v60\n"
        "v_add_f32 v61, v61, v60\n"
        "v_mov_b32 v60, v61\n"
        "s_nop 1\n"
        "v_permlane16_swap_b32 v61, v60\n"
        "v_add_f32 v61, v61, v60\n"
        "v_rcp_f32 v61, v61\n"
        "s_nop 0\n"
        "v_mul_f32 v162, v162, v61\n"
        "v_mul_f32 v163, v163, v103\n"
        "v_mul_f32 v163, v163, v61\n"
        "v_cvt_pk_bf16_f32 v168, v162, v163\n"
        "v_mul_f32 v164, v164, v104\n"
        "v_mul_f32 v164, v164, v61\n"
        "v_mul_f32 v165, v165, v105\n"
        "v_mul_f32 v165, v165, v61\n"
        "v_cvt_pk_bf16_f32 v169, v164, v165\n"
        "v_mul_f32 v64, v64, v106\n"
        "v_mul_f32 v64, v64, v61\n"
        "v_mul_f32 v65, v65, v107\n"
        "v_mul_f32 v65, v65, v61\n"
        "v_cvt_pk_bf16_f32 v170, v64, v65\n"
        "v_mul_f32 v66, v66, v108\n"
        "v_mul_f32 v66, v66, v61\n"
        "v_mul_f32 v67, v67, v109\n"
        "v_mul_f32 v67, v67, v61\n"
        "v_cvt_pk_bf16_f32 v171, v66, v67\n"
        "s_nop 1\n"
        "v_mfma_f32_16x16x32_bf16 v[64:67], v[168:171], v[114:117], 0\n"
        "v_mul_f32 v188, v188, v110\n"
        "v_mul_f32 v189, v189, v111\n"
        "v_mul_f32 v188, v188, v61\n"
        "v_mul_f32 v189, v189, v61\n"
        "v_cvt_pk_bf16_f32 v172, v188, v189\n"
        "v_mul_f32 v190, v190, v112\n"
        "v_mul_f32 v190, v190, v61\n"
        "v_mul_f32 v191, v191, v113\n"
        "v_mul_f32 v191, v191, v61\n"
        "v_cvt_pk_bf16_f32 v173, v190, v191\n"
        "v_mul_f32 v192, v192, v77\n"
        "v_mul_f32 v192, v192, v61\n"
        "v_cvt_pk_bf16_f32 v174, v192, 0\n"
        "s_nop 1\n"
        "v_mfma_f32_16x16x32_bf16 v[64:67], v[172:175], v[118:121], v[64:67]\n"
        "v_mfma_f32_16x16x32_bf16 v[64:67], v[176:179], v[94:97], v[64:67]\n"
        "v_mfma_f32_16x16x32_bf16 v[70:73], v[176:179], v[98:101], 0\n"
        "v_mfma_f32_16x16x32_bf16 v[70:73], v[172:175], v[126:129], v[70:73]\n"
        "v_mfma_f32_16x16x32_bf16 v[70:73], v[168:171], v[122:125], v[70:73]\n"
        "s_nop 1\n"
        "v_mfma_f32_16x16x32_bf16 v[102:105], v[168:171], v[130:133], 0\n"
        "s_nop 2\n"
        "v_mfma_f32_16x16x32_bf16 v[102:105], v[172:175], v[134:137], v[102:105]\n"
        "s_nop 0\n"
        "v_cvt_pk_bf16_f32 v106, v64, v70\n"
        "v_mfma_f32_16x16x32_bf16 v[102:105], v[180:183], v[94:97], v[102:105]\n"
        "v_mfma_f32_16x16x32_bf16 v[64:67], v[180:183], v[98:101], 0\n"
        "v_mfma_f32_16x16x32_bf16 v[64:67], v[172:175], v[142:145], v[64:67]\n"
        "v_mfma_f32_16x16x32_bf16 v[64:67], v[168:171], v[138:141], v[64:67]\n"
        "s_nop 1\n"
        "v_mfma_f32_16x16x32_bf16 v[70:73], v[168:171], v[146:149], 0\n"
        "s_nop 2\n"
        "v_mfma_f32_16x16x32_bf16 v[70:73], v[172:175], v[150:153], v[70:73]\n"
        "s_nop 0\n"
        "v_cvt_pk_bf16_f32 v107, v102, v64\n"
        "v_mfma_f32_16x16x32_bf16 v[70:73], v[184:187], v[94:97], v[70:73]\n"
        "v_mfma_f32_16x16x32_bf16 v[64:67], v[184:187], v[98:101], 0\n"
        "v_mfma_f32_16x16x32_bf16 v[64:67], v[172:175], v[158:161], v[64:67]\n"
        "s_add_u32 s46, s89, 590016\n"
        "v_mfma_f32_16x16x32_bf16 v[64:67], v[168:171], v[154:157], v[64:67]\n"
        "s_nop 2\n"
        "s_min_u32 s46, s46, s42\n"
        "s_nop 3\n"
        "v_cvt_pk_bf16_f32 v108, v70, v64\n"
        "buffer_store_dwordx3 v[106:108], v17, s[44:47], s90 offen offset:1152 sc1 nt\n"
        "s_nop 15\n"
        "s_cmp_lt_i32 s85, -1\n"
        "s_cbranch_scc1 FIN_%=\n"
        "s_mov_b32 s89, 0x80000000\n"
        "s_waitcnt vmcnt(0)\n"
        "ds_read_b128 v[104:107], v9\n"
        "ds_read_b128 v[110:113], v10\n"
        "ds_read_b128 v[116:119], v11\n"
        "ds_read_b128 v[124:127], v9 offset:3072\n"
        "ds_read_b128 v[130:133], v10 offset:3072\n"
        "ds_read_b128 v[136:139], v11 offset:3072\n"
        "ds_read_b128 v[144:147], v9 offset:6144\n"
        "ds_read_b128 v[150:153], v10 offset:6144\n"
        "ds_read_b128 v[156:159], v11 offset:6144\n"
        "s_waitcnt lgkmcnt(0)\n"
        "s_nop 7\n"
        "s_cmp_ge_i32 s85, 0\n"
        "s_cbranch_scc1 TOP_%=\n"
        "s_branch LAST_%=\n"
        "SP4_%=:\n"
        "s_sleep 1\n"
        "ds_read_u16 v73, v19 offset:3072\n"
        "s_waitcnt lgkmcnt(0)\n"
        "v_cmp_ne_u32_e64 s[24:25], s30, v73\n"
        "s_nop 4\n"
        "s_cmp_eq_u64 s[24:25], 0\n"
        "s_cbranch_scc1 BK4_%=\n"
        "s_sub_u32 s31, s31, 1\n"
        "s_cbranch_scc0 SP4_%=\n"
        "s_endpgm\n"
        "SP8_%=:\n"
        "s_sleep 1\n"
        "ds_read_u16 v123, v19 offset:3584\n"
        "s_waitcnt lgkmcnt(0)\n"
        "v_cmp_ne_u32_e64 s[24:25], s29, v123\n"
        "s_nop 4\n"
        "s_cmp_eq_u64 s[24:25], 0\n"
        "s_cbranch_scc1 BK8_%=\n"
        "s_sub_u32 s31, s31, 1\n"
        "s_cbranch_scc0 SP8_%=\n"
        "s_endpgm\n"
        "SP17_%=:\n"
        "s_sleep 1\n"
        "ds_read_u16 v76, v19 offset:3072\n"
        "s_waitcnt lgkmcnt(0)\n"
        "v_cmp_ne_u32_e64 s[24:25], s30, v76\n"
        "s_nop 4\n"
        "s_cmp_eq_u64 s[24:25], 0\n"
        "s_cbranch_scc1 BK17_%=\n"
        "s_sub_u32 s31, s31, 1\n"
        "s_cbranch_scc0 SP17_%=\n"
        "s_endpgm\n"
        :: "{v[0:15]}"(c0), "{v[16:23]}"(c1), "{v[24:27]}"(c2), "{v[200:207]}"(sp), "{v[28:43]}"(f0), "{v[44:47]}"(f1), "{s[16:19]}"(srdl), "{s[20:23]}"(sol), "{s[36:39]}"(srdx), "{s[40:43]}"(srdo), "{s[48:55]}"(si)
        : "v48", "v49", "v50", "v51", "v52", "v53", "v54", "v55", "v56", "v57", "v58", "v59", "v60", "v61", "v62", "v63", "v64", "v65", "v66", "v67", "v68", "v69", "v70", "v71", "v72", "v73", "v74", "v75", "v76", "v77", "v78", "v79", "v80", "v81", "v82", "v83", "v84", "v85", "v86", "v87", "v88", "v89", "v90", "v91", "v92", "v93", "v94", "v95", "v96", "v97", "v98", "v99", "v100", "v101", "v102", "v103", "v104", "v105", "v106", "v107", "v108", "v109", "v110", "v111", "v112", "v113", "v114", "v115", "v116", "v117", "v118", "v119", "v120", "v121", "v122", "v123", "v124", "v125", "v126", "v127", "v128", "v129", "v130", "v131", "v132", "v133", "v134", "v135", "v136", "v137", "v138", "v139", "v140", "v141", "v142", "v143", "v144", "v145", "v146", "v147", "v148", "v149", "v150", "v151", "v152", "v153", "v154", "v155", "v156", "v157", "v158", "v159", "v160", "v161", "v162", "v163", "v164", "v165", "v166", "v167", "v168", "v169", "v170", "v171", "v172", "v173", "v174", "v175", "v176", "v177", "v178", "v179", "v180", "v181", "v182", "v183", "v184", "v185", "v186", "v187", "v188", "v189", "v190", "v191", "v192", "v193", "v194", "v195", "v196", "v197", "v198", "v199", "v208", "v209", "v210", "v211", "v212", "v213", "v214", "v215", "v216", "v217", "v218", "v219", "v220", "v221", "v222", "v223", "v224", "v225", "v226", "v227", "v228", "v229", "v230", "v231", "v232", "v233", "v234", "v235", "v236", "v237", "v238", "v239", "v240", "v241", "v242", "v243", "v244", "v245", "v246", "v247", "v248", "v249", "v250", "v251", "v252", "v253", "v254", "v255", "a0", "a1", "a2", "a3", "a4", "a5", "a6", "a7", "a8", "a9", "a10", "a11", "a12", "a13", "a14", "a15", "a16", "a17", "a18", "a19", "a20", "a21", "a22", "a23", "a24", "a25", "a26", "a27", "a28", "a29", "a30", "a31", "a32", "a33", "a34", "a35", "a36", "a37", "a38", "a39", "a40", "a41", "a42", "a43", "a44", "a45", "a46", "a47", "a48", "a49", "a50", "a51", "a52", "a53", "a54", "a55", "a56", "a57", "a58", "a59", "a60", "a61", "a62", "a63", "a64", "a65", "a66", "a67", "a68", "a69", "a70", "a71", "a72", "a73", "a74", "a75", "a76", "a77", "a78", "a79", "a80", "a81", "a82", "a83", "a84", "a85", "a86", "a87", "a88", "a89", "a90", "a91", "a92", "a93", "a94", "a95", "a96", "a97", "a98", "a99", "a100", "a101", "a102", "a103", "a104", "a105", "a106", "a107", "a108", "a109", "a110", "a111", "a112", "a113", "a114", "a115", "a116", "a117", "a118", "a119", "a120", "a121", "a122", "a123", "a124", "a125", "a126", "a127", "a128", "a129", "a130", "a131", "a132", "a133", "a134", "a135", "a136", "a137", "a138", "a139", "a140", "a141", "a142", "a143", "a144", "a145", "a146", "a147", "a148", "a149", "a150", "a151", "a152", "a153", "a154", "a155", "a156", "a157", "a158", "a159", "a160", "a161", "a162", "a163", "a164", "a165", "a166", "a167", "a168", "a169", "a170", "a171", "a172", "a173", "a174", "a175", "a176", "a177", "a178", "a179", "a180", "a181", "a182", "a183", "a184", "a185", "a186", "a187", "a188", "a189", "a190", "a191", "a192", "a193", "a194", "a195", "a196", "a197", "a198", "a199", "a200", "a201", "a202", "a203", "a204", "a205", "a206", "a207", "a208", "a209", "a210", "a211", "a212", "a213", "a214", "a215", "a216", "a217", "a218", "a219", "a220", "a221", "a222", "a223", "a224", "a225", "a226", "a227", "a228", "a229", "a230", "a231", "a232", "a233", "a234", "a235", "a236", "a237", "a238", "a239", "a240", "a241", "a242", "a243", "a244", "a245", "a246", "a247", "a248", "a249", "a250", "a251", "a252", "a253", "a254", "a255", "s24", "s25", "s26", "s27", "s28", "s29", "s30", "s31", "s44", "s45", "s46", "s47", "s56", "s57", "s58", "s59", "s60", "s61", "s62", "s63", "s64", "s65", "s66", "s67", "s68", "s69", "s70", "s71", "s72", "s73", "s74", "s75", "s76", "s77", "s78", "s79", "s80", "s81", "s82", "s83", "s84", "s85", "s86", "s87", "s88", "s89", "s90", "s91", "s92", "s93", "s94", "s95", "vcc", "scc", "memory");
}

extern "C" void kernel_launch(void* const* d_in, const int* in_sizes, int n_in, void* d_out, int out_size, void* d_ws, size_t ws_size, hipStream_t stream) {
    (void)in_sizes; (void)n_in; (void)out_size; (void)ws_size;
    (void)hipFuncSetAttribute((const void*)swin_kernel, hipFuncAttributeMaxDynamicSharedMemorySize, LDS_BYTES);
    swin_kernel<<<GRID, 256, LDS_BYTES, stream>>>((const char*)d_in[0], (const char*)d_in[1], (const char*)d_in[2], (const char*)d_in[3], (const char*)d_in[4], (char*)d_out, (const char*)d_ws);
}
```

```cpp
#define NLC 5
#define WSTEP 32
#define CPL 8
#define SMASK 31914
#define ODDS 1
#include <hip/hip_runtime.h>
#include <hip/hip_bf16.h>

#define DI __device__ __forceinline__

using u32x2 = __attribute__((ext_vector_type(2))) unsigned;
using u32x4 = __attribute__((ext_vector_type(4))) unsigned;
using u32x8 = __attribute__((ext_vector_type(8))) unsigned;
using u32x16 = __attribute__((ext_vector_type(16))) unsigned;

constexpr int HH = 511, WW = 511, NWX = 73, NWIN = NWX * NWX, GRID = 256;
constexpr int TOKB = 192, ROWB = WW * TOKB;
constexpr unsigned FRAMEB = (unsigned)HH * ROWB, TOTALB = FRAMEB * 4;
constexpr unsigned FS = 9232, L_BUF = 0, L_STG = 4 * FS, REC = 784, NREC = 21, L_EXT = L_STG + 4 * 9216, L_MS = L_EXT + 4 * NREC * REC, L_STA = L_MS + 18432, L_PSC = L_STA + 512,
                   L_STB = L_PSC + 512, L_AUX = L_STB + 512, L_SLW = L_AUX + 1024, L_DUM = L_SLW + 1024, L_FA = L_DUM + 512, LDS_BYTES = 163840;
constexpr unsigned RAWM = 18432, L_GT = L_AUX + 768;
static_assert(L_EXT + 3 * RAWM <= L_MS && ((L_STG >> 10) & 3) == 0 && (L_STG & 1023) < 512 && L_FA + 1024 <= LDS_BYTES && (NWIN + GRID - 1) / GRID <= NREC, "LDS");

#define LP(T, a) ((__attribute__((address_space(3))) T*)(size_t)(unsigned)(a))
#define SB() __builtin_amdgcn_sched_barrier(0)
#define PIN(v) asm volatile("" : "+v"(v))
DI unsigned raw_lane(unsigned lane, unsigned m, unsigned A, unsigned B, unsigned C) {
    const unsigned n = 64 * m + lane, row = (n * 171) >> 11, pc = n - 12 * row;
    return (row >> 3) * (A & 0xffff) + ((row >> 1) & 3) * (B & 0xffff) + (row & 1) * (C & 0xffff) + (pc ^ ((row >> 2) & 3)) * 16 - 1024 * m;
}
DI u32x4 make_srd(unsigned lo, unsigned hi, unsigned bytes) { return u32x4{lo, hi & 0xffffu, bytes, 0x00020000u}; }
DI void raw_dma(unsigned v0, unsigned v1, unsigned v2, const u32x4& srd, unsigned so, unsigned dst) {
    asm volatile("s_mov_b32 m0, %5\n\ts_nop 0\n\tbuffer_load_dwordx4 %0, %3, %4 offen lds\n\tbuffer_load_dwordx4 %1, %3, %4 offen offset:1024 lds\n\tbuffer_load_dwordx4 %2, %3, %4 offen offset:2048 lds"
                 :: "v"(v0), "v"(v1), "v"(v2), "s"(srd), "s"(so), "s"(dst) : "memory");
}

__global__ __launch_bounds__(256, 1) void swin_kernel(const char* __restrict__ x_, const char* __restrict__ wqkv_t, const char* __restrict__ wo_t, const char* __restrict__ wqkv_s,
                                                           const char* __restrict__ wo_s, char* __restrict__ out_, const char* wf) {
    const unsigned k = __builtin_amdgcn_readfirstlane(threadIdx.x >> 6) & 3, lane = threadIdx.x & 63, k1 = k & 1, k2 = k >> 1;
    u32x2 kx, ko, kl, kr, kp;
    const unsigned long long kap = (unsigned long long)__builtin_amdgcn_kernarg_segment_ptr();
    asm volatile("s_load_dwordx2 %0, %4, %5\n\ts_load_dwordx2 %1, %4, %6\n\ts_load_dwordx2 %2, %4, 0x0\n\ts_load_dwordx2 %3, %4, 0x28"
                 : "=&s"(kr), "=&s"(kl), "=&s"(kx), "=&s"(ko) : "s"(kap), "s"(8 + 8 * k), "s"(8 + 16 * k2));
    SB();
    {
        unsigned long long pc; unsigned co = ((((blockIdx.x >> 3) * 4 + k) * CPL + lane / (64 / CPL)) & 511) * 128;
        asm volatile("s_getpc_b64 %0\n.Lswin_pc:\n\ts_mov_b32 m0, %2\n\tv_min_u32 %1, .Lswin_fin-.Lswin_pc, %1\n\tglobal_load_lds_dword %1, %0" : "=&s"(pc), "+v"(co) : "s"(L_DUM + 256) : "memory");
    }
    SB();
#define TS(i)
    const unsigned Bl = 1152 + 2304 * k2, Ql = 4608 - 3456 * k2;
    const unsigned Ar = 13824 - 12288 * k1, Br = k1 ? 384 : Bl, Cr = 576 - 384 * k1, Pr = 27648 - 24576 * k1, Qr = k1 ? 6144 : Ql, cr = 192 - 192 * k1, szr = 55296 - 36864 * k1;
    const unsigned ldr = L_STG + k * RAWM + k2 * RAWM, ldl = k1 * RAWM + k2 * (L_MS - k1 * (L_MS - L_EXT + RAWM));
    static_assert(L_STG + 2 * RAWM == L_EXT, "");
    unsigned vr[3];
#pragma unroll
    for (int m = 0; m < 3; ++m) { vr[m] = raw_lane(lane, m, Ar, Br, Cr); PIN(vr[m]); }
    unsigned vW = (((blockIdx.x >> 3) & 31) * 14 + lane) * 128 + (lane >= 14) * 0x40000000u; PIN(vW);
    SB();
    asm volatile("s_waitcnt lgkmcnt(0)" : "+s"(kr), "+s"(kl), "+s"(kx), "+s"(ko));
    const u32x4 srdr = make_srd(kr[0], kr[1], szr);
    asm volatile("s_mov_b32 m0, %2\n\ts_nop 0\n\tbuffer_load_dword %0, %1, 0 offen lds" :: "v"(vW), "s"(srdr), "s"(L_DUM) : "memory");
#define RDMA(n) do { SB(); raw_dma(vr[0], vr[1], vr[2], srdr, cr + (n & 1) * Pr + (n >> 1) * Qr, ldr + n * 3072); SB(); } while (0)
    RDMA(0); RDMA(1);
    TS(1);
    const u32x4 srdl = make_srd(kl[0], kl[1], 55296), srdx = make_srd(kx[0], kx[1], TOTALB), srdo = make_srd(ko[0], ko[1], TOTALB);
    const unsigned vb = (blockIdx.x & 7) * (GRID / 8) + (blockIdx.x >> 3);
    const unsigned grp = blockIdx.x & 7, gs = (grp >> 1) * 1332 + (grp & 1) * 672, gn = (grp & 1) ? 660 + (grp == 7) : 672;
    const unsigned w0 = gs + (blockIdx.x >> 3), nwin = (gn - (blockIdx.x >> 3) + 31) / 32, wy0 = w0 / NWX, wx0 = w0 - wy0 * NWX;
    static_assert(WSTEP == 32 && 4 * 1332 + 1 == NWIN, "");
    const unsigned base0 = wy0 * 7 * ROWB + wx0 * 7 * TOKB;
#define SLOT(n) do { constexpr int a_ = __builtin_popcount(SMASK & ((1 << (n)) - 1)); if ((SMASK >> (n)) & 1) { if (a_ < 4) RDMA((a_ + 2)); else LDMA((a_ - 4)); } } while (0)
    static_assert(__builtin_popcount(SMASK) == 4 + NLC, "");
    const unsigned i = lane & 15, g = lane >> 4, p = i >> 2, t = i & 3, cl = 384 * k1;
    u32x16 c0, f0; u32x8 c1, sp; u32x4 c2;
    const unsigned stg = L_STG + k * 9216, ext = L_EXT + k * (NREC * REC);
    unsigned vl[3];
#pragma unroll
    for (int m = 0; m < 3; ++m) { vl[m] = raw_lane(lane, m, 13824, Bl, 576); PIN(vl[m]); }
#define LDMA(n) do { SB(); raw_dma(vl[0], vl[1], vl[2], srdl, cl + (n & 1) * 27648 + (n >> 1) * Ql, ldl + n * 3072); SB(); } while (0)
    SLOT(0);
#pragma unroll
    for (int a = 0; a < 2; ++a) {
        const unsigned ra = (8 * p + 4 * a + t) * 192 + ((g ^ ((2 * p + a) & 3)) * 16);
        f0[a] = ldl + ra; f0[2 + a] = ldr + ra; f0[4 + a] = ldr + (8 * g + 4 * a + p) * 192 + ((t ^ ((2 * g + a) & 3)) * 16);
        PIN(f0[a]); PIN(f0[2 + a]); PIN(f0[4 + a]);
    }
    SLOT(1);
#pragma unroll
    for (int a = 0; a < 2; ++a) {
        const unsigned n = 16 * a + 4 * k + p;
        const unsigned w = w0 + n * WSTEP;
        f0[6 + a] = n < nwin ? t * FRAMEB + w * (7 * TOKB) + ((w * 57457) >> 22) * (7 * ROWB - NWX * 7 * TOKB) + 6 * ROWB + 6 * TOKB + g * 48 : 0x80000000u; PIN(f0[6 + a]);
        if (a == 0) SLOT(2);
    }
    f0[8] = i == 0 ? L_AUX + 192 + g * 16 : L_DUM; f0[15] = 0;
    SLOT(3);
    {
        const unsigned sh = 2 - 2 * k2, ts = 3072 + 16 * k2, ml = 64 - 48 * k2, xm = 64 * (k == 1), a0 = ldl + g * 256 + (k == 1 ? t * 16 : k1 ? 0 : (i ^ ((g & k & 2) << 2)) * 16);
#pragma unroll
        for (int n = 0; n < 6; ++n) {
            const unsigned z = (32 * (n >> 1) + 4 * (n & 1) + 8 * p + t) >> sh;
            const unsigned z6 = (z * 43) >> 8, tl = z - 6 * z6;
            f0[9 + n] = a0 + (tl * ts + ((z6 * ml) ^ ((tl << 5) & xm))) * k1 + n * 3072 * (1 - k1); PIN(f0[9 + n]);
            if (n == 0) SLOT(4);
            if (n == 2) SLOT(5);
            if (n == 4) SLOT(6);
        }
    }
    SLOT(7);
#pragma unroll
    for (int m = 0; m < 3; ++m) {
        const unsigned n = 64 * m + lane, tf = (n * 171) >> 13, r = n - 48 * tf, q = (r * 43) >> 9;
        const unsigned c = tf * FRAMEB + ((r - 12 * q) ^ tf) * 16;
#pragma unroll
        for (int u = 0; u < 3; ++u) {
            const unsigned px = 16 * u + 4 * k + q;
            c0[3 * u + m] = c + px * TOKB + ((px * 37) >> 8) * (ROWB - 7 * TOKB) - 1024 * ((3 * u + m) % 4); PIN(c0[3 * u + m]);
        }
        if (m == 0) SLOT(8);
        if (m == 1) SLOT(9);
        if (m == 2) SLOT(10);
    }
#pragma unroll
    for (int S = 0; S < 3; ++S) c0[9 + S] = stg + t * 768 + p * TOKB + ((3 * g + S) ^ t) * 16;
    c0[12] = L_BUF + t * FS + g * 256 + p * 64 + k * 16;
    c0[13] = L_BUF + k * FS + lane * 16;
    const unsigned vs = g * TOKB + 12 * i, brk = ROWB - 7 * TOKB, u = (p - g) & 3;
    c0[14] = vs; c0[15] = vs + (g >= 3 ? brk : 0);
    PIN(c0);
    SLOT(11);
    c1[0] = vs + (g >= 2 ? brk : 0); c1[1] = vs + (g >= 1 ? brk : 0);
    c1[2] = u < 3 ? L_STA + t * 128 + g * 32 + u * 8 + k * 2 : L_DUM;
    c1[3] = L_STB + t * 128 + (4 * g + k) * 8 + u * 2;
    c1[4] = L_STA + k * 128 + g * 32;
    c1[5] = L_STB + k * 128 + i * 8;
    c1[6] = L_STA + k * 128 + p * 32 + g * 8 + t * 2;
    c1[7] = ext - REC + (i & 1) * 192 + g * 16;
    c2[0] = ext - REC + 384 + 24 * i;
    c2[1] = p == g ? ~0u : 0u;
    c2[2] = L_AUX + (t < 2 ? t : 2) * 192 + g * 16;
    c2[3] = p == g ? L_AUX + 576 + t * 16 : L_AUX + 384;
    PIN(c1); PIN(c2);
    SLOT(12);
    sp[0] = lane * 16; sp[1] = L_MS + (16 * t + ((4 * g + p) ^ ((t & 2) << 2))) * 16;
#pragma unroll
    for (int T = 0; T < 2; ++T) {
        const unsigned n = 16 * T + 4 * t + p;
        sp[2 + T] = n < NREC ? ext + n * REC + g * 16 : L_DUM; sp[4 + T] = n < NREC && p == g ? ext + n * REC + 768 : L_DUM;
    }
    sp[6] = ext + g * REC + 384 + 24 * i; sp[7] = g == 0 ? ext + 20 * REC + 384 + 24 * i : L_DUM;
    PIN(sp);
    SLOT(13);
    {
        const unsigned e = 64 * k + lane, y = e - 144, one = (y < 16 ? (y >> 2) & 1 : y >> 5) ? 0x3f800000u : 0x3f80u;
        *LP(unsigned, L_AUX + e * 4) = e < 48 ? 0x3f803f80u : e == 192 ? 0x46800000u : y < 48 && t == (y < 16 ? y >> 3 : (y >> 2) & 3) ? one : 0u;
        *LP(unsigned, L_STA + k * 128 + (lane & 6) * 16 + 24 + (lane & 1) * 4) = 0x3c003c00u;
        *LP(unsigned, L_FA + e * 4) = 0u;
    }
    SLOT(14);
    const u32x4 f1 = {vl[0], vl[1], vl[2], 0u}, sol = {cl + Ql, cl + Ql + 27648, cl + 2 * Ql, cl + 2 * Ql + 27648};
    const u32x8 si = {(unsigned)nwin, base0, (unsigned)wx0, k * FRAMEB, stg, ldl, 0u, 0u};
    asm volatile(
        "s_getreg_b32 s15, hwreg(HW_REG_XCC_ID, 0, 1)\n"
        "s_cmp_lt_u32 s48, 21\n"
        "s_cselect_b32 s24, 2, 0\n"
        "s_or_b32 s15, s15, s24\n"
        "s_bitcmp1_b32 s52, 10\n"
        "s_cbranch_scc1 FV_%=\n"
        "s_mov_b32 s24, 0x3e16c740\n"
        "v_mov_b32 v50, 0x3f803f80\n"
        "v_mov_b32 v51, 0x3f803f80\n"
        "v_mov_b32 v52, 0x3f803f80\n"
        "v_mov_b32 v53, 0x3f803f80\n"
        "s_waitcnt vmcnt(15)\n"
        "ds_read_b64_tr_b16 a[72:73], v34 offset:0\n"
        "ds_read_b64_tr_b16 a[74:75], v35 offset:0\n"
        "ds_read_b64_tr_b16 a[76:77], v34 offset:6144\n"
        "ds_read_b64_tr_b16 a[78:79], v35 offset:6144\n"
        "ds_read_b64_tr_b16 a[80:81], v34 offset:12288\n"
        "ds_read_b64_tr_b16 a[82:83], v35 offset:12288\n"
        "s_add_u32 m0, s53, 15360\n"
        "s_nop 0\n"
        "buffer_load_dwordx4 v46, s[16:19], s23 offen lds\n"
        "ds_read_b64_tr_b16 a[84:85], v34 offset:8\n"
        "ds_read_b64_tr_b16 a[86:87], v35 offset:8\n"
        "ds_read_b64_tr_b16 a[88:89], v34 offset:6152\n"
        "ds_read_b64_tr_b16 a[90:91], v35 offset:6152\n"
        "ds_read_b64_tr_b16 a[92:93], v34 offset:12296\n"
        "ds_read_b64_tr_b16 a[94:95], v35 offset:12296\n"
        "buffer_load_dwordx4 v47, s[16:19], s23 offen offset:1024 lds\n"
        "ds_read_b64_tr_b16 a[96:97], v34 offset:64\n"
        "ds_read_b64_tr_b16 a[98:99], v35 offset:64\n"
        "ds_read_b64_tr_b16 a[100:101], v34 offset:6208\n"
        "ds_read_b64_tr_b16 a[102:103], v35 offset:6208\n"
        "ds_read_b64_tr_b16 a[104:105], v34 offset:12352\n"
        "ds_read_b64_tr_b16 a[106:107], v35 offset:12352\n"
        "buffer_load_dwordx4 v48, s[16:19], s23 offen offset:2048 lds\n"
        "ds_read_b64_tr_b16 a[108:109], v34 offset:72\n"
        "ds_read_b64_tr_b16 a[110:111], v35 offset:72\n"
        "ds_read_b64_tr_b16 a[112:113], v34 offset:6216\n"
        "ds_read_b64_tr_b16 a[114:115], v35 offset:6216\n"
        "ds_read_b64_tr_b16 a[116:117], v34 offset:12360\n"
        "ds_read_b64_tr_b16 a[118:119], v35 offset:12360\n"
        "ds_read_b64_tr_b16 a[120:121], v34 offset:128\n"
        "ds_read_b64_tr_b16 a[122:123], v35 offset:128\n"
        "ds_read_b64_tr_b16 a[124:125], v34 offset:6272\n"
        "ds_read_b64_tr_b16 a[126:127], v35 offset:6272\n"
        "ds_read_b64_tr_b16 a[128:129], v34 offset:12416\n"
        "ds_read_b64_tr_b16 a[130:131], v35 offset:12416\n"
        "ds_read_b64_tr_b16 a[132:133], v34 offset:136\n"
        "ds_read_b64_tr_b16 a[134:135], v35 offset:136\n"
        "ds_read_b64_tr_b16 a[136:137], v34 offset:6280\n"
        "ds_read_b64_tr_b16 a[138:139], v35 offset:6280\n"
        "ds_read_b64_tr_b16 a[140:141], v34 offset:12424\n"
        "ds_read_b64_tr_b16 a[142:143], v35 offset:12424\n"
        "s_waitcnt lgkmcnt(15)\n"
        "v_mfma_f32_16x16x32_bf16 v[46:49], a[72:75], v[50:53], 0\n"
        "s_waitcnt vmcnt(12)\n"
        "s_waitcnt vmcnt(6)\n"
        "v_mfma_f32_16x16x32_bf16 v[46:49], a[76:79], v[50:53], v[46:49]\n"
        "v_mfma_f32_16x16x32_bf16 v[46:49], a[80:83], v[50:53], v[46:49]\n"
        "s_waitcnt lgkmcnt(15)\n"
        "s_nop 0\n"
        "v_mfma_f32_16x16x32_bf16 v[54:57], a[92:95], v[50:53], 0\n"
        "s_nop 2\n"
        "v_mfma_f32_16x16x32_bf16 v[54:57], a[88:91], v[50:53], v[54:57]\n"
        "s_waitcnt lgkmcnt(15)\n"
        "v_mul_f32 v46, 0xbc2aaaab, v46\n"
        "v_mul_f32 v47, 0xbc2aaaab, v47\n"
        "v_mul_f32 v48, 0xbc2aaaab, v48\n"
        "v_mfma_f32_16x16x32_bf16 v[54:57], a[84:87], v[50:53], v[54:57]\n"
        "s_waitcnt vmcnt(0)\n"
        "v_mul_f32 v49, 0xbc2aaaab, v49\n"
        "v_cvt_pk_bf16_f32 v70, v46, v47\n"
        "v_cvt_pk_bf16_f32 v71, v48, v49\n"
        "v_mfma_f32_16x16x32_bf16 v[46:49], a[96:99], v[50:53], 0\n"
        "s_waitcnt lgkmcnt(12)\n"
        "ds_read_b128 a[0:3], v32\n"
        "v_mfma_f32_16x16x32_bf16 v[46:49], a[100:103], v[50:53], v[46:49]\n"
        "s_waitcnt lgkmcnt(7)\n"
        "ds_read_b128 a[4:7], v32 offset:64\n"
        "v_mul_f32 v54, 0xbc2aaaab, v54\n"
        "v_mul_f32 v55, 0xbc2aaaab, v55\n"
        "v_mfma_f32_16x16x32_bf16 v[46:49], a[104:107], v[50:53], v[46:49]\n"
        "s_waitcnt lgkmcnt(2)\n"
        "ds_read_b128 a[8:11], v32 offset:128\n"
        "v_mul_f32 v56, 0xbc2aaaab, v56\n"
        "v_mul_f32 v57, 0xbc2aaaab, v57\n"
        "v_mfma_f32_16x16x32_bf16 v[58:61], a[116:119], v[50:53], 0\n"
        "ds_read_b128 a[144:147], v30\n"
        "v_cvt_pk_bf16_f32 v72, v54, v55\n"
        "v_cvt_pk_bf16_f32 v73, v56, v57\n"
        "v_mfma_f32_16x16x32_bf16 v[58:61], a[112:115], v[50:53], v[58:61]\n"
        "ds_read_b128 a[148:151], v30 offset:64\n"
        "v_mul_f32 v46, 0xbc2aaaab, v46\n"
        "v_mul_f32 v47, 0xbc2aaaab, v47\n"
        "v_mfma_f32_16x16x32_bf16 v[58:61], a[108:111], v[50:53], v[58:61]\n"
        "ds_read_b128 a[152:155], v30 offset:128\n"
        "v_mul_f32 v48, 0xbc2aaaab, v48\n"
        "v_mul_f32 v49, 0xbc2aaaab, v49\n"
        "v_mfma_f32_16x16x32_bf16 v[54:57], a[120:123], v[50:53], 0\n"
        "ds_read_b128 a[156:159], v31\n"
        "v_cvt_pk_bf16_f32 v74, v46, v47\n"
        "v_cvt_pk_bf16_f32 v75, v48, v49\n"
        "v_mfma_f32_16x16x32_bf16 v[54:57], a[124:127], v[50:53], v[54:57]\n"
        "ds_read_b128 a[160:163], v31 offset:64\n"
        "v_mul_f32 v58, 0xbc2aaaab, v58\n"
        "v_mul_f32 v59, 0xbc2aaaab, v59\n"
        "v_mfma_f32_16x16x32_bf16 v[54:57], a[128:131], v[50:53], v[54:57]\n"
        "ds_read_b128 a[164:167], v31 offset:128\n"
        "v_mul_f32 v60, 0xbc2aaaab, v60\n"
        "v_mul_f32 v61, 0xbc2aaaab, v61\n"
        "v_mfma_f32_16x16x32_bf16 v[62:65], a[140:143], v[50:53], 0\n"
        "ds_read_b128 a[168:171], v30 offset:6144\n"
        "v_cvt_pk_bf16_f32 v76, v58, v59\n"
        "v_cvt_pk_bf16_f32 v77, v60, v61\n"
        "v_mfma_f32_16x16x32_bf16 v[62:65], a[136:139], v[50:53], v[62:65]\n"
        "ds_read_b128 a[172:175], v30 offset:6208\n"
        "v_mul_f32 v54, 0xbc2aaaab, v54\n"
        "v_mul_f32 v55, 0xbc2aaaab, v55\n"
        "v_mfma_f32_16x16x32_bf16 v[62:65], a[132:135], v[50:53], v[62:65]\n"
        "s_waitcnt lgkmcnt(8)\n"
        "ds_read_b128 a[176:179], v30 offset:6272\n"
        "v_mul_f32 v56, 0xbc2aaaab, v56\n"
        "v_mul_f32 v57, 0xbc2aaaab, v57\n"
        "s_waitcnt lgkmcnt(6)\n"
        "v_mfma_f32_16x16x32_bf16 v[46:49], a[144:147], v[70:73], 0\n"
        "ds_read_b128 a[180:183], v31 offset:6144\n"
        "v_cvt_pk_bf16_f32 v78, v54, v55\n"
        "v_cvt_pk_bf16_f32 v79, v56, v57\n"
        "v_mfma_f32_16x16x32_bf16 v[46:49], a[148:151], v[74:77], v[46:49]\n"
        "ds_read_b128 a[184:187], v31 offset:6208\n"
        "v_mul_f32 v62, 0xbc2aaaab, v62\n"
        "v_mul_f32 v63, 0xbc2aaaab, v63\n"
        "v_cvt_pk_bf16_f32 v80, v62, v63\n"
        "v_mul_f32 v64, 0xbc2aaaab, v64\n"
        "v_mul_f32 v65, 0xbc2aaaab, v65\n"
        "v_cvt_pk_bf16_f32 v81, v64, v65\n"
        "s_nop 1\n"
        "v_mfma_f32_16x16x32_bf16 v[46:49], a[152:155], v[78:81], v[46:49]\n"
        "s_waitcnt lgkmcnt(5)\n"
        "ds_read_b128 a[188:191], v31 offset:6272\n"
        "v_mfma_f32_16x16x32_bf16 v[54:57], a[152:155], a[8:11], v[46:49]\n"
        "buffer_load_dwordx4 v[188:191], v36, s[36:39], 0 offen\n"
        "v_mfma_f32_16x16x32_bf16 v[54:57], a[148:151], a[4:7], v[54:57]\n"
        "ds_read_b128 a[192:195], v30 offset:12288\n"
        "ds_read_b128 a[196:199], v30 offset:12352\n"
        "v_mfma_f32_16x16x32_bf16 v[54:57], a[144:147], a[0:3], v[54:57]\n"
        "s_waitcnt lgkmcnt(5)\n"
        "ds_read_b128 a[200:203], v30 offset:12416\n"
        "v_mfma_f32_16x16x32_bf16 v[50:53], a[156:159], v[70:73], 0\n"
        "s_nop 0\n"
        "ds_read_b128 a[204:207], v31 offset:12288\n"
        "v_mfma_f32_16x16x32_bf16 v[50:53], a[160:163], v[74:77], v[50:53]\n"
        "s_nop 0\n"
        "ds_read_b128 a[208:211], v31 offset:12352\n"
        "v_add_f32 v255, v54, v55\n"
        "v_add_f32 v255, v255, v56\n"
        "v_mfma_f32_16x16x32_bf16 v[50:53], a[164:167], v[78:81], v[50:53]\n"
        "ds_read_b128 a[212:215], v31 offset:12416\n"
        "v_add_f32 v255, v255, v57\n"
        "v_mfma_f32_16x16x32_bf16 v[82:85], a[164:167], a[8:11], v[50:53]\n"
        "ds_read_b128 a[12:15], v33\n"
        "v_mfma_f32_16x16x32_bf16 v[82:85], a[160:163], a[4:7], v[82:85]\n"
        "ds_read_b128 a[16:19], v33 offset:64\n"
        "v_mfma_f32_16x16x32_bf16 v[82:85], a[156:159], a[0:3], v[82:85]\n"
        "ds_read_b128 a[20:23], v33 offset:128\n"
        "v_mfma_f32_16x16x32_bf16 v[58:61], a[168:171], v[70:73], 0\n"
        "s_waitcnt lgkmcnt(9)\n"
        "buffer_load_dwordx4 v[194:197], v36, s[36:39], 0 offen offset:16\n"
        "v_mfma_f32_16x16x32_bf16 v[58:61], a[172:175], v[74:77], v[58:61]\n"
        "s_mov_b32 s78, 0xf0000f\n"
        "ds_read_b128 a[24:27], v32 offset:6144\n"
        "ds_read_b128 a[28:31], v32 offset:6208\n"
        "v_add_f32 v255, v255, v82\n"
        "v_mfma_f32_16x16x32_bf16 v[58:61], a[176:179], v[78:81], v[58:61]\n"
        "s_mov_b32 s79, 0xf0000f00\n"
        "ds_read_b128 a[32:35], v32 offset:6272\n"
        "v_add_f32 v255, v255, v83\n"
        "v_add_f32 v255, v255, v84\n"
        "v_mfma_f32_16x16x32_bf16 v[86:89], a[176:179], a[8:11], v[58:61]\n"
        "s_waitcnt lgkmcnt(9)\n"
        "ds_read_b128 a[36:39], v33 offset:6144\n"
        "v_add_f32 v255, v255, v85\n"
        "v_mfma_f32_16x16x32_bf16 v[86:89], a[172:175], a[4:7], v[86:89]\n"
        "s_mov_b32 s76, 0x11111111\n"
        "ds_read_b128 a[40:43], v33 offset:6208\n"
        "v_mfma_f32_16x16x32_bf16 v[86:89], a[168:171], a[0:3], v[86:89]\n"
        "s_mov_b32 s77, 0x11111111\n"
        "ds_read_b128 a[44:47], v33 offset:6272\n"
        "v_mfma_f32_16x16x32_bf16 v[66:69], a[180:183], v[70:73], 0\n"
        "s_waitcnt lgkmcnt(9)\n"
        "ds_read_b128 a[48:51], v32 offset:12288\n"
        "v_mfma_f32_16x16x32_bf16 v[66:69], a[184:187], v[74:77], v[66:69]\n"
        "s_mov_b32 s56, 0x22222222\n"
        "ds_read_b128 a[52:55], v32 offset:12352\n"
        "v_add_f32 v255, v255, v86\n"
        "v_add_f32 v255, v255, v87\n"
        "v_mfma_f32_16x16x32_bf16 v[66:69], a[188:191], v[78:81], v[66:69]\n"
        "s_mov_b32 s57, 0x22222222\n"
        "ds_read_b128 a[56:59], v32 offset:12416\n"
        "v_add_f32 v255, v255, v88\n"
        "v_add_f32 v255, v255, v89\n"
        "v_mfma_f32_16x16x32_bf16 v[90:93], a[188:191], a[8:11], v[66:69]\n"
        "s_mov_b32 s58, 0x44444444\n"
        "buffer_load_dwordx4 v[200:203], v36, s[36:39], 0 offen offset:32\n"
        "v_mfma_f32_16x16x32_bf16 v[90:93], a[184:187], a[4:7], v[90:93]\n"
        "s_waitcnt lgkmcnt(9)\n"
        "ds_read_b128 a[60:63], v33 offset:12288\n"
        "ds_read_b128 a[64:67], v33 offset:12352\n"
        "v_mfma_f32_16x16x32_bf16 v[90:93], a[180:183], a[0:3], v[90:93]\n"
        "s_mov_b32 s59, 0x44444444\n"
        "ds_read_b128 a[68:71], v33 offset:12416\n"
        "v_mfma_f32_16x16x32_bf16 v[62:65], a[192:195], v[70:73], 0\n"
        "s_mov_b32 s60, 0x88888888\n"
        "s_nop 1\n"
        "v_mfma_f32_16x16x32_bf16 v[62:65], a[196:199], v[74:77], v[62:65]\n"
        "s_mov_b32 s61, 0x88888888\n"
        "v_add_f32 v255, v255, v90\n"
        "v_add_f32 v255, v255, v91\n"
        "v_add_f32 v255, v255, v92\n"
        "v_mfma_f32_16x16x32_bf16 v[62:65], a[200:203], v[78:81], v[62:65]\n"
        "s_mov_b32 s62, 0xffff\n"
        "v_add_f32 v255, v255, v93\n"
        "v_mfma_f32_16x16x32_bf16 v[94:97], a[200:203], a[8:11], v[62:65]\n"
        "s_mov_b32 s63, 0x0\n"
        "v_mfma_f32_16x16x32_bf16 v[94:97], a[196:199], a[4:7], v[94:97]\n"
        "s_waitcnt lgkmcnt(9)\n"
        "v_mfma_f32_16x16x32_bf16 v[94:97], a[192:195], a[0:3], v[94:97]\n"
        "s_mov_b32 s64, 0xffff0000\n"
        "buffer_load_dwordx4 v[208:211], v37, s[36:39], 0 offen\n"
        "v_mfma_f32_16x16x32_bf16 v[30:33], a[204:207], v[70:73], 0\n"
        "s_mov_b32 s65, 0x0\n"
        "s_nop 1\n"
        "v_mfma_f32_16x16x32_bf16 v[30:33], a[208:211], v[74:77], v[30:33]\n"
        "s_mov_b32 s66, 0x0\n"
        "v_add_f32 v255, v255, v94\n"
        "v_add_f32 v255, v255, v95\n"
        "v_add_f32 v255, v255, v96\n"
        "v_mfma_f32_16x16x32_bf16 v[30:33], a[212:215], v[78:81], v[30:33]\n"
        "s_mov_b32 s67, 0xffff\n"
        "v_add_f32 v255, v255, v97\n"
        "v_mfma_f32_16x16x32_bf16 v[70:73], a[212:215], a[8:11], v[30:33]\n"
        "s_waitcnt lgkmcnt(0)\n"
        "v_mfma_f32_16x16x32_bf16 v[70:73], a[208:211], a[4:7], v[70:73]\n"
        "s_mov_b32 s68, 0x0\n"
        "v_mfma_f32_16x16x32_bf16 v[70:73], a[204:207], a[0:3], v[70:73]\n"
        "s_mov_b32 s69, 0xffff0000\n"
        "s_nop 0\n"
        "v_mfma_f32_16x16x32_bf16 v[98:101], a[144:147], a[12:15], v[46:49]\n"
        "s_mov_b32 s70, 0x0\n"
        "s_nop 1\n"
        "v_mfma_f32_16x16x32_bf16 v[98:101], a[148:151], a[16:19], v[98:101]\n"
        "s_mov_b32 s71, 0xffffffff\n"
        "v_add_f32 v255, v255, v70\n"
        "v_add_f32 v255, v255, v71\n"
        "v_add_f32 v255, v255, v72\n"
        "v_mfma_f32_16x16x32_bf16 v[98:101], a[152:155], a[20:23], v[98:101]\n"
        "s_barrier\n"
        "buffer_load_dwordx4 v[214:217], v37, s[36:39], 0 offen offset:16\n"
        "v_add_f32 v255, v255, v73\n"
        "v_mov_b32 v253, v255\n"
        "v_mfma_f32_16x16x32_bf16 v[106:109], a[164:167], a[20:23], v[50:53]\n"
        "s_mov_b32 s72, 0x0\n"
        "v_permlane32_swap_b32 v255, v253\n"
        "v_add_f32 v255, v255, v253\n"
        "v_mfma_f32_16x16x32_bf16 v[106:109], a[160:163], a[16:19], v[106:109]\n"
        "s_mov_b32 s73, 0x20000\n"
        "v_mov_b32 v253, v255\n"
        "v_add_f32 v254, v98, v99\n"
        "v_add_f32 v254, v254, v100\n"
        "v_mfma_f32_16x16x32_bf16 v[106:109], a[156:159], a[12:15], v[106:109]\n"
        "s_mov_b64 vcc, s[78:79]\n"
        "v_permlane16_swap_b32 v255, v253\n"
        "v_add_f32 v255, v255, v253\n"
        "v_mfma_f32_16x16x32_bf16 v[110:113], a[168:171], a[12:15], v[58:61]\n"
        "s_mov_b32 s80, 0x3c2aaaab\n"
        "v_mul_f32 v255, 0xbac909aa, v255\n"
        "v_add_f32 v254, v254, v101\n"
        "v_fma_f32 v54, v54, s24, v255\n"
        "v_mfma_f32_16x16x32_bf16 v[110:113], a[172:175], a[16:19], v[110:113]\n"
        "s_mov_b32 s81, 0x3727c5ac\n"
        "v_fma_f32 v55, v55, s24, v255\n"
        "v_fma_f32 v56, v56, s24, v255\n"
        "v_fma_f32 v57, v57, s24, v255\n"
        "v_mfma_f32_16x16x32_bf16 v[110:113], a[176:179], a[20:23], v[110:113]\n"
        "s_mov_b32 s82, 0x43800000\n"
        "v_fma_f32 v82, v82, s24, v255\n"
        "v_fma_f32 v83, v83, s24, v255\n"
        "v_fma_f32 v84, v84, s24, v255\n"
        "v_mfma_f32_16x16x32_bf16 v[102:105], a[188:191], a[20:23], v[66:69]\n"
        "s_mov_b32 s84, 0x80000000\n"
        "v_fma_f32 v85, v85, s24, v255\n"
        "v_fma_f32 v86, v86, s24, v255\n"
        "v_fma_f32 v87, v87, s24, v255\n"
        "v_mfma_f32_16x16x32_bf16 v[102:105], a[184:187], a[16:19], v[102:105]\n"
        "s_sub_u32 s85, s48, 1\n"
        "v_fma_f32 v88, v88, s24, v255\n"
        "v_fma_f32 v89, v89, s24, v255\n"
        "v_fma_f32 v90, v90, s24, v255\n"
        "v_mfma_f32_16x16x32_bf16 v[102:105], a[180:183], a[12:15], v[102:105]\n"
        "s_mov_b32 s86, 0x80000000\n"
        "buffer_load_dwordx4 v[220:223], v37, s[36:39], 0 offen offset:32\n"
        "v_fma_f32 v91, v91, s24, v255\n"
        "v_fma_f32 v92, v92, s24, v255\n"
        "v_mfma_f32_16x16x32_bf16 v[118:121], a[192:195], a[12:15], v[62:65]\n"
        "s_mov_b32 s89, 0x80000000\n"
        "v_fma_f32 v93, v93, s24, v255\n"
        "v_fma_f32 v94, v94, s24, v255\n"
        "v_fma_f32 v95, v95, s24, v255\n"
        "v_mfma_f32_16x16x32_bf16 v[118:121], a[196:199], a[16:19], v[118:121]\n"
        "s_mov_b32 s29, 1\n"
        "v_fma_f32 v96, v96, s24, v255\n"
        "v_fma_f32 v97, v97, s24, v255\n"
        "v_fma_f32 v70, v70, s24, v255\n"
        "v_mfma_f32_16x16x32_bf16 v[118:121], a[200:203], a[20:23], v[118:121]\n"
        "s_mov_b32 s31, 0x400000\n"
        "v_fma_f32 v71, v71, s24, v255\n"
        "v_fma_f32 v72, v72, s24, v255\n"
        "v_fma_f32 v73, v73, s24, v255\n"
        "v_mfma_f32_16x16x32_bf16 v[122:125], a[212:215], a[20:23], v[30:33]\n"
        "s_mov_b32 s87, s49\n"
        "v_cvt_pk_bf16_f32 v34, v54, v55\n"
        "v_cvt_pk_bf16_f32 v35, v56, v57\n"
        "v_cvt_pk_bf16_f32 v36, v82, v83\n"
        "v_mfma_f32_16x16x32_bf16 v[122:125], a[208:211], a[16:19], v[122:125]\n"
        "s_mov_b32 s88, s50\n"
        "v_cvt_pk_bf16_f32 v37, v84, v85\n"
        "v_cvt_pk_bf16_f32 v54, v86, v87\n"
        "v_cvt_pk_bf16_f32 v55, v88, v89\n"
        "v_mfma_f32_16x16x32_bf16 v[122:125], a[204:207], a[12:15], v[122:125]\n"
        "s_mov_b32 s54, 0x05040302\n"
        "ds_write_b128 v39, v[34:37]\n"
        "v_cvt_pk_bf16_f32 v56, v90, v91\n"
        "v_mfma_f32_16x16x32_bf16 v[34:37], a[144:147], a[24:27], v[46:49]\n"
        "s_mov_b32 s55, 0x07060302\n"
        "v_cvt_pk_bf16_f32 v57, v92, v93\n"
        "v_cvt_pk_bf16_f32 v74, v94, v95\n"
        "v_cvt_pk_bf16_f32 v75, v96, v97\n"
        "v_mfma_f32_16x16x32_bf16 v[34:37], a[148:151], a[28:31], v[34:37]\n"
        "s_add_u32 m0, s52, 0\n"
        "ds_write_b128 v39, v[54:57] offset:1024\n"
        "v_cvt_pk_bf16_f32 v76, v70, v71\n"
        "v_mfma_f32_16x16x32_bf16 v[34:37], a[152:155], a[32:35], v[34:37]\n"
        "s_mov_b64 s[44:45], s[40:41]\n"
        "buffer_load_dwordx4 v0, s[36:39], s49 offen lds\n"
        "v_cvt_pk_bf16_f32 v77, v72, v73\n"
        "v_add_f32 v254, v254, v106\n"
        "v_mfma_f32_16x16x32_bf16 v[54:57], a[164:167], a[32:35], v[50:53]\n"
        "s_mov_b32 s46, 0\n"
        "ds_write_b128 v39, v[74:77] offset:2048\n"
        "v_add_f32 v254, v254, v107\n"
        "v_mfma_f32_16x16x32_bf16 v[54:57], a[160:163], a[28:31], v[54:57]\n"
        "s_mov_b32 s47, s43\n"
        "v_add_f32 v254, v254, v108\n"
        "v_add_f32 v254, v254, v109\n"
        "v_add_f32 v254, v254, v110\n"
        "v_mfma_f32_16x16x32_bf16 v[54:57], a[156:159], a[24:27], v[54:57]\n"
        "s_waitcnt lgkmcnt(3)\n"
        "v_add_f32 v254, v254, v111\n"
        "v_add_f32 v254, v254, v112\n"
        "v_add_f32 v254, v254, v113\n"
        "v_mfma_f32_16x16x32_bf16 v[70:73], a[168:171], a[24:27], v[58:61]\n"
        "s_waitcnt lgkmcnt(3)\n"
        "v_add_f32 v254, v254, v102\n"
        "v_add_f32 v254, v254, v103\n"
        "v_add_f32 v254, v254, v104\n"
        "v_mfma_f32_16x16x32_bf16 v[70:73], a[172:175], a[28:31], v[70:73]\n"
        "s_waitcnt lgkmcnt(3)\n"
        "v_add_f32 v254, v254, v105\n"
        "v_add_f32 v254, v254, v118\n"
        "v_add_f32 v254, v254, v119\n"
        "v_mfma_f32_16x16x32_bf16 v[70:73], a[176:179], a[32:35], v[70:73]\n"
        "buffer_load_dwordx4 v1, s[36:39], s49 offen offset:1024 lds\n"
        "v_add_f32 v254, v254, v120\n"
        "v_add_f32 v254, v254, v121\n"
        "v_mfma_f32_16x16x32_bf16 v[114:117], a[188:191], a[32:35], v[66:69]\n"
        "v_add_f32 v254, v254, v122\n"
        "v_add_f32 v254, v254, v123\n"
        "v_add_f32 v254, v254, v124\n"
        "v_mfma_f32_16x16x32_bf16 v[114:117], a[184:187], a[28:31], v[114:117]\n"
        "v_add_f32 v254, v254, v125\n"
        "v_mov_b32 v253, v254\n"
        "v_add_f32 v255, v34, v35\n"
        "v_mfma_f32_16x16x32_bf16 v[114:117], a[180:183], a[24:27], v[114:117]\n"
        "v_permlane32_swap_b32 v254, v253\n"
        "v_add_f32 v254, v254, v253\n"
        "v_mfma_f32_16x16x32_bf16 v[126:129], a[192:195], a[24:27], v[62:65]\n"
        "v_mov_b32 v253, v254\n"
        "v_add_f32 v255, v255, v36\n"
        "v_add_f32 v255, v255, v37\n"
        "v_mfma_f32_16x16x32_bf16 v[126:129], a[196:199], a[28:31], v[126:129]\n"
        "v_permlane16_swap_b32 v254, v253\n"
        "v_add_f32 v254, v254, v253\n"
        "v_mfma_f32_16x16x32_bf16 v[126:129], a[200:203], a[32:35], v[126:129]\n"
        "buffer_load_dwordx4 v2, s[36:39], s49 offen offset:2048 lds\n"
        "v_mul_f32 v254, 0xbac909aa, v254\n"
        "v_fma_f32 v98, v98, s24, v254\n"
        "v_mfma_f32_16x16x32_bf16 v[130:133], a[212:215], a[32:35], v[30:33]\n"
        "v_fma_f32 v99, v99, s24, v254\n"
        "v_fma_f32 v100, v100, s24, v254\n"
        "v_fma_f32 v101, v101, s24, v254\n"
        "v_mfma_f32_16x16x32_bf16 v[130:133], a[208:211], a[28:31], v[130:133]\n"
        "v_fma_f32 v106, v106, s24, v254\n"
        "v_fma_f32 v107, v107, s24, v254\n"
        "v_fma_f32 v108, v108, s24, v254\n"
        "v_mfma_f32_16x16x32_bf16 v[130:133], a[204:207], a[24:27], v[130:133]\n"
        "v_fma_f32 v109, v109, s24, v254\n"
        "v_fma_f32 v110, v110, s24, v254\n"
        "v_fma_f32 v111, v111, s24, v254\n"
        "v_mfma_f32_16x16x32_bf16 v[74:77], a[144:147], a[36:39], v[46:49]\n"
        "v_fma_f32 v112, v112, s24, v254\n"
        "v_fma_f32 v113, v113, s24, v254\n"
        "v_fma_f32 v102, v102, s24, v254\n"
        "v_mfma_f32_16x16x32_bf16 v[74:77], a[148:151], a[40:43], v[74:77]\n"
        "v_fma_f32 v103, v103, s24, v254\n"
        "v_fma_f32 v104, v104, s24, v254\n"
        "v_fma_f32 v105, v105, s24, v254\n"
        "v_mfma_f32_16x16x32_bf16 v[74:77], a[152:155], a[44:47], v[74:77]\n"
        "buffer_load_dwordx4 v3, s[36:39], s49 offen offset:3072 lds\n"
        "v_fma_f32 v118, v118, s24, v254\n"
        "v_fma_f32 v119, v119, s24, v254\n"
        "v_mfma_f32_16x16x32_bf16 v[78:81], a[164:167], a[44:47], v[50:53]\n"
        "v_fma_f32 v120, v120, s24, v254\n"
        "v_fma_f32 v121, v121, s24, v254\n"
        "v_fma_f32 v122, v122, s24, v254\n"
        "v_mfma_f32_16x16x32_bf16 v[78:81], a[160:163], a[40:43], v[78:81]\n"
        "v_fma_f32 v123, v123, s24, v254\n"
        "v_fma_f32 v124, v124, s24, v254\n"
        "v_fma_f32 v125, v125, s24, v254\n"
        "v_mfma_f32_16x16x32_bf16 v[78:81], a[156:159], a[36:39], v[78:81]\n"
        "v_cvt_pk_bf16_f32 v84, v98, v99\n"
        "v_cvt_pk_bf16_f32 v85, v100, v101\n"
        "v_cvt_pk_bf16_f32 v86, v106, v107\n"
        "v_mfma_f32_16x16x32_bf16 v[90:93], a[168:171], a[36:39], v[58:61]\n"
        "v_cvt_pk_bf16_f32 v87, v108, v109\n"
        "v_cvt_pk_bf16_f32 v82, v110, v111\n"
        "v_cvt_pk_bf16_f32 v83, v112, v113\n"
        "v_mfma_f32_16x16x32_bf16 v[90:93], a[172:175], a[40:43], v[90:93]\n"
        "s_add_u32 m0, s52, 4096\n"
        "ds_write_b128 v40, v[84:87]\n"
        "v_cvt_pk_bf16_f32 v84, v102, v103\n"
        "v_mfma_f32_16x16x32_bf16 v[90:93], a[176:179], a[44:47], v[90:93]\n"
        "buffer_load_dwordx4 v4, s[36:39], s49 offen lds\n"
        "v_cvt_pk_bf16_f32 v85, v104, v105\n"
        "v_cvt_pk_bf16_f32 v94, v118, v119\n"
        "v_mfma_f32_16x16x32_bf16 v[86:89], a[188:191], a[44:47], v[66:69]\n"
        "ds_write_b128 v40, v[82:85] offset:1024\n"
        "v_cvt_pk_bf16_f32 v95, v120, v121\n"
        "v_mfma_f32_16x16x32_bf16 v[86:89], a[184:187], a[40:43], v[86:89]\n"
        "v_cvt_pk_bf16_f32 v96, v122, v123\n"
        "v_cvt_pk_bf16_f32 v97, v124, v125\n"
        "v_add_f32 v255, v255, v54\n"
        "v_mfma_f32_16x16x32_bf16 v[86:89], a[180:183], a[36:39], v[86:89]\n"
        "ds_write_b128 v40, v[94:97] offset:2048\n"
        "v_add_f32 v255, v255, v55\n"
        "v_mfma_f32_16x16x32_bf16 v[94:97], a[192:195], a[36:39], v[62:65]\n"
        "v_add_f32 v255, v255, v56\n"
        "v_add_f32 v255, v255, v57\n"
        "v_add_f32 v255, v255, v70\n"
        "v_mfma_f32_16x16x32_bf16 v[94:97], a[196:199], a[40:43], v[94:97]\n"
        "v_add_f32 v255, v255, v71\n"
        "v_add_f32 v255, v255, v72\n"
        "v_add_f32 v255, v255, v73\n"
        "v_mfma_f32_16x16x32_bf16 v[94:97], a[200:203], a[44:47], v[94:97]\n"
        "buffer_load_dwordx4 v5, s[36:39], s49 offen offset:1024 lds\n"
        "v_add_f32 v255, v255, v114\n"
        "v_add_f32 v255, v255, v115\n"
        "v_mfma_f32_16x16x32_bf16 v[82:85], a[212:215], a[44:47], v[30:33]\n"
        "v_add_f32 v255, v255, v116\n"
        "v_add_f32 v255, v255, v117\n"
        "v_add_f32 v255, v255, v126\n"
        "v_mfma_f32_16x16x32_bf16 v[82:85], a[208:211], a[40:43], v[82:85]\n"
        "v_add_f32 v255, v255, v127\n"
        "v_add_f32 v255, v255, v128\n"
        "v_add_f32 v255, v255, v129\n"
        "v_mfma_f32_16x16x32_bf16 v[82:85], a[204:207], a[36:39], v[82:85]\n"
        "v_add_f32 v255, v255, v130\n"
        "v_add_f32 v255, v255, v131\n"
        "v_add_f32 v255, v255, v132\n"
        "v_mfma_f32_16x16x32_bf16 v[102:105], a[144:147], a[48:51], v[46:49]\n"
        "v_add_f32 v255, v255, v133\n"
        "v_mov_b32 v254, v255\n"
        "v_add_f32 v134, v74, v75\n"
        "v_mfma_f32_16x16x32_bf16 v[102:105], a[148:151], a[52:55], v[102:105]\n"
        "v_permlane32_swap_b32 v255, v254\n"
        "v_add_f32 v255, v255, v254\n"
        "v_mfma_f32_16x16x32_bf16 v[102:105], a[152:155], a[56:59], v[102:105]\n"
        "buffer_load_dwordx4 v6, s[36:39], s49 offen offset:2048 lds\n"
        "v_mov_b32 v254, v255\n"
        "v_add_f32 v134, v134, v76\n"
        "v_mfma_f32_16x16x32_bf16 v[106:109], a[164:167], a[56:59], v[50:53]\n"
        "v_permlane16_swap_b32 v255, v254\n"
        "v_add_f32 v255, v255, v254\n"
        "v_mfma_f32_16x16x32_bf16 v[106:109], a[160:163], a[52:55], v[106:109]\n"
        "v_mul_f32 v255, 0xbac909aa, v255\n"
        "v_add_f32 v134, v134, v77\n"
        "v_fma_f32 v34, v34, s24, v255\n"
        "v_mfma_f32_16x16x32_bf16 v[106:109], a[156:159], a[48:51], v[106:109]\n"
        "v_fma_f32 v35, v35, s24, v255\n"
        "v_fma_f32 v36, v36, s24, v255\n"
        "v_fma_f32 v37, v37, s24, v255\n"
        "v_mfma_f32_16x16x32_bf16 v[98:101], a[168:171], a[48:51], v[58:61]\n"
        "v_fma_f32 v54, v54, s24, v255\n"
        "v_fma_f32 v55, v55, s24, v255\n"
        "v_fma_f32 v56, v56, s24, v255\n"
        "v_mfma_f32_16x16x32_bf16 v[98:101], a[172:175], a[52:55], v[98:101]\n"
        "v_fma_f32 v57, v57, s24, v255\n"
        "v_fma_f32 v70, v70, s24, v255\n"
        "v_fma_f32 v71, v71, s24, v255\n"
        "v_mfma_f32_16x16x32_bf16 v[98:101], a[176:179], a[56:59], v[98:101]\n"
        "buffer_load_dwordx4 v7, s[36:39], s49 offen offset:3072 lds\n"
        "v_fma_f32 v72, v72, s24, v255\n"
        "v_fma_f32 v73, v73, s24, v255\n"
        "v_mfma_f32_16x16x32_bf16 v[110:113], a[188:191], a[56:59], v[66:69]\n"
        "v_fma_f32 v114, v114, s24, v255\n"
        "v_fma_f32 v115, v115, s24, v255\n"
        "v_fma_f32 v116, v116, s24, v255\n"
        "v_mfma_f32_16x16x32_bf16 v[110:113], a[184:187], a[52:55], v[110:113]\n"
        "v_fma_f32 v117, v117, s24, v255\n"
        "v_fma_f32 v126, v126, s24, v255\n"
        "v_fma_f32 v127, v127, s24, v255\n"
        "v_mfma_f32_16x16x32_bf16 v[110:113], a[180:183], a[48:51], v[110:113]\n"
        "v_fma_f32 v128, v128, s24, v255\n"
        "v_fma_f32 v129, v129, s24, v255\n"
        "v_fma_f32 v130, v130, s24, v255\n"
        "v_mfma_f32_16x16x32_bf16 v[118:121], a[192:195], a[48:51], v[62:65]\n"
        "v_fma_f32 v131, v131, s24, v255\n"
        "v_fma_f32 v132, v132, s24, v255\n"
        "v_fma_f32 v133, v133, s24, v255\n"
        "v_mfma_f32_16x16x32_bf16 v[118:121], a[196:199], a[52:55], v[118:121]\n"
        "s_add_u32 m0, s52, 8192\n"
        "v_cvt_pk_bf16_f32 v122, v34, v35\n"
        "v_cvt_pk_bf16_f32 v123, v36, v37\n"
        "v_cvt_pk_bf16_f32 v124, v54, v55\n"
        "v_mfma_f32_16x16x32_bf16 v[118:121], a[200:203], a[56:59], v[118:121]\n"
        "buffer_load_dwordx4 v8, s[36:39], s49 offen lds\n"
        "v_cvt_pk_bf16_f32 v125, v56, v57\n"
        "v_cvt_pk_bf16_f32 v34, v70, v71\n"
        "v_mfma_f32_16x16x32_bf16 v[54:57], a[212:215], a[56:59], v[30:33]\n"
        "ds_write_b128 v41, v[122:125]\n"
        "v_cvt_pk_bf16_f32 v35, v72, v73\n"
        "v_mfma_f32_16x16x32_bf16 v[54:57], a[208:211], a[52:55], v[54:57]\n"
        "v_cvt_pk_bf16_f32 v36, v114, v115\n"
        "v_cvt_pk_bf16_f32 v37, v116, v117\n"
        "v_cvt_pk_bf16_f32 v70, v126, v127\n"
        "v_mfma_f32_16x16x32_bf16 v[54:57], a[204:207], a[48:51], v[54:57]\n"
        "ds_write_b128 v41, v[34:37] offset:1024\n"
        "v_cvt_pk_bf16_f32 v71, v128, v129\n"
        "v_mfma_f32_16x16x32_bf16 v[34:37], a[144:147], a[60:63], v[46:49]\n"
        "v_cvt_pk_bf16_f32 v72, v130, v131\n"
        "v_cvt_pk_bf16_f32 v73, v132, v133\n"
        "v_add_f32 v134, v134, v78\n"
        "v_mfma_f32_16x16x32_bf16 v[34:37], a[148:151], a[64:67], v[34:37]\n"
        "ds_write_b128 v41, v[70:73] offset:2048\n"
        "v_add_f32 v134, v134, v79\n"
        "v_mfma_f32_16x16x32_bf16 v[34:37], a[152:155], a[68:71], v[34:37]\n"
        "v_add_f32 v134, v134, v80\n"
        "v_add_f32 v134, v134, v81\n"
        "v_add_f32 v134, v134, v90\n"
        "v_mfma_f32_16x16x32_bf16 v[46:49], a[164:167], a[68:71], v[50:53]\n"
        "v_add_f32 v134, v134, v91\n"
        "v_add_f32 v134, v134, v92\n"
        "v_add_f32 v134, v134, v93\n"
        "v_mfma_f32_16x16x32_bf16 v[46:49], a[160:163], a[64:67], v[46:49]\n"
        "v_add_f32 v134, v134, v86\n"
        "v_add_f32 v134, v134, v87\n"
        "v_add_f32 v134, v134, v88\n"
        "v_mfma_f32_16x16x32_bf16 v[46:49], a[156:159], a[60:63], v[46:49]\n"
        "v_add_f32 v134, v134, v89\n"
        "v_add_f32 v134, v134, v94\n"
        "v_add_f32 v134, v134, v95\n"
        "v_mfma_f32_16x16x32_bf16 v[50:53], a[168:171], a[60:63], v[58:61]\n"
        "v_add_f32 v134, v134, v96\n"
        "v_add_f32 v134, v134, v97\n"
        "v_add_f32 v134, v134, v82\n"
        "v_mfma_f32_16x16x32_bf16 v[50:53], a[172:175], a[64:67], v[50:53]\n"
        "v_add_f32 v134, v134, v83\n"
        "v_add_f32 v134, v134, v84\n"
        "v_add_f32 v134, v134, v85\n"
        "v_mfma_f32_16x16x32_bf16 v[50:53], a[176:179], a[68:71], v[50:53]\n"
        "v_mov_b32 v135, v134\n"
        "v_add_f32 v70, v102, v103\n"
        "v_add_f32 v70, v70, v104\n"
        "v_mfma_f32_16x16x32_bf16 v[58:61], a[188:191], a[68:71], v[66:69]\n"
        "v_permlane32_swap_b32 v134, v135\n"
        "v_add_f32 v134, v134, v135\n"
        "v_mfma_f32_16x16x32_bf16 v[58:61], a[184:187], a[64:67], v[58:61]\n"
        "v_mov_b32 v135, v134\n"
        "v_add_f32 v70, v70, v105\n"
        "v_add_f32 v70, v70, v106\n"
        "v_mfma_f32_16x16x32_bf16 v[58:61], a[180:183], a[60:63], v[58:61]\n"
        "v_permlane16_swap_b32 v134, v135\n"
        "v_add_f32 v134, v134, v135\n"
        "v_mfma_f32_16x16x32_bf16 v[38:41], a[192:195], a[60:63], v[62:65]\n"
        "v_mul_f32 v134, 0xbac909aa, v134\n"
        "v_add_f32 v70, v70, v107\n"
        "v_add_f32 v70, v70, v108\n"
        "v_mfma_f32_16x16x32_bf16 v[38:41], a[196:199], a[64:67], v[38:41]\n"
        "v_add_f32 v70, v70, v109\n"
        "v_add_f32 v70, v70, v98\n"
        "v_add_f32 v70, v70, v99\n"
        "v_mfma_f32_16x16x32_bf16 v[38:41], a[200:203], a[68:71], v[38:41]\n"
        "v_add_f32 v70, v70, v100\n"
        "v_add_f32 v70, v70, v101\n"
        "v_add_f32 v70, v70, v110\n"
        "v_mfma_f32_16x16x32_bf16 v[62:65], a[212:215], a[68:71], v[30:33]\n"
        "v_add_f32 v70, v70, v111\n"
        "v_add_f32 v70, v70, v112\n"
        "v_add_f32 v70, v70, v113\n"
        "v_mfma_f32_16x16x32_bf16 v[62:65], a[208:211], a[64:67], v[62:65]\n"
        "v_add_f32 v70, v70, v118\n"
        "v_add_f32 v70, v70, v119\n"
        "v_add_f32 v70, v70, v120\n"
        "v_mfma_f32_16x16x32_bf16 v[62:65], a[204:207], a[60:63], v[62:65]\n"
        "v_add_f32 v70, v70, v121\n"
        "v_add_f32 v70, v70, v54\n"
        "v_add_f32 v70, v70, v55\n"
        "v_add_f32 v70, v70, v56\n"
        "v_add_f32 v70, v70, v57\n"
        "v_mov_b32 v71, v70\n"
        "v_add_f32 v114, v34, v35\n"
        "v_add_f32 v114, v114, v36\n"
        "v_permlane32_swap_b32 v70, v71\n"
        "v_add_f32 v70, v70, v71\n"
        "v_mov_b32 v71, v70\n"
        "v_add_f32 v114, v114, v37\n"
        "v_add_f32 v114, v114, v46\n"
        "v_permlane16_swap_b32 v70, v71\n"
        "v_add_f32 v70, v70, v71\n"
        "v_mul_f32 v70, 0xbac909aa, v70\n"
        "v_add_f32 v114, v114, v47\n"
        "v_add_f32 v114, v114, v48\n"
        "v_add_f32 v114, v114, v49\n"
        "v_add_f32 v114, v114, v50\n"
        "v_add_f32 v114, v114, v51\n"
        "v_add_f32 v114, v114, v52\n"
        "v_add_f32 v114, v114, v53\n"
        "v_add_f32 v114, v114, v58\n"
        "v_add_f32 v114, v114, v59\n"
        "v_add_f32 v114, v114, v60\n"
        "v_add_f32 v114, v114, v61\n"
        "v_add_f32 v114, v114, v38\n"
        "v_add_f32 v114, v114, v39\n"
        "v_add_f32 v114, v114, v40\n"
        "v_add_f32 v114, v114, v41\n"
        "v_pk_fma_f32 v[74:75], v[74:75], s[24:25], v[134:135] op_sel_hi:[1,0,0]\n"
        "v_pk_fma_f32 v[76:77], v[76:77], s[24:25], v[134:135] op_sel_hi:[1,0,0]\n"
        "v_pk_fma_f32 v[78:79], v[78:79], s[24:25], v[134:135] op_sel_hi:[1,0,0]\n"
        "v_pk_fma_f32 v[80:81], v[80:81], s[24:25], v[134:135] op_sel_hi:[1,0,0]\n"
        "v_pk_fma_f32 v[90:91], v[90:91], s[24:25], v[134:135] op_sel_hi:[1,0,0]\n"
        "v_pk_fma_f32 v[92:93], v[92:93], s[24:25], v[134:135] op_sel_hi:[1,0,0]\n"
        "v_pk_fma_f32 v[86:87], v[86:87], s[24:25], v[134:135] op_sel_hi:[1,0,0]\n"
        "v_pk_fma_f32 v[88:89], v[88:89], s[24:25], v[134:135] op_sel_hi:[1,0,0]\n"
        "v_pk_fma_f32 v[94:95], v[94:95], s[24:25], v[134:135] op_sel_hi:[1,0,0]\n"
        "v_pk_fma_f32 v[96:97], v[96:97], s[24:25], v[134:135] op_sel_hi:[1,0,0]\n"
        "v_pk_fma_f32 v[82:83], v[82:83], s[24:25], v[134:135] op_sel_hi:[1,0,0]\n"
        "v_pk_fma_f32 v[84:85], v[84:85], s[24:25], v[134:135] op_sel_hi:[1,0,0]\n"
        "v_pk_fma_f32 v[102:103], v[102:103], s[24:25], v[70:71] op_sel_hi:[1,0,0]\n"
        "v_pk_fma_f32 v[104:105], v[104:105], s[24:25], v[70:71] op_sel_hi:[1,0,0]\n"
        "v_pk_fma_f32 v[106:107], v[106:107], s[24:25], v[70:71] op_sel_hi:[1,0,0]\n"
        "v_pk_fma_f32 v[108:109], v[108:109], s[24:25], v[70:71] op_sel_hi:[1,0,0]\n"
        "v_pk_fma_f32 v[98:99], v[98:99], s[24:25], v[70:71] op_sel_hi:[1,0,0]\n"
        "v_pk_fma_f32 v[100:101], v[100:101], s[24:25], v[70:71] op_sel_hi:[1,0,0]\n"
        "v_pk_fma_f32 v[110:111], v[110:111], s[24:25], v[70:71] op_sel_hi:[1,0,0]\n"
        "v_pk_fma_f32 v[112:113], v[112:113], s[24:25], v[70:71] op_sel_hi:[1,0,0]\n"
        "v_pk_fma_f32 v[118:119], v[118:119], s[24:25], v[70:71] op_sel_hi:[1,0,0]\n"
        "v_pk_fma_f32 v[120:121], v[120:121], s[24:25], v[70:71] op_sel_hi:[1,0,0]\n"
        "v_pk_fma_f32 v[54:55], v[54:55], s[24:25], v[70:71] op_sel_hi:[1,0,0]\n"
        "v_pk_fma_f32 v[56:57], v[56:57], s[24:25], v[70:71] op_sel_hi:[1,0,0]\n"
        "v_cvt_pk_bf16_f32 v30, v74, v75\n"
        "v_cvt_pk_bf16_f32 v31, v76, v77\n"
        "v_cvt_pk_bf16_f32 v32, v78, v79\n"
        "v_cvt_pk_bf16_f32 v33, v80, v81\n"
        "v_cvt_pk_bf16_f32 v66, v90, v91\n"
        "v_cvt_pk_bf16_f32 v67, v92, v93\n"
        "v_cvt_pk_bf16_f32 v68, v86, v87\n"
        "v_cvt_pk_bf16_f32 v69, v88, v89\n"
        "v_cvt_pk_bf16_f32 v70, v94, v95\n"
        "v_cvt_pk_bf16_f32 v71, v96, v97\n"
        "v_cvt_pk_bf16_f32 v72, v82, v83\n"
        "v_cvt_pk_bf16_f32 v73, v84, v85\n"
        "v_cvt_pk_bf16_f32 v78, v102, v103\n"
        "v_cvt_pk_bf16_f32 v79, v104, v105\n"
        "v_cvt_pk_bf16_f32 v80, v106, v107\n"
        "v_cvt_pk_bf16_f32 v81, v108, v109\n"
        "v_cvt_pk_bf16_f32 v74, v98, v99\n"
        "v_cvt_pk_bf16_f32 v75, v100, v101\n"
        "v_cvt_pk_bf16_f32 v76, v110, v111\n"
        "v_cvt_pk_bf16_f32 v77, v112, v113\n"
        "v_cvt_pk_bf16_f32 v82, v118, v119\n"
        "v_cvt_pk_bf16_f32 v83, v120, v121\n"
        "v_cvt_pk_bf16_f32 v84, v54, v55\n"
        "v_cvt_pk_bf16_f32 v85, v56, v57\n"
        "v_add_f32 v114, v114, v62\n"
        "ds_write_b128 v42, v[30:33]\n"
        "ds_write_b128 v42, v[66:69] offset:1024\n"
        "ds_write_b128 v42, v[70:73] offset:2048\n"
        "ds_write_b128 v43, v[78:81]\n"
        "ds_write_b128 v43, v[74:77] offset:1024\n"
        "ds_write_b128 v43, v[82:85] offset:2048\n"
        "v_add_f32 v114, v114, v63\n"
        "v_add_f32 v114, v114, v64\n"
        "v_add_f32 v114, v114, v65\n"
        "v_mov_b32 v115, v114\n"
        "s_waitcnt vmcnt(9)\n"
        "s_nop 0\n"
        "v_permlane32_swap_b32 v114, v115\n"
        "v_add_f32 v114, v114, v115\n"
        "v_mov_b32 v115, v114\n"
        "s_nop 1\n"
        "v_permlane16_swap_b32 v114, v115\n"
        "v_add_f32 v114, v114, v115\n"
        "v_mul_f32 v114, 0xbac909aa, v114\n"
        "v_pk_fma_f32 v[34:35], v[34:35], s[24:25], v[114:115] op_sel_hi:[1,0,0]\n"
        "v_pk_fma_f32 v[36:37], v[36:37], s[24:25], v[114:115] op_sel_hi:[1,0,0]\n"
        "v_pk_fma_f32 v[46:47], v[46:47], s[24:25], v[114:115] op_sel_hi:[1,0,0]\n"
        "v_pk_fma_f32 v[48:49], v[48:49], s[24:25], v[114:115] op_sel_hi:[1,0,0]\n"
        "v_pk_fma_f32 v[50:51], v[50:51], s[24:25], v[114:115] op_sel_hi:[1,0,0]\n"
        "v_pk_fma_f32 v[52:53], v[52:53], s[24:25], v[114:115] op_sel_hi:[1,0,0]\n"
        "v_pk_fma_f32 v[58:59], v[58:59], s[24:25], v[114:115] op_sel_hi:[1,0,0]\n"
        "v_pk_fma_f32 v[60:61], v[60:61], s[24:25], v[114:115] op_sel_hi:[1,0,0]\n"
        "v_pk_fma_f32 v[38:39], v[38:39], s[24:25], v[114:115] op_sel_hi:[1,0,0]\n"
        "v_pk_fma_f32 v[40:41], v[40:41], s[24:25], v[114:115] op_sel_hi:[1,0,0]\n"
        "v_pk_fma_f32 v[62:63], v[62:63], s[24:25], v[114:115] op_sel_hi:[1,0,0]\n"
        "v_pk_fma_f32 v[64:65], v[64:65], s[24:25], v[114:115] op_sel_hi:[1,0,0]\n"
        "v_cvt_pk_bf16_f32 v54, v34, v35\n"
        "v_cvt_pk_bf16_f32 v55, v36, v37\n"
        "v_cvt_pk_bf16_f32 v56, v46, v47\n"
        "v_cvt_pk_bf16_f32 v57, v48, v49\n"
        "v_cvt_pk_bf16_f32 v34, v50, v51\n"
        "v_cvt_pk_bf16_f32 v35, v52, v53\n"
        "v_cvt_pk_bf16_f32 v36, v58, v59\n"
        "v_cvt_pk_bf16_f32 v37, v60, v61\n"
        "v_cvt_pk_bf16_f32 v30, v38, v39\n"
        "v_cvt_pk_bf16_f32 v31, v40, v41\n"
        "v_cvt_pk_bf16_f32 v32, v62, v63\n"
        "v_cvt_pk_bf16_f32 v33, v64, v65\n"
        "ds_write_b128 v44, v[54:57]\n"
        "ds_write_b128 v44, v[34:37] offset:1024\n"
        "ds_write_b128 v44, v[30:33] offset:2048\n"
        "s_waitcnt lgkmcnt(0)\n"
        "s_barrier\n"
        "s_branch FE_%=\n"
        "FV_%=:\n"
        "s_bitcmp1_b32 s52, 11\n"
        "s_cbranch_scc1 FG_%=\n"
        "v_mov_b32 v50, 0x3f803f80\n"
        "v_mov_b32 v51, 0x3f803f80\n"
        "v_mov_b32 v52, 0x3f803f80\n"
        "v_mov_b32 v53, 0x3f803f80\n"
        "s_waitcnt vmcnt(15)\n"
        "ds_read_b128 a[0:3], v32\n"
        "ds_read_b128 a[4:7], v32 offset:64\n"
        "ds_read_b128 a[8:11], v32 offset:128\n"
        "s_add_u32 m0, s53, 15360\n"
        "s_nop 0\n"
        "buffer_load_dwordx4 v46, s[16:19], s23 offen lds\n"
        "ds_read_b128 a[12:15], v33\n"
        "ds_read_b128 a[16:19], v33 offset:64\n"
        "ds_read_b128 a[20:23], v33 offset:128\n"
        "buffer_load_dwordx4 v47, s[16:19], s23 offen offset:1024 lds\n"
        "ds_read_b128 a[24:27], v32 offset:6144\n"
        "ds_read_b128 a[28:31], v32 offset:6208\n"
        "ds_read_b128 a[32:35], v32 offset:6272\n"
        "buffer_load_dwordx4 v48, s[16:19], s23 offen offset:2048 lds\n"
        "ds_read_b128 a[36:39], v33 offset:6144\n"
        "ds_read_b128 a[40:43], v33 offset:6208\n"
        "ds_read_b128 a[44:47], v33 offset:6272\n"
        "ds_read_b128 a[48:51], v32 offset:12288\n"
        "ds_read_b128 a[52:55], v32 offset:12352\n"
        "ds_read_b128 a[56:59], v32 offset:12416\n"
        "ds_read_b128 a[60:63], v33 offset:12288\n"
        "ds_read_b128 a[64:67], v33 offset:12352\n"
        "ds_read_b128 a[68:71], v33 offset:12416\n"
        "ds_read_b64_tr_b16 a[72:73], v34 offset:0\n"
        "ds_read_b64_tr_b16 a[74:75], v35 offset:0\n"
        "ds_read_b64_tr_b16 a[76:77], v34 offset:6144\n"
        "ds_read_b64_tr_b16 a[78:79], v35 offset:6144\n"
        "ds_read_b64_tr_b16 a[80:81], v34 offset:12288\n"
        "ds_read_b64_tr_b16 a[82:83], v35 offset:12288\n"
        "s_waitcnt lgkmcnt(15)\n"
        "v_mfma_f32_16x16x32_bf16 v[54:57], a[0:3], v[50:53], 0\n"
        "s_waitcnt vmcnt(12)\n"
        "s_waitcnt vmcnt(6)\n"
        "v_mfma_f32_16x16x32_bf16 v[54:57], a[4:7], v[50:53], v[54:57]\n"
        "s_waitcnt lgkmcnt(15)\n"
        "v_mfma_f32_16x16x32_bf16 v[54:57], a[8:11], v[50:53], v[54:57]\n"
        "s_waitcnt vmcnt(0)\n"
        "s_nop 0\n"
        "v_mfma_f32_16x16x32_bf16 v[58:61], a[20:23], v[50:53], 0\n"
        "s_waitcnt lgkmcnt(15)\n"
        "s_nop 1\n"
        "v_mfma_f32_16x16x32_bf16 v[58:61], a[16:19], v[50:53], v[58:61]\n"
        "s_waitcnt lgkmcnt(12)\n"
        "v_cvt_pk_bf16_f32 v46, v54, v55\n"
        "v_cvt_pk_bf16_f32 v47, v56, v57\n"
        "v_mfma_f32_16x16x32_bf16 v[58:61], a[12:15], v[50:53], v[58:61]\n"
        "s_waitcnt lgkmcnt(9)\n"
        "v_mfma_f32_16x16x32_bf16 v[54:57], a[24:27], v[50:53], 0\n"
        "s_waitcnt lgkmcnt(6)\n"
        "ds_read_b128 a[144:147], v30\n"
        "s_nop 0\n"
        "v_mfma_f32_16x16x32_bf16 v[54:57], a[28:31], v[50:53], v[54:57]\n"
        "s_waitcnt lgkmcnt(1)\n"
        "ds_read_b128 a[148:151], v30 offset:64\n"
        "v_cvt_pk_bf16_f32 v48, v58, v59\n"
        "v_cvt_pk_bf16_f32 v49, v60, v61\n"
        "v_mfma_f32_16x16x32_bf16 v[54:57], a[32:35], v[50:53], v[54:57]\n"
        "s_nop 0\n"
        "ds_read_b128 a[152:155], v30 offset:128\n"
        "v_mfma_f32_16x16x32_bf16 v[58:61], a[44:47], v[50:53], 0\n"
        "s_nop 0\n"
        "ds_read_b128 a[156:159], v31\n"
        "v_mfma_f32_16x16x32_bf16 v[58:61], a[40:43], v[50:53], v[58:61]\n"
        "s_nop 0\n"
        "ds_read_b128 a[160:163], v31 offset:64\n"
        "v_cvt_pk_bf16_f32 v66, v54, v55\n"
        "v_cvt_pk_bf16_f32 v67, v56, v57\n"
        "v_mfma_f32_16x16x32_bf16 v[58:61], a[36:39], v[50:53], v[58:61]\n"
        "s_nop 0\n"
        "ds_read_b128 a[164:167], v31 offset:128\n"
        "v_mfma_f32_16x16x32_bf16 v[54:57], a[48:51], v[50:53], 0\n"
        "s_nop 0\n"
        "ds_read_b128 a[168:171], v30 offset:6144\n"
        "v_mfma_f32_16x16x32_bf16 v[54:57], a[52:55], v[50:53], v[54:57]\n"
        "s_nop 0\n"
        "ds_read_b128 a[172:175], v30 offset:6208\n"
        "v_cvt_pk_bf16_f32 v68, v58, v59\n"
        "v_cvt_pk_bf16_f32 v69, v60, v61\n"
        "v_mfma_f32_16x16x32_bf16 v[54:57], a[56:59], v[50:53], v[54:57]\n"
        "s_nop 0\n"
        "ds_read_b128 a[176:179], v30 offset:6272\n"
        "v_mfma_f32_16x16x32_bf16 v[58:61], a[68:71], v[50:53], 0\n"
        "s_nop 0\n"
        "ds_read_b128 a[180:183], v31 offset:6144\n"
        "v_mfma_f32_16x16x32_bf16 v[58:61], a[64:67], v[50:53], v[58:61]\n"
        "s_nop 0\n"
        "ds_read_b128 a[184:187], v31 offset:6208\n"
        "v_cvt_pk_bf16_f32 v70, v54, v55\n"
        "v_cvt_pk_bf16_f32 v71, v56, v57\n"
        "v_mfma_f32_16x16x32_bf16 v[58:61], a[60:63], v[50:53], v[58:61]\n"
        "ds_read_b128 a[188:191], v31 offset:6272\n"
        "s_waitcnt lgkmcnt(9)\n"
        "v_mfma_f32_16x16x32_bf16 v[50:53], a[144:147], a[72:75], 0\n"
        "s_nop 0\n"
        "ds_read_b128 a[192:195], v30 offset:12288\n"
        "v_mfma_f32_16x16x32_bf16 v[50:53], a[148:151], a[76:79], v[50:53]\n"
        "s_nop 0\n"
        "ds_read_b128 a[196:199], v30 offset:12352\n"
        "v_cvt_pk_bf16_f32 v72, v58, v59\n"
        "v_cvt_pk_bf16_f32 v73, v60, v61\n"
        "v_mfma_f32_16x16x32_bf16 v[50:53], a[152:155], a[80:83], v[50:53]\n"
        "ds_read_b128 a[200:203], v30 offset:12416\n"
        "v_mfma_f32_16x16x32_bf16 v[54:57], a[152:155], v[70:73], 0\n"
        "s_waitcnt lgkmcnt(9)\n"
        "buffer_load_dwordx4 v[188:191], v36, s[36:39], 0 offen\n"
        "v_mfma_f32_16x16x32_bf16 v[54:57], a[148:151], v[66:69], v[54:57]\n"
        "s_nop 0\n"
        "ds_read_b128 a[204:207], v31 offset:12288\n"
        "ds_read_b128 a[208:211], v31 offset:12352\n"
        "v_add_f32 v254, v50, v51\n"
        "v_mfma_f32_16x16x32_bf16 v[54:57], a[144:147], v[46:49], v[54:57]\n"
        "ds_read_b128 a[212:215], v31 offset:12416\n"
        "v_add_f32 v254, v254, v52\n"
        "v_add_f32 v254, v254, v53\n"
        "v_mfma_f32_16x16x32_bf16 v[82:85], a[156:159], a[72:75], 0\n"
        "s_waitcnt lgkmcnt(9)\n"
        "ds_read_b64_tr_b16 a[84:85], v34 offset:8\n"
        "v_mfma_f32_16x16x32_bf16 v[82:85], a[160:163], a[76:79], v[82:85]\n"
        "ds_read_b64_tr_b16 a[86:87], v35 offset:8\n"
        "v_add_f32 v255, v54, v55\n"
        "v_add_f32 v255, v255, v56\n"
        "v_mfma_f32_16x16x32_bf16 v[82:85], a[164:167], a[80:83], v[82:85]\n"
        "ds_read_b64_tr_b16 a[88:89], v34 offset:6152\n"
        "v_add_f32 v255, v255, v57\n"
        "v_mfma_f32_16x16x32_bf16 v[30:33], a[164:167], v[70:73], 0\n"
        "s_waitcnt lgkmcnt(9)\n"
        "ds_read_b64_tr_b16 a[90:91], v35 offset:6152\n"
        "v_mfma_f32_16x16x32_bf16 v[30:33], a[160:163], v[66:69], v[30:33]\n"
        "s_nop 0\n"
        "ds_read_b64_tr_b16 a[92:93], v34 offset:12296\n"
        "v_add_f32 v254, v254, v82\n"
        "v_add_f32 v254, v254, v83\n"
        "v_mfma_f32_16x16x32_bf16 v[30:33], a[156:159], v[46:49], v[30:33]\n"
        "ds_read_b64_tr_b16 a[94:95], v35 offset:12296\n"
        "v_add_f32 v254, v254, v84\n"
        "v_add_f32 v254, v254, v85\n"
        "v_mfma_f32_16x16x32_bf16 v[88:91], a[168:171], a[72:75], 0\n"
        "s_waitcnt lgkmcnt(9)\n"
        "buffer_load_dwordx4 v[194:197], v36, s[36:39], 0 offen offset:16\n"
        "v_mfma_f32_16x16x32_bf16 v[88:91], a[172:175], a[76:79], v[88:91]\n"
        "s_mov_b32 s78, 0xf0000f\n"
        "ds_read_b64_tr_b16 a[96:97], v34 offset:64\n"
        "ds_read_b64_tr_b16 a[98:99], v35 offset:64\n"
        "v_add_f32 v255, v255, v30\n"
        "v_mfma_f32_16x16x32_bf16 v[88:91], a[176:179], a[80:83], v[88:91]\n"
        "s_mov_b32 s79, 0xf0000f00\n"
        "ds_read_b64_tr_b16 a[100:101], v34 offset:6208\n"
        "v_add_f32 v255, v255, v31\n"
        "v_add_f32 v255, v255, v32\n"
        "v_mfma_f32_16x16x32_bf16 v[58:61], a[176:179], v[70:73], 0\n"
        "s_mov_b32 s76, 0x11111111\n"
        "ds_read_b64_tr_b16 a[102:103], v35 offset:6208\n"
        "v_add_f32 v255, v255, v33\n"
        "v_add_f32 v254, v254, v88\n"
        "v_mfma_f32_16x16x32_bf16 v[58:61], a[172:175], v[66:69], v[58:61]\n"
        "s_waitcnt lgkmcnt(10)\n"
        "ds_read_b64_tr_b16 a[104:105], v34 offset:12352\n"
        "v_add_f32 v254, v254, v89\n"
        "v_add_f32 v254, v254, v90\n"
        "v_mfma_f32_16x16x32_bf16 v[58:61], a[168:171], v[46:49], v[58:61]\n"
        "s_mov_b32 s77, 0x11111111\n"
        "ds_read_b64_tr_b16 a[106:107], v35 offset:12352\n"
        "v_add_f32 v254, v254, v91\n"
        "v_mfma_f32_16x16x32_bf16 v[92:95], a[180:183], a[72:75], 0\n"
        "s_mov_b32 s56, 0x22222222\n"
        "ds_read_b64_tr_b16 a[108:109], v34 offset:72\n"
        "v_mfma_f32_16x16x32_bf16 v[92:95], a[184:187], a[76:79], v[92:95]\n"
        "s_mov_b32 s57, 0x22222222\n"
        "ds_read_b64_tr_b16 a[110:111], v35 offset:72\n"
        "v_add_f32 v255, v255, v58\n"
        "v_add_f32 v255, v255, v59\n"
        "v_mfma_f32_16x16x32_bf16 v[92:95], a[188:191], a[80:83], v[92:95]\n"
        "s_mov_b32 s58, 0x44444444\n"
        "ds_read_b64_tr_b16 a[112:113], v34 offset:6216\n"
        "v_add_f32 v255, v255, v60\n"
        "v_add_f32 v255, v255, v61\n"
        "v_mfma_f32_16x16x32_bf16 v[62:65], a[188:191], v[70:73], 0\n"
        "s_waitcnt lgkmcnt(9)\n"
        "buffer_load_dwordx4 v[200:203], v36, s[36:39], 0 offen offset:32\n"
        "v_mfma_f32_16x16x32_bf16 v[62:65], a[184:187], v[66:69], v[62:65]\n"
        "s_mov_b32 s59, 0x44444444\n"
        "ds_read_b64_tr_b16 a[114:115], v35 offset:6216\n"
        "ds_read_b64_tr_b16 a[116:117], v34 offset:12360\n"
        "v_add_f32 v254, v254, v92\n"
        "v_mfma_f32_16x16x32_bf16 v[62:65], a[180:183], v[46:49], v[62:65]\n"
        "s_mov_b32 s60, 0x88888888\n"
        "ds_read_b64_tr_b16 a[118:119], v35 offset:12360\n"
        "v_add_f32 v254, v254, v93\n"
        "v_add_f32 v254, v254, v94\n"
        "v_mfma_f32_16x16x32_bf16 v[96:99], a[192:195], a[72:75], 0\n"
        "s_mov_b32 s61, 0x88888888\n"
        "ds_read_b64_tr_b16 a[120:121], v34 offset:128\n"
        "v_add_f32 v254, v254, v95\n"
        "v_add_f32 v255, v255, v62\n"
        "v_mfma_f32_16x16x32_bf16 v[96:99], a[196:199], a[76:79], v[96:99]\n"
        "s_mov_b32 s62, 0xffff\n"
        "ds_read_b64_tr_b16 a[122:123], v35 offset:128\n"
        "v_add_f32 v255, v255, v63\n"
        "v_add_f32 v255, v255, v64\n"
        "v_mfma_f32_16x16x32_bf16 v[96:99], a[200:203], a[80:83], v[96:99]\n"
        "s_mov_b32 s63, 0x0\n"
        "ds_read_b64_tr_b16 a[124:125], v34 offset:6272\n"
        "v_add_f32 v255, v255, v65\n"
        "v_mfma_f32_16x16x32_bf16 v[110:113], a[200:203], v[70:73], 0\n"
        "s_mov_b32 s64, 0xffff0000\n"
        "ds_read_b64_tr_b16 a[126:127], v35 offset:6272\n"
        "v_mfma_f32_16x16x32_bf16 v[110:113], a[196:199], v[66:69], v[110:113]\n"
        "s_mov_b32 s65, 0x0\n"
        "ds_read_b64_tr_b16 a[128:129], v34 offset:12416\n"
        "v_add_f32 v254, v254, v96\n"
        "v_add_f32 v254, v254, v97\n"
        "v_mfma_f32_16x16x32_bf16 v[110:113], a[192:195], v[46:49], v[110:113]\n"
        "s_mov_b32 s66, 0x0\n"
        "ds_read_b64_tr_b16 a[130:131], v35 offset:12416\n"
        "v_add_f32 v254, v254, v98\n"
        "v_add_f32 v254, v254, v99\n"
        "v_mfma_f32_16x16x32_bf16 v[106:109], a[204:207], a[72:75], 0\n"
        "s_waitcnt lgkmcnt(12)\n"
        "buffer_load_dwordx4 v[208:211], v37, s[36:39], 0 offen\n"
        "v_mfma_f32_16x16x32_bf16 v[106:109], a[208:211], a[76:79], v[106:109]\n"
        "s_mov_b32 s67, 0xffff\n"
        "ds_read_b64_tr_b16 a[132:133], v34 offset:136\n"
        "ds_read_b64_tr_b16 a[134:135], v35 offset:136\n"
        "v_add_f32 v255, v255, v110\n"
        "v_mfma_f32_16x16x32_bf16 v[106:109], a[212:215], a[80:83], v[106:109]\n"
        "s_mov_b32 s68, 0x0\n"
        "ds_read_b64_tr_b16 a[136:137], v34 offset:6280\n"
        "v_add_f32 v255, v255, v111\n"
        "v_add_f32 v255, v255, v112\n"
        "v_mfma_f32_16x16x32_bf16 v[102:105], a[212:215], v[70:73], 0\n"
        "s_mov_b32 s69, 0xffff0000\n"
        "ds_read_b64_tr_b16 a[138:139], v35 offset:6280\n"
        "v_add_f32 v255, v255, v113\n"
        "v_add_f32 v254, v254, v106\n"
        "v_mfma_f32_16x16x32_bf16 v[102:105], a[208:211], v[66:69], v[102:105]\n"
        "s_mov_b32 s70, 0x0\n"
        "ds_read_b64_tr_b16 a[140:141], v34 offset:12424\n"
        "v_add_f32 v254, v254, v107\n"
        "v_add_f32 v254, v254, v108\n"
        "v_mfma_f32_16x16x32_bf16 v[102:105], a[204:207], v[46:49], v[102:105]\n"
        "s_mov_b32 s71, 0xffffffff\n"
        "ds_read_b64_tr_b16 a[142:143], v35 offset:12424\n"
        "v_add_f32 v254, v254, v109\n"
        "v_mov_b32 v253, v254\n"
        "v_mfma_f32_16x16x32_bf16 v[46:49], a[144:147], a[84:87], 0\n"
        "s_mov_b32 s72, 0x0\n"
        "v_permlane32_swap_b32 v254, v253\n"
        "v_add_f32 v254, v254, v253\n"
        "v_mfma_f32_16x16x32_bf16 v[46:49], a[148:151], a[88:91], v[46:49]\n"
        "s_mov_b32 s73, 0x20000\n"
        "v_mov_b32 v253, v254\n"
        "v_add_f32 v255, v255, v102\n"
        "v_add_f32 v255, v255, v103\n"
        "v_mfma_f32_16x16x32_bf16 v[46:49], a[152:155], a[92:95], v[46:49]\n"
        "s_mov_b64 vcc, s[78:79]\n"
        "buffer_load_dwordx4 v[214:217], v37, s[36:39], 0 offen offset:16\n"
        "v_add_f32 v255, v255, v104\n"
        "v_add_f32 v255, v255, v105\n"
        "v_mfma_f32_16x16x32_bf16 v[70:73], a[164:167], a[92:95], 0\n"
        "s_mov_b32 s80, 0x3c2aaaab\n"
        "v_permlane16_swap_b32 v254, v253\n"
        "v_add_f32 v254, v254, v253\n"
        "v_mfma_f32_16x16x32_bf16 v[70:73], a[160:163], a[88:91], v[70:73]\n"
        "s_mov_b32 s81, 0x3727c5ac\n"
        "v_mul_f32 v254, 0xbc2aaaab, v254\n"
        "v_add_f32 v50, v50, v254\n"
        "v_add_f32 v51, v51, v254\n"
        "v_mfma_f32_16x16x32_bf16 v[70:73], a[156:159], a[84:87], v[70:73]\n"
        "s_mov_b32 s82, 0x43800000\n"
        "v_add_f32 v52, v52, v254\n"
        "v_add_f32 v53, v53, v254\n"
        "v_add_f32 v82, v82, v254\n"
        "v_mfma_f32_16x16x32_bf16 v[74:77], a[168:171], a[84:87], 0\n"
        "s_mov_b32 s84, 0x80000000\n"
        "v_add_f32 v83, v83, v254\n"
        "v_add_f32 v84, v84, v254\n"
        "v_add_f32 v85, v85, v254\n"
        "v_mfma_f32_16x16x32_bf16 v[74:77], a[172:175], a[88:91], v[74:77]\n"
        "s_sub_u32 s85, s48, 1\n"
        "v_add_f32 v88, v88, v254\n"
        "v_add_f32 v89, v89, v254\n"
        "v_add_f32 v90, v90, v254\n"
        "v_mfma_f32_16x16x32_bf16 v[74:77], a[176:179], a[92:95], v[74:77]\n"
        "s_waitcnt lgkmcnt(0)\n"
        "v_add_f32 v91, v91, v254\n"
        "v_add_f32 v92, v92, v254\n"
        "v_add_f32 v93, v93, v254\n"
        "v_mfma_f32_16x16x32_bf16 v[66:69], a[188:191], a[92:95], 0\n"
        "s_barrier\n"
        "v_add_f32 v94, v94, v254\n"
        "v_add_f32 v95, v95, v254\n"
        "v_add_f32 v96, v96, v254\n"
        "v_mfma_f32_16x16x32_bf16 v[66:69], a[184:187], a[88:91], v[66:69]\n"
        "s_mov_b32 s86, 0x80000000\n"
        "v_add_f32 v97, v97, v254\n"
        "v_add_f32 v98, v98, v254\n"
        "v_add_f32 v99, v99, v254\n"
        "v_mfma_f32_16x16x32_bf16 v[66:69], a[180:183], a[84:87], v[66:69]\n"
        "s_mov_b32 s89, 0x80000000\n"
        "buffer_load_dwordx4 v[220:223], v37, s[36:39], 0 offen offset:32\n"
        "v_add_f32 v106, v106, v254\n"
        "v_add_f32 v107, v107, v254\n"
        "v_mfma_f32_16x16x32_bf16 v[78:81], a[192:195], a[84:87], 0\n"
        "s_mov_b32 s29, 1\n"
        "v_add_f32 v108, v108, v254\n"
        "v_add_f32 v109, v109, v254\n"
        "v_cvt_pk_bf16_f32 v34, v50, v51\n"
        "v_mfma_f32_16x16x32_bf16 v[78:81], a[196:199], a[88:91], v[78:81]\n"
        "s_mov_b32 s31, 0x400000\n"
        "v_cvt_pk_bf16_f32 v35, v52, v53\n"
        "v_cvt_pk_bf16_f32 v36, v82, v83\n"
        "v_cvt_pk_bf16_f32 v37, v84, v85\n"
        "v_mfma_f32_16x16x32_bf16 v[78:81], a[200:203], a[92:95], v[78:81]\n"
        "s_mov_b32 s87, s49\n"
        "ds_write_b128 v39, v[34:37]\n"
        "v_cvt_pk_bf16_f32 v34, v88, v89\n"
        "v_mfma_f32_16x16x32_bf16 v[86:89], a[212:215], a[92:95], 0\n"
        "s_mov_b32 s88, s50\n"
        "v_cvt_pk_bf16_f32 v35, v90, v91\n"
        "v_cvt_pk_bf16_f32 v36, v92, v93\n"
        "v_cvt_pk_bf16_f32 v37, v94, v95\n"
        "v_mfma_f32_16x16x32_bf16 v[86:89], a[208:211], a[88:91], v[86:89]\n"
        "s_mov_b32 s54, 0x05040302\n"
        "ds_write_b128 v39, v[34:37] offset:1024\n"
        "v_cvt_pk_bf16_f32 v34, v96, v97\n"
        "v_mfma_f32_16x16x32_bf16 v[86:89], a[204:207], a[84:87], v[86:89]\n"
        "s_mov_b32 s55, 0x07060302\n"
        "v_cvt_pk_bf16_f32 v35, v98, v99\n"
        "v_cvt_pk_bf16_f32 v36, v106, v107\n"
        "v_cvt_pk_bf16_f32 v37, v108, v109\n"
        "v_mfma_f32_16x16x32_bf16 v[50:53], a[144:147], a[96:99], 0\n"
        "s_mov_b64 s[44:45], s[40:41]\n"
        "ds_write_b128 v39, v[34:37] offset:2048\n"
        "v_mov_b32 v254, v255\n"
        "v_mfma_f32_16x16x32_bf16 v[50:53], a[148:151], a[100:103], v[50:53]\n"
        "s_add_u32 m0, s52, 0\n"
        "v_permlane32_swap_b32 v255, v254\n"
        "v_add_f32 v255, v255, v254\n"
        "v_mfma_f32_16x16x32_bf16 v[50:53], a[152:155], a[104:107], v[50:53]\n"
        "s_mov_b32 s46, 0\n"
        "buffer_load_dwordx4 v0, s[36:39], s49 offen lds\n"
        "v_mov_b32 v254, v255\n"
        "v_add_f32 v253, v46, v47\n"
        "v_mfma_f32_16x16x32_bf16 v[82:85], a[164:167], a[104:107], 0\n"
        "s_mov_b32 s47, s43\n"
        "v_permlane16_swap_b32 v255, v254\n"
        "v_add_f32 v255, v255, v254\n"
        "v_mfma_f32_16x16x32_bf16 v[82:85], a[160:163], a[100:103], v[82:85]\n"
        "s_waitcnt lgkmcnt(3)\n"
        "v_add_f32 v253, v253, v48\n"
        "v_mul_f32 v255, 0xbc2aaaab, v255\n"
        "v_add_f32 v253, v253, v49\n"
        "v_mfma_f32_16x16x32_bf16 v[82:85], a[156:159], a[96:99], v[82:85]\n"
        "s_waitcnt lgkmcnt(3)\n"
        "v_add_f32 v54, v54, v255\n"
        "v_add_f32 v55, v55, v255\n"
        "v_add_f32 v56, v56, v255\n"
        "v_mfma_f32_16x16x32_bf16 v[94:97], a[168:171], a[96:99], 0\n"
        "s_waitcnt lgkmcnt(3)\n"
        "v_add_f32 v57, v57, v255\n"
        "v_add_f32 v30, v30, v255\n"
        "v_add_f32 v31, v31, v255\n"
        "v_mfma_f32_16x16x32_bf16 v[94:97], a[172:175], a[100:103], v[94:97]\n"
        "v_add_f32 v32, v32, v255\n"
        "v_add_f32 v33, v33, v255\n"
        "v_add_f32 v58, v58, v255\n"
        "v_mfma_f32_16x16x32_bf16 v[94:97], a[176:179], a[104:107], v[94:97]\n"
        "buffer_load_dwordx4 v1, s[36:39], s49 offen offset:1024 lds\n"
        "v_add_f32 v59, v59, v255\n"
        "v_add_f32 v60, v60, v255\n"
        "v_mfma_f32_16x16x32_bf16 v[34:37], a[188:191], a[104:107], 0\n"
        "v_add_f32 v61, v61, v255\n"
        "v_add_f32 v62, v62, v255\n"
        "v_add_f32 v63, v63, v255\n"
        "v_mfma_f32_16x16x32_bf16 v[34:37], a[184:187], a[100:103], v[34:37]\n"
        "v_add_f32 v64, v64, v255\n"
        "v_add_f32 v65, v65, v255\n"
        "v_add_f32 v110, v110, v255\n"
        "v_mfma_f32_16x16x32_bf16 v[34:37], a[180:183], a[96:99], v[34:37]\n"
        "v_add_f32 v111, v111, v255\n"
        "v_add_f32 v112, v112, v255\n"
        "v_add_f32 v113, v113, v255\n"
        "v_mfma_f32_16x16x32_bf16 v[90:93], a[192:195], a[96:99], 0\n"
        "v_add_f32 v102, v102, v255\n"
        "v_add_f32 v103, v103, v255\n"
        "v_add_f32 v104, v104, v255\n"
        "v_mfma_f32_16x16x32_bf16 v[90:93], a[196:199], a[100:103], v[90:93]\n"
        "v_add_f32 v105, v105, v255\n"
        "v_cvt_pk_bf16_f32 v106, v54, v55\n"
        "v_cvt_pk_bf16_f32 v107, v56, v57\n"
        "v_mfma_f32_16x16x32_bf16 v[90:93], a[200:203], a[104:107], v[90:93]\n"
        "buffer_load_dwordx4 v2, s[36:39], s49 offen offset:2048 lds\n"
        "v_cvt_pk_bf16_f32 v108, v30, v31\n"
        "v_cvt_pk_bf16_f32 v109, v32, v33\n"
        "v_mfma_f32_16x16x32_bf16 v[98:101], a[212:215], a[104:107], 0\n"
        "ds_write_b128 v38, v[106:109]\n"
        "v_cvt_pk_bf16_f32 v30, v58, v59\n"
        "v_mfma_f32_16x16x32_bf16 v[98:101], a[208:211], a[100:103], v[98:101]\n"
        "v_cvt_pk_bf16_f32 v31, v60, v61\n"
        "v_cvt_pk_bf16_f32 v32, v62, v63\n"
        "v_cvt_pk_bf16_f32 v33, v64, v65\n"
        "v_mfma_f32_16x16x32_bf16 v[98:101], a[204:207], a[96:99], v[98:101]\n"
        "ds_write_b128 v38, v[30:33] offset:64\n"
        "v_cvt_pk_bf16_f32 v54, v110, v111\n"
        "v_mfma_f32_16x16x32_bf16 v[30:33], a[144:147], a[108:111], 0\n"
        "v_cvt_pk_bf16_f32 v55, v112, v113\n"
        "v_cvt_pk_bf16_f32 v56, v102, v103\n"
        "v_cvt_pk_bf16_f32 v57, v104, v105\n"
        "v_mfma_f32_16x16x32_bf16 v[30:33], a[148:151], a[112:115], v[30:33]\n"
        "ds_write_b128 v38, v[54:57] offset:128\n"
        "v_add_f32 v253, v253, v70\n"
        "v_mfma_f32_16x16x32_bf16 v[30:33], a[152:155], a[116:119], v[30:33]\n"
        "buffer_load_dwordx4 v3, s[36:39], s49 offen offset:3072 lds\n"
        "v_add_f32 v253, v253, v71\n"
        "v_add_f32 v253, v253, v72\n"
        "v_mfma_f32_16x16x32_bf16 v[54:57], a[164:167], a[116:119], 0\n"
        "v_add_f32 v253, v253, v73\n"
        "v_add_f32 v253, v253, v74\n"
        "v_add_f32 v253, v253, v75\n"
        "v_mfma_f32_16x16x32_bf16 v[54:57], a[160:163], a[112:115], v[54:57]\n"
        "v_add_f32 v253, v253, v76\n"
        "v_add_f32 v253, v253, v77\n"
        "v_add_f32 v253, v253, v66\n"
        "v_mfma_f32_16x16x32_bf16 v[54:57], a[156:159], a[108:111], v[54:57]\n"
        "v_add_f32 v253, v253, v67\n"
        "v_add_f32 v253, v253, v68\n"
        "v_add_f32 v253, v253, v69\n"
        "v_mfma_f32_16x16x32_bf16 v[102:105], a[168:171], a[108:111], 0\n"
        "v_add_f32 v253, v253, v78\n"
        "v_add_f32 v253, v253, v79\n"
        "v_add_f32 v253, v253, v80\n"
        "v_mfma_f32_16x16x32_bf16 v[102:105], a[172:175], a[112:115], v[102:105]\n"
        "s_add_u32 m0, s52, 4096\n"
        "v_add_f32 v253, v253, v81\n"
        "v_add_f32 v253, v253, v86\n"
        "v_add_f32 v253, v253, v87\n"
        "v_mfma_f32_16x16x32_bf16 v[102:105], a[176:179], a[116:119], v[102:105]\n"
        "buffer_load_dwordx4 v4, s[36:39], s49 offen lds\n"
        "v_add_f32 v253, v253, v88\n"
        "v_add_f32 v253, v253, v89\n"
        "v_mfma_f32_16x16x32_bf16 v[62:65], a[188:191], a[116:119], 0\n"
        "v_mov_b32 v254, v253\n"
        "v_add_f32 v255, v50, v51\n"
        "v_add_f32 v255, v255, v52\n"
        "v_mfma_f32_16x16x32_bf16 v[62:65], a[184:187], a[112:115], v[62:65]\n"
        "v_permlane32_swap_b32 v253, v254\n"
        "v_add_f32 v253, v253, v254\n"
        "v_mfma_f32_16x16x32_bf16 v[62:65], a[180:183], a[108:111], v[62:65]\n"
        "v_mov_b32 v254, v253\n"
        "v_add_f32 v255, v255, v53\n"
        "v_add_f32 v255, v255, v82\n"
        "v_mfma_f32_16x16x32_bf16 v[106:109], a[192:195], a[108:111], 0\n"
        "v_permlane16_swap_b32 v253, v254\n"
        "v_add_f32 v253, v253, v254\n"
        "v_mfma_f32_16x16x32_bf16 v[106:109], a[196:199], a[112:115], v[106:109]\n"
        "v_mul_f32 v253, 0xbc2aaaab, v253\n"
        "v_add_f32 v46, v46, v253\n"
        "v_add_f32 v47, v47, v253\n"
        "v_mfma_f32_16x16x32_bf16 v[106:109], a[200:203], a[116:119], v[106:109]\n"
        "buffer_load_dwordx4 v5, s[36:39], s49 offen offset:1024 lds\n"
        "v_add_f32 v48, v48, v253\n"
        "v_add_f32 v49, v49, v253\n"
        "v_mfma_f32_16x16x32_bf16 v[58:61], a[212:215], a[116:119], 0\n"
        "v_add_f32 v70, v70, v253\n"
        "v_add_f32 v71, v71, v253\n"
        "v_add_f32 v72, v72, v253\n"
        "v_mfma_f32_16x16x32_bf16 v[58:61], a[208:211], a[112:115], v[58:61]\n"
        "v_add_f32 v73, v73, v253\n"
        "v_add_f32 v74, v74, v253\n"
        "v_add_f32 v75, v75, v253\n"
        "v_mfma_f32_16x16x32_bf16 v[58:61], a[204:207], a[108:111], v[58:61]\n"
        "v_add_f32 v76, v76, v253\n"
        "v_add_f32 v77, v77, v253\n"
        "v_add_f32 v66, v66, v253\n"
        "v_mfma_f32_16x16x32_bf16 v[110:113], a[144:147], a[120:123], 0\n"
        "v_add_f32 v67, v67, v253\n"
        "v_add_f32 v68, v68, v253\n"
        "v_add_f32 v69, v69, v253\n"
        "v_mfma_f32_16x16x32_bf16 v[110:113], a[148:151], a[124:127], v[110:113]\n"
        "v_add_f32 v78, v78, v253\n"
        "v_add_f32 v79, v79, v253\n"
        "v_add_f32 v80, v80, v253\n"
        "v_mfma_f32_16x16x32_bf16 v[110:113], a[152:155], a[128:131], v[110:113]\n"
        "buffer_load_dwordx4 v6, s[36:39], s49 offen offset:2048 lds\n"
        "v_add_f32 v81, v81, v253\n"
        "v_add_f32 v86, v86, v253\n"
        "v_mfma_f32_16x16x32_bf16 v[114:117], a[164:167], a[128:131], 0\n"
        "v_add_f32 v87, v87, v253\n"
        "v_add_f32 v88, v88, v253\n"
        "v_add_f32 v89, v89, v253\n"
        "v_mfma_f32_16x16x32_bf16 v[114:117], a[160:163], a[124:127], v[114:117]\n"
        "v_cvt_pk_bf16_f32 v118, v46, v47\n"
        "v_cvt_pk_bf16_f32 v119, v48, v49\n"
        "v_cvt_pk_bf16_f32 v120, v70, v71\n"
        "v_mfma_f32_16x16x32_bf16 v[114:117], a[156:159], a[120:123], v[114:117]\n"
        "v_cvt_pk_bf16_f32 v121, v72, v73\n"
        "v_cvt_pk_bf16_f32 v70, v74, v75\n"
        "v_cvt_pk_bf16_f32 v71, v76, v77\n"
        "v_mfma_f32_16x16x32_bf16 v[46:49], a[168:171], a[120:123], 0\n"
        "ds_write_b128 v40, v[118:121]\n"
        "v_cvt_pk_bf16_f32 v72, v66, v67\n"
        "v_mfma_f32_16x16x32_bf16 v[46:49], a[172:175], a[124:127], v[46:49]\n"
        "v_cvt_pk_bf16_f32 v73, v68, v69\n"
        "v_cvt_pk_bf16_f32 v74, v78, v79\n"
        "v_cvt_pk_bf16_f32 v75, v80, v81\n"
        "v_mfma_f32_16x16x32_bf16 v[46:49], a[176:179], a[128:131], v[46:49]\n"
        "ds_write_b128 v40, v[70:73] offset:1024\n"
        "v_cvt_pk_bf16_f32 v76, v86, v87\n"
        "v_mfma_f32_16x16x32_bf16 v[66:69], a[188:191], a[128:131], 0\n"
        "buffer_load_dwordx4 v7, s[36:39], s49 offen offset:3072 lds\n"
        "v_cvt_pk_bf16_f32 v77, v88, v89\n"
        "v_add_f32 v255, v255, v83\n"
        "v_mfma_f32_16x16x32_bf16 v[66:69], a[184:187], a[124:127], v[66:69]\n"
        "ds_write_b128 v40, v[74:77] offset:2048\n"
        "v_add_f32 v255, v255, v84\n"
        "v_mfma_f32_16x16x32_bf16 v[66:69], a[180:183], a[120:123], v[66:69]\n"
        "v_add_f32 v255, v255, v85\n"
        "v_add_f32 v255, v255, v94\n"
        "v_add_f32 v255, v255, v95\n"
        "v_mfma_f32_16x16x32_bf16 v[70:73], a[192:195], a[120:123], 0\n"
        "v_add_f32 v255, v255, v96\n"
        "v_add_f32 v255, v255, v97\n"
        "v_add_f32 v255, v255, v34\n"
        "v_mfma_f32_16x16x32_bf16 v[70:73], a[196:199], a[124:127], v[70:73]\n"
        "s_add_u32 m0, s52, 8192\n"
        "v_add_f32 v255, v255, v35\n"
        "v_add_f32 v255, v255, v36\n"
        "v_add_f32 v255, v255, v37\n"
        "v_mfma_f32_16x16x32_bf16 v[70:73], a[200:203], a[128:131], v[70:73]\n"
        "buffer_load_dwordx4 v8, s[36:39], s49 offen lds\n"
        "v_add_f32 v255, v255, v90\n"
        "v_add_f32 v255, v255, v91\n"
        "v_mfma_f32_16x16x32_bf16 v[78:81], a[212:215], a[128:131], 0\n"
        "v_add_f32 v255, v255, v92\n"
        "v_add_f32 v255, v255, v93\n"
        "v_add_f32 v255, v255, v98\n"
        "v_mfma_f32_16x16x32_bf16 v[78:81], a[208:211], a[124:127], v[78:81]\n"
        "v_add_f32 v255, v255, v99\n"
        "v_add_f32 v255, v255, v100\n"
        "v_add_f32 v255, v255, v101\n"
        "v_mfma_f32_16x16x32_bf16 v[78:81], a[204:207], a[120:123], v[78:81]\n"
        "v_mov_b32 v254, v255\n"
        "v_add_f32 v38, v30, v31\n"
        "v_add_f32 v38, v38, v32\n"
        "v_mfma_f32_16x16x32_bf16 v[74:77], a[144:147], a[132:135], 0\n"
        "v_permlane32_swap_b32 v255, v254\n"
        "v_add_f32 v255, v255, v254\n"
        "v_mfma_f32_16x16x32_bf16 v[74:77], a[148:151], a[136:139], v[74:77]\n"
        "v_mov_b32 v254, v255\n"
        "v_add_f32 v38, v38, v33\n"
        "v_add_f32 v38, v38, v54\n"
        "v_mfma_f32_16x16x32_bf16 v[74:77], a[152:155], a[140:143], v[74:77]\n"
        "v_permlane16_swap_b32 v255, v254\n"
        "v_add_f32 v255, v255, v254\n"
        "v_mfma_f32_16x16x32_bf16 v[86:89], a[164:167], a[140:143], 0\n"
        "v_mul_f32 v255, 0xbc2aaaab, v255\n"
        "v_add_f32 v50, v50, v255\n"
        "v_add_f32 v51, v51, v255\n"
        "v_mfma_f32_16x16x32_bf16 v[86:89], a[160:163], a[136:139], v[86:89]\n"
        "v_add_f32 v52, v52, v255\n"
        "v_add_f32 v53, v53, v255\n"
        "v_add_f32 v82, v82, v255\n"
        "v_mfma_f32_16x16x32_bf16 v[86:89], a[156:159], a[132:135], v[86:89]\n"
        "v_add_f32 v83, v83, v255\n"
        "v_add_f32 v84, v84, v255\n"
        "v_add_f32 v85, v85, v255\n"
        "v_mfma_f32_16x16x32_bf16 v[118:121], a[168:171], a[132:135], 0\n"
        "v_add_f32 v94, v94, v255\n"
        "v_add_f32 v95, v95, v255\n"
        "v_add_f32 v96, v96, v255\n"
        "v_mfma_f32_16x16x32_bf16 v[118:121], a[172:175], a[136:139], v[118:121]\n"
        "v_add_f32 v97, v97, v255\n"
        "v_add_f32 v34, v34, v255\n"
        "v_add_f32 v35, v35, v255\n"
        "v_mfma_f32_16x16x32_bf16 v[118:121], a[176:179], a[140:143], v[118:121]\n"
        "v_add_f32 v36, v36, v255\n"
        "v_add_f32 v37, v37, v255\n"
        "v_add_f32 v90, v90, v255\n"
        "v_mfma_f32_16x16x32_bf16 v[126:129], a[188:191], a[140:143], 0\n"
        "v_add_f32 v91, v91, v255\n"
        "v_add_f32 v92, v92, v255\n"
        "v_add_f32 v93, v93, v255\n"
        "v_mfma_f32_16x16x32_bf16 v[126:129], a[184:187], a[136:139], v[126:129]\n"
        "v_add_f32 v98, v98, v255\n"
        "v_add_f32 v99, v99, v255\n"
        "v_add_f32 v100, v100, v255\n"
        "v_mfma_f32_16x16x32_bf16 v[126:129], a[180:183], a[132:135], v[126:129]\n"
        "v_add_f32 v101, v101, v255\n"
        "v_cvt_pk_bf16_f32 v122, v50, v51\n"
        "v_cvt_pk_bf16_f32 v123, v52, v53\n"
        "v_mfma_f32_16x16x32_bf16 v[50:53], a[192:195], a[132:135], 0\n"
        "v_cvt_pk_bf16_f32 v124, v82, v83\n"
        "v_cvt_pk_bf16_f32 v125, v84, v85\n"
        "v_cvt_pk_bf16_f32 v82, v94, v95\n"
        "v_mfma_f32_16x16x32_bf16 v[50:53], a[196:199], a[136:139], v[50:53]\n"
        "ds_write_b128 v41, v[122:125]\n"
        "v_cvt_pk_bf16_f32 v83, v96, v97\n"
        "v_mfma_f32_16x16x32_bf16 v[50:53], a[200:203], a[140:143], v[50:53]\n"
        "v_cvt_pk_bf16_f32 v84, v34, v35\n"
        "v_cvt_pk_bf16_f32 v85, v36, v37\n"
        "v_cvt_pk_bf16_f32 v34, v90, v91\n"
        "v_mfma_f32_16x16x32_bf16 v[94:97], a[212:215], a[140:143], 0\n"
        "ds_write_b128 v41, v[82:85] offset:1024\n"
        "v_cvt_pk_bf16_f32 v35, v92, v93\n"
        "v_mfma_f32_16x16x32_bf16 v[94:97], a[208:211], a[136:139], v[94:97]\n"
        "v_cvt_pk_bf16_f32 v36, v98, v99\n"
        "v_cvt_pk_bf16_f32 v37, v100, v101\n"
        "v_add_f32 v38, v38, v55\n"
        "v_mfma_f32_16x16x32_bf16 v[94:97], a[204:207], a[132:135], v[94:97]\n"
        "ds_write_b128 v41, v[34:37] offset:2048\n"
        "v_add_f32 v38, v38, v56\n"
        "v_add_f32 v38, v38, v57\n"
        "v_add_f32 v38, v38, v102\n"
        "v_add_f32 v38, v38, v103\n"
        "v_add_f32 v38, v38, v104\n"
        "v_add_f32 v38, v38, v105\n"
        "v_add_f32 v38, v38, v62\n"
        "v_add_f32 v38, v38, v63\n"
        "v_add_f32 v38, v38, v64\n"
        "v_add_f32 v38, v38, v65\n"
        "v_add_f32 v38, v38, v106\n"
        "v_add_f32 v38, v38, v107\n"
        "v_add_f32 v38, v38, v108\n"
        "v_add_f32 v38, v38, v109\n"
        "v_add_f32 v38, v38, v58\n"
        "v_add_f32 v38, v38, v59\n"
        "v_add_f32 v38, v38, v60\n"
        "v_add_f32 v38, v38, v61\n"
        "v_mov_b32 v39, v38\n"
        "v_add_f32 v34, v110, v111\n"
        "v_add_f32 v34, v34, v112\n"
        "v_permlane32_swap_b32 v38, v39\n"
        "v_add_f32 v38, v38, v39\n"
        "v_mov_b32 v39, v38\n"
        "v_add_f32 v34, v34, v113\n"
        "v_add_f32 v34, v34, v114\n"
        "v_permlane16_swap_b32 v38, v39\n"
        "v_add_f32 v38, v38, v39\n"
        "v_mul_f32 v38, 0xbc2aaaab, v38\n"
        "v_add_f32 v34, v34, v115\n"
        "v_add_f32 v34, v34, v116\n"
        "v_add_f32 v34, v34, v117\n"
        "v_add_f32 v34, v34, v46\n"
        "v_add_f32 v34, v34, v47\n"
        "v_add_f32 v34, v34, v48\n"
        "v_add_f32 v34, v34, v49\n"
        "v_add_f32 v34, v34, v66\n"
        "v_add_f32 v34, v34, v67\n"
        "v_add_f32 v34, v34, v68\n"
        "v_add_f32 v34, v34, v69\n"
        "v_add_f32 v34, v34, v70\n"
        "v_add_f32 v34, v34, v71\n"
        "v_add_f32 v34, v34, v72\n"
        "v_add_f32 v34, v34, v73\n"
        "v_add_f32 v34, v34, v78\n"
        "v_add_f32 v34, v34, v79\n"
        "v_add_f32 v34, v34, v80\n"
        "v_add_f32 v34, v34, v81\n"
        "v_mov_b32 v35, v34\n"
        "v_add_f32 v82, v74, v75\n"
        "v_add_f32 v82, v82, v76\n"
        "v_permlane32_swap_b32 v34, v35\n"
        "v_add_f32 v34, v34, v35\n"
        "v_mov_b32 v35, v34\n"
        "v_add_f32 v82, v82, v77\n"
        "v_add_f32 v82, v82, v86\n"
        "v_permlane16_swap_b32 v34, v35\n"
        "v_add_f32 v34, v34, v35\n"
        "v_mul_f32 v34, 0xbc2aaaab, v34\n"
        "v_add_f32 v82, v82, v87\n"
        "v_add_f32 v82, v82, v88\n"
        "v_add_f32 v82, v82, v89\n"
        "v_add_f32 v82, v82, v118\n"
        "v_add_f32 v82, v82, v119\n"
        "v_add_f32 v82, v82, v120\n"
        "v_add_f32 v82, v82, v121\n"
        "v_add_f32 v82, v82, v126\n"
        "v_add_f32 v82, v82, v127\n"
        "v_add_f32 v82, v82, v128\n"
        "v_add_f32 v82, v82, v129\n"
        "v_add_f32 v82, v82, v50\n"
        "v_add_f32 v82, v82, v51\n"
        "v_add_f32 v82, v82, v52\n"
        "v_add_f32 v82, v82, v53\n"
        "v_pk_add_f32 v[30:31], v[30:31], v[38:39] op_sel_hi:[1,0]\n"
        "v_pk_add_f32 v[32:33], v[32:33], v[38:39] op_sel_hi:[1,0]\n"
        "v_pk_add_f32 v[54:55], v[54:55], v[38:39] op_sel_hi:[1,0]\n"
        "v_pk_add_f32 v[56:57], v[56:57], v[38:39] op_sel_hi:[1,0]\n"
        "v_pk_add_f32 v[102:103], v[102:103], v[38:39] op_sel_hi:[1,0]\n"
        "v_pk_add_f32 v[104:105], v[104:105], v[38:39] op_sel_hi:[1,0]\n"
        "v_pk_add_f32 v[62:63], v[62:63], v[38:39] op_sel_hi:[1,0]\n"
        "v_pk_add_f32 v[64:65], v[64:65], v[38:39] op_sel_hi:[1,0]\n"
        "v_pk_add_f32 v[106:107], v[106:107], v[38:39] op_sel_hi:[1,0]\n"
        "v_pk_add_f32 v[108:109], v[108:109], v[38:39] op_sel_hi:[1,0]\n"
        "v_pk_add_f32 v[58:59], v[58:59], v[38:39] op_sel_hi:[1,0]\n"
        "v_pk_add_f32 v[60:61], v[60:61], v[38:39] op_sel_hi:[1,0]\n"
        "v_pk_add_f32 v[110:111], v[110:111], v[34:35] op_sel_hi:[1,0]\n"
        "v_pk_add_f32 v[112:113], v[112:113], v[34:35] op_sel_hi:[1,0]\n"
        "v_pk_add_f32 v[114:115], v[114:115], v[34:35] op_sel_hi:[1,0]\n"
        "v_pk_add_f32 v[116:117], v[116:117], v[34:35] op_sel_hi:[1,0]\n"
        "v_pk_add_f32 v[46:47], v[46:47], v[34:35] op_sel_hi:[1,0]\n"
        "v_pk_add_f32 v[48:49], v[48:49], v[34:35] op_sel_hi:[1,0]\n"
        "v_pk_add_f32 v[66:67], v[66:67], v[34:35] op_sel_hi:[1,0]\n"
        "v_pk_add_f32 v[68:69], v[68:69], v[34:35] op_sel_hi:[1,0]\n"
        "v_pk_add_f32 v[70:71], v[70:71], v[34:35] op_sel_hi:[1,0]\n"
        "v_pk_add_f32 v[72:73], v[72:73], v[34:35] op_sel_hi:[1,0]\n"
        "v_pk_add_f32 v[78:79], v[78:79], v[34:35] op_sel_hi:[1,0]\n"
        "v_pk_add_f32 v[80:81], v[80:81], v[34:35] op_sel_hi:[1,0]\n"
        "v_cvt_pk_bf16_f32 v34, v30, v31\n"
        "v_cvt_pk_bf16_f32 v35, v32, v33\n"
        "v_cvt_pk_bf16_f32 v36, v54, v55\n"
        "v_cvt_pk_bf16_f32 v37, v56, v57\n"
        "v_cvt_pk_bf16_f32 v54, v102, v103\n"
        "v_cvt_pk_bf16_f32 v55, v104, v105\n"
        "v_cvt_pk_bf16_f32 v56, v62, v63\n"
        "v_cvt_pk_bf16_f32 v57, v64, v65\n"
        "v_cvt_pk_bf16_f32 v38, v106, v107\n"
        "v_cvt_pk_bf16_f32 v39, v108, v109\n"
        "v_cvt_pk_bf16_f32 v40, v58, v59\n"
        "v_cvt_pk_bf16_f32 v41, v60, v61\n"
        "v_cvt_pk_bf16_f32 v58, v110, v111\n"
        "v_cvt_pk_bf16_f32 v59, v112, v113\n"
        "v_cvt_pk_bf16_f32 v60, v114, v115\n"
        "v_cvt_pk_bf16_f32 v61, v116, v117\n"
        "v_cvt_pk_bf16_f32 v62, v46, v47\n"
        "v_cvt_pk_bf16_f32 v63, v48, v49\n"
        "v_cvt_pk_bf16_f32 v64, v66, v67\n"
        "v_cvt_pk_bf16_f32 v65, v68, v69\n"
        "v_cvt_pk_bf16_f32 v30, v70, v71\n"
        "v_cvt_pk_bf16_f32 v31, v72, v73\n"
        "v_cvt_pk_bf16_f32 v32, v78, v79\n"
        "v_cvt_pk_bf16_f32 v33, v80, v81\n"
        "v_add_f32 v82, v82, v94\n"
        "ds_write_b128 v42, v[34:37]\n"
        "ds_write_b128 v42, v[54:57] offset:1024\n"
        "ds_write_b128 v42, v[38:41] offset:2048\n"
        "ds_write_b128 v43, v[58:61]\n"
        "ds_write_b128 v43, v[62:65] offset:1024\n"
        "ds_write_b128 v43, v[30:33] offset:2048\n"
        "v_add_f32 v82, v82, v95\n"
        "v_add_f32 v82, v82, v96\n"
        "v_add_f32 v82, v82, v97\n"
        "v_mov_b32 v83, v82\n"
        "s_waitcnt vmcnt(9)\n"
        "s_nop 0\n"
        "v_permlane32_swap_b32 v82, v83\n"
        "v_add_f32 v82, v82, v83\n"
        "v_mov_b32 v83, v82\n"
        "s_nop 1\n"
        "v_permlane16_swap_b32 v82, v83\n"
        "v_add_f32 v82, v82, v83\n"
        "v_mul_f32 v82, 0xbc2aaaab, v82\n"
        "v_pk_add_f32 v[74:75], v[74:75], v[82:83] op_sel_hi:[1,0]\n"
        "v_pk_add_f32 v[76:77], v[76:77], v[82:83] op_sel_hi:[1,0]\n"
        "v_pk_add_f32 v[86:87], v[86:87], v[82:83] op_sel_hi:[1,0]\n"
        "v_pk_add_f32 v[88:89], v[88:89], v[82:83] op_sel_hi:[1,0]\n"
        "v_pk_add_f32 v[118:119], v[118:119], v[82:83] op_sel_hi:[1,0]\n"
        "v_pk_add_f32 v[120:121], v[120:121], v[82:83] op_sel_hi:[1,0]\n"
        "v_pk_add_f32 v[126:127], v[126:127], v[82:83] op_sel_hi:[1,0]\n"
        "v_pk_add_f32 v[128:129], v[128:129], v[82:83] op_sel_hi:[1,0]\n"
        "v_pk_add_f32 v[50:51], v[50:51], v[82:83] op_sel_hi:[1,0]\n"
        "v_pk_add_f32 v[52:53], v[52:53], v[82:83] op_sel_hi:[1,0]\n"
        "v_pk_add_f32 v[94:95], v[94:95], v[82:83] op_sel_hi:[1,0]\n"
        "v_pk_add_f32 v[96:97], v[96:97], v[82:83] op_sel_hi:[1,0]\n"
        "v_cvt_pk_bf16_f32 v34, v74, v75\n"
        "v_cvt_pk_bf16_f32 v35, v76, v77\n"
        "v_cvt_pk_bf16_f32 v36, v86, v87\n"
        "v_cvt_pk_bf16_f32 v37, v88, v89\n"
        "v_cvt_pk_bf16_f32 v38, v118, v119\n"
        "v_cvt_pk_bf16_f32 v39, v120, v121\n"
        "v_cvt_pk_bf16_f32 v40, v126, v127\n"
        "v_cvt_pk_bf16_f32 v41, v128, v129\n"
        "v_cvt_pk_bf16_f32 v30, v50, v51\n"
        "v_cvt_pk_bf16_f32 v31, v52, v53\n"
        "v_cvt_pk_bf16_f32 v32, v94, v95\n"
        "v_cvt_pk_bf16_f32 v33, v96, v97\n"
        "ds_write_b128 v44, v[34:37]\n"
        "ds_write_b128 v44, v[38:41] offset:1024\n"
        "ds_write_b128 v44, v[30:33] offset:2048\n"
        "s_waitcnt lgkmcnt(0)\n"
        "s_barrier\n"
        "s_branch FE_%=\n"
        "FG_%=:\n"
        "s_mov_b32 s24, 0x471c4000\n"
        "v_mov_b32 v255, 0x3f803f80\n"
        "v_mov_b32 v255, 0x3f803f80\n"
        "v_mov_b32 v255, 0x3f803f80\n"
        "v_mov_b32 v255, 0x3f803f80\n"
        "s_waitcnt vmcnt(15)\n"
        "ds_read_b64_tr_b16 a[72:73], v34 offset:0\n"
        "ds_read_b64_tr_b16 a[74:75], v35 offset:0\n"
        "ds_read_b64_tr_b16 a[76:77], v34 offset:6144\n"
        "ds_read_b64_tr_b16 a[78:79], v35 offset:6144\n"
        "ds_read_b64_tr_b16 a[80:81], v34 offset:12288\n"
        "ds_read_b64_tr_b16 a[82:83], v35 offset:12288\n"
        "s_add_u32 m0, s53, 15360\n"
        "s_nop 0\n"
        "buffer_load_dwordx4 v46, s[16:19], s23 offen lds\n"
        "ds_read_b64_tr_b16 a[84:85], v34 offset:8\n"
        "ds_read_b64_tr_b16 a[86:87], v35 offset:8\n"
        "ds_read_b64_tr_b16 a[88:89], v34 offset:6152\n"
        "ds_read_b64_tr_b16 a[90:91], v35 offset:6152\n"
        "ds_read_b64_tr_b16 a[92:93], v34 offset:12296\n"
        "ds_read_b64_tr_b16 a[94:95], v35 offset:12296\n"
        "buffer_load_dwordx4 v47, s[16:19], s23 offen offset:1024 lds\n"
        "ds_read_b64_tr_b16 a[96:97], v34 offset:64\n"
        "ds_read_b64_tr_b16 a[98:99], v35 offset:64\n"
        "ds_read_b64_tr_b16 a[100:101], v34 offset:6208\n"
        "ds_read_b64_tr_b16 a[102:103], v35 offset:6208\n"
        "ds_read_b64_tr_b16 a[104:105], v34 offset:12352\n"
        "ds_read_b64_tr_b16 a[106:107], v35 offset:12352\n"
        "buffer_load_dwordx4 v48, s[16:19], s23 offen offset:2048 lds\n"
        "ds_read_b64_tr_b16 a[108:109], v34 offset:72\n"
        "ds_read_b64_tr_b16 a[110:111], v35 offset:72\n"
        "ds_read_b64_tr_b16 a[112:113], v34 offset:6216\n"
        "ds_read_b64_tr_b16 a[114:115], v35 offset:6216\n"
        "ds_read_b64_tr_b16 a[116:117], v34 offset:12360\n"
        "ds_read_b64_tr_b16 a[118:119], v35 offset:12360\n"
        "ds_read_b64_tr_b16 a[120:121], v34 offset:128\n"
        "ds_read_b64_tr_b16 a[122:123], v35 offset:128\n"
        "ds_read_b64_tr_b16 a[124:125], v34 offset:6272\n"
        "ds_read_b64_tr_b16 a[126:127], v35 offset:6272\n"
        "ds_read_b64_tr_b16 a[128:129], v34 offset:12416\n"
        "ds_read_b64_tr_b16 a[130:131], v35 offset:12416\n"
        "ds_read_b64_tr_b16 a[132:133], v34 offset:136\n"
        "ds_read_b64_tr_b16 a[134:135], v35 offset:136\n"
        "ds_read_b64_tr_b16 a[136:137], v34 offset:6280\n"
        "ds_read_b64_tr_b16 a[138:139], v35 offset:6280\n"
        "ds_read_b64_tr_b16 a[140:141], v34 offset:12424\n"
        "ds_read_b64_tr_b16 a[142:143], v35 offset:12424\n"
        "s_waitcnt vmcnt(12)\n"
        "ds_read_b128 a[144:147], v30\n"
        "ds_read_b128 a[148:151], v30 offset:64\n"
        "ds_read_b128 a[152:155], v30 offset:128\n"
        "ds_read_b128 a[156:159], v31\n"
        "ds_read_b128 a[160:163], v31 offset:64\n"
        "ds_read_b128 a[164:167], v31 offset:128\n"
        "s_waitcnt lgkmcnt(15)\n"
        "s_waitcnt lgkmcnt(3)\n"
        "v_mfma_f32_16x16x32_bf16 v[50:53], a[144:147], a[72:75], 0\n"
        "s_waitcnt vmcnt(6)\n"
        "ds_read_b128 a[168:171], v30 offset:6144\n"
        "v_mov_b32 v255, 161664\n"
        "v_mfma_f32_16x16x32_bf16 v[50:53], a[148:151], a[76:79], v[50:53]\n"
        "s_waitcnt vmcnt(0)\n"
        "ds_read_b128 a[172:175], v30 offset:6208\n"
        "ds_read_b128 a[176:179], v30 offset:6272\n"
        "v_mfma_f32_16x16x32_bf16 v[50:53], a[152:155], a[80:83], v[50:53]\n"
        "s_waitcnt lgkmcnt(6)\n"
        "ds_read_b128 a[180:183], v31 offset:6144\n"
        "ds_read_b128 a[184:187], v31 offset:6208\n"
        "s_waitcnt lgkmcnt(5)\n"
        "v_mfma_f32_16x16x32_bf16 v[58:61], a[164:167], a[80:83], 0\n"
        "s_waitcnt lgkmcnt(5)\n"
        "ds_read_b128 a[188:191], v31 offset:6272\n"
        "ds_read_b128 a[192:195], v30 offset:12288\n"
        "v_add_f32 v254, v50, v51\n"
        "v_mfma_f32_16x16x32_bf16 v[58:61], a[160:163], a[76:79], v[58:61]\n"
        "s_waitcnt lgkmcnt(7)\n"
        "ds_read_b128 a[196:199], v30 offset:12352\n"
        "ds_read_b128 a[200:203], v30 offset:12416\n"
        "v_add_f32 v254, v254, v52\n"
        "v_mfma_f32_16x16x32_bf16 v[58:61], a[156:159], a[72:75], v[58:61]\n"
        "s_waitcnt lgkmcnt(9)\n"
        "ds_read_b128 a[204:207], v31 offset:12288\n"
        "v_add_f32 v254, v254, v53\n"
        "s_waitcnt lgkmcnt(7)\n"
        "v_mfma_f32_16x16x32_bf16 v[70:73], a[168:171], a[72:75], 0\n"
        "s_waitcnt lgkmcnt(7)\n"
        "ds_read_b128 a[208:211], v31 offset:12352\n"
        "ds_read_b128 a[212:215], v31 offset:12416\n"
        "v_add_f32 v254, v254, v58\n"
        "v_mfma_f32_16x16x32_bf16 v[70:73], a[172:175], a[76:79], v[70:73]\n"
        "s_waitcnt lgkmcnt(9)\n"
        "v_add_f32 v254, v254, v59\n"
        "v_add_f32 v254, v254, v60\n"
        "v_add_f32 v254, v254, v61\n"
        "v_mfma_f32_16x16x32_bf16 v[70:73], a[176:179], a[80:83], v[70:73]\n"
        "s_waitcnt lgkmcnt(6)\n"
        "s_nop 0\n"
        "v_mfma_f32_16x16x32_bf16 v[66:69], a[188:191], a[80:83], 0\n"
        "s_waitcnt lgkmcnt(3)\n"
        "s_nop 1\n"
        "v_mfma_f32_16x16x32_bf16 v[66:69], a[184:187], a[76:79], v[66:69]\n"
        "s_nop 0\n"
        "v_add_f32 v254, v254, v70\n"
        "v_add_f32 v254, v254, v71\n"
        "v_add_f32 v254, v254, v72\n"
        "v_mfma_f32_16x16x32_bf16 v[66:69], a[180:183], a[72:75], v[66:69]\n"
        "s_nop 0\n"
        "v_add_f32 v254, v254, v73\n"
        "v_mfma_f32_16x16x32_bf16 v[78:81], a[192:195], a[72:75], 0\n"
        "s_nop 2\n"
        "v_mfma_f32_16x16x32_bf16 v[78:81], a[196:199], a[76:79], v[78:81]\n"
        "s_nop 0\n"
        "v_add_f32 v254, v254, v66\n"
        "v_add_f32 v254, v254, v67\n"
        "v_add_f32 v254, v254, v68\n"
        "v_mfma_f32_16x16x32_bf16 v[78:81], a[200:203], a[80:83], v[78:81]\n"
        "s_nop 0\n"
        "v_add_f32 v254, v254, v69\n"
        "s_waitcnt lgkmcnt(0)\n"
        "v_mfma_f32_16x16x32_bf16 v[74:77], a[212:215], a[80:83], 0\n"
        "s_nop 1\n"
        "v_mfma_f32_16x16x32_bf16 v[74:77], a[208:211], a[76:79], v[74:77]\n"
        "s_nop 0\n"
        "v_add_f32 v254, v254, v78\n"
        "v_add_f32 v254, v254, v79\n"
        "v_add_f32 v254, v254, v80\n"
        "v_mfma_f32_16x16x32_bf16 v[74:77], a[204:207], a[72:75], v[74:77]\n"
        "s_nop 0\n"
        "v_add_f32 v254, v254, v81\n"
        "v_mfma_f32_16x16x32_bf16 v[30:33], a[144:147], a[84:87], 0\n"
        "s_nop 2\n"
        "v_mfma_f32_16x16x32_bf16 v[30:33], a[148:151], a[88:91], v[30:33]\n"
        "s_nop 0\n"
        "v_add_f32 v254, v254, v74\n"
        "v_add_f32 v254, v254, v75\n"
        "v_add_f32 v254, v254, v76\n"
        "v_mfma_f32_16x16x32_bf16 v[30:33], a[152:155], a[92:95], v[30:33]\n"
        "buffer_load_dwordx4 v[188:191], v36, s[36:39], 0 offen\n"
        "v_add_f32 v254, v254, v77\n"
        "v_mov_b32 v253, v254\n"
        "v_mfma_f32_16x16x32_bf16 v[46:49], a[164:167], a[92:95], 0\n"
        "s_nop 0\n"
        "v_permlane32_swap_b32 v254, v253\n"
        "v_add_f32 v254, v254, v253\n"
        "v_mfma_f32_16x16x32_bf16 v[46:49], a[160:163], a[88:91], v[46:49]\n"
        "v_mov_b32 v253, v254\n"
        "v_add_f32 v252, v30, v31\n"
        "v_add_f32 v252, v252, v32\n"
        "v_mfma_f32_16x16x32_bf16 v[46:49], a[156:159], a[84:87], v[46:49]\n"
        "v_permlane16_swap_b32 v254, v253\n"
        "v_add_f32 v254, v254, v253\n"
        "v_mfma_f32_16x16x32_bf16 v[92:95], a[168:171], a[84:87], 0\n"
        "v_mul_f32 v254, 0xbc2aaaab, v254\n"
        "v_add_f32 v252, v252, v33\n"
        "v_add_f32 v50, v50, v254\n"
        "v_mfma_f32_16x16x32_bf16 v[92:95], a[172:175], a[88:91], v[92:95]\n"
        "v_add_f32 v51, v51, v254\n"
        "v_add_f32 v52, v52, v254\n"
        "v_add_f32 v53, v53, v254\n"
        "v_mfma_f32_16x16x32_bf16 v[92:95], a[176:179], a[92:95], v[92:95]\n"
        "v_add_f32 v58, v58, v254\n"
        "v_add_f32 v59, v59, v254\n"
        "v_add_f32 v60, v60, v254\n"
        "v_mfma_f32_16x16x32_bf16 v[102:105], a[188:191], a[92:95], 0\n"
        "v_add_f32 v61, v61, v254\n"
        "v_add_f32 v70, v70, v254\n"
        "v_add_f32 v71, v71, v254\n"
        "v_mfma_f32_16x16x32_bf16 v[102:105], a[184:187], a[88:91], v[102:105]\n"
        "v_add_f32 v72, v72, v254\n"
        "v_add_f32 v73, v73, v254\n"
        "v_add_f32 v66, v66, v254\n"
        "v_mfma_f32_16x16x32_bf16 v[102:105], a[180:183], a[84:87], v[102:105]\n"
        "buffer_load_dwordx4 v[194:197], v36, s[36:39], 0 offen offset:16\n"
        "v_add_f32 v67, v67, v254\n"
        "v_add_f32 v68, v68, v254\n"
        "v_mfma_f32_16x16x32_bf16 v[106:109], a[192:195], a[84:87], 0\n"
        "s_barrier\n"
        "v_add_f32 v69, v69, v254\n"
        "v_add_f32 v78, v78, v254\n"
        "v_add_f32 v79, v79, v254\n"
        "v_mfma_f32_16x16x32_bf16 v[106:109], a[196:199], a[88:91], v[106:109]\n"
        "s_mov_b32 s78, 0xf0000f\n"
        "v_add_f32 v80, v80, v254\n"
        "v_add_f32 v81, v81, v254\n"
        "v_add_f32 v74, v74, v254\n"
        "v_mfma_f32_16x16x32_bf16 v[106:109], a[200:203], a[92:95], v[106:109]\n"
        "s_mov_b32 s79, 0xf0000f00\n"
        "v_add_f32 v75, v75, v254\n"
        "v_add_f32 v76, v76, v254\n"
        "v_add_f32 v77, v77, v254\n"
        "v_mfma_f32_16x16x32_bf16 v[110:113], a[212:215], a[92:95], 0\n"
        "s_mov_b32 s76, 0x11111111\n"
        "v_cvt_pk_bf16_f32 v54, v50, v51\n"
        "v_cvt_pk_bf16_f32 v55, v52, v53\n"
        "v_cvt_pk_bf16_f32 v56, v58, v59\n"
        "v_mfma_f32_16x16x32_bf16 v[110:113], a[208:211], a[88:91], v[110:113]\n"
        "s_mov_b32 s77, 0x11111111\n"
        "v_cvt_pk_bf16_f32 v57, v60, v61\n"
        "v_cvt_pk_bf16_f32 v62, v70, v71\n"
        "v_cvt_pk_bf16_f32 v63, v72, v73\n"
        "v_mfma_f32_16x16x32_bf16 v[110:113], a[204:207], a[84:87], v[110:113]\n"
        "s_mov_b32 s56, 0x22222222\n"
        "ds_write_b128 v39, v[54:57]\n"
        "v_cvt_pk_bf16_f32 v64, v66, v67\n"
        "v_mfma_f32_16x16x32_bf16 v[58:61], a[144:147], a[96:99], 0\n"
        "s_mov_b32 s57, 0x22222222\n"
        "v_cvt_pk_bf16_f32 v65, v68, v69\n"
        "v_cvt_pk_bf16_f32 v50, v78, v79\n"
        "v_cvt_pk_bf16_f32 v51, v80, v81\n"
        "v_mfma_f32_16x16x32_bf16 v[58:61], a[148:151], a[100:103], v[58:61]\n"
        "s_mov_b32 s58, 0x44444444\n"
        "ds_write_b128 v39, v[62:65] offset:1024\n"
        "v_cvt_pk_bf16_f32 v52, v74, v75\n"
        "v_mfma_f32_16x16x32_bf16 v[58:61], a[152:155], a[104:107], v[58:61]\n"
        "s_mov_b32 s59, 0x44444444\n"
        "buffer_load_dwordx4 v[200:203], v36, s[36:39], 0 offen offset:32\n"
        "v_cvt_pk_bf16_f32 v53, v76, v77\n"
        "v_add_f32 v252, v252, v46\n"
        "v_mfma_f32_16x16x32_bf16 v[66:69], a[164:167], a[104:107], 0\n"
        "s_mov_b32 s60, 0x88888888\n"
        "ds_write_b128 v39, v[50:53] offset:2048\n"
        "v_add_f32 v252, v252, v47\n"
        "v_mfma_f32_16x16x32_bf16 v[66:69], a[160:163], a[100:103], v[66:69]\n"
        "s_mov_b32 s61, 0x88888888\n"
        "v_add_f32 v252, v252, v48\n"
        "v_add_f32 v252, v252, v49\n"
        "v_add_f32 v252, v252, v92\n"
        "v_mfma_f32_16x16x32_bf16 v[66:69], a[156:159], a[96:99], v[66:69]\n"
        "s_mov_b32 s62, 0xffff\n"
        "v_add_f32 v252, v252, v93\n"
        "v_add_f32 v252, v252, v94\n"
        "v_add_f32 v252, v252, v95\n"
        "v_mfma_f32_16x16x32_bf16 v[70:73], a[168:171], a[96:99], 0\n"
        "s_mov_b32 s63, 0x0\n"
        "v_add_f32 v252, v252, v102\n"
        "v_add_f32 v252, v252, v103\n"
        "v_add_f32 v252, v252, v104\n"
        "v_mfma_f32_16x16x32_bf16 v[70:73], a[172:175], a[100:103], v[70:73]\n"
        "s_mov_b32 s64, 0xffff0000\n"
        "v_add_f32 v252, v252, v105\n"
        "v_add_f32 v252, v252, v106\n"
        "v_add_f32 v252, v252, v107\n"
        "v_mfma_f32_16x16x32_bf16 v[70:73], a[176:179], a[104:107], v[70:73]\n"
        "s_mov_b32 s65, 0x0\n"
        "v_add_f32 v252, v252, v108\n"
        "v_add_f32 v252, v252, v109\n"
        "v_add_f32 v252, v252, v110\n"
        "v_mfma_f32_16x16x32_bf16 v[78:81], a[188:191], a[104:107], 0\n"
        "s_mov_b32 s66, 0x0\n"
        "v_add_f32 v252, v252, v111\n"
        "v_add_f32 v252, v252, v112\n"
        "v_add_f32 v252, v252, v113\n"
        "v_mfma_f32_16x16x32_bf16 v[78:81], a[184:187], a[100:103], v[78:81]\n"
        "s_mov_b32 s67, 0xffff\n"
        "v_mov_b32 v254, v252\n"
        "v_add_f32 v253, v58, v59\n"
        "v_add_f32 v253, v253, v60\n"
        "v_mfma_f32_16x16x32_bf16 v[78:81], a[180:183], a[96:99], v[78:81]\n"
        "s_mov_b32 s68, 0x0\n"
        "buffer_load_dwordx4 v[208:211], v37, s[36:39], 0 offen\n"
        "v_add_f32 v253, v253, v61\n"
        "v_add_f32 v253, v253, v66\n"
        "v_mfma_f32_16x16x32_bf16 v[118:121], a[192:195], a[96:99], 0\n"
        "s_mov_b32 s69, 0xffff0000\n"
        "v_permlane32_swap_b32 v252, v254\n"
        "v_add_f32 v252, v252, v254\n"
        "v_mfma_f32_16x16x32_bf16 v[118:121], a[196:199], a[100:103], v[118:121]\n"
        "s_mov_b32 s70, 0x0\n"
        "v_mov_b32 v254, v252\n"
        "v_add_f32 v253, v253, v67\n"
        "v_add_f32 v253, v253, v68\n"
        "v_mfma_f32_16x16x32_bf16 v[118:121], a[200:203], a[104:107], v[118:121]\n"
        "s_mov_b32 s71, 0xffffffff\n"
        "v_permlane16_swap_b32 v252, v254\n"
        "v_add_f32 v252, v252, v254\n"
        "v_mfma_f32_16x16x32_bf16 v[114:117], a[212:215], a[104:107], 0\n"
        "s_mov_b32 s72, 0x0\n"
        "v_mul_f32 v252, 0xbc2aaaab, v252\n"
        "v_add_f32 v30, v30, v252\n"
        "v_add_f32 v31, v31, v252\n"
        "v_mfma_f32_16x16x32_bf16 v[114:117], a[208:211], a[100:103], v[114:117]\n"
        "s_mov_b32 s73, 0x20000\n"
        "v_add_f32 v32, v32, v252\n"
        "v_add_f32 v33, v33, v252\n"
        "v_add_f32 v46, v46, v252\n"
        "v_mfma_f32_16x16x32_bf16 v[114:117], a[204:207], a[96:99], v[114:117]\n"
        "s_mov_b64 vcc, s[78:79]\n"
        "v_add_f32 v47, v47, v252\n"
        "v_add_f32 v48, v48, v252\n"
        "v_add_f32 v49, v49, v252\n"
        "v_mfma_f32_16x16x32_bf16 v[74:77], a[144:147], a[108:111], 0\n"
        "s_mov_b32 s80, 0x3c2aaaab\n"
        "v_add_f32 v92, v92, v252\n"
        "v_add_f32 v93, v93, v252\n"
        "v_add_f32 v94, v94, v252\n"
        "v_mfma_f32_16x16x32_bf16 v[74:77], a[148:151], a[112:115], v[74:77]\n"
        "s_mov_b32 s81, 0x3727c5ac\n"
        "v_add_f32 v95, v95, v252\n"
        "v_add_f32 v102, v102, v252\n"
        "v_add_f32 v103, v103, v252\n"
        "v_mfma_f32_16x16x32_bf16 v[74:77], a[152:155], a[116:119], v[74:77]\n"
        "s_mov_b32 s82, 0x43800000\n"
        "buffer_load_dwordx4 v[214:217], v37, s[36:39], 0 offen offset:16\n"
        "v_add_f32 v104, v104, v252\n"
        "v_add_f32 v105, v105, v252\n"
        "v_mfma_f32_16x16x32_bf16 v[82:85], a[164:167], a[116:119], 0\n"
        "s_mov_b32 s84, 0x80000000\n"
        "v_add_f32 v106, v106, v252\n"
        "v_add_f32 v107, v107, v252\n"
        "v_add_f32 v108, v108, v252\n"
        "v_mfma_f32_16x16x32_bf16 v[82:85], a[160:163], a[112:115], v[82:85]\n"
        "s_sub_u32 s85, s48, 1\n"
        "v_add_f32 v109, v109, v252\n"
        "v_add_f32 v110, v110, v252\n"
        "v_add_f32 v111, v111, v252\n"
        "v_mfma_f32_16x16x32_bf16 v[82:85], a[156:159], a[108:111], v[82:85]\n"
        "s_mov_b32 s86, 0x80000000\n"
        "v_add_f32 v112, v112, v252\n"
        "v_add_f32 v113, v113, v252\n"
        "v_cvt_pk_bf16_f32 v86, v30, v31\n"
        "v_mfma_f32_16x16x32_bf16 v[98:101], a[168:171], a[108:111], 0\n"
        "s_mov_b32 s89, 0x80000000\n"
        "v_cvt_pk_bf16_f32 v87, v32, v33\n"
        "v_cvt_pk_bf16_f32 v88, v46, v47\n"
        "v_cvt_pk_bf16_f32 v89, v48, v49\n"
        "v_mfma_f32_16x16x32_bf16 v[98:101], a[172:175], a[112:115], v[98:101]\n"
        "s_mov_b32 s29, 1\n"
        "ds_write_b128 v40, v[86:89]\n"
        "v_cvt_pk_bf16_f32 v90, v92, v93\n"
        "v_mfma_f32_16x16x32_bf16 v[98:101], a[176:179], a[116:119], v[98:101]\n"
        "s_mov_b32 s31, 0x400000\n"
        "v_cvt_pk_bf16_f32 v91, v94, v95\n"
        "v_cvt_pk_bf16_f32 v92, v102, v103\n"
        "v_cvt_pk_bf16_f32 v93, v104, v105\n"
        "v_mfma_f32_16x16x32_bf16 v[94:97], a[188:191], a[116:119], 0\n"
        "s_mov_b32 s87, s49\n"
        "v_cvt_pk_bf16_f32 v46, v106, v107\n"
        "v_cvt_pk_bf16_f32 v47, v108, v109\n"
        "v_cvt_pk_bf16_f32 v48, v110, v111\n"
        "v_mfma_f32_16x16x32_bf16 v[94:97], a[184:187], a[112:115], v[94:97]\n"
        "s_mov_b32 s88, s50\n"
        "v_cvt_pk_bf16_f32 v49, v112, v113\n"
        "v_add_f32 v253, v253, v69\n"
        "v_add_f32 v253, v253, v70\n"
        "v_mfma_f32_16x16x32_bf16 v[94:97], a[180:183], a[108:111], v[94:97]\n"
        "s_mov_b32 s54, 0x05040302\n"
        "buffer_load_dwordx4 v[220:223], v37, s[36:39], 0 offen offset:32\n"
        "v_add_f32 v253, v253, v71\n"
        "v_add_f32 v253, v253, v72\n"
        "v_mfma_f32_16x16x32_bf16 v[102:105], a[192:195], a[108:111], 0\n"
        "s_mov_b32 s55, 0x07060302\n"
        "v_add_f32 v253, v253, v73\n"
        "v_add_f32 v253, v253, v78\n"
        "v_add_f32 v253, v253, v79\n"
        "v_mfma_f32_16x16x32_bf16 v[102:105], a[196:199], a[112:115], v[102:105]\n"
        "s_mov_b64 s[44:45], s[40:41]\n"
        "v_add_f32 v253, v253, v80\n"
        "v_add_f32 v253, v253, v81\n"
        "v_add_f32 v253, v253, v118\n"
        "v_mfma_f32_16x16x32_bf16 v[102:105], a[200:203], a[116:119], v[102:105]\n"
        "s_mov_b32 s46, 0\n"
        "v_add_f32 v253, v253, v119\n"
        "v_add_f32 v253, v253, v120\n"
        "v_add_f32 v253, v253, v121\n"
        "v_mfma_f32_16x16x32_bf16 v[30:33], a[212:215], a[116:119], 0\n"
        "s_mov_b32 s47, s43\n"
        "v_add_f32 v253, v253, v114\n"
        "v_add_f32 v253, v253, v115\n"
        "v_add_f32 v253, v253, v116\n"
        "v_mfma_f32_16x16x32_bf16 v[30:33], a[208:211], a[112:115], v[30:33]\n"
        "v_add_f32 v253, v253, v117\n"
        "v_mov_b32 v254, v253\n"
        "v_add_f32 v38, v74, v75\n"
        "v_mfma_f32_16x16x32_bf16 v[30:33], a[204:207], a[108:111], v[30:33]\n"
        "v_permlane32_swap_b32 v253, v254\n"
        "v_add_f32 v253, v253, v254\n"
        "v_mfma_f32_16x16x32_bf16 v[106:109], a[144:147], a[120:123], 0\n"
        "v_mov_b32 v254, v253\n"
        "v_add_f32 v38, v38, v76\n"
        "v_add_f32 v38, v38, v77\n"
        "v_mfma_f32_16x16x32_bf16 v[106:109], a[148:151], a[124:127], v[106:109]\n"
        "s_add_u32 m0, s52, 0\n"
        "v_permlane16_swap_b32 v253, v254\n"
        "v_add_f32 v253, v253, v254\n"
        "v_mfma_f32_16x16x32_bf16 v[106:109], a[152:155], a[128:131], v[106:109]\n"
        "buffer_load_dwordx4 v0, s[36:39], s49 offen lds\n"
        "v_mul_f32 v253, 0xbc2aaaab, v253\n"
        "v_add_f32 v58, v58, v253\n"
        "v_mfma_f32_16x16x32_bf16 v[34:37], a[164:167], a[128:131], 0\n"
        "v_add_f32 v59, v59, v253\n"
        "v_add_f32 v60, v60, v253\n"
        "v_add_f32 v61, v61, v253\n"
        "v_mfma_f32_16x16x32_bf16 v[34:37], a[160:163], a[124:127], v[34:37]\n"
        "v_add_f32 v66, v66, v253\n"
        "v_add_f32 v67, v67, v253\n"
        "v_add_f32 v68, v68, v253\n"
        "v_mfma_f32_16x16x32_bf16 v[34:37], a[156:159], a[120:123], v[34:37]\n"
        "v_add_f32 v69, v69, v253\n"
        "v_add_f32 v70, v70, v253\n"
        "v_add_f32 v71, v71, v253\n"
        "v_mfma_f32_16x16x32_bf16 v[130:133], a[168:171], a[120:123], 0\n"
        "v_add_f32 v72, v72, v253\n"
        "v_add_f32 v73, v73, v253\n"
        "v_add_f32 v78, v78, v253\n"
        "v_mfma_f32_16x16x32_bf16 v[130:133], a[172:175], a[124:127], v[130:133]\n"
        "v_add_f32 v79, v79, v253\n"
        "v_add_f32 v80, v80, v253\n"
        "v_add_f32 v81, v81, v253\n"
        "v_mfma_f32_16x16x32_bf16 v[130:133], a[176:179], a[128:131], v[130:133]\n"
        "buffer_load_dwordx4 v1, s[36:39], s49 offen offset:1024 lds\n"
        "v_add_f32 v118, v118, v253\n"
        "v_add_f32 v119, v119, v253\n"
        "v_mfma_f32_16x16x32_bf16 v[122:125], a[188:191], a[128:131], 0\n"
        "v_add_f32 v120, v120, v253\n"
        "v_add_f32 v121, v121, v253\n"
        "v_add_f32 v114, v114, v253\n"
        "v_mfma_f32_16x16x32_bf16 v[122:125], a[184:187], a[124:127], v[122:125]\n"
        "v_add_f32 v115, v115, v253\n"
        "v_add_f32 v116, v116, v253\n"
        "v_add_f32 v117, v117, v253\n"
        "v_mfma_f32_16x16x32_bf16 v[122:125], a[180:183], a[120:123], v[122:125]\n"
        "v_cvt_pk_bf16_f32 v110, v58, v59\n"
        "v_cvt_pk_bf16_f32 v111, v60, v61\n"
        "v_cvt_pk_bf16_f32 v112, v66, v67\n"
        "v_mfma_f32_16x16x32_bf16 v[126:129], a[192:195], a[120:123], 0\n"
        "v_cvt_pk_bf16_f32 v113, v68, v69\n"
        "v_cvt_pk_bf16_f32 v58, v70, v71\n"
        "v_cvt_pk_bf16_f32 v59, v72, v73\n"
        "v_mfma_f32_16x16x32_bf16 v[126:129], a[196:199], a[124:127], v[126:129]\n"
        "v_cvt_pk_bf16_f32 v60, v78, v79\n"
        "v_cvt_pk_bf16_f32 v61, v80, v81\n"
        "v_cvt_pk_bf16_f32 v66, v118, v119\n"
        "v_mfma_f32_16x16x32_bf16 v[126:129], a[200:203], a[128:131], v[126:129]\n"
        "buffer_load_dwordx4 v2, s[36:39], s49 offen offset:2048 lds\n"
        "v_cvt_pk_bf16_f32 v67, v120, v121\n"
        "v_cvt_pk_bf16_f32 v68, v114, v115\n"
        "v_mfma_f32_16x16x32_bf16 v[118:121], a[212:215], a[128:131], 0\n"
        "v_cvt_pk_bf16_f32 v69, v116, v117\n"
        "v_add_f32 v38, v38, v82\n"
        "v_add_f32 v38, v38, v83\n"
        "v_mfma_f32_16x16x32_bf16 v[118:121], a[208:211], a[124:127], v[118:121]\n"
        "v_add_f32 v38, v38, v84\n"
        "v_add_f32 v38, v38, v85\n"
        "v_add_f32 v38, v38, v98\n"
        "v_mfma_f32_16x16x32_bf16 v[118:121], a[204:207], a[120:123], v[118:121]\n"
        "v_add_f32 v38, v38, v99\n"
        "v_add_f32 v38, v38, v100\n"
        "v_add_f32 v38, v38, v101\n"
        "v_mfma_f32_16x16x32_bf16 v[70:73], a[144:147], a[132:135], 0\n"
        "v_add_f32 v38, v38, v94\n"
        "v_add_f32 v38, v38, v95\n"
        "v_add_f32 v38, v38, v96\n"
        "v_mfma_f32_16x16x32_bf16 v[70:73], a[148:151], a[136:139], v[70:73]\n"
        "v_add_f32 v38, v38, v97\n"
        "v_add_f32 v38, v38, v102\n"
        "v_add_f32 v38, v38, v103\n"
        "v_mfma_f32_16x16x32_bf16 v[70:73], a[152:155], a[140:143], v[70:73]\n"
        "buffer_load_dwordx4 v3, s[36:39], s49 offen offset:3072 lds\n"
        "v_add_f32 v38, v38, v104\n"
        "v_add_f32 v38, v38, v105\n"
        "v_mfma_f32_16x16x32_bf16 v[114:117], a[164:167], a[140:143], 0\n"
        "v_add_f32 v38, v38, v30\n"
        "v_add_f32 v38, v38, v31\n"
        "v_add_f32 v38, v38, v32\n"
        "v_mfma_f32_16x16x32_bf16 v[114:117], a[160:163], a[136:139], v[114:117]\n"
        "v_add_f32 v38, v38, v33\n"
        "v_mov_b32 v39, v38\n"
        "v_add_f32 v150, v106, v107\n"
        "v_mfma_f32_16x16x32_bf16 v[114:117], a[156:159], a[132:135], v[114:117]\n"
        "v_permlane32_swap_b32 v38, v39\n"
        "v_add_f32 v38, v38, v39\n"
        "v_mfma_f32_16x16x32_bf16 v[138:141], a[168:171], a[132:135], 0\n"
        "v_mov_b32 v39, v38\n"
        "v_add_f32 v150, v150, v108\n"
        "v_add_f32 v150, v150, v109\n"
        "v_mfma_f32_16x16x32_bf16 v[138:141], a[172:175], a[136:139], v[138:141]\n"
        "s_add_u32 m0, s52, 4096\n"
        "v_permlane16_swap_b32 v38, v39\n"
        "v_add_f32 v38, v38, v39\n"
        "v_mfma_f32_16x16x32_bf16 v[138:141], a[176:179], a[140:143], v[138:141]\n"
        "buffer_load_dwordx4 v4, s[36:39], s49 offen lds\n"
        "v_mul_f32 v38, 0xbc2aaaab, v38\n"
        "v_add_f32 v150, v150, v34\n"
        "v_mfma_f32_16x16x32_bf16 v[78:81], a[188:191], a[140:143], 0\n"
        "v_add_f32 v150, v150, v35\n"
        "v_add_f32 v150, v150, v36\n"
        "v_add_f32 v150, v150, v37\n"
        "v_mfma_f32_16x16x32_bf16 v[78:81], a[184:187], a[136:139], v[78:81]\n"
        "v_add_f32 v150, v150, v130\n"
        "v_add_f32 v150, v150, v131\n"
        "v_add_f32 v150, v150, v132\n"
        "v_mfma_f32_16x16x32_bf16 v[78:81], a[180:183], a[132:135], v[78:81]\n"
        "v_add_f32 v150, v150, v133\n"
        "v_add_f32 v150, v150, v122\n"
        "v_add_f32 v150, v150, v123\n"
        "v_mfma_f32_16x16x32_bf16 v[142:145], a[192:195], a[132:135], 0\n"
        "v_add_f32 v150, v150, v124\n"
        "v_add_f32 v150, v150, v125\n"
        "v_add_f32 v150, v150, v126\n"
        "v_mfma_f32_16x16x32_bf16 v[142:145], a[196:199], a[136:139], v[142:145]\n"
        "v_add_f32 v150, v150, v127\n"
        "v_add_f32 v150, v150, v128\n"
        "v_add_f32 v150, v150, v129\n"
        "v_mfma_f32_16x16x32_bf16 v[142:145], a[200:203], a[140:143], v[142:145]\n"
        "buffer_load_dwordx4 v5, s[36:39], s49 offen offset:1024 lds\n"
        "v_add_f32 v150, v150, v118\n"
        "v_add_f32 v150, v150, v119\n"
        "v_mfma_f32_16x16x32_bf16 v[134:137], a[212:215], a[140:143], 0\n"
        "v_add_f32 v150, v150, v120\n"
        "v_add_f32 v150, v150, v121\n"
        "v_mov_b32 v151, v150\n"
        "v_mfma_f32_16x16x32_bf16 v[134:137], a[208:211], a[136:139], v[134:137]\n"
        "ds_write_b128 v40, v[90:93] offset:1024\n"
        "v_add_f32 v152, v70, v71\n"
        "v_mfma_f32_16x16x32_bf16 v[134:137], a[204:207], a[132:135], v[134:137]\n"
        "ds_write_b128 v40, v[46:49] offset:2048\n"
        "v_add_f32 v152, v152, v72\n"
        "v_pk_add_f32 v[74:75], v[74:75], v[38:39] op_sel_hi:[1,0]\n"
        "v_cvt_pk_bf16_f32 v146, v74, v75\n"
        "v_pk_add_f32 v[76:77], v[76:77], v[38:39] op_sel_hi:[1,0]\n"
        "v_cvt_pk_bf16_f32 v147, v76, v77\n"
        "v_pk_add_f32 v[82:83], v[82:83], v[38:39] op_sel_hi:[1,0]\n"
        "v_cvt_pk_bf16_f32 v148, v82, v83\n"
        "v_pk_add_f32 v[84:85], v[84:85], v[38:39] op_sel_hi:[1,0]\n"
        "v_cvt_pk_bf16_f32 v149, v84, v85\n"
        "v_pk_add_f32 v[98:99], v[98:99], v[38:39] op_sel_hi:[1,0]\n"
        "v_cvt_pk_bf16_f32 v82, v98, v99\n"
        "v_pk_add_f32 v[100:101], v[100:101], v[38:39] op_sel_hi:[1,0]\n"
        "v_cvt_pk_bf16_f32 v83, v100, v101\n"
        "v_pk_add_f32 v[94:95], v[94:95], v[38:39] op_sel_hi:[1,0]\n"
        "v_cvt_pk_bf16_f32 v84, v94, v95\n"
        "v_pk_add_f32 v[96:97], v[96:97], v[38:39] op_sel_hi:[1,0]\n"
        "v_cvt_pk_bf16_f32 v85, v96, v97\n"
        "v_pk_add_f32 v[102:103], v[102:103], v[38:39] op_sel_hi:[1,0]\n"
        "v_cvt_pk_bf16_f32 v74, v102, v103\n"
        "v_pk_add_f32 v[104:105], v[104:105], v[38:39] op_sel_hi:[1,0]\n"
        "v_cvt_pk_bf16_f32 v75, v104, v105\n"
        "v_pk_add_f32 v[30:31], v[30:31], v[38:39] op_sel_hi:[1,0]\n"
        "v_cvt_pk_bf16_f32 v76, v30, v31\n"
        "v_pk_add_f32 v[32:33], v[32:33], v[38:39] op_sel_hi:[1,0]\n"
        "v_cvt_pk_bf16_f32 v77, v32, v33\n"
        "v_permlane32_swap_b32 v150, v151\n"
        "v_add_f32 v150, v150, v151\n"
        "v_mov_b32 v151, v150\n"
        "s_nop 1\n"
        "v_permlane16_swap_b32 v150, v151\n"
        "v_add_f32 v150, v150, v151\n"
        "v_mul_f32 v150, 0xbc2aaaab, v150\n"
        "v_pk_add_f32 v[106:107], v[106:107], v[150:151] op_sel_hi:[1,0]\n"
        "v_cvt_pk_bf16_f32 v94, v106, v107\n"
        "v_pk_add_f32 v[108:109], v[108:109], v[150:151] op_sel_hi:[1,0]\n"
        "v_cvt_pk_bf16_f32 v95, v108, v109\n"
        "v_pk_add_f32 v[34:35], v[34:35], v[150:151] op_sel_hi:[1,0]\n"
        "v_cvt_pk_bf16_f32 v96, v34, v35\n"
        "v_pk_add_f32 v[36:37], v[36:37], v[150:151] op_sel_hi:[1,0]\n"
        "v_cvt_pk_bf16_f32 v97, v36, v37\n"
        "v_pk_add_f32 v[130:131], v[130:131], v[150:151] op_sel_hi:[1,0]\n"
        "v_cvt_pk_bf16_f32 v30, v130, v131\n"
        "v_pk_add_f32 v[132:133], v[132:133], v[150:151] op_sel_hi:[1,0]\n"
        "v_cvt_pk_bf16_f32 v31, v132, v133\n"
        "v_pk_add_f32 v[122:123], v[122:123], v[150:151] op_sel_hi:[1,0]\n"
        "v_cvt_pk_bf16_f32 v32, v122, v123\n"
        "v_pk_add_f32 v[124:125], v[124:125], v[150:151] op_sel_hi:[1,0]\n"
        "v_cvt_pk_bf16_f32 v33, v124, v125\n"
        "v_pk_add_f32 v[126:127], v[126:127], v[150:151] op_sel_hi:[1,0]\n"
        "v_cvt_pk_bf16_f32 v34, v126, v127\n"
        "v_pk_add_f32 v[128:129], v[128:129], v[150:151] op_sel_hi:[1,0]\n"
        "v_cvt_pk_bf16_f32 v35, v128, v129\n"
        "v_pk_add_f32 v[118:119], v[118:119], v[150:151] op_sel_hi:[1,0]\n"
        "v_cvt_pk_bf16_f32 v36, v118, v119\n"
        "v_pk_add_f32 v[120:121], v[120:121], v[150:151] op_sel_hi:[1,0]\n"
        "v_cvt_pk_bf16_f32 v37, v120, v121\n"
        "v_add_f32 v152, v152, v73\n"
        "v_add_f32 v152, v152, v114\n"
        "v_add_f32 v152, v152, v115\n"
        "v_add_f32 v152, v152, v116\n"
        "v_add_f32 v152, v152, v117\n"
        "v_add_f32 v152, v152, v138\n"
        "v_add_f32 v152, v152, v139\n"
        "v_add_f32 v152, v152, v140\n"
        "v_add_f32 v152, v152, v141\n"
        "v_add_f32 v152, v152, v78\n"
        "v_add_f32 v152, v152, v79\n"
        "v_add_f32 v152, v152, v80\n"
        "v_add_f32 v152, v152, v81\n"
        "v_add_f32 v152, v152, v142\n"
        "v_add_f32 v152, v152, v143\n"
        "v_add_f32 v152, v152, v144\n"
        "v_add_f32 v152, v152, v145\n"
        "v_add_f32 v152, v152, v134\n"
        "v_add_f32 v152, v152, v135\n"
        "v_add_f32 v152, v152, v136\n"
        "v_add_f32 v152, v152, v137\n"
        "v_mov_b32 v153, v152\n"
        "s_nop 1\n"
        "v_permlane32_swap_b32 v152, v153\n"
        "v_add_f32 v152, v152, v153\n"
        "v_mov_b32 v153, v152\n"
        "s_nop 1\n"
        "v_permlane16_swap_b32 v152, v153\n"
        "v_add_f32 v152, v152, v153\n"
        "v_mul_f32 v152, 0xbc2aaaab, v152\n"
        "v_pk_add_f32 v[70:71], v[70:71], v[152:153] op_sel_hi:[1,0]\n"
        "v_cvt_pk_bf16_f32 v98, v70, v71\n"
        "v_pk_add_f32 v[72:73], v[72:73], v[152:153] op_sel_hi:[1,0]\n"
        "v_cvt_pk_bf16_f32 v99, v72, v73\n"
        "v_pk_add_f32 v[114:115], v[114:115], v[152:153] op_sel_hi:[1,0]\n"
        "v_cvt_pk_bf16_f32 v100, v114, v115\n"
        "v_pk_add_f32 v[116:117], v[116:117], v[152:153] op_sel_hi:[1,0]\n"
        "v_cvt_pk_bf16_f32 v101, v116, v117\n"
        "v_pk_add_f32 v[138:139], v[138:139], v[152:153] op_sel_hi:[1,0]\n"
        "v_cvt_pk_bf16_f32 v70, v138, v139\n"
        "v_pk_add_f32 v[140:141], v[140:141], v[152:153] op_sel_hi:[1,0]\n"
        "v_cvt_pk_bf16_f32 v71, v140, v141\n"
        "v_pk_add_f32 v[78:79], v[78:79], v[152:153] op_sel_hi:[1,0]\n"
        "v_cvt_pk_bf16_f32 v72, v78, v79\n"
        "v_pk_add_f32 v[80:81], v[80:81], v[152:153] op_sel_hi:[1,0]\n"
        "v_cvt_pk_bf16_f32 v73, v80, v81\n"
        "v_pk_add_f32 v[142:143], v[142:143], v[152:153] op_sel_hi:[1,0]\n"
        "v_cvt_pk_bf16_f32 v78, v142, v143\n"
        "v_pk_add_f32 v[144:145], v[144:145], v[152:153] op_sel_hi:[1,0]\n"
        "v_cvt_pk_bf16_f32 v79, v144, v145\n"
        "v_pk_add_f32 v[134:135], v[134:135], v[152:153] op_sel_hi:[1,0]\n"
        "v_cvt_pk_bf16_f32 v80, v134, v135\n"
        "v_pk_add_f32 v[136:137], v[136:137], v[152:153] op_sel_hi:[1,0]\n"
        "v_cvt_pk_bf16_f32 v81, v136, v137\n"
        "s_nop 1\n"
        "v_mfma_f32_16x16x32_bf16 v[102:105], v[54:57], v[54:57], 0\n"
        "ds_write_b128 v41, v[110:113]\n"
        "v_mfma_f32_16x16x32_bf16 v[102:105], v[62:65], v[62:65], v[102:105]\n"
        "ds_write_b128 v41, v[58:61] offset:1024\n"
        "v_mfma_f32_16x16x32_bf16 v[102:105], v[50:53], v[50:53], v[102:105]\n"
        "buffer_load_dwordx4 v6, s[36:39], s49 offen offset:2048 lds\n"
        "v_mfma_f32_16x16x32_bf16 v[102:105], v[86:89], v[86:89], v[102:105]\n"
        "ds_write_b128 v41, v[66:69] offset:2048\n"
        "v_mfma_f32_16x16x32_bf16 v[102:105], v[90:93], v[90:93], v[102:105]\n"
        "ds_write_b128 v42, v[146:149]\n"
        "v_mfma_f32_16x16x32_bf16 v[102:105], v[46:49], v[46:49], v[102:105]\n"
        "ds_write_b128 v42, v[82:85] offset:1024\n"
        "v_mfma_f32_16x16x32_bf16 v[102:105], v[110:113], v[110:113], v[102:105]\n"
        "ds_write_b128 v42, v[74:77] offset:2048\n"
        "v_mfma_f32_16x16x32_bf16 v[102:105], v[58:61], v[58:61], v[102:105]\n"
        "ds_write_b128 v43, v[94:97]\n"
        "v_mfma_f32_16x16x32_bf16 v[102:105], v[66:69], v[66:69], v[102:105]\n"
        "buffer_load_dwordx4 v7, s[36:39], s49 offen offset:3072 lds\n"
        "v_mfma_f32_16x16x32_bf16 v[102:105], v[146:149], v[146:149], v[102:105]\n"
        "ds_write_b128 v43, v[30:33] offset:1024\n"
        "v_mfma_f32_16x16x32_bf16 v[102:105], v[82:85], v[82:85], v[102:105]\n"
        "ds_write_b128 v43, v[34:37] offset:2048\n"
        "v_mfma_f32_16x16x32_bf16 v[102:105], v[74:77], v[74:77], v[102:105]\n"
        "ds_write_b128 v44, v[98:101]\n"
        "v_mfma_f32_16x16x32_bf16 v[102:105], v[94:97], v[94:97], v[102:105]\n"
        "ds_write_b128 v44, v[70:73] offset:1024\n"
        "v_mfma_f32_16x16x32_bf16 v[102:105], v[30:33], v[30:33], v[102:105]\n"
        "s_add_u32 m0, s52, 8192\n"
        "ds_write_b128 v44, v[78:81] offset:2048\n"
        "v_mfma_f32_16x16x32_bf16 v[102:105], v[34:37], v[34:37], v[102:105]\n"
        "buffer_load_dwordx4 v8, s[36:39], s49 offen lds\n"
        "v_mfma_f32_16x16x32_bf16 v[102:105], v[98:101], v[98:101], v[102:105]\n"
        "v_mfma_f32_16x16x32_bf16 v[102:105], v[70:73], v[70:73], v[102:105]\n"
        "v_mfma_f32_16x16x32_bf16 v[102:105], v[78:81], v[78:81], v[102:105]\n"
        "s_nop 2\n"
        "s_waitcnt vmcnt(9)\n"
        "s_nop 3\n"
        "v_max3_f32 v254, v102, v103, v104\n"
        "v_max_f32 v254, v254, v105\n"
        "v_cmp_nge_f32_e64 s[74:75], s24, v254\n"
        "v_mov_b32 v254, 160384\n"
        "s_nop 0\n"
        "v_cndmask_b32_e64 v254, v255, v254, s[74:75]\n"
        "v_mov_b32 v255, 0\n"
        "ds_write_b32 v254, v255\n"
        "s_waitcnt lgkmcnt(0)\n"
        "s_barrier\n"
        "FE_%=:\n"
        "v_mov_b32 v28, s80\n"
        "ds_read_b128 v[96:99], v26\n"
        "ds_read_b128 v[68:71], v26 offset:64\n"
        "ds_read_b128 v[100:103], v26 offset:128\n"
        "v_xor_b32 v255, 64, v226\n"
        "ds_read_b128 a[0:3], v226\n"
        "ds_read_b128 a[4:7], v226 offset:1024\n"
        "ds_read_b128 a[8:11], v226 offset:2048\n"
        "ds_read_b128 a[12:15], v226 offset:3072\n"
        "ds_read_b128 a[16:19], v226 offset:4096\n"
        "ds_read_b128 a[20:23], v226 offset:5120\n"
        "ds_read_b128 a[24:27], v226 offset:6144\n"
        "ds_read_b128 a[28:31], v226 offset:7168\n"
        "ds_read_b128 a[32:35], v226 offset:8192\n"
        "v_mfma_f32_16x16x32_bf16 v[40:43], v[188:191], v[188:191], 0\n"
        "ds_read_b128 a[36:39], v226 offset:9216\n"
        "v_add_u32 v157, 73792, v226\n"
        "v_mfma_f32_16x16x32_bf16 v[40:43], v[194:197], v[194:197], v[40:43]\n"
        "ds_read_b128 a[40:43], v226 offset:10240\n"
        "v_lshrrev_b32 v225, 2, v226\n"
        "v_mfma_f32_16x16x32_bf16 v[40:43], v[200:203], v[200:203], v[40:43]\n"
        "ds_read_b128 a[44:47], v226 offset:11264\n"
        "v_mov_b32 v253, 160384\n"
        "v_mfma_f32_16x16x32_bf16 v[34:37], v[208:211], v[208:211], 0\n"
        "ds_read_b128 a[48:51], v226 offset:12288\n"
        "v_and_b32 v225, 128, v225\n"
        "v_mfma_f32_16x16x32_bf16 v[34:37], v[214:217], v[214:217], v[34:37]\n"
        "s_nop 0\n"
        "ds_read_b128 a[52:55], v226 offset:13312\n"
        "v_cndmask_b32_e64 v252, v40, v41, s[56:57]\n"
        "v_mfma_f32_16x16x32_bf16 v[34:37], v[220:223], v[220:223], v[34:37]\n"
        "s_waitcnt lgkmcnt(14)\n"
        "ds_read_b128 a[56:59], v226 offset:14336\n"
        "v_cndmask_b32_e64 v252, v252, v42, s[58:59]\n"
        "v_mfma_f32_16x16x32_bf16 v[104:107], v[100:103], v[220:223], 0\n"
        "ds_read_b128 a[60:63], v226 offset:15360\n"
        "v_cndmask_b32_e64 v252, v252, v43, s[60:61]\n"
        "v_mfma_f32_16x16x32_bf16 v[104:107], v[68:71], v[214:217], v[104:107]\n"
        "ds_read_b128 a[64:67], v226 offset:16384\n"
        "v_cndmask_b32_e64 v254, v34, v35, s[56:57]\n"
        "v_mfma_f32_16x16x32_bf16 v[104:107], v[96:99], v[208:211], v[104:107]\n"
        "s_waitcnt lgkmcnt(14)\n"
        "ds_read_b128 a[68:71], v226 offset:17408\n"
        "v_cndmask_b32_e64 v254, v254, v36, s[58:59]\n"
        "v_mfma_f32_16x16x32_bf16 v[114:117], v[96:99], v[188:191], 0\n"
        "s_waitcnt lgkmcnt(12)\n"
        "ds_read_b128 a[72:75], v226 offset:18432\n"
        "v_cndmask_b32_e64 v254, v254, v37, s[60:61]\n"
        "v_mfma_f32_16x16x32_bf16 v[114:117], v[68:71], v[194:197], v[114:117]\n"
        "s_waitcnt lgkmcnt(10)\n"
        "ds_read_b128 a[76:79], v226 offset:19456\n"
        "v_cndmask_b32_dpp v252, v254, v252, vcc row_ror:4 row_mask:0xf bank_mask:0xf\n"
        "v_mfma_f32_16x16x32_bf16 v[114:117], v[100:103], v[200:203], v[114:117]\n"
        "ds_read_b128 a[80:83], v226 offset:20480\n"
        "v_xor_b32 v225, v225, v226\n"
        "v_mfma_f32_16x16x32_bf16 v[34:37], a[8:11], v[200:203], 0\n"
        "ds_read_b128 a[84:87], v226 offset:21504\n"
        "v_add_u32 v225, 139648, v225\n"
        "v_mfma_f32_16x16x32_bf16 v[34:37], a[4:7], v[194:197], v[34:37]\n"
        "s_waitcnt lgkmcnt(10)\n"
        "ds_read_b128 a[88:91], v226 offset:22528\n"
        "v_cndmask_b32_dpp v151, v104, v114, vcc row_ror:4 row_mask:0xf bank_mask:0xf\n"
        "v_mfma_f32_16x16x32_bf16 v[34:37], a[0:3], v[188:191], v[34:37]\n"
        "ds_read_b128 a[92:95], v226 offset:23552\n"
        "v_mul_f32 v251, s80, v151\n"
        "v_mfma_f32_16x16x32_bf16 v[106:109], a[12:15], v[188:191], 0\n"
        "ds_read_b128 a[96:99], v255 offset:24576\n"
        "v_fma_f32 v251, -v251, v151, v252\n"
        "v_mfma_f32_16x16x32_bf16 v[106:109], a[16:19], v[194:197], v[106:109]\n"
        "s_waitcnt lgkmcnt(10)\n"
        "ds_read_b128 a[100:103], v255 offset:25600\n"
        "v_cvt_pk_bf16_f32 v40, v34, v35\n"
        "v_mfma_f32_16x16x32_bf16 v[106:109], a[20:23], v[200:203], v[106:109]\n"
        "ds_read_b128 a[104:107], v255 offset:26624\n"
        "v_cvt_pk_bf16_f32 v41, v36, v37\n"
        "v_mfma_f32_16x16x32_bf16 v[110:113], a[32:35], v[200:203], 0\n"
        "ds_read_b128 a[108:111], v255 offset:27648\n"
        "v_max_f32 v251, 0, v251\n"
        "v_mfma_f32_16x16x32_bf16 v[110:113], a[28:31], v[194:197], v[110:113]\n"
        "s_waitcnt lgkmcnt(10)\n"
        "ds_read_b128 a[112:115], v255 offset:28672\n"
        "v_cvt_pk_bf16_f32 v42, v106, v107\n"
        "v_mfma_f32_16x16x32_bf16 v[110:113], a[24:27], v[188:191], v[110:113]\n"
        "ds_read_b128 a[116:119], v255 offset:29696\n"
        "v_cvt_pk_bf16_f32 v43, v108, v109\n"
        "v_mfma_f32_16x16x32_bf16 v[116:119], a[36:39], v[188:191], 0\n"
        "ds_read_b128 a[120:123], v226 offset:30720\n"
        "v_fmaak_f32 v251, v28, v251, 0x3727c5ac\n"
        "v_mfma_f32_16x16x32_bf16 v[116:119], a[40:43], v[194:197], v[116:119]\n"
        "s_nop 0\n"
        "ds_read_b128 a[124:127], v226 offset:31744\n"
        "v_cvt_pk_bf16_f32 v34, v110, v111\n"
        "v_mfma_f32_16x16x32_bf16 v[116:119], a[44:47], v[200:203], v[116:119]\n"
        "ds_read_b128 a[128:131], v226 offset:32768\n"
        "v_cvt_pk_bf16_f32 v35, v112, v113\n"
        "v_mfma_f32_16x16x32_bf16 v[106:109], a[56:59], v[200:203], 0\n"
        "ds_read_b128 a[132:135], v226 offset:33792\n"
        "v_cndmask_b32_dpp v156, v105, v115, vcc row_ror:4 row_mask:0xf bank_mask:0xf\n"
        "v_mfma_f32_16x16x32_bf16 v[106:109], a[52:55], v[194:197], v[106:109]\n"
        "s_nop 0\n"
        "ds_read_b128 a[136:139], v226 offset:34816\n"
        "v_cvt_pk_bf16_f32 v36, v116, v117\n"
        "v_mfma_f32_16x16x32_bf16 v[106:109], a[48:51], v[188:191], v[106:109]\n"
        "s_nop 0\n"
        "ds_read_b128 a[140:143], v226 offset:35840\n"
        "v_cvt_pk_bf16_f32 v37, v118, v119\n"
        "v_mfma_f32_16x16x32_bf16 v[110:113], a[60:63], v[188:191], 0\n"
        "s_nop 0\n"
        "v_rsq_f32 v254, v251\n"
        "v_mfma_f32_16x16x32_bf16 v[110:113], a[64:67], v[194:197], v[110:113]\n"
        "s_nop 0\n"
        "v_cvt_pk_bf16_f32 v104, v106, v107\n"
        "v_cvt_pk_bf16_f32 v105, v108, v109\n"
        "v_mfma_f32_16x16x32_bf16 v[110:113], a[68:71], v[200:203], v[110:113]\n"
        "s_nop 1\n"
        "v_mfma_f32_16x16x32_bf16 v[114:117], a[8:11], v[220:223], 0\n"
        "s_nop 0\n"
        "v_mov_b32 v255, 0x3c003c00\n"
        "v_cndmask_b32_e64 v56, 0, v255, s[76:77]\n"
        "v_mfma_f32_16x16x32_bf16 v[114:117], a[4:7], v[214:217], v[114:117]\n"
        "s_nop 0\n"
        "v_cvt_pk_bf16_f32 v106, v110, v111\n"
        "v_cvt_pk_bf16_f32 v107, v112, v113\n"
        "v_mfma_f32_16x16x32_bf16 v[114:117], a[0:3], v[208:211], v[114:117]\n"
        "s_nop 0\n"
        "v_cndmask_b32_e64 v57, 0, v255, s[56:57]\n"
        "v_cndmask_b32_e64 v58, 0, v255, s[58:59]\n"
        "v_mfma_f32_16x16x32_bf16 v[108:111], v[188:191], v[40:43], 0\n"
        "s_nop 0\n"
        "v_cndmask_b32_e64 v59, 0, v255, s[60:61]\n"
        "v_mfma_f32_16x16x32_bf16 v[108:111], v[194:197], v[34:37], v[108:111]\n"
        "s_nop 0\n"
        "v_cvt_pk_bf16_f32 v112, v114, v115\n"
        "v_cvt_pk_bf16_f32 v113, v116, v117\n"
        "v_mfma_f32_16x16x32_bf16 v[108:111], v[200:203], v[104:107], v[108:111]\n"
        "v_mfma_f32_16x16x32_bf16 v[34:37], a[20:23], v[220:223], 0\n"
        "v_mfma_f32_16x16x32_bf16 v[34:37], a[16:19], v[214:217], v[34:37]\n"
        "s_waitcnt lgkmcnt(0)\n"
        "v_mfma_f32_16x16x32_bf16 v[34:37], a[12:15], v[208:211], v[34:37]\n"
        "s_barrier\n"
        "ds_read_b128 a[144:147], v225\n"
        "v_mfma_f32_16x16x32_bf16 v[40:43], a[24:27], v[208:211], 0\n"
        "s_nop 0\n"
        "ds_read_b128 a[148:151], v225 offset:1024\n"
        "s_nop 0\n"
        "v_mfma_f32_16x16x32_bf16 v[40:43], a[28:31], v[214:217], v[40:43]\n"
        "s_nop 0\n"
        "v_cvt_pk_bf16_f32 v114, v34, v35\n"
        "v_cvt_pk_bf16_f32 v115, v36, v37\n"
        "v_mfma_f32_16x16x32_bf16 v[40:43], a[32:35], v[220:223], v[40:43]\n"
        "s_nop 0\n"
        "v_mfma_f32_16x16x32_bf16 v[34:37], a[44:47], v[220:223], 0\n"
        "s_nop 2\n"
        "v_mfma_f32_16x16x32_bf16 v[34:37], a[40:43], v[214:217], v[34:37]\n"
        "s_nop 0\n"
        "ds_read_b128 a[152:155], v225 offset:2048\n"
        "v_cvt_pk_bf16_f32 v116, v40, v41\n"
        "v_mfma_f32_16x16x32_bf16 v[34:37], a[36:39], v[208:211], v[34:37]\n"
        "s_nop 0\n"
        "v_cvt_pk_bf16_f32 v117, v42, v43\n"
        "v_mfma_f32_16x16x32_bf16 v[104:107], a[48:51], v[208:211], 0\n"
        "s_nop 2\n"
        "v_mfma_f32_16x16x32_bf16 v[104:107], a[52:55], v[214:217], v[104:107]\n"
        "s_nop 0\n"
        "v_cvt_pk_bf16_f32 v118, v34, v35\n"
        "v_cvt_pk_bf16_f32 v119, v36, v37\n"
        "v_mfma_f32_16x16x32_bf16 v[104:107], a[56:59], v[220:223], v[104:107]\n"
        "s_nop 0\n"
        "ds_read_b128 a[156:159], v225 offset:3072\n"
        "v_mfma_f32_16x16x32_bf16 v[34:37], a[68:71], v[220:223], 0\n"
        "s_nop 2\n"
        "v_mfma_f32_16x16x32_bf16 v[34:37], a[64:67], v[214:217], v[34:37]\n"
        "s_nop 0\n"
        "v_cvt_pk_bf16_f32 v40, v104, v105\n"
        "v_cvt_pk_bf16_f32 v41, v106, v107\n"
        "v_mfma_f32_16x16x32_bf16 v[34:37], a[60:63], v[208:211], v[34:37]\n"
        "s_nop 0\n"
        "ds_read_b128 a[160:163], v225 offset:4096\n"
        "v_mfma_f32_16x16x32_bf16 v[120:123], v[188:191], a[72:75], 0\n"
        "s_nop 2\n"
        "v_mfma_f32_16x16x32_bf16 v[120:123], v[194:197], a[76:79], v[120:123]\n"
        "s_nop 0\n"
        "v_cvt_pk_bf16_f32 v42, v34, v35\n"
        "v_cvt_pk_bf16_f32 v43, v36, v37\n"
        "v_mfma_f32_16x16x32_bf16 v[120:123], v[200:203], a[80:83], v[120:123]\n"
        "s_nop 1\n"
        "v_mfma_f32_16x16x32_bf16 v[104:107], v[200:203], a[92:95], 0\n"
        "s_nop 0\n"
        "ds_read_b128 a[164:167], v225 offset:5120\n"
        "s_nop 0\n"
        "v_mfma_f32_16x16x32_bf16 v[104:107], v[194:197], a[88:91], v[104:107]\n"
        "s_nop 0\n"
        "v_cvt_pk_bf16_f32 v30, v120, v121\n"
        "v_cvt_pk_bf16_f32 v31, v122, v123\n"
        "v_mfma_f32_16x16x32_bf16 v[104:107], v[188:191], a[84:87], v[104:107]\n"
        "s_nop 0\n"
        "v_mfma_f32_16x16x32_bf16 v[120:123], v[208:211], v[112:115], 0\n"
        "s_nop 2\n"
        "v_mfma_f32_16x16x32_bf16 v[120:123], v[214:217], v[116:119], v[120:123]\n"
        "s_nop 0\n"
        "ds_read_b128 a[168:171], v225 offset:6144\n"
        "v_cvt_pk_bf16_f32 v34, v104, v105\n"
        "v_mfma_f32_16x16x32_bf16 v[120:123], v[220:223], v[40:43], v[120:123]\n"
        "s_waitcnt vmcnt(0)\n"
        "v_cvt_pk_bf16_f32 v35, v106, v107\n"
        "v_mfma_f32_16x16x32_bf16 v[142:145], v[200:203], a[104:107], 0\n"
        "s_nop 0\n"
        "ds_read_b128 v[146:149], v9\n"
        "v_mfma_f32_16x16x32_bf16 v[142:145], v[194:197], a[100:103], v[142:145]\n"
        "s_nop 0\n"
        "ds_read_b128 v[152:155], v10\n"
        "v_cndmask_b32_dpp v252, v120, v108, vcc row_ror:4 row_mask:0xf bank_mask:0xf\n"
        "v_mfma_f32_16x16x32_bf16 v[142:145], v[188:191], a[96:99], v[142:145]\n"
        "ds_read_b128 v[158:161], v11\n"
        "v_cndmask_b32_dpp v137, v121, v109, vcc row_ror:4 row_mask:0xf bank_mask:0xf\n"
        "v_mfma_f32_16x16x32_bf16 v[162:165], v[188:191], a[108:111], 0\n"
        "ds_read_b128 v[126:129], v9 offset:3072\n"
        "v_cndmask_b32_dpp v150, v122, v110, vcc row_ror:4 row_mask:0xf bank_mask:0xf\n"
        "v_mfma_f32_16x16x32_bf16 v[162:165], v[194:197], a[112:115], v[162:165]\n"
        "ds_read_b128 v[132:135], v10 offset:3072\n"
        "v_cndmask_b32_dpp v226, v123, v111, vcc row_ror:4 row_mask:0xf bank_mask:0xf\n"
        "v_mfma_f32_16x16x32_bf16 v[162:165], v[200:203], a[116:119], v[162:165]\n"
        "ds_read_b128 v[138:141], v11 offset:3072\n"
        "v_mul_f32_dpp v252, v254, v252 quad_perm:[0,0,0,0] row_mask:0xf bank_mask:0xf\n"
        "v_mfma_f32_16x16x32_bf16 v[122:125], v[200:203], a[128:131], 0\n"
        "ds_read_b128 v[106:109], v9 offset:6144\n"
        "v_mul_f32_dpp v137, v254, v137 quad_perm:[1,1,1,1] row_mask:0xf bank_mask:0xf\n"
        "v_mfma_f32_16x16x32_bf16 v[122:125], v[194:197], a[124:127], v[122:125]\n"
        "ds_read_b128 v[112:115], v10 offset:6144\n"
        "v_mul_f32_dpp v150, v254, v150 quad_perm:[2,2,2,2] row_mask:0xf bank_mask:0xf\n"
        "v_mfma_f32_16x16x32_bf16 v[122:125], v[188:191], a[120:123], v[122:125]\n"
        "ds_read_b128 v[118:121], v11 offset:6144\n"
        "v_mul_f32_dpp v226, v254, v226 quad_perm:[3,3,3,3] row_mask:0xf bank_mask:0xf\n"
        "v_mfma_f32_16x16x32_bf16 v[170:173], v[188:191], a[132:135], 0\n"
        "ds_read_b128 a[172:175], v225 offset:7168\n"
        "v_max3_f32 v255, v252, v137, v150\n"
        "v_mfma_f32_16x16x32_bf16 v[170:173], v[194:197], a[136:139], v[170:173]\n"
        "s_waitcnt lgkmcnt(7)\n"
        "ds_read_b128 a[176:179], v225 offset:8192\n"
        "v_max_f32 v255, v255, v226\n"
        "v_mfma_f32_16x16x32_bf16 v[170:173], v[200:203], a[140:143], v[170:173]\n"
        "ds_read_b128 a[180:183], v225 offset:9216\n"
        "v_mul_f32 v255, v255, v254\n"
        "v_mfma_f32_16x16x32_bf16 v[234:237], v[146:149], v[146:149], 0\n"
        "s_mov_b32 s26, s89\n"
        "s_mov_b32 s30, s29\n"
        "s_add_u32 s88, s88, 32\n"
        "v_mfma_f32_16x16x32_bf16 v[234:237], v[152:155], v[152:155], v[234:237]\n"
        "s_add_u32 s89, s86, s51\n"
        "ds_read_b128 a[184:187], v225 offset:10240\n"
        "v_fma_f32 v252, v252, v254, -v255\n"
        "v_mfma_f32_16x16x32_bf16 v[234:237], v[158:161], v[158:161], v[234:237]\n"
        "s_add_u32 s29, s29, 1\n"
        "v_fma_f32 v137, v137, v254, -v255\n"
        "v_fma_f32 v150, v150, v254, -v255\n"
        "v_mfma_f32_16x16x32_bf16 v[246:249], v[100:103], v[158:161], 0\n"
        "s_cmp_ge_u32 s88, 73\n"
        "v_fma_f32 v226, v226, v254, -v255\n"
        "v_cvt_pk_bf16_f32 v42, v142, v143\n"
        "v_mfma_f32_16x16x32_bf16 v[246:249], v[68:71], v[152:155], v[246:249]\n"
        "s_waitcnt lgkmcnt(7)\n"
        "ds_read_b128 a[188:191], v225 offset:11264\n"
        "v_cvt_pk_bf16_f32 v43, v144, v145\n"
        "v_mfma_f32_16x16x32_bf16 v[246:249], v[96:99], v[146:149], v[246:249]\n"
        "s_mov_b32 s86, s87\n"
        "v_exp_f32 v252, v252\n"
        "v_mfma_f32_16x16x32_bf16 v[174:177], a[0:3], v[146:149], 0\n"
        "s_cselect_b32 s92, 588672, 0\n"
        "v_exp_f32 v137, v137\n"
        "v_mfma_f32_16x16x32_bf16 v[174:177], a[4:7], v[152:155], v[174:177]\n"
        "s_cselect_b32 s94, 73, 0\n"
        "s_add_u32 s87, s87, 43008\n"
        "v_add_f32 v255, v252, v137\n"
        "v_mfma_f32_16x16x32_bf16 v[174:177], a[8:11], v[158:161], v[174:177]\n"
        "s_sub_u32 s88, s88, s94\n"
        "ds_read_b128 a[192:195], v225 offset:12288\n"
        "v_cvt_pk_bf16_f32 v46, v162, v163\n"
        "v_mfma_f32_16x16x32_bf16 v[248:251], a[20:23], v[158:161], 0\n"
        "s_add_u32 s87, s87, s92\n"
        "ds_read_b64 v[32:33], v27\n"
        "ds_read_b64 v[44:45], v27\n"
        "v_mfma_f32_16x16x32_bf16 v[248:251], a[16:19], v[152:155], v[248:251]\n"
        "s_cmp_gt_i32 s85, 0\n"
        "ds_read_b64 v[38:39], v27\n"
        "v_cvt_pk_bf16_f32 v47, v164, v165\n"
        "v_mfma_f32_16x16x32_bf16 v[248:251], a[12:15], v[146:149], v[248:251]\n"
        "s_cselect_b32 s94, s87, s84\n"
        "ds_read_b32 v253, v253\n"
        "v_cvt_pk_bf16_f32 v36, v122, v123\n"
        "v_mfma_f32_16x16x32_bf16 v[238:241], a[24:27], v[146:149], 0\n"
        "s_add_u32 m0, s52, 0\n"
        "v_exp_f32 v150, v150\n"
        "v_mfma_f32_16x16x32_bf16 v[238:241], a[28:31], v[152:155], v[238:241]\n"
        "s_waitcnt lgkmcnt(10)\n"
        "v_exp_f32 v226, v226\n"
        "v_mfma_f32_16x16x32_bf16 v[238:241], a[32:35], v[158:161], v[238:241]\n"
        "s_sub_u32 s85, s85, 1\n"
        "buffer_load_dwordx4 v0, s[36:39], s94 offen lds\n"
        "v_add_f32 v245, v150, v226\n"
        "v_mfma_f32_16x16x32_bf16 v[166:169], a[44:47], v[158:161], 0\n"
        "v_add_f32 v255, v255, v245\n"
        "v_cvt_pk_bf16_f32 v37, v124, v125\n"
        "v_mfma_f32_16x16x32_bf16 v[166:169], a[40:43], v[152:155], v[166:169]\n"
        "v_rcp_f32 v255, v255\n"
        "v_mfma_f32_16x16x32_bf16 v[166:169], a[36:39], v[146:149], v[166:169]\n"
        "v_mul_f32 v252, v252, v255\n"
        "v_mul_f32 v137, v137, v255\n"
        "v_mfma_f32_16x16x32_bf16 v[122:125], a[48:51], v[146:149], 0\n"
        "s_waitcnt lgkmcnt(1)\n"
        "v_mul_f32 v150, v150, v255\n"
        "v_mul_f32 v226, v226, v255\n"
        "v_mfma_f32_16x16x32_bf16 v[122:125], a[52:55], v[152:155], v[122:125]\n"
        "s_waitcnt lgkmcnt(0)\n"
        "buffer_load_dwordx4 v1, s[36:39], s94 offen offset:1024 lds\n"
        "v_mul_f32_dpp v252, v254, v252 quad_perm:[0,0,0,0] row_mask:0xf bank_mask:0xf\n"
        "v_mfma_f32_16x16x32_bf16 v[122:125], a[56:59], v[158:161], v[122:125]\n"
        "v_mul_f32_dpp v137, v254, v137 quad_perm:[1,1,1,1] row_mask:0xf bank_mask:0xf\n"
        "v_mul_f32_dpp v150, v254, v150 quad_perm:[2,2,2,2] row_mask:0xf bank_mask:0xf\n"
        "v_mfma_f32_16x16x32_bf16 v[242:245], a[68:71], v[158:161], 0\n"
        "v_mul_f32_dpp v226, v254, v226 quad_perm:[3,3,3,3] row_mask:0xf bank_mask:0xf\n"
        "v_cvt_pk_bf16_f32 v255, v252, v137\n"
        "v_mfma_f32_16x16x32_bf16 v[242:245], a[64:67], v[152:155], v[242:245]\n"
        "v_cvt_pk_bf16_f32 v224, v150, v226\n"
        "v_and_b32 v186, v25, v255\n"
        "v_mfma_f32_16x16x32_bf16 v[242:245], a[60:63], v[146:149], v[242:245]\n"
        "v_and_b32 v187, v25, v224\n"
        "v_mov_b64 v[192:193], v[186:187]\n"
        "s_nop 0\n"
        "v_mfma_f32_16x16x32_bf16 v[162:165], v[30:33], v[186:189], 0\n"
        "buffer_load_dwordx4 v2, s[36:39], s94 offen offset:2048 lds\n"
        "v_mov_b64 v[198:199], v[186:187]\n"
        "v_mfma_f32_16x16x32_bf16 v[142:145], v[32:35], v[190:193], 0\n"
        "ds_read_b128 a[196:199], v225 offset:13312\n"
        "v_readfirstlane_b32 s83, v253\n"
        "v_mfma_f32_16x16x32_bf16 v[182:185], v[208:211], a[72:75], 0\n"
        "ds_read_b128 a[200:203], v225 offset:14336\n"
        "v_mov_b64 v[204:205], v[186:187]\n"
        "v_mfma_f32_16x16x32_bf16 v[182:185], v[214:217], a[76:79], v[182:185]\n"
        "ds_read_b128 a[204:207], v225 offset:15360\n"
        "v_mul_f32_dpp v136, v156, v252 quad_perm:[0,0,0,0] row_mask:0xf bank_mask:0xf\n"
        "v_mfma_f32_16x16x32_bf16 v[182:185], v[220:223], a[80:83], v[182:185]\n"
        "ds_read_b128 a[208:211], v225 offset:16384\n"
        "v_and_b32_dpp v206, v255, v25 row_ror:12 row_mask:0xf bank_mask:0xf\n"
        "v_mfma_f32_16x16x32_bf16 v[252:255], v[220:223], a[92:95], 0\n"
        "buffer_load_dwordx4 v3, s[36:39], s94 offen offset:3072 lds\n"
        "v_and_b32_dpp v207, v224, v25 row_ror:12 row_mask:0xf bank_mask:0xf\n"
        "v_mfma_f32_16x16x32_bf16 v[252:255], v[214:217], a[88:91], v[252:255]\n"
        "ds_read_b128 a[212:215], v225 offset:17408\n"
        "v_cvt_pk_bf16_f32 v40, v170, v171\n"
        "v_mfma_f32_16x16x32_bf16 v[252:255], v[208:211], a[84:87], v[252:255]\n"
        "ds_read_b128 a[216:219], v157\n"
        "v_cvt_pk_bf16_f32 v41, v172, v173\n"
        "v_mfma_f32_16x16x32_bf16 v[188:191], v[42:45], v[192:195], 0\n"
        "s_add_u32 m0, s52, 4096\n"
        "v_mov_b64 v[212:213], v[206:207]\n"
        "v_mov_b64 v[218:219], v[206:207]\n"
        "v_mfma_f32_16x16x32_bf16 v[170:173], v[44:47], v[196:199], 0\n"
        "v_mov_b64 v[224:225], v[206:207]\n"
        "v_cvt_pk_bf16_f32 v186, v162, v163\n"
        "v_mfma_f32_16x16x32_bf16 v[178:181], v[208:211], a[96:99], 0\n"
        "buffer_load_dwordx4 v4, s[36:39], s94 offen lds\n"
        "v_cvt_pk_bf16_f32 v162, v164, v165\n"
        "v_mfma_f32_16x16x32_bf16 v[178:181], v[214:217], a[100:103], v[178:181]\n"
        "ds_read_b128 a[220:223], v157 offset:1024\n"
        "v_cvt_pk_bf16_f32 v34, v142, v143\n"
        "v_mfma_f32_16x16x32_bf16 v[178:181], v[220:223], a[104:107], v[178:181]\n"
        "v_cvt_pk_bf16_f32 v187, v144, v145\n"
        "v_cvt_pk_bf16_f32 v163, v188, v189\n"
        "v_mfma_f32_16x16x32_bf16 v[142:145], v[220:223], a[116:119], 0\n"
        "v_cvt_pk_bf16_f32 v35, v190, v191\n"
        "v_cvt_pk_bf16_f32 v188, v170, v171\n"
        "v_mfma_f32_16x16x32_bf16 v[142:145], v[214:217], a[112:115], v[142:145]\n"
        "ds_read_b128 a[224:227], v157 offset:2048\n"
        "v_cvt_pk_bf16_f32 v164, v172, v173\n"
        "v_mfma_f32_16x16x32_bf16 v[142:145], v[208:211], a[108:111], v[142:145]\n"
        "buffer_load_dwordx4 v5, s[36:39], s94 offen offset:1024 lds\n"
        "v_fmac_f32_dpp v136, v156, v137 quad_perm:[1,1,1,1] row_mask:0xf bank_mask:0xf\n"
        "v_mfma_f32_16x16x32_bf16 v[170:173], v[36:39], v[198:201], 0\n"
        "v_cvt_pk_bf16_f32 v190, v174, v175\n"
        "v_cvt_pk_bf16_f32 v191, v176, v177\n"
        "v_mfma_f32_16x16x32_bf16 v[196:199], v[38:41], v[202:205], 0\n"
        "s_nop 0\n"
        "ds_read_b128 a[228:231], v157 offset:3088\n"
        "v_cvt_pk_bf16_f32 v192, v248, v249\n"
        "v_mfma_f32_16x16x32_bf16 v[174:177], v[208:211], a[120:123], 0\n"
        "s_nop 0\n"
        "v_cvt_pk_bf16_f32 v36, v170, v171\n"
        "v_cvt_pk_bf16_f32 v189, v172, v173\n"
        "v_mfma_f32_16x16x32_bf16 v[174:177], v[214:217], a[124:127], v[174:177]\n"
        "ds_write_b128 v12, v[186:189]\n"
        "v_mfma_f32_16x16x32_bf16 v[174:177], v[220:223], a[128:131], v[174:177]\n"
        "buffer_load_dwordx4 v6, s[36:39], s94 offen offset:2048 lds\n"
        "v_cvt_pk_bf16_f32 v165, v196, v197\n"
        "v_mfma_f32_16x16x32_bf16 v[194:197], v[220:223], a[140:143], 0\n"
        "ds_write_b128 v12, v[162:165] offset:1024\n"
        "v_mfma_f32_16x16x32_bf16 v[194:197], v[214:217], a[136:139], v[194:197]\n"
        "ds_read_b128 a[232:235], v157 offset:4112\n"
        "v_cvt_pk_bf16_f32 v37, v198, v199\n"
        "v_mfma_f32_16x16x32_bf16 v[194:197], v[208:211], a[132:135], v[194:197]\n"
        "ds_write_b128 v12, v[34:37] offset:2048\n"
        "v_mfma_f32_16x16x32_bf16 v[170:173], v[186:189], v[186:189], 0\n"
        "v_cvt_pk_bf16_f32 v193, v250, v251\n"
        "v_cvt_pk_bf16_f32 v186, v238, v239\n"
        "v_mfma_f32_16x16x32_bf16 v[170:173], v[162:165], v[162:165], v[170:173]\n"
        "buffer_load_dwordx4 v7, s[36:39], s94 offen offset:3072 lds\n"
        "v_cvt_pk_bf16_f32 v187, v240, v241\n"
        "v_mfma_f32_16x16x32_bf16 v[170:173], v[34:37], v[34:37], v[170:173]\n"
        "ds_read_b128 a[236:239], v157 offset:5136\n"
        "v_cvt_pk_bf16_f32 v188, v166, v167\n"
        "v_mfma_f32_16x16x32_bf16 v[162:165], v[126:129], v[126:129], 0\n"
        "v_cvt_pk_bf16_f32 v30, v182, v183\n"
        "v_cvt_pk_bf16_f32 v31, v184, v185\n"
        "v_mfma_f32_16x16x32_bf16 v[162:165], v[132:135], v[132:135], v[162:165]\n"
        "s_add_u32 m0, s52, 8192\n"
        "ds_read_b128 a[240:243], v157 offset:6176\n"
        "v_cvt_pk_bf16_f32 v34, v252, v253\n"
        "v_mfma_f32_16x16x32_bf16 v[162:165], v[138:141], v[138:141], v[162:165]\n"
        "v_cvt_pk_bf16_f32 v35, v254, v255\n"
        "v_cvt_pk_bf16_f32 v42, v178, v179\n"
        "v_mfma_f32_16x16x32_bf16 v[182:185], v[100:103], v[138:141], 0\n"
        "buffer_load_dwordx4 v8, s[36:39], s94 offen lds\n"
        "v_cvt_pk_bf16_f32 v43, v180, v181\n"
        "v_mfma_f32_16x16x32_bf16 v[182:185], v[68:71], v[132:135], v[182:185]\n"
        "v_cvt_pk_bf16_f32 v46, v142, v143\n"
        "v_cvt_pk_bf16_f32 v47, v144, v145\n"
        "v_mfma_f32_16x16x32_bf16 v[182:185], v[96:99], v[126:129], v[182:185]\n"
        "ds_read_b128 a[244:247], v157 offset:7200\n"
        "v_cvt_pk_bf16_f32 v36, v174, v175\n"
        "v_mfma_f32_16x16x32_bf16 v[178:181], v[30:33], v[206:209], 0\n"
        "v_cvt_pk_bf16_f32 v37, v176, v177\n"
        "v_cvt_pk_bf16_f32 v40, v194, v195\n"
        "v_mfma_f32_16x16x32_bf16 v[198:201], v[32:35], v[210:213], 0\n"
        "v_cvt_pk_bf16_f32 v41, v196, v197\n"
        "v_cvt_pk_bf16_f32 v189, v168, v169\n"
        "v_mfma_f32_16x16x32_bf16 v[166:169], v[42:45], v[212:215], 0\n"
        "v_cvt_pk_bf16_f32 v142, v122, v123\n"
        "v_cvt_pk_bf16_f32 v143, v124, v125\n"
        "v_mfma_f32_16x16x32_bf16 v[202:205], v[44:47], v[216:219], 0\n"
        "ds_read_b128 a[248:251], v157 offset:8224\n"
        "v_cvt_pk_bf16_f32 v144, v242, v243\n"
        "v_mfma_f32_16x16x32_bf16 v[122:125], v[36:39], v[218:221], 0\n"
        "v_cvt_pk_bf16_f32 v145, v244, v245\n"
        "v_cvt_pk_bf16_f32 v184, v178, v179\n"
        "v_mfma_f32_16x16x32_bf16 v[174:177], v[38:41], v[222:225], 0\n"
        "v_cvt_pk_bf16_f32 v40, v180, v181\n"
        "v_cvt_pk_bf16_f32 v194, v198, v199\n"
        "v_mfma_f32_16x16x32_bf16 v[178:181], v[146:149], v[190:193], 0\n"
        "ds_read_b128 a[252:255], v157 offset:9264\n"
        "v_cvt_pk_bf16_f32 v185, v200, v201\n"
        "v_mfma_f32_16x16x32_bf16 v[178:181], v[152:155], v[186:189], v[178:181]\n"
        "v_cvt_pk_bf16_f32 v41, v166, v167\n"
        "v_cvt_pk_bf16_f32 v195, v168, v169\n"
        "v_mfma_f32_16x16x32_bf16 v[178:181], v[158:161], v[142:145], v[178:181]\n"
        "v_cvt_pk_bf16_f32 v186, v202, v203\n"
        "v_cvt_pk_bf16_f32 v42, v204, v205\n"
        "v_mfma_f32_16x16x32_bf16 v[34:37], a[8:11], v[138:141], 0\n"
        "v_cvt_pk_bf16_f32 v187, v124, v125\n"
        "v_cvt_pk_bf16_f32 v43, v174, v175\n"
        "v_mfma_f32_16x16x32_bf16 v[34:37], a[4:7], v[132:135], v[34:37]\n"
        "ds_write_b128 v12, v[184:187] offset:3072\n"
        "v_mfma_f32_16x16x32_bf16 v[34:37], a[0:3], v[126:129], v[34:37]\n"
        "ds_write_b128 v12, v[40:43] offset:4096\n"
        "v_mfma_f32_16x16x32_bf16 v[142:145], v[184:187], v[184:187], 0\n"
        "v_cvt_pk_bf16_f32 v196, v122, v123\n"
        "v_cvt_pk_bf16_f32 v197, v176, v177\n"
        "v_mfma_f32_16x16x32_bf16 v[142:145], v[40:43], v[40:43], v[142:145]\n"
        "v_fmac_f32_dpp v136, v156, v150 quad_perm:[2,2,2,2] row_mask:0xf bank_mask:0xf\n"
        "v_cndmask_b32_e64 v254, v170, v171, s[56:57]\n"
        "v_mfma_f32_16x16x32_bf16 v[142:145], v[194:197], v[194:197], v[142:145]\n"
        "ds_write_b128 v12, v[194:197] offset:5120\n"
        "v_mfma_f32_16x16x32_bf16 v[40:43], a[20:23], v[138:141], 0\n"
        "v_fmac_f32_dpp v136, v156, v226 quad_perm:[3,3,3,3] row_mask:0xf bank_mask:0xf\n"
        "v_add_f32 v136, v136, v151\n"
        "v_mfma_f32_16x16x32_bf16 v[40:43], a[16:19], v[132:135], v[40:43]\n"
        "v_mul_f32 v253, s80, v136\n"
        "v_cndmask_b32_e64 v254, v254, v172, s[58:59]\n"
        "v_mfma_f32_16x16x32_bf16 v[40:43], a[12:15], v[126:129], v[40:43]\n"
        "ds_read_b128 v[92:95], v157 offset:10288\n"
        "v_cndmask_b32_e64 v254, v254, v173, s[60:61]\n"
        "v_mfma_f32_16x16x32_bf16 v[122:125], a[24:27], v[126:129], 0\n"
        "ds_read_b128 v[52:55], v157 offset:11312\n"
        "v_cvt_pk_bf16_f32 v184, v34, v35\n"
        "v_mfma_f32_16x16x32_bf16 v[122:125], a[28:31], v[132:135], v[122:125]\n"
        "ds_read_b128 v[64:67], v157 offset:12352\n"
        "v_cndmask_b32_e64 v255, v142, v143, s[56:57]\n"
        "v_mfma_f32_16x16x32_bf16 v[122:125], a[32:35], v[138:141], v[122:125]\n"
        "v_cndmask_b32_e64 v255, v255, v144, s[58:59]\n"
        "v_cndmask_b32_e64 v255, v255, v145, s[60:61]\n"
        "v_mfma_f32_16x16x32_bf16 v[142:145], a[44:47], v[138:141], 0\n"
        "ds_read_b128 v[84:87], v157 offset:13376\n"
        "v_cndmask_b32_dpp v254, v255, v254, vcc row_ror:4 row_mask:0xf bank_mask:0xf\n"
        "v_mfma_f32_16x16x32_bf16 v[142:145], a[40:43], v[132:135], v[142:145]\n"
        "v_fma_f32 v253, -v253, v136, v254\n"
        "v_max_f32 v253, 0, v253\n"
        "v_mfma_f32_16x16x32_bf16 v[142:145], a[36:39], v[126:129], v[142:145]\n"
        "v_sub_f32 v254, 0x47c00000, v254\n"
        "v_fmaak_f32 v253, v28, v253, 0x3727c5ac\n"
        "v_mfma_f32_16x16x32_bf16 v[174:177], a[48:51], v[126:129], 0\n"
        "v_bfi_b32 v253, s84, v254, v253\n"
        "v_cvt_pk_bf16_f32 v185, v36, v37\n"
        "v_mfma_f32_16x16x32_bf16 v[174:177], a[52:55], v[132:135], v[174:177]\n"
        "ds_read_b128 v[76:79], v157 offset:14400\n"
        "v_cvt_pk_bf16_f32 v186, v40, v41\n"
        "v_mfma_f32_16x16x32_bf16 v[174:177], a[56:59], v[138:141], v[174:177]\n"
        "v_rsq_f32 v255, v253\n"
        "v_mfma_f32_16x16x32_bf16 v[34:37], a[68:71], v[138:141], 0\n"
        "v_cvt_f16_f32 v255, v255\n"
        "v_cvt_pk_bf16_f32 v187, v42, v43\n"
        "v_mfma_f32_16x16x32_bf16 v[34:37], a[64:67], v[132:135], v[34:37]\n"
        "ds_write_b16 v18, v255\n"
        "v_mfma_f32_16x16x32_bf16 v[34:37], a[60:63], v[126:129], v[34:37]\n"
        "ds_write_b16 v19, v255\n"
        "v_mfma_f32_16x16x32_bf16 v[40:43], v[106:109], v[106:109], 0\n"
        "ds_read_b128 v[60:63], v157 offset:15440\n"
        "v_cvt_pk_bf16_f32 v170, v122, v123\n"
        "v_mfma_f32_16x16x32_bf16 v[40:43], v[112:115], v[112:115], v[40:43]\n"
        "v_cvt_pk_bf16_f32 v171, v124, v125\n"
        "v_cvt_pk_bf16_f32 v172, v142, v143\n"
        "v_mfma_f32_16x16x32_bf16 v[40:43], v[118:121], v[118:121], v[40:43]\n"
        "ds_read_b128 v[72:75], v157 offset:16464\n"
        "v_cvt_pk_bf16_f32 v173, v144, v145\n"
        "v_mfma_f32_16x16x32_bf16 v[142:145], v[100:103], v[118:121], 0\n"
        "v_cvt_pk_bf16_f32 v166, v174, v175\n"
        "v_cvt_pk_bf16_f32 v167, v176, v177\n"
        "v_mfma_f32_16x16x32_bf16 v[142:145], v[68:71], v[112:115], v[142:145]\n"
        "v_cvt_pk_bf16_f32 v168, v34, v35\n"
        "v_cvt_pk_bf16_f32 v169, v36, v37\n"
        "v_mfma_f32_16x16x32_bf16 v[142:145], v[96:99], v[106:109], v[142:145]\n"
        "v_cndmask_b32_e64 v254, v234, v235, s[56:57]\n"
        "v_cndmask_b32_e64 v254, v254, v236, s[58:59]\n"
        "v_mfma_f32_16x16x32_bf16 v[122:125], v[126:129], v[184:187], 0\n"
        "ds_read_b128 v[48:51], v157 offset:17488\n"
        "v_cndmask_b32_e64 v254, v254, v237, s[60:61]\n"
        "v_mfma_f32_16x16x32_bf16 v[122:125], v[132:135], v[170:173], v[122:125]\n"
        "s_waitcnt lgkmcnt(3)\n"
        "v_cndmask_b32_e64 v253, v162, v163, s[56:57]\n"
        "v_cndmask_b32_e64 v253, v253, v164, s[58:59]\n"
        "v_mfma_f32_16x16x32_bf16 v[122:125], v[138:141], v[166:169], v[122:125]\n"
        "s_barrier\n"
        "v_cndmask_b32_e64 v253, v253, v165, s[60:61]\n"
        "v_cndmask_b32_e64 v255, v40, v41, s[56:57]\n"
        "v_mfma_f32_16x16x32_bf16 v[164:167], a[8:11], v[118:121], 0\n"
        "v_cndmask_b32_e64 v255, v255, v42, s[58:59]\n"
        "v_cndmask_b32_e64 v255, v255, v43, s[60:61]\n"
        "v_mfma_f32_16x16x32_bf16 v[164:167], a[4:7], v[112:115], v[164:167]\n"
        "s_nop 0\n"
        "v_cndmask_b32_dpp v253, v255, v253, vcc row_ror:4 row_mask:0xf bank_mask:0xf\n"
        "v_cndmask_b32_dpp v255, v142, v182, vcc row_ror:4 row_mask:0xf bank_mask:0xf\n"
        "v_mfma_f32_16x16x32_bf16 v[164:167], a[0:3], v[106:109], v[164:167]\n"
        "v_cndmask_b32_dpp v254, v253, v254, vcc row_ror:4 row_mask:0xf bank_mask:0xf\n"
        "v_cndmask_b32_dpp v255, v255, v246, vcc row_ror:4 row_mask:0xf bank_mask:0xf\n"
        "v_mfma_f32_16x16x32_bf16 v[34:37], a[12:15], v[106:109], 0\n"
        "s_nop 0\n"
        "v_mul_f32 v253, s80, v255\n"
        "v_fma_f32 v253, -v253, v255, v254\n"
        "v_mfma_f32_16x16x32_bf16 v[34:37], a[16:19], v[112:115], v[34:37]\n"
        "s_nop 0\n"
        "v_cvt_pk_bf16_f32 v162, v164, v165\n"
        "v_cvt_pk_bf16_f32 v163, v166, v167\n"
        "v_mfma_f32_16x16x32_bf16 v[34:37], a[20:23], v[118:121], v[34:37]\n"
        "s_waitcnt lgkmcnt(0)\n"
        "v_max_f32 v253, 0, v253\n"
        "v_fmaak_f32 v253, v28, v253, 0x3727c5ac\n"
        "v_mfma_f32_16x16x32_bf16 v[40:43], a[32:35], v[118:121], 0\n"
        "s_nop 0\n"
        "v_rsq_f32 v248, v253\n"
        "v_mfma_f32_16x16x32_bf16 v[40:43], a[28:31], v[112:115], v[40:43]\n"
        "s_nop 0\n"
        "v_cvt_pk_bf16_f32 v164, v34, v35\n"
        "v_cvt_pk_bf16_f32 v165, v36, v37\n"
        "v_mfma_f32_16x16x32_bf16 v[40:43], a[24:27], v[106:109], v[40:43]\n"
        "s_nop 0\n"
        "v_cndmask_b32_dpp v254, v143, v183, vcc row_ror:4 row_mask:0xf bank_mask:0xf\n"
        "v_mfma_f32_16x16x32_bf16 v[142:145], a[36:39], v[106:109], 0\n"
        "s_nop 0\n"
        "v_cndmask_b32_dpp v254, v254, v247, vcc row_ror:4 row_mask:0xf bank_mask:0xf\n"
        "s_nop 0\n"
        "v_mfma_f32_16x16x32_bf16 v[142:145], a[40:43], v[112:115], v[142:145]\n"
        "s_nop 0\n"
        "v_cvt_pk_bf16_f32 v34, v40, v41\n"
        "v_cvt_pk_bf16_f32 v35, v42, v43\n"
        "v_mfma_f32_16x16x32_bf16 v[142:145], a[44:47], v[118:121], v[142:145]\n"
        "s_nop 0\n"
        "v_mfma_f32_16x16x32_bf16 v[168:171], a[56:59], v[118:121], 0\n"
        "s_nop 0\n"
        "ds_read_b128 v[198:201], v13\n"
        "ds_read_b128 v[190:193], v13 offset:1024\n"
        "v_mfma_f32_16x16x32_bf16 v[168:171], a[52:55], v[112:115], v[168:171]\n"
        "s_nop 0\n"
        "ds_read_b128 v[206:209], v13 offset:2048\n"
        "v_cvt_pk_bf16_f32 v36, v142, v143\n"
        "v_mfma_f32_16x16x32_bf16 v[168:171], a[48:51], v[106:109], v[168:171]\n"
        "s_nop 0\n"
        "v_cvt_pk_bf16_f32 v37, v144, v145\n"
        "v_mfma_f32_16x16x32_bf16 v[40:43], a[60:63], v[106:109], 0\n"
        "s_nop 2\n"
        "v_mfma_f32_16x16x32_bf16 v[40:43], a[64:67], v[112:115], v[40:43]\n"
        "s_nop 0\n"
        "v_cvt_pk_bf16_f32 v166, v168, v169\n"
        "v_cvt_pk_bf16_f32 v167, v170, v171\n"
        "v_mfma_f32_16x16x32_bf16 v[40:43], a[68:71], v[118:121], v[40:43]\n"
        "s_nop 1\n"
        "v_mfma_f32_16x16x32_bf16 v[142:145], v[158:161], a[80:83], 0\n"
        "s_nop 2\n"
        "v_mfma_f32_16x16x32_bf16 v[142:145], v[152:155], a[76:79], v[142:145]\n"
        "s_nop 0\n"
        "v_cvt_pk_bf16_f32 v168, v40, v41\n"
        "v_cvt_pk_bf16_f32 v169, v42, v43\n"
        "v_mfma_f32_16x16x32_bf16 v[142:145], v[146:149], a[72:75], v[142:145]\n"
        "s_nop 1\n"
        "v_mfma_f32_16x16x32_bf16 v[170:173], v[146:149], a[84:87], 0\n"
        "s_waitcnt lgkmcnt(0)\n"
        "s_nop 1\n"
        "v_mfma_f32_16x16x32_bf16 v[170:173], v[152:155], a[88:91], v[170:173]\n"
        "s_nop 0\n"
        "v_cvt_pk_bf16_f32 v30, v142, v143\n"
        "v_cvt_pk_bf16_f32 v31, v144, v145\n"
        "v_mfma_f32_16x16x32_bf16 v[170:173], v[158:161], a[92:95], v[170:173]\n"
        "s_nop 1\n"
        "v_mfma_f32_16x16x32_bf16 v[40:43], v[106:109], v[162:165], 0\n"
        "s_nop 2\n"
        "v_mfma_f32_16x16x32_bf16 v[40:43], v[112:115], v[34:37], v[40:43]\n"
        "s_nop 0\n"
        "v_cvt_pk_bf16_f32 v34, v170, v171\n"
        "v_cvt_pk_bf16_f32 v35, v172, v173\n"
        "v_mfma_f32_16x16x32_bf16 v[40:43], v[118:121], v[166:169], v[40:43]\n"
        "s_nop 1\n"
        "v_mfma_f32_16x16x32_bf16 v[142:145], a[152:155], v[206:209], 0\n"
        "s_nop 2\n"
        "v_mfma_f32_16x16x32_bf16 v[142:145], a[148:151], v[190:193], v[142:145]\n"
        "s_nop 0\n"
        "v_cndmask_b32_dpp v226, v40, v122, vcc row_ror:4 row_mask:0xf bank_mask:0xf\n"
        "v_cndmask_b32_dpp v117, v41, v123, vcc row_ror:4 row_mask:0xf bank_mask:0xf\n"
        "v_mfma_f32_16x16x32_bf16 v[142:145], a[144:147], v[198:201], v[142:145]\n"
        "v_cndmask_b32_dpp v29, v42, v124, vcc row_ror:4 row_mask:0xf bank_mask:0xf\n"
        "v_cndmask_b32_dpp v253, v43, v125, vcc row_ror:4 row_mask:0xf bank_mask:0xf\n"
        "v_mfma_f32_16x16x32_bf16 v[122:125], a[156:159], v[198:201], 0\n"
        "ds_read_b128 v[202:205], v13 offset:3072\n"
        "ds_read_b128 v[194:197], v13 offset:4096\n"
        "v_mfma_f32_16x16x32_bf16 v[122:125], a[160:163], v[190:193], v[122:125]\n"
        "ds_read_b128 v[210:213], v13 offset:5120\n"
        "ds_read_b128 v[214:217], v20\n"
        "v_mfma_f32_16x16x32_bf16 v[122:125], a[164:167], v[206:209], v[122:125]\n"
        "ds_read_b64 v[36:37], v21\n"
        "v_cndmask_b32_dpp v226, v226, v178, vcc row_ror:4 row_mask:0xf bank_mask:0xf\n"
        "v_mfma_f32_16x16x32_bf16 v[172:175], a[176:179], v[206:209], 0\n"
        "v_cndmask_b32_dpp v117, v117, v179, vcc row_ror:4 row_mask:0xf bank_mask:0xf\n"
        "v_cndmask_b32_dpp v29, v29, v180, vcc row_ror:4 row_mask:0xf bank_mask:0xf\n"
        "v_mfma_f32_16x16x32_bf16 v[172:175], a[172:175], v[190:193], v[172:175]\n"
        "v_cndmask_b32_dpp v253, v253, v181, vcc row_ror:4 row_mask:0xf bank_mask:0xf\n"
        "v_mul_f32_dpp v226, v248, v226 quad_perm:[0,0,0,0] row_mask:0xf bank_mask:0xf\n"
        "v_mfma_f32_16x16x32_bf16 v[172:175], a[168:171], v[198:201], v[172:175]\n"
        "v_mul_f32_dpp v117, v248, v117 quad_perm:[1,1,1,1] row_mask:0xf bank_mask:0xf\n"
        "v_mul_f32_dpp v29, v248, v29 quad_perm:[2,2,2,2] row_mask:0xf bank_mask:0xf\n"
        "v_mfma_f32_16x16x32_bf16 v[162:165], a[180:183], v[198:201], 0\n"
        "v_mul_f32_dpp v253, v248, v253 quad_perm:[3,3,3,3] row_mask:0xf bank_mask:0xf\n"
        "v_max3_f32 v252, v226, v117, v29\n"
        "v_mfma_f32_16x16x32_bf16 v[162:165], a[184:187], v[190:193], v[162:165]\n"
        "v_max_f32 v252, v252, v253\n"
        "v_mul_f32 v252, v252, v248\n"
        "v_mfma_f32_16x16x32_bf16 v[162:165], a[188:191], v[206:209], v[162:165]\n"
        "v_fma_f32 v226, v226, v248, -v252\n"
        "v_fma_f32 v117, v117, v248, -v252\n"
        "v_mfma_f32_16x16x32_bf16 v[40:43], a[200:203], v[206:209], 0\n"
        "v_fma_f32 v29, v29, v248, -v252\n"
        "v_fma_f32 v253, v253, v248, -v252\n"
        "v_mfma_f32_16x16x32_bf16 v[40:43], a[196:199], v[190:193], v[40:43]\n"
        "s_waitcnt lgkmcnt(0)\n"
        "v_cvt_f32_f16_sdwa v246, v36 dst_sel:DWORD dst_unused:UNUSED_PAD src0_sel:WORD_0\n"
        "v_cvt_f32_f16_sdwa v247, v36 dst_sel:DWORD dst_unused:UNUSED_PAD src0_sel:WORD_1\n"
        "v_mfma_f32_16x16x32_bf16 v[40:43], a[192:195], v[198:201], v[40:43]\n"
        "s_barrier\n"
        "v_lshrrev_b32 v111, 0, v36\n"
        "v_lshrrev_b32 v105, 16, v36\n"
        "v_mfma_f32_16x16x32_bf16 v[180:183], a[204:207], v[198:201], 0\n"
        "v_cvt_f32_f16_sdwa v250, v214 dst_sel:DWORD dst_unused:UNUSED_PAD src0_sel:WORD_0\n"
        "v_cvt_f32_f16_sdwa v252, v214 dst_sel:DWORD dst_unused:UNUSED_PAD src0_sel:WORD_1\n"
        "v_mfma_f32_16x16x32_bf16 v[180:183], a[208:211], v[190:193], v[180:183]\n"
        "v_cvt_f32_f16_sdwa v251, v215 dst_sel:DWORD dst_unused:UNUSED_PAD src0_sel:WORD_0\n"
        "v_cvt_f32_f16_sdwa v249, v215 dst_sel:DWORD dst_unused:UNUSED_PAD src0_sel:WORD_1\n"
        "v_mfma_f32_16x16x32_bf16 v[180:183], a[212:215], v[206:209], v[180:183]\n"
        "v_exp_f32 v226, v226\n"
        "v_mfma_f32_16x16x32_bf16 v[218:221], a[152:155], v[210:213], 0\n"
        "v_exp_f32 v117, v117\n"
        "v_mfma_f32_16x16x32_bf16 v[218:221], a[148:151], v[194:197], v[218:221]\n"
        "v_exp_f32 v29, v29\n"
        "v_mfma_f32_16x16x32_bf16 v[218:221], a[144:147], v[202:205], v[218:221]\n"
        "v_exp_f32 v253, v253\n"
        "v_mfma_f32_16x16x32_bf16 v[168:171], a[156:159], v[202:205], 0\n"
        "v_cvt_pk_bf16_f32 v184, v142, v143\n"
        "v_cvt_pk_bf16_f32 v185, v144, v145\n"
        "v_mfma_f32_16x16x32_bf16 v[168:171], a[160:163], v[194:197], v[168:171]\n"
        "v_add_f32 v241, v226, v117\n"
        "v_add_f32 v245, v29, v253\n"
        "v_mfma_f32_16x16x32_bf16 v[168:171], a[164:167], v[210:213], v[168:171]\n"
        "v_add_f32 v241, v241, v245\n"
        "v_cvt_pk_bf16_f32 v186, v122, v123\n"
        "v_mfma_f32_16x16x32_bf16 v[176:179], a[176:179], v[210:213], 0\n"
        "v_cvt_pk_bf16_f32 v187, v124, v125\n"
        "v_cvt_pk_bf16_f32 v222, v172, v173\n"
        "v_mfma_f32_16x16x32_bf16 v[176:179], a[172:175], v[194:197], v[176:179]\n"
        "ds_write_b128 v228, v[184:187]\n"
        "v_mfma_f32_16x16x32_bf16 v[176:179], a[168:171], v[202:205], v[176:179]\n"
        "v_cvt_pk_bf16_f32 v223, v174, v175\n"
        "v_cvt_pk_bf16_f32 v224, v162, v163\n"
        "v_mfma_f32_16x16x32_bf16 v[172:175], a[180:183], v[202:205], 0\n"
        "v_cvt_pk_bf16_f32 v225, v164, v165\n"
        "v_cvt_pk_bf16_f32 v122, v40, v41\n"
        "v_mfma_f32_16x16x32_bf16 v[172:175], a[184:187], v[194:197], v[172:175]\n"
        "ds_write_b128 v228, v[222:225] offset:64\n"
        "v_mfma_f32_16x16x32_bf16 v[172:175], a[188:191], v[210:213], v[172:175]\n"
        "v_cvt_pk_bf16_f32 v123, v42, v43\n"
        "v_cvt_pk_bf16_f32 v124, v180, v181\n"
        "v_mfma_f32_16x16x32_bf16 v[40:43], a[200:203], v[210:213], 0\n"
        "v_cvt_pk_bf16_f32 v125, v182, v183\n"
        "v_cvt_pk_bf16_f32 v180, v218, v219\n"
        "v_mfma_f32_16x16x32_bf16 v[40:43], a[196:199], v[194:197], v[40:43]\n"
        "ds_write_b128 v228, v[122:125] offset:128\n"
        "v_mfma_f32_16x16x32_bf16 v[40:43], a[192:195], v[202:205], v[40:43]\n"
        "v_rcp_f32 v241, v241\n"
        "v_mfma_f32_16x16x32_bf16 v[242:245], a[204:207], v[202:205], 0\n"
        "v_mul_f32 v226, v226, v241\n"
        "v_mul_f32 v117, v117, v241\n"
        "v_mfma_f32_16x16x32_bf16 v[242:245], a[208:211], v[194:197], v[242:245]\n"
        "v_mul_f32 v29, v29, v241\n"
        "v_mul_f32 v253, v253, v241\n"
        "v_mfma_f32_16x16x32_bf16 v[242:245], a[212:215], v[210:213], v[242:245]\n"
        "v_mul_f32_dpp v226, v248, v226 quad_perm:[0,0,0,0] row_mask:0xf bank_mask:0xf\n"
        "v_mul_f32_dpp v117, v248, v117 quad_perm:[1,1,1,1] row_mask:0xf bank_mask:0xf\n"
        "v_mfma_f32_16x16x32_bf16 v[234:237], v[158:161], a[104:107], 0\n"
        "v_mul_f32_dpp v29, v248, v29 quad_perm:[2,2,2,2] row_mask:0xf bank_mask:0xf\n"
        "v_mul_f32_dpp v253, v248, v253 quad_perm:[3,3,3,3] row_mask:0xf bank_mask:0xf\n"
        "v_mfma_f32_16x16x32_bf16 v[234:237], v[152:155], a[100:103], v[234:237]\n"
        "v_cvt_pk_bf16_f32 v137, v226, v117\n"
        "v_cvt_pk_bf16_f32 v136, v29, v253\n"
        "v_mfma_f32_16x16x32_bf16 v[234:237], v[146:149], a[96:99], v[234:237]\n"
        "v_and_b32 v144, v25, v137\n"
        "v_and_b32 v145, v25, v136\n"
        "v_mfma_f32_16x16x32_bf16 v[164:167], v[146:149], a[108:111], 0\n"
        "v_mov_b64 v[150:151], v[144:145]\n"
        "v_mov_b64 v[156:157], v[144:145]\n"
        "v_mfma_f32_16x16x32_bf16 v[164:167], v[152:155], a[112:115], v[164:167]\n"
        "v_mov_b64 v[162:163], v[144:145]\n"
        "v_cvt_pk_bf16_f32 v181, v220, v221\n"
        "v_mfma_f32_16x16x32_bf16 v[164:167], v[158:161], a[116:119], v[164:167]\n"
        "v_cvt_pk_bf16_f32 v182, v168, v169\n"
        "v_cvt_pk_bf16_f32 v183, v170, v171\n"
        "v_mfma_f32_16x16x32_bf16 v[218:221], v[30:33], v[144:147], 0\n"
        "ds_write_b128 v229, v[180:183]\n"
        "v_mfma_f32_16x16x32_bf16 v[168:171], v[32:35], v[148:151], 0\n"
        "v_cvt_pk_bf16_f32 v142, v176, v177\n"
        "v_cvt_pk_bf16_f32 v143, v178, v179\n"
        "v_mfma_f32_16x16x32_bf16 v[238:241], v[198:201], v[184:187], 0\n"
        "v_cvt_pk_bf16_f32 v144, v172, v173\n"
        "v_cvt_pk_bf16_f32 v145, v174, v175\n"
        "v_mfma_f32_16x16x32_bf16 v[238:241], v[190:193], v[222:225], v[238:241]\n"
        "ds_write_b128 v229, v[142:145] offset:64\n"
        "v_mfma_f32_16x16x32_bf16 v[238:241], v[206:209], v[122:125], v[238:241]\n"
        "v_cvt_pk_bf16_f32 v122, v40, v41\n"
        "v_cvt_pk_bf16_f32 v123, v42, v43\n"
        "v_mfma_f32_16x16x32_bf16 v[34:37], v[202:205], v[180:183], 0\n"
        "v_cvt_pk_bf16_f32 v124, v242, v243\n"
        "v_cvt_pk_bf16_f32 v125, v244, v245\n"
        "v_mfma_f32_16x16x32_bf16 v[34:37], v[194:197], v[142:145], v[34:37]\n"
        "ds_write_b128 v229, v[122:125] offset:128\n"
        "v_mfma_f32_16x16x32_bf16 v[34:37], v[210:213], v[122:125], v[34:37]\n"
        "ds_read_b128 v[88:91], v27 offset:64\n"
        "ds_read_b128 v[80:83], v27 offset:128\n"
        "v_mfma_f32_16x16x32_bf16 v[174:177], v[158:161], a[128:131], 0\n"
        "v_and_b32_dpp v124, v137, v25 row_ror:12 row_mask:0xf bank_mask:0xf\n"
        "v_and_b32_dpp v125, v136, v25 row_ror:12 row_mask:0xf bank_mask:0xf\n"
        "v_mfma_f32_16x16x32_bf16 v[174:177], v[152:155], a[124:127], v[174:177]\n"
        "v_mov_b64 v[130:131], v[124:125]\n"
        "v_cvt_pk_bf16_f32 v42, v234, v235\n"
        "v_mfma_f32_16x16x32_bf16 v[174:177], v[146:149], a[120:123], v[174:177]\n"
        "v_cvt_pk_bf16_f32 v43, v236, v237\n"
        "v_cvt_pk_bf16_f32 v46, v164, v165\n"
        "v_mfma_f32_16x16x32_bf16 v[234:237], v[146:149], a[132:135], 0\n"
        "v_cvt_pk_bf16_f32 v47, v166, v167\n"
        "v_cvt_pk_bf16_f32 v142, v218, v219\n"
        "v_mfma_f32_16x16x32_bf16 v[234:237], v[152:155], a[136:139], v[234:237]\n"
        "v_cvt_pk_bf16_f32 v218, v220, v221\n"
        "v_cvt_pk_bf16_f32 v164, v168, v169\n"
        "v_mfma_f32_16x16x32_bf16 v[234:237], v[158:161], a[140:143], v[234:237]\n"
        "v_cvt_pk_bf16_f32 v143, v170, v171\n"
        "v_cndmask_b32_e64 v110, v238, v239, s[56:57]\n"
        "v_mfma_f32_16x16x32_bf16 v[166:169], v[42:45], v[150:153], 0\n"
        "v_cndmask_b32_e64 v110, v110, v240, s[58:59]\n"
        "v_cndmask_b32_e64 v110, v110, v241, s[60:61]\n"
        "v_mfma_f32_16x16x32_bf16 v[238:241], v[44:47], v[154:157], 0\n"
        "s_waitcnt lgkmcnt(0)\n"
        "v_mul_f32 v110, v110, v246\n"
        "v_mul_f32 v110, v110, v246\n"
        "v_mfma_f32_16x16x32_bf16 v[150:153], v[206:209], a[224:227], 0\n"
        "v_exp_f32 v110, v110\n"
        "v_mfma_f32_16x16x32_bf16 v[150:153], v[190:193], a[220:223], v[150:153]\n"
        "ds_read_b64_tr_b16 v[222:223], v227\n"
        "ds_read_b64_tr_b16 v[224:225], v227 offset:3072\n"
        "v_mfma_f32_16x16x32_bf16 v[150:153], v[198:201], a[216:219], v[150:153]\n"
        "ds_read_b64_tr_b16 v[182:183], v227 offset:6144\n"
        "ds_read_b64_tr_b16 v[184:185], v227 offset:9216\n"
        "v_mfma_f32_16x16x32_bf16 v[170:173], v[198:201], a[228:231], 0\n"
        "ds_read_b64_tr_b16 v[146:147], v227 offset:12288\n"
        "ds_read_b64_tr_b16 v[148:149], v227 offset:15360\n"
        "v_mfma_f32_16x16x32_bf16 v[170:173], v[190:193], a[232:235], v[170:173]\n"
        "ds_write_b64 v230, v[110:111]\n"
        "v_mfma_f32_16x16x32_bf16 v[170:173], v[206:209], a[236:239], v[170:173]\n"
        "v_cndmask_b32_e64 v104, v34, v35, s[56:57]\n"
        "v_cndmask_b32_e64 v104, v104, v36, s[58:59]\n"
        "v_mfma_f32_16x16x32_bf16 v[186:189], v[198:201], v[88:91], 0\n"
        "v_cndmask_b32_e64 v104, v104, v37, s[60:61]\n"
        "v_mul_f32 v104, v104, v247\n"
        "v_mfma_f32_16x16x32_bf16 v[178:181], v[198:201], v[80:83], 0\n"
        "v_mul_f32 v104, v104, v247\n"
        "v_cvt_pk_bf16_f32 v36, v174, v175\n"
        "v_mfma_f32_16x16x32_bf16 v[242:245], v[138:141], a[80:83], 0\n"
        "v_exp_f32 v104, v104\n"
        "v_mfma_f32_16x16x32_bf16 v[242:245], v[132:135], a[76:79], v[242:245]\n"
        "ds_write_b64 v231, v[104:105]\n"
        "v_mfma_f32_16x16x32_bf16 v[242:245], v[126:129], a[72:75], v[242:245]\n"
        "v_cvt_pk_bf16_f32 v37, v176, v177\n"
        "v_cvt_pk_bf16_f32 v40, v234, v235\n"
        "v_mfma_f32_16x16x32_bf16 v[174:177], v[126:129], a[84:87], 0\n"
        "s_waitcnt lgkmcnt(2)\n"
        "v_cvt_pk_bf16_f32 v41, v236, v237\n"
        "v_cvt_pk_bf16_f32 v219, v166, v167\n"
        "v_mfma_f32_16x16x32_bf16 v[174:177], v[132:135], a[88:91], v[174:177]\n"
        "v_cvt_pk_bf16_f32 v165, v168, v169\n"
        "v_cvt_pk_bf16_f32 v144, v238, v239\n"
        "v_mfma_f32_16x16x32_bf16 v[174:177], v[138:141], a[92:95], v[174:177]\n"
        "v_cvt_pk_bf16_f32 v220, v240, v241\n"
        "v_mul_f32 v150, v150, v250\n"
        "v_mfma_f32_16x16x32_bf16 v[234:237], v[36:39], v[156:159], 0\n"
        "v_mul_f32 v151, v151, v252\n"
        "v_mul_f32 v152, v152, v251\n"
        "v_mfma_f32_16x16x32_bf16 v[166:169], v[38:41], v[160:163], 0\n"
        "v_mul_f32 v153, v153, v249\n"
        "v_mul_f32 v170, v170, v250\n"
        "v_mfma_f32_16x16x32_bf16 v[158:161], v[146:149], v[206:209], 0\n"
        "v_mul_f32 v171, v171, v252\n"
        "v_mul_f32 v172, v172, v251\n"
        "v_mfma_f32_16x16x32_bf16 v[158:161], v[182:185], v[190:193], v[158:161]\n"
        "v_mul_f32 v173, v173, v249\n"
        "v_cvt_pk_f16_f32 v150, v150, v170\n"
        "v_mfma_f32_16x16x32_bf16 v[158:161], v[222:225], v[198:201], v[158:161]\n"
        "v_cvt_pk_f16_f32 v151, v151, v171\n"
        "v_cvt_pk_f16_f32 v152, v152, v172\n"
        "v_mfma_f32_16x16x32_bf16 v[238:241], v[222:225], v[202:205], 0\n"
        "v_cvt_pk_f16_f32 v153, v153, v173\n"
        "v_perm_b32 v40, v150, v186, s54\n"
        "v_mfma_f32_16x16x32_bf16 v[238:241], v[182:185], v[194:197], v[238:241]\n"
        "v_perm_b32 v36, v151, v187, s54\n"
        "v_perm_b32 v30, v152, v188, s54\n"
        "v_mfma_f32_16x16x32_bf16 v[238:241], v[146:149], v[210:213], v[238:241]\n"
        "v_perm_b32 v34, v153, v189, s54\n"
        "v_perm_b32 v41, v150, v178, s55\n"
        "v_mfma_f32_16x16x32_bf16 v[146:149], v[138:141], a[104:107], 0\n"
        "ds_write_b64 v232, v[40:41]\n"
        "v_mfma_f32_16x16x32_bf16 v[146:149], v[132:135], a[100:103], v[146:149]\n"
        "v_perm_b32 v37, v151, v179, s55\n"
        "v_perm_b32 v31, v152, v180, s55\n"
        "v_mfma_f32_16x16x32_bf16 v[146:149], v[126:129], a[96:99], v[146:149]\n"
        "ds_write_b64 v232, v[36:37] offset:3136\n"
        "v_mfma_f32_16x16x32_bf16 v[154:157], v[126:129], a[108:111], 0\n"
        "ds_write_b64 v232, v[30:31] offset:6272\n"
        "v_mfma_f32_16x16x32_bf16 v[154:157], v[132:135], a[112:115], v[154:157]\n"
        "v_perm_b32 v35, v153, v181, s55\n"
        "v_cvt_pk_bf16_f32 v145, v236, v237\n"
        "v_mfma_f32_16x16x32_bf16 v[154:157], v[138:141], a[116:119], v[154:157]\n"
        "ds_write_b128 v12, v[142:145]\n"
        "v_cvt_pk_bf16_f32 v30, v242, v243\n"
        "v_cvt_pk_bf16_f32 v31, v244, v245\n"
        "s_nop 1\n"
        "v_mfma_f32_16x16x32_bf16 v[222:225], v[30:33], v[124:127], 0\n"
        "ds_write_b64 v232, v[34:35] offset:9408\n"
        "v_cvt_pk_bf16_f32 v34, v174, v175\n"
        "v_cvt_pk_bf16_f32 v35, v176, v177\n"
        "s_nop 1\n"
        "v_mfma_f32_16x16x32_bf16 v[184:187], v[32:35], v[128:131], 0\n"
        "v_cvt_pk_bf16_f32 v221, v166, v167\n"
        "v_cvt_pk_bf16_f32 v166, v234, v235\n"
        "v_mfma_f32_16x16x32_bf16 v[172:175], v[206:209], a[248:251], 0\n"
        "ds_write_b128 v12, v[218:221] offset:1024\n"
        "v_mfma_f32_16x16x32_bf16 v[172:175], v[190:193], a[244:247], v[172:175]\n"
        "v_cvt_pk_bf16_f32 v167, v168, v169\n"
        "v_cvt_f32_f16_sdwa v247, v216 dst_sel:DWORD dst_unused:UNUSED_PAD src0_sel:WORD_0\n"
        "v_mfma_f32_16x16x32_bf16 v[172:175], v[198:201], a[240:243], v[172:175]\n"
        "ds_write_b128 v12, v[164:167] offset:2048\n"
        "v_mfma_f32_16x16x32_bf16 v[34:37], v[198:201], a[252:255], 0\n"
        "v_cvt_f32_f16_sdwa v246, v216 dst_sel:DWORD dst_unused:UNUSED_PAD src0_sel:WORD_1\n"
        "v_mul_f32_dpp v248, v254, v226 quad_perm:[0,0,0,0] row_mask:0xf bank_mask:0xf\n"
        "v_mfma_f32_16x16x32_bf16 v[34:37], v[190:193], v[92:95], v[34:37]\n"
        "ds_read_b64_tr_b16 v[214:215], v227 offset:8\n"
        "ds_read_b64_tr_b16 v[216:217], v227 offset:3080\n"
        "v_mfma_f32_16x16x32_bf16 v[34:37], v[206:209], v[52:55], v[34:37]\n"
        "ds_read_b64_tr_b16 v[176:177], v227 offset:6152\n"
        "ds_read_b64_tr_b16 v[178:179], v227 offset:9224\n"
        "v_mfma_f32_16x16x32_bf16 v[150:153], v[190:193], v[88:91], 0\n"
        "ds_read_b64_tr_b16 v[168:169], v227 offset:12296\n"
        "ds_read_b64_tr_b16 v[170:171], v227 offset:15368\n"
        "v_mfma_f32_16x16x32_bf16 v[234:237], v[190:193], v[80:83], 0\n"
        "v_and_b32_dpp v104, v137, v25 row_ror:8 row_mask:0xf bank_mask:0xf\n"
        "v_and_b32_dpp v105, v136, v25 row_ror:8 row_mask:0xf bank_mask:0xf\n"
        "v_mfma_f32_16x16x32_bf16 v[180:183], v[142:145], v[142:145], 0\n"
        "v_mov_b64 v[136:137], v[124:125]\n"
        "v_mov_b64 v[142:143], v[124:125]\n"
        "v_mfma_f32_16x16x32_bf16 v[180:183], v[218:221], v[218:221], v[180:183]\n"
        "v_mov_b64 v[110:111], v[104:105]\n"
        "v_fmac_f32_dpp v248, v254, v117 quad_perm:[1,1,1,1] row_mask:0xf bank_mask:0xf\n"
        "v_mfma_f32_16x16x32_bf16 v[180:183], v[164:167], v[164:167], v[180:183]\n"
        "v_mov_b64 v[116:117], v[104:105]\n"
        "v_fmac_f32_dpp v248, v254, v29 quad_perm:[2,2,2,2] row_mask:0xf bank_mask:0xf\n"
        "v_mfma_f32_16x16x32_bf16 v[162:165], v[138:141], a[128:131], 0\n"
        "v_cvt_pk_bf16_f32 v42, v146, v147\n"
        "v_cvt_pk_bf16_f32 v43, v148, v149\n"
        "v_mfma_f32_16x16x32_bf16 v[162:165], v[132:135], a[124:127], v[162:165]\n"
        "v_cvt_pk_bf16_f32 v46, v154, v155\n"
        "v_cvt_pk_bf16_f32 v47, v156, v157\n"
        "v_mfma_f32_16x16x32_bf16 v[162:165], v[126:129], a[120:123], v[162:165]\n"
        "v_mul_f32 v172, v172, v250\n"
        "v_mul_f32 v173, v173, v252\n"
        "v_mfma_f32_16x16x32_bf16 v[144:147], v[126:129], a[132:135], 0\n"
        "s_waitcnt lgkmcnt(0)\n"
        "v_mul_f32 v174, v174, v251\n"
        "v_mul_f32 v175, v175, v249\n"
        "v_mfma_f32_16x16x32_bf16 v[144:147], v[132:135], a[136:139], v[144:147]\n"
        "v_mul_f32 v34, v34, v250\n"
        "v_mul_f32 v35, v35, v252\n"
        "v_mfma_f32_16x16x32_bf16 v[144:147], v[138:141], a[140:143], v[144:147]\n"
        "v_mul_f32 v36, v36, v251\n"
        "v_mul_f32 v37, v37, v249\n"
        "v_mfma_f32_16x16x32_bf16 v[126:129], v[42:45], v[130:133], 0\n"
        "v_cvt_pk_f16_f32 v172, v172, v34\n"
        "v_cvt_pk_f16_f32 v173, v173, v35\n"
        "v_mfma_f32_16x16x32_bf16 v[218:221], v[44:47], v[134:137], 0\n"
        "v_cvt_pk_f16_f32 v174, v174, v36\n"
        "v_cvt_pk_f16_f32 v175, v175, v37\n"
        "v_mfma_f32_16x16x32_bf16 v[130:133], v[168:171], v[206:209], 0\n"
        "v_perm_b32 v30, v172, v150, s54\n"
        "v_perm_b32 v31, v172, v234, s55\n"
        "v_mfma_f32_16x16x32_bf16 v[130:133], v[176:179], v[190:193], v[130:133]\n"
        "ds_write_b64 v232, v[30:31] offset:8\n"
        "v_mfma_f32_16x16x32_bf16 v[130:133], v[214:217], v[198:201], v[130:133]\n"
        "v_perm_b32 v30, v173, v151, s54\n"
        "v_perm_b32 v31, v173, v235, s55\n"
        "v_mfma_f32_16x16x32_bf16 v[122:125], v[214:217], v[202:205], 0\n"
        "ds_write_b64 v232, v[30:31] offset:3144\n"
        "v_mfma_f32_16x16x32_bf16 v[122:125], v[176:179], v[194:197], v[122:125]\n"
        "v_perm_b32 v30, v174, v152, s54\n"
        "v_perm_b32 v31, v174, v236, s55\n"
        "v_mfma_f32_16x16x32_bf16 v[122:125], v[168:171], v[210:213], v[122:125]\n"
        "ds_write_b64 v232, v[30:31] offset:6280\n"
        "v_mfma_f32_16x16x32_bf16 v[176:179], v[118:121], a[80:83], 0\n"
        "v_perm_b32 v30, v175, v153, s54\n"
        "v_perm_b32 v31, v175, v237, s55\n"
        "v_mfma_f32_16x16x32_bf16 v[176:179], v[112:115], a[76:79], v[176:179]\n"
        "ds_write_b64 v232, v[30:31] offset:9416\n"
        "v_mfma_f32_16x16x32_bf16 v[176:179], v[106:109], a[72:75], v[176:179]\n"
        "v_cvt_pk_bf16_f32 v34, v158, v159\n"
        "v_cvt_pk_bf16_f32 v35, v160, v161\n"
        "v_mfma_f32_16x16x32_bf16 v[152:155], v[106:109], a[84:87], 0\n"
        "v_cvt_pk_bf16_f32 v134, v238, v239\n"
        "v_cvt_pk_bf16_f32 v135, v240, v241\n"
        "v_mfma_f32_16x16x32_bf16 v[152:155], v[112:115], a[88:91], v[152:155]\n"
        "v_cvt_pk_bf16_f32 v36, v162, v163\n"
        "v_cvt_pk_bf16_f32 v37, v164, v165\n"
        "v_mfma_f32_16x16x32_bf16 v[152:155], v[118:121], a[92:95], v[152:155]\n"
        "v_cvt_pk_bf16_f32 v40, v144, v145\n"
        "v_cvt_pk_bf16_f32 v41, v146, v147\n"
        "v_mfma_f32_16x16x32_bf16 v[156:159], v[36:39], v[136:139], 0\n"
        "v_cvt_pk_bf16_f32 v36, v130, v131\n"
        "v_cvt_pk_bf16_f32 v37, v132, v133\n"
        "v_mfma_f32_16x16x32_bf16 v[144:147], v[38:41], v[140:143], 0\n"
        "ds_write_b128 v228, v[34:37] offset:192\n"
        "v_mfma_f32_16x16x32_bf16 v[160:163], v[206:209], v[76:79], 0\n"
        "v_cvt_pk_bf16_f32 v148, v222, v223\n"
        "v_cvt_pk_bf16_f32 v130, v224, v225\n"
        "v_mfma_f32_16x16x32_bf16 v[160:163], v[190:193], v[84:87], v[160:163]\n"
        "ds_read_b64_tr_b16 v[172:173], v227 offset:1024\n"
        "ds_read_b64_tr_b16 v[174:175], v227 offset:4096\n"
        "v_mfma_f32_16x16x32_bf16 v[160:163], v[198:201], v[64:67], v[160:163]\n"
        "ds_read_b64_tr_b16 v[214:215], v227 offset:7168\n"
        "ds_read_b64_tr_b16 v[216:217], v227 offset:10240\n"
        "v_mfma_f32_16x16x32_bf16 v[40:43], v[198:201], v[60:63], 0\n"
        "ds_read_b64_tr_b16 v[164:165], v227 offset:13312\n"
        "ds_read_b64_tr_b16 v[166:167], v227 offset:16384\n"
        "v_mfma_f32_16x16x32_bf16 v[40:43], v[190:193], v[72:75], v[40:43]\n"
        "v_cvt_pk_bf16_f32 v136, v122, v123\n"
        "v_cvt_pk_bf16_f32 v137, v124, v125\n"
        "v_mfma_f32_16x16x32_bf16 v[40:43], v[206:209], v[48:51], v[40:43]\n"
        "ds_write_b128 v229, v[134:137] offset:192\n"
        "v_mfma_f32_16x16x32_bf16 v[134:137], v[206:209], v[88:91], 0\n"
        "v_cvt_pk_bf16_f32 v142, v184, v185\n"
        "v_cvt_pk_bf16_f32 v149, v186, v187\n"
        "v_mfma_f32_16x16x32_bf16 v[168:171], v[206:209], v[80:83], 0\n"
        "v_cvt_pk_bf16_f32 v131, v126, v127\n"
        "v_cvt_pk_bf16_f32 v143, v128, v129\n"
        "v_mfma_f32_16x16x32_bf16 v[184:187], v[118:121], a[104:107], 0\n"
        "v_cvt_pk_bf16_f32 v150, v218, v219\n"
        "v_cvt_pk_bf16_f32 v151, v158, v159\n"
        "v_mfma_f32_16x16x32_bf16 v[184:187], v[112:115], a[100:103], v[184:187]\n"
        "ds_write_b128 v12, v[148:151] offset:3072\n"
        "v_mfma_f32_16x16x32_bf16 v[184:187], v[106:109], a[96:99], v[184:187]\n"
        "s_waitcnt lgkmcnt(2)\n"
        "v_cvt_pk_bf16_f32 v132, v220, v221\n"
        "v_cvt_pk_bf16_f32 v30, v176, v177\n"
        "v_mfma_f32_16x16x32_bf16 v[122:125], v[106:109], a[108:111], 0\n"
        "v_cvt_pk_bf16_f32 v31, v178, v179\n"
        "v_cvt_pk_bf16_f32 v133, v144, v145\n"
        "v_mfma_f32_16x16x32_bf16 v[122:125], v[112:115], a[112:115], v[122:125]\n"
        "ds_write_b128 v12, v[130:133] offset:4096\n"
        "v_mfma_f32_16x16x32_bf16 v[122:125], v[118:121], a[116:119], v[122:125]\n"
        "v_cvt_pk_bf16_f32 v34, v152, v153\n"
        "v_cvt_pk_bf16_f32 v35, v154, v155\n"
        "v_mfma_f32_16x16x32_bf16 v[126:129], v[30:33], v[104:107], 0\n"
        "v_cvt_pk_bf16_f32 v144, v156, v157\n"
        "v_cvt_pk_bf16_f32 v145, v146, v147\n"
        "v_mfma_f32_16x16x32_bf16 v[138:141], v[32:35], v[108:111], 0\n"
        "ds_write_b128 v12, v[142:145] offset:5120\n"
        "v_mfma_f32_16x16x32_bf16 v[156:159], v[164:167], v[206:209], 0\n"
        "v_mul_f32 v160, v160, v250\n"
        "v_mul_f32 v161, v161, v252\n"
        "v_mfma_f32_16x16x32_bf16 v[156:159], v[214:217], v[190:193], v[156:159]\n"
        "v_mul_f32 v162, v162, v251\n"
        "v_mul_f32 v163, v163, v249\n"
        "v_mfma_f32_16x16x32_bf16 v[156:159], v[172:175], v[198:201], v[156:159]\n"
        "v_mul_f32 v40, v40, v250\n"
        "v_mul_f32 v41, v41, v252\n"
        "v_mfma_f32_16x16x32_bf16 v[34:37], v[172:175], v[202:205], 0\n"
        "v_mul_f32 v42, v42, v251\n"
        "v_mul_f32 v43, v43, v249\n"
        "v_mfma_f32_16x16x32_bf16 v[34:37], v[214:217], v[194:197], v[34:37]\n"
        "v_cvt_pk_f16_f32 v160, v160, v40\n"
        "v_cvt_pk_f16_f32 v161, v161, v41\n"
        "v_mfma_f32_16x16x32_bf16 v[34:37], v[164:167], v[210:213], v[34:37]\n"
        "v_cvt_pk_f16_f32 v162, v162, v42\n"
        "v_cvt_pk_f16_f32 v163, v163, v43\n"
        "v_mfma_f32_16x16x32_bf16 v[152:155], v[148:151], v[148:151], 0\n"
        "v_perm_b32 v30, v160, v134, s54\n"
        "v_perm_b32 v31, v160, v168, s55\n"
        "v_mfma_f32_16x16x32_bf16 v[152:155], v[130:133], v[130:133], v[152:155]\n"
        "ds_write_b64 v232, v[30:31] offset:16\n"
        "v_mfma_f32_16x16x32_bf16 v[152:155], v[142:145], v[142:145], v[152:155]\n"
        "v_perm_b32 v30, v161, v135, s54\n"
        "v_perm_b32 v31, v161, v169, s55\n"
        "v_mfma_f32_16x16x32_bf16 v[172:175], v[118:121], a[128:131], 0\n"
        "ds_write_b64 v232, v[30:31] offset:3152\n"
        "v_mfma_f32_16x16x32_bf16 v[172:175], v[112:115], a[124:127], v[172:175]\n"
        "ds_read_b64_tr_b16 v[142:143], v227 offset:1032\n"
        "ds_read_b64_tr_b16 v[144:145], v227 offset:4104\n"
        "v_mfma_f32_16x16x32_bf16 v[172:175], v[106:109], a[120:123], v[172:175]\n"
        "ds_read_b64_tr_b16 v[130:131], v227 offset:7176\n"
        "ds_read_b64_tr_b16 v[132:133], v227 offset:10248\n"
        "v_mfma_f32_16x16x32_bf16 v[146:149], v[106:109], a[132:135], 0\n"
        "ds_read_b64_tr_b16 v[164:165], v227 offset:13320\n"
        "ds_read_b64_tr_b16 v[166:167], v227 offset:16392\n"
        "v_mfma_f32_16x16x32_bf16 v[146:149], v[112:115], a[136:139], v[146:149]\n"
        "v_perm_b32 v30, v162, v136, s54\n"
        "v_perm_b32 v31, v162, v170, s55\n"
        "v_mfma_f32_16x16x32_bf16 v[146:149], v[118:121], a[140:143], v[146:149]\n"
        "ds_write_b64 v232, v[30:31] offset:6288\n"
        "v_cvt_pk_bf16_f32 v42, v184, v185\n"
        "v_cvt_pk_bf16_f32 v43, v186, v187\n"
        "s_nop 1\n"
        "v_mfma_f32_16x16x32_bf16 v[106:109], v[42:45], v[110:113], 0\n"
        "v_perm_b32 v30, v163, v137, s54\n"
        "v_perm_b32 v31, v163, v171, s55\n"
        "v_cvt_pk_bf16_f32 v46, v122, v123\n"
        "v_cvt_pk_bf16_f32 v47, v124, v125\n"
        "s_nop 1\n"
        "v_mfma_f32_16x16x32_bf16 v[160:163], v[44:47], v[114:117], 0\n"
        "ds_write_b64 v232, v[30:31] offset:9424\n"
        "v_mfma_f32_16x16x32_bf16 v[184:187], v[210:213], a[224:227], 0\n"
        "v_mov_b64 v[122:123], v[104:105]\n"
        "v_fmac_f32_dpp v248, v254, v253 quad_perm:[3,3,3,3] row_mask:0xf bank_mask:0xf\n"
        "v_mfma_f32_16x16x32_bf16 v[184:187], v[194:197], a[220:223], v[184:187]\n"
        "s_waitcnt lgkmcnt(2)\n"
        "v_add_f32 v248, v248, v255\n"
        "v_mul_f32 v255, s80, v248\n"
        "v_mfma_f32_16x16x32_bf16 v[184:187], v[202:205], a[216:219], v[184:187]\n"
        "v_cndmask_b32_e64 v253, v180, v181, s[56:57]\n"
        "v_cndmask_b32_e64 v253, v253, v182, s[58:59]\n"
        "v_mfma_f32_16x16x32_bf16 v[110:113], v[202:205], a[228:231], 0\n"
        "v_cndmask_b32_e64 v253, v253, v183, s[60:61]\n"
        "v_cvt_pk_bf16_f32 v150, v126, v127\n"
        "v_mfma_f32_16x16x32_bf16 v[110:113], v[194:197], a[232:235], v[110:113]\n"
        "v_cvt_pk_bf16_f32 v168, v128, v129\n"
        "v_cvt_pk_bf16_f32 v114, v138, v139\n"
        "v_mfma_f32_16x16x32_bf16 v[110:113], v[210:213], a[236:239], v[110:113]\n"
        "v_cvt_pk_bf16_f32 v151, v140, v141\n"
        "v_cvt_pk_bf16_f32 v138, v156, v157\n"
        "v_mfma_f32_16x16x32_bf16 v[176:179], v[202:205], v[88:91], 0\n"
        "v_cvt_pk_bf16_f32 v139, v158, v159\n"
        "v_cvt_pk_bf16_f32 v124, v34, v35\n"
        "v_mfma_f32_16x16x32_bf16 v[134:137], v[202:205], v[80:83], 0\n"
        "v_cvt_pk_bf16_f32 v125, v36, v37\n"
        "v_cndmask_b32_e64 v254, v152, v153, s[56:57]\n"
        "v_mfma_f32_16x16x32_bf16 v[156:159], v[164:167], v[206:209], 0\n"
        "v_cndmask_b32_e64 v254, v254, v154, s[58:59]\n"
        "v_cndmask_b32_e64 v254, v254, v155, s[60:61]\n"
        "v_mfma_f32_16x16x32_bf16 v[156:159], v[130:133], v[190:193], v[156:159]\n"
        "v_cvt_pk_bf16_f32 v36, v172, v173\n"
        "v_cvt_pk_bf16_f32 v37, v174, v175\n"
        "v_mfma_f32_16x16x32_bf16 v[156:159], v[142:145], v[198:201], v[156:159]\n"
        "v_cvt_pk_bf16_f32 v40, v146, v147\n"
        "v_cvt_pk_bf16_f32 v41, v148, v149\n"
        "v_mfma_f32_16x16x32_bf16 v[146:149], v[142:145], v[202:205], 0\n"
        "v_cvt_pk_bf16_f32 v169, v106, v107\n"
        "v_cvt_pk_bf16_f32 v115, v108, v109\n"
        "v_mfma_f32_16x16x32_bf16 v[146:149], v[130:133], v[194:197], v[146:149]\n"
        "ds_read_b64_tr_b16 v[104:105], v227 offset:2048\n"
        "ds_read_b64_tr_b16 v[106:107], v227 offset:5120\n"
        "v_mfma_f32_16x16x32_bf16 v[146:149], v[164:167], v[210:213], v[146:149]\n"
        "ds_read_b64_tr_b16 v[126:127], v227 offset:8192\n"
        "ds_read_b64_tr_b16 v[128:129], v227 offset:11264\n"
        "v_mfma_f32_16x16x32_bf16 v[130:133], v[36:39], v[116:119], 0\n"
        "ds_read_b64_tr_b16 v[172:173], v227 offset:14336\n"
        "ds_read_b64_tr_b16 v[174:175], v227 offset:17408\n"
        "v_mfma_f32_16x16x32_bf16 v[116:119], v[38:41], v[120:123], 0\n"
        "v_cvt_pk_bf16_f32 v152, v160, v161\n"
        "v_cvt_pk_bf16_f32 v170, v162, v163\n"
        "v_mfma_f32_16x16x32_bf16 v[34:37], v[210:213], a[248:251], 0\n"
        "v_mul_f32 v184, v184, v247\n"
        "v_mul_f32 v185, v185, v246\n"
        "v_mfma_f32_16x16x32_bf16 v[34:37], v[194:197], a[244:247], v[34:37]\n"
        "v_mul_f32 v110, v110, v247\n"
        "v_mul_f32 v111, v111, v246\n"
        "v_mfma_f32_16x16x32_bf16 v[34:37], v[202:205], a[240:243], v[34:37]\n"
        "v_cvt_pk_f16_f32 v184, v184, v110\n"
        "v_cvt_pk_f16_f32 v185, v185, v111\n"
        "v_mfma_f32_16x16x32_bf16 v[160:163], v[202:205], a[252:255], 0\n"
        "v_perm_b32 v30, v184, v176, s54\n"
        "v_perm_b32 v46, v185, v177, s54\n"
        "v_mfma_f32_16x16x32_bf16 v[160:163], v[194:197], v[92:95], v[160:163]\n"
        "v_perm_b32 v31, v184, v134, s55\n"
        "v_perm_b32 v47, v185, v135, s55\n"
        "v_mfma_f32_16x16x32_bf16 v[160:163], v[210:213], v[52:55], v[160:163]\n"
        "ds_write_b64 v232, v[30:31] offset:12544\n"
        "v_mfma_f32_16x16x32_bf16 v[40:43], v[194:197], v[88:91], 0\n"
        "s_waitcnt lgkmcnt(1)\n"
        "ds_write_b64 v233, v[46:47]\n"
        "v_mfma_f32_16x16x32_bf16 v[108:111], v[194:197], v[80:83], 0\n"
        "v_cvt_pk_bf16_f32 v140, v156, v157\n"
        "v_cvt_pk_bf16_f32 v141, v158, v159\n"
        "v_cvt_pk_bf16_f32 v153, v132, v133\n"
        "s_nop 1\n"
        "v_mfma_f32_16x16x32_bf16 v[162:165], v[150:153], v[150:153], 0\n"
        "ds_write_b128 v228, v[138:141] offset:256\n"
        "v_cvt_pk_bf16_f32 v171, v116, v117\n"
        "s_nop 1\n"
        "v_mfma_f32_16x16x32_bf16 v[162:165], v[168:171], v[168:171], v[162:165]\n"
        "ds_write_b128 v12, v[150:153] offset:6144\n"
        "v_cvt_pk_bf16_f32 v116, v130, v131\n"
        "v_cvt_pk_bf16_f32 v117, v118, v119\n"
        "s_nop 1\n"
        "v_mfma_f32_16x16x32_bf16 v[162:165], v[114:117], v[114:117], v[162:165]\n"
        "ds_read_b64_tr_b16 v[136:137], v227 offset:2056\n"
        "ds_read_b64_tr_b16 v[138:139], v227 offset:5128\n"
        "v_mfma_f32_16x16x32_bf16 v[176:179], v[172:175], v[206:209], 0\n"
        "ds_read_b64_tr_b16 v[118:119], v227 offset:8200\n"
        "ds_read_b64_tr_b16 v[120:121], v227 offset:11272\n"
        "v_mfma_f32_16x16x32_bf16 v[176:179], v[126:129], v[190:193], v[176:179]\n"
        "ds_read_b64_tr_b16 v[110:111], v227 offset:14344\n"
        "ds_read_b64_tr_b16 v[112:113], v227 offset:17416\n"
        "v_mfma_f32_16x16x32_bf16 v[176:179], v[104:107], v[198:201], v[176:179]\n"
        "ds_write_b128 v12, v[168:171] offset:7168\n"
        "v_mfma_f32_16x16x32_bf16 v[142:145], v[104:107], v[202:205], 0\n"
        "ds_write_b128 v12, v[114:117] offset:8192\n"
        "v_mfma_f32_16x16x32_bf16 v[142:145], v[126:129], v[194:197], v[142:145]\n"
        "v_cvt_pk_bf16_f32 v126, v146, v147\n"
        "v_cvt_pk_bf16_f32 v127, v148, v149\n"
        "v_mfma_f32_16x16x32_bf16 v[142:145], v[172:175], v[210:213], v[142:145]\n"
        "ds_write_b128 v229, v[124:127] offset:256\n"
        "v_mfma_f32_16x16x32_bf16 v[104:107], v[210:213], v[76:79], 0\n"
        "s_waitcnt lgkmcnt(3)\n"
        "v_mul_f32 v34, v34, v247\n"
        "v_mul_f32 v35, v35, v246\n"
        "v_mfma_f32_16x16x32_bf16 v[104:107], v[194:197], v[84:87], v[104:107]\n"
        "v_mul_f32 v160, v160, v247\n"
        "v_mul_f32 v161, v161, v246\n"
        "v_mfma_f32_16x16x32_bf16 v[104:107], v[202:205], v[64:67], v[104:107]\n"
        "v_cvt_pk_f16_f32 v34, v34, v160\n"
        "v_cvt_pk_f16_f32 v35, v35, v161\n"
        "v_mfma_f32_16x16x32_bf16 v[122:125], v[202:205], v[60:63], 0\n"
        "s_waitcnt vmcnt(0)\n"
        "v_perm_b32 v36, v34, v40, s54\n"
        "v_perm_b32 v30, v35, v41, s54\n"
        "v_mfma_f32_16x16x32_bf16 v[122:125], v[194:197], v[72:75], v[122:125]\n"
        "ds_read_b128 v[146:149], v9\n"
        "v_perm_b32 v37, v34, v108, s55\n"
        "v_mfma_f32_16x16x32_bf16 v[122:125], v[210:213], v[48:51], v[122:125]\n"
        "ds_write_b64 v232, v[36:37] offset:12552\n"
        "v_mfma_f32_16x16x32_bf16 v[40:43], v[210:213], v[88:91], 0\n"
        "ds_read_b128 v[152:155], v10\n"
        "v_perm_b32 v31, v35, v109, s55\n"
        "v_mfma_f32_16x16x32_bf16 v[34:37], v[210:213], v[80:83], 0\n"
        "ds_write_b64 v233, v[30:31] offset:8\n"
        "v_mfma_f32_16x16x32_bf16 v[168:171], v[110:113], v[206:209], 0\n"
        "ds_read_b128 v[158:161], v11\n"
        "v_cndmask_b32_e64 v252, v162, v163, s[56:57]\n"
        "v_mfma_f32_16x16x32_bf16 v[168:171], v[118:121], v[190:193], v[168:171]\n"
        "ds_read_b128 v[126:129], v9 offset:3072\n"
        "v_cndmask_b32_e64 v252, v252, v164, s[58:59]\n"
        "v_mfma_f32_16x16x32_bf16 v[168:171], v[136:139], v[198:201], v[168:171]\n"
        "ds_read_b128 v[132:135], v10 offset:3072\n"
        "v_cndmask_b32_e64 v252, v252, v165, s[60:61]\n"
        "v_mfma_f32_16x16x32_bf16 v[172:175], v[136:139], v[202:205], 0\n"
        "ds_read_b128 v[138:141], v11 offset:3072\n"
        "v_cndmask_b32_dpp v254, v252, v254, vcc row_ror:4 row_mask:0xf bank_mask:0xf\n"
        "v_mfma_f32_16x16x32_bf16 v[172:175], v[118:121], v[194:197], v[172:175]\n"
        "ds_read_b128 v[106:109], v9 offset:6144\n"
        "v_cndmask_b32_dpp v253, v254, v253, vcc row_ror:4 row_mask:0xf bank_mask:0xf\n"
        "v_mfma_f32_16x16x32_bf16 v[172:175], v[110:113], v[210:213], v[172:175]\n"
        "ds_read_b128 v[112:115], v10 offset:6144\n"
        "v_fma_f32 v255, -v255, v248, v253\n"
        "v_mov_b32 v254, s29\n"
        "v_max_f32 v255, 0, v255\n"
        "v_sub_f32 v253, 0x47c00000, v253\n"
        "v_fmaak_f32 v255, v28, v255, 0x3727c5ac\n"
        "v_bfi_b32 v255, s84, v253, v255\n"
        "v_cvt_pk_bf16_f32 v162, v176, v177\n"
        "v_cvt_pk_bf16_f32 v163, v178, v179\n"
        "v_rsq_f32 v253, v255\n"
        "v_cvt_pk_bf16_f32 v166, v142, v143\n"
        "v_cvt_f16_f32 v253, v253\n"
        "ds_write_b16 v18, v253\n"
        "ds_write_b16 v19, v253\n"
        "ds_write_b16 v22, v254 offset:4096\n"
        "v_cvt_pk_bf16_f32 v167, v144, v145\n"
        "v_mul_f32 v104, v104, v247\n"
        "v_mul_f32 v105, v105, v246\n"
        "ds_read_b128 v[118:121], v11 offset:6144\n"
        "v_mul_f32 v122, v122, v247\n"
        "v_mul_f32 v123, v123, v246\n"
        "v_cvt_pk_f16_f32 v104, v104, v122\n"
        "v_cvt_pk_f16_f32 v105, v105, v123\n"
        "v_perm_b32 v30, v104, v40, s54\n"
        "v_perm_b32 v36, v105, v41, s54\n"
        "v_perm_b32 v31, v104, v34, s55\n"
        "v_perm_b32 v37, v105, v35, s55\n"
        "ds_write_b64 v232, v[30:31] offset:12560\n"
        "ds_write_b64 v233, v[36:37] offset:16\n"
        "v_cvt_pk_bf16_f32 v164, v168, v169\n"
        "v_cvt_pk_bf16_f32 v165, v170, v171\n"
        "ds_write_b128 v228, v[162:165] offset:320\n"
        "v_cvt_pk_bf16_f32 v168, v172, v173\n"
        "v_cvt_pk_bf16_f32 v169, v174, v175\n"
        "ds_write_b128 v229, v[166:169] offset:320\n"
        "s_waitcnt lgkmcnt(0)\n"
        "s_nop 7\n"
        ".p2align 6\n"
        "TOP_%=:\n"
        "v_mfma_f32_16x16x32_bf16 v[198:201], v[146:149], v[146:149], 0\n"
        "s_mov_b32 s26, s89\n"
        "ds_read_b128 v[218:221], v20 offset:512\n"
        "s_mov_b32 s30, s29\n"
        "v_mfma_f32_16x16x32_bf16 v[198:201], v[152:155], v[152:155], v[198:201]\n"
        "s_add_u32 s88, s88, 32\n"
        "ds_read_b128 v[230:233], v20 offset:528\n"
        "s_add_u32 s89, s86, s51\n"
        "v_mfma_f32_16x16x32_bf16 v[198:201], v[158:161], v[158:161], v[198:201]\n"
        "s_add_u32 s29, s29, 1\n"
        "v_add_u32 v23, 784, v23\n"
        "s_cmp_ge_u32 s88, 73\n"
        "v_mfma_f32_16x16x32_bf16 v[34:37], v[100:103], v[158:161], 0\n"
        "s_mov_b32 s86, s87\n"
        "v_add_u32 v24, 784, v24\n"
        "v_cvt_pk_bf16_f32 v224, v202, v206\n"
        "v_mfma_f32_16x16x32_bf16 v[34:37], v[68:71], v[152:155], v[34:37]\n"
        "s_cselect_b32 s92, 588672, 0\n"
        "v_cvt_pk_bf16_f32 v236, v203, v207\n"
        "v_lshlrev_b32 v226, 16, v124\n"
        "v_mfma_f32_16x16x32_bf16 v[34:37], v[96:99], v[146:149], v[34:37]\n"
        "s_cselect_b32 s94, 73, 0\n"
        "s_add_u32 s27, s26, 392448\n"
        "s_add_u32 s28, s26, 490560\n"
        "v_mfma_f32_16x16x32_bf16 v[214:217], a[0:3], v[146:149], 0\n"
        "buffer_store_dwordx3 v[222:224], v15, s[40:43], s27 offen offset:768 sc1 nt\n"
        "v_lshlrev_b32 v227, 16, v176\n"
        "v_lshlrev_b32 v228, 16, v196\n"
        "v_cvt_pk_bf16_f32 v252, v204, v208\n"
        "v_lshlrev_b32 v229, 16, v192\n"
        "v_mfma_f32_16x16x32_bf16 v[214:217], a[4:7], v[152:155], v[214:217]\n"
        "buffer_store_dwordx3 v[234:236], v14, s[40:43], s28 offen offset:192 sc1 nt\n"
        "s_add_u32 s87, s87, 43008\n"
        "v_lshlrev_b32 v206, 16, v172\n"
        "v_lshlrev_b32 v207, 16, v188\n"
        "v_cvt_pk_bf16_f32 v248, v205, v209\n"
        "v_mfma_f32_16x16x32_bf16 v[214:217], a[8:11], v[158:161], v[214:217]\n"
        "s_sub_u32 s88, s88, s94\n"
        "v_cndmask_b32_e64 v254, v198, v199, s[56:57]\n"
        "v_cndmask_b32_e64 v254, v254, v200, s[58:59]\n"
        "v_mfma_f32_16x16x32_bf16 v[222:225], a[20:23], v[158:161], 0\n"
        "s_add_u32 s87, s87, s92\n"
        "v_cndmask_b32_e64 v254, v254, v201, s[60:61]\n"
        "v_mfma_f32_16x16x32_bf16 v[222:225], a[16:19], v[152:155], v[222:225]\n"
        "buffer_store_dwordx3 v[250:252], v16, s[40:43], s28 offen offset:960 sc1 nt\n"
        "v_cvt_pk_bf16_f32 v212, v214, v215\n"
        "v_cvt_pk_bf16_f32 v213, v216, v217\n"
        "v_mfma_f32_16x16x32_bf16 v[222:225], a[12:15], v[146:149], v[222:225]\n"
        "s_waitcnt lgkmcnt(0)\n"
        "v_pk_mul_f16 v198, v218, v142\n"
        "v_pk_mul_f16 v199, v218, v162\n"
        "v_mfma_f32_16x16x32_bf16 v[208:211], a[24:27], v[146:149], 0\n"
        "s_add_u32 s27, s26, 588672\n"
        "v_pk_mul_f16 v200, v218, v178\n"
        "v_pk_mul_f16 v201, v218, v166\n"
        "v_mfma_f32_16x16x32_bf16 v[208:211], a[28:31], v[152:155], v[208:211]\n"
        "buffer_store_dwordx3 v[246:248], v14, s[40:43], s27 offen offset:384 sc1 nt\n"
        "s_cmp_gt_i32 s85, 0\n"
        "v_pk_mul_f16 v202, v218, v182\n"
        "v_pk_mul_f16 v203, v218, v40\n"
        "v_pk_fma_f16 v198, v219, v143, v198\n"
        "v_mfma_f32_16x16x32_bf16 v[208:211], a[32:35], v[158:161], v[208:211]\n"
        "s_cselect_b32 s94, s87, s84\n"
        "v_pk_fma_f16 v199, v219, v163, v199\n"
        "v_pk_fma_f16 v200, v219, v179, v200\n"
        "v_mfma_f32_16x16x32_bf16 v[234:237], a[44:47], v[158:161], 0\n"
        "s_sub_u32 s85, s85, 1\n"
        "v_pk_fma_f16 v201, v219, v167, v201\n"
        "v_pk_fma_f16 v202, v219, v183, v202\n"
        "v_mfma_f32_16x16x32_bf16 v[234:237], a[40:43], v[152:155], v[234:237]\n"
        "s_waitcnt lgkmcnt(0)\n"
        "v_pk_fma_f16 v203, v219, v41, v203\n"
        "v_pk_fma_f16 v198, v220, v144, v198\n"
        "v_mfma_f32_16x16x32_bf16 v[234:237], a[36:39], v[146:149], v[234:237]\n"
        "s_waitcnt lgkmcnt(0)\n"
        "v_pk_fma_f16 v199, v220, v164, v199\n"
        "v_pk_fma_f16 v200, v220, v180, v200\n"
        "v_mfma_f32_16x16x32_bf16 v[216:219], a[48:51], v[146:149], 0\n"
        "v_pk_fma_f16 v201, v220, v168, v201\n"
        "v_pk_fma_f16 v202, v220, v184, v202\n"
        "v_mfma_f32_16x16x32_bf16 v[216:219], a[52:55], v[152:155], v[216:219]\n"
        "v_pk_fma_f16 v203, v220, v42, v203\n"
        "v_pk_fma_f16 v198, v221, v145, v198\n"
        "v_mfma_f32_16x16x32_bf16 v[216:219], a[56:59], v[158:161], v[216:219]\n"
        "v_pk_fma_f16 v199, v221, v165, v199\n"
        "v_pk_fma_f16 v200, v221, v181, v200\n"
        "v_mfma_f32_16x16x32_bf16 v[162:165], a[68:71], v[158:161], 0\n"
        "v_pk_fma_f16 v201, v221, v169, v201\n"
        "v_pk_fma_f16 v202, v221, v185, v202\n"
        "v_mfma_f32_16x16x32_bf16 v[162:165], a[64:67], v[152:155], v[162:165]\n"
        "v_pk_fma_f16 v203, v221, v43, v203\n"
        "v_pk_fma_f16 v198, v230, v122, v198\n"
        "v_mfma_f32_16x16x32_bf16 v[162:165], a[60:63], v[146:149], v[162:165]\n"
        "v_pk_fma_f16 v199, v230, v174, v199\n"
        "v_cvt_pk_bf16_f32 v214, v222, v223\n"
        "v_mfma_f32_16x16x32_bf16 v[166:169], v[126:129], v[126:129], 0\n"
        "v_cvt_pk_bf16_f32 v215, v224, v225\n"
        "v_cvt_pk_bf16_f32 v178, v208, v209\n"
        "v_mfma_f32_16x16x32_bf16 v[166:169], v[132:135], v[132:135], v[166:169]\n"
        "v_cvt_pk_bf16_f32 v179, v210, v211\n"
        "v_cvt_pk_bf16_f32 v180, v234, v235\n"
        "v_mfma_f32_16x16x32_bf16 v[166:169], v[138:141], v[138:141], v[166:169]\n"
        "v_cvt_pk_bf16_f32 v181, v236, v237\n"
        "v_cvt_pk_bf16_f32 v182, v216, v217\n"
        "v_mfma_f32_16x16x32_bf16 v[142:145], v[100:103], v[138:141], 0\n"
        "v_cvt_pk_bf16_f32 v183, v218, v219\n"
        "v_cvt_pk_bf16_f32 v184, v162, v163\n"
        "v_mfma_f32_16x16x32_bf16 v[142:145], v[68:71], v[132:135], v[142:145]\n"
        "v_cvt_pk_bf16_f32 v185, v164, v165\n"
        "v_pk_fma_f16 v200, v230, v194, v200\n"
        "v_mfma_f32_16x16x32_bf16 v[142:145], v[96:99], v[126:129], v[142:145]\n"
        "v_pk_fma_f16 v201, v230, v190, v201\n"
        "v_pk_fma_f16 v202, v230, v170, v202\n"
        "v_mfma_f32_16x16x32_bf16 v[40:43], v[146:149], v[212:215], 0\n"
        "s_cmp_lg_u32 s15, 1\n"
        "v_pk_fma_f16 v203, v230, v186, v203\n"
        "v_pk_fma_f16 v198, v231, v123, v198\n"
        "v_mfma_f32_16x16x32_bf16 v[40:43], v[152:155], v[178:181], v[40:43]\n"
        "s_cbranch_scc1 ZZ_%=\n"
        "ZB_%=:\n"
        "v_pk_fma_f16 v199, v231, v175, v199\n"
        "v_pk_fma_f16 v200, v231, v195, v200\n"
        "v_mfma_f32_16x16x32_bf16 v[40:43], v[158:161], v[182:185], v[40:43]\n"
        "v_pk_fma_f16 v201, v231, v191, v201\n"
        "v_pk_fma_f16 v202, v231, v171, v202\n"
        "v_mfma_f32_16x16x32_bf16 v[162:165], a[8:11], v[138:141], 0\n"
        "v_pk_fma_f16 v203, v231, v187, v203\n"
        "v_pk_fma_f16 v198, v232, v124, v198\n"
        "v_mfma_f32_16x16x32_bf16 v[162:165], a[4:7], v[132:135], v[162:165]\n"
        "v_pk_fma_f16 v199, v232, v176, v199\n"
        "v_pk_fma_f16 v200, v232, v196, v200\n"
        "v_mfma_f32_16x16x32_bf16 v[162:165], a[0:3], v[126:129], v[162:165]\n"
        "v_pk_fma_f16 v201, v232, v192, v201\n"
        "v_pk_fma_f16 v202, v232, v172, v202\n"
        "v_mfma_f32_16x16x32_bf16 v[170:173], a[12:15], v[126:129], 0\n"
        "s_nop 0\n"
        "v_pk_fma_f16 v203, v232, v188, v203\n"
        "v_cndmask_b32_e64 v255, v166, v167, s[56:57]\n"
        "v_mfma_f32_16x16x32_bf16 v[170:173], a[16:19], v[132:135], v[170:173]\n"
        "s_add_u32 s46, s26, 590016\n"
        "v_cvt_pk_bf16_f32 v122, v162, v163\n"
        "v_cvt_pk_bf16_f32 v123, v164, v165\n"
        "v_mfma_f32_16x16x32_bf16 v[170:173], a[20:23], v[138:141], v[170:173]\n"
        "s_nop 0\n"
        "v_cndmask_b32_e64 v255, v255, v168, s[58:59]\n"
        "v_cndmask_b32_e64 v255, v255, v169, s[60:61]\n"
        "v_mfma_f32_16x16x32_f16 v[226:229], v[56:59], v[198:201], v[226:229]\n"
        "s_min_u32 s46, s46, s42\n"
        "s_nop 0\n"
        "v_mfma_f32_16x16x32_f16 v[204:207], v[56:59], v[200:203], v[204:207]\n"
        "s_nop 0\n"
        "v_cvt_pk_bf16_f32 v124, v170, v171\n"
        "v_cvt_pk_bf16_f32 v125, v172, v173\n"
        "v_mfma_f32_16x16x32_bf16 v[164:167], a[32:35], v[138:141], 0\n"
        "s_nop 0\n"
        "v_cvt_pk_bf16_f32 v174, v226, v227\n"
        "v_cvt_pk_bf16_f32 v175, v228, v229\n"
        "v_mfma_f32_16x16x32_bf16 v[164:167], a[28:31], v[132:135], v[164:167]\n"
        "v_cvt_pk_bf16_f32 v176, v206, v207\n"
        "v_mfma_f32_16x16x32_bf16 v[164:167], a[24:27], v[126:129], v[164:167]\n"
        "s_nop 1\n"
        "v_mfma_f32_16x16x32_bf16 v[170:173], a[36:39], v[126:129], 0\n"
        "s_nop 2\n"
        "v_mfma_f32_16x16x32_bf16 v[170:173], a[40:43], v[132:135], v[170:173]\n"
        "buffer_store_dwordx3 v[174:176], v17, s[44:47], s27 offen offset:1152 sc1 nt\n"
        "v_cvt_pk_bf16_f32 v162, v164, v165\n"
        "v_cvt_pk_bf16_f32 v163, v166, v167\n"
        "v_mfma_f32_16x16x32_bf16 v[170:173], a[44:47], v[138:141], v[170:173]\n"
        "s_nop 1\n"
        "v_mfma_f32_16x16x32_bf16 v[166:169], a[56:59], v[138:141], 0\n"
        "s_nop 2\n"
        "v_mfma_f32_16x16x32_bf16 v[166:169], a[52:55], v[132:135], v[166:169]\n"
        "s_nop 0\n"
        "v_cvt_pk_bf16_f32 v164, v170, v171\n"
        "v_cvt_pk_bf16_f32 v165, v172, v173\n"
        "v_mfma_f32_16x16x32_bf16 v[166:169], a[48:51], v[126:129], v[166:169]\n"
        "s_nop 1\n"
        "v_mfma_f32_16x16x32_bf16 v[170:173], a[60:63], v[126:129], 0\n"
        "s_nop 2\n"
        "v_mfma_f32_16x16x32_bf16 v[170:173], a[64:67], v[132:135], v[170:173]\n"
        "s_nop 0\n"
        "v_cvt_pk_bf16_f32 v178, v166, v167\n"
        "v_cvt_pk_bf16_f32 v179, v168, v169\n"
        "v_mfma_f32_16x16x32_bf16 v[170:173], a[68:71], v[138:141], v[170:173]\n"
        "s_nop 0\n"
        "ds_read_u16 v252, v19 offset:3072\n"
        "v_mfma_f32_16x16x32_bf16 v[166:169], v[106:109], v[106:109], 0\n"
        "s_nop 2\n"
        "v_mfma_f32_16x16x32_bf16 v[166:169], v[112:115], v[112:115], v[166:169]\n"
        "s_nop 0\n"
        "v_cvt_pk_bf16_f32 v180, v170, v171\n"
        "v_cvt_pk_bf16_f32 v181, v172, v173\n"
        "v_mfma_f32_16x16x32_bf16 v[166:169], v[118:121], v[118:121], v[166:169]\n"
        "s_nop 1\n"
        "v_mfma_f32_16x16x32_bf16 v[170:173], v[100:103], v[118:121], 0\n"
        "s_nop 2\n"
        "v_mfma_f32_16x16x32_bf16 v[170:173], v[68:71], v[112:115], v[170:173]\n"
        "s_nop 0\n"
        "v_cndmask_b32_e64 v253, v166, v167, s[56:57]\n"
        "v_cndmask_b32_e64 v253, v253, v168, s[58:59]\n"
        "v_mfma_f32_16x16x32_bf16 v[170:173], v[96:99], v[106:109], v[170:173]\n"
        "v_cndmask_b32_e64 v253, v253, v169, s[60:61]\n"
        "v_mfma_f32_16x16x32_bf16 v[174:177], v[126:129], v[122:125], 0\n"
        "s_nop 0\n"
        "v_cndmask_b32_dpp v255, v253, v255, vcc row_ror:4 row_mask:0xf bank_mask:0xf\n"
        "v_mfma_f32_16x16x32_bf16 v[174:177], v[132:135], v[162:165], v[174:177]\n"
        "s_waitcnt lgkmcnt(0)\n"
        "v_cmp_ne_u32_e64 s[24:25], s30, v252\n"
        "v_cndmask_b32_dpp v254, v255, v254, vcc row_ror:4 row_mask:0xf bank_mask:0xf\n"
        "v_mfma_f32_16x16x32_bf16 v[174:177], v[138:141], v[178:181], v[174:177]\n"
        "v_cndmask_b32_dpp v253, v170, v142, vcc row_ror:4 row_mask:0xf bank_mask:0xf\n"
        "v_cndmask_b32_dpp v255, v171, v143, vcc row_ror:4 row_mask:0xf bank_mask:0xf\n"
        "v_mfma_f32_16x16x32_bf16 v[170:173], a[8:11], v[118:121], 0\n"
        "s_add_u32 m0, s52, 0\n"
        "v_cndmask_b32_dpp v253, v253, v34, vcc row_ror:4 row_mask:0xf bank_mask:0xf\n"
        "v_mul_f32 v251, s80, v253\n"
        "v_mfma_f32_16x16x32_bf16 v[170:173], a[4:7], v[112:115], v[170:173]\n"
        "s_cmp_lg_u64 s[24:25], 0\n"
        "ds_read_b128 v[166:169], v23\n"
        "v_fma_f32 v251, -v251, v253, v254\n"
        "v_mfma_f32_16x16x32_bf16 v[170:173], a[0:3], v[106:109], v[170:173]\n"
        "s_cbranch_scc1 SP4_%=\n"
        "BK4_%=:\n"
        "buffer_load_dwordx4 v0, s[36:39], s94 offen lds\n"
        "v_max_f32 v251, 0, v251\n"
        "v_mfma_f32_16x16x32_bf16 v[162:165], a[12:15], v[106:109], 0\n"
        "ds_read_b128 v[122:125], v23 offset:64\n"
        "ds_read_b128 v[142:145], v23 offset:128\n"
        "v_mfma_f32_16x16x32_bf16 v[162:165], a[16:19], v[112:115], v[162:165]\n"
        "ds_read_b64 v[36:37], v24 offset:360\n"
        "v_mov_b32 v254, s29\n"
        "v_mfma_f32_16x16x32_bf16 v[162:165], a[20:23], v[118:121], v[162:165]\n"
        "ds_read_b128 v[222:225], v13\n"
        "v_cvt_pk_bf16_f32 v194, v170, v171\n"
        "v_mfma_f32_16x16x32_bf16 v[178:181], a[32:35], v[118:121], 0\n"
        "ds_read_b128 v[206:209], v13 offset:1024\n"
        "ds_read_b128 v[218:221], v13 offset:2048\n"
        "v_mfma_f32_16x16x32_bf16 v[178:181], a[28:31], v[112:115], v[178:181]\n"
        "ds_read_b128 v[226:229], v13 offset:3072\n"
        "v_cvt_pk_bf16_f32 v195, v172, v173\n"
        "v_mfma_f32_16x16x32_bf16 v[178:181], a[24:27], v[106:109], v[178:181]\n"
        "buffer_load_dwordx4 v1, s[36:39], s94 offen offset:1024 lds\n"
        "v_cvt_pk_bf16_f32 v196, v162, v163\n"
        "v_mfma_f32_16x16x32_bf16 v[186:189], a[36:39], v[106:109], 0\n"
        "ds_read_b128 v[234:237], v13 offset:4096\n"
        "ds_read_b128 v[230:233], v13 offset:5120\n"
        "v_mfma_f32_16x16x32_bf16 v[186:189], a[40:43], v[112:115], v[186:189]\n"
        "ds_read_b128 v[202:205], v13 offset:6144\n"
        "ds_read_b128 v[210:213], v13 offset:7168\n"
        "v_mfma_f32_16x16x32_bf16 v[186:189], a[44:47], v[118:121], v[186:189]\n"
        "ds_read_b128 v[198:201], v13 offset:8192\n"
        "ds_read_b128 v[214:217], v20\n"
        "v_mfma_f32_16x16x32_bf16 v[182:185], a[56:59], v[118:121], 0\n"
        "s_waitcnt lgkmcnt(10)\n"
        "ds_read_b64 v[246:247], v20 offset:16\n"
        "ds_read_u16 v250, v22\n"
        "v_mfma_f32_16x16x32_bf16 v[182:185], a[52:55], v[112:115], v[182:185]\n"
        "ds_read_b64 v[172:173], v21\n"
        "v_readlane_b32 s93, v37, 1\n"
        "v_mfma_f32_16x16x32_bf16 v[182:185], a[48:51], v[106:109], v[182:185]\n"
        "s_waitcnt lgkmcnt(10)\n"
        "buffer_load_dwordx4 v2, s[36:39], s94 offen offset:2048 lds\n"
        "v_cvt_pk_bf16_f32 v197, v164, v165\n"
        "v_mfma_f32_16x16x32_bf16 v[162:165], a[60:63], v[106:109], 0\n"
        "ds_write_b16 v22, v254 offset:4608\n"
        "v_mfma_f32_16x16x32_bf16 v[162:165], a[64:67], v[112:115], v[162:165]\n"
        "v_cvt_pk_bf16_f32 v190, v178, v179\n"
        "v_cvt_pk_bf16_f32 v191, v180, v181\n"
        "v_mfma_f32_16x16x32_bf16 v[162:165], a[68:71], v[118:121], v[162:165]\n"
        "s_waitcnt lgkmcnt(8)\n"
        "v_cvt_pk_bf16_f32 v192, v186, v187\n"
        "v_cvt_pk_bf16_f32 v193, v188, v189\n"
        "v_mfma_f32_16x16x32_bf16 v[238:241], v[166:169], v[222:225], 0\n"
        "v_cvt_pk_bf16_f32 v186, v182, v183\n"
        "v_cvt_pk_bf16_f32 v187, v184, v185\n"
        "v_mfma_f32_16x16x32_bf16 v[238:241], v[122:125], v[206:209], v[238:241]\n"
        "s_nop 0\n"
        "v_cvt_pk_bf16_f32 v188, v162, v163\n"
        "v_cvt_pk_bf16_f32 v189, v164, v165\n"
        "v_mfma_f32_16x16x32_bf16 v[238:241], v[142:145], v[218:221], v[238:241]\n"
        "buffer_load_dwordx4 v3, s[36:39], s94 offen offset:3072 lds\n"
        "v_cndmask_b32_e64 v36, 0, v36, s[72:73]\n"
        "v_mfma_f32_16x16x32_bf16 v[162:165], v[142:145], v[230:233], 0\n"
        "s_waitcnt lgkmcnt(1)\n"
        "v_fmaak_f32 v251, v28, v251, 0x3727c5ac\n"
        "v_cndmask_b32_dpp v255, v255, v35, vcc row_ror:4 row_mask:0xf bank_mask:0xf\n"
        "v_mfma_f32_16x16x32_bf16 v[162:165], v[122:125], v[234:237], v[162:165]\n"
        "v_rsq_f32 v244, v251\n"
        "v_mfma_f32_16x16x32_bf16 v[162:165], v[166:169], v[226:229], v[162:165]\n"
        "s_nop 1\n"
        "v_mfma_f32_16x16x32_bf16 v[178:181], v[166:169], v[202:205], 0\n"
        "s_add_u32 m0, s52, 4096\n"
        "s_nop 1\n"
        "v_mfma_f32_16x16x32_bf16 v[178:181], v[122:125], v[210:213], v[178:181]\n"
        "s_nop 0\n"
        "v_cndmask_b32_e64 v245, v238, v162, s[64:65]\n"
        "v_cndmask_b32_e64 v251, v239, v163, s[64:65]\n"
        "v_mfma_f32_16x16x32_bf16 v[178:181], v[142:145], v[198:201], v[178:181]\n"
        "s_nop 0\n"
        "buffer_load_dwordx4 v4, s[36:39], s94 offen lds\n"
        "v_mfma_f32_16x16x32_bf16 v[142:145], v[106:109], v[194:197], 0\n"
        "s_nop 2\n"
        "v_mfma_f32_16x16x32_bf16 v[142:145], v[112:115], v[190:193], v[142:145]\n"
        "s_nop 0\n"
        "v_cndmask_b32_e64 v245, v245, v178, s[70:71]\n"
        "v_cndmask_b32_e64 v251, v251, v179, s[70:71]\n"
        "v_mfma_f32_16x16x32_bf16 v[142:145], v[118:121], v[186:189], v[142:145]\n"
        "v_cvt_pk_f16_f32 v245, v245, v251\n"
        "v_pk_mul_f16 v245, v245, v250 op_sel_hi:[1,0]\n"
        "v_mfma_f32_16x16x32_bf16 v[162:165], a[152:155], v[218:221], 0\n"
        "v_pk_mul_f16 v245, v245, s93 op_sel_hi:[1,0]\n"
        "v_mfma_f32_16x16x32_bf16 v[162:165], a[148:151], v[206:209], v[162:165]\n"
        "v_exp_f16_sdwa v245, v245 dst_sel:WORD_0 dst_unused:UNUSED_PRESERVE src0_sel:WORD_0\n"
        "v_mfma_f32_16x16x32_bf16 v[162:165], a[144:147], v[222:225], v[162:165]\n"
        "buffer_load_dwordx4 v5, s[36:39], s94 offen offset:1024 lds\n"
        "v_cndmask_b32_dpp v252, v142, v174, vcc row_ror:4 row_mask:0xf bank_mask:0xf\n"
        "v_mfma_f32_16x16x32_bf16 v[122:125], a[156:159], v[222:225], 0\n"
        "v_cndmask_b32_dpp v254, v143, v175, vcc row_ror:4 row_mask:0xf bank_mask:0xf\n"
        "v_cndmask_b32_dpp v248, v144, v176, vcc row_ror:4 row_mask:0xf bank_mask:0xf\n"
        "v_mfma_f32_16x16x32_bf16 v[122:125], a[160:163], v[206:209], v[122:125]\n"
        "v_cndmask_b32_dpp v249, v145, v177, vcc row_ror:4 row_mask:0xf bank_mask:0xf\n"
        "v_cndmask_b32_dpp v252, v252, v40, vcc row_ror:4 row_mask:0xf bank_mask:0xf\n"
        "v_mfma_f32_16x16x32_bf16 v[122:125], a[164:167], v[218:221], v[122:125]\n"
        "v_cndmask_b32_dpp v254, v254, v41, vcc row_ror:4 row_mask:0xf bank_mask:0xf\n"
        "v_cndmask_b32_dpp v248, v248, v42, vcc row_ror:4 row_mask:0xf bank_mask:0xf\n"
        "v_mfma_f32_16x16x32_bf16 v[176:179], a[176:179], v[218:221], 0\n"
        "v_cndmask_b32_dpp v249, v249, v43, vcc row_ror:4 row_mask:0xf bank_mask:0xf\n"
        "v_mul_f32_dpp v252, v244, v252 quad_perm:[0,0,0,0] row_mask:0xf bank_mask:0xf\n"
        "v_mfma_f32_16x16x32_bf16 v[176:179], a[172:175], v[206:209], v[176:179]\n"
        "v_mul_f32_dpp v254, v244, v254 quad_perm:[1,1,1,1] row_mask:0xf bank_mask:0xf\n"
        "v_mul_f32_dpp v248, v244, v248 quad_perm:[2,2,2,2] row_mask:0xf bank_mask:0xf\n"
        "v_mfma_f32_16x16x32_bf16 v[176:179], a[168:171], v[222:225], v[176:179]\n"
        "buffer_load_dwordx4 v6, s[36:39], s94 offen offset:2048 lds\n"
        "v_mul_f32_dpp v249, v244, v249 quad_perm:[3,3,3,3] row_mask:0xf bank_mask:0xf\n"
        "v_mfma_f32_16x16x32_bf16 v[142:145], a[180:183], v[222:225], 0\n"
        "v_max3_f32 v251, v252, v254, v248\n"
        "v_max_f32 v251, v251, v249\n"
        "v_mfma_f32_16x16x32_bf16 v[142:145], a[184:187], v[206:209], v[142:145]\n"
        "v_mul_f32 v251, v251, v244\n"
        "v_fma_f32 v252, v252, v244, -v251\n"
        "v_mfma_f32_16x16x32_bf16 v[142:145], a[188:191], v[218:221], v[142:145]\n"
        "v_fma_f32 v254, v254, v244, -v251\n"
        "v_fma_f32 v248, v248, v244, -v251\n"
        "v_mfma_f32_16x16x32_bf16 v[166:169], a[200:203], v[218:221], 0\n"
        "v_fma_f32 v249, v249, v244, -v251\n"
        "v_cvt_pk_bf16_f32 v40, v162, v163\n"
        "v_mfma_f32_16x16x32_bf16 v[166:169], a[196:199], v[206:209], v[166:169]\n"
        "v_exp_f32 v252, v252\n"
        "v_mfma_f32_16x16x32_bf16 v[166:169], a[192:195], v[222:225], v[166:169]\n"
        "buffer_load_dwordx4 v7, s[36:39], s94 offen offset:3072 lds\n"
        "v_cvt_pk_bf16_f32 v41, v164, v165\n"
        "v_mfma_f32_16x16x32_bf16 v[162:165], a[204:207], v[222:225], 0\n"
        "v_cvt_pk_bf16_f32 v42, v122, v123\n"
        "v_cvt_pk_bf16_f32 v43, v124, v125\n"
        "v_mfma_f32_16x16x32_bf16 v[162:165], a[208:211], v[206:209], v[162:165]\n"
        "v_cvt_pk_bf16_f32 v174, v176, v177\n"
        "v_cvt_pk_bf16_f32 v175, v178, v179\n"
        "v_mfma_f32_16x16x32_bf16 v[162:165], a[212:215], v[218:221], v[162:165]\n"
        "v_cvt_pk_bf16_f32 v176, v142, v143\n"
        "v_cvt_pk_bf16_f32 v177, v144, v145\n"
        "v_mfma_f32_16x16x32_bf16 v[178:181], v[158:161], a[80:83], 0\n"
        "s_add_u32 m0, s52, 8192\n"
        "v_cvt_pk_bf16_f32 v142, v166, v167\n"
        "v_cvt_pk_bf16_f32 v143, v168, v169\n"
        "v_mfma_f32_16x16x32_bf16 v[178:181], v[152:155], a[76:79], v[178:181]\n"
        "s_nop 0\n"
        "v_cvt_pk_bf16_f32 v144, v162, v163\n"
        "v_cvt_pk_bf16_f32 v145, v164, v165\n"
        "v_mfma_f32_16x16x32_bf16 v[178:181], v[146:149], a[72:75], v[178:181]\n"
        "buffer_load_dwordx4 v8, s[36:39], s94 offen lds\n"
        "v_mfma_f32_16x16x32_bf16 v[122:125], v[146:149], a[84:87], 0\n"
        "v_exp_f32 v254, v254\n"
        "v_mfma_f32_16x16x32_bf16 v[122:125], v[152:155], a[88:91], v[122:125]\n"
        "v_exp_f32 v248, v248\n"
        "v_mfma_f32_16x16x32_bf16 v[122:125], v[158:161], a[92:95], v[122:125]\n"
        "v_exp_f32 v249, v249\n"
        "v_mfma_f32_16x16x32_bf16 v[182:185], v[222:225], v[40:43], 0\n"
        "v_add_f32 v251, v252, v254\n"
        "v_add_f32 v243, v248, v249\n"
        "v_mfma_f32_16x16x32_bf16 v[182:185], v[206:209], v[174:177], v[182:185]\n"
        "v_add_f32 v251, v251, v243\n"
        "v_cvt_pk_bf16_f32 v30, v178, v179\n"
        "v_mfma_f32_16x16x32_bf16 v[182:185], v[218:221], v[142:145], v[182:185]\n"
        "v_rcp_f32 v251, v251\n"
        "v_mfma_f32_16x16x32_bf16 v[164:167], v[230:233], v[142:145], 0\n"
        "v_exp_f16_sdwa v245, v245 dst_sel:WORD_1 dst_unused:UNUSED_PRESERVE src0_sel:WORD_1\n"
        "v_mfma_f32_16x16x32_bf16 v[164:167], v[234:237], v[174:177], v[164:167]\n"
        "v_mul_f32 v252, v252, v251\n"
        "v_mul_f32 v254, v254, v251\n"
        "v_mfma_f32_16x16x32_bf16 v[164:167], v[226:229], v[40:43], v[164:167]\n"
        "v_mul_f32 v248, v248, v251\n"
        "v_mul_f32 v249, v249, v251\n"
        "v_mfma_f32_16x16x32_bf16 v[168:171], v[202:205], v[40:43], 0\n"
        "v_mul_f32_dpp v252, v244, v252 quad_perm:[0,0,0,0] row_mask:0xf bank_mask:0xf\n"
        "v_mul_f32_dpp v254, v244, v254 quad_perm:[1,1,1,1] row_mask:0xf bank_mask:0xf\n"
        "v_mfma_f32_16x16x32_bf16 v[168:171], v[210:213], v[174:177], v[168:171]\n"
        "v_mul_f32_dpp v248, v244, v248 quad_perm:[2,2,2,2] row_mask:0xf bank_mask:0xf\n"
        "v_mul_f32_dpp v249, v244, v249 quad_perm:[3,3,3,3] row_mask:0xf bank_mask:0xf\n"
        "v_mfma_f32_16x16x32_bf16 v[168:171], v[198:201], v[142:145], v[168:171]\n"
        "v_cvt_pk_bf16_f32 v197, v252, v254\n"
        "v_cvt_pk_bf16_f32 v244, v248, v249\n"
        "v_mfma_f32_16x16x32_bf16 v[174:177], v[158:161], a[104:107], 0\n"
        "v_and_b32 v144, v25, v197\n"
        "v_and_b32 v145, v25, v244\n"
        "v_mfma_f32_16x16x32_bf16 v[174:177], v[152:155], a[100:103], v[174:177]\n"
        "v_mov_b64 v[150:151], v[144:145]\n"
        "v_cvt_pk_bf16_f32 v31, v180, v181\n"
        "v_mfma_f32_16x16x32_bf16 v[174:177], v[146:149], a[96:99], v[174:177]\n"
        "v_cvt_pk_bf16_f32 v34, v122, v123\n"
        "v_cvt_pk_bf16_f32 v35, v124, v125\n"
        "v_mfma_f32_16x16x32_bf16 v[122:125], v[146:149], a[108:111], 0\n"
        "v_cvt_f32_f16 v243, v245\n"
        "v_cndmask_b32_e64 v243, v243, v36, s[68:69]\n"
        "v_mfma_f32_16x16x32_bf16 v[122:125], v[152:155], a[112:115], v[122:125]\n"
        "s_nop 0\n"
        "v_add_f32_dpp v251, v243, v243 row_ror:8 row_mask:0xf bank_mask:0xf\n"
        "v_mov_b64 v[156:157], v[144:145]\n"
        "v_mfma_f32_16x16x32_bf16 v[122:125], v[158:161], a[116:119], v[122:125]\n"
        "v_add_f32_dpp v251, v251, v251 row_ror:4 row_mask:0xf bank_mask:0xf\n"
        "v_cvt_pk_bf16_f32 v42, v174, v175\n"
        "v_mfma_f32_16x16x32_bf16 v[178:181], v[30:33], v[144:147], 0\n"
        "ds_read_u16 v242, v19 offset:3584\n"
        "v_add_f32_dpp v251, v251, v251 row_ror:2 row_mask:0xf bank_mask:0xf\n"
        "v_mfma_f32_16x16x32_bf16 v[186:189], v[32:35], v[148:151], 0\n"
        "s_nop 0\n"
        "v_add_f32_dpp v251, v251, v251 row_ror:1 row_mask:0xf bank_mask:0xf\n"
        "v_cvt_pk_bf16_f32 v43, v176, v177\n"
        "v_mfma_f32_16x16x32_bf16 v[192:195], v[158:161], a[128:131], 0\n"
        "v_add_f32_dpp v251, v251, v251 row_bcast:15 row_mask:0xa bank_mask:0xf\n"
        "v_cvt_pk_bf16_f32 v46, v122, v123\n"
        "v_mfma_f32_16x16x32_bf16 v[192:195], v[152:155], a[124:127], v[192:195]\n"
        "v_cvt_pk_bf16_f32 v47, v124, v125\n"
        "v_add_f32_dpp v251, v251, v251 row_bcast:31 row_mask:0xc bank_mask:0xf\n"
        "v_mfma_f32_16x16x32_bf16 v[192:195], v[146:149], a[120:123], v[192:195]\n"
        "v_readlane_b32 s95, v251, 63\n"
        "v_mov_b64 v[162:163], v[144:145]\n"
        "v_mfma_f32_16x16x32_bf16 v[142:145], v[146:149], a[132:135], 0\n"
        "v_rcp_f32 v191, s95\n"
        "v_mfma_f32_16x16x32_bf16 v[142:145], v[152:155], a[136:139], v[142:145]\n"
        "v_mul_f32 v243, v243, v191\n"
        "v_cvt_f16_f32 v243, v243\n"
        "v_mfma_f32_16x16x32_bf16 v[142:145], v[158:161], a[140:143], v[142:145]\n"
        "v_mul_f16 v243, v243, v250\n"
        "v_and_b32_dpp v124, v197, v25 row_ror:12 row_mask:0xf bank_mask:0xf\n"
        "v_mfma_f32_16x16x32_bf16 v[174:177], v[42:45], v[150:153], 0\n"
        "s_waitcnt lgkmcnt(0)\n"
        "ds_write_b16 v22, v243 offset:512\n"
        "v_mfma_f32_16x16x32_bf16 v[146:149], v[44:47], v[154:157], 0\n"
        "v_cmp_ne_u32_e64 s[24:25], s29, v242\n"
        "v_and_b32_dpp v125, v244, v25 row_ror:12 row_mask:0xf bank_mask:0xf\n"
        "v_mfma_f32_16x16x32_bf16 v[150:153], v[138:141], a[80:83], 0\n"
        "v_cvt_pk_bf16_f32 v36, v192, v193\n"
        "v_cvt_pk_bf16_f32 v37, v194, v195\n"
        "v_mfma_f32_16x16x32_bf16 v[150:153], v[132:135], a[76:79], v[150:153]\n"
        "s_cmp_lg_u64 s[24:25], 0\n"
        "v_cvt_pk_bf16_f32 v40, v142, v143\n"
        "v_cvt_pk_bf16_f32 v41, v144, v145\n"
        "v_mfma_f32_16x16x32_bf16 v[150:153], v[126:129], a[72:75], v[150:153]\n"
        "s_cbranch_scc1 SP8_%=\n"
        "BK8_%=:\n"
        "v_mov_b64 v[130:131], v[124:125]\n"
        "v_cvt_pk_bf16_f32 v238, v178, v179\n"
        "v_mfma_f32_16x16x32_bf16 v[192:195], v[126:129], a[84:87], 0\n"
        "v_cvt_pk_bf16_f32 v154, v180, v181\n"
        "v_cvt_pk_bf16_f32 v178, v186, v187\n"
        "v_mfma_f32_16x16x32_bf16 v[192:195], v[132:135], a[88:91], v[192:195]\n"
        "v_cvt_pk_bf16_f32 v239, v188, v189\n"
        "v_cvt_pk_bf16_f32 v155, v174, v175\n"
        "v_mfma_f32_16x16x32_bf16 v[192:195], v[138:141], a[92:95], v[192:195]\n"
        "v_cvt_pk_bf16_f32 v179, v176, v177\n"
        "v_cvt_pk_bf16_f32 v240, v146, v147\n"
        "v_mfma_f32_16x16x32_bf16 v[174:177], v[36:39], v[156:159], 0\n"
        "s_nop 0\n"
        "v_cvt_pk_bf16_f32 v30, v150, v151\n"
        "v_cvt_pk_bf16_f32 v31, v152, v153\n"
        "v_mfma_f32_16x16x32_bf16 v[142:145], v[38:41], v[160:163], 0\n"
        "s_nop 0\n"
        "v_cvt_pk_bf16_f32 v34, v192, v193\n"
        "v_cvt_pk_bf16_f32 v35, v194, v195\n"
        "v_mfma_f32_16x16x32_bf16 v[150:153], v[138:141], a[104:107], 0\n"
        "v_cvt_pk_bf16_f32 v241, v176, v177\n"
        "v_cvt_pk_bf16_f32 v156, v148, v149\n"
        "v_mfma_f32_16x16x32_bf16 v[150:153], v[132:135], a[100:103], v[150:153]\n"
        "ds_write_b128 v12, v[238:241]\n"
        "v_mfma_f32_16x16x32_bf16 v[150:153], v[126:129], a[96:99], v[150:153]\n"
        "v_cvt_pk_bf16_f32 v180, v174, v175\n"
        "v_cvt_pk_bf16_f32 v157, v142, v143\n"
        "v_mfma_f32_16x16x32_bf16 v[146:149], v[126:129], a[108:111], 0\n"
        "ds_write_b128 v12, v[154:157] offset:1024\n"
        "v_mfma_f32_16x16x32_bf16 v[146:149], v[132:135], a[112:115], v[146:149]\n"
        "v_cvt_pk_bf16_f32 v181, v144, v145\n"
        "v_and_b32 v251, 0xffff0000, v245\n"
        "v_mfma_f32_16x16x32_bf16 v[146:149], v[138:141], a[116:119], v[146:149]\n"
        "ds_write_b128 v12, v[178:181] offset:2048\n"
        "v_mfma_f32_16x16x32_bf16 v[158:161], v[30:33], v[124:127], 0\n"
        "s_nop 0\n"
        "v_mov_b64 v[136:137], v[124:125]\n"
        "v_cvt_pk_bf16_f32 v42, v150, v151\n"
        "v_mfma_f32_16x16x32_bf16 v[174:177], v[32:35], v[128:131], 0\n"
        "s_nop 0\n"
        "v_cvt_pk_bf16_f32 v43, v152, v153\n"
        "v_cvt_pk_bf16_f32 v46, v146, v147\n"
        "v_mfma_f32_16x16x32_bf16 v[150:153], v[238:241], v[238:241], 0\n"
        "v_cvt_pk_bf16_f32 v47, v148, v149\n"
        "v_mul_f32_dpp v250, v255, v252 quad_perm:[0,0,0,0] row_mask:0xf bank_mask:0xf\n"
        "v_mfma_f32_16x16x32_bf16 v[150:153], v[154:157], v[154:157], v[150:153]\n"
        "v_and_b32_dpp v104, v197, v25 row_ror:8 row_mask:0xf bank_mask:0xf\n"
        "v_and_b32_dpp v105, v244, v25 row_ror:8 row_mask:0xf bank_mask:0xf\n"
        "v_mfma_f32_16x16x32_bf16 v[150:153], v[178:181], v[178:181], v[150:153]\n"
        "v_mov_b64 v[142:143], v[124:125]\n"
        "v_mov_b64 v[110:111], v[104:105]\n"
        "v_mfma_f32_16x16x32_bf16 v[154:157], v[138:141], a[128:131], 0\n"
        "v_mov_b64 v[116:117], v[104:105]\n"
        "v_fmac_f32_dpp v250, v255, v254 quad_perm:[1,1,1,1] row_mask:0xf bank_mask:0xf\n"
        "v_mfma_f32_16x16x32_bf16 v[154:157], v[132:135], a[124:127], v[154:157]\n"
        "v_mov_b64 v[122:123], v[104:105]\n"
        "v_fmac_f32_dpp v250, v255, v248 quad_perm:[2,2,2,2] row_mask:0xf bank_mask:0xf\n"
        "v_mfma_f32_16x16x32_bf16 v[154:157], v[126:129], a[120:123], v[154:157]\n"
        "s_nop 0\n"
        "v_fmac_f32_dpp v250, v255, v249 quad_perm:[3,3,3,3] row_mask:0xf bank_mask:0xf\n"
        "v_add_f32 v250, v250, v253\n"
        "v_mfma_f32_16x16x32_bf16 v[178:181], v[126:129], a[132:135], 0\n"
        "v_cvt_pk_f16_f32 v146, v182, v183\n"
        "v_cvt_pk_f16_f32 v147, v184, v185\n"
        "v_mfma_f32_16x16x32_bf16 v[178:181], v[132:135], a[136:139], v[178:181]\n"
        "s_nop 0\n"
        "v_cvt_pk_bf16_f32 v36, v154, v155\n"
        "v_cvt_pk_bf16_f32 v37, v156, v157\n"
        "v_mfma_f32_16x16x32_bf16 v[178:181], v[138:141], a[140:143], v[178:181]\n"
        "v_cvt_pk_bf16_f32 v182, v158, v159\n"
        "v_cvt_pk_bf16_f32 v158, v160, v161\n"
        "v_mfma_f32_16x16x32_bf16 v[124:127], v[42:45], v[130:133], 0\n"
        "s_nop 0\n"
        "v_cvt_pk_bf16_f32 v128, v174, v175\n"
        "v_cvt_pk_bf16_f32 v183, v176, v177\n"
        "v_mfma_f32_16x16x32_bf16 v[130:133], v[44:47], v[134:137], 0\n"
        "s_nop 0\n"
        "v_cvt_pk_bf16_f32 v40, v178, v179\n"
        "v_cvt_pk_bf16_f32 v41, v180, v181\n"
        "v_mfma_f32_16x16x32_bf16 v[174:177], v[118:121], a[80:83], 0\n"
        "v_cvt_pk_bf16_f32 v159, v124, v125\n"
        "v_cvt_pk_bf16_f32 v129, v126, v127\n"
        "v_mfma_f32_16x16x32_bf16 v[174:177], v[112:115], a[76:79], v[174:177]\n"
        "s_nop 0\n"
        "v_cvt_pk_bf16_f32 v184, v130, v131\n"
        "v_cvt_pk_bf16_f32 v160, v132, v133\n"
        "v_mfma_f32_16x16x32_bf16 v[174:177], v[106:109], a[72:75], v[174:177]\n"
        "v_pk_mul_f16 v146, v146, v172 op_sel_hi:[1,0]\n"
        "v_pk_mul_f16 v147, v147, v172 op_sel_hi:[1,0]\n"
        "v_mfma_f32_16x16x32_bf16 v[154:157], v[106:109], a[84:87], 0\n"
        "s_nop 0\n"
        "v_pk_mul_f16 v146, v146, v214\n"
        "v_pk_mul_f16 v147, v147, v215\n"
        "v_mfma_f32_16x16x32_bf16 v[154:157], v[112:115], a[88:91], v[154:157]\n"
        "s_nop 0\n"
        "v_cvt_pk_bf16_f32 v30, v174, v175\n"
        "v_cvt_pk_bf16_f32 v31, v176, v177\n"
        "v_mfma_f32_16x16x32_bf16 v[154:157], v[118:121], a[92:95], v[154:157]\n"
        "v_exp_f16_sdwa v146, v146 dst_sel:WORD_0 dst_unused:UNUSED_PRESERVE src0_sel:WORD_0\n"
        "v_mfma_f32_16x16x32_bf16 v[132:135], v[36:39], v[136:139], 0\n"
        "v_exp_f16_sdwa v147, v147 dst_sel:WORD_0 dst_unused:UNUSED_PRESERVE src0_sel:WORD_0\n"
        "v_mfma_f32_16x16x32_bf16 v[124:127], v[38:41], v[140:143], 0\n"
        "s_nop 0\n"
        "v_exp_f16_sdwa v146, v146 dst_sel:WORD_1 dst_unused:UNUSED_PRESERVE src0_sel:WORD_1\n"
        "v_mfma_f32_16x16x32_bf16 v[140:143], v[118:121], a[104:107], 0\n"
        "s_nop 0\n"
        "v_cvt_pk_bf16_f32 v34, v154, v155\n"
        "v_cvt_pk_bf16_f32 v35, v156, v157\n"
        "v_mfma_f32_16x16x32_bf16 v[140:143], v[112:115], a[100:103], v[140:143]\n"
        "v_cvt_pk_bf16_f32 v185, v134, v135\n"
        "v_cvt_pk_bf16_f32 v130, v132, v133\n"
        "v_mfma_f32_16x16x32_bf16 v[140:143], v[106:109], a[96:99], v[140:143]\n"
        "ds_write_b128 v12, v[182:185] offset:3072\n"
        "v_mfma_f32_16x16x32_bf16 v[132:135], v[106:109], a[108:111], 0\n"
        "v_cvt_pk_bf16_f32 v161, v124, v125\n"
        "v_cvt_pk_bf16_f32 v131, v126, v127\n"
        "v_mfma_f32_16x16x32_bf16 v[132:135], v[112:115], a[112:115], v[132:135]\n"
        "ds_write_b128 v12, v[158:161] offset:4096\n"
        "v_mfma_f32_16x16x32_bf16 v[132:135], v[118:121], a[116:119], v[132:135]\n"
        "s_nop 0\n"
        "ds_write_b128 v12, v[128:131] offset:5120\n"
        "v_mfma_f32_16x16x32_bf16 v[136:139], v[30:33], v[104:107], 0\n"
        "s_nop 0\n"
        "v_cvt_pk_bf16_f32 v42, v140, v141\n"
        "v_cvt_pk_bf16_f32 v43, v142, v143\n"
        "v_mfma_f32_16x16x32_bf16 v[140:143], v[32:35], v[108:111], 0\n"
        "s_nop 0\n"
        "v_cvt_pk_bf16_f32 v46, v132, v133\n"
        "v_cvt_pk_bf16_f32 v47, v134, v135\n"
        "v_mfma_f32_16x16x32_bf16 v[132:135], v[182:185], v[182:185], 0\n"
        "v_exp_f16_sdwa v147, v147 dst_sel:WORD_1 dst_unused:UNUSED_PRESERVE src0_sel:WORD_1\n"
        "v_mfma_f32_16x16x32_bf16 v[132:135], v[158:161], v[158:161], v[132:135]\n"
        "v_mul_f32 v255, s80, v250\n"
        "v_pk_add_f16 v126, v146, v147\n"
        "v_mfma_f32_16x16x32_bf16 v[132:135], v[128:131], v[128:131], v[132:135]\n"
        "v_cvt_pk_f16_f32 v148, v164, v165\n"
        "v_cvt_pk_f16_f32 v149, v166, v167\n"
        "v_mfma_f32_16x16x32_bf16 v[158:161], v[118:121], a[128:131], 0\n"
        "v_pk_mul_f16 v148, v148, v172 op_sel_hi:[1,0]\n"
        "v_pk_mul_f16 v149, v149, v172 op_sel_hi:[1,0]\n"
        "v_mfma_f32_16x16x32_bf16 v[158:161], v[112:115], a[124:127], v[158:161]\n"
        "v_pk_mul_f16 v148, v148, v216\n"
        "v_pk_mul_f16 v149, v149, v217\n"
        "v_mfma_f32_16x16x32_bf16 v[158:161], v[106:109], a[120:123], v[158:161]\n"
        "s_nop 0\n"
        "v_exp_f16_sdwa v148, v148 dst_sel:WORD_0 dst_unused:UNUSED_PRESERVE src0_sel:WORD_0\n"
        "v_mfma_f32_16x16x32_bf16 v[154:157], v[106:109], a[132:135], 0\n"
        "s_nop 0\n"
        "v_cvt_pk_bf16_f32 v104, v136, v137\n"
        "v_cvt_pk_bf16_f32 v128, v138, v139\n"
        "v_mfma_f32_16x16x32_bf16 v[154:157], v[112:115], a[136:139], v[154:157]\n"
        "s_nop 0\n"
        "v_cvt_pk_bf16_f32 v36, v158, v159\n"
        "v_cvt_pk_bf16_f32 v37, v160, v161\n"
        "v_mfma_f32_16x16x32_bf16 v[154:157], v[118:121], a[140:143], v[154:157]\n"
        "s_nop 0\n"
        "v_cvt_pk_bf16_f32 v136, v140, v141\n"
        "v_cvt_pk_bf16_f32 v105, v142, v143\n"
        "v_mfma_f32_16x16x32_bf16 v[106:109], v[42:45], v[110:113], 0\n"
        "s_nop 0\n"
        "v_exp_f16_sdwa v149, v149 dst_sel:WORD_0 dst_unused:UNUSED_PRESERVE src0_sel:WORD_0\n"
        "v_mfma_f32_16x16x32_bf16 v[138:141], v[44:47], v[114:117], 0\n"
        "s_nop 0\n"
        "v_cvt_pk_bf16_f32 v40, v154, v155\n"
        "v_cvt_pk_bf16_f32 v41, v156, v157\n"
        "v_mfma_f32_16x16x32_bf16 v[112:115], a[152:155], v[230:233], 0\n"
        "s_nop 0\n"
        "v_cvt_pk_bf16_f32 v129, v106, v107\n"
        "v_cvt_pk_bf16_f32 v137, v108, v109\n"
        "v_mfma_f32_16x16x32_bf16 v[112:115], a[148:151], v[234:237], v[112:115]\n"
        "v_cvt_pk_bf16_f32 v106, v138, v139\n"
        "v_cvt_pk_bf16_f32 v130, v140, v141\n"
        "v_mfma_f32_16x16x32_bf16 v[112:115], a[144:147], v[226:229], v[112:115]\n"
        "v_exp_f16_sdwa v148, v148 dst_sel:WORD_1 dst_unused:UNUSED_PRESERVE src0_sel:WORD_1\n"
        "v_mfma_f32_16x16x32_bf16 v[108:111], v[36:39], v[116:119], 0\n"
        "s_nop 0\n"
        "v_exp_f16_sdwa v149, v149 dst_sel:WORD_1 dst_unused:UNUSED_PRESERVE src0_sel:WORD_1\n"
        "v_mfma_f32_16x16x32_bf16 v[34:37], v[38:41], v[120:123], 0\n"
        "s_nop 0\n"
        "v_pk_add_f16 v126, v126, v148\n"
        "v_cndmask_b32_e64 v254, v150, v151, s[56:57]\n"
        "v_mfma_f32_16x16x32_bf16 v[40:43], a[156:159], v[226:229], 0\n"
        "s_nop 0\n"
        "v_cvt_pk_bf16_f32 v107, v110, v111\n"
        "v_cvt_pk_bf16_f32 v138, v108, v109\n"
        "v_mfma_f32_16x16x32_bf16 v[40:43], a[160:163], v[234:237], v[40:43]\n"
        "ds_write_b128 v12, v[104:107] offset:6144\n"
        "v_mfma_f32_16x16x32_bf16 v[40:43], a[164:167], v[230:233], v[40:43]\n"
        "v_cvt_pk_bf16_f32 v131, v34, v35\n"
        "v_cvt_pk_bf16_f32 v139, v36, v37\n"
        "v_mfma_f32_16x16x32_bf16 v[108:111], a[176:179], v[230:233], 0\n"
        "ds_write_b128 v12, v[128:131] offset:7168\n"
        "v_mfma_f32_16x16x32_bf16 v[108:111], a[172:175], v[234:237], v[108:111]\n"
        "ds_write_b128 v12, v[136:139] offset:8192\n"
        "v_mfma_f32_16x16x32_bf16 v[108:111], a[168:171], v[226:229], v[108:111]\n"
        "v_cndmask_b32_e64 v254, v254, v152, s[58:59]\n"
        "v_cndmask_b32_e64 v252, v132, v133, s[56:57]\n"
        "v_mfma_f32_16x16x32_bf16 v[116:119], v[104:107], v[104:107], 0\n"
        "v_cndmask_b32_e64 v252, v252, v134, s[58:59]\n"
        "v_cndmask_b32_e64 v252, v252, v135, s[60:61]\n"
        "v_mfma_f32_16x16x32_bf16 v[116:119], v[128:131], v[128:131], v[116:119]\n"
        "v_cvt_pk_bf16_f32 v34, v112, v113\n"
        "v_cvt_pk_bf16_f32 v35, v114, v115\n"
        "v_mfma_f32_16x16x32_bf16 v[116:119], v[136:139], v[136:139], v[116:119]\n"
        "v_cvt_pk_bf16_f32 v36, v40, v41\n"
        "v_cvt_pk_bf16_f32 v37, v42, v43\n"
        "v_mfma_f32_16x16x32_bf16 v[104:107], a[188:191], v[230:233], 0\n"
        "s_nop 0\n"
        "v_cvt_pk_bf16_f32 v40, v108, v109\n"
        "v_cvt_pk_bf16_f32 v41, v110, v111\n"
        "v_mfma_f32_16x16x32_bf16 v[104:107], a[184:187], v[234:237], v[104:107]\n"
        "s_nop 0\n"
        "v_cndmask_b32_e64 v253, v116, v117, s[56:57]\n"
        "v_cndmask_b32_e64 v253, v253, v118, s[58:59]\n"
        "v_mfma_f32_16x16x32_bf16 v[104:107], a[180:183], v[226:229], v[104:107]\n"
        "v_cndmask_b32_e64 v253, v253, v119, s[60:61]\n"
        "v_cndmask_b32_e64 v254, v254, v153, s[60:61]\n"
        "v_mfma_f32_16x16x32_bf16 v[110:113], a[192:195], v[226:229], 0\n"
        "s_nop 0\n"
        "v_cndmask_b32_dpp v252, v253, v252, vcc row_ror:4 row_mask:0xf bank_mask:0xf\n"
        "v_pk_add_f16 v126, v126, v149\n"
        "v_mfma_f32_16x16x32_bf16 v[110:113], a[196:199], v[234:237], v[110:113]\n"
        "s_nop 0\n"
        "v_cvt_pk_bf16_f32 v42, v104, v105\n"
        "v_cvt_pk_bf16_f32 v43, v106, v107\n"
        "v_mfma_f32_16x16x32_bf16 v[110:113], a[200:203], v[230:233], v[110:113]\n"
        "v_cndmask_b32_dpp v254, v252, v254, vcc row_ror:4 row_mask:0xf bank_mask:0xf\n"
        "v_fma_f32 v255, -v255, v250, v254\n"
        "v_mfma_f32_16x16x32_bf16 v[116:119], a[212:215], v[230:233], 0\n"
        "s_nop 0\n"
        "v_mov_b32 v253, s29\n"
        "v_max_f32 v255, 0, v255\n"
        "v_mfma_f32_16x16x32_bf16 v[116:119], a[208:211], v[234:237], v[116:119]\n"
        "s_nop 0\n"
        "v_cvt_pk_bf16_f32 v108, v110, v111\n"
        "v_cvt_pk_bf16_f32 v109, v112, v113\n"
        "v_mfma_f32_16x16x32_bf16 v[116:119], a[204:207], v[226:229], v[116:119]\n"
        "v_sub_f32 v254, 0x47c00000, v254\n"
        "v_fmaak_f32 v255, v28, v255, 0x3727c5ac\n"
        "v_mfma_f32_16x16x32_bf16 v[104:107], a[144:147], v[202:205], 0\n"
        "s_nop 0\n"
        "v_bfi_b32 v255, s84, v254, v255\n"
        "v_cvt_pk_f16_f32 v114, v168, v169\n"
        "v_mfma_f32_16x16x32_bf16 v[104:107], a[148:151], v[210:213], v[104:107]\n"
        "s_nop 0\n"
        "v_cvt_pk_bf16_f32 v110, v116, v117\n"
        "v_cvt_pk_bf16_f32 v111, v118, v119\n"
        "v_mfma_f32_16x16x32_bf16 v[104:107], a[152:155], v[198:201], v[104:107]\n"
        "v_rsq_f32 v254, v255\n"
        "v_mfma_f32_16x16x32_bf16 v[136:139], v[222:225], v[34:37], 0\n"
        "v_cvt_f16_f32 v254, v254\n"
        "v_cvt_pk_f16_f32 v115, v170, v171\n"
        "v_mfma_f32_16x16x32_bf16 v[136:139], v[206:209], v[40:43], v[136:139]\n"
        "ds_write_b16 v18, v254\n"
        "v_mfma_f32_16x16x32_bf16 v[136:139], v[218:221], v[108:111], v[136:139]\n"
        "ds_write_b16 v19, v254\n"
        "v_mfma_f32_16x16x32_bf16 v[116:119], v[230:233], v[108:111], 0\n"
        "ds_write_b16 v22, v253 offset:4096\n"
        "v_mfma_f32_16x16x32_bf16 v[116:119], v[234:237], v[40:43], v[116:119]\n"
        "v_pk_mul_f16 v114, v114, v172 op_sel_hi:[1,0]\n"
        "v_pk_mul_f16 v115, v115, v172 op_sel_hi:[1,0]\n"
        "v_mfma_f32_16x16x32_bf16 v[116:119], v[226:229], v[34:37], v[116:119]\n"
        "v_pk_mul_f16 v114, v114, v246\n"
        "v_pk_mul_f16 v115, v115, v247\n"
        "v_mfma_f32_16x16x32_bf16 v[120:123], v[202:205], v[34:37], 0\n"
        "v_exp_f16_sdwa v114, v114 dst_sel:WORD_0 dst_unused:UNUSED_PRESERVE src0_sel:WORD_0\n"
        "v_mfma_f32_16x16x32_bf16 v[120:123], v[210:213], v[40:43], v[120:123]\n"
        "v_exp_f16_sdwa v115, v115 dst_sel:WORD_0 dst_unused:UNUSED_PRESERVE src0_sel:WORD_0\n"
        "v_mfma_f32_16x16x32_bf16 v[120:123], v[198:201], v[108:111], v[120:123]\n"
        "v_exp_f16_sdwa v114, v114 dst_sel:WORD_1 dst_unused:UNUSED_PRESERVE src0_sel:WORD_1\n"
        "v_mfma_f32_16x16x32_bf16 v[34:37], a[164:167], v[198:201], 0\n"
        "v_exp_f16_sdwa v115, v115 dst_sel:WORD_1 dst_unused:UNUSED_PRESERVE src0_sel:WORD_1\n"
        "v_mfma_f32_16x16x32_bf16 v[34:37], a[160:163], v[210:213], v[34:37]\n"
        "v_pk_add_f16 v126, v126, v114\n"
        "v_pk_add_f16 v126, v126, v115\n"
        "v_mfma_f32_16x16x32_bf16 v[34:37], a[156:159], v[202:205], v[34:37]\n"
        "v_cvt_pk_bf16_f32 v108, v104, v105\n"
        "v_cvt_pk_f16_f32 v130, v136, v137\n"
        "v_mfma_f32_16x16x32_bf16 v[134:137], a[168:171], v[202:205], 0\n"
        "v_cvt_pk_f16_f32 v131, v138, v139\n"
        "v_pk_mul_f16 v130, v130, v172 op_sel:[0,1]\n"
        "v_mfma_f32_16x16x32_bf16 v[134:137], a[172:175], v[210:213], v[134:137]\n"
        "v_pk_mul_f16 v131, v131, v172 op_sel:[0,1]\n"
        "v_pk_mul_f16 v130, v130, v214\n"
        "v_mfma_f32_16x16x32_bf16 v[134:137], a[176:179], v[198:201], v[134:137]\n"
        "v_pk_mul_f16 v131, v131, v215\n"
        "v_cvt_pk_bf16_f32 v109, v106, v107\n"
        "v_mfma_f32_16x16x32_bf16 v[104:107], a[188:191], v[198:201], 0\n"
        "v_exp_f16_sdwa v130, v130 dst_sel:WORD_0 dst_unused:UNUSED_PRESERVE src0_sel:WORD_0\n"
        "v_mfma_f32_16x16x32_bf16 v[104:107], a[184:187], v[210:213], v[104:107]\n"
        "v_exp_f16_sdwa v131, v131 dst_sel:WORD_0 dst_unused:UNUSED_PRESERVE src0_sel:WORD_0\n"
        "v_mfma_f32_16x16x32_bf16 v[104:107], a[180:183], v[202:205], v[104:107]\n"
        "v_exp_f16_sdwa v130, v130 dst_sel:WORD_1 dst_unused:UNUSED_PRESERVE src0_sel:WORD_1\n"
        "v_mfma_f32_16x16x32_bf16 v[40:43], a[192:195], v[202:205], 0\n"
        "v_exp_f16_sdwa v131, v131 dst_sel:WORD_1 dst_unused:UNUSED_PRESERVE src0_sel:WORD_1\n"
        "v_mfma_f32_16x16x32_bf16 v[40:43], a[196:199], v[210:213], v[40:43]\n"
        "v_pk_add_f16 v127, v130, v131\n"
        "v_cvt_pk_f16_f32 v132, v116, v117\n"
        "v_mfma_f32_16x16x32_bf16 v[40:43], a[200:203], v[198:201], v[40:43]\n"
        "v_cvt_pk_f16_f32 v133, v118, v119\n"
        "v_pk_mul_f16 v132, v132, v172 op_sel:[0,1]\n"
        "v_mfma_f32_16x16x32_bf16 v[116:119], a[212:215], v[198:201], 0\n"
        "v_pk_mul_f16 v133, v133, v172 op_sel:[0,1]\n"
        "v_cvt_pk_bf16_f32 v110, v34, v35\n"
        "v_mfma_f32_16x16x32_bf16 v[116:119], a[208:211], v[210:213], v[116:119]\n"
        "v_cvt_pk_bf16_f32 v111, v36, v37\n"
        "v_cvt_pk_bf16_f32 v34, v134, v135\n"
        "v_mfma_f32_16x16x32_bf16 v[116:119], a[204:207], v[202:205], v[116:119]\n"
        "v_cvt_pk_bf16_f32 v35, v136, v137\n"
        "v_cvt_pk_bf16_f32 v36, v104, v105\n"
        "v_mfma_f32_16x16x32_bf16 v[134:137], v[222:225], a[216:219], 0\n"
        "v_cvt_pk_bf16_f32 v37, v106, v107\n"
        "v_cvt_pk_bf16_f32 v104, v40, v41\n"
        "v_mfma_f32_16x16x32_bf16 v[134:137], v[206:209], a[220:223], v[134:137]\n"
        "s_nop 0\n"
        "v_cvt_pk_bf16_f32 v105, v42, v43\n"
        "v_cvt_pk_bf16_f32 v106, v116, v117\n"
        "v_mfma_f32_16x16x32_bf16 v[134:137], v[218:221], a[224:227], v[134:137]\n"
        "v_cvt_pk_bf16_f32 v107, v118, v119\n"
        "v_pk_mul_f16 v132, v132, v216\n"
        "v_mfma_f32_16x16x32_bf16 v[40:43], v[230:233], a[224:227], 0\n"
        "v_pk_mul_f16 v133, v133, v217\n"
        "v_cvt_pk_f16_f32 v118, v120, v121\n"
        "v_mfma_f32_16x16x32_bf16 v[40:43], v[234:237], a[220:223], v[40:43]\n"
        "v_exp_f16_sdwa v132, v132 dst_sel:WORD_0 dst_unused:UNUSED_PRESERVE src0_sel:WORD_0\n"
        "v_mfma_f32_16x16x32_bf16 v[40:43], v[226:229], a[216:219], v[40:43]\n"
        "v_exp_f16_sdwa v133, v133 dst_sel:WORD_0 dst_unused:UNUSED_PRESERVE src0_sel:WORD_0\n"
        "v_mfma_f32_16x16x32_bf16 v[138:141], v[202:205], a[216:219], 0\n"
        "v_exp_f16_sdwa v132, v132 dst_sel:WORD_1 dst_unused:UNUSED_PRESERVE src0_sel:WORD_1\n"
        "v_mfma_f32_16x16x32_bf16 v[138:141], v[210:213], a[220:223], v[138:141]\n"
        "v_exp_f16_sdwa v133, v133 dst_sel:WORD_1 dst_unused:UNUSED_PRESERVE src0_sel:WORD_1\n"
        "v_mfma_f32_16x16x32_bf16 v[138:141], v[198:201], a[224:227], v[138:141]\n"
        "v_pk_add_f16 v127, v127, v132\n"
        "v_pk_add_f16 v127, v127, v133\n"
        "v_mfma_f32_16x16x32_bf16 v[150:153], v[222:225], v[108:111], 0\n"
        "v_cvt_pk_f16_f32 v119, v122, v123\n"
        "v_pk_mul_f16 v118, v118, v172 op_sel:[0,1]\n"
        "v_mfma_f32_16x16x32_bf16 v[150:153], v[206:209], v[34:37], v[150:153]\n"
        "v_pk_mul_f16 v119, v119, v172 op_sel:[0,1]\n"
        "v_pk_mul_f16 v118, v118, v246\n"
        "v_mfma_f32_16x16x32_bf16 v[150:153], v[218:221], v[104:107], v[150:153]\n"
        "v_pk_mul_f16 v119, v119, v247\n"
        "v_cvt_pk_f16_f32 v142, v134, v135\n"
        "v_mfma_f32_16x16x32_bf16 v[154:157], v[230:233], v[104:107], 0\n"
        "v_cndmask_b32_e64 v116, 0, v251, s[62:63]\n"
        "v_cndmask_b32_e64 v120, 0, v251, s[64:65]\n"
        "v_mfma_f32_16x16x32_bf16 v[154:157], v[234:237], v[34:37], v[154:157]\n"
        "v_pk_add_f16 v126, v126, v116\n"
        "v_cvt_pk_f16_f32 v143, v136, v137\n"
        "v_mfma_f32_16x16x32_bf16 v[154:157], v[226:229], v[108:111], v[154:157]\n"
        "v_exp_f16_sdwa v118, v118 dst_sel:WORD_0 dst_unused:UNUSED_PRESERVE src0_sel:WORD_0\n"
        "v_mfma_f32_16x16x32_bf16 v[134:137], v[202:205], v[108:111], 0\n"
        "v_mov_b64 v[128:129], 0\n"
        "v_cvt_pk_f16_f32 v144, v40, v41\n"
        "v_mfma_f32_16x16x32_bf16 v[134:137], v[210:213], v[34:37], v[134:137]\n"
        "v_exp_f16_sdwa v119, v119 dst_sel:WORD_0 dst_unused:UNUSED_PRESERVE src0_sel:WORD_0\n"
        "v_mfma_f32_16x16x32_bf16 v[134:137], v[198:201], v[104:107], v[134:137]\n"
        "v_exp_f16_sdwa v118, v118 dst_sel:WORD_1 dst_unused:UNUSED_PRESERVE src0_sel:WORD_1\n"
        "v_mfma_f32_16x16x32_bf16 v[108:111], v[198:201], a[236:239], 0\n"
        "v_exp_f16_sdwa v119, v119 dst_sel:WORD_1 dst_unused:UNUSED_PRESERVE src0_sel:WORD_1\n"
        "v_mfma_f32_16x16x32_bf16 v[108:111], v[210:213], a[232:235], v[108:111]\n"
        "v_pk_add_f16 v127, v127, v118\n"
        "v_pk_add_f16 v127, v127, v119\n"
        "v_mfma_f32_16x16x32_bf16 v[108:111], v[202:205], a[228:231], v[108:111]\n"
        "v_pk_add_f16 v127, v127, v120\n"
        "v_cvt_pk_f16_f32 v145, v42, v43\n"
        "v_mfma_f32_16x16x32_bf16 v[104:107], v[226:229], a[228:231], 0\n"
        "v_cvt_pk_f16_f32 v122, v138, v139\n"
        "v_cvt_pk_f16_f32 v123, v140, v141\n"
        "v_mfma_f32_16x16x32_bf16 v[104:107], v[234:237], a[232:235], v[104:107]\n"
        "v_cvt_pk_f16_f32 v238, v150, v151\n"
        "v_cvt_pk_f16_f32 v239, v152, v153\n"
        "v_mfma_f32_16x16x32_bf16 v[104:107], v[230:233], a[236:239], v[104:107]\n"
        "v_pk_mul_f16 v238, v238, v173 op_sel_hi:[1,0]\n"
        "v_pk_mul_f16 v239, v239, v173 op_sel_hi:[1,0]\n"
        "v_mfma_f32_16x16x32_bf16 v[34:37], v[218:221], a[236:239], 0\n"
        "v_pk_mul_f16 v238, v238, v214\n"
        "v_pk_mul_f16 v239, v239, v215\n"
        "v_mfma_f32_16x16x32_bf16 v[34:37], v[206:209], a[232:235], v[34:37]\n"
        "v_exp_f16_sdwa v238, v238 dst_sel:WORD_0 dst_unused:UNUSED_PRESERVE src0_sel:WORD_0\n"
        "v_mfma_f32_16x16x32_bf16 v[34:37], v[222:225], a[228:231], v[34:37]\n"
        "v_exp_f16_sdwa v239, v239 dst_sel:WORD_0 dst_unused:UNUSED_PRESERVE src0_sel:WORD_0\n"
        "v_mfma_f32_16x16x32_bf16 v[166:169], v[222:225], a[240:243], 0\n"
        "v_exp_f16_sdwa v238, v238 dst_sel:WORD_1 dst_unused:UNUSED_PRESERVE src0_sel:WORD_1\n"
        "v_mfma_f32_16x16x32_bf16 v[166:169], v[206:209], a[244:247], v[166:169]\n"
        "v_exp_f16_sdwa v239, v239 dst_sel:WORD_1 dst_unused:UNUSED_PRESERVE src0_sel:WORD_1\n"
        "v_mfma_f32_16x16x32_bf16 v[166:169], v[218:221], a[248:251], v[166:169]\n"
        "v_cvt_pk_f16_f32 v240, v154, v155\n"
        "v_cvt_pk_f16_f32 v241, v156, v157\n"
        "v_mfma_f32_16x16x32_bf16 v[40:43], v[230:233], a[248:251], 0\n"
        "v_pk_mul_f16 v240, v240, v173 op_sel_hi:[1,0]\n"
        "v_pk_mul_f16 v241, v241, v173 op_sel_hi:[1,0]\n"
        "v_mfma_f32_16x16x32_bf16 v[40:43], v[234:237], a[244:247], v[40:43]\n"
        "v_pk_mul_f16 v240, v240, v216\n"
        "v_pk_mul_f16 v241, v241, v217\n"
        "v_mfma_f32_16x16x32_bf16 v[40:43], v[226:229], a[240:243], v[40:43]\n"
        "v_exp_f16_sdwa v240, v240 dst_sel:WORD_0 dst_unused:UNUSED_PRESERVE src0_sel:WORD_0\n"
        "v_mfma_f32_16x16x32_bf16 v[158:161], v[202:205], a[240:243], 0\n"
        "v_exp_f16_sdwa v241, v241 dst_sel:WORD_0 dst_unused:UNUSED_PRESERVE src0_sel:WORD_0\n"
        "v_mfma_f32_16x16x32_bf16 v[158:161], v[210:213], a[244:247], v[158:161]\n"
        "v_exp_f16_sdwa v240, v240 dst_sel:WORD_1 dst_unused:UNUSED_PRESERVE src0_sel:WORD_1\n"
        "v_mfma_f32_16x16x32_bf16 v[158:161], v[198:201], a[248:251], v[158:161]\n"
        "v_exp_f16_sdwa v241, v241 dst_sel:WORD_1 dst_unused:UNUSED_PRESERVE src0_sel:WORD_1\n"
        "v_mfma_f32_16x16x32_f16 v[150:153], v[56:59], v[126:129], 0\n"
        "v_cvt_pk_f16_f32 v242, v134, v135\n"
        "v_cvt_pk_f16_f32 v243, v136, v137\n"
        "v_mfma_f32_16x16x32_bf16 v[154:157], v[198:201], v[52:55], 0\n"
        "v_pk_mul_f16 v242, v242, v173 op_sel_hi:[1,0]\n"
        "v_pk_mul_f16 v243, v243, v173 op_sel_hi:[1,0]\n"
        "v_mfma_f32_16x16x32_bf16 v[154:157], v[210:213], v[92:95], v[154:157]\n"
        "v_pk_mul_f16 v242, v242, v246\n"
        "v_pk_mul_f16 v243, v243, v247\n"
        "v_mfma_f32_16x16x32_bf16 v[154:157], v[202:205], a[252:255], v[154:157]\n"
        "v_exp_f16_sdwa v242, v242 dst_sel:WORD_0 dst_unused:UNUSED_PRESERVE src0_sel:WORD_0\n"
        "v_mfma_f32_16x16x32_bf16 v[134:137], v[226:229], a[252:255], 0\n"
        "v_cndmask_b32_e64 v244, 0, v251, s[66:67]\n"
        "v_cvt_pk_f16_f32 v174, v108, v109\n"
        "v_mfma_f32_16x16x32_bf16 v[134:137], v[234:237], v[92:95], v[134:137]\n"
        "ds_read2_b32 v[124:125], v24 offset0:0 offset1:0\n"
        "ds_read2_b32 v[176:177], v24 offset0:1 offset1:1\n"
        "v_mfma_f32_16x16x32_bf16 v[134:137], v[230:233], v[52:55], v[134:137]\n"
        "ds_read2_b32 v[196:197], v24 offset0:2 offset1:2\n"
        "ds_read2_b32 v[192:193], v24 offset0:3 offset1:3\n"
        "v_mfma_f32_16x16x32_bf16 v[138:141], v[218:221], v[52:55], 0\n"
        "ds_read2_b32 v[172:173], v24 offset0:4 offset1:4\n"
        "v_mov_b32 v117, 0\n"
        "v_mfma_f32_16x16x32_bf16 v[138:141], v[206:209], v[92:95], v[138:141]\n"
        "ds_read2_b32 v[188:189], v24 offset0:5 offset1:5\n"
        "v_cvt_pk_f16_f32 v175, v110, v111\n"
        "v_mfma_f32_16x16x32_bf16 v[138:141], v[222:225], a[252:255], v[138:141]\n"
        "v_exp_f16_sdwa v243, v243 dst_sel:WORD_0 dst_unused:UNUSED_PRESERVE src0_sel:WORD_0\n"
        "v_mfma_f32_16x16x32_bf16 v[184:187], v[222:225], v[64:67], 0\n"
        "v_exp_f16_sdwa v242, v242 dst_sel:WORD_1 dst_unused:UNUSED_PRESERVE src0_sel:WORD_1\n"
        "v_mfma_f32_16x16x32_bf16 v[184:187], v[206:209], v[84:87], v[184:187]\n"
        "v_exp_f16_sdwa v243, v243 dst_sel:WORD_1 dst_unused:UNUSED_PRESERVE src0_sel:WORD_1\n"
        "v_mfma_f32_16x16x32_bf16 v[184:187], v[218:221], v[76:79], v[184:187]\n"
        "v_cvt_pk_f16_f32 v164, v104, v105\n"
        "v_cvt_pk_f16_f32 v165, v106, v107\n"
        "v_mfma_f32_16x16x32_bf16 v[248:251], v[230:233], v[76:79], 0\n"
        "v_cvt_pk_f16_f32 v162, v34, v35\n"
        "v_cvt_pk_f16_f32 v163, v36, v37\n"
        "v_mfma_f32_16x16x32_bf16 v[248:251], v[234:237], v[84:87], v[248:251]\n"
        "v_cvt_pk_f16_f32 v178, v166, v167\n"
        "v_cvt_pk_f16_f32 v179, v168, v169\n"
        "v_mfma_f32_16x16x32_bf16 v[248:251], v[226:229], v[64:67], v[248:251]\n"
        "v_cvt_pk_f16_f32 v180, v40, v41\n"
        "v_cvt_pk_f16_f32 v181, v42, v43\n"
        "v_mfma_f32_16x16x32_bf16 v[252:255], v[202:205], v[64:67], 0\n"
        "s_waitcnt lgkmcnt(0)\n"
        "v_pk_add_f16 v128, v238, v239\n"
        "v_pk_add_f16 v128, v128, v240\n"
        "v_mfma_f32_16x16x32_bf16 v[252:255], v[210:213], v[84:87], v[252:255]\n"
        "v_pk_add_f16 v128, v128, v241\n"
        "v_pk_add_f16 v128, v128, v242\n"
        "v_mfma_f32_16x16x32_bf16 v[252:255], v[198:201], v[76:79], v[252:255]\n"
        "v_cvt_pk_f16_f32 v194, v158, v159\n"
        "v_cvt_pk_f16_f32 v195, v160, v161\n"
        "v_mfma_f32_16x16x32_bf16 v[40:43], v[198:201], v[48:51], 0\n"
        "v_pk_add_f16 v128, v128, v243\n"
        "v_pk_add_f16 v128, v128, v244\n"
        "v_mfma_f32_16x16x32_bf16 v[40:43], v[210:213], v[72:75], v[40:43]\n"
        "v_rcp_f32 v190, v150\n"
        "v_mfma_f32_16x16x32_bf16 v[40:43], v[202:205], v[60:63], v[40:43]\n"
        "v_cvt_f16_f32 v245, v190\n"
        "v_pk_mul_f16 v146, v146, v245 op_sel_hi:[1,0]\n"
        "v_mfma_f32_16x16x32_bf16 v[104:107], v[226:229], v[60:63], 0\n"
        "v_pk_mul_f16 v147, v147, v245 op_sel_hi:[1,0]\n"
        "v_pk_mul_f16 v148, v148, v245 op_sel_hi:[1,0]\n"
        "v_mfma_f32_16x16x32_bf16 v[104:107], v[234:237], v[72:75], v[104:107]\n"
        "v_pk_mul_f16 v149, v149, v245 op_sel_hi:[1,0]\n"
        "v_pk_mul_f16 v114, v114, v245 op_sel_hi:[1,0]\n"
        "v_mfma_f32_16x16x32_bf16 v[104:107], v[230:233], v[48:51], v[104:107]\n"
        "v_pk_mul_f16 v115, v115, v245 op_sel_hi:[1,0]\n"
        "v_pk_mul_f16 v116, v116, v245 op_sel_hi:[1,0]\n"
        "v_mfma_f32_16x16x32_bf16 v[34:37], v[218:221], v[48:51], 0\n"
        "v_mov_b32 v121, 0\n"
        "v_pk_mul_f16 v146, v146, v214\n"
        "v_mfma_f32_16x16x32_bf16 v[34:37], v[206:209], v[72:75], v[34:37]\n"
        "v_pk_mul_f16 v147, v147, v215\n"
        "v_pk_mul_f16 v148, v148, v216\n"
        "v_mfma_f32_16x16x32_bf16 v[34:37], v[222:225], v[60:63], v[34:37]\n"
        "v_pk_mul_f16 v149, v149, v217\n"
        "v_pk_mul_f16 v114, v114, v246\n"
        "v_mfma_f32_16x16x32_f16 v[110:113], v[56:59], v[126:129], 0\n"
        "v_pk_mul_f16 v115, v115, v247\n"
        "v_max3_f32 v245, s95, v150, v151\n"
        "v_mfma_f32_16x16x32_f16 v[126:129], v[146:149], v[142:145], 0\n"
        "v_rcp_f32 v183, v151\n"
        "v_mfma_f32_16x16x32_f16 v[126:129], v[114:117], v[122:125], v[126:129]\n"
        "v_add_f32 v47, v191, v190\n"
        "v_cvt_f16_f32 v191, v183\n"
        "v_mfma_f32_16x16x32_bf16 v[126:129], v[222:225], v[88:91], v[126:129]\n"
        "v_add_f32 v47, v47, v183\n"
        "v_pk_mul_f16 v130, v130, v191 op_sel_hi:[1,0]\n"
        "v_mfma_f32_16x16x32_bf16 v[158:161], v[222:225], v[80:83], 0\n"
        "v_pk_mul_f16 v131, v131, v191 op_sel_hi:[1,0]\n"
        "v_pk_mul_f16 v132, v132, v191 op_sel_hi:[1,0]\n"
        "v_mfma_f32_16x16x32_f16 v[158:161], v[114:117], v[174:177], v[158:161]\n"
        "v_pk_mul_f16 v133, v133, v191 op_sel_hi:[1,0]\n"
        "v_pk_mul_f16 v118, v118, v191 op_sel_hi:[1,0]\n"
        "v_mfma_f32_16x16x32_f16 v[158:161], v[146:149], v[162:165], v[158:161]\n"
        "v_pk_mul_f16 v119, v119, v191 op_sel_hi:[1,0]\n"
        "v_pk_mul_f16 v120, v120, v191 op_sel_hi:[1,0]\n"
        "v_mfma_f32_16x16x32_f16 v[108:111], v[146:149], v[178:181], 0\n"
        "v_cvt_pk_f16_f32 v190, v154, v155\n"
        "v_cvt_pk_f16_f32 v191, v156, v157\n"
        "v_mfma_f32_16x16x32_f16 v[108:111], v[114:117], v[194:197], v[108:111]\n"
        "v_cvt_pk_f16_f32 v168, v134, v135\n"
        "v_cvt_pk_f16_f32 v169, v136, v137\n"
        "v_mfma_f32_16x16x32_bf16 v[108:111], v[206:209], v[88:91], v[108:111]\n"
        "v_cvt_pk_f16_f32 v166, v138, v139\n"
        "v_cvt_pk_f16_f32 v167, v140, v141\n"
        "v_mfma_f32_16x16x32_bf16 v[150:153], v[206:209], v[80:83], 0\n"
        "v_cvt_pk_f16_f32 v182, v184, v185\n"
        "v_cvt_pk_f16_f32 v183, v186, v187\n"
        "v_mfma_f32_16x16x32_f16 v[150:153], v[114:117], v[190:193], v[150:153]\n"
        "v_cvt_pk_f16_f32 v184, v248, v249\n"
        "v_cvt_pk_f16_f32 v185, v250, v251\n"
        "v_mfma_f32_16x16x32_f16 v[150:153], v[146:149], v[166:169], v[150:153]\n"
        "v_cvt_pk_f16_f32 v170, v252, v253\n"
        "v_cvt_pk_f16_f32 v171, v254, v255\n"
        "v_mfma_f32_16x16x32_f16 v[134:137], v[146:149], v[182:185], 0\n"
        "v_pk_mul_f16 v130, v130, v214\n"
        "v_pk_mul_f16 v131, v131, v215\n"
        "v_mfma_f32_16x16x32_f16 v[134:137], v[114:117], v[170:173], v[134:137]\n"
        "v_cvt_pk_f16_f32 v186, v40, v41\n"
        "v_cvt_pk_f16_f32 v187, v42, v43\n"
        "v_mfma_f32_16x16x32_bf16 v[134:137], v[218:221], v[88:91], v[134:137]\n"
        "v_cvt_pk_f16_f32 v42, v104, v105\n"
        "v_cvt_pk_f16_f32 v43, v106, v107\n"
        "v_mfma_f32_16x16x32_bf16 v[138:141], v[218:221], v[80:83], 0\n"
        "v_cvt_pk_f16_f32 v40, v34, v35\n"
        "v_cvt_pk_f16_f32 v41, v36, v37\n"
        "v_mfma_f32_16x16x32_f16 v[138:141], v[114:117], v[186:189], v[138:141]\n"
        "v_pk_mul_f16 v132, v132, v216\n"
        "v_pk_mul_f16 v133, v133, v217\n"
        "v_mfma_f32_16x16x32_f16 v[138:141], v[146:149], v[40:43], v[138:141]\n"
        "s_add_u32 s90, s89, 0\n"
        "v_pk_mul_f16 v118, v118, v246\n"
        "v_pk_mul_f16 v119, v119, v247\n"
        "v_mfma_f32_16x16x32_f16 v[34:37], v[130:133], v[142:145], 0\n"
        "v_rcp_f32 v255, v112\n"
        "v_mfma_f32_16x16x32_f16 v[34:37], v[118:121], v[122:125], v[34:37]\n"
        "v_max_f32 v245, v245, v112\n"
        "v_add_f32 v47, v47, v255\n"
        "v_mfma_f32_16x16x32_bf16 v[34:37], v[226:229], v[88:91], v[34:37]\n"
        "v_fma_f32 v245, v47, s82, v245\n"
        "v_cmp_nlt_f32_e64 s[74:75], v245, s83\n"
        "v_mfma_f32_16x16x32_bf16 v[112:115], v[226:229], v[80:83], 0\n"
        "v_cvt_pk_bf16_f32 v154, v126, v158\n"
        "v_cvt_pk_bf16_f32 v155, v108, v150\n"
        "v_mfma_f32_16x16x32_f16 v[112:115], v[118:121], v[174:177], v[112:115]\n"
        "s_cmp_lg_u64 s[74:75], 0\n"
        "v_cvt_pk_bf16_f32 v156, v134, v138\n"
        "v_mov_b32 v245, 0\n"
        "v_mfma_f32_16x16x32_f16 v[112:115], v[130:133], v[162:165], v[112:115]\n"
        "s_cbranch_scc1 SLOW_%=\n"
        "v_cvt_pk_bf16_f32 v146, v127, v159\n"
        "v_cvt_pk_bf16_f32 v147, v109, v151\n"
        "v_mfma_f32_16x16x32_f16 v[104:107], v[130:133], v[178:181], 0\n"
        "buffer_store_dwordx3 v[154:156], v14, s[40:43], s90 offen sc1 nt\n"
        "v_cvt_pk_bf16_f32 v148, v135, v139\n"
        "v_cvt_f16_f32 v254, v255\n"
        "v_cvt_pk_bf16_f32 v126, v128, v160\n"
        "v_cvt_pk_bf16_f32 v127, v110, v152\n"
        "v_mfma_f32_16x16x32_f16 v[104:107], v[118:121], v[194:197], v[104:107]\n"
        "s_add_u32 s91, s89, 98112\n"
        "v_cvt_pk_bf16_f32 v128, v136, v140\n"
        "v_pk_mul_f16 v238, v238, v254 op_sel_hi:[1,0]\n"
        "v_mfma_f32_16x16x32_bf16 v[104:107], v[234:237], v[88:91], v[104:107]\n"
        "buffer_store_dwordx3 v[146:148], v15, s[40:43], s90 offen offset:768 sc1 nt\n"
        "v_cvt_pk_bf16_f32 v134, v129, v161\n"
        "v_cvt_pk_bf16_f32 v135, v111, v153\n"
        "v_cvt_pk_bf16_f32 v136, v137, v141\n"
        "v_pk_mul_f16 v239, v239, v254 op_sel_hi:[1,0]\n"
        "v_mfma_f32_16x16x32_bf16 v[108:111], v[234:237], v[80:83], 0\n"
        "v_pk_mul_f16 v240, v240, v254 op_sel_hi:[1,0]\n"
        "v_pk_mul_f16 v241, v241, v254 op_sel_hi:[1,0]\n"
        "v_mfma_f32_16x16x32_f16 v[108:111], v[118:121], v[190:193], v[108:111]\n"
        "buffer_store_dwordx3 v[126:128], v14, s[40:43], s91 offen offset:192 sc1 nt\n"
        "v_pk_mul_f16 v242, v242, v254 op_sel_hi:[1,0]\n"
        "v_pk_mul_f16 v243, v243, v254 op_sel_hi:[1,0]\n"
        "v_pk_mul_f16 v244, v244, v254 op_sel_hi:[1,0]\n"
        "v_pk_mul_f16 v238, v238, v214\n"
        "v_mfma_f32_16x16x32_f16 v[108:111], v[130:133], v[166:169], v[108:111]\n"
        "v_pk_mul_f16 v239, v239, v215\n"
        "v_pk_mul_f16 v240, v240, v216\n"
        "v_mfma_f32_16x16x32_f16 v[126:129], v[130:133], v[182:185], 0\n"
        "buffer_store_dwordx3 v[134:136], v16, s[40:43], s91 offen offset:960 sc1 nt\n"
        "v_pk_mul_f16 v241, v241, v217\n"
        "v_pk_mul_f16 v242, v242, v246\n"
        "v_pk_mul_f16 v243, v243, v247\n"
        "v_cvt_pk_bf16_f32 v138, v34, v112\n"
        "v_mfma_f32_16x16x32_f16 v[126:129], v[118:121], v[170:173], v[126:129]\n"
        "v_cvt_pk_bf16_f32 v112, v35, v113\n"
        "v_cvt_pk_bf16_f32 v116, v36, v114\n"
        "v_mfma_f32_16x16x32_bf16 v[126:129], v[230:233], v[88:91], v[126:129]\n"
        "v_cvt_pk_bf16_f32 v139, v104, v108\n"
        "v_cvt_pk_bf16_f32 v113, v105, v109\n"
        "v_mfma_f32_16x16x32_bf16 v[134:137], v[230:233], v[80:83], 0\n"
        "v_cvt_pk_bf16_f32 v117, v106, v110\n"
        "v_cvt_pk_bf16_f32 v214, v37, v115\n"
        "v_mfma_f32_16x16x32_f16 v[134:137], v[118:121], v[186:189], v[134:137]\n"
        "v_cvt_pk_bf16_f32 v215, v107, v111\n"
        "v_mfma_f32_16x16x32_f16 v[134:137], v[130:133], v[40:43], v[134:137]\n"
        "s_nop 1\n"
        "v_mfma_f32_16x16x32_f16 v[34:37], v[238:241], v[142:145], 0\n"
        "s_nop 2\n"
        "v_mfma_f32_16x16x32_f16 v[34:37], v[242:245], v[122:125], v[34:37]\n"
        "s_waitcnt vmcnt(4)\n"
        "v_cvt_pk_bf16_f32 v140, v126, v134\n"
        "v_cvt_pk_bf16_f32 v114, v127, v135\n"
        "v_mfma_f32_16x16x32_bf16 v[34:37], v[202:205], v[88:91], v[34:37]\n"
        "s_add_u32 s90, s89, 196224\n"
        "ds_read_b128 v[146:149], v9\n"
        "v_cvt_pk_bf16_f32 v118, v128, v136\n"
        "v_mfma_f32_16x16x32_bf16 v[104:107], v[202:205], v[80:83], 0\n"
        "ds_read_b128 v[152:155], v10\n"
        "v_cvt_pk_bf16_f32 v216, v129, v137\n"
        "v_mfma_f32_16x16x32_f16 v[104:107], v[242:245], v[174:177], v[104:107]\n"
        "ds_read_b128 v[158:161], v11\n"
        "v_mfma_f32_16x16x32_f16 v[104:107], v[238:241], v[162:165], v[104:107]\n"
        "s_add_u32 s91, s89, 294336\n"
        "ds_read_b128 v[126:129], v9 offset:3072\n"
        "v_mfma_f32_16x16x32_f16 v[206:209], v[238:241], v[178:181], 0\n"
        "buffer_store_dwordx3 v[138:140], v14, s[40:43], s90 offen offset:384 sc1 nt\n"
        "v_mfma_f32_16x16x32_f16 v[206:209], v[242:245], v[194:197], v[206:209]\n"
        "ds_read_b128 v[132:135], v10 offset:3072\n"
        "ds_read_b128 v[138:141], v11 offset:3072\n"
        "v_mfma_f32_16x16x32_bf16 v[206:209], v[210:213], v[88:91], v[206:209]\n"
        "buffer_store_dwordx3 v[112:114], v17, s[40:43], s90 offen offset:1152 sc1 nt\n"
        "v_cvt_pk_bf16_f32 v222, v34, v104\n"
        "v_cvt_pk_bf16_f32 v234, v35, v105\n"
        "v_cvt_pk_bf16_f32 v250, v36, v106\n"
        "v_cvt_pk_bf16_f32 v246, v37, v107\n"
        "v_mfma_f32_16x16x32_bf16 v[34:37], v[210:213], v[80:83], 0\n"
        "s_add_u32 s90, s89, 392448\n"
        "ds_read_b128 v[106:109], v9 offset:6144\n"
        "ds_read_b128 v[112:115], v10 offset:6144\n"
        "v_mfma_f32_16x16x32_f16 v[34:37], v[242:245], v[190:193], v[34:37]\n"
        "buffer_store_dwordx3 v[116:118], v14, s[40:43], s91 offen offset:576 sc1 nt\n"
        "v_mfma_f32_16x16x32_f16 v[34:37], v[238:241], v[166:169], v[34:37]\n"
        "s_waitcnt lgkmcnt(5)\n"
        "ds_read_b128 v[118:121], v11 offset:6144\n"
        "v_mfma_f32_16x16x32_f16 v[202:205], v[238:241], v[182:185], 0\n"
        "buffer_store_dwordx3 v[214:216], v14, s[40:43], s90 offen sc1 nt\n"
        "s_nop 1\n"
        "v_mfma_f32_16x16x32_f16 v[202:205], v[242:245], v[170:173], v[202:205]\n"
        "s_nop 0\n"
        "v_cvt_pk_bf16_f32 v223, v206, v34\n"
        "v_cvt_pk_bf16_f32 v235, v207, v35\n"
        "v_mfma_f32_16x16x32_bf16 v[202:205], v[198:201], v[88:91], v[202:205]\n"
        "v_cvt_pk_bf16_f32 v251, v208, v36\n"
        "v_cvt_pk_bf16_f32 v247, v209, v37\n"
        "v_mfma_f32_16x16x32_bf16 v[206:209], v[198:201], v[80:83], 0\n"
        "s_cmp_ge_i32 s85, 0\n"
        "v_mfma_f32_16x16x32_f16 v[206:209], v[242:245], v[186:189], v[206:209]\n"
        "v_mfma_f32_16x16x32_f16 v[206:209], v[238:241], v[40:43], v[206:209]\n"
        "s_cbranch_scc1 TOP_%=\n"
        "LAST_%=:\n"
        "s_waitcnt lgkmcnt(0)\n"
        "s_nop 7\n"
        "s_mov_b32 s26, s89\n"
        "s_add_u32 s27, s26, 392448\n"
        "v_cvt_pk_bf16_f32 v224, v202, v206\n"
        "buffer_store_dwordx3 v[222:224], v15, s[40:43], s27 offen offset:768 sc1 nt\n"
        "v_cvt_pk_bf16_f32 v236, v203, v207\n"
        "s_add_u32 s28, s26, 490560\n"
        "buffer_store_dwordx3 v[234:236], v14, s[40:43], s28 offen offset:192 sc1 nt\n"
        "v_cvt_pk_bf16_f32 v252, v204, v208\n"
        "buffer_store_dwordx3 v[250:252], v16, s[40:43], s28 offen offset:960 sc1 nt\n"
        "v_cvt_pk_bf16_f32 v248, v205, v209\n"
        "s_add_u32 s27, s26, 588672\n"
        "buffer_store_dwordx3 v[246:248], v14, s[40:43], s27 offen offset:384 sc1 nt\n"
        "ds_read_u16 v255, v19 offset:3072\n"
        "s_waitcnt lgkmcnt(0)\n"
        "s_mov_b32 s30, s29\n"
        "v_cmp_ne_u32_e64 s[24:25], s30, v255\n"
        "s_nop 3\n"
        "s_cmp_lg_u64 s[24:25], 0\n"
        "s_cbranch_scc1 SP18_%=\n"
        "BK18_%=:\n"
        "ds_read_b128 v[222:225], v13\n"
        "ds_read_b128 v[206:209], v13 offset:1024\n"
        "ds_read_b128 v[218:221], v13 offset:2048\n"
        "ds_read_b128 v[226:229], v13 offset:3072\n"
        "ds_read_b128 v[234:237], v13 offset:4096\n"
        "ds_read_b128 v[230:233], v13 offset:5120\n"
        "ds_read_b128 v[202:205], v13 offset:6144\n"
        "ds_read_b128 v[210:213], v13 offset:7168\n"
        "ds_read_b128 v[198:201], v13 offset:8192\n"
        "ds_read_b128 v[214:217], v20\n"
        "ds_read_b64 v[30:31], v20 offset:16\n"
        "ds_read_u16 v255, v22\n"
        "ds_read_b64 v[46:47], v21\n"
        "s_waitcnt lgkmcnt(10)\n"
        "v_mfma_f32_16x16x32_bf16 v[108:111], a[144:147], v[222:225], 0\n"
        "s_add_u32 s89, s86, s51\n"
        "ds_read_b128 v[138:141], v20 offset:512\n"
        "v_add_u32 v23, 784, v23\n"
        "v_mfma_f32_16x16x32_bf16 v[108:111], a[148:151], v[206:209], v[108:111]\n"
        "s_add_u32 s29, s29, 1\n"
        "ds_read_b128 v[118:121], v20 offset:528\n"
        "v_add_u32 v24, 784, v24\n"
        "v_mfma_f32_16x16x32_bf16 v[108:111], a[152:155], v[218:221], v[108:111]\n"
        "s_mov_b32 s86, s87\n"
        "ds_read_b128 v[134:137], v23\n"
        "v_lshlrev_b32 v34, 16, v124\n"
        "v_mfma_f32_16x16x32_bf16 v[112:115], a[164:167], v[218:221], 0\n"
        "s_add_u32 s88, s88, 32\n"
        "ds_read_b128 v[130:133], v23 offset:64\n"
        "v_lshlrev_b32 v35, 16, v176\n"
        "v_mfma_f32_16x16x32_bf16 v[112:115], a[160:163], v[206:209], v[112:115]\n"
        "s_add_u32 s87, s87, 43008\n"
        "ds_read_b128 v[154:157], v23 offset:128\n"
        "v_lshlrev_b32 v36, 16, v196\n"
        "v_mfma_f32_16x16x32_bf16 v[112:115], a[156:159], v[222:225], v[112:115]\n"
        "s_cmp_ge_u32 s88, 73\n"
        "ds_read_b64 v[242:243], v24 offset:360\n"
        "v_lshlrev_b32 v37, 16, v192\n"
        "v_mfma_f32_16x16x32_bf16 v[126:129], a[168:171], v[222:225], 0\n"
        "s_cselect_b32 s92, 588672, 0\n"
        "v_lshlrev_b32 v106, 16, v172\n"
        "v_lshlrev_b32 v107, 16, v188\n"
        "v_mfma_f32_16x16x32_bf16 v[126:129], a[172:175], v[206:209], v[126:129]\n"
        "s_cselect_b32 s94, 73, 0\n"
        "v_cvt_pk_bf16_f32 v146, v108, v109\n"
        "v_cvt_pk_bf16_f32 v147, v110, v111\n"
        "v_mfma_f32_16x16x32_bf16 v[126:129], a[176:179], v[218:221], v[126:129]\n"
        "s_sub_u32 s88, s88, s94\n"
        "v_cvt_pk_bf16_f32 v148, v112, v113\n"
        "v_cvt_pk_bf16_f32 v149, v114, v115\n"
        "v_mfma_f32_16x16x32_bf16 v[114:117], a[188:191], v[218:221], 0\n"
        "s_waitcnt lgkmcnt(4)\n"
        "v_pk_mul_f16 v108, v138, v142\n"
        "v_pk_mul_f16 v109, v138, v162\n"
        "v_mfma_f32_16x16x32_bf16 v[114:117], a[184:187], v[206:209], v[114:117]\n"
        "s_add_u32 s87, s87, s92\n"
        "v_pk_mul_f16 v110, v138, v178\n"
        "v_pk_mul_f16 v111, v138, v166\n"
        "v_mfma_f32_16x16x32_bf16 v[114:117], a[180:183], v[222:225], v[114:117]\n"
        "s_cmp_gt_i32 s85, 0\n"
        "v_pk_mul_f16 v112, v138, v182\n"
        "v_pk_mul_f16 v113, v138, v40\n"
        "v_mfma_f32_16x16x32_bf16 v[158:161], a[192:195], v[222:225], 0\n"
        "s_cselect_b32 s94, s87, s84\n"
        "v_pk_fma_f16 v108, v139, v143, v108\n"
        "v_pk_fma_f16 v109, v139, v163, v109\n"
        "v_mfma_f32_16x16x32_bf16 v[158:161], a[196:199], v[206:209], v[158:161]\n"
        "s_waitcnt lgkmcnt(0)\n"
        "v_pk_fma_f16 v110, v139, v179, v110\n"
        "v_pk_fma_f16 v111, v139, v167, v111\n"
        "v_mfma_f32_16x16x32_bf16 v[158:161], a[200:203], v[218:221], v[158:161]\n"
        "s_sub_u32 s85, s85, 1\n"
        "v_pk_fma_f16 v112, v139, v183, v112\n"
        "v_pk_fma_f16 v113, v139, v41, v113\n"
        "v_mfma_f32_16x16x32_bf16 v[150:153], a[212:215], v[218:221], 0\n"
        "s_waitcnt lgkmcnt(0)\n"
        "v_readlane_b32 s93, v243, 1\n"
        "v_cndmask_b32_e64 v242, 0, v242, s[72:73]\n"
        "v_mfma_f32_16x16x32_bf16 v[150:153], a[208:211], v[206:209], v[150:153]\n"
        "s_waitcnt lgkmcnt(0)\n"
        "v_pk_fma_f16 v108, v140, v144, v108\n"
        "v_pk_fma_f16 v109, v140, v164, v109\n"
        "v_mfma_f32_16x16x32_bf16 v[150:153], a[204:207], v[222:225], v[150:153]\n"
        "v_pk_fma_f16 v110, v140, v180, v110\n"
        "v_pk_fma_f16 v111, v140, v168, v111\n"
        "v_mfma_f32_16x16x32_bf16 v[238:241], v[134:137], v[222:225], 0\n"
        "v_pk_fma_f16 v112, v140, v184, v112\n"
        "v_pk_fma_f16 v113, v140, v42, v113\n"
        "v_mfma_f32_16x16x32_bf16 v[238:241], v[130:133], v[206:209], v[238:241]\n"
        "v_pk_fma_f16 v108, v141, v145, v108\n"
        "v_pk_fma_f16 v109, v141, v165, v109\n"
        "v_mfma_f32_16x16x32_bf16 v[238:241], v[154:157], v[218:221], v[238:241]\n"
        "v_pk_fma_f16 v110, v141, v181, v110\n"
        "v_pk_fma_f16 v111, v141, v169, v111\n"
        "v_mfma_f32_16x16x32_bf16 v[142:145], v[154:157], v[230:233], 0\n"
        "v_pk_fma_f16 v112, v141, v185, v112\n"
        "v_pk_fma_f16 v113, v141, v43, v113\n"
        "v_mfma_f32_16x16x32_bf16 v[142:145], v[130:133], v[234:237], v[142:145]\n"
        "v_pk_fma_f16 v108, v118, v122, v108\n"
        "v_pk_fma_f16 v109, v118, v174, v109\n"
        "v_mfma_f32_16x16x32_bf16 v[142:145], v[134:137], v[226:229], v[142:145]\n"
        "v_pk_fma_f16 v110, v118, v194, v110\n"
        "v_pk_fma_f16 v111, v118, v190, v111\n"
        "v_mfma_f32_16x16x32_bf16 v[138:141], v[134:137], v[202:205], 0\n"
        "v_pk_fma_f16 v112, v118, v170, v112\n"
        "v_pk_fma_f16 v113, v118, v186, v113\n"
        "v_mfma_f32_16x16x32_bf16 v[138:141], v[130:133], v[210:213], v[138:141]\n"
        "v_pk_fma_f16 v108, v119, v123, v108\n"
        "v_pk_fma_f16 v109, v119, v175, v109\n"
        "v_mfma_f32_16x16x32_bf16 v[138:141], v[154:157], v[198:201], v[138:141]\n"
        "v_pk_fma_f16 v110, v119, v195, v110\n"
        "v_pk_fma_f16 v111, v119, v191, v111\n"
        "v_mfma_f32_16x16x32_bf16 v[130:133], v[222:225], v[146:149], 0\n"
        "v_pk_fma_f16 v112, v119, v171, v112\n"
        "v_pk_fma_f16 v113, v119, v187, v113\n"
        "v_cvt_pk_bf16_f32 v40, v126, v127\n"
        "v_cvt_pk_bf16_f32 v41, v128, v129\n"
        "v_cvt_pk_bf16_f32 v42, v114, v115\n"
        "v_cvt_pk_bf16_f32 v43, v116, v117\n"
        "s_nop 1\n"
        "v_mfma_f32_16x16x32_bf16 v[130:133], v[206:209], v[40:43], v[130:133]\n"
        "v_pk_fma_f16 v108, v120, v124, v108\n"
        "v_pk_fma_f16 v109, v120, v176, v109\n"
        "v_cvt_pk_bf16_f32 v122, v158, v159\n"
        "v_cvt_pk_bf16_f32 v123, v160, v161\n"
        "v_cvt_pk_bf16_f32 v124, v150, v151\n"
        "v_cvt_pk_bf16_f32 v125, v152, v153\n"
        "s_nop 1\n"
        "v_mfma_f32_16x16x32_bf16 v[130:133], v[218:221], v[122:125], v[130:133]\n"
        "v_pk_fma_f16 v110, v120, v196, v110\n"
        "v_pk_fma_f16 v111, v120, v192, v111\n"
        "v_mfma_f32_16x16x32_bf16 v[114:117], v[230:233], v[122:125], 0\n"
        "v_pk_fma_f16 v112, v120, v172, v112\n"
        "v_pk_fma_f16 v113, v120, v188, v113\n"
        "v_mfma_f32_16x16x32_bf16 v[114:117], v[234:237], v[40:43], v[114:117]\n"
        "v_cndmask_b32_e64 v252, v238, v142, s[64:65]\n"
        "v_cndmask_b32_e64 v254, v239, v143, s[64:65]\n"
        "v_mfma_f32_16x16x32_bf16 v[114:117], v[226:229], v[146:149], v[114:117]\n"
        "v_cndmask_b32_e64 v252, v252, v138, s[70:71]\n"
        "v_cndmask_b32_e64 v254, v254, v139, s[70:71]\n"
        "v_mfma_f32_16x16x32_bf16 v[118:121], v[202:205], v[146:149], 0\n"
        "v_cvt_pk_f16_f32 v252, v252, v254\n"
        "v_pk_mul_f16 v252, v252, v255 op_sel_hi:[1,0]\n"
        "v_mfma_f32_16x16x32_bf16 v[118:121], v[210:213], v[40:43], v[118:121]\n"
        "s_add_u32 s46, s26, 590016\n"
        "v_pk_mul_f16 v252, v252, s93 op_sel_hi:[1,0]\n"
        "v_cvt_pk_f16_f32 v178, v130, v131\n"
        "v_mfma_f32_16x16x32_bf16 v[118:121], v[198:201], v[122:125], v[118:121]\n"
        "v_exp_f16_sdwa v252, v252 dst_sel:WORD_0 dst_unused:UNUSED_PRESERVE src0_sel:WORD_0\n"
        "v_mfma_f32_16x16x32_f16 v[34:37], v[56:59], v[108:111], v[34:37]\n"
        "s_min_u32 s46, s46, s42\n"
        "v_exp_f16_sdwa v252, v252 dst_sel:WORD_1 dst_unused:UNUSED_PRESERVE src0_sel:WORD_1\n"
        "v_mfma_f32_16x16x32_f16 v[104:107], v[56:59], v[110:113], v[104:107]\n"
        "v_and_b32 v254, 0xffff0000, v252\n"
        "v_cvt_f32_f16 v253, v252\n"
        "v_mfma_f32_16x16x32_bf16 v[40:43], a[152:155], v[230:233], 0\n"
        "v_cndmask_b32_e64 v253, v253, v242, s[68:69]\n"
        "v_cvt_pk_f16_f32 v179, v132, v133\n"
        "v_mfma_f32_16x16x32_bf16 v[40:43], a[148:151], v[234:237], v[40:43]\n"
        "v_add_f32_dpp v252, v253, v253 row_ror:8 row_mask:0xf bank_mask:0xf\n"
        "v_pk_mul_f16 v178, v178, v46 op_sel_hi:[1,0]\n"
        "v_mfma_f32_16x16x32_bf16 v[40:43], a[144:147], v[226:229], v[40:43]\n"
        "v_pk_mul_f16 v179, v179, v46 op_sel_hi:[1,0]\n"
        "v_pk_mul_f16 v178, v178, v214\n"
        "v_mfma_f32_16x16x32_bf16 v[108:111], a[156:159], v[226:229], 0\n"
        "v_pk_mul_f16 v179, v179, v215\n"
        "v_add_f32_dpp v252, v252, v252 row_ror:4 row_mask:0xf bank_mask:0xf\n"
        "v_mfma_f32_16x16x32_bf16 v[108:111], a[160:163], v[234:237], v[108:111]\n"
        "v_exp_f16_sdwa v178, v178 dst_sel:WORD_0 dst_unused:UNUSED_PRESERVE src0_sel:WORD_0\n"
        "v_mfma_f32_16x16x32_bf16 v[108:111], a[164:167], v[230:233], v[108:111]\n"
        "v_exp_f16_sdwa v179, v179 dst_sel:WORD_0 dst_unused:UNUSED_PRESERVE src0_sel:WORD_0\n"
        "v_mfma_f32_16x16x32_bf16 v[130:133], a[176:179], v[230:233], 0\n"
        "v_exp_f16_sdwa v178, v178 dst_sel:WORD_1 dst_unused:UNUSED_PRESERVE src0_sel:WORD_1\n"
        "v_mfma_f32_16x16x32_bf16 v[130:133], a[172:175], v[234:237], v[130:133]\n"
        "v_exp_f16_sdwa v179, v179 dst_sel:WORD_1 dst_unused:UNUSED_PRESERVE src0_sel:WORD_1\n"
        "v_mfma_f32_16x16x32_bf16 v[130:133], a[168:171], v[226:229], v[130:133]\n"
        "v_add_f32_dpp v252, v252, v252 row_ror:2 row_mask:0xf bank_mask:0xf\n"
        "v_cvt_pk_bf16_f32 v134, v34, v35\n"
        "v_mfma_f32_16x16x32_bf16 v[126:129], a[180:183], v[226:229], 0\n"
        "v_cvt_pk_bf16_f32 v135, v36, v37\n"
        "v_cvt_pk_bf16_f32 v136, v106, v107\n"
        "v_mfma_f32_16x16x32_bf16 v[126:129], a[184:187], v[234:237], v[126:129]\n"
        "buffer_store_dwordx3 v[134:136], v17, s[44:47], s27 offen offset:1152 sc1 nt\n"
        "v_pk_add_f16 v124, v178, v179\n"
        "v_add_f32_dpp v252, v252, v252 row_ror:1 row_mask:0xf bank_mask:0xf\n"
        "v_cvt_pk_f16_f32 v180, v114, v115\n"
        "v_cvt_pk_f16_f32 v181, v116, v117\n"
        "v_mfma_f32_16x16x32_bf16 v[126:129], a[188:191], v[230:233], v[126:129]\n"
        "v_add_f32_dpp v252, v252, v252 row_bcast:15 row_mask:0xa bank_mask:0xf\n"
        "v_pk_mul_f16 v180, v180, v46 op_sel_hi:[1,0]\n"
        "v_mfma_f32_16x16x32_bf16 v[34:37], a[200:203], v[230:233], 0\n"
        "v_pk_mul_f16 v181, v181, v46 op_sel_hi:[1,0]\n"
        "v_pk_mul_f16 v180, v180, v216\n"
        "v_mfma_f32_16x16x32_bf16 v[34:37], a[196:199], v[234:237], v[34:37]\n"
        "v_pk_mul_f16 v181, v181, v217\n"
        "v_add_f32_dpp v252, v252, v252 row_bcast:31 row_mask:0xc bank_mask:0xf\n"
        "v_mfma_f32_16x16x32_bf16 v[34:37], a[192:195], v[226:229], v[34:37]\n"
        "v_exp_f16_sdwa v180, v180 dst_sel:WORD_0 dst_unused:UNUSED_PRESERVE src0_sel:WORD_0\n"
        "v_mfma_f32_16x16x32_bf16 v[134:137], a[204:207], v[226:229], 0\n"
        "v_exp_f16_sdwa v181, v181 dst_sel:WORD_0 dst_unused:UNUSED_PRESERVE src0_sel:WORD_0\n"
        "v_mfma_f32_16x16x32_bf16 v[134:137], a[208:211], v[234:237], v[134:137]\n"
        "v_readlane_b32 s95, v252, 63\n"
        "v_cvt_pk_f16_f32 v182, v118, v119\n"
        "v_mfma_f32_16x16x32_bf16 v[134:137], a[212:215], v[230:233], v[134:137]\n"
        "v_exp_f16_sdwa v180, v180 dst_sel:WORD_1 dst_unused:UNUSED_PRESERVE src0_sel:WORD_1\n"
        "v_mfma_f32_16x16x32_bf16 v[112:115], v[218:221], a[224:227], 0\n"
        "v_exp_f16_sdwa v181, v181 dst_sel:WORD_1 dst_unused:UNUSED_PRESERVE src0_sel:WORD_1\n"
        "v_mfma_f32_16x16x32_bf16 v[112:115], v[206:209], a[220:223], v[112:115]\n"
        "v_pk_add_f16 v124, v124, v180\n"
        "v_pk_add_f16 v124, v124, v181\n"
        "v_mfma_f32_16x16x32_bf16 v[112:115], v[222:225], a[216:219], v[112:115]\n"
        "v_rcp_f32 v251, s95\n"
        "v_mfma_f32_16x16x32_bf16 v[104:107], v[226:229], a[216:219], 0\n"
        "v_mul_f32 v253, v253, v251\n"
        "v_cvt_f16_f32 v253, v253\n"
        "v_mfma_f32_16x16x32_bf16 v[104:107], v[234:237], a[220:223], v[104:107]\n"
        "v_mul_f16 v253, v253, v255\n"
        "v_cvt_pk_f16_f32 v183, v120, v121\n"
        "v_mfma_f32_16x16x32_bf16 v[104:107], v[230:233], a[224:227], v[104:107]\n"
        "ds_write_b16 v22, v253 offset:512\n"
        "v_mfma_f32_16x16x32_bf16 v[116:119], v[198:201], a[224:227], 0\n"
        "v_pk_mul_f16 v182, v182, v46 op_sel_hi:[1,0]\n"
        "v_pk_mul_f16 v183, v183, v46 op_sel_hi:[1,0]\n"
        "v_mfma_f32_16x16x32_bf16 v[116:119], v[210:213], a[220:223], v[116:119]\n"
        "v_pk_mul_f16 v182, v182, v30\n"
        "v_pk_mul_f16 v183, v183, v31\n"
        "v_mfma_f32_16x16x32_bf16 v[116:119], v[202:205], a[216:219], v[116:119]\n"
        "v_exp_f16_sdwa v182, v182 dst_sel:WORD_0 dst_unused:UNUSED_PRESERVE src0_sel:WORD_0\n"
        "v_cvt_pk_bf16_f32 v120, v40, v41\n"
        "v_cvt_pk_bf16_f32 v121, v42, v43\n"
        "v_cvt_pk_bf16_f32 v122, v108, v109\n"
        "v_cvt_pk_bf16_f32 v123, v110, v111\n"
        "s_nop 1\n"
        "v_mfma_f32_16x16x32_bf16 v[138:141], v[222:225], v[120:123], 0\n"
        "v_cvt_pk_bf16_f32 v108, v130, v131\n"
        "v_cvt_pk_bf16_f32 v109, v132, v133\n"
        "v_cvt_pk_bf16_f32 v110, v126, v127\n"
        "v_cvt_pk_bf16_f32 v111, v128, v129\n"
        "s_nop 1\n"
        "v_mfma_f32_16x16x32_bf16 v[138:141], v[206:209], v[108:111], v[138:141]\n"
        "v_cvt_pk_bf16_f32 v40, v34, v35\n"
        "v_cvt_pk_bf16_f32 v41, v36, v37\n"
        "v_cvt_pk_bf16_f32 v42, v134, v135\n"
        "v_cvt_pk_bf16_f32 v43, v136, v137\n"
        "s_nop 1\n"
        "v_mfma_f32_16x16x32_bf16 v[138:141], v[218:221], v[40:43], v[138:141]\n"
        "v_exp_f16_sdwa v183, v183 dst_sel:WORD_0 dst_unused:UNUSED_PRESERVE src0_sel:WORD_0\n"
        "v_mfma_f32_16x16x32_bf16 v[126:129], v[230:233], v[40:43], 0\n"
        "v_exp_f16_sdwa v182, v182 dst_sel:WORD_1 dst_unused:UNUSED_PRESERVE src0_sel:WORD_1\n"
        "v_mfma_f32_16x16x32_bf16 v[126:129], v[234:237], v[108:111], v[126:129]\n"
        "v_exp_f16_sdwa v183, v183 dst_sel:WORD_1 dst_unused:UNUSED_PRESERVE src0_sel:WORD_1\n"
        "v_mfma_f32_16x16x32_bf16 v[126:129], v[226:229], v[120:123], v[126:129]\n"
        "v_pk_add_f16 v124, v124, v182\n"
        "v_pk_add_f16 v124, v124, v183\n"
        "v_mfma_f32_16x16x32_bf16 v[34:37], v[202:205], v[120:123], 0\n"
        "v_cvt_pk_f16_f32 v186, v112, v113\n"
        "v_cvt_pk_f16_f32 v187, v114, v115\n"
        "v_mfma_f32_16x16x32_bf16 v[34:37], v[210:213], v[108:111], v[34:37]\n"
        "v_cvt_pk_f16_f32 v188, v104, v105\n"
        "v_cvt_pk_f16_f32 v189, v106, v107\n"
        "v_mfma_f32_16x16x32_bf16 v[34:37], v[198:201], v[40:43], v[34:37]\n"
        "v_cvt_pk_f16_f32 v194, v116, v117\n"
        "v_cvt_pk_f16_f32 v195, v118, v119\n"
        "v_mfma_f32_16x16x32_bf16 v[108:111], a[152:155], v[198:201], 0\n"
        "v_cvt_pk_f16_f32 v190, v138, v139\n"
        "v_cvt_pk_f16_f32 v191, v140, v141\n"
        "v_mfma_f32_16x16x32_bf16 v[108:111], a[148:151], v[210:213], v[108:111]\n"
        "v_pk_mul_f16 v190, v190, v46 op_sel:[0,1]\n"
        "v_pk_mul_f16 v191, v191, v46 op_sel:[0,1]\n"
        "v_mfma_f32_16x16x32_bf16 v[108:111], a[144:147], v[202:205], v[108:111]\n"
        "v_pk_mul_f16 v190, v190, v214\n"
        "v_pk_mul_f16 v191, v191, v215\n"
        "v_mfma_f32_16x16x32_bf16 v[120:123], a[156:159], v[202:205], 0\n"
        "v_exp_f16_sdwa v190, v190 dst_sel:WORD_0 dst_unused:UNUSED_PRESERVE src0_sel:WORD_0\n"
        "v_mfma_f32_16x16x32_bf16 v[120:123], a[160:163], v[210:213], v[120:123]\n"
        "v_exp_f16_sdwa v191, v191 dst_sel:WORD_0 dst_unused:UNUSED_PRESERVE src0_sel:WORD_0\n"
        "v_mfma_f32_16x16x32_bf16 v[120:123], a[164:167], v[198:201], v[120:123]\n"
        "v_exp_f16_sdwa v190, v190 dst_sel:WORD_1 dst_unused:UNUSED_PRESERVE src0_sel:WORD_1\n"
        "v_mfma_f32_16x16x32_bf16 v[40:43], a[176:179], v[198:201], 0\n"
        "v_exp_f16_sdwa v191, v191 dst_sel:WORD_1 dst_unused:UNUSED_PRESERVE src0_sel:WORD_1\n"
        "v_mfma_f32_16x16x32_bf16 v[40:43], a[172:175], v[210:213], v[40:43]\n"
        "v_pk_add_f16 v125, v190, v191\n"
        "v_cvt_pk_f16_f32 v192, v126, v127\n"
        "v_mfma_f32_16x16x32_bf16 v[40:43], a[168:171], v[202:205], v[40:43]\n"
        "v_cvt_pk_f16_f32 v193, v128, v129\n"
        "v_pk_mul_f16 v192, v192, v46 op_sel:[0,1]\n"
        "v_mfma_f32_16x16x32_bf16 v[112:115], a[180:183], v[202:205], 0\n"
        "v_pk_mul_f16 v193, v193, v46 op_sel:[0,1]\n"
        "v_pk_mul_f16 v192, v192, v216\n"
        "v_mfma_f32_16x16x32_bf16 v[112:115], a[184:187], v[210:213], v[112:115]\n"
        "v_pk_mul_f16 v193, v193, v217\n"
        "v_cvt_pk_f16_f32 v238, v34, v35\n"
        "v_mfma_f32_16x16x32_bf16 v[112:115], a[188:191], v[198:201], v[112:115]\n"
        "v_exp_f16_sdwa v192, v192 dst_sel:WORD_0 dst_unused:UNUSED_PRESERVE src0_sel:WORD_0\n"
        "v_mfma_f32_16x16x32_bf16 v[116:119], a[200:203], v[198:201], 0\n"
        "v_exp_f16_sdwa v193, v193 dst_sel:WORD_0 dst_unused:UNUSED_PRESERVE src0_sel:WORD_0\n"
        "v_mfma_f32_16x16x32_bf16 v[116:119], a[196:199], v[210:213], v[116:119]\n"
        "v_exp_f16_sdwa v192, v192 dst_sel:WORD_1 dst_unused:UNUSED_PRESERVE src0_sel:WORD_1\n"
        "v_mfma_f32_16x16x32_bf16 v[116:119], a[192:195], v[202:205], v[116:119]\n"
        "v_exp_f16_sdwa v193, v193 dst_sel:WORD_1 dst_unused:UNUSED_PRESERVE src0_sel:WORD_1\n"
        "v_mfma_f32_16x16x32_bf16 v[104:107], a[204:207], v[202:205], 0\n"
        "v_pk_add_f16 v125, v125, v192\n"
        "v_pk_add_f16 v125, v125, v193\n"
        "v_mfma_f32_16x16x32_bf16 v[104:107], a[208:211], v[210:213], v[104:107]\n"
        "v_cvt_pk_f16_f32 v239, v36, v37\n"
        "v_pk_mul_f16 v238, v238, v46 op_sel:[0,1]\n"
        "v_mfma_f32_16x16x32_bf16 v[104:107], a[212:215], v[198:201], v[104:107]\n"
        "v_pk_mul_f16 v239, v239, v46 op_sel:[0,1]\n"
        "v_pk_mul_f16 v238, v238, v30\n"
        "v_mfma_f32_16x16x32_bf16 v[34:37], v[198:201], a[236:239], 0\n"
        "v_pk_mul_f16 v239, v239, v31\n"
        "v_cvt_pk_bf16_f32 v128, v108, v109\n"
        "v_mfma_f32_16x16x32_bf16 v[34:37], v[210:213], a[232:235], v[34:37]\n"
        "v_exp_f16_sdwa v238, v238 dst_sel:WORD_0 dst_unused:UNUSED_PRESERVE src0_sel:WORD_0\n"
        "v_mfma_f32_16x16x32_bf16 v[34:37], v[202:205], a[228:231], v[34:37]\n"
        "v_cvt_pk_bf16_f32 v129, v110, v111\n"
        "v_cvt_pk_bf16_f32 v130, v120, v121\n"
        "v_mfma_f32_16x16x32_bf16 v[108:111], v[226:229], a[228:231], 0\n"
        "v_cndmask_b32_e64 v184, 0, v254, s[62:63]\n"
        "v_cndmask_b32_e64 v240, 0, v254, s[64:65]\n"
        "v_mfma_f32_16x16x32_bf16 v[108:111], v[234:237], a[232:235], v[108:111]\n"
        "v_pk_add_f16 v124, v124, v184\n"
        "v_cvt_pk_bf16_f32 v131, v122, v123\n"
        "v_mfma_f32_16x16x32_bf16 v[108:111], v[230:233], a[236:239], v[108:111]\n"
        "v_cvt_pk_bf16_f32 v120, v40, v41\n"
        "v_cvt_pk_bf16_f32 v121, v42, v43\n"
        "v_mfma_f32_16x16x32_bf16 v[132:135], v[218:221], a[236:239], 0\n"
        "v_mov_b64 v[126:127], 0\n"
        "v_cvt_pk_bf16_f32 v122, v112, v113\n"
        "v_mfma_f32_16x16x32_bf16 v[132:135], v[206:209], a[232:235], v[132:135]\n"
        "v_cvt_pk_bf16_f32 v123, v114, v115\n"
        "v_cvt_pk_bf16_f32 v112, v116, v117\n"
        "v_mfma_f32_16x16x32_bf16 v[132:135], v[222:225], a[228:231], v[132:135]\n"
        "v_cvt_pk_bf16_f32 v113, v118, v119\n"
        "v_cvt_pk_bf16_f32 v114, v104, v105\n"
        "v_mfma_f32_16x16x32_bf16 v[40:43], v[222:225], v[128:131], 0\n"
        "v_cvt_pk_bf16_f32 v115, v106, v107\n"
        "v_cvt_pk_f16_f32 v246, v34, v35\n"
        "v_mfma_f32_16x16x32_bf16 v[40:43], v[206:209], v[120:123], v[40:43]\n"
        "v_exp_f16_sdwa v239, v239 dst_sel:WORD_0 dst_unused:UNUSED_PRESERVE src0_sel:WORD_0\n"
        "v_mfma_f32_16x16x32_bf16 v[40:43], v[218:221], v[112:115], v[40:43]\n"
        "v_exp_f16_sdwa v238, v238 dst_sel:WORD_1 dst_unused:UNUSED_PRESERVE src0_sel:WORD_1\n"
        "v_mfma_f32_16x16x32_bf16 v[116:119], v[230:233], v[112:115], 0\n"
        "v_exp_f16_sdwa v239, v239 dst_sel:WORD_1 dst_unused:UNUSED_PRESERVE src0_sel:WORD_1\n"
        "v_mfma_f32_16x16x32_bf16 v[116:119], v[234:237], v[120:123], v[116:119]\n"
        "v_pk_add_f16 v125, v125, v238\n"
        "v_pk_add_f16 v125, v125, v239\n"
        "v_mfma_f32_16x16x32_bf16 v[116:119], v[226:229], v[128:131], v[116:119]\n"
        "v_pk_add_f16 v125, v125, v240\n"
        "v_cvt_pk_f16_f32 v247, v36, v37\n"
        "v_mfma_f32_16x16x32_bf16 v[104:107], v[202:205], v[128:131], 0\n"
        "v_cvt_pk_f16_f32 v36, v108, v109\n"
        "v_cvt_pk_f16_f32 v37, v110, v111\n"
        "v_mfma_f32_16x16x32_bf16 v[104:107], v[210:213], v[120:123], v[104:107]\n"
        "v_cvt_pk_f16_f32 v34, v132, v133\n"
        "v_cvt_pk_f16_f32 v35, v134, v135\n"
        "v_mfma_f32_16x16x32_bf16 v[104:107], v[198:201], v[112:115], v[104:107]\n"
        "v_cvt_pk_f16_f32 v242, v40, v41\n"
        "v_cvt_pk_f16_f32 v243, v42, v43\n"
        "v_mfma_f32_16x16x32_bf16 v[108:111], v[218:221], a[248:251], 0\n"
        "v_pk_mul_f16 v242, v242, v47 op_sel_hi:[1,0]\n"
        "v_pk_mul_f16 v243, v243, v47 op_sel_hi:[1,0]\n"
        "v_mfma_f32_16x16x32_bf16 v[108:111], v[206:209], a[244:247], v[108:111]\n"
        "v_pk_mul_f16 v242, v242, v214\n"
        "v_pk_mul_f16 v243, v243, v215\n"
        "v_mfma_f32_16x16x32_bf16 v[108:111], v[222:225], a[240:243], v[108:111]\n"
        "v_exp_f16_sdwa v242, v242 dst_sel:WORD_0 dst_unused:UNUSED_PRESERVE src0_sel:WORD_0\n"
        "v_mfma_f32_16x16x32_bf16 v[144:147], v[226:229], a[240:243], 0\n"
        "v_exp_f16_sdwa v243, v243 dst_sel:WORD_0 dst_unused:UNUSED_PRESERVE src0_sel:WORD_0\n"
        "v_mfma_f32_16x16x32_bf16 v[144:147], v[234:237], a[244:247], v[144:147]\n"
        "v_exp_f16_sdwa v242, v242 dst_sel:WORD_1 dst_unused:UNUSED_PRESERVE src0_sel:WORD_1\n"
        "v_mfma_f32_16x16x32_bf16 v[144:147], v[230:233], a[248:251], v[144:147]\n"
        "v_exp_f16_sdwa v243, v243 dst_sel:WORD_1 dst_unused:UNUSED_PRESERVE src0_sel:WORD_1\n"
        "v_mfma_f32_16x16x32_bf16 v[148:151], v[198:201], a[248:251], 0\n"
        "v_cvt_pk_f16_f32 v244, v116, v117\n"
        "v_cvt_pk_f16_f32 v245, v118, v119\n"
        "v_mfma_f32_16x16x32_bf16 v[148:151], v[210:213], a[244:247], v[148:151]\n"
        "v_pk_mul_f16 v244, v244, v47 op_sel_hi:[1,0]\n"
        "v_pk_mul_f16 v245, v245, v47 op_sel_hi:[1,0]\n"
        "v_mfma_f32_16x16x32_bf16 v[148:151], v[202:205], a[240:243], v[148:151]\n"
        "v_pk_mul_f16 v244, v244, v216\n"
        "v_pk_mul_f16 v245, v245, v217\n"
        "v_mfma_f32_16x16x32_f16 v[112:115], v[56:59], v[124:127], 0\n"
        "v_exp_f16_sdwa v244, v244 dst_sel:WORD_0 dst_unused:UNUSED_PRESERVE src0_sel:WORD_0\n"
        "v_mfma_f32_16x16x32_bf16 v[174:177], v[202:205], a[252:255], 0\n"
        "v_exp_f16_sdwa v245, v245 dst_sel:WORD_0 dst_unused:UNUSED_PRESERVE src0_sel:WORD_0\n"
        "v_mfma_f32_16x16x32_bf16 v[174:177], v[210:213], v[92:95], v[174:177]\n"
        "v_exp_f16_sdwa v244, v244 dst_sel:WORD_1 dst_unused:UNUSED_PRESERVE src0_sel:WORD_1\n"
        "v_mfma_f32_16x16x32_bf16 v[174:177], v[198:201], v[52:55], v[174:177]\n"
        "v_exp_f16_sdwa v245, v245 dst_sel:WORD_1 dst_unused:UNUSED_PRESERVE src0_sel:WORD_1\n"
        "v_mfma_f32_16x16x32_bf16 v[128:131], v[230:233], v[52:55], 0\n"
        "v_cndmask_b32_e64 v42, 0, v254, s[66:67]\n"
        "v_cvt_pk_f16_f32 v40, v104, v105\n"
        "v_mfma_f32_16x16x32_bf16 v[128:131], v[234:237], v[92:95], v[128:131]\n"
        "ds_read2_b32 v[196:197], v24 offset0:0 offset1:0\n"
        "ds_read2_b32 v[248:249], v24 offset0:1 offset1:1\n"
        "v_mfma_f32_16x16x32_bf16 v[128:131], v[226:229], a[252:255], v[128:131]\n"
        "ds_read2_b32 v[118:119], v24 offset0:2 offset1:2\n"
        "ds_read2_b32 v[252:253], v24 offset0:3 offset1:3\n"
        "v_mfma_f32_16x16x32_bf16 v[140:143], v[222:225], a[252:255], 0\n"
        "ds_read2_b32 v[114:115], v24 offset0:4 offset1:4\n"
        "v_mov_b32 v185, 0\n"
        "v_mfma_f32_16x16x32_bf16 v[140:143], v[206:209], v[92:95], v[140:143]\n"
        "ds_read2_b32 v[122:123], v24 offset0:5 offset1:5\n"
        "v_cvt_pk_f16_f32 v41, v106, v107\n"
        "v_mfma_f32_16x16x32_bf16 v[140:143], v[218:221], v[52:55], v[140:143]\n"
        "v_pk_mul_f16 v40, v40, v47 op_sel_hi:[1,0]\n"
        "v_pk_mul_f16 v41, v41, v47 op_sel_hi:[1,0]\n"
        "v_mfma_f32_16x16x32_bf16 v[136:139], v[218:221], v[76:79], 0\n"
        "v_pk_mul_f16 v40, v40, v30\n"
        "v_pk_mul_f16 v41, v41, v31\n"
        "v_mfma_f32_16x16x32_bf16 v[136:139], v[206:209], v[84:87], v[136:139]\n"
        "v_exp_f16_sdwa v40, v40 dst_sel:WORD_0 dst_unused:UNUSED_PRESERVE src0_sel:WORD_0\n"
        "v_mfma_f32_16x16x32_bf16 v[136:139], v[222:225], v[64:67], v[136:139]\n"
        "v_exp_f16_sdwa v41, v41 dst_sel:WORD_0 dst_unused:UNUSED_PRESERVE src0_sel:WORD_0\n"
        "v_mfma_f32_16x16x32_bf16 v[132:135], v[226:229], v[64:67], 0\n"
        "v_exp_f16_sdwa v40, v40 dst_sel:WORD_1 dst_unused:UNUSED_PRESERVE src0_sel:WORD_1\n"
        "v_mfma_f32_16x16x32_bf16 v[132:135], v[234:237], v[84:87], v[132:135]\n"
        "v_exp_f16_sdwa v41, v41 dst_sel:WORD_1 dst_unused:UNUSED_PRESERVE src0_sel:WORD_1\n"
        "v_mfma_f32_16x16x32_bf16 v[132:135], v[230:233], v[76:79], v[132:135]\n"
        "v_cvt_pk_f16_f32 v104, v108, v109\n"
        "v_cvt_pk_f16_f32 v105, v110, v111\n"
        "v_mfma_f32_16x16x32_bf16 v[154:157], v[198:201], v[76:79], 0\n"
        "s_waitcnt lgkmcnt(0)\n"
        "v_lshlrev_b32 v108, 16, v196\n"
        "v_lshlrev_b32 v109, 16, v248\n"
        "v_mfma_f32_16x16x32_bf16 v[154:157], v[210:213], v[84:87], v[154:157]\n"
        "v_lshlrev_b32 v110, 16, v118\n"
        "v_lshlrev_b32 v111, 16, v252\n"
        "v_mfma_f32_16x16x32_bf16 v[154:157], v[202:205], v[64:67], v[154:157]\n"
        "v_lshlrev_b32 v254, 16, v114\n"
        "v_lshlrev_b32 v255, 16, v122\n"
        "v_mfma_f32_16x16x32_bf16 v[158:161], v[202:205], v[60:63], 0\n"
        "v_cvt_pk_f16_f32 v106, v144, v145\n"
        "v_cvt_pk_f16_f32 v107, v146, v147\n"
        "v_mfma_f32_16x16x32_bf16 v[158:161], v[210:213], v[72:75], v[158:161]\n"
        "v_pk_add_f16 v126, v242, v243\n"
        "v_pk_add_f16 v126, v126, v244\n"
        "v_mfma_f32_16x16x32_bf16 v[158:161], v[198:201], v[48:51], v[158:161]\n"
        "v_pk_add_f16 v126, v126, v245\n"
        "v_pk_add_f16 v126, v126, v40\n"
        "v_mfma_f32_16x16x32_bf16 v[162:165], v[230:233], v[48:51], 0\n"
        "v_cvt_pk_f16_f32 v116, v148, v149\n"
        "v_cvt_pk_f16_f32 v117, v150, v151\n"
        "v_mfma_f32_16x16x32_bf16 v[162:165], v[234:237], v[72:75], v[162:165]\n"
        "v_pk_add_f16 v126, v126, v41\n"
        "v_pk_add_f16 v126, v126, v42\n"
        "v_mfma_f32_16x16x32_bf16 v[162:165], v[226:229], v[60:63], v[162:165]\n"
        "v_rcp_f32 v250, v112\n"
        "v_mfma_f32_16x16x32_bf16 v[144:147], v[222:225], v[60:63], 0\n"
        "v_mov_b32 v241, 0\n"
        "v_cvt_f16_f32 v173, v250\n"
        "v_mfma_f32_16x16x32_bf16 v[144:147], v[206:209], v[72:75], v[144:147]\n"
        "v_pk_mul_f16 v178, v178, v173 op_sel_hi:[1,0]\n"
        "v_pk_mul_f16 v179, v179, v173 op_sel_hi:[1,0]\n"
        "v_mfma_f32_16x16x32_bf16 v[144:147], v[218:221], v[48:51], v[144:147]\n"
        "v_pk_mul_f16 v180, v180, v173 op_sel_hi:[1,0]\n"
        "v_pk_mul_f16 v181, v181, v173 op_sel_hi:[1,0]\n"
        "v_mfma_f32_16x16x32_f16 v[150:153], v[56:59], v[124:127], 0\n"
        "v_pk_mul_f16 v182, v182, v173 op_sel_hi:[1,0]\n"
        "v_pk_mul_f16 v183, v183, v173 op_sel_hi:[1,0]\n"
        "v_pk_mul_f16 v178, v178, v214\n"
        "v_pk_mul_f16 v179, v179, v215\n"
        "v_pk_mul_f16 v180, v180, v216\n"
        "v_pk_mul_f16 v181, v181, v217\n"
        "s_nop 1\n"
        "v_mfma_f32_16x16x32_f16 v[148:151], v[178:181], v[186:189], 0\n"
        "v_pk_mul_f16 v184, v184, v173 op_sel_hi:[1,0]\n"
        "v_pk_mul_f16 v182, v182, v30\n"
        "v_pk_mul_f16 v183, v183, v31\n"
        "s_nop 1\n"
        "v_mfma_f32_16x16x32_f16 v[148:151], v[182:185], v[194:197], v[148:151]\n"
        "v_rcp_f32 v169, v113\n"
        "v_mfma_f32_16x16x32_bf16 v[148:151], v[222:225], v[88:91], v[148:151]\n"
        "v_max3_f32 v153, s95, v112, v113\n"
        "v_add_f32 v47, v251, v250\n"
        "v_mfma_f32_16x16x32_bf16 v[170:173], v[222:225], v[80:83], 0\n"
        "v_cvt_f16_f32 v251, v169\n"
        "v_add_f32 v47, v47, v169\n"
        "v_mfma_f32_16x16x32_f16 v[170:173], v[182:185], v[246:249], v[170:173]\n"
        "v_pk_mul_f16 v190, v190, v251 op_sel_hi:[1,0]\n"
        "v_pk_mul_f16 v191, v191, v251 op_sel_hi:[1,0]\n"
        "v_mfma_f32_16x16x32_f16 v[170:173], v[178:181], v[34:37], v[170:173]\n"
        "v_pk_mul_f16 v192, v192, v251 op_sel_hi:[1,0]\n"
        "v_pk_mul_f16 v193, v193, v251 op_sel_hi:[1,0]\n"
        "v_mfma_f32_16x16x32_f16 v[166:169], v[178:181], v[104:107], 0\n"
        "v_pk_mul_f16 v238, v238, v251 op_sel_hi:[1,0]\n"
        "v_pk_mul_f16 v239, v239, v251 op_sel_hi:[1,0]\n"
        "v_mfma_f32_16x16x32_f16 v[166:169], v[182:185], v[116:119], v[166:169]\n"
        "v_pk_mul_f16 v240, v240, v251 op_sel_hi:[1,0]\n"
        "v_cvt_pk_f16_f32 v250, v174, v175\n"
        "v_mfma_f32_16x16x32_bf16 v[166:169], v[206:209], v[88:91], v[166:169]\n"
        "v_cvt_pk_f16_f32 v251, v176, v177\n"
        "v_cvt_pk_f16_f32 v126, v128, v129\n"
        "v_mfma_f32_16x16x32_bf16 v[174:177], v[206:209], v[80:83], 0\n"
        "v_cvt_pk_f16_f32 v127, v130, v131\n"
        "v_cvt_pk_f16_f32 v124, v140, v141\n"
        "v_mfma_f32_16x16x32_f16 v[174:177], v[182:185], v[250:253], v[174:177]\n"
        "v_cvt_pk_f16_f32 v125, v142, v143\n"
        "v_cvt_pk_f16_f32 v128, v136, v137\n"
        "s_nop 0\n"
        "v_mfma_f32_16x16x32_f16 v[174:177], v[178:181], v[124:127], v[174:177]\n"
        "v_cvt_pk_f16_f32 v129, v138, v139\n"
        "v_cvt_pk_f16_f32 v130, v132, v133\n"
        "v_cvt_pk_f16_f32 v131, v134, v135\n"
        "s_nop 1\n"
        "v_mfma_f32_16x16x32_f16 v[140:143], v[178:181], v[128:131], 0\n"
        "v_cvt_pk_f16_f32 v112, v154, v155\n"
        "v_cvt_pk_f16_f32 v113, v156, v157\n"
        "s_nop 1\n"
        "v_mfma_f32_16x16x32_f16 v[140:143], v[182:185], v[112:115], v[140:143]\n"
        "v_pk_mul_f16 v190, v190, v214\n"
        "v_pk_mul_f16 v191, v191, v215\n"
        "v_mfma_f32_16x16x32_bf16 v[140:143], v[218:221], v[88:91], v[140:143]\n"
        "v_mov_b32 v43, 0\n"
        "v_cvt_pk_f16_f32 v120, v158, v159\n"
        "v_mfma_f32_16x16x32_bf16 v[132:135], v[218:221], v[80:83], 0\n"
        "v_cvt_pk_f16_f32 v121, v160, v161\n"
        "v_cvt_pk_f16_f32 v138, v162, v163\n"
        "s_nop 0\n"
        "v_mfma_f32_16x16x32_f16 v[132:135], v[182:185], v[120:123], v[132:135]\n"
        "v_cvt_pk_f16_f32 v139, v164, v165\n"
        "v_cvt_pk_f16_f32 v136, v144, v145\n"
        "v_cvt_pk_f16_f32 v137, v146, v147\n"
        "s_nop 1\n"
        "v_mfma_f32_16x16x32_f16 v[132:135], v[178:181], v[136:139], v[132:135]\n"
        "s_add_u32 s90, s89, 0\n"
        "v_pk_mul_f16 v192, v192, v216\n"
        "v_pk_mul_f16 v193, v193, v217\n"
        "s_nop 1\n"
        "v_mfma_f32_16x16x32_f16 v[154:157], v[190:193], v[186:189], 0\n"
        "v_pk_mul_f16 v238, v238, v30\n"
        "v_pk_mul_f16 v239, v239, v31\n"
        "s_nop 1\n"
        "v_mfma_f32_16x16x32_f16 v[154:157], v[238:241], v[194:197], v[154:157]\n"
        "v_rcp_f32 v185, v152\n"
        "v_mfma_f32_16x16x32_bf16 v[154:157], v[226:229], v[88:91], v[154:157]\n"
        "v_max_f32 v153, v153, v152\n"
        "v_add_f32 v47, v47, v185\n"
        "v_mfma_f32_16x16x32_bf16 v[158:161], v[226:229], v[80:83], 0\n"
        "v_fma_f32 v153, v47, s82, v153\n"
        "v_cmp_nlt_f32_e64 s[74:75], v153, s83\n"
        "v_mfma_f32_16x16x32_f16 v[158:161], v[238:241], v[246:249], v[158:161]\n"
        "v_cvt_pk_bf16_f32 v164, v148, v170\n"
        "v_cvt_pk_bf16_f32 v165, v166, v174\n"
        "v_mfma_f32_16x16x32_f16 v[158:161], v[190:193], v[34:37], v[158:161]\n"
        "s_cmp_lg_u64 s[74:75], 0\n"
        "s_cbranch_scc1 SLOW_%=\n"
        "v_cvt_pk_bf16_f32 v166, v140, v132\n"
        "v_mfma_f32_16x16x32_f16 v[144:147], v[190:193], v[104:107], 0\n"
        "buffer_store_dwordx3 v[164:166], v14, s[40:43], s90 offen sc1 nt\n"
        "v_cvt_pk_bf16_f32 v162, v149, v171\n"
        "v_cvt_pk_bf16_f32 v163, v167, v175\n"
        "v_cvt_pk_bf16_f32 v164, v141, v133\n"
        "v_cvt_f16_f32 v229, v185\n"
        "v_mfma_f32_16x16x32_f16 v[144:147], v[238:241], v[116:119], v[144:147]\n"
        "s_add_u32 s91, s89, 98112\n"
        "v_cvt_pk_bf16_f32 v170, v150, v172\n"
        "v_cvt_pk_bf16_f32 v171, v168, v176\n"
        "v_mfma_f32_16x16x32_bf16 v[144:147], v[234:237], v[88:91], v[144:147]\n"
        "buffer_store_dwordx3 v[162:164], v15, s[40:43], s90 offen offset:768 sc1 nt\n"
        "v_cvt_pk_bf16_f32 v172, v142, v134\n"
        "v_pk_mul_f16 v242, v242, v229 op_sel_hi:[1,0]\n"
        "v_cvt_pk_bf16_f32 v140, v151, v173\n"
        "v_cvt_pk_bf16_f32 v141, v169, v177\n"
        "v_mfma_f32_16x16x32_bf16 v[162:165], v[234:237], v[80:83], 0\n"
        "ds_read_b128 v[166:169], v20 offset:512\n"
        "ds_read_b128 v[148:151], v20 offset:528\n"
        "v_mfma_f32_16x16x32_f16 v[162:165], v[238:241], v[250:253], v[162:165]\n"
        "buffer_store_dwordx3 v[170:172], v14, s[40:43], s91 offen offset:192 sc1 nt\n"
        "v_cvt_pk_bf16_f32 v142, v143, v135\n"
        "v_pk_mul_f16 v243, v243, v229 op_sel_hi:[1,0]\n"
        "v_pk_mul_f16 v244, v244, v229 op_sel_hi:[1,0]\n"
        "v_pk_mul_f16 v245, v245, v229 op_sel_hi:[1,0]\n"
        "v_mfma_f32_16x16x32_f16 v[162:165], v[190:193], v[124:127], v[162:165]\n"
        "v_pk_mul_f16 v40, v40, v229 op_sel_hi:[1,0]\n"
        "v_pk_mul_f16 v41, v41, v229 op_sel_hi:[1,0]\n"
        "v_mfma_f32_16x16x32_f16 v[132:135], v[190:193], v[128:131], 0\n"
        "buffer_store_dwordx3 v[140:142], v16, s[40:43], s91 offen offset:960 sc1 nt\n"
        "v_pk_mul_f16 v42, v42, v229 op_sel_hi:[1,0]\n"
        "v_pk_mul_f16 v242, v242, v214\n"
        "v_pk_mul_f16 v243, v243, v215\n"
        "v_pk_mul_f16 v244, v244, v216\n"
        "v_mfma_f32_16x16x32_f16 v[132:135], v[238:241], v[112:115], v[132:135]\n"
        "v_pk_mul_f16 v245, v245, v217\n"
        "v_pk_mul_f16 v40, v40, v30\n"
        "v_mfma_f32_16x16x32_bf16 v[132:135], v[230:233], v[88:91], v[132:135]\n"
        "v_pk_mul_f16 v41, v41, v31\n"
        "v_cvt_pk_bf16_f32 v178, v154, v158\n"
        "v_mfma_f32_16x16x32_bf16 v[170:173], v[230:233], v[80:83], 0\n"
        "v_cvt_pk_bf16_f32 v179, v144, v162\n"
        "v_cvt_pk_bf16_f32 v182, v155, v159\n"
        "v_mfma_f32_16x16x32_f16 v[170:173], v[238:241], v[120:123], v[170:173]\n"
        "s_waitcnt lgkmcnt(0)\n"
        "v_pk_mul_f16 v140, v166, v186\n"
        "v_pk_fma_f16 v140, v167, v187, v140\n"
        "v_mfma_f32_16x16x32_f16 v[170:173], v[190:193], v[136:139], v[170:173]\n"
        "v_pk_fma_f16 v140, v168, v188, v140\n"
        "v_pk_fma_f16 v140, v169, v189, v140\n"
        "v_mfma_f32_16x16x32_f16 v[152:155], v[242:245], v[186:189], 0\n"
        "v_pk_fma_f16 v140, v148, v194, v140\n"
        "v_pk_fma_f16 v140, v149, v195, v140\n"
        "v_mfma_f32_16x16x32_f16 v[152:155], v[40:43], v[194:197], v[152:155]\n"
        "s_add_u32 s90, s89, 196224\n"
        "v_pk_fma_f16 v140, v150, v196, v140\n"
        "v_pk_mul_f16 v141, v166, v34\n"
        "v_mfma_f32_16x16x32_bf16 v[152:155], v[202:205], v[88:91], v[152:155]\n"
        "v_pk_fma_f16 v141, v167, v35, v141\n"
        "v_pk_fma_f16 v141, v168, v36, v141\n"
        "v_mfma_f32_16x16x32_bf16 v[174:177], v[202:205], v[80:83], 0\n"
        "v_pk_fma_f16 v141, v169, v37, v141\n"
        "v_pk_fma_f16 v141, v148, v246, v141\n"
        "v_mfma_f32_16x16x32_f16 v[174:177], v[40:43], v[246:249], v[174:177]\n"
        "v_pk_fma_f16 v141, v149, v247, v141\n"
        "v_pk_fma_f16 v141, v150, v248, v141\n"
        "v_mfma_f32_16x16x32_f16 v[174:177], v[242:245], v[34:37], v[174:177]\n"
        "s_add_u32 s91, s89, 294336\n"
        "v_pk_mul_f16 v142, v166, v104\n"
        "v_pk_fma_f16 v142, v167, v105, v142\n"
        "v_mfma_f32_16x16x32_f16 v[34:37], v[242:245], v[104:107], 0\n"
        "v_pk_fma_f16 v142, v168, v106, v142\n"
        "v_pk_fma_f16 v142, v169, v107, v142\n"
        "v_mfma_f32_16x16x32_f16 v[34:37], v[40:43], v[116:119], v[34:37]\n"
        "v_pk_fma_f16 v142, v148, v116, v142\n"
        "v_pk_fma_f16 v142, v149, v117, v142\n"
        "v_mfma_f32_16x16x32_bf16 v[34:37], v[210:213], v[88:91], v[34:37]\n"
        "v_pk_fma_f16 v142, v150, v118, v142\n"
        "v_pk_mul_f16 v143, v166, v124\n"
        "v_mfma_f32_16x16x32_bf16 v[104:107], v[210:213], v[80:83], 0\n"
        "v_pk_fma_f16 v143, v167, v125, v143\n"
        "v_pk_fma_f16 v143, v168, v126, v143\n"
        "v_mfma_f32_16x16x32_f16 v[104:107], v[40:43], v[250:253], v[104:107]\n"
        "v_pk_fma_f16 v143, v169, v127, v143\n"
        "v_pk_fma_f16 v143, v148, v250, v143\n"
        "v_mfma_f32_16x16x32_f16 v[104:107], v[242:245], v[124:127], v[104:107]\n"
        "v_pk_fma_f16 v143, v149, v251, v143\n"
        "v_cvt_pk_bf16_f32 v180, v132, v170\n"
        "v_mfma_f32_16x16x32_f16 v[116:119], v[242:245], v[128:131], 0\n"
        "buffer_store_dwordx3 v[178:180], v14, s[40:43], s90 offen offset:384 sc1 nt\n"
        "v_pk_fma_f16 v143, v150, v252, v143\n"
        "v_cvt_pk_bf16_f32 v183, v145, v163\n"
        "v_cvt_pk_bf16_f32 v184, v133, v171\n"
        "v_pk_mul_f16 v144, v166, v128\n"
        "v_mfma_f32_16x16x32_f16 v[116:119], v[40:43], v[112:115], v[116:119]\n"
        "buffer_store_dwordx3 v[182:184], v17, s[40:43], s90 offen offset:1152 sc1 nt\n"
        "v_cvt_pk_bf16_f32 v124, v156, v160\n"
        "v_cvt_pk_bf16_f32 v125, v146, v164\n"
        "v_cvt_pk_bf16_f32 v126, v134, v172\n"
        "v_pk_fma_f16 v144, v167, v129, v144\n"
        "v_mfma_f32_16x16x32_bf16 v[116:119], v[198:201], v[88:91], v[116:119]\n"
        "buffer_store_dwordx3 v[124:126], v14, s[40:43], s91 offen offset:576 sc1 nt\n"
        "v_cvt_pk_bf16_f32 v132, v157, v161\n"
        "v_cvt_pk_bf16_f32 v133, v147, v165\n"
        "s_add_u32 s90, s89, 392448\n"
        "v_cvt_pk_bf16_f32 v134, v135, v173\n"
        "v_mfma_f32_16x16x32_bf16 v[124:127], v[198:201], v[80:83], 0\n"
        "buffer_store_dwordx3 v[132:134], v14, s[40:43], s90 offen sc1 nt\n"
        "v_pk_fma_f16 v144, v168, v130, v144\n"
        "v_pk_fma_f16 v144, v169, v131, v144\n"
        "v_pk_fma_f16 v144, v148, v112, v144\n"
        "v_pk_fma_f16 v144, v149, v113, v144\n"
        "v_mfma_f32_16x16x32_f16 v[124:127], v[40:43], v[120:123], v[124:127]\n"
        "v_pk_fma_f16 v144, v150, v114, v144\n"
        "v_pk_mul_f16 v145, v166, v136\n"
        "v_mfma_f32_16x16x32_f16 v[124:127], v[242:245], v[136:139], v[124:127]\n"
        "v_pk_fma_f16 v145, v167, v137, v145\n"
        "v_pk_fma_f16 v145, v168, v138, v145\n"
        "v_mfma_f32_16x16x32_f16 v[108:111], v[56:59], v[140:143], v[108:111]\n"
        "s_add_u32 s46, s89, 590016\n"
        "v_pk_fma_f16 v145, v169, v139, v145\n"
        "v_pk_fma_f16 v145, v148, v120, v145\n"
        "v_pk_fma_f16 v145, v149, v121, v145\n"
        "v_pk_fma_f16 v145, v150, v122, v145\n"
        "s_nop 1\n"
        "v_mfma_f32_16x16x32_f16 v[252:255], v[56:59], v[142:145], v[252:255]\n"
        "v_cvt_pk_bf16_f32 v112, v152, v174\n"
        "v_cvt_pk_bf16_f32 v40, v153, v175\n"
        "v_cvt_pk_bf16_f32 v120, v154, v176\n"
        "v_cvt_pk_bf16_f32 v128, v155, v177\n"
        "v_cvt_pk_bf16_f32 v113, v34, v104\n"
        "v_cvt_pk_bf16_f32 v41, v35, v105\n"
        "v_cvt_pk_bf16_f32 v114, v116, v124\n"
        "buffer_store_dwordx3 v[112:114], v15, s[40:43], s90 offen offset:768 sc1 nt\n"
        "v_cvt_pk_bf16_f32 v121, v36, v106\n"
        "v_cvt_pk_bf16_f32 v129, v37, v107\n"
        "v_cvt_pk_bf16_f32 v42, v117, v125\n"
        "s_add_u32 s91, s89, 490560\n"
        "v_cvt_pk_bf16_f32 v122, v118, v126\n"
        "buffer_store_dwordx3 v[40:42], v14, s[40:43], s91 offen offset:192 sc1 nt\n"
        "s_min_u32 s46, s46, s42\n"
        "v_cvt_pk_bf16_f32 v130, v119, v127\n"
        "v_cvt_pk_bf16_f32 v34, v108, v109\n"
        "v_cvt_pk_bf16_f32 v35, v110, v111\n"
        "v_cvt_pk_bf16_f32 v36, v254, v255\n"
        "buffer_store_dwordx3 v[120:122], v16, s[40:43], s91 offen offset:960 sc1 nt\n"
        "s_add_u32 s90, s89, 588672\n"
        "buffer_store_dwordx3 v[128:130], v14, s[40:43], s90 offen offset:384 sc1 nt\n"
        "buffer_store_dwordx3 v[34:36], v17, s[44:47], s90 offen offset:1152 sc1 nt\n"
        "FIN_%=:\n"
        ".Lswin_fin:\n"
        "s_endpgm\n"
        "SLOW_%=:\n"
        "s_waitcnt lgkmcnt(0)\n"
        "s_nop 15\n"
        "v_mbcnt_lo_u32_b32 v255, -1, 0\n"
        "v_mbcnt_hi_u32_b32 v255, -1, v255\n"
        "v_and_b32 v254, 15, v255\n"
        "v_mul_u32_u24 v252, 24, v254\n"
        "v_sub_u32 v253, v24, v252\n"
        "v_lshrrev_b32 v250, 4, v255\n"
        "v_mul_u32_u24 v252, 96, v250\n"
        "v_add_u32 v253, v253, v252\n"
        "ds_read_b64 v[132:133], v253\n"
        "ds_read_b64 v[30:31], v253 offset:24\n"
        "ds_read_b64 v[34:35], v253 offset:48\n"
        "ds_read_b64 v[46:47], v253 offset:72\n"
        "ds_read_b64 v[40:41], v253 offset:8\n"
        "ds_read_b64 v[136:137], v253 offset:32\n"
        "ds_read_b64 v[104:105], v253 offset:56\n"
        "ds_read_b64 v[144:145], v253 offset:80\n"
        "ds_read_b64 v[124:125], v253 offset:16\n"
        "ds_read_b64 v[116:117], v253 offset:40\n"
        "ds_read_b64 v[108:109], v253 offset:64\n"
        "ds_read_b64 v[140:141], v253 offset:88\n"
        "v_mfma_f32_16x16x32_bf16 v[120:123], v[222:225], v[222:225], 0\n"
        "v_mov_b32 v253, 0x05040100\n"
        "v_subrev_u32 v248, 158080, v20\n"
        "v_mfma_f32_16x16x32_bf16 v[120:123], v[206:209], v[206:209], v[120:123]\n"
        "v_mov_b32 v249, 161664\n"
        "v_mov_b32 v166, 0x3f803f80\n"
        "v_mfma_f32_16x16x32_bf16 v[120:123], v[218:221], v[218:221], v[120:123]\n"
        "v_mov_b32 v167, 0x3f803f80\n"
        "v_mov_b32 v168, 0x3f803f80\n"
        "v_mov_b32 v169, 0x3f803f80\n"
        "s_nop 1\n"
        "v_mfma_f32_16x16x32_bf16 v[112:115], v[166:169], v[218:221], 0\n"
        "v_mov_b32 v255, 0xff800000\n"
        "v_mov_b32 v139, 0\n"
        "v_mfma_f32_16x16x32_bf16 v[112:115], v[166:169], v[206:209], v[112:115]\n"
        "s_waitcnt lgkmcnt(11)\n"
        "v_mov_b32 v43, 0\n"
        "v_mov_b32 v143, 0\n"
        "v_mfma_f32_16x16x32_bf16 v[112:115], v[166:169], v[222:225], v[112:115]\n"
        "s_waitcnt lgkmcnt(10)\n"
        "v_mov_b32 v119, 0\n"
        "v_mov_b32 v107, 0\n"
        "v_mfma_f32_16x16x32_bf16 v[128:131], v[226:229], v[226:229], 0\n"
        "s_waitcnt lgkmcnt(9)\n"
        "v_mov_b32 v37, 0\n"
        "v_mov_b32 v111, 0\n"
        "v_mfma_f32_16x16x32_bf16 v[128:131], v[234:237], v[234:237], v[128:131]\n"
        "s_waitcnt lgkmcnt(8)\n"
        "v_mov_b32 v147, 0\n"
        "v_mov_b32 v135, 0\n"
        "v_mfma_f32_16x16x32_bf16 v[128:131], v[230:233], v[230:233], v[128:131]\n"
        "s_waitcnt lgkmcnt(7)\n"
        "v_mov_b32 v127, 0\n"
        "v_and_b32 v248, 384, v248\n"
        "v_mfma_f32_16x16x32_bf16 v[162:165], v[166:169], v[230:233], 0\n"
        "s_waitcnt lgkmcnt(6)\n"
        "v_lshlrev_b32 v248, 1, v248\n"
        "v_add_u32 v248, 160640, v248\n"
        "v_mfma_f32_16x16x32_bf16 v[162:165], v[166:169], v[234:237], v[162:165]\n"
        "s_waitcnt lgkmcnt(5)\n"
        "v_lshlrev_b32 v251, 2, v254\n"
        "v_lshlrev_b32 v252, 4, v250\n"
        "v_mfma_f32_16x16x32_bf16 v[162:165], v[166:169], v[226:229], v[162:165]\n"
        "s_waitcnt lgkmcnt(4)\n"
        "v_add_u32 v251, v251, v248\n"
        "v_add_u32 v252, v252, v248\n"
        "v_mfma_f32_16x16x32_bf16 v[152:155], v[202:205], v[202:205], 0\n"
        "s_waitcnt lgkmcnt(3)\n"
        "v_cndmask_b32 v249, v249, v251, vcc\n"
        "v_cndmask_b32_e64 v254, v120, v121, s[56:57]\n"
        "v_mfma_f32_16x16x32_bf16 v[152:155], v[210:213], v[210:213], v[152:155]\n"
        "s_waitcnt lgkmcnt(2)\n"
        "v_cndmask_b32_e64 v254, v254, v122, s[58:59]\n"
        "v_cndmask_b32_e64 v254, v254, v123, s[60:61]\n"
        "v_mfma_f32_16x16x32_bf16 v[152:155], v[198:201], v[198:201], v[152:155]\n"
        "s_waitcnt lgkmcnt(1)\n"
        "v_mul_f32 v254, s80, v254\n"
        "v_mul_f32 v250, s80, v112\n"
        "v_mfma_f32_16x16x32_bf16 v[156:159], v[166:169], v[198:201], 0\n"
        "s_waitcnt lgkmcnt(0)\n"
        "v_fma_f32 v250, -v250, v250, v254\n"
        "v_max_f32 v250, 0, v250\n"
        "v_mfma_f32_16x16x32_bf16 v[156:159], v[166:169], v[210:213], v[156:159]\n"
        "v_add_f32 v250, s81, v250\n"
        "v_perm_b32 v120, v133, v132, v253\n"
        "v_mfma_f32_16x16x32_bf16 v[156:159], v[166:169], v[202:205], v[156:159]\n"
        "v_rsq_f32 v250, v250\n"
        "v_perm_b32 v121, v31, v30, v253\n"
        "v_perm_b32 v122, v35, v34, v253\n"
        "v_perm_b32 v123, v47, v46, v253\n"
        "s_nop 1\n"
        "v_mfma_f32_16x16x32_bf16 v[170:173], v[120:123], v[120:123], 0\n"
        "ds_write_b32 v249, v250\n"
        "v_perm_b32 v112, v41, v40, v253\n"
        "v_perm_b32 v113, v137, v136, v253\n"
        "v_perm_b32 v114, v105, v104, v253\n"
        "v_perm_b32 v115, v145, v144, v253\n"
        "s_nop 1\n"
        "v_mfma_f32_16x16x32_bf16 v[170:173], v[112:115], v[112:115], v[170:173]\n"
        "v_perm_b32 v148, v125, v124, v253\n"
        "v_perm_b32 v149, v117, v116, v253\n"
        "v_perm_b32 v150, v109, v108, v253\n"
        "v_perm_b32 v151, v141, v140, v253\n"
        "s_nop 1\n"
        "v_mfma_f32_16x16x32_bf16 v[170:173], v[148:151], v[148:151], v[170:173]\n"
        "v_cndmask_b32_e64 v254, v128, v129, s[56:57]\n"
        "v_cndmask_b32_e64 v254, v254, v130, s[58:59]\n"
        "v_mfma_f32_16x16x32_bf16 v[158:161], v[166:169], v[148:151], 0\n"
        "v_cndmask_b32_e64 v254, v254, v131, s[60:61]\n"
        "v_mul_f32 v254, s80, v254\n"
        "v_mfma_f32_16x16x32_bf16 v[158:161], v[166:169], v[112:115], v[158:161]\n"
        "v_mul_f32 v253, s80, v162\n"
        "v_fma_f32 v253, -v253, v253, v254\n"
        "v_mfma_f32_16x16x32_bf16 v[158:161], v[166:169], v[120:123], v[158:161]\n"
        "v_max_f32 v253, 0, v253\n"
        "v_add_f32 v253, s81, v253\n"
        "v_mfma_f32_16x16x32_bf16 v[130:133], v[222:225], a[216:219], 0\n"
        "v_rsq_f32 v253, v253\n"
        "v_mfma_f32_16x16x32_bf16 v[130:133], v[206:209], a[220:223], v[130:133]\n"
        "ds_write_b32 v249, v253 offset:64\n"
        "v_mfma_f32_16x16x32_bf16 v[130:133], v[218:221], a[224:227], v[130:133]\n"
        "v_cndmask_b32_e64 v254, v152, v153, s[56:57]\n"
        "v_cndmask_b32_e64 v254, v254, v154, s[58:59]\n"
        "v_mfma_f32_16x16x32_bf16 v[164:167], v[230:233], a[224:227], 0\n"
        "v_cndmask_b32_e64 v254, v254, v155, s[60:61]\n"
        "v_mul_f32 v254, s80, v254\n"
        "v_mfma_f32_16x16x32_bf16 v[164:167], v[234:237], a[220:223], v[164:167]\n"
        "v_mul_f32 v253, s80, v156\n"
        "v_fma_f32 v253, -v253, v253, v254\n"
        "v_mfma_f32_16x16x32_bf16 v[164:167], v[226:229], a[216:219], v[164:167]\n"
        "v_max_f32 v253, 0, v253\n"
        "v_add_f32 v253, s81, v253\n"
        "v_mfma_f32_16x16x32_bf16 v[160:163], v[202:205], a[216:219], 0\n"
        "v_rsq_f32 v253, v253\n"
        "v_mfma_f32_16x16x32_bf16 v[160:163], v[210:213], a[220:223], v[160:163]\n"
        "ds_write_b32 v249, v253 offset:128\n"
        "v_mfma_f32_16x16x32_bf16 v[160:163], v[198:201], a[224:227], v[160:163]\n"
        "v_cndmask_b32_e64 v253, v170, v171, s[56:57]\n"
        "v_cndmask_b32_e64 v253, v253, v172, s[58:59]\n"
        "v_mfma_f32_16x16x32_bf16 v[152:155], v[148:151], a[224:227], 0\n"
        "v_cndmask_b32_e64 v253, v253, v173, s[60:61]\n"
        "v_mul_f32 v253, s80, v253\n"
        "v_mfma_f32_16x16x32_bf16 v[152:155], v[112:115], a[220:223], v[152:155]\n"
        "v_mul_f32 v254, s80, v158\n"
        "v_fma_f32 v254, -v254, v254, v253\n"
        "v_mfma_f32_16x16x32_bf16 v[152:155], v[120:123], a[216:219], v[152:155]\n"
        "v_max_f32 v254, 0, v254\n"
        "v_add_f32 v254, s81, v254\n"
        "v_mfma_f32_16x16x32_bf16 v[156:159], v[202:205], a[228:231], 0\n"
        "v_rsq_f32 v254, v254\n"
        "v_mfma_f32_16x16x32_bf16 v[156:159], v[210:213], a[232:235], v[156:159]\n"
        "ds_write_b32 v249, v254 offset:192\n"
        "v_mfma_f32_16x16x32_bf16 v[156:159], v[198:201], a[236:239], v[156:159]\n"
        "v_cvt_pk_bf16_f32 v128, v130, v131\n"
        "v_cvt_pk_bf16_f32 v129, v132, v133\n"
        "v_mfma_f32_16x16x32_bf16 v[168:171], v[230:233], a[236:239], 0\n"
        "v_cvt_pk_bf16_f32 v130, v164, v165\n"
        "v_cvt_pk_bf16_f32 v131, v166, v167\n"
        "v_mfma_f32_16x16x32_bf16 v[168:171], v[234:237], a[232:235], v[168:171]\n"
        "v_cvt_pk_bf16_f32 v136, v160, v161\n"
        "v_cvt_pk_bf16_f32 v137, v162, v163\n"
        "v_mfma_f32_16x16x32_bf16 v[168:171], v[226:229], a[228:231], v[168:171]\n"
        "s_nop 0\n"
        "v_cvt_pk_bf16_f32 v138, v152, 0\n"
        "v_cvt_pk_bf16_f32 v40, v156, v157\n"
        "v_mfma_f32_16x16x32_bf16 v[160:163], v[222:225], a[228:231], 0\n"
        "s_nop 0\n"
        "v_cvt_pk_bf16_f32 v41, v158, v159\n"
        "v_mfma_f32_16x16x32_bf16 v[160:163], v[206:209], a[232:235], v[160:163]\n"
        "s_nop 0\n"
        "v_cvt_pk_bf16_f32 v154, v168, v169\n"
        "v_cvt_pk_bf16_f32 v155, v170, v171\n"
        "v_mfma_f32_16x16x32_bf16 v[160:163], v[218:221], a[236:239], v[160:163]\n"
        "s_nop 1\n"
        "v_mfma_f32_16x16x32_bf16 v[156:159], v[148:151], a[236:239], 0\n"
        "s_nop 2\n"
        "v_mfma_f32_16x16x32_bf16 v[156:159], v[112:115], a[232:235], v[156:159]\n"
        "s_nop 0\n"
        "v_cvt_pk_bf16_f32 v152, v160, v161\n"
        "v_cvt_pk_bf16_f32 v153, v162, v163\n"
        "v_mfma_f32_16x16x32_bf16 v[156:159], v[120:123], a[228:231], v[156:159]\n"
        "s_nop 1\n"
        "v_mfma_f32_16x16x32_bf16 v[162:165], v[222:225], a[240:243], 0\n"
        "s_nop 2\n"
        "v_mfma_f32_16x16x32_bf16 v[162:165], v[206:209], a[244:247], v[162:165]\n"
        "s_nop 0\n"
        "v_cvt_pk_bf16_f32 v42, v156, 0\n"
        "v_mfma_f32_16x16x32_bf16 v[162:165], v[218:221], a[248:251], v[162:165]\n"
        "s_nop 1\n"
        "v_mfma_f32_16x16x32_bf16 v[166:169], v[230:233], a[248:251], 0\n"
        "s_nop 2\n"
        "v_mfma_f32_16x16x32_bf16 v[166:169], v[234:237], a[244:247], v[166:169]\n"
        "s_nop 0\n"
        "v_cvt_pk_bf16_f32 v160, v162, v163\n"
        "v_cvt_pk_bf16_f32 v161, v164, v165\n"
        "v_mfma_f32_16x16x32_bf16 v[166:169], v[226:229], a[240:243], v[166:169]\n"
        "s_nop 1\n"
        "v_mfma_f32_16x16x32_bf16 v[156:159], v[202:205], a[240:243], 0\n"
        "s_nop 2\n"
        "v_mfma_f32_16x16x32_bf16 v[156:159], v[210:213], a[244:247], v[156:159]\n"
        "s_nop 0\n"
        "v_cvt_pk_bf16_f32 v162, v166, v167\n"
        "v_cvt_pk_bf16_f32 v163, v168, v169\n"
        "v_mfma_f32_16x16x32_bf16 v[156:159], v[198:201], a[248:251], v[156:159]\n"
        "s_nop 1\n"
        "v_mfma_f32_16x16x32_bf16 v[164:167], v[148:151], a[248:251], 0\n"
        "s_nop 2\n"
        "v_mfma_f32_16x16x32_bf16 v[164:167], v[112:115], a[244:247], v[164:167]\n"
        "s_nop 0\n"
        "v_cvt_pk_bf16_f32 v140, v156, v157\n"
        "v_cvt_pk_bf16_f32 v141, v158, v159\n"
        "v_mfma_f32_16x16x32_bf16 v[164:167], v[120:123], a[240:243], v[164:167]\n"
        "s_nop 1\n"
        "v_mfma_f32_16x16x32_bf16 v[156:159], v[202:205], a[252:255], 0\n"
        "s_nop 2\n"
        "v_mfma_f32_16x16x32_bf16 v[156:159], v[210:213], v[92:95], v[156:159]\n"
        "s_nop 0\n"
        "v_cvt_pk_bf16_f32 v142, v164, 0\n"
        "v_mfma_f32_16x16x32_bf16 v[156:159], v[198:201], v[52:55], v[156:159]\n"
        "s_nop 1\n"
        "v_mfma_f32_16x16x32_bf16 v[164:167], v[230:233], v[52:55], 0\n"
        "s_nop 2\n"
        "v_mfma_f32_16x16x32_bf16 v[164:167], v[234:237], v[92:95], v[164:167]\n"
        "s_nop 0\n"
        "v_cvt_pk_bf16_f32 v116, v156, v157\n"
        "v_cvt_pk_bf16_f32 v117, v158, v159\n"
        "v_mfma_f32_16x16x32_bf16 v[164:167], v[226:229], a[252:255], v[164:167]\n"
        "s_nop 1\n"
        "v_mfma_f32_16x16x32_bf16 v[168:171], v[222:225], a[252:255], 0\n"
        "s_nop 2\n"
        "v_mfma_f32_16x16x32_bf16 v[168:171], v[206:209], v[92:95], v[168:171]\n"
        "s_nop 0\n"
        "v_cvt_pk_bf16_f32 v158, v164, v165\n"
        "v_cvt_pk_bf16_f32 v159, v166, v167\n"
        "v_mfma_f32_16x16x32_bf16 v[168:171], v[218:221], v[52:55], v[168:171]\n"
        "s_nop 1\n"
        "v_mfma_f32_16x16x32_bf16 v[164:167], v[148:151], v[52:55], 0\n"
        "s_nop 2\n"
        "v_mfma_f32_16x16x32_bf16 v[164:167], v[112:115], v[92:95], v[164:167]\n"
        "s_nop 0\n"
        "v_cvt_pk_bf16_f32 v156, v168, v169\n"
        "v_cvt_pk_bf16_f32 v157, v170, v171\n"
        "v_mfma_f32_16x16x32_bf16 v[164:167], v[120:123], a[252:255], v[164:167]\n"
        "s_nop 1\n"
        "v_mfma_f32_16x16x32_bf16 v[168:171], v[222:225], v[64:67], 0\n"
        "s_nop 2\n"
        "v_mfma_f32_16x16x32_bf16 v[168:171], v[206:209], v[84:87], v[168:171]\n"
        "s_nop 0\n"
        "v_cvt_pk_bf16_f32 v118, v164, 0\n"
        "v_mfma_f32_16x16x32_bf16 v[168:171], v[218:221], v[76:79], v[168:171]\n"
        "s_nop 1\n"
        "v_mfma_f32_16x16x32_bf16 v[176:179], v[230:233], v[76:79], 0\n"
        "s_nop 2\n"
        "v_mfma_f32_16x16x32_bf16 v[176:179], v[234:237], v[84:87], v[176:179]\n"
        "s_nop 0\n"
        "v_cvt_pk_bf16_f32 v164, v168, v169\n"
        "v_cvt_pk_bf16_f32 v165, v170, v171\n"
        "v_mfma_f32_16x16x32_bf16 v[176:179], v[226:229], v[64:67], v[176:179]\n"
        "s_nop 1\n"
        "v_mfma_f32_16x16x32_bf16 v[172:175], v[202:205], v[64:67], 0\n"
        "s_nop 2\n"
        "v_mfma_f32_16x16x32_bf16 v[172:175], v[210:213], v[84:87], v[172:175]\n"
        "s_nop 0\n"
        "v_cvt_pk_bf16_f32 v166, v176, v177\n"
        "v_cvt_pk_bf16_f32 v167, v178, v179\n"
        "v_mfma_f32_16x16x32_bf16 v[172:175], v[198:201], v[76:79], v[172:175]\n"
        "s_nop 1\n"
        "v_mfma_f32_16x16x32_bf16 v[168:171], v[148:151], v[76:79], 0\n"
        "s_nop 2\n"
        "v_mfma_f32_16x16x32_bf16 v[168:171], v[112:115], v[84:87], v[168:171]\n"
        "s_nop 0\n"
        "v_cvt_pk_bf16_f32 v104, v172, v173\n"
        "v_cvt_pk_bf16_f32 v105, v174, v175\n"
        "v_mfma_f32_16x16x32_bf16 v[168:171], v[120:123], v[64:67], v[168:171]\n"
        "s_nop 1\n"
        "v_mfma_f32_16x16x32_bf16 v[172:175], v[202:205], v[60:63], 0\n"
        "s_nop 2\n"
        "v_mfma_f32_16x16x32_bf16 v[172:175], v[210:213], v[72:75], v[172:175]\n"
        "s_nop 0\n"
        "v_cvt_pk_bf16_f32 v106, v168, 0\n"
        "v_mfma_f32_16x16x32_bf16 v[172:175], v[198:201], v[48:51], v[172:175]\n"
        "v_mfma_f32_16x16x32_bf16 v[192:195], v[230:233], v[48:51], 0\n"
        "v_mfma_f32_16x16x32_bf16 v[192:195], v[234:237], v[72:75], v[192:195]\n"
        "ds_read_b128 v[168:171], v252\n"
        "ds_read_b128 v[176:179], v252 offset:64\n"
        "v_mfma_f32_16x16x32_bf16 v[192:195], v[226:229], v[60:63], v[192:195]\n"
        "ds_read_b128 v[180:183], v252 offset:128\n"
        "ds_read_b32 v250, v251\n"
        "v_mfma_f32_16x16x32_bf16 v[188:191], v[222:225], v[60:63], 0\n"
        "ds_read_b32 v252, v251 offset:64\n"
        "ds_read_b32 v254, v251 offset:128\n"
        "v_mfma_f32_16x16x32_bf16 v[188:191], v[206:209], v[72:75], v[188:191]\n"
        "ds_read_b32 v253, v251 offset:192\n"
        "v_cvt_pk_bf16_f32 v34, v172, v173\n"
        "v_mfma_f32_16x16x32_bf16 v[188:191], v[218:221], v[48:51], v[188:191]\n"
        "s_nop 0\n"
        "v_cvt_pk_bf16_f32 v35, v174, v175\n"
        "v_cvt_pk_bf16_f32 v174, v192, v193\n"
        "v_mfma_f32_16x16x32_bf16 v[184:187], v[148:151], v[48:51], 0\n"
        "s_nop 0\n"
        "v_cvt_pk_bf16_f32 v175, v194, v195\n"
        "v_mfma_f32_16x16x32_bf16 v[184:187], v[112:115], v[72:75], v[184:187]\n"
        "s_nop 0\n"
        "v_cvt_pk_bf16_f32 v172, v188, v189\n"
        "v_cvt_pk_bf16_f32 v173, v190, v191\n"
        "v_mfma_f32_16x16x32_bf16 v[184:187], v[120:123], v[60:63], v[184:187]\n"
        "s_nop 1\n"
        "v_mfma_f32_16x16x32_bf16 v[188:191], a[144:147], v[222:225], 0\n"
        "s_nop 2\n"
        "v_mfma_f32_16x16x32_bf16 v[188:191], a[148:151], v[206:209], v[188:191]\n"
        "s_nop 0\n"
        "v_cvt_pk_bf16_f32 v36, v184, 0\n"
        "v_mfma_f32_16x16x32_bf16 v[188:191], a[152:155], v[218:221], v[188:191]\n"
        "s_nop 1\n"
        "v_mfma_f32_16x16x32_bf16 v[184:187], a[164:167], v[218:221], 0\n"
        "s_waitcnt lgkmcnt(0)\n"
        "s_nop 1\n"
        "v_mfma_f32_16x16x32_bf16 v[184:187], a[160:163], v[206:209], v[184:187]\n"
        "s_nop 0\n"
        "v_cvt_pk_bf16_f32 v238, v188, v189\n"
        "v_cvt_pk_bf16_f32 v239, v190, v191\n"
        "v_mfma_f32_16x16x32_bf16 v[184:187], a[156:159], v[222:225], v[184:187]\n"
        "s_nop 1\n"
        "v_mfma_f32_16x16x32_bf16 v[188:191], a[168:171], v[222:225], 0\n"
        "s_nop 2\n"
        "v_mfma_f32_16x16x32_bf16 v[188:191], a[172:175], v[206:209], v[188:191]\n"
        "s_nop 0\n"
        "v_cvt_pk_bf16_f32 v240, v184, v185\n"
        "v_cvt_pk_bf16_f32 v241, v186, v187\n"
        "v_mfma_f32_16x16x32_bf16 v[188:191], a[176:179], v[218:221], v[188:191]\n"
        "s_nop 1\n"
        "v_mfma_f32_16x16x32_bf16 v[184:187], a[188:191], v[218:221], 0\n"
        "s_nop 2\n"
        "v_mfma_f32_16x16x32_bf16 v[184:187], a[184:187], v[206:209], v[184:187]\n"
        "s_nop 0\n"
        "v_cvt_pk_bf16_f32 v214, v188, v189\n"
        "v_cvt_pk_bf16_f32 v215, v190, v191\n"
        "v_mfma_f32_16x16x32_bf16 v[184:187], a[180:183], v[222:225], v[184:187]\n"
        "s_nop 1\n"
        "v_mfma_f32_16x16x32_bf16 v[188:191], a[192:195], v[222:225], 0\n"
        "s_nop 2\n"
        "v_mfma_f32_16x16x32_bf16 v[188:191], a[196:199], v[206:209], v[188:191]\n"
        "s_nop 0\n"
        "v_cvt_pk_bf16_f32 v216, v184, v185\n"
        "v_cvt_pk_bf16_f32 v217, v186, v187\n"
        "v_mfma_f32_16x16x32_bf16 v[188:191], a[200:203], v[218:221], v[188:191]\n"
        "s_nop 1\n"
        "v_mfma_f32_16x16x32_bf16 v[192:195], a[212:215], v[218:221], 0\n"
        "s_nop 2\n"
        "v_mfma_f32_16x16x32_bf16 v[192:195], a[208:211], v[206:209], v[192:195]\n"
        "s_nop 0\n"
        "v_cvt_pk_bf16_f32 v184, v188, v189\n"
        "v_cvt_pk_bf16_f32 v185, v190, v191\n"
        "v_mfma_f32_16x16x32_bf16 v[192:195], a[204:207], v[222:225], v[192:195]\n"
        "s_nop 1\n"
        "v_mfma_f32_16x16x32_bf16 v[188:191], v[222:225], v[238:241], 0\n"
        "s_nop 2\n"
        "v_mfma_f32_16x16x32_bf16 v[188:191], v[206:209], v[214:217], v[188:191]\n"
        "s_nop 0\n"
        "v_cvt_pk_bf16_f32 v186, v192, v193\n"
        "v_cvt_pk_bf16_f32 v187, v194, v195\n"
        "s_nop 1\n"
        "v_mfma_f32_16x16x32_bf16 v[188:191], v[218:221], v[184:187], v[188:191]\n"
        "s_nop 1\n"
        "v_mfma_f32_16x16x32_bf16 v[192:195], v[230:233], v[184:187], 0\n"
        "s_nop 2\n"
        "v_mfma_f32_16x16x32_bf16 v[192:195], v[234:237], v[214:217], v[192:195]\n"
        "s_nop 0\n"
        "v_mul_f32 v188, v188, v168\n"
        "v_mul_f32 v189, v189, v169\n"
        "v_mfma_f32_16x16x32_bf16 v[192:195], v[226:229], v[238:241], v[192:195]\n"
        "s_nop 0\n"
        "v_mul_f32 v190, v190, v170\n"
        "v_mul_f32 v191, v191, v171\n"
        "v_mfma_f32_16x16x32_bf16 v[242:245], v[202:205], v[238:241], 0\n"
        "s_nop 0\n"
        "v_max3_f32 v251, v188, v189, v190\n"
        "v_mfma_f32_16x16x32_bf16 v[242:245], v[210:213], v[214:217], v[242:245]\n"
        "s_nop 0\n"
        "v_mul_f32 v192, v192, v176\n"
        "v_mul_f32 v193, v193, v177\n"
        "v_mfma_f32_16x16x32_bf16 v[242:245], v[198:201], v[184:187], v[242:245]\n"
        "v_mul_f32 v194, v194, v178\n"
        "v_mul_f32 v195, v195, v179\n"
        "v_mfma_f32_16x16x32_bf16 v[246:249], v[148:151], v[184:187], 0\n"
        "s_nop 0\n"
        "v_max3_f32 v251, v251, v191, v192\n"
        "v_max3_f32 v251, v251, v193, v194\n"
        "v_mfma_f32_16x16x32_bf16 v[246:249], v[112:115], v[214:217], v[246:249]\n"
        "s_nop 0\n"
        "v_mul_f32 v242, v242, v180\n"
        "v_mul_f32 v243, v243, v181\n"
        "v_mfma_f32_16x16x32_bf16 v[246:249], v[120:123], v[238:241], v[246:249]\n"
        "s_nop 0\n"
        "v_mul_f32 v244, v244, v182\n"
        "v_mul_f32 v245, v245, v183\n"
        "v_max3_f32 v251, v251, v195, v242\n"
        "v_max3_f32 v251, v251, v243, v244\n"
        "s_nop 2\n"
        "v_mul_f32 v246, v246, v253\n"
        "v_cndmask_b32_e64 v246, v255, v246, s[62:63]\n"
        "v_max3_f32 v251, v251, v245, v246\n"
        "v_mov_b32 v249, v251\n"
        "s_nop 1\n"
        "v_permlane32_swap_b32 v251, v249\n"
        "v_max_f32 v251, v251, v249\n"
        "v_mov_b32 v249, v251\n"
        "s_nop 1\n"
        "v_permlane16_swap_b32 v251, v249\n"
        "v_max_f32 v251, v251, v249\n"
        "v_mul_f32 v251, v251, v250\n"
        "v_fma_f32 v188, v188, v250, -v251\n"
        "v_exp_f32 v188, v188\n"
        "v_fma_f32 v189, v189, v250, -v251\n"
        "v_exp_f32 v189, v189\n"
        "s_nop 0\n"
        "v_add_f32 v249, v188, v189\n"
        "v_mul_f32 v188, v188, v168\n"
        "v_fma_f32 v246, v246, v250, -v251\n"
        "v_exp_f32 v246, v246\n"
        "v_fma_f32 v245, v245, v250, -v251\n"
        "v_exp_f32 v245, v245\n"
        "v_fma_f32 v244, v244, v250, -v251\n"
        "v_exp_f32 v244, v244\n"
        "v_fma_f32 v243, v243, v250, -v251\n"
        "v_exp_f32 v243, v243\n"
        "v_fma_f32 v242, v242, v250, -v251\n"
        "v_exp_f32 v242, v242\n"
        "v_fma_f32 v195, v195, v250, -v251\n"
        "v_exp_f32 v195, v195\n"
        "v_fma_f32 v194, v194, v250, -v251\n"
        "v_exp_f32 v194, v194\n"
        "v_fma_f32 v193, v193, v250, -v251\n"
        "v_exp_f32 v193, v193\n"
        "v_fma_f32 v192, v192, v250, -v251\n"
        "v_exp_f32 v192, v192\n"
        "v_fma_f32 v191, v191, v250, -v251\n"
        "v_exp_f32 v191, v191\n"
        "v_fma_f32 v190, v190, v250, -v251\n"
        "v_exp_f32 v190, v190\n"
        "s_nop 0\n"
        "v_add_f32 v249, v249, v190\n"
        "v_add_f32 v249, v249, v191\n"
        "v_add_f32 v249, v249, v192\n"
        "v_add_f32 v249, v249, v193\n"
        "v_add_f32 v249, v249, v194\n"
        "v_add_f32 v249, v249, v195\n"
        "v_add_f32 v249, v249, v242\n"
        "v_add_f32 v249, v249, v243\n"
        "v_add_f32 v249, v249, v244\n"
        "v_add_f32 v249, v249, v245\n"
        "v_add_f32 v249, v249, v246\n"
        "v_mov_b32 v251, v249\n"
        "s_nop 1\n"
        "v_permlane32_swap_b32 v249, v251\n"
        "v_add_f32 v249, v249, v251\n"
        "v_mov_b32 v251, v249\n"
        "s_nop 1\n"
        "v_permlane16_swap_b32 v249, v251\n"
        "v_add_f32 v249, v249, v251\n"
        "v_rcp_f32 v249, v249\n"
        "s_nop 0\n"
        "v_mul_f32 v188, v188, v249\n"
        "v_mul_f32 v189, v189, v169\n"
        "v_mul_f32 v189, v189, v249\n"
        "v_cvt_pk_bf16_f32 v214, v188, v189\n"
        "v_mul_f32 v190, v190, v170\n"
        "v_mul_f32 v190, v190, v249\n"
        "v_mul_f32 v191, v191, v171\n"
        "v_mul_f32 v191, v191, v249\n"
        "v_cvt_pk_bf16_f32 v215, v190, v191\n"
        "v_mul_f32 v192, v192, v176\n"
        "v_mul_f32 v192, v192, v249\n"
        "v_mul_f32 v193, v193, v177\n"
        "v_mul_f32 v193, v193, v249\n"
        "v_cvt_pk_bf16_f32 v216, v192, v193\n"
        "v_mul_f32 v194, v194, v178\n"
        "v_mul_f32 v194, v194, v249\n"
        "v_mul_f32 v195, v195, v179\n"
        "v_mul_f32 v195, v195, v249\n"
        "v_cvt_pk_bf16_f32 v217, v194, v195\n"
        "s_nop 1\n"
        "v_mfma_f32_16x16x32_bf16 v[184:187], v[214:217], v[128:131], 0\n"
        "v_mul_f32 v242, v242, v180\n"
        "v_mul_f32 v243, v243, v181\n"
        "v_mul_f32 v242, v242, v249\n"
        "v_mul_f32 v243, v243, v249\n"
        "v_cvt_pk_bf16_f32 v108, v242, v243\n"
        "v_mul_f32 v244, v244, v182\n"
        "v_mul_f32 v244, v244, v249\n"
        "v_mul_f32 v245, v245, v183\n"
        "v_mul_f32 v245, v245, v249\n"
        "v_cvt_pk_bf16_f32 v109, v244, v245\n"
        "v_mul_f32 v246, v246, v253\n"
        "v_mul_f32 v246, v246, v249\n"
        "v_cvt_pk_bf16_f32 v110, v246, 0\n"
        "s_nop 1\n"
        "v_mfma_f32_16x16x32_bf16 v[184:187], v[108:111], v[136:139], v[184:187]\n"
        "v_mfma_f32_16x16x32_bf16 v[184:187], v[222:225], v[88:91], v[184:187]\n"
        "v_mfma_f32_16x16x32_bf16 v[188:191], v[222:225], v[80:83], 0\n"
        "v_mfma_f32_16x16x32_bf16 v[188:191], v[108:111], v[40:43], v[188:191]\n"
        "v_mfma_f32_16x16x32_bf16 v[188:191], v[214:217], v[152:155], v[188:191]\n"
        "s_add_u32 s90, s89, 0\n"
        "s_nop 0\n"
        "v_mfma_f32_16x16x32_bf16 v[192:195], v[214:217], v[160:163], 0\n"
        "s_nop 2\n"
        "v_mfma_f32_16x16x32_bf16 v[192:195], v[108:111], v[140:143], v[192:195]\n"
        "s_nop 0\n"
        "v_cvt_pk_bf16_f32 v124, v184, v188\n"
        "v_cvt_pk_bf16_f32 v238, v185, v189\n"
        "v_mfma_f32_16x16x32_bf16 v[192:195], v[206:209], v[88:91], v[192:195]\n"
        "v_cvt_pk_bf16_f32 v132, v186, v190\n"
        "v_cvt_pk_bf16_f32 v144, v187, v191\n"
        "v_mfma_f32_16x16x32_bf16 v[184:187], v[206:209], v[80:83], 0\n"
        "s_add_u32 s91, s89, 98112\n"
        "v_mfma_f32_16x16x32_bf16 v[184:187], v[108:111], v[116:119], v[184:187]\n"
        "v_mfma_f32_16x16x32_bf16 v[184:187], v[214:217], v[156:159], v[184:187]\n"
        "s_nop 1\n"
        "v_mfma_f32_16x16x32_bf16 v[188:191], v[214:217], v[164:167], 0\n"
        "s_nop 2\n"
        "v_mfma_f32_16x16x32_bf16 v[188:191], v[108:111], v[104:107], v[188:191]\n"
        "s_nop 0\n"
        "v_cvt_pk_bf16_f32 v125, v192, v184\n"
        "v_cvt_pk_bf16_f32 v239, v193, v185\n"
        "v_mfma_f32_16x16x32_bf16 v[188:191], v[218:221], v[88:91], v[188:191]\n"
        "v_cvt_pk_bf16_f32 v133, v194, v186\n"
        "v_cvt_pk_bf16_f32 v145, v195, v187\n"
        "v_mfma_f32_16x16x32_bf16 v[184:187], v[218:221], v[80:83], 0\n"
        "v_mfma_f32_16x16x32_bf16 v[184:187], v[108:111], v[34:37], v[184:187]\n"
        "v_mfma_f32_16x16x32_bf16 v[184:187], v[214:217], v[172:175], v[184:187]\n"
        "s_nop 1\n"
        "v_mfma_f32_16x16x32_bf16 v[108:111], a[144:147], v[226:229], 0\n"
        "s_nop 2\n"
        "v_mfma_f32_16x16x32_bf16 v[108:111], a[148:151], v[234:237], v[108:111]\n"
        "s_nop 0\n"
        "v_cvt_pk_bf16_f32 v126, v188, v184\n"
        "v_cvt_pk_bf16_f32 v240, v189, v185\n"
        "v_mfma_f32_16x16x32_bf16 v[108:111], a[152:155], v[230:233], v[108:111]\n"
        "s_nop 0\n"
        "v_cvt_pk_bf16_f32 v134, v190, v186\n"
        "v_cvt_pk_bf16_f32 v146, v191, v187\n"
        "v_mfma_f32_16x16x32_bf16 v[188:191], a[164:167], v[230:233], 0\n"
        "s_nop 1\n"
        "v_mfma_f32_16x16x32_bf16 v[188:191], a[160:163], v[234:237], v[188:191]\n"
        "s_nop 0\n"
        "v_cvt_pk_bf16_f32 v184, v108, v109\n"
        "v_cvt_pk_bf16_f32 v185, v110, v111\n"
        "v_mfma_f32_16x16x32_bf16 v[188:191], a[156:159], v[226:229], v[188:191]\n"
        "s_nop 0\n"
        "v_mfma_f32_16x16x32_bf16 v[108:111], a[168:171], v[226:229], 0\n"
        "buffer_store_dwordx3 v[124:126], v14, s[40:43], s90 offen sc1 nt\n"
        "s_nop 2\n"
        "v_mfma_f32_16x16x32_bf16 v[108:111], a[172:175], v[234:237], v[108:111]\n"
        "s_nop 0\n"
        "v_cvt_pk_bf16_f32 v186, v188, v189\n"
        "v_cvt_pk_bf16_f32 v187, v190, v191\n"
        "v_mfma_f32_16x16x32_bf16 v[108:111], a[176:179], v[230:233], v[108:111]\n"
        "buffer_store_dwordx3 v[238:240], v15, s[40:43], s90 offen offset:768 sc1 nt\n"
        "s_nop 0\n"
        "v_mfma_f32_16x16x32_bf16 v[214:217], a[188:191], v[230:233], 0\n"
        "s_nop 2\n"
        "v_mfma_f32_16x16x32_bf16 v[214:217], a[184:187], v[234:237], v[214:217]\n"
        "buffer_store_dwordx3 v[132:134], v14, s[40:43], s91 offen offset:192 sc1 nt\n"
        "v_cvt_pk_bf16_f32 v188, v108, v109\n"
        "v_cvt_pk_bf16_f32 v189, v110, v111\n"
        "v_mfma_f32_16x16x32_bf16 v[214:217], a[180:183], v[226:229], v[214:217]\n"
        "s_nop 0\n"
        "v_mfma_f32_16x16x32_bf16 v[192:195], a[192:195], v[226:229], 0\n"
        "buffer_store_dwordx3 v[144:146], v16, s[40:43], s91 offen offset:960 sc1 nt\n"
        "s_nop 2\n"
        "v_mfma_f32_16x16x32_bf16 v[192:195], a[196:199], v[234:237], v[192:195]\n"
        "s_nop 0\n"
        "v_cvt_pk_bf16_f32 v190, v214, v215\n"
        "v_cvt_pk_bf16_f32 v191, v216, v217\n"
        "v_mfma_f32_16x16x32_bf16 v[192:195], a[200:203], v[230:233], v[192:195]\n"
        "s_nop 1\n"
        "v_mfma_f32_16x16x32_bf16 v[108:111], a[212:215], v[230:233], 0\n"
        "s_nop 2\n"
        "v_mfma_f32_16x16x32_bf16 v[108:111], a[208:211], v[234:237], v[108:111]\n"
        "s_nop 0\n"
        "v_cvt_pk_bf16_f32 v214, v192, v193\n"
        "v_cvt_pk_bf16_f32 v215, v194, v195\n"
        "v_mfma_f32_16x16x32_bf16 v[108:111], a[204:207], v[226:229], v[108:111]\n"
        "s_nop 1\n"
        "v_mfma_f32_16x16x32_bf16 v[238:241], v[222:225], v[184:187], 0\n"
        "s_nop 2\n"
        "v_mfma_f32_16x16x32_bf16 v[238:241], v[206:209], v[188:191], v[238:241]\n"
        "s_nop 0\n"
        "v_cvt_pk_bf16_f32 v216, v108, v109\n"
        "v_cvt_pk_bf16_f32 v217, v110, v111\n"
        "s_nop 1\n"
        "v_mfma_f32_16x16x32_bf16 v[238:241], v[218:221], v[214:217], v[238:241]\n"
        "s_nop 1\n"
        "v_mfma_f32_16x16x32_bf16 v[108:111], v[230:233], v[214:217], 0\n"
        "s_nop 2\n"
        "v_mfma_f32_16x16x32_bf16 v[108:111], v[234:237], v[188:191], v[108:111]\n"
        "s_nop 0\n"
        "v_mul_f32 v238, v238, v168\n"
        "v_mul_f32 v239, v239, v169\n"
        "v_mfma_f32_16x16x32_bf16 v[108:111], v[226:229], v[184:187], v[108:111]\n"
        "s_nop 0\n"
        "v_mul_f32 v240, v240, v170\n"
        "v_mul_f32 v241, v241, v171\n"
        "v_mfma_f32_16x16x32_bf16 v[192:195], v[202:205], v[184:187], 0\n"
        "s_nop 0\n"
        "v_max3_f32 v250, v238, v239, v240\n"
        "v_mfma_f32_16x16x32_bf16 v[192:195], v[210:213], v[188:191], v[192:195]\n"
        "s_nop 0\n"
        "v_mul_f32 v108, v108, v176\n"
        "v_mul_f32 v109, v109, v177\n"
        "v_mfma_f32_16x16x32_bf16 v[192:195], v[198:201], v[214:217], v[192:195]\n"
        "v_mul_f32 v110, v110, v178\n"
        "v_mul_f32 v111, v111, v179\n"
        "v_mfma_f32_16x16x32_bf16 v[242:245], v[148:151], v[214:217], 0\n"
        "s_nop 0\n"
        "v_max3_f32 v250, v250, v241, v108\n"
        "v_max3_f32 v250, v250, v109, v110\n"
        "v_mfma_f32_16x16x32_bf16 v[242:245], v[112:115], v[188:191], v[242:245]\n"
        "s_nop 0\n"
        "v_mul_f32 v192, v192, v180\n"
        "v_mul_f32 v193, v193, v181\n"
        "v_mfma_f32_16x16x32_bf16 v[242:245], v[120:123], v[184:187], v[242:245]\n"
        "s_nop 0\n"
        "v_mul_f32 v194, v194, v182\n"
        "v_mul_f32 v195, v195, v183\n"
        "v_max3_f32 v250, v250, v111, v192\n"
        "v_max3_f32 v250, v250, v193, v194\n"
        "s_nop 2\n"
        "v_mul_f32 v242, v242, v253\n"
        "v_cndmask_b32_e64 v242, v255, v242, s[62:63]\n"
        "v_max3_f32 v250, v250, v195, v242\n"
        "v_mov_b32 v251, v250\n"
        "s_nop 1\n"
        "v_permlane32_swap_b32 v250, v251\n"
        "v_max_f32 v250, v250, v251\n"
        "v_mov_b32 v251, v250\n"
        "s_nop 1\n"
        "v_permlane16_swap_b32 v250, v251\n"
        "v_max_f32 v250, v250, v251\n"
        "v_mul_f32 v250, v250, v252\n"
        "v_fma_f32 v238, v238, v252, -v250\n"
        "v_exp_f32 v238, v238\n"
        "v_fma_f32 v239, v239, v252, -v250\n"
        "v_exp_f32 v239, v239\n"
        "s_nop 0\n"
        "v_add_f32 v251, v238, v239\n"
        "v_mul_f32 v238, v238, v168\n"
        "v_fma_f32 v242, v242, v252, -v250\n"
        "v_exp_f32 v242, v242\n"
        "v_fma_f32 v195, v195, v252, -v250\n"
        "v_exp_f32 v195, v195\n"
        "v_fma_f32 v194, v194, v252, -v250\n"
        "v_exp_f32 v194, v194\n"
        "v_fma_f32 v193, v193, v252, -v250\n"
        "v_exp_f32 v193, v193\n"
        "v_fma_f32 v192, v192, v252, -v250\n"
        "v_exp_f32 v192, v192\n"
        "v_fma_f32 v111, v111, v252, -v250\n"
        "v_exp_f32 v111, v111\n"
        "v_fma_f32 v110, v110, v252, -v250\n"
        "v_exp_f32 v110, v110\n"
        "v_fma_f32 v109, v109, v252, -v250\n"
        "v_exp_f32 v109, v109\n"
        "v_fma_f32 v108, v108, v252, -v250\n"
        "v_exp_f32 v108, v108\n"
        "v_fma_f32 v241, v241, v252, -v250\n"
        "v_exp_f32 v241, v241\n"
        "v_fma_f32 v240, v240, v252, -v250\n"
        "v_exp_f32 v240, v240\n"
        "s_nop 0\n"
        "v_add_f32 v251, v251, v240\n"
        "v_add_f32 v251, v251, v241\n"
        "v_add_f32 v251, v251, v108\n"
        "v_add_f32 v251, v251, v109\n"
        "v_add_f32 v251, v251, v110\n"
        "v_add_f32 v251, v251, v111\n"
        "v_add_f32 v251, v251, v192\n"
        "v_add_f32 v251, v251, v193\n"
        "v_add_f32 v251, v251, v194\n"
        "v_add_f32 v251, v251, v195\n"
        "v_add_f32 v251, v251, v242\n"
        "v_mov_b32 v252, v251\n"
        "s_nop 1\n"
        "v_permlane32_swap_b32 v251, v252\n"
        "v_add_f32 v251, v251, v252\n"
        "v_mov_b32 v252, v251\n"
        "s_nop 1\n"
        "v_permlane16_swap_b32 v251, v252\n"
        "v_add_f32 v251, v251, v252\n"
        "v_rcp_f32 v251, v251\n"
        "s_nop 0\n"
        "v_mul_f32 v238, v238, v251\n"
        "v_mul_f32 v239, v239, v169\n"
        "v_mul_f32 v239, v239, v251\n"
        "v_cvt_pk_bf16_f32 v188, v238, v239\n"
        "v_mul_f32 v240, v240, v170\n"
        "v_mul_f32 v240, v240, v251\n"
        "v_mul_f32 v241, v241, v171\n"
        "v_mul_f32 v241, v241, v251\n"
        "v_cvt_pk_bf16_f32 v189, v240, v241\n"
        "v_mul_f32 v108, v108, v176\n"
        "v_mul_f32 v108, v108, v251\n"
        "v_mul_f32 v109, v109, v177\n"
        "v_mul_f32 v109, v109, v251\n"
        "v_cvt_pk_bf16_f32 v190, v108, v109\n"
        "v_mul_f32 v110, v110, v178\n"
        "v_mul_f32 v110, v110, v251\n"
        "v_mul_f32 v111, v111, v179\n"
        "v_mul_f32 v111, v111, v251\n"
        "v_cvt_pk_bf16_f32 v191, v110, v111\n"
        "s_nop 1\n"
        "v_mfma_f32_16x16x32_bf16 v[108:111], v[188:191], v[128:131], 0\n"
        "v_mul_f32 v192, v192, v180\n"
        "v_mul_f32 v193, v193, v181\n"
        "v_mul_f32 v192, v192, v251\n"
        "v_mul_f32 v193, v193, v251\n"
        "v_cvt_pk_bf16_f32 v144, v192, v193\n"
        "v_mul_f32 v194, v194, v182\n"
        "v_mul_f32 v194, v194, v251\n"
        "v_mul_f32 v195, v195, v183\n"
        "v_mul_f32 v195, v195, v251\n"
        "v_cvt_pk_bf16_f32 v145, v194, v195\n"
        "v_mul_f32 v242, v242, v253\n"
        "v_mul_f32 v242, v242, v251\n"
        "v_cvt_pk_bf16_f32 v146, v242, 0\n"
        "s_nop 1\n"
        "v_mfma_f32_16x16x32_bf16 v[108:111], v[144:147], v[136:139], v[108:111]\n"
        "v_mfma_f32_16x16x32_bf16 v[108:111], v[226:229], v[88:91], v[108:111]\n"
        "v_mfma_f32_16x16x32_bf16 v[192:195], v[226:229], v[80:83], 0\n"
        "v_mfma_f32_16x16x32_bf16 v[192:195], v[144:147], v[40:43], v[192:195]\n"
        "v_mfma_f32_16x16x32_bf16 v[192:195], v[188:191], v[152:155], v[192:195]\n"
        "s_add_u32 s90, s89, 196224\n"
        "s_nop 0\n"
        "v_mfma_f32_16x16x32_bf16 v[184:187], v[188:191], v[160:163], 0\n"
        "s_nop 2\n"
        "v_mfma_f32_16x16x32_bf16 v[184:187], v[144:147], v[140:143], v[184:187]\n"
        "s_nop 0\n"
        "v_cvt_pk_bf16_f32 v238, v108, v192\n"
        "v_cvt_pk_bf16_f32 v214, v109, v193\n"
        "v_mfma_f32_16x16x32_bf16 v[184:187], v[234:237], v[88:91], v[184:187]\n"
        "v_cvt_pk_bf16_f32 v132, v110, v194\n"
        "v_cvt_pk_bf16_f32 v124, v111, v195\n"
        "v_mfma_f32_16x16x32_bf16 v[192:195], v[234:237], v[80:83], 0\n"
        "s_add_u32 s91, s89, 294336\n"
        "v_mfma_f32_16x16x32_bf16 v[192:195], v[144:147], v[116:119], v[192:195]\n"
        "v_mfma_f32_16x16x32_bf16 v[192:195], v[188:191], v[156:159], v[192:195]\n"
        "s_nop 1\n"
        "v_mfma_f32_16x16x32_bf16 v[108:111], v[188:191], v[164:167], 0\n"
        "s_nop 2\n"
        "v_mfma_f32_16x16x32_bf16 v[108:111], v[144:147], v[104:107], v[108:111]\n"
        "s_nop 0\n"
        "v_cvt_pk_bf16_f32 v239, v184, v192\n"
        "v_cvt_pk_bf16_f32 v215, v185, v193\n"
        "v_mfma_f32_16x16x32_bf16 v[108:111], v[230:233], v[88:91], v[108:111]\n"
        "v_cvt_pk_bf16_f32 v133, v186, v194\n"
        "v_cvt_pk_bf16_f32 v125, v187, v195\n"
        "v_mfma_f32_16x16x32_bf16 v[184:187], v[230:233], v[80:83], 0\n"
        "v_mfma_f32_16x16x32_bf16 v[184:187], v[144:147], v[34:37], v[184:187]\n"
        "v_mfma_f32_16x16x32_bf16 v[184:187], v[188:191], v[172:175], v[184:187]\n"
        "s_nop 1\n"
        "v_mfma_f32_16x16x32_bf16 v[144:147], a[144:147], v[202:205], 0\n"
        "s_nop 2\n"
        "v_mfma_f32_16x16x32_bf16 v[144:147], a[148:151], v[210:213], v[144:147]\n"
        "s_nop 0\n"
        "v_cvt_pk_bf16_f32 v240, v108, v184\n"
        "v_cvt_pk_bf16_f32 v216, v109, v185\n"
        "v_mfma_f32_16x16x32_bf16 v[144:147], a[152:155], v[198:201], v[144:147]\n"
        "s_nop 0\n"
        "v_cvt_pk_bf16_f32 v134, v110, v186\n"
        "v_cvt_pk_bf16_f32 v126, v111, v187\n"
        "v_mfma_f32_16x16x32_bf16 v[184:187], a[164:167], v[198:201], 0\n"
        "s_nop 1\n"
        "v_mfma_f32_16x16x32_bf16 v[184:187], a[160:163], v[210:213], v[184:187]\n"
        "s_nop 0\n"
        "v_cvt_pk_bf16_f32 v188, v144, v145\n"
        "v_cvt_pk_bf16_f32 v189, v146, v147\n"
        "v_mfma_f32_16x16x32_bf16 v[184:187], a[156:159], v[202:205], v[184:187]\n"
        "s_nop 0\n"
        "v_mfma_f32_16x16x32_bf16 v[108:111], a[168:171], v[202:205], 0\n"
        "buffer_store_dwordx3 v[238:240], v14, s[40:43], s90 offen offset:384 sc1 nt\n"
        "s_nop 2\n"
        "v_mfma_f32_16x16x32_bf16 v[108:111], a[172:175], v[210:213], v[108:111]\n"
        "s_nop 0\n"
        "v_cvt_pk_bf16_f32 v190, v184, v185\n"
        "v_cvt_pk_bf16_f32 v191, v186, v187\n"
        "v_mfma_f32_16x16x32_bf16 v[108:111], a[176:179], v[198:201], v[108:111]\n"
        "buffer_store_dwordx3 v[214:216], v17, s[40:43], s90 offen offset:1152 sc1 nt\n"
        "s_add_u32 s90, s89, 392448\n"
        "v_mfma_f32_16x16x32_bf16 v[184:187], a[188:191], v[198:201], 0\n"
        "s_nop 2\n"
        "v_mfma_f32_16x16x32_bf16 v[184:187], a[184:187], v[210:213], v[184:187]\n"
        "buffer_store_dwordx3 v[132:134], v14, s[40:43], s91 offen offset:576 sc1 nt\n"
        "v_cvt_pk_bf16_f32 v144, v108, v109\n"
        "v_cvt_pk_bf16_f32 v145, v110, v111\n"
        "v_mfma_f32_16x16x32_bf16 v[184:187], a[180:183], v[202:205], v[184:187]\n"
        "s_nop 0\n"
        "v_mfma_f32_16x16x32_bf16 v[108:111], a[192:195], v[202:205], 0\n"
        "buffer_store_dwordx3 v[124:126], v14, s[40:43], s90 offen sc1 nt\n"
        "s_nop 2\n"
        "v_mfma_f32_16x16x32_bf16 v[108:111], a[196:199], v[210:213], v[108:111]\n"
        "s_nop 0\n"
        "v_cvt_pk_bf16_f32 v146, v184, v185\n"
        "v_cvt_pk_bf16_f32 v147, v186, v187\n"
        "v_mfma_f32_16x16x32_bf16 v[108:111], a[200:203], v[198:201], v[108:111]\n"
        "s_nop 1\n"
        "v_mfma_f32_16x16x32_bf16 v[184:187], a[212:215], v[198:201], 0\n"
        "s_nop 2\n"
        "v_mfma_f32_16x16x32_bf16 v[184:187], a[208:211], v[210:213], v[184:187]\n"
        "s_nop 0\n"
        "v_cvt_pk_bf16_f32 v192, v108, v109\n"
        "v_cvt_pk_bf16_f32 v193, v110, v111\n"
        "v_mfma_f32_16x16x32_bf16 v[184:187], a[204:207], v[202:205], v[184:187]\n"
        "s_nop 1\n"
        "v_mfma_f32_16x16x32_bf16 v[108:111], v[222:225], v[188:191], 0\n"
        "s_nop 2\n"
        "v_mfma_f32_16x16x32_bf16 v[108:111], v[206:209], v[144:147], v[108:111]\n"
        "s_nop 0\n"
        "v_cvt_pk_bf16_f32 v194, v184, v185\n"
        "v_cvt_pk_bf16_f32 v195, v186, v187\n"
        "s_nop 1\n"
        "v_mfma_f32_16x16x32_bf16 v[108:111], v[218:221], v[192:195], v[108:111]\n"
        "s_nop 1\n"
        "v_mfma_f32_16x16x32_bf16 v[214:217], v[230:233], v[192:195], 0\n"
        "s_nop 2\n"
        "v_mfma_f32_16x16x32_bf16 v[214:217], v[234:237], v[144:147], v[214:217]\n"
        "s_nop 0\n"
        "v_mul_f32 v108, v108, v168\n"
        "v_mul_f32 v109, v109, v169\n"
        "v_mfma_f32_16x16x32_bf16 v[214:217], v[226:229], v[188:191], v[214:217]\n"
        "s_nop 0\n"
        "v_mul_f32 v110, v110, v170\n"
        "v_mul_f32 v111, v111, v171\n"
        "v_mfma_f32_16x16x32_bf16 v[238:241], v[202:205], v[188:191], 0\n"
        "s_nop 0\n"
        "v_max3_f32 v251, v108, v109, v110\n"
        "v_mfma_f32_16x16x32_bf16 v[238:241], v[210:213], v[144:147], v[238:241]\n"
        "s_nop 0\n"
        "v_mul_f32 v214, v214, v176\n"
        "v_mul_f32 v215, v215, v177\n"
        "v_mfma_f32_16x16x32_bf16 v[238:241], v[198:201], v[192:195], v[238:241]\n"
        "v_mul_f32 v216, v216, v178\n"
        "v_mul_f32 v217, v217, v179\n"
        "v_mfma_f32_16x16x32_bf16 v[184:187], v[148:151], v[192:195], 0\n"
        "s_nop 0\n"
        "v_max3_f32 v251, v251, v111, v214\n"
        "v_max3_f32 v251, v251, v215, v216\n"
        "v_mfma_f32_16x16x32_bf16 v[184:187], v[112:115], v[144:147], v[184:187]\n"
        "s_nop 0\n"
        "v_mul_f32 v238, v238, v180\n"
        "v_mul_f32 v239, v239, v181\n"
        "v_mfma_f32_16x16x32_bf16 v[184:187], v[120:123], v[188:191], v[184:187]\n"
        "s_nop 0\n"
        "v_mul_f32 v240, v240, v182\n"
        "v_mul_f32 v241, v241, v183\n"
        "v_max3_f32 v251, v251, v217, v238\n"
        "v_max3_f32 v251, v251, v239, v240\n"
        "s_nop 2\n"
        "v_mul_f32 v184, v184, v253\n"
        "v_cndmask_b32_e64 v184, v255, v184, s[62:63]\n"
        "v_max3_f32 v251, v251, v241, v184\n"
        "v_mov_b32 v252, v251\n"
        "s_nop 1\n"
        "v_permlane32_swap_b32 v251, v252\n"
        "v_max_f32 v251, v251, v252\n"
        "v_mov_b32 v252, v251\n"
        "s_nop 1\n"
        "v_permlane16_swap_b32 v251, v252\n"
        "v_max_f32 v251, v251, v252\n"
        "v_mul_f32 v251, v251, v254\n"
        "v_fma_f32 v108, v108, v254, -v251\n"
        "v_exp_f32 v108, v108\n"
        "v_fma_f32 v109, v109, v254, -v251\n"
        "v_exp_f32 v109, v109\n"
        "s_nop 0\n"
        "v_add_f32 v252, v108, v109\n"
        "v_mul_f32 v108, v108, v168\n"
        "v_fma_f32 v184, v184, v254, -v251\n"
        "v_exp_f32 v184, v184\n"
        "v_fma_f32 v241, v241, v254, -v251\n"
        "v_exp_f32 v241, v241\n"
        "v_fma_f32 v240, v240, v254, -v251\n"
        "v_exp_f32 v240, v240\n"
        "v_fma_f32 v239, v239, v254, -v251\n"
        "v_exp_f32 v239, v239\n"
        "v_fma_f32 v238, v238, v254, -v251\n"
        "v_exp_f32 v238, v238\n"
        "v_fma_f32 v217, v217, v254, -v251\n"
        "v_exp_f32 v217, v217\n"
        "v_fma_f32 v216, v216, v254, -v251\n"
        "v_exp_f32 v216, v216\n"
        "v_fma_f32 v215, v215, v254, -v251\n"
        "v_exp_f32 v215, v215\n"
        "v_fma_f32 v214, v214, v254, -v251\n"
        "v_exp_f32 v214, v214\n"
        "v_fma_f32 v111, v111, v254, -v251\n"
        "v_exp_f32 v111, v111\n"
        "v_fma_f32 v110, v110, v254, -v251\n"
        "v_exp_f32 v110, v110\n"
        "s_nop 0\n"
        "v_add_f32 v252, v252, v110\n"
        "v_add_f32 v252, v252, v111\n"
        "v_add_f32 v252, v252, v214\n"
        "v_add_f32 v252, v252, v215\n"
        "v_add_f32 v252, v252, v216\n"
        "v_add_f32 v252, v252, v217\n"
        "v_add_f32 v252, v252, v238\n"
        "v_add_f32 v252, v252, v239\n"
        "v_add_f32 v252, v252, v240\n"
        "v_add_f32 v252, v252, v241\n"
        "v_add_f32 v252, v252, v184\n"
        "v_mov_b32 v254, v252\n"
        "s_nop 1\n"
        "v_permlane32_swap_b32 v252, v254\n"
        "v_add_f32 v252, v252, v254\n"
        "v_mov_b32 v254, v252\n"
        "s_nop 1\n"
        "v_permlane16_swap_b32 v252, v254\n"
        "v_add_f32 v252, v252, v254\n"
        "v_rcp_f32 v252, v252\n"
        "s_nop 0\n"
        "v_mul_f32 v108, v108, v252\n"
        "v_mul_f32 v109, v109, v169\n"
        "v_mul_f32 v109, v109, v252\n"
        "v_cvt_pk_bf16_f32 v188, v108, v109\n"
        "v_mul_f32 v110, v110, v170\n"
        "v_mul_f32 v110, v110, v252\n"
        "v_mul_f32 v111, v111, v171\n"
        "v_mul_f32 v111, v111, v252\n"
        "v_cvt_pk_bf16_f32 v189, v110, v111\n"
        "v_mul_f32 v214, v214, v176\n"
        "v_mul_f32 v214, v214, v252\n"
        "v_mul_f32 v215, v215, v177\n"
        "v_mul_f32 v215, v215, v252\n"
        "v_cvt_pk_bf16_f32 v190, v214, v215\n"
        "v_mul_f32 v216, v216, v178\n"
        "v_mul_f32 v216, v216, v252\n"
        "v_mul_f32 v217, v217, v179\n"
        "v_mul_f32 v217, v217, v252\n"
        "v_cvt_pk_bf16_f32 v191, v216, v217\n"
        "s_nop 1\n"
        "v_mfma_f32_16x16x32_bf16 v[108:111], v[188:191], v[128:131], 0\n"
        "v_mul_f32 v238, v238, v180\n"
        "v_mul_f32 v239, v239, v181\n"
        "v_mul_f32 v238, v238, v252\n"
        "v_mul_f32 v239, v239, v252\n"
        "v_cvt_pk_bf16_f32 v132, v238, v239\n"
        "v_mul_f32 v240, v240, v182\n"
        "v_mul_f32 v240, v240, v252\n"
        "v_mul_f32 v241, v241, v183\n"
        "v_mul_f32 v241, v241, v252\n"
        "v_cvt_pk_bf16_f32 v133, v240, v241\n"
        "v_mul_f32 v184, v184, v253\n"
        "v_mul_f32 v184, v184, v252\n"
        "v_cvt_pk_bf16_f32 v134, v184, 0\n"
        "s_nop 1\n"
        "v_mfma_f32_16x16x32_bf16 v[108:111], v[132:135], v[136:139], v[108:111]\n"
        "v_mfma_f32_16x16x32_bf16 v[108:111], v[202:205], v[88:91], v[108:111]\n"
        "v_mfma_f32_16x16x32_bf16 v[184:187], v[202:205], v[80:83], 0\n"
        "v_mfma_f32_16x16x32_bf16 v[184:187], v[132:135], v[40:43], v[184:187]\n"
        "v_mfma_f32_16x16x32_bf16 v[184:187], v[188:191], v[152:155], v[184:187]\n"
        "s_nop 1\n"
        "v_mfma_f32_16x16x32_bf16 v[144:147], v[188:191], v[160:163], 0\n"
        "s_nop 2\n"
        "v_mfma_f32_16x16x32_bf16 v[144:147], v[132:135], v[140:143], v[144:147]\n"
        "s_add_u32 s91, s89, 490560\n"
        "v_cvt_pk_bf16_f32 v238, v108, v184\n"
        "v_cvt_pk_bf16_f32 v124, v109, v185\n"
        "v_mfma_f32_16x16x32_bf16 v[144:147], v[210:213], v[88:91], v[144:147]\n"
        "v_cvt_pk_bf16_f32 v192, v110, v186\n"
        "v_cvt_pk_bf16_f32 v214, v111, v187\n"
        "v_mfma_f32_16x16x32_bf16 v[108:111], v[210:213], v[80:83], 0\n"
        "v_mfma_f32_16x16x32_bf16 v[108:111], v[132:135], v[116:119], v[108:111]\n"
        "v_mfma_f32_16x16x32_bf16 v[108:111], v[188:191], v[156:159], v[108:111]\n"
        "s_nop 1\n"
        "v_mfma_f32_16x16x32_bf16 v[184:187], v[188:191], v[164:167], 0\n"
        "s_nop 2\n"
        "v_mfma_f32_16x16x32_bf16 v[184:187], v[132:135], v[104:107], v[184:187]\n"
        "s_nop 0\n"
        "v_cvt_pk_bf16_f32 v239, v144, v108\n"
        "v_cvt_pk_bf16_f32 v125, v145, v109\n"
        "v_mfma_f32_16x16x32_bf16 v[184:187], v[198:201], v[88:91], v[184:187]\n"
        "v_cvt_pk_bf16_f32 v193, v146, v110\n"
        "v_cvt_pk_bf16_f32 v215, v147, v111\n"
        "v_mfma_f32_16x16x32_bf16 v[108:111], v[198:201], v[80:83], 0\n"
        "v_mfma_f32_16x16x32_bf16 v[108:111], v[132:135], v[34:37], v[108:111]\n"
        "v_mfma_f32_16x16x32_bf16 v[108:111], v[188:191], v[172:175], v[108:111]\n"
        "s_nop 1\n"
        "v_mfma_f32_16x16x32_bf16 v[144:147], a[144:147], v[120:123], 0\n"
        "s_nop 2\n"
        "v_mfma_f32_16x16x32_bf16 v[144:147], a[148:151], v[112:115], v[144:147]\n"
        "s_nop 0\n"
        "v_cvt_pk_bf16_f32 v240, v184, v108\n"
        "v_cvt_pk_bf16_f32 v126, v185, v109\n"
        "v_mfma_f32_16x16x32_bf16 v[144:147], a[152:155], v[148:151], v[144:147]\n"
        "s_nop 0\n"
        "v_cvt_pk_bf16_f32 v194, v186, v110\n"
        "v_cvt_pk_bf16_f32 v216, v187, v111\n"
        "v_mfma_f32_16x16x32_bf16 v[132:135], a[164:167], v[148:151], 0\n"
        "s_nop 1\n"
        "v_mfma_f32_16x16x32_bf16 v[132:135], a[160:163], v[112:115], v[132:135]\n"
        "s_nop 0\n"
        "v_cvt_pk_bf16_f32 v108, v144, v145\n"
        "v_cvt_pk_bf16_f32 v109, v146, v147\n"
        "v_mfma_f32_16x16x32_bf16 v[132:135], a[156:159], v[120:123], v[132:135]\n"
        "s_nop 0\n"
        "v_mfma_f32_16x16x32_bf16 v[184:187], a[168:171], v[120:123], 0\n"
        "buffer_store_dwordx3 v[238:240], v15, s[40:43], s90 offen offset:768 sc1 nt\n"
        "s_add_u32 s90, s89, 588672\n"
        "s_nop 1\n"
        "v_mfma_f32_16x16x32_bf16 v[184:187], a[172:175], v[112:115], v[184:187]\n"
        "s_nop 0\n"
        "v_cvt_pk_bf16_f32 v110, v132, v133\n"
        "v_cvt_pk_bf16_f32 v111, v134, v135\n"
        "v_mfma_f32_16x16x32_bf16 v[184:187], a[176:179], v[148:151], v[184:187]\n"
        "buffer_store_dwordx3 v[124:126], v14, s[40:43], s91 offen offset:192 sc1 nt\n"
        "s_nop 0\n"
        "v_mfma_f32_16x16x32_bf16 v[132:135], a[188:191], v[148:151], 0\n"
        "s_nop 2\n"
        "v_mfma_f32_16x16x32_bf16 v[132:135], a[184:187], v[112:115], v[132:135]\n"
        "buffer_store_dwordx3 v[192:194], v16, s[40:43], s91 offen offset:960 sc1 nt\n"
        "v_cvt_pk_bf16_f32 v144, v184, v185\n"
        "v_cvt_pk_bf16_f32 v145, v186, v187\n"
        "v_mfma_f32_16x16x32_bf16 v[132:135], a[180:183], v[120:123], v[132:135]\n"
        "s_nop 0\n"
        "v_mfma_f32_16x16x32_bf16 v[184:187], a[192:195], v[120:123], 0\n"
        "buffer_store_dwordx3 v[214:216], v14, s[40:43], s90 offen offset:384 sc1 nt\n"
        "s_nop 2\n"
        "v_mfma_f32_16x16x32_bf16 v[184:187], a[196:199], v[112:115], v[184:187]\n"
        "s_nop 0\n"
        "v_cvt_pk_bf16_f32 v146, v132, v133\n"
        "v_cvt_pk_bf16_f32 v147, v134, v135\n"
        "v_mfma_f32_16x16x32_bf16 v[184:187], a[200:203], v[148:151], v[184:187]\n"
        "s_nop 1\n"
        "v_mfma_f32_16x16x32_bf16 v[132:135], a[212:215], v[148:151], 0\n"
        "s_nop 2\n"
        "v_mfma_f32_16x16x32_bf16 v[132:135], a[208:211], v[112:115], v[132:135]\n"
        "s_nop 0\n"
        "v_cvt_pk_bf16_f32 v214, v184, v185\n"
        "v_cvt_pk_bf16_f32 v215, v186, v187\n"
        "v_mfma_f32_16x16x32_bf16 v[132:135], a[204:207], v[120:123], v[132:135]\n"
        "s_nop 1\n"
        "v_mfma_f32_16x16x32_bf16 v[184:187], v[222:225], v[108:111], 0\n"
        "s_nop 2\n"
        "v_mfma_f32_16x16x32_bf16 v[184:187], v[206:209], v[144:147], v[184:187]\n"
        "s_nop 0\n"
        "v_cvt_pk_bf16_f32 v216, v132, v133\n"
        "v_cvt_pk_bf16_f32 v217, v134, v135\n"
        "s_nop 1\n"
        "v_mfma_f32_16x16x32_bf16 v[184:187], v[218:221], v[214:217], v[184:187]\n"
        "s_nop 1\n"
        "v_mfma_f32_16x16x32_bf16 v[192:195], v[230:233], v[214:217], 0\n"
        "s_nop 2\n"
        "v_mfma_f32_16x16x32_bf16 v[192:195], v[234:237], v[144:147], v[192:195]\n"
        "s_nop 0\n"
        "v_mul_f32 v184, v184, v168\n"
        "v_mul_f32 v185, v185, v169\n"
        "v_mfma_f32_16x16x32_bf16 v[192:195], v[226:229], v[108:111], v[192:195]\n"
        "s_nop 0\n"
        "v_mul_f32 v186, v186, v170\n"
        "v_mul_f32 v187, v187, v171\n"
        "v_mfma_f32_16x16x32_bf16 v[188:191], v[202:205], v[108:111], 0\n"
        "s_nop 0\n"
        "v_max3_f32 v254, v184, v185, v186\n"
        "v_mfma_f32_16x16x32_bf16 v[188:191], v[210:213], v[144:147], v[188:191]\n"
        "s_nop 0\n"
        "v_mul_f32 v192, v192, v176\n"
        "v_mul_f32 v193, v193, v177\n"
        "v_mfma_f32_16x16x32_bf16 v[188:191], v[198:201], v[214:217], v[188:191]\n"
        "v_mul_f32 v194, v194, v178\n"
        "v_mul_f32 v195, v195, v179\n"
        "v_mfma_f32_16x16x32_bf16 v[132:135], v[148:151], v[214:217], 0\n"
        "s_nop 0\n"
        "v_max3_f32 v254, v254, v187, v192\n"
        "v_max3_f32 v254, v254, v193, v194\n"
        "v_mfma_f32_16x16x32_bf16 v[132:135], v[112:115], v[144:147], v[132:135]\n"
        "s_nop 0\n"
        "v_mul_f32 v188, v188, v180\n"
        "v_mul_f32 v189, v189, v181\n"
        "v_mfma_f32_16x16x32_bf16 v[132:135], v[120:123], v[108:111], v[132:135]\n"
        "s_nop 0\n"
        "v_mul_f32 v190, v190, v182\n"
        "v_mul_f32 v191, v191, v183\n"
        "v_max3_f32 v254, v254, v195, v188\n"
        "v_max3_f32 v254, v254, v189, v190\n"
        "s_nop 2\n"
        "v_mul_f32 v132, v132, v253\n"
        "v_cndmask_b32_e64 v132, v255, v132, s[62:63]\n"
        "v_max3_f32 v254, v254, v191, v132\n"
        "v_mov_b32 v255, v254\n"
        "s_nop 1\n"
        "v_permlane32_swap_b32 v254, v255\n"
        "v_max_f32 v254, v254, v255\n"
        "v_mov_b32 v255, v254\n"
        "s_nop 1\n"
        "v_permlane16_swap_b32 v254, v255\n"
        "v_max_f32 v254, v254, v255\n"
        "v_mul_f32 v254, v254, v253\n"
        "v_fma_f32 v184, v184, v253, -v254\n"
        "v_exp_f32 v184, v184\n"
        "v_fma_f32 v185, v185, v253, -v254\n"
        "v_exp_f32 v185, v185\n"
        "s_nop 0\n"
        "v_add_f32 v255, v184, v185\n"
        "v_mul_f32 v184, v184, v168\n"
        "v_fma_f32 v132, v132, v253, -v254\n"
        "v_exp_f32 v132, v132\n"
        "v_fma_f32 v191, v191, v253, -v254\n"
        "v_exp_f32 v191, v191\n"
        "v_fma_f32 v190, v190, v253, -v254\n"
        "v_exp_f32 v190, v190\n"
        "v_fma_f32 v189, v189, v253, -v254\n"
        "v_exp_f32 v189, v189\n"
        "v_fma_f32 v188, v188, v253, -v254\n"
        "v_exp_f32 v188, v188\n"
        "v_fma_f32 v195, v195, v253, -v254\n"
        "v_exp_f32 v195, v195\n"
        "v_fma_f32 v194, v194, v253, -v254\n"
        "v_exp_f32 v194, v194\n"
        "v_fma_f32 v193, v193, v253, -v254\n"
        "v_exp_f32 v193, v193\n"
        "v_fma_f32 v192, v192, v253, -v254\n"
        "v_exp_f32 v192, v192\n"
        "v_fma_f32 v187, v187, v253, -v254\n"
        "v_exp_f32 v187, v187\n"
        "v_fma_f32 v186, v186, v253, -v254\n"
        "v_exp_f32 v186, v186\n"
        "s_nop 0\n"
        "v_add_f32 v255, v255, v186\n"
        "v_add_f32 v255, v255, v187\n"
        "v_add_f32 v255, v255, v192\n"
        "v_add_f32 v255, v255, v193\n"
        "v_add_f32 v255, v255, v194\n"
        "v_add_f32 v255, v255, v195\n"
        "v_add_f32 v255, v255, v188\n"
        "v_add_f32 v255, v255, v189\n"
        "v_add_f32 v255, v255, v190\n"
        "v_add_f32 v255, v255, v191\n"
        "v_add_f32 v255, v255, v132\n"
        "v_mov_b32 v254, v255\n"
        "s_nop 1\n"
        "v_permlane32_swap_b32 v255, v254\n"
        "v_add_f32 v255, v255, v254\n"
        "v_mov_b32 v254, v255\n"
        "s_nop 1\n"
        "v_permlane16_swap_b32 v255, v254\n"
        "v_add_f32 v255, v255, v254\n"
        "v_rcp_f32 v255, v255\n"
        "s_nop 0\n"
        "v_mul_f32 v184, v184, v255\n"
        "v_mul_f32 v185, v185, v169\n"
        "v_mul_f32 v185, v185, v255\n"
        "v_cvt_pk_bf16_f32 v144, v184, v185\n"
        "v_mul_f32 v186, v186, v170\n"
        "v_mul_f32 v186, v186, v255\n"
        "v_mul_f32 v187, v187, v171\n"
        "v_mul_f32 v187, v187, v255\n"
        "v_cvt_pk_bf16_f32 v145, v186, v187\n"
        "v_mul_f32 v192, v192, v176\n"
        "v_mul_f32 v192, v192, v255\n"
        "v_mul_f32 v193, v193, v177\n"
        "v_mul_f32 v193, v193, v255\n"
        "v_cvt_pk_bf16_f32 v146, v192, v193\n"
        "v_mul_f32 v194, v194, v178\n"
        "v_mul_f32 v194, v194, v255\n"
        "v_mul_f32 v195, v195, v179\n"
        "v_mul_f32 v195, v195, v255\n"
        "v_cvt_pk_bf16_f32 v147, v194, v195\n"
        "s_nop 1\n"
        "v_mfma_f32_16x16x32_bf16 v[108:111], v[144:147], v[128:131], 0\n"
        "v_mul_f32 v188, v188, v180\n"
        "v_mul_f32 v189, v189, v181\n"
        "v_mul_f32 v188, v188, v255\n"
        "v_mul_f32 v189, v189, v255\n"
        "v_cvt_pk_bf16_f32 v124, v188, v189\n"
        "v_mul_f32 v190, v190, v182\n"
        "v_mul_f32 v190, v190, v255\n"
        "v_mul_f32 v191, v191, v183\n"
        "v_mul_f32 v191, v191, v255\n"
        "v_cvt_pk_bf16_f32 v125, v190, v191\n"
        "v_mul_f32 v132, v132, v253\n"
        "v_mul_f32 v132, v132, v255\n"
        "v_cvt_pk_bf16_f32 v126, v132, 0\n"
        "s_nop 1\n"
        "v_mfma_f32_16x16x32_bf16 v[108:111], v[124:127], v[136:139], v[108:111]\n"
        "v_mfma_f32_16x16x32_bf16 v[108:111], v[120:123], v[88:91], v[108:111]\n"
        "v_mfma_f32_16x16x32_bf16 v[128:131], v[120:123], v[80:83], 0\n"
        "v_mfma_f32_16x16x32_bf16 v[128:131], v[124:127], v[40:43], v[128:131]\n"
        "v_mfma_f32_16x16x32_bf16 v[128:131], v[144:147], v[152:155], v[128:131]\n"
        "s_nop 1\n"
        "v_mfma_f32_16x16x32_bf16 v[120:123], v[144:147], v[160:163], 0\n"
        "s_nop 2\n"
        "v_mfma_f32_16x16x32_bf16 v[120:123], v[124:127], v[140:143], v[120:123]\n"
        "s_nop 0\n"
        "v_cvt_pk_bf16_f32 v130, v108, v128\n"
        "v_mfma_f32_16x16x32_bf16 v[120:123], v[112:115], v[88:91], v[120:123]\n"
        "v_mfma_f32_16x16x32_bf16 v[108:111], v[112:115], v[80:83], 0\n"
        "v_mfma_f32_16x16x32_bf16 v[108:111], v[124:127], v[116:119], v[108:111]\n"
        "v_mfma_f32_16x16x32_bf16 v[108:111], v[144:147], v[156:159], v[108:111]\n"
        "s_nop 1\n"
        "v_mfma_f32_16x16x32_bf16 v[40:43], v[144:147], v[164:167], 0\n"
        "s_nop 2\n"
        "v_mfma_f32_16x16x32_bf16 v[40:43], v[124:127], v[104:107], v[40:43]\n"
        "s_nop 0\n"
        "v_cvt_pk_bf16_f32 v131, v120, v108\n"
        "v_mfma_f32_16x16x32_bf16 v[40:43], v[148:151], v[88:91], v[40:43]\n"
        "v_mfma_f32_16x16x32_bf16 v[104:107], v[148:151], v[80:83], 0\n"
        "v_mfma_f32_16x16x32_bf16 v[104:107], v[124:127], v[34:37], v[104:107]\n"
        "s_add_u32 s46, s89, 590016\n"
        "v_mfma_f32_16x16x32_bf16 v[104:107], v[144:147], v[172:175], v[104:107]\n"
        "s_nop 2\n"
        "s_min_u32 s46, s46, s42\n"
        "s_nop 3\n"
        "v_cvt_pk_bf16_f32 v132, v40, v104\n"
        "buffer_store_dwordx3 v[130:132], v17, s[44:47], s90 offen offset:1152 sc1 nt\n"
        "s_nop 15\n"
        "s_cmp_lt_i32 s85, -1\n"
        "s_cbranch_scc1 FIN_%=\n"
        "s_mov_b32 s89, 0x80000000\n"
        "s_waitcnt vmcnt(0)\n"
        "ds_read_b128 v[146:149], v9\n"
        "ds_read_b128 v[152:155], v10\n"
        "ds_read_b128 v[158:161], v11\n"
        "ds_read_b128 v[126:129], v9 offset:3072\n"
        "ds_read_b128 v[132:135], v10 offset:3072\n"
        "ds_read_b128 v[138:141], v11 offset:3072\n"
        "ds_read_b128 v[106:109], v9 offset:6144\n"
        "ds_read_b128 v[112:115], v10 offset:6144\n"
        "ds_read_b128 v[118:121], v11 offset:6144\n"
        "s_waitcnt lgkmcnt(0)\n"
        "s_nop 7\n"
        "s_cmp_ge_i32 s85, 0\n"
        "s_cbranch_scc1 TOP_%=\n"
        "s_branch LAST_%=\n"
        "ZZ_%=:\n"
        "s_bitcmp1_b32 s15, 0\n"
        "s_cbranch_scc1 ZY_%=\n"
        "s_sleep 2\n"
        "ZY_%=:\n"
        "s_bitcmp0_b32 s15, 1\n"
        "s_cbranch_scc1 ZB_%=\n"
        "s_sleep 4\n"
        "s_branch ZB_%=\n"
        "SP4_%=:\n"
        "s_sleep 1\n"
        "ds_read_u16 v252, v19 offset:3072\n"
        "s_waitcnt lgkmcnt(0)\n"
        "v_cmp_ne_u32_e64 s[24:25], s30, v252\n"
        "s_nop 4\n"
        "s_cmp_eq_u64 s[24:25], 0\n"
        "s_cbranch_scc1 BK4_%=\n"
        "s_sub_u32 s31, s31, 1\n"
        "s_cbranch_scc0 SP4_%=\n"
        "s_endpgm\n"
        "SP8_%=:\n"
        "s_sleep 1\n"
        "ds_read_u16 v242, v19 offset:3584\n"
        "s_waitcnt lgkmcnt(0)\n"
        "v_cmp_ne_u32_e64 s[24:25], s29, v242\n"
        "s_nop 4\n"
        "s_cmp_eq_u64 s[24:25], 0\n"
        "s_cbranch_scc1 BK8_%=\n"
        "s_sub_u32 s31, s31, 1\n"
        "s_cbranch_scc0 SP8_%=\n"
        "s_endpgm\n"
        "SP18_%=:\n"
        "s_sleep 1\n"
        "ds_read_u16 v255, v19 offset:3072\n"
        "s_waitcnt lgkmcnt(0)\n"
        "v_cmp_ne_u32_e64 s[24:25], s30, v255\n"
        "s_nop 4\n"
        "s_cmp_eq_u64 s[24:25], 0\n"
        "s_cbranch_scc1 BK18_%=\n"
        "s_sub_u32 s31, s31, 1\n"
        "s_cbranch_scc0 SP18_%=\n"
        "s_endpgm\n"
        :: "{v[0:15]}"(c0), "{v[16:23]}"(c1), "{v[24:27]}"(c2), "{v[226:233]}"(sp), "{v[30:45]}"(f0), "{v[46:49]}"(f1), "{s[16:19]}"(srdl), "{s[20:23]}"(sol), "{s[36:39]}"(srdx), "{s[40:43]}"(srdo), "{s[48:55]}"(si)
        : "v28", "v29", "v50", "v51", "v52", "v53", "v54", "v55", "v56", "v57", "v58", "v59", "v60", "v61", "v62", "v63", "v64", "v65", "v66", "v67", "v68", "v69", "v70", "v71", "v72", "v73", "v74", "v75", "v76", "v77", "v78", "v79", "v80", "v81", "v82", "v83", "v84", "v85", "v86", "v87", "v88", "v89", "v90", "v91", "v92", "v93", "v94", "v95", "v96", "v97", "v98", "v99", "v100", "v101", "v102", "v103", "v104", "v105", "v106", "v107", "v108", "v109", "v110", "v111", "v112", "v113", "v114", "v115", "v116", "v117", "v118", "v119", "v120", "v121", "v122", "v123", "v124", "v125", "v126", "v127", "v128", "v129", "v130", "v131", "v132", "v133", "v134", "v135", "v136", "v137", "v138", "v139", "v140", "v141", "v142", "v143", "v144", "v145", "v146", "v147", "v148", "v149", "v150", "v151", "v152", "v153", "v154", "v155", "v156", "v157", "v158", "v159", "v160", "v161", "v162", "v163", "v164", "v165", "v166", "v167", "v168", "v169", "v170", "v171", "v172", "v173", "v174", "v175", "v176", "v177", "v178", "v179", "v180", "v181", "v182", "v183", "v184", "v185", "v186", "v187", "v188", "v189", "v190", "v191", "v192", "v193", "v194", "v195", "v196", "v197", "v198", "v199", "v200", "v201", "v202", "v203", "v204", "v205", "v206", "v207", "v208", "v209", "v210", "v211", "v212", "v213", "v214", "v215", "v216", "v217", "v218", "v219", "v220", "v221", "v222", "v223", "v224", "v225", "v234", "v235", "v236", "v237", "v238", "v239", "v240", "v241", "v242", "v243", "v244", "v245", "v246", "v247", "v248", "v249", "v250", "v251", "v252", "v253", "v254", "v255", "a0", "a1", "a2", "a3", "a4", "a5", "a6", "a7", "a8", "a9", "a10", "a11", "a12", "a13", "a14", "a15", "a16", "a17", "a18", "a19", "a20", "a21", "a22", "a23", "a24", "a25", "a26", "a27", "a28", "a29", "a30", "a31", "a32", "a33", "a34", "a35", "a36", "a37", "a38", "a39", "a40", "a41", "a42", "a43", "a44", "a45", "a46", "a47", "a48", "a49", "a50", "a51", "a52", "a53", "a54", "a55", "a56", "a57", "a58", "a59", "a60", "a61", "a62", "a63", "a64", "a65", "a66", "a67", "a68", "a69", "a70", "a71", "a72", "a73", "a74", "a75", "a76", "a77", "a78", "a79", "a80", "a81", "a82", "a83", "a84", "a85", "a86", "a87", "a88", "a89", "a90", "a91", "a92", "a93", "a94", "a95", "a96", "a97", "a98", "a99", "a100", "a101", "a102", "a103", "a104", "a105", "a106", "a107", "a108", "a109", "a110", "a111", "a112", "a113", "a114", "a115", "a116", "a117", "a118", "a119", "a120", "a121", "a122", "a123", "a124", "a125", "a126", "a127", "a128", "a129", "a130", "a131", "a132", "a133", "a134", "a135", "a136", "a137", "a138", "a139", "a140", "a141", "a142", "a143", "a144", "a145", "a146", "a147", "a148", "a149", "a150", "a151", "a152", "a153", "a154", "a155", "a156", "a157", "a158", "a159", "a160", "a161", "a162", "a163", "a164", "a165", "a166", "a167", "a168", "a169", "a170", "a171", "a172", "a173", "a174", "a175", "a176", "a177", "a178", "a179", "a180", "a181", "a182", "a183", "a184", "a185", "a186", "a187", "a188", "a189", "a190", "a191", "a192", "a193", "a194", "a195", "a196", "a197", "a198", "a199", "a200", "a201", "a202", "a203", "a204", "a205", "a206", "a207", "a208", "a209", "a210", "a211", "a212", "a213", "a214", "a215", "a216", "a217", "a218", "a219", "a220", "a221", "a222", "a223", "a224", "a225", "a226", "a227", "a228", "a229", "a230", "a231", "a232", "a233", "a234", "a235", "a236", "a237", "a238", "a239", "a240", "a241", "a242", "a243", "a244", "a245", "a246", "a247", "a248", "a249", "a250", "a251", "a252", "a253", "a254", "a255", "s15", "s24", "s25", "s26", "s27", "s28", "s29", "s30", "s31", "s44", "s45", "s46", "s47", "s56", "s57", "s58", "s59", "s60", "s61", "s62", "s63", "s64", "s65", "s66", "s67", "s68", "s69", "s70", "s71", "s72", "s73", "s74", "s75", "s76", "s77", "s78", "s79", "s80", "s81", "s82", "s83", "s84", "s85", "s86", "s87", "s88", "s89", "s90", "s91", "s92", "s93", "s94", "s95", "vcc", "scc", "memory");
}

extern "C" void kernel_launch(void* const* d_in, const int* in_sizes, int n_in, void* d_out, int out_size, void* d_ws, size_t ws_size, hipStream_t stream) {
    (void)in_sizes; (void)n_in; (void)out_size; (void)ws_size;
    (void)hipFuncSetAttribute((const void*)swin_kernel, hipFuncAttributeMaxDynamicSharedMemorySize, LDS_BYTES);
    swin_kernel<<<GRID, 256, LDS_BYTES, stream>>>((const char*)d_in[0], (const char*)d_in[1], (const char*)d_in[2], (const char*)d_in[3], (const char*)d_in[4], (char*)d_out, (const char*)d_ws);
}
```

```cpp
#define NLC 5
#define WSTEP 32
#define CPL 8
#define SMASK 31914
#define ODDS 1
#define DYN 1
#include <hip/hip_runtime.h>
#include <hip/hip_bf16.h>

#define DI __device__ __forceinline__

using u32x2 = __attribute__((ext_vector_type(2))) unsigned;
using u32x4 = __attribute__((ext_vector_type(4))) unsigned;
using u32x8 = __attribute__((ext_vector_type(8))) unsigned;
using u32x16 = __attribute__((ext_vector_type(16))) unsigned;

constexpr int HH = 511, WW = 511, NWX = 73, NWIN = NWX * NWX, GRID = 256;
constexpr int TOKB = 192, ROWB = WW * TOKB;
constexpr unsigned FRAMEB = (unsigned)HH * ROWB, TOTALB = FRAMEB * 4;
constexpr unsigned FS = 9232, L_BUF = 0, L_STG = 4 * FS, REC = 1584, NREC = DYN ? 27 : 21, L_EXT = L_STG + 4 * 9216, L_MS = L_EXT + 2 * NREC * REC - (DYN ? 18432 : 0), L_STA = L_MS + 18432, L_PSC = L_STA + 512,
                   L_STB = L_PSC + 512, L_AUX = L_STB + 512, L_DUM = L_AUX + 1024, L_FA = L_DUM + 512, LDS_BYTES = 163840;
constexpr unsigned RAWM = 18432, L_GT = L_AUX + 768;
static_assert(L_EXT + 3 * RAWM <= L_MS && ((L_STG >> 10) & 3) == 0 && (L_STG & 1023) < 512 && L_FA + 1024 + 16 <= LDS_BYTES && (NWIN + GRID - 1) / GRID <= NREC, "LDS");

#define LP(T, a) ((__attribute__((address_space(3))) T*)(size_t)(unsigned)(a))
#define SB() __builtin_amdgcn_sched_barrier(0)
#define PIN(v) asm volatile("" : "+v"(v))
DI unsigned raw_lane(unsigned lane, unsigned m, unsigned A, unsigned B, unsigned C) {
    const unsigned n = 64 * m + lane, row = (n * 171) >> 11, pc = n - 12 * row;
    return (row >> 3) * (A & 0xffff) + ((row >> 1) & 3) * (B & 0xffff) + (row & 1) * (C & 0xffff) + (pc ^ ((row >> 2) & 3)) * 16 - 1024 * m;
}
DI u32x4 make_srd(unsigned lo, unsigned hi, unsigned bytes) { return u32x4{lo, hi & 0xffffu, bytes, 0x00020000u}; }
DI void raw_dma(unsigned v0, unsigned v1, unsigned v2, const u32x4& srd, unsigned so, unsigned dst) {
    asm volatile("s_mov_b32 m0, %5\n\ts_nop 0\n\tbuffer_load_dwordx4 %0, %3, %4 offen lds\n\tbuffer_load_dwordx4 %1, %3, %4 offen offset:1024 lds\n\tbuffer_load_dwordx4 %2, %3, %4 offen offset:2048 lds"
                 :: "v"(v0), "v"(v1), "v"(v2), "s"(srd), "s"(so), "s"(dst) : "memory");
}

__global__ __launch_bounds__(256, 1) void swin_kernel(const char* __restrict__ x_, const char* __restrict__ wqkv_t, const char* __restrict__ wo_t, const char* __restrict__ wqkv_s,
                                                           const char* __restrict__ wo_s, char* __restrict__ out_, const char* wf) {
    const unsigned k = __builtin_amdgcn_readfirstlane(threadIdx.x >> 6) & 3, lane = threadIdx.x & 63, k1 = k & 1, k2 = k >> 1;
    u32x2 kx, kl, kr; u32x4 ko;
    const unsigned long long kap = (unsigned long long)__builtin_amdgcn_kernarg_segment_ptr();
    asm volatile("s_load_dwordx2 %0, %4, %5\n\ts_load_dwordx2 %1, %4, %6\n\ts_load_dwordx2 %2, %4, 0x0\n\ts_load_dwordx4 %3, %4, 0x28"
                 : "=&s"(kr), "=&s"(kl), "=&s"(kx), "=&s"(ko) : "s"(kap), "s"(8 + 8 * k), "s"(8 + 16 * k2));
    SB();
    {
        unsigned long long pc; unsigned co = ((((blockIdx.x >> 3) * 4 + k) * CPL + lane / (64 / CPL)) & 511) * 128;
        asm volatile("s_getpc_b64 %0\n.Lswin_pc:\n\ts_mov_b32 m0, %2\n\tv_min_u32 %1, .Lswin_fin-.Lswin_pc, %1\n\tglobal_load_lds_dword %1, %0" : "=&s"(pc), "+v"(co) : "s"(L_DUM + 256) : "memory");
    }
    SB();
#define TS(i)
    const unsigned Bl = 1152 + 2304 * k2, Ql = 4608 - 3456 * k2;
    const unsigned Ar = 13824 - 12288 * k1, Br = k1 ? 384 : Bl, Cr = 576 - 384 * k1, Pr = 27648 - 24576 * k1, Qr = k1 ? 6144 : Ql, cr = 192 - 192 * k1, szr = 55296 - 36864 * k1;
    const unsigned ldr = L_STG + k * RAWM + k2 * RAWM, ldl = k1 * RAWM + k2 * (L_MS - k1 * (L_MS - L_EXT + RAWM));
    static_assert(L_STG + 2 * RAWM == L_EXT, "");
    unsigned vr[3];
#pragma unroll
    for (int m = 0; m < 3; ++m) { vr[m] = raw_lane(lane, m, Ar, Br, Cr); PIN(vr[m]); }
    unsigned vW = (((blockIdx.x >> 3) & 31) * 14 + lane) * 128 + (lane >= 14) * 0x40000000u; PIN(vW);
    SB();
    asm volatile("s_waitcnt lgkmcnt(0)" : "+s"(kr), "+s"(kl), "+s"(kx), "+s"(ko));
    const u32x4 srdr = make_srd(kr[0], kr[1], szr);
    asm volatile("s_mov_b32 m0, %2\n\ts_nop 0\n\tbuffer_load_dword %0, %1, 0 offen lds" :: "v"(vW), "s"(srdr), "s"(L_DUM) : "memory");
#define RDMA(n) do { SB(); raw_dma(vr[0], vr[1], vr[2], srdr, cr + (n & 1) * Pr + (n >> 1) * Qr, ldr + n * 3072); SB(); } while (0)
    RDMA(0); RDMA(1);
    TS(1);
    const u32x4 srdl = make_srd(kl[0], kl[1], 55296), srdx = make_srd(kx[0], kx[1], TOTALB), srdo = make_srd(ko[0], ko[1], TOTALB);
    const unsigned vb = (blockIdx.x & 7) * (GRID / 8) + (blockIdx.x >> 3);
    const unsigned w0 = (blockIdx.x & 7) * 640 + (blockIdx.x >> 3), nwin = 5120 + (blockIdx.x >> 3), wy0 = w0 / NWX, wx0 = w0 - wy0 * NWX, own4 = (blockIdx.x & 7) * 4;
    static_assert(WSTEP == 32, "");
    const unsigned base0 = wy0 * 7 * ROWB + wx0 * 7 * TOKB;
#define SLOT(n) do { constexpr int a_ = __builtin_popcount(SMASK & ((1 << (n)) - 1)); if ((SMASK >> (n)) & 1) { if (a_ < 4) RDMA((a_ + 2)); else LDMA((a_ - 4)); } } while (0)
    static_assert(__builtin_popcount(SMASK) == 4 + NLC, "");
    const unsigned i = lane & 15, g = lane >> 4, p = i >> 2, t = i & 3, cl = 384 * k1;
    u32x16 c0, f0; u32x8 c1, sp; u32x4 c2;
    const unsigned stg = L_STG + k * 9216, ext = L_EXT + k * 384, exv = L_EXT + NREC * REC + k * 392;
    unsigned vl[3];
#pragma unroll
    for (int m = 0; m < 3; ++m) { vl[m] = raw_lane(lane, m, 13824, Bl, 576); PIN(vl[m]); }
#define LDMA(n) do { SB(); raw_dma(vl[0], vl[1], vl[2], srdl, cl + (n & 1) * 27648 + (n >> 1) * Ql, ldl + n * 3072); SB(); } while (0)
    SLOT(0);
#pragma unroll
    for (int a = 0; a < 2; ++a) {
        const unsigned ra = (8 * p + 4 * a + t) * 192 + ((g ^ ((2 * p + a) & 3)) * 16);
        f0[a] = ldl + ra; f0[2 + a] = ldr + ra; f0[4 + a] = ldr + (8 * g + 4 * a + p) * 192 + ((t ^ ((2 * g + a) & 3)) * 16);
        PIN(f0[a]); PIN(f0[2 + a]); PIN(f0[4 + a]);
    }
    SLOT(1);
#pragma unroll
    for (int a = 0; a < 2; ++a) {
        const unsigned n = 16 * a + 4 * k + p;
        const unsigned w = (DYN && n >= 20 ? nwin - 20 * WSTEP : w0) + n * WSTEP;
        f0[6 + a] = (DYN ? w < NWIN : n < nwin) ? t * FRAMEB + w * (7 * TOKB) + ((w * 57457) >> 22) * (7 * ROWB - NWX * 7 * TOKB) + 6 * ROWB + 6 * TOKB + g * 48 : 0x80000000u; PIN(f0[6 + a]);
        if (a == 0) SLOT(2);
    }
    f0[8] = i == 0 ? L_AUX + 192 + g * 16 : L_DUM; f0[15] = 0;
    SLOT(3);
    {
        const unsigned sh = 2 - 2 * k2, ts = 3072 + 16 * k2, ml = 64 - 48 * k2, xm = 64 * (k == 1), a0 = ldl + g * 256 + (k == 1 ? t * 16 : k1 ? 0 : (i ^ ((g & k & 2) << 2)) * 16);
#pragma unroll
        for (int n = 0; n < 6; ++n) {
            const unsigned z = (32 * (n >> 1) + 4 * (n & 1) + 8 * p + t) >> sh;
            const unsigned z6 = (z * 43) >> 8, tl = z - 6 * z6;
            f0[9 + n] = a0 + (tl * ts + ((z6 * ml) ^ ((tl << 5) & xm))) * k1 + n * 3072 * (1 - k1); PIN(f0[9 + n]);
            if (n == 0) SLOT(4);
            if (n == 2) SLOT(5);
            if (n == 4) SLOT(6);
        }
    }
    SLOT(7);
#pragma unroll
    for (int m = 0; m < 3; ++m) {
        const unsigned n = 64 * m + lane, tf = (n * 171) >> 13, r = n - 48 * tf, q = (r * 43) >> 9;
        const unsigned c = tf * FRAMEB + ((r - 12 * q) ^ tf) * 16;
#pragma unroll
        for (int u = 0; u < 3; ++u) {
            const unsigned px = 16 * u + 4 * k + q;
            c0[3 * u + m] = c + px * TOKB + ((px * 37) >> 8) * (ROWB - 7 * TOKB) - 1024 * ((3 * u + m) % 4); PIN(c0[3 * u + m]);
        }
        if (m == 0) SLOT(8);
        if (m == 1) SLOT(9);
        if (m == 2) SLOT(10);
    }
#pragma unroll
    for (int S = 0; S < 3; ++S) c0[9 + S] = stg + t * 768 + p * TOKB + ((3 * g + S) ^ t) * 16;
    c0[12] = L_BUF + t * FS + g * 256 + p * 64 + k * 16;
    c0[13] = L_BUF + k * FS + lane * 16;
    const unsigned vs = g * TOKB + 12 * i, brk = ROWB - 7 * TOKB, u = (p - g) & 3;
    c0[14] = vs; c0[15] = vs + (g >= 3 ? brk : 0);
    PIN(c0);
    SLOT(11);
    c1[0] = vs + (g >= 2 ? brk : 0); c1[1] = vs + (g >= 1 ? brk : 0);
    c1[2] = u < 3 ? L_STA + t * 128 + g * 32 + u * 8 + k * 2 : L_DUM;
    c1[3] = L_STB + t * 128 + (4 * g + k) * 8 + u * 2;
    c1[4] = L_STA + k * 128 + g * 32;
    c1[5] = L_STB + k * 128 + i * 8;
    c1[6] = L_STA + k * 128 + p * 32 + g * 8 + t * 2;
    c1[7] = ext - REC + (i & 1) * 192 + g * 16;
    c2[0] = exv - REC + 24 * i;
    c2[1] = p == g ? ~0u : 0u;
    c2[2] = L_AUX + (t < 2 ? t : 2) * 192 + g * 16;
    c2[3] = p == g ? L_AUX + 576 + t * 16 : L_AUX + 384;
    PIN(c1); PIN(c2);
    SLOT(12);
    sp[0] = lane * 16; sp[1] = L_MS + (16 * t + ((4 * g + p) ^ ((t & 2) << 2))) * 16;
#pragma unroll
    for (int T = 0; T < 2; ++T) {
        const unsigned n = 16 * T + 4 * t + p;
        sp[2 + T] = n < NREC ? ext + n * REC + g * 16 : L_DUM + (i & 7) * 16; sp[4 + T] = n < NREC && p == g ? exv + n * REC + 384 : L_DUM;
    }
    sp[6] = exv + g * REC + 24 * i; sp[7] = g < (DYN ? 3 : 1) ? exv + ((DYN ? 24 : 20) + g) * REC + 24 * i : L_DUM + 24 * i;
    PIN(sp);
    SLOT(13);
    {
        const unsigned e = 64 * k + lane, y = e - 144, one = (y < 16 ? (y >> 2) & 1 : y >> 5) ? 0x3f800000u : 0x3f80u;
        *LP(unsigned, L_AUX + e * 4) = e < 48 ? 0x3f803f80u : e == 192 ? 0x46800000u : y < 48 && t == (y < 16 ? y >> 3 : (y >> 2) & 3) ? one : 0u;
        *LP(unsigned, L_STA + k * 128 + (lane & 6) * 16 + 24 + (lane & 1) * 4) = 0x3c003c00u;
        *LP(unsigned, L_FA + e * 4) = 0u;
    }
    SLOT(14);
    const u32x4 f1 = {vl[0], vl[1], vl[2], 0u}, sol = {cl + Ql, cl + Ql + 27648, cl + 2 * Ql, cl + 2 * Ql + 27648};
    const unsigned long long gl = (((unsigned long long)ko[3] << 32) | ko[2]) + (blockIdx.x >> 3) * 128;
    const u32x8 si = {(unsigned)nwin, base0, (unsigned)wx0, k * FRAMEB, stg, ldl, (unsigned)gl, (unsigned)(gl >> 32) & 0xffffu};
    asm volatile(
        "s_bitcmp1_b32 s52, 10\n"
        "s_cbranch_scc1 FV_%=\n"
        "s_mov_b32 s24, 0x3e16c740\n"
        "v_mov_b32 v50, 0x3f803f80\n"
        "v_mov_b32 v51, 0x3f803f80\n"
        "v_mov_b32 v52, 0x3f803f80\n"
        "v_mov_b32 v53, 0x3f803f80\n"
        "s_waitcnt vmcnt(15)\n"
        "ds_read_b64_tr_b16 a[72:73], v34 offset:0\n"
        "ds_read_b64_tr_b16 a[74:75], v35 offset:0\n"
        "ds_read_b64_tr_b16 a[76:77], v34 offset:6144\n"
        "ds_read_b64_tr_b16 a[78:79], v35 offset:6144\n"
        "ds_read_b64_tr_b16 a[80:81], v34 offset:12288\n"
        "ds_read_b64_tr_b16 a[82:83], v35 offset:12288\n"
        "s_add_u32 m0, s53, 15360\n"
        "s_nop 0\n"
        "buffer_load_dwordx4 v46, s[16:19], s23 offen lds\n"
        "ds_read_b64_tr_b16 a[84:85], v34 offset:8\n"
        "ds_read_b64_tr_b16 a[86:87], v35 offset:8\n"
        "ds_read_b64_tr_b16 a[88:89], v34 offset:6152\n"
        "ds_read_b64_tr_b16 a[90:91], v35 offset:6152\n"
        "ds_read_b64_tr_b16 a[92:93], v34 offset:12296\n"
        "ds_read_b64_tr_b16 a[94:95], v35 offset:12296\n"
        "buffer_load_dwordx4 v47, s[16:19], s23 offen offset:1024 lds\n"
        "ds_read_b64_tr_b16 a[96:97], v34 offset:64\n"
        "ds_read_b64_tr_b16 a[98:99], v35 offset:64\n"
        "ds_read_b64_tr_b16 a[100:101], v34 offset:6208\n"
        "ds_read_b64_tr_b16 a[102:103], v35 offset:6208\n"
        "ds_read_b64_tr_b16 a[104:105], v34 offset:12352\n"
        "ds_read_b64_tr_b16 a[106:107], v35 offset:12352\n"
        "buffer_load_dwordx4 v48, s[16:19], s23 offen offset:2048 lds\n"
        "ds_read_b64_tr_b16 a[108:109], v34 offset:72\n"
        "ds_read_b64_tr_b16 a[110:111], v35 offset:72\n"
        "ds_read_b64_tr_b16 a[112:113], v34 offset:6216\n"
        "ds_read_b64_tr_b16 a[114:115], v35 offset:6216\n"
        "ds_read_b64_tr_b16 a[116:117], v34 offset:12360\n"
        "ds_read_b64_tr_b16 a[118:119], v35 offset:12360\n"
        "ds_read_b64_tr_b16 a[120:121], v34 offset:128\n"
        "ds_read_b64_tr_b16 a[122:123], v35 offset:128\n"
        "ds_read_b64_tr_b16 a[124:125], v34 offset:6272\n"
        "ds_read_b64_tr_b16 a[126:127], v35 offset:6272\n"
        "ds_read_b64_tr_b16 a[128:129], v34 offset:12416\n"
        "ds_read_b64_tr_b16 a[130:131], v35 offset:12416\n"
        "ds_read_b64_tr_b16 a[132:133], v34 offset:136\n"
        "ds_read_b64_tr_b16 a[134:135], v35 offset:136\n"
        "ds_read_b64_tr_b16 a[136:137], v34 offset:6280\n"
        "ds_read_b64_tr_b16 a[138:139], v35 offset:6280\n"
        "ds_read_b64_tr_b16 a[140:141], v34 offset:12424\n"
        "ds_read_b64_tr_b16 a[142:143], v35 offset:12424\n"
        "s_waitcnt lgkmcnt(15)\n"
        "v_mfma_f32_16x16x32_bf16 v[46:49], a[72:75], v[50:53], 0\n"
        "s_waitcnt vmcnt(12)\n"
        "s_waitcnt vmcnt(6)\n"
        "v_mfma_f32_16x16x32_bf16 v[46:49], a[76:79], v[50:53], v[46:49]\n"
        "v_mfma_f32_16x16x32_bf16 v[46:49], a[80:83], v[50:53], v[46:49]\n"
        "s_waitcnt lgkmcnt(15)\n"
        "s_nop 0\n"
        "v_mfma_f32_16x16x32_bf16 v[58:61], a[92:95], v[50:53], 0\n"
        "s_nop 2\n"
        "v_mfma_f32_16x16x32_bf16 v[58:61], a[88:91], v[50:53], v[58:61]\n"
        "s_waitcnt lgkmcnt(15)\n"
        "v_mul_f32 v46, 0xbc2aaaab, v46\n"
        "v_mul_f32 v47, 0xbc2aaaab, v47\n"
        "v_mul_f32 v48, 0xbc2aaaab, v48\n"
        "v_mfma_f32_16x16x32_bf16 v[58:61], a[84:87], v[50:53], v[58:61]\n"
        "s_waitcnt vmcnt(0)\n"
        "v_mul_f32 v49, 0xbc2aaaab, v49\n"
        "v_cvt_pk_bf16_f32 v54, v46, v47\n"
        "v_cvt_pk_bf16_f32 v55, v48, v49\n"
        "v_mfma_f32_16x16x32_bf16 v[46:49], a[96:99], v[50:53], 0\n"
        "s_waitcnt lgkmcnt(12)\n"
        "ds_read_b128 a[0:3], v32\n"
        "v_mfma_f32_16x16x32_bf16 v[46:49], a[100:103], v[50:53], v[46:49]\n"
        "s_waitcnt lgkmcnt(7)\n"
        "ds_read_b128 a[4:7], v32 offset:64\n"
        "v_mul_f32 v58, 0xbc2aaaab, v58\n"
        "v_mul_f32 v59, 0xbc2aaaab, v59\n"
        "v_mfma_f32_16x16x32_bf16 v[46:49], a[104:107], v[50:53], v[46:49]\n"
        "s_waitcnt lgkmcnt(2)\n"
        "ds_read_b128 a[8:11], v32 offset:128\n"
        "v_mul_f32 v60, 0xbc2aaaab, v60\n"
        "v_mul_f32 v61, 0xbc2aaaab, v61\n"
        "v_mfma_f32_16x16x32_bf16 v[66:69], a[116:119], v[50:53], 0\n"
        "ds_read_b128 a[144:147], v30\n"
        "v_cvt_pk_bf16_f32 v56, v58, v59\n"
        "v_cvt_pk_bf16_f32 v57, v60, v61\n"
        "v_mfma_f32_16x16x32_bf16 v[66:69], a[112:115], v[50:53], v[66:69]\n"
        "ds_read_b128 a[148:151], v30 offset:64\n"
        "v_mul_f32 v46, 0xbc2aaaab, v46\n"
        "v_mul_f32 v47, 0xbc2aaaab, v47\n"
        "v_mfma_f32_16x16x32_bf16 v[66:69], a[108:111], v[50:53], v[66:69]\n"
        "ds_read_b128 a[152:155], v30 offset:128\n"
        "v_mul_f32 v48, 0xbc2aaaab, v48\n"
        "v_mul_f32 v49, 0xbc2aaaab, v49\n"
        "v_mfma_f32_16x16x32_bf16 v[58:61], a[120:123], v[50:53], 0\n"
        "ds_read_b128 a[156:159], v31\n"
        "v_cvt_pk_bf16_f32 v62, v46, v47\n"
        "v_cvt_pk_bf16_f32 v63, v48, v49\n"
        "v_mfma_f32_16x16x32_bf16 v[58:61], a[124:127], v[50:53], v[58:61]\n"
        "ds_read_b128 a[160:163], v31 offset:64\n"
        "v_mul_f32 v66, 0xbc2aaaab, v66\n"
        "v_mul_f32 v67, 0xbc2aaaab, v67\n"
        "v_mfma_f32_16x16x32_bf16 v[58:61], a[128:131], v[50:53], v[58:61]\n"
        "ds_read_b128 a[164:167], v31 offset:128\n"
        "v_mul_f32 v68, 0xbc2aaaab, v68\n"
        "v_mul_f32 v69, 0xbc2aaaab, v69\n"
        "v_mfma_f32_16x16x32_bf16 v[70:73], a[140:143], v[50:53], 0\n"
        "ds_read_b128 a[168:171], v30 offset:6144\n"
        "v_cvt_pk_bf16_f32 v64, v66, v67\n"
        "v_cvt_pk_bf16_f32 v65, v68, v69\n"
        "v_mfma_f32_16x16x32_bf16 v[70:73], a[136:139], v[50:53], v[70:73]\n"
        "ds_read_b128 a[172:175], v30 offset:6208\n"
        "v_mul_f32 v58, 0xbc2aaaab, v58\n"
        "v_mul_f32 v59, 0xbc2aaaab, v59\n"
        "v_mfma_f32_16x16x32_bf16 v[70:73], a[132:135], v[50:53], v[70:73]\n"
        "s_waitcnt lgkmcnt(8)\n"
        "ds_read_b128 a[176:179], v30 offset:6272\n"
        "v_mul_f32 v60, 0xbc2aaaab, v60\n"
        "v_mul_f32 v61, 0xbc2aaaab, v61\n"
        "s_waitcnt lgkmcnt(6)\n"
        "v_mfma_f32_16x16x32_bf16 v[46:49], a[144:147], v[54:57], 0\n"
        "ds_read_b128 a[180:183], v31 offset:6144\n"
        "v_cvt_pk_bf16_f32 v102, v58, v59\n"
        "v_cvt_pk_bf16_f32 v103, v60, v61\n"
        "v_mfma_f32_16x16x32_bf16 v[46:49], a[148:151], v[62:65], v[46:49]\n"
        "ds_read_b128 a[184:187], v31 offset:6208\n"
        "v_mul_f32 v70, 0xbc2aaaab, v70\n"
        "v_mul_f32 v71, 0xbc2aaaab, v71\n"
        "v_cvt_pk_bf16_f32 v104, v70, v71\n"
        "v_mul_f32 v72, 0xbc2aaaab, v72\n"
        "v_mul_f32 v73, 0xbc2aaaab, v73\n"
        "v_cvt_pk_bf16_f32 v105, v72, v73\n"
        "s_nop 1\n"
        "v_mfma_f32_16x16x32_bf16 v[46:49], a[152:155], v[102:105], v[46:49]\n"
        "s_waitcnt lgkmcnt(5)\n"
        "ds_read_b128 a[188:191], v31 offset:6272\n"
        "v_mfma_f32_16x16x32_bf16 v[70:73], a[152:155], a[8:11], v[46:49]\n"
        "buffer_load_dwordx4 v[74:77], v36, s[36:39], 0 offen\n"
        "v_mfma_f32_16x16x32_bf16 v[70:73], a[148:151], a[4:7], v[70:73]\n"
        "ds_read_b128 a[192:195], v30 offset:12288\n"
        "ds_read_b128 a[196:199], v30 offset:12352\n"
        "v_mfma_f32_16x16x32_bf16 v[70:73], a[144:147], a[0:3], v[70:73]\n"
        "s_waitcnt lgkmcnt(5)\n"
        "ds_read_b128 a[200:203], v30 offset:12416\n"
        "v_mfma_f32_16x16x32_bf16 v[66:69], a[156:159], v[54:57], 0\n"
        "s_nop 0\n"
        "ds_read_b128 a[204:207], v31 offset:12288\n"
        "v_mfma_f32_16x16x32_bf16 v[66:69], a[160:163], v[62:65], v[66:69]\n"
        "s_nop 0\n"
        "ds_read_b128 a[208:211], v31 offset:12352\n"
        "v_add_f32 v255, v70, v71\n"
        "v_add_f32 v255, v255, v72\n"
        "v_mfma_f32_16x16x32_bf16 v[66:69], a[164:167], v[102:105], v[66:69]\n"
        "ds_read_b128 a[212:215], v31 offset:12416\n"
        "v_add_f32 v255, v255, v73\n"
        "v_mfma_f32_16x16x32_bf16 v[106:109], a[164:167], a[8:11], v[66:69]\n"
        "ds_read_b128 a[12:15], v33\n"
        "v_mfma_f32_16x16x32_bf16 v[106:109], a[160:163], a[4:7], v[106:109]\n"
        "ds_read_b128 a[16:19], v33 offset:64\n"
        "v_mfma_f32_16x16x32_bf16 v[106:109], a[156:159], a[0:3], v[106:109]\n"
        "ds_read_b128 a[20:23], v33 offset:128\n"
        "v_mfma_f32_16x16x32_bf16 v[90:93], a[168:171], v[54:57], 0\n"
        "s_waitcnt lgkmcnt(9)\n"
        "buffer_load_dwordx4 v[80:83], v36, s[36:39], 0 offen offset:16\n"
        "v_mfma_f32_16x16x32_bf16 v[90:93], a[172:175], v[62:65], v[90:93]\n"
        "s_mov_b32 s78, 0xf0000f\n"
        "ds_read_b128 a[24:27], v32 offset:6144\n"
        "ds_read_b128 a[28:31], v32 offset:6208\n"
        "v_add_f32 v255, v255, v106\n"
        "v_mfma_f32_16x16x32_bf16 v[90:93], a[176:179], v[102:105], v[90:93]\n"
        "s_mov_b32 s79, 0xf0000f00\n"
        "ds_read_b128 a[32:35], v32 offset:6272\n"
        "v_add_f32 v255, v255, v107\n"
        "v_add_f32 v255, v255, v108\n"
        "v_mfma_f32_16x16x32_bf16 v[110:113], a[176:179], a[8:11], v[90:93]\n"
        "s_waitcnt lgkmcnt(9)\n"
        "ds_read_b128 a[36:39], v33 offset:6144\n"
        "v_add_f32 v255, v255, v109\n"
        "v_mfma_f32_16x16x32_bf16 v[110:113], a[172:175], a[4:7], v[110:113]\n"
        "s_mov_b32 s76, 0x11111111\n"
        "ds_read_b128 a[40:43], v33 offset:6208\n"
        "v_mfma_f32_16x16x32_bf16 v[110:113], a[168:171], a[0:3], v[110:113]\n"
        "s_mov_b32 s77, 0x11111111\n"
        "ds_read_b128 a[44:47], v33 offset:6272\n"
        "v_mfma_f32_16x16x32_bf16 v[98:101], a[180:183], v[54:57], 0\n"
        "s_waitcnt lgkmcnt(9)\n"
        "ds_read_b128 a[48:51], v32 offset:12288\n"
        "v_mfma_f32_16x16x32_bf16 v[98:101], a[184:187], v[62:65], v[98:101]\n"
        "s_mov_b32 s56, 0x22222222\n"
        "ds_read_b128 a[52:55], v32 offset:12352\n"
        "v_add_f32 v255, v255, v110\n"
        "v_add_f32 v255, v255, v111\n"
        "v_mfma_f32_16x16x32_bf16 v[98:101], a[188:191], v[102:105], v[98:101]\n"
        "s_mov_b32 s57, 0x22222222\n"
        "ds_read_b128 a[56:59], v32 offset:12416\n"
        "v_add_f32 v255, v255, v112\n"
        "v_add_f32 v255, v255, v113\n"
        "v_mfma_f32_16x16x32_bf16 v[114:117], a[188:191], a[8:11], v[98:101]\n"
        "s_mov_b32 s58, 0x44444444\n"
        "buffer_load_dwordx4 v[86:89], v36, s[36:39], 0 offen offset:32\n"
        "v_mfma_f32_16x16x32_bf16 v[114:117], a[184:187], a[4:7], v[114:117]\n"
        "s_waitcnt lgkmcnt(9)\n"
        "ds_read_b128 a[60:63], v33 offset:12288\n"
        "ds_read_b128 a[64:67], v33 offset:12352\n"
        "v_mfma_f32_16x16x32_bf16 v[114:117], a[180:183], a[0:3], v[114:117]\n"
        "s_mov_b32 s59, 0x44444444\n"
        "ds_read_b128 a[68:71], v33 offset:12416\n"
        "v_mfma_f32_16x16x32_bf16 v[94:97], a[192:195], v[54:57], 0\n"
        "s_mov_b32 s60, 0x88888888\n"
        "s_nop 1\n"
        "v_mfma_f32_16x16x32_bf16 v[94:97], a[196:199], v[62:65], v[94:97]\n"
        "s_mov_b32 s61, 0x88888888\n"
        "v_add_f32 v255, v255, v114\n"
        "v_add_f32 v255, v255, v115\n"
        "v_add_f32 v255, v255, v116\n"
        "v_mfma_f32_16x16x32_bf16 v[94:97], a[200:203], v[102:105], v[94:97]\n"
        "s_mov_b32 s62, 0xffff\n"
        "v_add_f32 v255, v255, v117\n"
        "v_mfma_f32_16x16x32_bf16 v[118:121], a[200:203], a[8:11], v[94:97]\n"
        "s_mov_b32 s63, 0x0\n"
        "v_mfma_f32_16x16x32_bf16 v[118:121], a[196:199], a[4:7], v[118:121]\n"
        "s_waitcnt lgkmcnt(9)\n"
        "v_mfma_f32_16x16x32_bf16 v[118:121], a[192:195], a[0:3], v[118:121]\n"
        "s_mov_b32 s64, 0xffff0000\n"
        "buffer_load_dwordx4 v[50:53], v37, s[36:39], 0 offen\n"
        "v_mfma_f32_16x16x32_bf16 v[30:33], a[204:207], v[54:57], 0\n"
        "s_mov_b32 s65, 0x0\n"
        "s_nop 1\n"
        "v_mfma_f32_16x16x32_bf16 v[30:33], a[208:211], v[62:65], v[30:33]\n"
        "s_mov_b32 s66, 0x0\n"
        "v_add_f32 v255, v255, v118\n"
        "v_add_f32 v255, v255, v119\n"
        "v_add_f32 v255, v255, v120\n"
        "v_mfma_f32_16x16x32_bf16 v[30:33], a[212:215], v[102:105], v[30:33]\n"
        "s_mov_b32 s67, 0xffff\n"
        "v_add_f32 v255, v255, v121\n"
        "v_mfma_f32_16x16x32_bf16 v[102:105], a[212:215], a[8:11], v[30:33]\n"
        "s_waitcnt lgkmcnt(0)\n"
        "v_mfma_f32_16x16x32_bf16 v[102:105], a[208:211], a[4:7], v[102:105]\n"
        "s_mov_b32 s68, 0x0\n"
        "v_mfma_f32_16x16x32_bf16 v[102:105], a[204:207], a[0:3], v[102:105]\n"
        "s_mov_b32 s69, 0xffff0000\n"
        "s_nop 0\n"
        "v_mfma_f32_16x16x32_bf16 v[122:125], a[144:147], a[12:15], v[46:49]\n"
        "s_mov_b32 s70, 0x0\n"
        "s_nop 1\n"
        "v_mfma_f32_16x16x32_bf16 v[122:125], a[148:151], a[16:19], v[122:125]\n"
        "s_mov_b32 s71, 0xffffffff\n"
        "v_add_f32 v255, v255, v102\n"
        "v_add_f32 v255, v255, v103\n"
        "v_add_f32 v255, v255, v104\n"
        "v_mfma_f32_16x16x32_bf16 v[122:125], a[152:155], a[20:23], v[122:125]\n"
        "s_barrier\n"
        "buffer_load_dwordx4 v[56:59], v37, s[36:39], 0 offen offset:16\n"
        "v_add_f32 v255, v255, v105\n"
        "v_mov_b32 v253, v255\n"
        "v_mfma_f32_16x16x32_bf16 v[130:133], a[164:167], a[20:23], v[66:69]\n"
        "s_mov_b32 s72, 0x0\n"
        "v_permlane32_swap_b32 v255, v253\n"
        "v_add_f32 v255, v255, v253\n"
        "v_mfma_f32_16x16x32_bf16 v[130:133], a[160:163], a[16:19], v[130:133]\n"
        "s_mov_b32 s73, 0x20000\n"
        "v_mov_b32 v253, v255\n"
        "v_add_f32 v254, v122, v123\n"
        "v_add_f32 v254, v254, v124\n"
        "v_mfma_f32_16x16x32_bf16 v[130:133], a[156:159], a[12:15], v[130:133]\n"
        "s_mov_b64 vcc, s[78:79]\n"
        "v_permlane16_swap_b32 v255, v253\n"
        "v_add_f32 v255, v255, v253\n"
        "v_mfma_f32_16x16x32_bf16 v[134:137], a[168:171], a[12:15], v[90:93]\n"
        "s_mov_b32 s80, 0x3c2aaaab\n"
        "v_mul_f32 v255, 0xbac909aa, v255\n"
        "v_add_f32 v254, v254, v125\n"
        "v_fma_f32 v70, v70, s24, v255\n"
        "v_mfma_f32_16x16x32_bf16 v[134:137], a[172:175], a[16:19], v[134:137]\n"
        "s_mov_b32 s81, 0x3727c5ac\n"
        "v_fma_f32 v71, v71, s24, v255\n"
        "v_fma_f32 v72, v72, s24, v255\n"
        "v_fma_f32 v73, v73, s24, v255\n"
        "v_mfma_f32_16x16x32_bf16 v[134:137], a[176:179], a[20:23], v[134:137]\n"
        "s_mov_b32 s82, 0x43800000\n"
        "v_fma_f32 v106, v106, s24, v255\n"
        "v_fma_f32 v107, v107, s24, v255\n"
        "v_fma_f32 v108, v108, s24, v255\n"
        "v_mfma_f32_16x16x32_bf16 v[126:129], a[188:191], a[20:23], v[98:101]\n"
        "s_mov_b32 s84, 0x80000000\n"
        "v_fma_f32 v109, v109, s24, v255\n"
        "v_fma_f32 v110, v110, s24, v255\n"
        "v_fma_f32 v111, v111, s24, v255\n"
        "v_mfma_f32_16x16x32_bf16 v[126:129], a[184:187], a[16:19], v[126:129]\n"
        "s_mov_b32 s85, 19\n"
        "v_fma_f32 v112, v112, s24, v255\n"
        "v_fma_f32 v113, v113, s24, v255\n"
        "v_fma_f32 v114, v114, s24, v255\n"
        "v_mfma_f32_16x16x32_bf16 v[126:129], a[180:183], a[12:15], v[126:129]\n"
        "s_mov_b32 s86, 0x80000000\n"
        "buffer_load_dwordx4 v[62:65], v37, s[36:39], 0 offen offset:32\n"
        "v_fma_f32 v115, v115, s24, v255\n"
        "v_fma_f32 v116, v116, s24, v255\n"
        "v_mfma_f32_16x16x32_bf16 v[138:141], a[192:195], a[12:15], v[94:97]\n"
        "s_mov_b32 s89, 0x80000000\n"
        "v_fma_f32 v117, v117, s24, v255\n"
        "v_fma_f32 v118, v118, s24, v255\n"
        "v_fma_f32 v119, v119, s24, v255\n"
        "v_mfma_f32_16x16x32_bf16 v[138:141], a[196:199], a[16:19], v[138:141]\n"
        "s_mov_b32 s29, 1\n"
        "v_fma_f32 v120, v120, s24, v255\n"
        "v_fma_f32 v121, v121, s24, v255\n"
        "v_fma_f32 v102, v102, s24, v255\n"
        "v_mfma_f32_16x16x32_bf16 v[138:141], a[200:203], a[20:23], v[138:141]\n"
        "s_mov_b32 s31, 0x400000\n"
        "v_fma_f32 v103, v103, s24, v255\n"
        "v_fma_f32 v104, v104, s24, v255\n"
        "v_fma_f32 v105, v105, s24, v255\n"
        "v_mfma_f32_16x16x32_bf16 v[146:149], a[212:215], a[20:23], v[30:33]\n"
        "s_mov_b32 s87, s49\n"
        "v_cvt_pk_bf16_f32 v34, v70, v71\n"
        "v_cvt_pk_bf16_f32 v35, v72, v73\n"
        "v_cvt_pk_bf16_f32 v36, v106, v107\n"
        "v_mfma_f32_16x16x32_bf16 v[146:149], a[208:211], a[16:19], v[146:149]\n"
        "s_mov_b32 s88, s50\n"
        "v_cvt_pk_bf16_f32 v37, v108, v109\n"
        "v_cvt_pk_bf16_f32 v70, v110, v111\n"
        "v_cvt_pk_bf16_f32 v71, v112, v113\n"
        "v_mfma_f32_16x16x32_bf16 v[146:149], a[204:207], a[12:15], v[146:149]\n"
        "s_mov_b64 s[44:45], s[40:41]\n"
        "ds_write_b128 v39, v[34:37]\n"
        "v_cvt_pk_bf16_f32 v72, v114, v115\n"
        "v_mfma_f32_16x16x32_bf16 v[34:37], a[144:147], a[24:27], v[46:49]\n"
        "s_mov_b32 s46, 0\n"
        "v_cvt_pk_bf16_f32 v73, v116, v117\n"
        "v_cvt_pk_bf16_f32 v106, v118, v119\n"
        "v_cvt_pk_bf16_f32 v107, v120, v121\n"
        "v_mfma_f32_16x16x32_bf16 v[34:37], a[148:151], a[28:31], v[34:37]\n"
        "s_add_u32 m0, s52, 0\n"
        "ds_write_b128 v39, v[70:73] offset:1024\n"
        "v_cvt_pk_bf16_f32 v108, v102, v103\n"
        "v_mfma_f32_16x16x32_bf16 v[34:37], a[152:155], a[32:35], v[34:37]\n"
        "s_mov_b32 s47, s43\n"
        "buffer_load_dwordx4 v0, s[36:39], s49 offen lds\n"
        "v_cvt_pk_bf16_f32 v109, v104, v105\n"
        "v_add_f32 v254, v254, v130\n"
        "v_mfma_f32_16x16x32_bf16 v[70:73], a[164:167], a[32:35], v[66:69]\n"
        "s_waitcnt lgkmcnt(2)\n"
        "ds_write_b128 v39, v[106:109] offset:2048\n"
        "v_add_f32 v254, v254, v131\n"
        "v_mfma_f32_16x16x32_bf16 v[70:73], a[160:163], a[28:31], v[70:73]\n"
        "s_waitcnt lgkmcnt(3)\n"
        "v_add_f32 v254, v254, v132\n"
        "v_add_f32 v254, v254, v133\n"
        "v_add_f32 v254, v254, v134\n"
        "v_mfma_f32_16x16x32_bf16 v[70:73], a[156:159], a[24:27], v[70:73]\n"
        "s_waitcnt lgkmcnt(3)\n"
        "v_add_f32 v254, v254, v135\n"
        "v_add_f32 v254, v254, v136\n"
        "v_add_f32 v254, v254, v137\n"
        "v_mfma_f32_16x16x32_bf16 v[102:105], a[168:171], a[24:27], v[90:93]\n"
        "v_add_f32 v254, v254, v126\n"
        "v_add_f32 v254, v254, v127\n"
        "v_add_f32 v254, v254, v128\n"
        "v_mfma_f32_16x16x32_bf16 v[102:105], a[172:175], a[28:31], v[102:105]\n"
        "v_add_f32 v254, v254, v129\n"
        "v_add_f32 v254, v254, v138\n"
        "v_add_f32 v254, v254, v139\n"
        "v_mfma_f32_16x16x32_bf16 v[102:105], a[176:179], a[32:35], v[102:105]\n"
        "buffer_load_dwordx4 v1, s[36:39], s49 offen offset:1024 lds\n"
        "v_add_f32 v254, v254, v140\n"
        "v_add_f32 v254, v254, v141\n"
        "v_mfma_f32_16x16x32_bf16 v[142:145], a[188:191], a[32:35], v[98:101]\n"
        "v_add_f32 v254, v254, v146\n"
        "v_add_f32 v254, v254, v147\n"
        "v_add_f32 v254, v254, v148\n"
        "v_mfma_f32_16x16x32_bf16 v[142:145], a[184:187], a[28:31], v[142:145]\n"
        "v_add_f32 v254, v254, v149\n"
        "v_mov_b32 v253, v254\n"
        "v_add_f32 v255, v34, v35\n"
        "v_mfma_f32_16x16x32_bf16 v[142:145], a[180:183], a[24:27], v[142:145]\n"
        "v_permlane32_swap_b32 v254, v253\n"
        "v_add_f32 v254, v254, v253\n"
        "v_mfma_f32_16x16x32_bf16 v[150:153], a[192:195], a[24:27], v[94:97]\n"
        "v_mov_b32 v253, v254\n"
        "v_add_f32 v255, v255, v36\n"
        "v_add_f32 v255, v255, v37\n"
        "v_mfma_f32_16x16x32_bf16 v[150:153], a[196:199], a[28:31], v[150:153]\n"
        "v_permlane16_swap_b32 v254, v253\n"
        "v_add_f32 v254, v254, v253\n"
        "v_mfma_f32_16x16x32_bf16 v[150:153], a[200:203], a[32:35], v[150:153]\n"
        "buffer_load_dwordx4 v2, s[36:39], s49 offen offset:2048 lds\n"
        "v_mul_f32 v254, 0xbac909aa, v254\n"
        "v_fma_f32 v122, v122, s24, v254\n"
        "v_mfma_f32_16x16x32_bf16 v[154:157], a[212:215], a[32:35], v[30:33]\n"
        "v_fma_f32 v123, v123, s24, v254\n"
        "v_fma_f32 v124, v124, s24, v254\n"
        "v_fma_f32 v125, v125, s24, v254\n"
        "v_mfma_f32_16x16x32_bf16 v[154:157], a[208:211], a[28:31], v[154:157]\n"
        "v_fma_f32 v130, v130, s24, v254\n"
        "v_fma_f32 v131, v131, s24, v254\n"
        "v_fma_f32 v132, v132, s24, v254\n"
        "v_mfma_f32_16x16x32_bf16 v[154:157], a[204:207], a[24:27], v[154:157]\n"
        "v_fma_f32 v133, v133, s24, v254\n"
        "v_fma_f32 v134, v134, s24, v254\n"
        "v_fma_f32 v135, v135, s24, v254\n"
        "v_mfma_f32_16x16x32_bf16 v[106:109], a[144:147], a[36:39], v[46:49]\n"
        "v_fma_f32 v136, v136, s24, v254\n"
        "v_fma_f32 v137, v137, s24, v254\n"
        "v_fma_f32 v126, v126, s24, v254\n"
        "v_mfma_f32_16x16x32_bf16 v[106:109], a[148:151], a[40:43], v[106:109]\n"
        "v_fma_f32 v127, v127, s24, v254\n"
        "v_fma_f32 v128, v128, s24, v254\n"
        "v_fma_f32 v129, v129, s24, v254\n"
        "v_mfma_f32_16x16x32_bf16 v[106:109], a[152:155], a[44:47], v[106:109]\n"
        "buffer_load_dwordx4 v3, s[36:39], s49 offen offset:3072 lds\n"
        "v_fma_f32 v138, v138, s24, v254\n"
        "v_fma_f32 v139, v139, s24, v254\n"
        "v_mfma_f32_16x16x32_bf16 v[110:113], a[164:167], a[44:47], v[66:69]\n"
        "v_fma_f32 v140, v140, s24, v254\n"
        "v_fma_f32 v141, v141, s24, v254\n"
        "v_fma_f32 v146, v146, s24, v254\n"
        "v_mfma_f32_16x16x32_bf16 v[110:113], a[160:163], a[40:43], v[110:113]\n"
        "v_fma_f32 v147, v147, s24, v254\n"
        "v_fma_f32 v148, v148, s24, v254\n"
        "v_fma_f32 v149, v149, s24, v254\n"
        "v_mfma_f32_16x16x32_bf16 v[110:113], a[156:159], a[36:39], v[110:113]\n"
        "v_cvt_pk_bf16_f32 v116, v122, v123\n"
        "v_cvt_pk_bf16_f32 v117, v124, v125\n"
        "v_cvt_pk_bf16_f32 v118, v130, v131\n"
        "v_mfma_f32_16x16x32_bf16 v[122:125], a[168:171], a[36:39], v[90:93]\n"
        "v_cvt_pk_bf16_f32 v119, v132, v133\n"
        "v_cvt_pk_bf16_f32 v114, v134, v135\n"
        "v_cvt_pk_bf16_f32 v115, v136, v137\n"
        "v_mfma_f32_16x16x32_bf16 v[122:125], a[172:175], a[40:43], v[122:125]\n"
        "s_add_u32 m0, s52, 4096\n"
        "ds_write_b128 v40, v[116:119]\n"
        "v_cvt_pk_bf16_f32 v116, v126, v127\n"
        "v_mfma_f32_16x16x32_bf16 v[122:125], a[176:179], a[44:47], v[122:125]\n"
        "buffer_load_dwordx4 v4, s[36:39], s49 offen lds\n"
        "v_cvt_pk_bf16_f32 v117, v128, v129\n"
        "v_cvt_pk_bf16_f32 v126, v138, v139\n"
        "v_mfma_f32_16x16x32_bf16 v[118:121], a[188:191], a[44:47], v[98:101]\n"
        "ds_write_b128 v40, v[114:117] offset:1024\n"
        "v_cvt_pk_bf16_f32 v127, v140, v141\n"
        "v_mfma_f32_16x16x32_bf16 v[118:121], a[184:187], a[40:43], v[118:121]\n"
        "v_cvt_pk_bf16_f32 v128, v146, v147\n"
        "v_cvt_pk_bf16_f32 v129, v148, v149\n"
        "v_add_f32 v255, v255, v70\n"
        "v_mfma_f32_16x16x32_bf16 v[118:121], a[180:183], a[36:39], v[118:121]\n"
        "ds_write_b128 v40, v[126:129] offset:2048\n"
        "v_add_f32 v255, v255, v71\n"
        "v_mfma_f32_16x16x32_bf16 v[126:129], a[192:195], a[36:39], v[94:97]\n"
        "v_add_f32 v255, v255, v72\n"
        "v_add_f32 v255, v255, v73\n"
        "v_add_f32 v255, v255, v102\n"
        "v_mfma_f32_16x16x32_bf16 v[126:129], a[196:199], a[40:43], v[126:129]\n"
        "v_add_f32 v255, v255, v103\n"
        "v_add_f32 v255, v255, v104\n"
        "v_add_f32 v255, v255, v105\n"
        "v_mfma_f32_16x16x32_bf16 v[126:129], a[200:203], a[44:47], v[126:129]\n"
        "buffer_load_dwordx4 v5, s[36:39], s49 offen offset:1024 lds\n"
        "v_add_f32 v255, v255, v142\n"
        "v_add_f32 v255, v255, v143\n"
        "v_mfma_f32_16x16x32_bf16 v[114:117], a[212:215], a[44:47], v[30:33]\n"
        "v_add_f32 v255, v255, v144\n"
        "v_add_f32 v255, v255, v145\n"
        "v_add_f32 v255, v255, v150\n"
        "v_mfma_f32_16x16x32_bf16 v[114:117], a[208:211], a[40:43], v[114:117]\n"
        "v_add_f32 v255, v255, v151\n"
        "v_add_f32 v255, v255, v152\n"
        "v_add_f32 v255, v255, v153\n"
        "v_mfma_f32_16x16x32_bf16 v[114:117], a[204:207], a[36:39], v[114:117]\n"
        "v_add_f32 v255, v255, v154\n"
        "v_add_f32 v255, v255, v155\n"
        "v_add_f32 v255, v255, v156\n"
        "v_mfma_f32_16x16x32_bf16 v[134:137], a[144:147], a[48:51], v[46:49]\n"
        "v_add_f32 v255, v255, v157\n"
        "v_mov_b32 v254, v255\n"
        "v_add_f32 v54, v106, v107\n"
        "v_mfma_f32_16x16x32_bf16 v[134:137], a[148:151], a[52:55], v[134:137]\n"
        "v_permlane32_swap_b32 v255, v254\n"
        "v_add_f32 v255, v255, v254\n"
        "v_mfma_f32_16x16x32_bf16 v[134:137], a[152:155], a[56:59], v[134:137]\n"
        "buffer_load_dwordx4 v6, s[36:39], s49 offen offset:2048 lds\n"
        "v_mov_b32 v254, v255\n"
        "v_add_f32 v54, v54, v108\n"
        "v_mfma_f32_16x16x32_bf16 v[138:141], a[164:167], a[56:59], v[66:69]\n"
        "v_permlane16_swap_b32 v255, v254\n"
        "v_add_f32 v255, v255, v254\n"
        "v_mfma_f32_16x16x32_bf16 v[138:141], a[160:163], a[52:55], v[138:141]\n"
        "v_mul_f32 v255, 0xbac909aa, v255\n"
        "v_add_f32 v54, v54, v109\n"
        "v_fma_f32 v34, v34, s24, v255\n"
        "v_mfma_f32_16x16x32_bf16 v[138:141], a[156:159], a[48:51], v[138:141]\n"
        "v_fma_f32 v35, v35, s24, v255\n"
        "v_fma_f32 v36, v36, s24, v255\n"
        "v_fma_f32 v37, v37, s24, v255\n"
        "v_mfma_f32_16x16x32_bf16 v[130:133], a[168:171], a[48:51], v[90:93]\n"
        "v_fma_f32 v70, v70, s24, v255\n"
        "v_fma_f32 v71, v71, s24, v255\n"
        "v_fma_f32 v72, v72, s24, v255\n"
        "v_mfma_f32_16x16x32_bf16 v[130:133], a[172:175], a[52:55], v[130:133]\n"
        "v_fma_f32 v73, v73, s24, v255\n"
        "v_fma_f32 v102, v102, s24, v255\n"
        "v_fma_f32 v103, v103, s24, v255\n"
        "v_mfma_f32_16x16x32_bf16 v[130:133], a[176:179], a[56:59], v[130:133]\n"
        "buffer_load_dwordx4 v7, s[36:39], s49 offen offset:3072 lds\n"
        "v_fma_f32 v104, v104, s24, v255\n"
        "v_fma_f32 v105, v105, s24, v255\n"
        "v_mfma_f32_16x16x32_bf16 v[146:149], a[188:191], a[56:59], v[98:101]\n"
        "v_fma_f32 v142, v142, s24, v255\n"
        "v_fma_f32 v143, v143, s24, v255\n"
        "v_fma_f32 v144, v144, s24, v255\n"
        "v_mfma_f32_16x16x32_bf16 v[146:149], a[184:187], a[52:55], v[146:149]\n"
        "v_fma_f32 v145, v145, s24, v255\n"
        "v_fma_f32 v150, v150, s24, v255\n"
        "v_fma_f32 v151, v151, s24, v255\n"
        "v_mfma_f32_16x16x32_bf16 v[146:149], a[180:183], a[48:51], v[146:149]\n"
        "v_fma_f32 v152, v152, s24, v255\n"
        "v_fma_f32 v153, v153, s24, v255\n"
        "v_fma_f32 v154, v154, s24, v255\n"
        "v_mfma_f32_16x16x32_bf16 v[158:161], a[192:195], a[48:51], v[94:97]\n"
        "v_fma_f32 v155, v155, s24, v255\n"
        "v_fma_f32 v156, v156, s24, v255\n"
        "v_fma_f32 v157, v157, s24, v255\n"
        "v_mfma_f32_16x16x32_bf16 v[158:161], a[196:199], a[52:55], v[158:161]\n"
        "s_add_u32 m0, s52, 8192\n"
        "v_cvt_pk_bf16_f32 v172, v34, v35\n"
        "v_cvt_pk_bf16_f32 v173, v36, v37\n"
        "v_cvt_pk_bf16_f32 v174, v70, v71\n"
        "v_mfma_f32_16x16x32_bf16 v[158:161], a[200:203], a[56:59], v[158:161]\n"
        "buffer_load_dwordx4 v8, s[36:39], s49 offen lds\n"
        "v_cvt_pk_bf16_f32 v175, v72, v73\n"
        "v_cvt_pk_bf16_f32 v34, v102, v103\n"
        "v_mfma_f32_16x16x32_bf16 v[70:73], a[212:215], a[56:59], v[30:33]\n"
        "ds_write_b128 v41, v[172:175]\n"
        "v_cvt_pk_bf16_f32 v35, v104, v105\n"
        "v_mfma_f32_16x16x32_bf16 v[70:73], a[208:211], a[52:55], v[70:73]\n"
        "v_cvt_pk_bf16_f32 v36, v142, v143\n"
        "v_cvt_pk_bf16_f32 v37, v144, v145\n"
        "v_cvt_pk_bf16_f32 v102, v150, v151\n"
        "v_mfma_f32_16x16x32_bf16 v[70:73], a[204:207], a[48:51], v[70:73]\n"
        "ds_write_b128 v41, v[34:37] offset:1024\n"
        "v_cvt_pk_bf16_f32 v103, v152, v153\n"
        "v_mfma_f32_16x16x32_bf16 v[34:37], a[144:147], a[60:63], v[46:49]\n"
        "v_cvt_pk_bf16_f32 v104, v154, v155\n"
        "v_cvt_pk_bf16_f32 v105, v156, v157\n"
        "v_add_f32 v54, v54, v110\n"
        "v_mfma_f32_16x16x32_bf16 v[34:37], a[148:151], a[64:67], v[34:37]\n"
        "ds_write_b128 v41, v[102:105] offset:2048\n"
        "v_add_f32 v54, v54, v111\n"
        "v_mfma_f32_16x16x32_bf16 v[34:37], a[152:155], a[68:71], v[34:37]\n"
        "v_add_f32 v54, v54, v112\n"
        "v_add_f32 v54, v54, v113\n"
        "v_add_f32 v54, v54, v122\n"
        "v_mfma_f32_16x16x32_bf16 v[46:49], a[164:167], a[68:71], v[66:69]\n"
        "v_add_f32 v54, v54, v123\n"
        "v_add_f32 v54, v54, v124\n"
        "v_add_f32 v54, v54, v125\n"
        "v_mfma_f32_16x16x32_bf16 v[46:49], a[160:163], a[64:67], v[46:49]\n"
        "v_add_f32 v54, v54, v118\n"
        "v_add_f32 v54, v54, v119\n"
        "v_add_f32 v54, v54, v120\n"
        "v_mfma_f32_16x16x32_bf16 v[46:49], a[156:159], a[60:63], v[46:49]\n"
        "v_add_f32 v54, v54, v121\n"
        "v_add_f32 v54, v54, v126\n"
        "v_add_f32 v54, v54, v127\n"
        "v_mfma_f32_16x16x32_bf16 v[66:69], a[168:171], a[60:63], v[90:93]\n"
        "v_add_f32 v54, v54, v128\n"
        "v_add_f32 v54, v54, v129\n"
        "v_add_f32 v54, v54, v114\n"
        "v_mfma_f32_16x16x32_bf16 v[66:69], a[172:175], a[64:67], v[66:69]\n"
        "v_add_f32 v54, v54, v115\n"
        "v_add_f32 v54, v54, v116\n"
        "v_add_f32 v54, v54, v117\n"
        "v_mfma_f32_16x16x32_bf16 v[66:69], a[176:179], a[68:71], v[66:69]\n"
        "v_mov_b32 v55, v54\n"
        "v_add_f32 v60, v134, v135\n"
        "v_add_f32 v60, v60, v136\n"
        "v_mfma_f32_16x16x32_bf16 v[90:93], a[188:191], a[68:71], v[98:101]\n"
        "v_permlane32_swap_b32 v54, v55\n"
        "v_add_f32 v54, v54, v55\n"
        "v_mfma_f32_16x16x32_bf16 v[90:93], a[184:187], a[64:67], v[90:93]\n"
        "v_mov_b32 v55, v54\n"
        "v_add_f32 v60, v60, v137\n"
        "v_add_f32 v60, v60, v138\n"
        "v_mfma_f32_16x16x32_bf16 v[90:93], a[180:183], a[60:63], v[90:93]\n"
        "v_permlane16_swap_b32 v54, v55\n"
        "v_add_f32 v54, v54, v55\n"
        "v_mfma_f32_16x16x32_bf16 v[38:41], a[192:195], a[60:63], v[94:97]\n"
        "v_mul_f32 v54, 0xbac909aa, v54\n"
        "v_add_f32 v60, v60, v139\n"
        "v_add_f32 v60, v60, v140\n"
        "v_mfma_f32_16x16x32_bf16 v[38:41], a[196:199], a[64:67], v[38:41]\n"
        "v_add_f32 v60, v60, v141\n"
        "v_add_f32 v60, v60, v130\n"
        "v_add_f32 v60, v60, v131\n"
        "v_mfma_f32_16x16x32_bf16 v[38:41], a[200:203], a[68:71], v[38:41]\n"
        "v_add_f32 v60, v60, v132\n"
        "v_add_f32 v60, v60, v133\n"
        "v_add_f32 v60, v60, v146\n"
        "v_mfma_f32_16x16x32_bf16 v[94:97], a[212:215], a[68:71], v[30:33]\n"
        "v_add_f32 v60, v60, v147\n"
        "v_add_f32 v60, v60, v148\n"
        "v_add_f32 v60, v60, v149\n"
        "v_mfma_f32_16x16x32_bf16 v[94:97], a[208:211], a[64:67], v[94:97]\n"
        "v_add_f32 v60, v60, v158\n"
        "v_add_f32 v60, v60, v159\n"
        "v_add_f32 v60, v60, v160\n"
        "v_mfma_f32_16x16x32_bf16 v[94:97], a[204:207], a[60:63], v[94:97]\n"
        "v_add_f32 v60, v60, v161\n"
        "v_add_f32 v60, v60, v70\n"
        "v_add_f32 v60, v60, v71\n"
        "v_add_f32 v60, v60, v72\n"
        "v_add_f32 v60, v60, v73\n"
        "v_mov_b32 v61, v60\n"
        "v_add_f32 v78, v34, v35\n"
        "v_add_f32 v78, v78, v36\n"
        "v_permlane32_swap_b32 v60, v61\n"
        "v_add_f32 v60, v60, v61\n"
        "v_mov_b32 v61, v60\n"
        "v_add_f32 v78, v78, v37\n"
        "v_add_f32 v78, v78, v46\n"
        "v_permlane16_swap_b32 v60, v61\n"
        "v_add_f32 v60, v60, v61\n"
        "v_mul_f32 v60, 0xbac909aa, v60\n"
        "v_add_f32 v78, v78, v47\n"
        "v_add_f32 v78, v78, v48\n"
        "v_add_f32 v78, v78, v49\n"
        "v_add_f32 v78, v78, v66\n"
        "v_add_f32 v78, v78, v67\n"
        "v_add_f32 v78, v78, v68\n"
        "v_add_f32 v78, v78, v69\n"
        "v_add_f32 v78, v78, v90\n"
        "v_add_f32 v78, v78, v91\n"
        "v_add_f32 v78, v78, v92\n"
        "v_add_f32 v78, v78, v93\n"
        "v_add_f32 v78, v78, v38\n"
        "v_add_f32 v78, v78, v39\n"
        "v_add_f32 v78, v78, v40\n"
        "v_add_f32 v78, v78, v41\n"
        "v_pk_fma_f32 v[106:107], v[106:107], s[24:25], v[54:55] op_sel_hi:[1,0,0]\n"
        "v_pk_fma_f32 v[108:109], v[108:109], s[24:25], v[54:55] op_sel_hi:[1,0,0]\n"
        "v_pk_fma_f32 v[110:111], v[110:111], s[24:25], v[54:55] op_sel_hi:[1,0,0]\n"
        "v_pk_fma_f32 v[112:113], v[112:113], s[24:25], v[54:55] op_sel_hi:[1,0,0]\n"
        "v_pk_fma_f32 v[122:123], v[122:123], s[24:25], v[54:55] op_sel_hi:[1,0,0]\n"
        "v_pk_fma_f32 v[124:125], v[124:125], s[24:25], v[54:55] op_sel_hi:[1,0,0]\n"
        "v_pk_fma_f32 v[118:119], v[118:119], s[24:25], v[54:55] op_sel_hi:[1,0,0]\n"
        "v_pk_fma_f32 v[120:121], v[120:121], s[24:25], v[54:55] op_sel_hi:[1,0,0]\n"
        "v_pk_fma_f32 v[126:127], v[126:127], s[24:25], v[54:55] op_sel_hi:[1,0,0]\n"
        "v_pk_fma_f32 v[128:129], v[128:129], s[24:25], v[54:55] op_sel_hi:[1,0,0]\n"
        "v_pk_fma_f32 v[114:115], v[114:115], s[24:25], v[54:55] op_sel_hi:[1,0,0]\n"
        "v_pk_fma_f32 v[116:117], v[116:117], s[24:25], v[54:55] op_sel_hi:[1,0,0]\n"
        "v_pk_fma_f32 v[134:135], v[134:135], s[24:25], v[60:61] op_sel_hi:[1,0,0]\n"
        "v_pk_fma_f32 v[136:137], v[136:137], s[24:25], v[60:61] op_sel_hi:[1,0,0]\n"
        "v_pk_fma_f32 v[138:139], v[138:139], s[24:25], v[60:61] op_sel_hi:[1,0,0]\n"
        "v_pk_fma_f32 v[140:141], v[140:141], s[24:25], v[60:61] op_sel_hi:[1,0,0]\n"
        "v_pk_fma_f32 v[130:131], v[130:131], s[24:25], v[60:61] op_sel_hi:[1,0,0]\n"
        "v_pk_fma_f32 v[132:133], v[132:133], s[24:25], v[60:61] op_sel_hi:[1,0,0]\n"
        "v_pk_fma_f32 v[146:147], v[146:147], s[24:25], v[60:61] op_sel_hi:[1,0,0]\n"
        "v_pk_fma_f32 v[148:149], v[148:149], s[24:25], v[60:61] op_sel_hi:[1,0,0]\n"
        "v_pk_fma_f32 v[158:159], v[158:159], s[24:25], v[60:61] op_sel_hi:[1,0,0]\n"
        "v_pk_fma_f32 v[160:161], v[160:161], s[24:25], v[60:61] op_sel_hi:[1,0,0]\n"
        "v_pk_fma_f32 v[70:71], v[70:71], s[24:25], v[60:61] op_sel_hi:[1,0,0]\n"
        "v_pk_fma_f32 v[72:73], v[72:73], s[24:25], v[60:61] op_sel_hi:[1,0,0]\n"
        "v_cvt_pk_bf16_f32 v30, v106, v107\n"
        "v_cvt_pk_bf16_f32 v31, v108, v109\n"
        "v_cvt_pk_bf16_f32 v32, v110, v111\n"
        "v_cvt_pk_bf16_f32 v33, v112, v113\n"
        "v_cvt_pk_bf16_f32 v98, v122, v123\n"
        "v_cvt_pk_bf16_f32 v99, v124, v125\n"
        "v_cvt_pk_bf16_f32 v100, v118, v119\n"
        "v_cvt_pk_bf16_f32 v101, v120, v121\n"
        "v_cvt_pk_bf16_f32 v102, v126, v127\n"
        "v_cvt_pk_bf16_f32 v103, v128, v129\n"
        "v_cvt_pk_bf16_f32 v104, v114, v115\n"
        "v_cvt_pk_bf16_f32 v105, v116, v117\n"
        "v_cvt_pk_bf16_f32 v110, v134, v135\n"
        "v_cvt_pk_bf16_f32 v111, v136, v137\n"
        "v_cvt_pk_bf16_f32 v112, v138, v139\n"
        "v_cvt_pk_bf16_f32 v113, v140, v141\n"
        "v_cvt_pk_bf16_f32 v106, v130, v131\n"
        "v_cvt_pk_bf16_f32 v107, v132, v133\n"
        "v_cvt_pk_bf16_f32 v108, v146, v147\n"
        "v_cvt_pk_bf16_f32 v109, v148, v149\n"
        "v_cvt_pk_bf16_f32 v114, v158, v159\n"
        "v_cvt_pk_bf16_f32 v115, v160, v161\n"
        "v_cvt_pk_bf16_f32 v116, v70, v71\n"
        "v_cvt_pk_bf16_f32 v117, v72, v73\n"
        "v_add_f32 v78, v78, v94\n"
        "ds_write_b128 v42, v[30:33]\n"
        "ds_write_b128 v42, v[98:101] offset:1024\n"
        "ds_write_b128 v42, v[102:105] offset:2048\n"
        "ds_write_b128 v43, v[110:113]\n"
        "ds_write_b128 v43, v[106:109] offset:1024\n"
        "ds_write_b128 v43, v[114:117] offset:2048\n"
        "v_add_f32 v78, v78, v95\n"
        "v_add_f32 v78, v78, v96\n"
        "v_add_f32 v78, v78, v97\n"
        "v_mov_b32 v79, v78\n"
        "s_waitcnt vmcnt(9)\n"
        "s_nop 0\n"
        "v_permlane32_swap_b32 v78, v79\n"
        "v_add_f32 v78, v78, v79\n"
        "v_mov_b32 v79, v78\n"
        "s_nop 1\n"
        "v_permlane16_swap_b32 v78, v79\n"
        "v_add_f32 v78, v78, v79\n"
        "v_mul_f32 v78, 0xbac909aa, v78\n"
        "v_pk_fma_f32 v[34:35], v[34:35], s[24:25], v[78:79] op_sel_hi:[1,0,0]\n"
        "v_pk_fma_f32 v[36:37], v[36:37], s[24:25], v[78:79] op_sel_hi:[1,0,0]\n"
        "v_pk_fma_f32 v[46:47], v[46:47], s[24:25], v[78:79] op_sel_hi:[1,0,0]\n"
        "v_pk_fma_f32 v[48:49], v[48:49], s[24:25], v[78:79] op_sel_hi:[1,0,0]\n"
        "v_pk_fma_f32 v[66:67], v[66:67], s[24:25], v[78:79] op_sel_hi:[1,0,0]\n"
        "v_pk_fma_f32 v[68:69], v[68:69], s[24:25], v[78:79] op_sel_hi:[1,0,0]\n"
        "v_pk_fma_f32 v[90:91], v[90:91], s[24:25], v[78:79] op_sel_hi:[1,0,0]\n"
        "v_pk_fma_f32 v[92:93], v[92:93], s[24:25], v[78:79] op_sel_hi:[1,0,0]\n"
        "v_pk_fma_f32 v[38:39], v[38:39], s[24:25], v[78:79] op_sel_hi:[1,0,0]\n"
        "v_pk_fma_f32 v[40:41], v[40:41], s[24:25], v[78:79] op_sel_hi:[1,0,0]\n"
        "v_pk_fma_f32 v[94:95], v[94:95], s[24:25], v[78:79] op_sel_hi:[1,0,0]\n"
        "v_pk_fma_f32 v[96:97], v[96:97], s[24:25], v[78:79] op_sel_hi:[1,0,0]\n"
        "v_cvt_pk_bf16_f32 v70, v34, v35\n"
        "v_cvt_pk_bf16_f32 v71, v36, v37\n"
        "v_cvt_pk_bf16_f32 v72, v46, v47\n"
        "v_cvt_pk_bf16_f32 v73, v48, v49\n"
        "v_cvt_pk_bf16_f32 v34, v66, v67\n"
        "v_cvt_pk_bf16_f32 v35, v68, v69\n"
        "v_cvt_pk_bf16_f32 v36, v90, v91\n"
        "v_cvt_pk_bf16_f32 v37, v92, v93\n"
        "v_cvt_pk_bf16_f32 v30, v38, v39\n"
        "v_cvt_pk_bf16_f32 v31, v40, v41\n"
        "v_cvt_pk_bf16_f32 v32, v94, v95\n"
        "v_cvt_pk_bf16_f32 v33, v96, v97\n"
        "ds_write_b128 v44, v[70:73]\n"
        "ds_write_b128 v44, v[34:37] offset:1024\n"
        "ds_write_b128 v44, v[30:33] offset:2048\n"
        "s_waitcnt lgkmcnt(0)\n"
        "s_barrier\n"
        "s_branch FE_%=\n"
        "FV_%=:\n"
        "s_bitcmp1_b32 s52, 11\n"
        "s_cbranch_scc1 FG_%=\n"
        "v_mov_b32 v50, 0x3f803f80\n"
        "v_mov_b32 v51, 0x3f803f80\n"
        "v_mov_b32 v52, 0x3f803f80\n"
        "v_mov_b32 v53, 0x3f803f80\n"
        "s_waitcnt vmcnt(15)\n"
        "ds_read_b128 a[0:3], v32\n"
        "ds_read_b128 a[4:7], v32 offset:64\n"
        "ds_read_b128 a[8:11], v32 offset:128\n"
        "s_add_u32 m0, s53, 15360\n"
        "s_nop 0\n"
        "buffer_load_dwordx4 v46, s[16:19], s23 offen lds\n"
        "ds_read_b128 a[12:15], v33\n"
        "ds_read_b128 a[16:19], v33 offset:64\n"
        "ds_read_b128 a[20:23], v33 offset:128\n"
        "buffer_load_dwordx4 v47, s[16:19], s23 offen offset:1024 lds\n"
        "ds_read_b128 a[24:27], v32 offset:6144\n"
        "ds_read_b128 a[28:31], v32 offset:6208\n"
        "ds_read_b128 a[32:35], v32 offset:6272\n"
        "buffer_load_dwordx4 v48, s[16:19], s23 offen offset:2048 lds\n"
        "ds_read_b128 a[36:39], v33 offset:6144\n"
        "ds_read_b128 a[40:43], v33 offset:6208\n"
        "ds_read_b128 a[44:47], v33 offset:6272\n"
        "ds_read_b128 a[48:51], v32 offset:12288\n"
        "ds_read_b128 a[52:55], v32 offset:12352\n"
        "ds_read_b128 a[56:59], v32 offset:12416\n"
        "ds_read_b128 a[60:63], v33 offset:12288\n"
        "ds_read_b128 a[64:67], v33 offset:12352\n"
        "ds_read_b128 a[68:71], v33 offset:12416\n"
        "ds_read_b64_tr_b16 a[72:73], v34 offset:0\n"
        "ds_read_b64_tr_b16 a[74:75], v35 offset:0\n"
        "ds_read_b64_tr_b16 a[76:77], v34 offset:6144\n"
        "ds_read_b64_tr_b16 a[78:79], v35 offset:6144\n"
        "ds_read_b64_tr_b16 a[80:81], v34 offset:12288\n"
        "ds_read_b64_tr_b16 a[82:83], v35 offset:12288\n"
        "s_waitcnt lgkmcnt(15)\n"
        "v_mfma_f32_16x16x32_bf16 v[54:57], a[0:3], v[50:53], 0\n"
        "s_waitcnt vmcnt(12)\n"
        "s_waitcnt vmcnt(6)\n"
        "v_mfma_f32_16x16x32_bf16 v[54:57], a[4:7], v[50:53], v[54:57]\n"
        "s_waitcnt lgkmcnt(15)\n"
        "v_mfma_f32_16x16x32_bf16 v[54:57], a[8:11], v[50:53], v[54:57]\n"
        "s_waitcnt vmcnt(0)\n"
        "s_nop 0\n"
        "v_mfma_f32_16x16x32_bf16 v[58:61], a[20:23], v[50:53], 0\n"
        "s_waitcnt lgkmcnt(15)\n"
        "s_nop 1\n"
        "v_mfma_f32_16x16x32_bf16 v[58:61], a[16:19], v[50:53], v[58:61]\n"
        "s_waitcnt lgkmcnt(12)\n"
        "v_cvt_pk_bf16_f32 v46, v54, v55\n"
        "v_cvt_pk_bf16_f32 v47, v56, v57\n"
        "v_mfma_f32_16x16x32_bf16 v[58:61], a[12:15], v[50:53], v[58:61]\n"
        "s_waitcnt lgkmcnt(9)\n"
        "v_mfma_f32_16x16x32_bf16 v[54:57], a[24:27], v[50:53], 0\n"
        "s_waitcnt lgkmcnt(6)\n"
        "ds_read_b128 a[144:147], v30\n"
        "s_nop 0\n"
        "v_mfma_f32_16x16x32_bf16 v[54:57], a[28:31], v[50:53], v[54:57]\n"
        "s_waitcnt lgkmcnt(1)\n"
        "ds_read_b128 a[148:151], v30 offset:64\n"
        "v_cvt_pk_bf16_f32 v48, v58, v59\n"
        "v_cvt_pk_bf16_f32 v49, v60, v61\n"
        "v_mfma_f32_16x16x32_bf16 v[54:57], a[32:35], v[50:53], v[54:57]\n"
        "s_nop 0\n"
        "ds_read_b128 a[152:155], v30 offset:128\n"
        "v_mfma_f32_16x16x32_bf16 v[62:65], a[44:47], v[50:53], 0\n"
        "s_nop 0\n"
        "ds_read_b128 a[156:159], v31\n"
        "v_mfma_f32_16x16x32_bf16 v[62:65], a[40:43], v[50:53], v[62:65]\n"
        "s_nop 0\n"
        "ds_read_b128 a[160:163], v31 offset:64\n"
        "v_cvt_pk_bf16_f32 v58, v54, v55\n"
        "v_cvt_pk_bf16_f32 v59, v56, v57\n"
        "v_mfma_f32_16x16x32_bf16 v[62:65], a[36:39], v[50:53], v[62:65]\n"
        "s_nop 0\n"
        "ds_read_b128 a[164:167], v31 offset:128\n"
        "v_mfma_f32_16x16x32_bf16 v[54:57], a[48:51], v[50:53], 0\n"
        "s_nop 0\n"
        "ds_read_b128 a[168:171], v30 offset:6144\n"
        "v_mfma_f32_16x16x32_bf16 v[54:57], a[52:55], v[50:53], v[54:57]\n"
        "s_nop 0\n"
        "ds_read_b128 a[172:175], v30 offset:6208\n"
        "v_cvt_pk_bf16_f32 v60, v62, v63\n"
        "v_cvt_pk_bf16_f32 v61, v64, v65\n"
        "v_mfma_f32_16x16x32_bf16 v[54:57], a[56:59], v[50:53], v[54:57]\n"
        "s_nop 0\n"
        "ds_read_b128 a[176:179], v30 offset:6272\n"
        "v_mfma_f32_16x16x32_bf16 v[70:73], a[68:71], v[50:53], 0\n"
        "s_nop 0\n"
        "ds_read_b128 a[180:183], v31 offset:6144\n"
        "v_mfma_f32_16x16x32_bf16 v[70:73], a[64:67], v[50:53], v[70:73]\n"
        "s_nop 0\n"
        "ds_read_b128 a[184:187], v31 offset:6208\n"
        "v_cvt_pk_bf16_f32 v62, v54, v55\n"
        "v_cvt_pk_bf16_f32 v63, v56, v57\n"
        "v_mfma_f32_16x16x32_bf16 v[70:73], a[60:63], v[50:53], v[70:73]\n"
        "ds_read_b128 a[188:191], v31 offset:6272\n"
        "s_waitcnt lgkmcnt(9)\n"
        "v_mfma_f32_16x16x32_bf16 v[66:69], a[144:147], a[72:75], 0\n"
        "s_nop 0\n"
        "ds_read_b128 a[192:195], v30 offset:12288\n"
        "v_mfma_f32_16x16x32_bf16 v[66:69], a[148:151], a[76:79], v[66:69]\n"
        "s_nop 0\n"
        "ds_read_b128 a[196:199], v30 offset:12352\n"
        "v_cvt_pk_bf16_f32 v64, v70, v71\n"
        "v_cvt_pk_bf16_f32 v65, v72, v73\n"
        "v_mfma_f32_16x16x32_bf16 v[66:69], a[152:155], a[80:83], v[66:69]\n"
        "ds_read_b128 a[200:203], v30 offset:12416\n"
        "v_mfma_f32_16x16x32_bf16 v[70:73], a[152:155], v[62:65], 0\n"
        "s_waitcnt lgkmcnt(9)\n"
        "buffer_load_dwordx4 v[74:77], v36, s[36:39], 0 offen\n"
        "v_mfma_f32_16x16x32_bf16 v[70:73], a[148:151], v[58:61], v[70:73]\n"
        "s_nop 0\n"
        "ds_read_b128 a[204:207], v31 offset:12288\n"
        "ds_read_b128 a[208:211], v31 offset:12352\n"
        "v_add_f32 v254, v66, v67\n"
        "v_mfma_f32_16x16x32_bf16 v[70:73], a[144:147], v[46:49], v[70:73]\n"
        "ds_read_b128 a[212:215], v31 offset:12416\n"
        "v_add_f32 v254, v254, v68\n"
        "v_add_f32 v254, v254, v69\n"
        "v_mfma_f32_16x16x32_bf16 v[114:117], a[156:159], a[72:75], 0\n"
        "s_waitcnt lgkmcnt(9)\n"
        "ds_read_b64_tr_b16 a[84:85], v34 offset:8\n"
        "v_mfma_f32_16x16x32_bf16 v[114:117], a[160:163], a[76:79], v[114:117]\n"
        "ds_read_b64_tr_b16 a[86:87], v35 offset:8\n"
        "v_add_f32 v255, v70, v71\n"
        "v_add_f32 v255, v255, v72\n"
        "v_mfma_f32_16x16x32_bf16 v[114:117], a[164:167], a[80:83], v[114:117]\n"
        "ds_read_b64_tr_b16 a[88:89], v34 offset:6152\n"
        "v_add_f32 v255, v255, v73\n"
        "v_mfma_f32_16x16x32_bf16 v[30:33], a[164:167], v[62:65], 0\n"
        "s_waitcnt lgkmcnt(9)\n"
        "ds_read_b64_tr_b16 a[90:91], v35 offset:6152\n"
        "v_mfma_f32_16x16x32_bf16 v[30:33], a[160:163], v[58:61], v[30:33]\n"
        "s_nop 0\n"
        "ds_read_b64_tr_b16 a[92:93], v34 offset:12296\n"
        "v_add_f32 v254, v254, v114\n"
        "v_add_f32 v254, v254, v115\n"
        "v_mfma_f32_16x16x32_bf16 v[30:33], a[156:159], v[46:49], v[30:33]\n"
        "ds_read_b64_tr_b16 a[94:95], v35 offset:12296\n"
        "v_add_f32 v254, v254, v116\n"
        "v_add_f32 v254, v254, v117\n"
        "v_mfma_f32_16x16x32_bf16 v[120:123], a[168:171], a[72:75], 0\n"
        "s_waitcnt lgkmcnt(9)\n"
        "buffer_load_dwordx4 v[80:83], v36, s[36:39], 0 offen offset:16\n"
        "v_mfma_f32_16x16x32_bf16 v[120:123], a[172:175], a[76:79], v[120:123]\n"
        "s_mov_b32 s78, 0xf0000f\n"
        "ds_read_b64_tr_b16 a[96:97], v34 offset:64\n"
        "ds_read_b64_tr_b16 a[98:99], v35 offset:64\n"
        "v_add_f32 v255, v255, v30\n"
        "v_mfma_f32_16x16x32_bf16 v[120:123], a[176:179], a[80:83], v[120:123]\n"
        "s_mov_b32 s79, 0xf0000f00\n"
        "ds_read_b64_tr_b16 a[100:101], v34 offset:6208\n"
        "v_add_f32 v255, v255, v31\n"
        "v_add_f32 v255, v255, v32\n"
        "v_mfma_f32_16x16x32_bf16 v[90:93], a[176:179], v[62:65], 0\n"
        "s_mov_b32 s76, 0x11111111\n"
        "ds_read_b64_tr_b16 a[102:103], v35 offset:6208\n"
        "v_add_f32 v255, v255, v33\n"
        "v_add_f32 v254, v254, v120\n"
        "v_mfma_f32_16x16x32_bf16 v[90:93], a[172:175], v[58:61], v[90:93]\n"
        "s_waitcnt lgkmcnt(10)\n"
        "ds_read_b64_tr_b16 a[104:105], v34 offset:12352\n"
        "v_add_f32 v254, v254, v121\n"
        "v_add_f32 v254, v254, v122\n"
        "v_mfma_f32_16x16x32_bf16 v[90:93], a[168:171], v[46:49], v[90:93]\n"
        "s_mov_b32 s77, 0x11111111\n"
        "ds_read_b64_tr_b16 a[106:107], v35 offset:12352\n"
        "v_add_f32 v254, v254, v123\n"
        "v_mfma_f32_16x16x32_bf16 v[124:127], a[180:183], a[72:75], 0\n"
        "s_mov_b32 s56, 0x22222222\n"
        "ds_read_b64_tr_b16 a[108:109], v34 offset:72\n"
        "v_mfma_f32_16x16x32_bf16 v[124:127], a[184:187], a[76:79], v[124:127]\n"
        "s_mov_b32 s57, 0x22222222\n"
        "ds_read_b64_tr_b16 a[110:111], v35 offset:72\n"
        "v_add_f32 v255, v255, v90\n"
        "v_add_f32 v255, v255, v91\n"
        "v_mfma_f32_16x16x32_bf16 v[124:127], a[188:191], a[80:83], v[124:127]\n"
        "s_mov_b32 s58, 0x44444444\n"
        "ds_read_b64_tr_b16 a[112:113], v34 offset:6216\n"
        "v_add_f32 v255, v255, v92\n"
        "v_add_f32 v255, v255, v93\n"
        "v_mfma_f32_16x16x32_bf16 v[94:97], a[188:191], v[62:65], 0\n"
        "s_waitcnt lgkmcnt(9)\n"
        "buffer_load_dwordx4 v[86:89], v36, s[36:39], 0 offen offset:32\n"
        "v_mfma_f32_16x16x32_bf16 v[94:97], a[184:187], v[58:61], v[94:97]\n"
        "s_mov_b32 s59, 0x44444444\n"
        "ds_read_b64_tr_b16 a[114:115], v35 offset:6216\n"
        "ds_read_b64_tr_b16 a[116:117], v34 offset:12360\n"
        "v_add_f32 v254, v254, v124\n"
        "v_mfma_f32_16x16x32_bf16 v[94:97], a[180:183], v[46:49], v[94:97]\n"
        "s_mov_b32 s60, 0x88888888\n"
        "ds_read_b64_tr_b16 a[118:119], v35 offset:12360\n"
        "v_add_f32 v254, v254, v125\n"
        "v_add_f32 v254, v254, v126\n"
        "v_mfma_f32_16x16x32_bf16 v[128:131], a[192:195], a[72:75], 0\n"
        "s_mov_b32 s61, 0x88888888\n"
        "ds_read_b64_tr_b16 a[120:121], v34 offset:128\n"
        "v_add_f32 v254, v254, v127\n"
        "v_add_f32 v255, v255, v94\n"
        "v_mfma_f32_16x16x32_bf16 v[128:131], a[196:199], a[76:79], v[128:131]\n"
        "s_mov_b32 s62, 0xffff\n"
        "ds_read_b64_tr_b16 a[122:123], v35 offset:128\n"
        "v_add_f32 v255, v255, v95\n"
        "v_add_f32 v255, v255, v96\n"
        "v_mfma_f32_16x16x32_bf16 v[128:131], a[200:203], a[80:83], v[128:131]\n"
        "s_mov_b32 s63, 0x0\n"
        "ds_read_b64_tr_b16 a[124:125], v34 offset:6272\n"
        "v_add_f32 v255, v255, v97\n"
        "v_mfma_f32_16x16x32_bf16 v[142:145], a[200:203], v[62:65], 0\n"
        "s_mov_b32 s64, 0xffff0000\n"
        "ds_read_b64_tr_b16 a[126:127], v35 offset:6272\n"
        "v_mfma_f32_16x16x32_bf16 v[142:145], a[196:199], v[58:61], v[142:145]\n"
        "s_mov_b32 s65, 0x0\n"
        "ds_read_b64_tr_b16 a[128:129], v34 offset:12416\n"
        "v_add_f32 v254, v254, v128\n"
        "v_add_f32 v254, v254, v129\n"
        "v_mfma_f32_16x16x32_bf16 v[142:145], a[192:195], v[46:49], v[142:145]\n"
        "s_mov_b32 s66, 0x0\n"
        "ds_read_b64_tr_b16 a[130:131], v35 offset:12416\n"
        "v_add_f32 v254, v254, v130\n"
        "v_add_f32 v254, v254, v131\n"
        "v_mfma_f32_16x16x32_bf16 v[138:141], a[204:207], a[72:75], 0\n"
        "s_waitcnt lgkmcnt(12)\n"
        "buffer_load_dwordx4 v[50:53], v37, s[36:39], 0 offen\n"
        "v_mfma_f32_16x16x32_bf16 v[138:141], a[208:211], a[76:79], v[138:141]\n"
        "s_mov_b32 s67, 0xffff\n"
        "ds_read_b64_tr_b16 a[132:133], v34 offset:136\n"
        "ds_read_b64_tr_b16 a[134:135], v35 offset:136\n"
        "v_add_f32 v255, v255, v142\n"
        "v_mfma_f32_16x16x32_bf16 v[138:141], a[212:215], a[80:83], v[138:141]\n"
        "s_mov_b32 s68, 0x0\n"
        "ds_read_b64_tr_b16 a[136:137], v34 offset:6280\n"
        "v_add_f32 v255, v255, v143\n"
        "v_add_f32 v255, v255, v144\n"
        "v_mfma_f32_16x16x32_bf16 v[134:137], a[212:215], v[62:65], 0\n"
        "s_mov_b32 s69, 0xffff0000\n"
        "ds_read_b64_tr_b16 a[138:139], v35 offset:6280\n"
        "v_add_f32 v255, v255, v145\n"
        "v_add_f32 v254, v254, v138\n"
        "v_mfma_f32_16x16x32_bf16 v[134:137], a[208:211], v[58:61], v[134:137]\n"
        "s_mov_b32 s70, 0x0\n"
        "ds_read_b64_tr_b16 a[140:141], v34 offset:12424\n"
        "v_add_f32 v254, v254, v139\n"
        "v_add_f32 v254, v254, v140\n"
        "v_mfma_f32_16x16x32_bf16 v[134:137], a[204:207], v[46:49], v[134:137]\n"
        "s_mov_b32 s71, 0xffffffff\n"
        "ds_read_b64_tr_b16 a[142:143], v35 offset:12424\n"
        "v_add_f32 v254, v254, v141\n"
        "v_mov_b32 v253, v254\n"
        "v_mfma_f32_16x16x32_bf16 v[46:49], a[144:147], a[84:87], 0\n"
        "s_mov_b32 s72, 0x0\n"
        "v_permlane32_swap_b32 v254, v253\n"
        "v_add_f32 v254, v254, v253\n"
        "v_mfma_f32_16x16x32_bf16 v[46:49], a[148:151], a[88:91], v[46:49]\n"
        "s_mov_b32 s73, 0x20000\n"
        "v_mov_b32 v253, v254\n"
        "v_add_f32 v255, v255, v134\n"
        "v_add_f32 v255, v255, v135\n"
        "v_mfma_f32_16x16x32_bf16 v[46:49], a[152:155], a[92:95], v[46:49]\n"
        "s_mov_b64 vcc, s[78:79]\n"
        "buffer_load_dwordx4 v[56:59], v37, s[36:39], 0 offen offset:16\n"
        "v_add_f32 v255, v255, v136\n"
        "v_add_f32 v255, v255, v137\n"
        "v_mfma_f32_16x16x32_bf16 v[102:105], a[164:167], a[92:95], 0\n"
        "s_mov_b32 s80, 0x3c2aaaab\n"
        "v_permlane16_swap_b32 v254, v253\n"
        "v_add_f32 v254, v254, v253\n"
        "v_mfma_f32_16x16x32_bf16 v[102:105], a[160:163], a[88:91], v[102:105]\n"
        "s_mov_b32 s81, 0x3727c5ac\n"
        "v_mul_f32 v254, 0xbc2aaaab, v254\n"
        "v_add_f32 v66, v66, v254\n"
        "v_add_f32 v67, v67, v254\n"
        "v_mfma_f32_16x16x32_bf16 v[102:105], a[156:159], a[84:87], v[102:105]\n"
        "s_mov_b32 s82, 0x43800000\n"
        "v_add_f32 v68, v68, v254\n"
        "v_add_f32 v69, v69, v254\n"
        "v_add_f32 v114, v114, v254\n"
        "v_mfma_f32_16x16x32_bf16 v[106:109], a[168:171], a[84:87], 0\n"
        "s_mov_b32 s84, 0x80000000\n"
        "v_add_f32 v115, v115, v254\n"
        "v_add_f32 v116, v116, v254\n"
        "v_add_f32 v117, v117, v254\n"
        "v_mfma_f32_16x16x32_bf16 v[106:109], a[172:175], a[88:91], v[106:109]\n"
        "s_mov_b32 s85, 19\n"
        "v_add_f32 v120, v120, v254\n"
        "v_add_f32 v121, v121, v254\n"
        "v_add_f32 v122, v122, v254\n"
        "v_mfma_f32_16x16x32_bf16 v[106:109], a[176:179], a[92:95], v[106:109]\n"
        "s_waitcnt lgkmcnt(0)\n"
        "v_add_f32 v123, v123, v254\n"
        "v_add_f32 v124, v124, v254\n"
        "v_add_f32 v125, v125, v254\n"
        "v_mfma_f32_16x16x32_bf16 v[98:101], a[188:191], a[92:95], 0\n"
        "s_barrier\n"
        "v_add_f32 v126, v126, v254\n"
        "v_add_f32 v127, v127, v254\n"
        "v_add_f32 v128, v128, v254\n"
        "v_mfma_f32_16x16x32_bf16 v[98:101], a[184:187], a[88:91], v[98:101]\n"
        "s_mov_b32 s86, 0x80000000\n"
        "v_add_f32 v129, v129, v254\n"
        "v_add_f32 v130, v130, v254\n"
        "v_add_f32 v131, v131, v254\n"
        "v_mfma_f32_16x16x32_bf16 v[98:101], a[180:183], a[84:87], v[98:101]\n"
        "s_mov_b32 s89, 0x80000000\n"
        "buffer_load_dwordx4 v[62:65], v37, s[36:39], 0 offen offset:32\n"
        "v_add_f32 v138, v138, v254\n"
        "v_add_f32 v139, v139, v254\n"
        "v_mfma_f32_16x16x32_bf16 v[110:113], a[192:195], a[84:87], 0\n"
        "s_mov_b32 s29, 1\n"
        "v_add_f32 v140, v140, v254\n"
        "v_add_f32 v141, v141, v254\n"
        "v_cvt_pk_bf16_f32 v34, v66, v67\n"
        "v_mfma_f32_16x16x32_bf16 v[110:113], a[196:199], a[88:91], v[110:113]\n"
        "s_mov_b32 s31, 0x400000\n"
        "v_cvt_pk_bf16_f32 v35, v68, v69\n"
        "v_cvt_pk_bf16_f32 v36, v114, v115\n"
        "v_cvt_pk_bf16_f32 v37, v116, v117\n"
        "v_mfma_f32_16x16x32_bf16 v[110:113], a[200:203], a[92:95], v[110:113]\n"
        "s_mov_b32 s87, s49\n"
        "ds_write_b128 v39, v[34:37]\n"
        "v_cvt_pk_bf16_f32 v34, v120, v121\n"
        "v_mfma_f32_16x16x32_bf16 v[118:121], a[212:215], a[92:95], 0\n"
        "s_mov_b32 s88, s50\n"
        "v_cvt_pk_bf16_f32 v35, v122, v123\n"
        "v_cvt_pk_bf16_f32 v36, v124, v125\n"
        "v_cvt_pk_bf16_f32 v37, v126, v127\n"
        "v_mfma_f32_16x16x32_bf16 v[118:121], a[208:211], a[88:91], v[118:121]\n"
        "s_mov_b64 s[44:45], s[40:41]\n"
        "ds_write_b128 v39, v[34:37] offset:1024\n"
        "v_cvt_pk_bf16_f32 v34, v128, v129\n"
        "v_mfma_f32_16x16x32_bf16 v[118:121], a[204:207], a[84:87], v[118:121]\n"
        "s_mov_b32 s46, 0\n"
        "v_cvt_pk_bf16_f32 v35, v130, v131\n"
        "v_cvt_pk_bf16_f32 v36, v138, v139\n"
        "v_cvt_pk_bf16_f32 v37, v140, v141\n"
        "v_mfma_f32_16x16x32_bf16 v[66:69], a[144:147], a[96:99], 0\n"
        "s_mov_b32 s47, s43\n"
        "ds_write_b128 v39, v[34:37] offset:2048\n"
        "v_mov_b32 v254, v255\n"
        "v_mfma_f32_16x16x32_bf16 v[66:69], a[148:151], a[100:103], v[66:69]\n"
        "s_add_u32 m0, s52, 0\n"
        "v_permlane32_swap_b32 v255, v254\n"
        "v_add_f32 v255, v255, v254\n"
        "v_mfma_f32_16x16x32_bf16 v[66:69], a[152:155], a[104:107], v[66:69]\n"
        "s_waitcnt lgkmcnt(3)\n"
        "buffer_load_dwordx4 v0, s[36:39], s49 offen lds\n"
        "v_mov_b32 v254, v255\n"
        "v_add_f32 v253, v46, v47\n"
        "v_mfma_f32_16x16x32_bf16 v[114:117], a[164:167], a[104:107], 0\n"
        "s_waitcnt lgkmcnt(3)\n"
        "v_permlane16_swap_b32 v255, v254\n"
        "v_add_f32 v255, v255, v254\n"
        "v_mfma_f32_16x16x32_bf16 v[114:117], a[160:163], a[100:103], v[114:117]\n"
        "s_waitcnt lgkmcnt(3)\n"
        "v_add_f32 v253, v253, v48\n"
        "v_mul_f32 v255, 0xbc2aaaab, v255\n"
        "v_add_f32 v253, v253, v49\n"
        "v_mfma_f32_16x16x32_bf16 v[114:117], a[156:159], a[96:99], v[114:117]\n"
        "v_add_f32 v70, v70, v255\n"
        "v_add_f32 v71, v71, v255\n"
        "v_add_f32 v72, v72, v255\n"
        "v_mfma_f32_16x16x32_bf16 v[126:129], a[168:171], a[96:99], 0\n"
        "v_add_f32 v73, v73, v255\n"
        "v_add_f32 v30, v30, v255\n"
        "v_add_f32 v31, v31, v255\n"
        "v_mfma_f32_16x16x32_bf16 v[126:129], a[172:175], a[100:103], v[126:129]\n"
        "v_add_f32 v32, v32, v255\n"
        "v_add_f32 v33, v33, v255\n"
        "v_add_f32 v90, v90, v255\n"
        "v_mfma_f32_16x16x32_bf16 v[126:129], a[176:179], a[104:107], v[126:129]\n"
        "buffer_load_dwordx4 v1, s[36:39], s49 offen offset:1024 lds\n"
        "v_add_f32 v91, v91, v255\n"
        "v_add_f32 v92, v92, v255\n"
        "v_mfma_f32_16x16x32_bf16 v[34:37], a[188:191], a[104:107], 0\n"
        "v_add_f32 v93, v93, v255\n"
        "v_add_f32 v94, v94, v255\n"
        "v_add_f32 v95, v95, v255\n"
        "v_mfma_f32_16x16x32_bf16 v[34:37], a[184:187], a[100:103], v[34:37]\n"
        "v_add_f32 v96, v96, v255\n"
        "v_add_f32 v97, v97, v255\n"
        "v_add_f32 v142, v142, v255\n"
        "v_mfma_f32_16x16x32_bf16 v[34:37], a[180:183], a[96:99], v[34:37]\n"
        "v_add_f32 v143, v143, v255\n"
        "v_add_f32 v144, v144, v255\n"
        "v_add_f32 v145, v145, v255\n"
        "v_mfma_f32_16x16x32_bf16 v[122:125], a[192:195], a[96:99], 0\n"
        "v_add_f32 v134, v134, v255\n"
        "v_add_f32 v135, v135, v255\n"
        "v_add_f32 v136, v136, v255\n"
        "v_mfma_f32_16x16x32_bf16 v[122:125], a[196:199], a[100:103], v[122:125]\n"
        "v_add_f32 v137, v137, v255\n"
        "v_cvt_pk_bf16_f32 v138, v70, v71\n"
        "v_cvt_pk_bf16_f32 v139, v72, v73\n"
        "v_mfma_f32_16x16x32_bf16 v[122:125], a[200:203], a[104:107], v[122:125]\n"
        "buffer_load_dwordx4 v2, s[36:39], s49 offen offset:2048 lds\n"
        "v_cvt_pk_bf16_f32 v140, v30, v31\n"
        "v_cvt_pk_bf16_f32 v141, v32, v33\n"
        "v_mfma_f32_16x16x32_bf16 v[130:133], a[212:215], a[104:107], 0\n"
        "ds_write_b128 v38, v[138:141]\n"
        "v_cvt_pk_bf16_f32 v30, v90, v91\n"
        "v_mfma_f32_16x16x32_bf16 v[130:133], a[208:211], a[100:103], v[130:133]\n"
        "v_cvt_pk_bf16_f32 v31, v92, v93\n"
        "v_cvt_pk_bf16_f32 v32, v94, v95\n"
        "v_cvt_pk_bf16_f32 v33, v96, v97\n"
        "v_mfma_f32_16x16x32_bf16 v[130:133], a[204:207], a[96:99], v[130:133]\n"
        "ds_write_b128 v38, v[30:33] offset:64\n"
        "v_cvt_pk_bf16_f32 v70, v142, v143\n"
        "v_mfma_f32_16x16x32_bf16 v[30:33], a[144:147], a[108:111], 0\n"
        "v_cvt_pk_bf16_f32 v71, v144, v145\n"
        "v_cvt_pk_bf16_f32 v72, v134, v135\n"
        "v_cvt_pk_bf16_f32 v73, v136, v137\n"
        "v_mfma_f32_16x16x32_bf16 v[30:33], a[148:151], a[112:115], v[30:33]\n"
        "ds_write_b128 v38, v[70:73] offset:128\n"
        "v_add_f32 v253, v253, v102\n"
        "v_mfma_f32_16x16x32_bf16 v[30:33], a[152:155], a[116:119], v[30:33]\n"
        "buffer_load_dwordx4 v3, s[36:39], s49 offen offset:3072 lds\n"
        "v_add_f32 v253, v253, v103\n"
        "v_add_f32 v253, v253, v104\n"
        "v_mfma_f32_16x16x32_bf16 v[70:73], a[164:167], a[116:119], 0\n"
        "v_add_f32 v253, v253, v105\n"
        "v_add_f32 v253, v253, v106\n"
        "v_add_f32 v253, v253, v107\n"
        "v_mfma_f32_16x16x32_bf16 v[70:73], a[160:163], a[112:115], v[70:73]\n"
        "v_add_f32 v253, v253, v108\n"
        "v_add_f32 v253, v253, v109\n"
        "v_add_f32 v253, v253, v98\n"
        "v_mfma_f32_16x16x32_bf16 v[70:73], a[156:159], a[108:111], v[70:73]\n"
        "v_add_f32 v253, v253, v99\n"
        "v_add_f32 v253, v253, v100\n"
        "v_add_f32 v253, v253, v101\n"
        "v_mfma_f32_16x16x32_bf16 v[134:137], a[168:171], a[108:111], 0\n"
        "v_add_f32 v253, v253, v110\n"
        "v_add_f32 v253, v253, v111\n"
        "v_add_f32 v253, v253, v112\n"
        "v_mfma_f32_16x16x32_bf16 v[134:137], a[172:175], a[112:115], v[134:137]\n"
        "s_add_u32 m0, s52, 4096\n"
        "v_add_f32 v253, v253, v113\n"
        "v_add_f32 v253, v253, v118\n"
        "v_add_f32 v253, v253, v119\n"
        "v_mfma_f32_16x16x32_bf16 v[134:137], a[176:179], a[116:119], v[134:137]\n"
        "buffer_load_dwordx4 v4, s[36:39], s49 offen lds\n"
        "v_add_f32 v253, v253, v120\n"
        "v_add_f32 v253, v253, v121\n"
        "v_mfma_f32_16x16x32_bf16 v[94:97], a[188:191], a[116:119], 0\n"
        "v_mov_b32 v254, v253\n"
        "v_add_f32 v255, v66, v67\n"
        "v_add_f32 v255, v255, v68\n"
        "v_mfma_f32_16x16x32_bf16 v[94:97], a[184:187], a[112:115], v[94:97]\n"
        "v_permlane32_swap_b32 v253, v254\n"
        "v_add_f32 v253, v253, v254\n"
        "v_mfma_f32_16x16x32_bf16 v[94:97], a[180:183], a[108:111], v[94:97]\n"
        "v_mov_b32 v254, v253\n"
        "v_add_f32 v255, v255, v69\n"
        "v_add_f32 v255, v255, v114\n"
        "v_mfma_f32_16x16x32_bf16 v[138:141], a[192:195], a[108:111], 0\n"
        "v_permlane16_swap_b32 v253, v254\n"
        "v_add_f32 v253, v253, v254\n"
        "v_mfma_f32_16x16x32_bf16 v[138:141], a[196:199], a[112:115], v[138:141]\n"
        "v_mul_f32 v253, 0xbc2aaaab, v253\n"
        "v_add_f32 v46, v46, v253\n"
        "v_add_f32 v47, v47, v253\n"
        "v_mfma_f32_16x16x32_bf16 v[138:141], a[200:203], a[116:119], v[138:141]\n"
        "buffer_load_dwordx4 v5, s[36:39], s49 offen offset:1024 lds\n"
        "v_add_f32 v48, v48, v253\n"
        "v_add_f32 v49, v49, v253\n"
        "v_mfma_f32_16x16x32_bf16 v[90:93], a[212:215], a[116:119], 0\n"
        "v_add_f32 v102, v102, v253\n"
        "v_add_f32 v103, v103, v253\n"
        "v_add_f32 v104, v104, v253\n"
        "v_mfma_f32_16x16x32_bf16 v[90:93], a[208:211], a[112:115], v[90:93]\n"
        "v_add_f32 v105, v105, v253\n"
        "v_add_f32 v106, v106, v253\n"
        "v_add_f32 v107, v107, v253\n"
        "v_mfma_f32_16x16x32_bf16 v[90:93], a[204:207], a[108:111], v[90:93]\n"
        "v_add_f32 v108, v108, v253\n"
        "v_add_f32 v109, v109, v253\n"
        "v_add_f32 v98, v98, v253\n"
        "v_mfma_f32_16x16x32_bf16 v[142:145], a[144:147], a[120:123], 0\n"
        "v_add_f32 v99, v99, v253\n"
        "v_add_f32 v100, v100, v253\n"
        "v_add_f32 v101, v101, v253\n"
        "v_mfma_f32_16x16x32_bf16 v[142:145], a[148:151], a[124:127], v[142:145]\n"
        "v_add_f32 v110, v110, v253\n"
        "v_add_f32 v111, v111, v253\n"
        "v_add_f32 v112, v112, v253\n"
        "v_mfma_f32_16x16x32_bf16 v[142:145], a[152:155], a[128:131], v[142:145]\n"
        "buffer_load_dwordx4 v6, s[36:39], s49 offen offset:2048 lds\n"
        "v_add_f32 v113, v113, v253\n"
        "v_add_f32 v118, v118, v253\n"
        "v_mfma_f32_16x16x32_bf16 v[146:149], a[164:167], a[128:131], 0\n"
        "v_add_f32 v119, v119, v253\n"
        "v_add_f32 v120, v120, v253\n"
        "v_add_f32 v121, v121, v253\n"
        "v_mfma_f32_16x16x32_bf16 v[146:149], a[160:163], a[124:127], v[146:149]\n"
        "v_cvt_pk_bf16_f32 v150, v46, v47\n"
        "v_cvt_pk_bf16_f32 v151, v48, v49\n"
        "v_cvt_pk_bf16_f32 v152, v102, v103\n"
        "v_mfma_f32_16x16x32_bf16 v[146:149], a[156:159], a[120:123], v[146:149]\n"
        "v_cvt_pk_bf16_f32 v153, v104, v105\n"
        "v_cvt_pk_bf16_f32 v102, v106, v107\n"
        "v_cvt_pk_bf16_f32 v103, v108, v109\n"
        "v_mfma_f32_16x16x32_bf16 v[46:49], a[168:171], a[120:123], 0\n"
        "ds_write_b128 v40, v[150:153]\n"
        "v_cvt_pk_bf16_f32 v104, v98, v99\n"
        "v_mfma_f32_16x16x32_bf16 v[46:49], a[172:175], a[124:127], v[46:49]\n"
        "v_cvt_pk_bf16_f32 v105, v100, v101\n"
        "v_cvt_pk_bf16_f32 v106, v110, v111\n"
        "v_cvt_pk_bf16_f32 v107, v112, v113\n"
        "v_mfma_f32_16x16x32_bf16 v[46:49], a[176:179], a[128:131], v[46:49]\n"
        "ds_write_b128 v40, v[102:105] offset:1024\n"
        "v_cvt_pk_bf16_f32 v108, v118, v119\n"
        "v_mfma_f32_16x16x32_bf16 v[98:101], a[188:191], a[128:131], 0\n"
        "buffer_load_dwordx4 v7, s[36:39], s49 offen offset:3072 lds\n"
        "v_cvt_pk_bf16_f32 v109, v120, v121\n"
        "v_add_f32 v255, v255, v115\n"
        "v_mfma_f32_16x16x32_bf16 v[98:101], a[184:187], a[124:127], v[98:101]\n"
        "ds_write_b128 v40, v[106:109] offset:2048\n"
        "v_add_f32 v255, v255, v116\n"
        "v_mfma_f32_16x16x32_bf16 v[98:101], a[180:183], a[120:123], v[98:101]\n"
        "v_add_f32 v255, v255, v117\n"
        "v_add_f32 v255, v255, v126\n"
        "v_add_f32 v255, v255, v127\n"
        "v_mfma_f32_16x16x32_bf16 v[102:105], a[192:195], a[120:123], 0\n"
        "v_add_f32 v255, v255, v128\n"
        "v_add_f32 v255, v255, v129\n"
        "v_add_f32 v255, v255, v34\n"
        "v_mfma_f32_16x16x32_bf16 v[102:105], a[196:199], a[124:127], v[102:105]\n"
        "s_add_u32 m0, s52, 8192\n"
        "v_add_f32 v255, v255, v35\n"
        "v_add_f32 v255, v255, v36\n"
        "v_add_f32 v255, v255, v37\n"
        "v_mfma_f32_16x16x32_bf16 v[102:105], a[200:203], a[128:131], v[102:105]\n"
        "buffer_load_dwordx4 v8, s[36:39], s49 offen lds\n"
        "v_add_f32 v255, v255, v122\n"
        "v_add_f32 v255, v255, v123\n"
        "v_mfma_f32_16x16x32_bf16 v[110:113], a[212:215], a[128:131], 0\n"
        "v_add_f32 v255, v255, v124\n"
        "v_add_f32 v255, v255, v125\n"
        "v_add_f32 v255, v255, v130\n"
        "v_mfma_f32_16x16x32_bf16 v[110:113], a[208:211], a[124:127], v[110:113]\n"
        "v_add_f32 v255, v255, v131\n"
        "v_add_f32 v255, v255, v132\n"
        "v_add_f32 v255, v255, v133\n"
        "v_mfma_f32_16x16x32_bf16 v[110:113], a[204:207], a[120:123], v[110:113]\n"
        "v_mov_b32 v254, v255\n"
        "v_add_f32 v38, v30, v31\n"
        "v_add_f32 v38, v38, v32\n"
        "v_mfma_f32_16x16x32_bf16 v[106:109], a[144:147], a[132:135], 0\n"
        "v_permlane32_swap_b32 v255, v254\n"
        "v_add_f32 v255, v255, v254\n"
        "v_mfma_f32_16x16x32_bf16 v[106:109], a[148:151], a[136:139], v[106:109]\n"
        "v_mov_b32 v254, v255\n"
        "v_add_f32 v38, v38, v33\n"
        "v_add_f32 v38, v38, v70\n"
        "v_mfma_f32_16x16x32_bf16 v[106:109], a[152:155], a[140:143], v[106:109]\n"
        "v_permlane16_swap_b32 v255, v254\n"
        "v_add_f32 v255, v255, v254\n"
        "v_mfma_f32_16x16x32_bf16 v[118:121], a[164:167], a[140:143], 0\n"
        "v_mul_f32 v255, 0xbc2aaaab, v255\n"
        "v_add_f32 v66, v66, v255\n"
        "v_add_f32 v67, v67, v255\n"
        "v_mfma_f32_16x16x32_bf16 v[118:121], a[160:163], a[136:139], v[118:121]\n"
        "v_add_f32 v68, v68, v255\n"
        "v_add_f32 v69, v69, v255\n"
        "v_add_f32 v114, v114, v255\n"
        "v_mfma_f32_16x16x32_bf16 v[118:121], a[156:159], a[132:135], v[118:121]\n"
        "v_add_f32 v115, v115, v255\n"
        "v_add_f32 v116, v116, v255\n"
        "v_add_f32 v117, v117, v255\n"
        "v_mfma_f32_16x16x32_bf16 v[150:153], a[168:171], a[132:135], 0\n"
        "v_add_f32 v126, v126, v255\n"
        "v_add_f32 v127, v127, v255\n"
        "v_add_f32 v128, v128, v255\n"
        "v_mfma_f32_16x16x32_bf16 v[150:153], a[172:175], a[136:139], v[150:153]\n"
        "v_add_f32 v129, v129, v255\n"
        "v_add_f32 v34, v34, v255\n"
        "v_add_f32 v35, v35, v255\n"
        "v_mfma_f32_16x16x32_bf16 v[150:153], a[176:179], a[140:143], v[150:153]\n"
        "v_add_f32 v36, v36, v255\n"
        "v_add_f32 v37, v37, v255\n"
        "v_add_f32 v122, v122, v255\n"
        "v_mfma_f32_16x16x32_bf16 v[158:161], a[188:191], a[140:143], 0\n"
        "v_add_f32 v123, v123, v255\n"
        "v_add_f32 v124, v124, v255\n"
        "v_add_f32 v125, v125, v255\n"
        "v_mfma_f32_16x16x32_bf16 v[158:161], a[184:187], a[136:139], v[158:161]\n"
        "v_add_f32 v130, v130, v255\n"
        "v_add_f32 v131, v131, v255\n"
        "v_add_f32 v132, v132, v255\n"
        "v_mfma_f32_16x16x32_bf16 v[158:161], a[180:183], a[132:135], v[158:161]\n"
        "v_add_f32 v133, v133, v255\n"
        "v_cvt_pk_bf16_f32 v154, v66, v67\n"
        "v_cvt_pk_bf16_f32 v155, v68, v69\n"
        "v_mfma_f32_16x16x32_bf16 v[66:69], a[192:195], a[132:135], 0\n"
        "v_cvt_pk_bf16_f32 v156, v114, v115\n"
        "v_cvt_pk_bf16_f32 v157, v116, v117\n"
        "v_cvt_pk_bf16_f32 v114, v126, v127\n"
        "v_mfma_f32_16x16x32_bf16 v[66:69], a[196:199], a[136:139], v[66:69]\n"
        "ds_write_b128 v41, v[154:157]\n"
        "v_cvt_pk_bf16_f32 v115, v128, v129\n"
        "v_mfma_f32_16x16x32_bf16 v[66:69], a[200:203], a[140:143], v[66:69]\n"
        "v_cvt_pk_bf16_f32 v116, v34, v35\n"
        "v_cvt_pk_bf16_f32 v117, v36, v37\n"
        "v_cvt_pk_bf16_f32 v34, v122, v123\n"
        "v_mfma_f32_16x16x32_bf16 v[126:129], a[212:215], a[140:143], 0\n"
        "ds_write_b128 v41, v[114:117] offset:1024\n"
        "v_cvt_pk_bf16_f32 v35, v124, v125\n"
        "v_mfma_f32_16x16x32_bf16 v[126:129], a[208:211], a[136:139], v[126:129]\n"
        "v_cvt_pk_bf16_f32 v36, v130, v131\n"
        "v_cvt_pk_bf16_f32 v37, v132, v133\n"
        "v_add_f32 v38, v38, v71\n"
        "v_mfma_f32_16x16x32_bf16 v[126:129], a[204:207], a[132:135], v[126:129]\n"
        "ds_write_b128 v41, v[34:37] offset:2048\n"
        "v_add_f32 v38, v38, v72\n"
        "v_add_f32 v38, v38, v73\n"
        "v_add_f32 v38, v38, v134\n"
        "v_add_f32 v38, v38, v135\n"
        "v_add_f32 v38, v38, v136\n"
        "v_add_f32 v38, v38, v137\n"
        "v_add_f32 v38, v38, v94\n"
        "v_add_f32 v38, v38, v95\n"
        "v_add_f32 v38, v38, v96\n"
        "v_add_f32 v38, v38, v97\n"
        "v_add_f32 v38, v38, v138\n"
        "v_add_f32 v38, v38, v139\n"
        "v_add_f32 v38, v38, v140\n"
        "v_add_f32 v38, v38, v141\n"
        "v_add_f32 v38, v38, v90\n"
        "v_add_f32 v38, v38, v91\n"
        "v_add_f32 v38, v38, v92\n"
        "v_add_f32 v38, v38, v93\n"
        "v_mov_b32 v39, v38\n"
        "v_add_f32 v34, v142, v143\n"
        "v_add_f32 v34, v34, v144\n"
        "v_permlane32_swap_b32 v38, v39\n"
        "v_add_f32 v38, v38, v39\n"
        "v_mov_b32 v39, v38\n"
        "v_add_f32 v34, v34, v145\n"
        "v_add_f32 v34, v34, v146\n"
        "v_permlane16_swap_b32 v38, v39\n"
        "v_add_f32 v38, v38, v39\n"
        "v_mul_f32 v38, 0xbc2aaaab, v38\n"
        "v_add_f32 v34, v34, v147\n"
        "v_add_f32 v34, v34, v148\n"
        "v_add_f32 v34, v34, v149\n"
        "v_add_f32 v34, v34, v46\n"
        "v_add_f32 v34, v34, v47\n"
        "v_add_f32 v34, v34, v48\n"
        "v_add_f32 v34, v34, v49\n"
        "v_add_f32 v34, v34, v98\n"
        "v_add_f32 v34, v34, v99\n"
        "v_add_f32 v34, v34, v100\n"
        "v_add_f32 v34, v34, v101\n"
        "v_add_f32 v34, v34, v102\n"
        "v_add_f32 v34, v34, v103\n"
        "v_add_f32 v34, v34, v104\n"
        "v_add_f32 v34, v34, v105\n"
        "v_add_f32 v34, v34, v110\n"
        "v_add_f32 v34, v34, v111\n"
        "v_add_f32 v34, v34, v112\n"
        "v_add_f32 v34, v34, v113\n"
        "v_mov_b32 v35, v34\n"
        "v_add_f32 v54, v106, v107\n"
        "v_add_f32 v54, v54, v108\n"
        "v_permlane32_swap_b32 v34, v35\n"
        "v_add_f32 v34, v34, v35\n"
        "v_mov_b32 v35, v34\n"
        "v_add_f32 v54, v54, v109\n"
        "v_add_f32 v54, v54, v118\n"
        "v_permlane16_swap_b32 v34, v35\n"
        "v_add_f32 v34, v34, v35\n"
        "v_mul_f32 v34, 0xbc2aaaab, v34\n"
        "v_add_f32 v54, v54, v119\n"
        "v_add_f32 v54, v54, v120\n"
        "v_add_f32 v54, v54, v121\n"
        "v_add_f32 v54, v54, v150\n"
        "v_add_f32 v54, v54, v151\n"
        "v_add_f32 v54, v54, v152\n"
        "v_add_f32 v54, v54, v153\n"
        "v_add_f32 v54, v54, v158\n"
        "v_add_f32 v54, v54, v159\n"
        "v_add_f32 v54, v54, v160\n"
        "v_add_f32 v54, v54, v161\n"
        "v_add_f32 v54, v54, v66\n"
        "v_add_f32 v54, v54, v67\n"
        "v_add_f32 v54, v54, v68\n"
        "v_add_f32 v54, v54, v69\n"
        "v_pk_add_f32 v[30:31], v[30:31], v[38:39] op_sel_hi:[1,0]\n"
        "v_pk_add_f32 v[32:33], v[32:33], v[38:39] op_sel_hi:[1,0]\n"
        "v_pk_add_f32 v[70:71], v[70:71], v[38:39] op_sel_hi:[1,0]\n"
        "v_pk_add_f32 v[72:73], v[72:73], v[38:39] op_sel_hi:[1,0]\n"
        "v_pk_add_f32 v[134:135], v[134:135], v[38:39] op_sel_hi:[1,0]\n"
        "v_pk_add_f32 v[136:137], v[136:137], v[38:39] op_sel_hi:[1,0]\n"
        "v_pk_add_f32 v[94:95], v[94:95], v[38:39] op_sel_hi:[1,0]\n"
        "v_pk_add_f32 v[96:97], v[96:97], v[38:39] op_sel_hi:[1,0]\n"
        "v_pk_add_f32 v[138:139], v[138:139], v[38:39] op_sel_hi:[1,0]\n"
        "v_pk_add_f32 v[140:141], v[140:141], v[38:39] op_sel_hi:[1,0]\n"
        "v_pk_add_f32 v[90:91], v[90:91], v[38:39] op_sel_hi:[1,0]\n"
        "v_pk_add_f32 v[92:93], v[92:93], v[38:39] op_sel_hi:[1,0]\n"
        "v_pk_add_f32 v[142:143], v[142:143], v[34:35] op_sel_hi:[1,0]\n"
        "v_pk_add_f32 v[144:145], v[144:145], v[34:35] op_sel_hi:[1,0]\n"
        "v_pk_add_f32 v[146:147], v[146:147], v[34:35] op_sel_hi:[1,0]\n"
        "v_pk_add_f32 v[148:149], v[148:149], v[34:35] op_sel_hi:[1,0]\n"
        "v_pk_add_f32 v[46:47], v[46:47], v[34:35] op_sel_hi:[1,0]\n"
        "v_pk_add_f32 v[48:49], v[48:49], v[34:35] op_sel_hi:[1,0]\n"
        "v_pk_add_f32 v[98:99], v[98:99], v[34:35] op_sel_hi:[1,0]\n"
        "v_pk_add_f32 v[100:101], v[100:101], v[34:35] op_sel_hi:[1,0]\n"
        "v_pk_add_f32 v[102:103], v[102:103], v[34:35] op_sel_hi:[1,0]\n"
        "v_pk_add_f32 v[104:105], v[104:105], v[34:35] op_sel_hi:[1,0]\n"
        "v_pk_add_f32 v[110:111], v[110:111], v[34:35] op_sel_hi:[1,0]\n"
        "v_pk_add_f32 v[112:113], v[112:113], v[34:35] op_sel_hi:[1,0]\n"
        "v_cvt_pk_bf16_f32 v34, v30, v31\n"
        "v_cvt_pk_bf16_f32 v35, v32, v33\n"
        "v_cvt_pk_bf16_f32 v36, v70, v71\n"
        "v_cvt_pk_bf16_f32 v37, v72, v73\n"
        "v_cvt_pk_bf16_f32 v70, v134, v135\n"
        "v_cvt_pk_bf16_f32 v71, v136, v137\n"
        "v_cvt_pk_bf16_f32 v72, v94, v95\n"
        "v_cvt_pk_bf16_f32 v73, v96, v97\n"
        "v_cvt_pk_bf16_f32 v38, v138, v139\n"
        "v_cvt_pk_bf16_f32 v39, v140, v141\n"
        "v_cvt_pk_bf16_f32 v40, v90, v91\n"
        "v_cvt_pk_bf16_f32 v41, v92, v93\n"
        "v_cvt_pk_bf16_f32 v90, v142, v143\n"
        "v_cvt_pk_bf16_f32 v91, v144, v145\n"
        "v_cvt_pk_bf16_f32 v92, v146, v147\n"
        "v_cvt_pk_bf16_f32 v93, v148, v149\n"
        "v_cvt_pk_bf16_f32 v94, v46, v47\n"
        "v_cvt_pk_bf16_f32 v95, v48, v49\n"
        "v_cvt_pk_bf16_f32 v96, v98, v99\n"
        "v_cvt_pk_bf16_f32 v97, v100, v101\n"
        "v_cvt_pk_bf16_f32 v30, v102, v103\n"
        "v_cvt_pk_bf16_f32 v31, v104, v105\n"
        "v_cvt_pk_bf16_f32 v32, v110, v111\n"
        "v_cvt_pk_bf16_f32 v33, v112, v113\n"
        "v_add_f32 v54, v54, v126\n"
        "ds_write_b128 v42, v[34:37]\n"
        "ds_write_b128 v42, v[70:73] offset:1024\n"
        "ds_write_b128 v42, v[38:41] offset:2048\n"
        "ds_write_b128 v43, v[90:93]\n"
        "ds_write_b128 v43, v[94:97] offset:1024\n"
        "ds_write_b128 v43, v[30:33] offset:2048\n"
        "v_add_f32 v54, v54, v127\n"
        "v_add_f32 v54, v54, v128\n"
        "v_add_f32 v54, v54, v129\n"
        "v_mov_b32 v55, v54\n"
        "s_waitcnt vmcnt(9)\n"
        "s_nop 0\n"
        "v_permlane32_swap_b32 v54, v55\n"
        "v_add_f32 v54, v54, v55\n"
        "v_mov_b32 v55, v54\n"
        "s_nop 1\n"
        "v_permlane16_swap_b32 v54, v55\n"
        "v_add_f32 v54, v54, v55\n"
        "v_mul_f32 v54, 0xbc2aaaab, v54\n"
        "v_pk_add_f32 v[106:107], v[106:107], v[54:55] op_sel_hi:[1,0]\n"
        "v_pk_add_f32 v[108:109], v[108:109], v[54:55] op_sel_hi:[1,0]\n"
        "v_pk_add_f32 v[118:119], v[118:119], v[54:55] op_sel_hi:[1,0]\n"
        "v_pk_add_f32 v[120:121], v[120:121], v[54:55] op_sel_hi:[1,0]\n"
        "v_pk_add_f32 v[150:151], v[150:151], v[54:55] op_sel_hi:[1,0]\n"
        "v_pk_add_f32 v[152:153], v[152:153], v[54:55] op_sel_hi:[1,0]\n"
        "v_pk_add_f32 v[158:159], v[158:159], v[54:55] op_sel_hi:[1,0]\n"
        "v_pk_add_f32 v[160:161], v[160:161], v[54:55] op_sel_hi:[1,0]\n"
        "v_pk_add_f32 v[66:67], v[66:67], v[54:55] op_sel_hi:[1,0]\n"
        "v_pk_add_f32 v[68:69], v[68:69], v[54:55] op_sel_hi:[1,0]\n"
        "v_pk_add_f32 v[126:127], v[126:127], v[54:55] op_sel_hi:[1,0]\n"
        "v_pk_add_f32 v[128:129], v[128:129], v[54:55] op_sel_hi:[1,0]\n"
        "v_cvt_pk_bf16_f32 v34, v106, v107\n"
        "v_cvt_pk_bf16_f32 v35, v108, v109\n"
        "v_cvt_pk_bf16_f32 v36, v118, v119\n"
        "v_cvt_pk_bf16_f32 v37, v120, v121\n"
        "v_cvt_pk_bf16_f32 v38, v150, v151\n"
        "v_cvt_pk_bf16_f32 v39, v152, v153\n"
        "v_cvt_pk_bf16_f32 v40, v158, v159\n"
        "v_cvt_pk_bf16_f32 v41, v160, v161\n"
        "v_cvt_pk_bf16_f32 v30, v66, v67\n"
        "v_cvt_pk_bf16_f32 v31, v68, v69\n"
        "v_cvt_pk_bf16_f32 v32, v126, v127\n"
        "v_cvt_pk_bf16_f32 v33, v128, v129\n"
        "ds_write_b128 v44, v[34:37]\n"
        "ds_write_b128 v44, v[38:41] offset:1024\n"
        "ds_write_b128 v44, v[30:33] offset:2048\n"
        "s_waitcnt lgkmcnt(0)\n"
        "s_barrier\n"
        "s_branch FE_%=\n"
        "FG_%=:\n"
        "s_mov_b32 s24, 0x471c4000\n"
        "v_mov_b32 v255, 0x3f803f80\n"
        "v_mov_b32 v255, 0x3f803f80\n"
        "v_mov_b32 v255, 0x3f803f80\n"
        "v_mov_b32 v255, 0x3f803f80\n"
        "s_waitcnt vmcnt(15)\n"
        "ds_read_b64_tr_b16 a[72:73], v34 offset:0\n"
        "ds_read_b64_tr_b16 a[74:75], v35 offset:0\n"
        "ds_read_b64_tr_b16 a[76:77], v34 offset:6144\n"
        "ds_read_b64_tr_b16 a[78:79], v35 offset:6144\n"
        "ds_read_b64_tr_b16 a[80:81], v34 offset:12288\n"
        "ds_read_b64_tr_b16 a[82:83], v35 offset:12288\n"
        "s_add_u32 m0, s53, 15360\n"
        "s_nop 0\n"
        "buffer_load_dwordx4 v46, s[16:19], s23 offen lds\n"
        "ds_read_b64_tr_b16 a[84:85], v34 offset:8\n"
        "ds_read_b64_tr_b16 a[86:87], v35 offset:8\n"
        "ds_read_b64_tr_b16 a[88:89], v34 offset:6152\n"
        "ds_read_b64_tr_b16 a[90:91], v35 offset:6152\n"
        "ds_read_b64_tr_b16 a[92:93], v34 offset:12296\n"
        "ds_read_b64_tr_b16 a[94:95], v35 offset:12296\n"
        "buffer_load_dwordx4 v47, s[16:19], s23 offen offset:1024 lds\n"
        "ds_read_b64_tr_b16 a[96:97], v34 offset:64\n"
        "ds_read_b64_tr_b16 a[98:99], v35 offset:64\n"
        "ds_read_b64_tr_b16 a[100:101], v34 offset:6208\n"
        "ds_read_b64_tr_b16 a[102:103], v35 offset:6208\n"
        "ds_read_b64_tr_b16 a[104:105], v34 offset:12352\n"
        "ds_read_b64_tr_b16 a[106:107], v35 offset:12352\n"
        "buffer_load_dwordx4 v48, s[16:19], s23 offen offset:2048 lds\n"
        "ds_read_b64_tr_b16 a[108:109], v34 offset:72\n"
        "ds_read_b64_tr_b16 a[110:111], v35 offset:72\n"
        "ds_read_b64_tr_b16 a[112:113], v34 offset:6216\n"
        "ds_read_b64_tr_b16 a[114:115], v35 offset:6216\n"
        "ds_read_b64_tr_b16 a[116:117], v34 offset:12360\n"
        "ds_read_b64_tr_b16 a[118:119], v35 offset:12360\n"
        "ds_read_b64_tr_b16 a[120:121], v34 offset:128\n"
        "ds_read_b64_tr_b16 a[122:123], v35 offset:128\n"
        "ds_read_b64_tr_b16 a[124:125], v34 offset:6272\n"
        "ds_read_b64_tr_b16 a[126:127], v35 offset:6272\n"
        "ds_read_b64_tr_b16 a[128:129], v34 offset:12416\n"
        "ds_read_b64_tr_b16 a[130:131], v35 offset:12416\n"
        "ds_read_b64_tr_b16 a[132:133], v34 offset:136\n"
        "ds_read_b64_tr_b16 a[134:135], v35 offset:136\n"
        "ds_read_b64_tr_b16 a[136:137], v34 offset:6280\n"
        "ds_read_b64_tr_b16 a[138:139], v35 offset:6280\n"
        "ds_read_b64_tr_b16 a[140:141], v34 offset:12424\n"
        "ds_read_b64_tr_b16 a[142:143], v35 offset:12424\n"
        "s_waitcnt vmcnt(12)\n"
        "ds_read_b128 a[144:147], v30\n"
        "ds_read_b128 a[148:151], v30 offset:64\n"
        "ds_read_b128 a[152:155], v30 offset:128\n"
        "ds_read_b128 a[156:159], v31\n"
        "ds_read_b128 a[160:163], v31 offset:64\n"
        "ds_read_b128 a[164:167], v31 offset:128\n"
        "s_waitcnt lgkmcnt(15)\n"
        "s_waitcnt lgkmcnt(3)\n"
        "v_mfma_f32_16x16x32_bf16 v[50:53], a[144:147], a[72:75], 0\n"
        "s_waitcnt vmcnt(6)\n"
        "ds_read_b128 a[168:171], v30 offset:6144\n"
        "v_mov_b32 v255, 161888\n"
        "v_mfma_f32_16x16x32_bf16 v[50:53], a[148:151], a[76:79], v[50:53]\n"
        "s_waitcnt vmcnt(0)\n"
        "ds_read_b128 a[172:175], v30 offset:6208\n"
        "ds_read_b128 a[176:179], v30 offset:6272\n"
        "v_mfma_f32_16x16x32_bf16 v[50:53], a[152:155], a[80:83], v[50:53]\n"
        "s_waitcnt lgkmcnt(6)\n"
        "ds_read_b128 a[180:183], v31 offset:6144\n"
        "ds_read_b128 a[184:187], v31 offset:6208\n"
        "s_waitcnt lgkmcnt(5)\n"
        "v_mfma_f32_16x16x32_bf16 v[54:57], a[164:167], a[80:83], 0\n"
        "s_waitcnt lgkmcnt(5)\n"
        "ds_read_b128 a[188:191], v31 offset:6272\n"
        "ds_read_b128 a[192:195], v30 offset:12288\n"
        "v_add_f32 v254, v50, v51\n"
        "v_mfma_f32_16x16x32_bf16 v[54:57], a[160:163], a[76:79], v[54:57]\n"
        "s_waitcnt lgkmcnt(7)\n"
        "ds_read_b128 a[196:199], v30 offset:12352\n"
        "ds_read_b128 a[200:203], v30 offset:12416\n"
        "v_add_f32 v254, v254, v52\n"
        "v_mfma_f32_16x16x32_bf16 v[54:57], a[156:159], a[72:75], v[54:57]\n"
        "s_waitcnt lgkmcnt(9)\n"
        "ds_read_b128 a[204:207], v31 offset:12288\n"
        "v_add_f32 v254, v254, v53\n"
        "s_waitcnt lgkmcnt(7)\n"
        "v_mfma_f32_16x16x32_bf16 v[62:65], a[168:171], a[72:75], 0\n"
        "s_waitcnt lgkmcnt(7)\n"
        "ds_read_b128 a[208:211], v31 offset:12352\n"
        "ds_read_b128 a[212:215], v31 offset:12416\n"
        "v_add_f32 v254, v254, v54\n"
        "v_mfma_f32_16x16x32_bf16 v[62:65], a[172:175], a[76:79], v[62:65]\n"
        "s_waitcnt lgkmcnt(9)\n"
        "v_add_f32 v254, v254, v55\n"
        "v_add_f32 v254, v254, v56\n"
        "v_add_f32 v254, v254, v57\n"
        "v_mfma_f32_16x16x32_bf16 v[62:65], a[176:179], a[80:83], v[62:65]\n"
        "s_waitcnt lgkmcnt(6)\n"
        "s_nop 0\n"
        "v_mfma_f32_16x16x32_bf16 v[58:61], a[188:191], a[80:83], 0\n"
        "s_waitcnt lgkmcnt(3)\n"
        "s_nop 1\n"
        "v_mfma_f32_16x16x32_bf16 v[58:61], a[184:187], a[76:79], v[58:61]\n"
        "s_nop 0\n"
        "v_add_f32 v254, v254, v62\n"
        "v_add_f32 v254, v254, v63\n"
        "v_add_f32 v254, v254, v64\n"
        "v_mfma_f32_16x16x32_bf16 v[58:61], a[180:183], a[72:75], v[58:61]\n"
        "s_nop 0\n"
        "v_add_f32 v254, v254, v65\n"
        "v_mfma_f32_16x16x32_bf16 v[84:87], a[192:195], a[72:75], 0\n"
        "s_nop 2\n"
        "v_mfma_f32_16x16x32_bf16 v[84:87], a[196:199], a[76:79], v[84:87]\n"
        "s_nop 0\n"
        "v_add_f32 v254, v254, v58\n"
        "v_add_f32 v254, v254, v59\n"
        "v_add_f32 v254, v254, v60\n"
        "v_mfma_f32_16x16x32_bf16 v[84:87], a[200:203], a[80:83], v[84:87]\n"
        "s_nop 0\n"
        "v_add_f32 v254, v254, v61\n"
        "s_waitcnt lgkmcnt(0)\n"
        "v_mfma_f32_16x16x32_bf16 v[98:101], a[212:215], a[80:83], 0\n"
        "s_nop 1\n"
        "v_mfma_f32_16x16x32_bf16 v[98:101], a[208:211], a[76:79], v[98:101]\n"
        "s_nop 0\n"
        "v_add_f32 v254, v254, v84\n"
        "v_add_f32 v254, v254, v85\n"
        "v_add_f32 v254, v254, v86\n"
        "v_mfma_f32_16x16x32_bf16 v[98:101], a[204:207], a[72:75], v[98:101]\n"
        "s_nop 0\n"
        "v_add_f32 v254, v254, v87\n"
        "v_mfma_f32_16x16x32_bf16 v[30:33], a[144:147], a[84:87], 0\n"
        "s_nop 2\n"
        "v_mfma_f32_16x16x32_bf16 v[30:33], a[148:151], a[88:91], v[30:33]\n"
        "s_nop 0\n"
        "v_add_f32 v254, v254, v98\n"
        "v_add_f32 v254, v254, v99\n"
        "v_add_f32 v254, v254, v100\n"
        "v_mfma_f32_16x16x32_bf16 v[30:33], a[152:155], a[92:95], v[30:33]\n"
        "buffer_load_dwordx4 v[74:77], v36, s[36:39], 0 offen\n"
        "v_add_f32 v254, v254, v101\n"
        "v_mov_b32 v253, v254\n"
        "v_mfma_f32_16x16x32_bf16 v[46:49], a[164:167], a[92:95], 0\n"
        "s_nop 0\n"
        "v_permlane32_swap_b32 v254, v253\n"
        "v_add_f32 v254, v254, v253\n"
        "v_mfma_f32_16x16x32_bf16 v[46:49], a[160:163], a[88:91], v[46:49]\n"
        "v_mov_b32 v253, v254\n"
        "v_add_f32 v252, v30, v31\n"
        "v_add_f32 v252, v252, v32\n"
        "v_mfma_f32_16x16x32_bf16 v[46:49], a[156:159], a[84:87], v[46:49]\n"
        "v_permlane16_swap_b32 v254, v253\n"
        "v_add_f32 v254, v254, v253\n"
        "v_mfma_f32_16x16x32_bf16 v[124:127], a[168:171], a[84:87], 0\n"
        "v_mul_f32 v254, 0xbc2aaaab, v254\n"
        "v_add_f32 v252, v252, v33\n"
        "v_add_f32 v50, v50, v254\n"
        "v_mfma_f32_16x16x32_bf16 v[124:127], a[172:175], a[88:91], v[124:127]\n"
        "v_add_f32 v51, v51, v254\n"
        "v_add_f32 v52, v52, v254\n"
        "v_add_f32 v53, v53, v254\n"
        "v_mfma_f32_16x16x32_bf16 v[124:127], a[176:179], a[92:95], v[124:127]\n"
        "v_add_f32 v54, v54, v254\n"
        "v_add_f32 v55, v55, v254\n"
        "v_add_f32 v56, v56, v254\n"
        "v_mfma_f32_16x16x32_bf16 v[134:137], a[188:191], a[92:95], 0\n"
        "v_add_f32 v57, v57, v254\n"
        "v_add_f32 v62, v62, v254\n"
        "v_add_f32 v63, v63, v254\n"
        "v_mfma_f32_16x16x32_bf16 v[134:137], a[184:187], a[88:91], v[134:137]\n"
        "v_add_f32 v64, v64, v254\n"
        "v_add_f32 v65, v65, v254\n"
        "v_add_f32 v58, v58, v254\n"
        "v_mfma_f32_16x16x32_bf16 v[134:137], a[180:183], a[84:87], v[134:137]\n"
        "buffer_load_dwordx4 v[80:83], v36, s[36:39], 0 offen offset:16\n"
        "v_add_f32 v59, v59, v254\n"
        "v_add_f32 v60, v60, v254\n"
        "v_mfma_f32_16x16x32_bf16 v[138:141], a[192:195], a[84:87], 0\n"
        "s_barrier\n"
        "v_add_f32 v61, v61, v254\n"
        "v_add_f32 v84, v84, v254\n"
        "v_add_f32 v85, v85, v254\n"
        "v_mfma_f32_16x16x32_bf16 v[138:141], a[196:199], a[88:91], v[138:141]\n"
        "s_mov_b32 s78, 0xf0000f\n"
        "v_add_f32 v86, v86, v254\n"
        "v_add_f32 v87, v87, v254\n"
        "v_add_f32 v98, v98, v254\n"
        "v_mfma_f32_16x16x32_bf16 v[138:141], a[200:203], a[92:95], v[138:141]\n"
        "s_mov_b32 s79, 0xf0000f00\n"
        "v_add_f32 v99, v99, v254\n"
        "v_add_f32 v100, v100, v254\n"
        "v_add_f32 v101, v101, v254\n"
        "v_mfma_f32_16x16x32_bf16 v[142:145], a[212:215], a[92:95], 0\n"
        "s_mov_b32 s76, 0x11111111\n"
        "v_cvt_pk_bf16_f32 v70, v50, v51\n"
        "v_cvt_pk_bf16_f32 v71, v52, v53\n"
        "v_cvt_pk_bf16_f32 v72, v54, v55\n"
        "v_mfma_f32_16x16x32_bf16 v[142:145], a[208:211], a[88:91], v[142:145]\n"
        "s_mov_b32 s77, 0x11111111\n"
        "v_cvt_pk_bf16_f32 v73, v56, v57\n"
        "v_cvt_pk_bf16_f32 v94, v62, v63\n"
        "v_cvt_pk_bf16_f32 v95, v64, v65\n"
        "v_mfma_f32_16x16x32_bf16 v[142:145], a[204:207], a[84:87], v[142:145]\n"
        "s_mov_b32 s56, 0x22222222\n"
        "ds_write_b128 v39, v[70:73]\n"
        "v_cvt_pk_bf16_f32 v96, v58, v59\n"
        "v_mfma_f32_16x16x32_bf16 v[90:93], a[144:147], a[96:99], 0\n"
        "s_mov_b32 s57, 0x22222222\n"
        "v_cvt_pk_bf16_f32 v97, v60, v61\n"
        "v_cvt_pk_bf16_f32 v66, v84, v85\n"
        "v_cvt_pk_bf16_f32 v67, v86, v87\n"
        "v_mfma_f32_16x16x32_bf16 v[90:93], a[148:151], a[100:103], v[90:93]\n"
        "s_mov_b32 s58, 0x44444444\n"
        "ds_write_b128 v39, v[94:97] offset:1024\n"
        "v_cvt_pk_bf16_f32 v68, v98, v99\n"
        "v_mfma_f32_16x16x32_bf16 v[90:93], a[152:155], a[104:107], v[90:93]\n"
        "s_mov_b32 s59, 0x44444444\n"
        "buffer_load_dwordx4 v[86:89], v36, s[36:39], 0 offen offset:32\n"
        "v_cvt_pk_bf16_f32 v69, v100, v101\n"
        "v_add_f32 v252, v252, v46\n"
        "v_mfma_f32_16x16x32_bf16 v[98:101], a[164:167], a[104:107], 0\n"
        "s_mov_b32 s60, 0x88888888\n"
        "ds_write_b128 v39, v[66:69] offset:2048\n"
        "v_add_f32 v252, v252, v47\n"
        "v_mfma_f32_16x16x32_bf16 v[98:101], a[160:163], a[100:103], v[98:101]\n"
        "s_mov_b32 s61, 0x88888888\n"
        "v_add_f32 v252, v252, v48\n"
        "v_add_f32 v252, v252, v49\n"
        "v_add_f32 v252, v252, v124\n"
        "v_mfma_f32_16x16x32_bf16 v[98:101], a[156:159], a[96:99], v[98:101]\n"
        "s_mov_b32 s62, 0xffff\n"
        "v_add_f32 v252, v252, v125\n"
        "v_add_f32 v252, v252, v126\n"
        "v_add_f32 v252, v252, v127\n"
        "v_mfma_f32_16x16x32_bf16 v[102:105], a[168:171], a[96:99], 0\n"
        "s_mov_b32 s63, 0x0\n"
        "v_add_f32 v252, v252, v134\n"
        "v_add_f32 v252, v252, v135\n"
        "v_add_f32 v252, v252, v136\n"
        "v_mfma_f32_16x16x32_bf16 v[102:105], a[172:175], a[100:103], v[102:105]\n"
        "s_mov_b32 s64, 0xffff0000\n"
        "v_add_f32 v252, v252, v137\n"
        "v_add_f32 v252, v252, v138\n"
        "v_add_f32 v252, v252, v139\n"
        "v_mfma_f32_16x16x32_bf16 v[102:105], a[176:179], a[104:107], v[102:105]\n"
        "s_mov_b32 s65, 0x0\n"
        "v_add_f32 v252, v252, v140\n"
        "v_add_f32 v252, v252, v141\n"
        "v_add_f32 v252, v252, v142\n"
        "v_mfma_f32_16x16x32_bf16 v[110:113], a[188:191], a[104:107], 0\n"
        "s_mov_b32 s66, 0x0\n"
        "v_add_f32 v252, v252, v143\n"
        "v_add_f32 v252, v252, v144\n"
        "v_add_f32 v252, v252, v145\n"
        "v_mfma_f32_16x16x32_bf16 v[110:113], a[184:187], a[100:103], v[110:113]\n"
        "s_mov_b32 s67, 0xffff\n"
        "v_mov_b32 v254, v252\n"
        "v_add_f32 v253, v90, v91\n"
        "v_add_f32 v253, v253, v92\n"
        "v_mfma_f32_16x16x32_bf16 v[110:113], a[180:183], a[96:99], v[110:113]\n"
        "s_mov_b32 s68, 0x0\n"
        "buffer_load_dwordx4 v[50:53], v37, s[36:39], 0 offen\n"
        "v_add_f32 v253, v253, v93\n"
        "v_add_f32 v253, v253, v98\n"
        "v_mfma_f32_16x16x32_bf16 v[150:153], a[192:195], a[96:99], 0\n"
        "s_mov_b32 s69, 0xffff0000\n"
        "v_permlane32_swap_b32 v252, v254\n"
        "v_add_f32 v252, v252, v254\n"
        "v_mfma_f32_16x16x32_bf16 v[150:153], a[196:199], a[100:103], v[150:153]\n"
        "s_mov_b32 s70, 0x0\n"
        "v_mov_b32 v254, v252\n"
        "v_add_f32 v253, v253, v99\n"
        "v_add_f32 v253, v253, v100\n"
        "v_mfma_f32_16x16x32_bf16 v[150:153], a[200:203], a[104:107], v[150:153]\n"
        "s_mov_b32 s71, 0xffffffff\n"
        "v_permlane16_swap_b32 v252, v254\n"
        "v_add_f32 v252, v252, v254\n"
        "v_mfma_f32_16x16x32_bf16 v[146:149], a[212:215], a[104:107], 0\n"
        "s_mov_b32 s72, 0x0\n"
        "v_mul_f32 v252, 0xbc2aaaab, v252\n"
        "v_add_f32 v30, v30, v252\n"
        "v_add_f32 v31, v31, v252\n"
        "v_mfma_f32_16x16x32_bf16 v[146:149], a[208:211], a[100:103], v[146:149]\n"
        "s_mov_b32 s73, 0x20000\n"
        "v_add_f32 v32, v32, v252\n"
        "v_add_f32 v33, v33, v252\n"
        "v_add_f32 v46, v46, v252\n"
        "v_mfma_f32_16x16x32_bf16 v[146:149], a[204:207], a[96:99], v[146:149]\n"
        "s_mov_b64 vcc, s[78:79]\n"
        "v_add_f32 v47, v47, v252\n"
        "v_add_f32 v48, v48, v252\n"
        "v_add_f32 v49, v49, v252\n"
        "v_mfma_f32_16x16x32_bf16 v[106:109], a[144:147], a[108:111], 0\n"
        "s_mov_b32 s80, 0x3c2aaaab\n"
        "v_add_f32 v124, v124, v252\n"
        "v_add_f32 v125, v125, v252\n"
        "v_add_f32 v126, v126, v252\n"
        "v_mfma_f32_16x16x32_bf16 v[106:109], a[148:151], a[112:115], v[106:109]\n"
        "s_mov_b32 s81, 0x3727c5ac\n"
        "v_add_f32 v127, v127, v252\n"
        "v_add_f32 v134, v134, v252\n"
        "v_add_f32 v135, v135, v252\n"
        "v_mfma_f32_16x16x32_bf16 v[106:109], a[152:155], a[116:119], v[106:109]\n"
        "s_mov_b32 s82, 0x43800000\n"
        "buffer_load_dwordx4 v[56:59], v37, s[36:39], 0 offen offset:16\n"
        "v_add_f32 v136, v136, v252\n"
        "v_add_f32 v137, v137, v252\n"
        "v_mfma_f32_16x16x32_bf16 v[114:117], a[164:167], a[116:119], 0\n"
        "s_mov_b32 s84, 0x80000000\n"
        "v_add_f32 v138, v138, v252\n"
        "v_add_f32 v139, v139, v252\n"
        "v_add_f32 v140, v140, v252\n"
        "v_mfma_f32_16x16x32_bf16 v[114:117], a[160:163], a[112:115], v[114:117]\n"
        "s_mov_b32 s85, 19\n"
        "v_add_f32 v141, v141, v252\n"
        "v_add_f32 v142, v142, v252\n"
        "v_add_f32 v143, v143, v252\n"
        "v_mfma_f32_16x16x32_bf16 v[114:117], a[156:159], a[108:111], v[114:117]\n"
        "s_mov_b32 s86, 0x80000000\n"
        "v_add_f32 v144, v144, v252\n"
        "v_add_f32 v145, v145, v252\n"
        "v_cvt_pk_bf16_f32 v118, v30, v31\n"
        "v_mfma_f32_16x16x32_bf16 v[130:133], a[168:171], a[108:111], 0\n"
        "s_mov_b32 s89, 0x80000000\n"
        "v_cvt_pk_bf16_f32 v119, v32, v33\n"
        "v_cvt_pk_bf16_f32 v120, v46, v47\n"
        "v_cvt_pk_bf16_f32 v121, v48, v49\n"
        "v_mfma_f32_16x16x32_bf16 v[130:133], a[172:175], a[112:115], v[130:133]\n"
        "s_mov_b32 s29, 1\n"
        "ds_write_b128 v40, v[118:121]\n"
        "v_cvt_pk_bf16_f32 v122, v124, v125\n"
        "v_mfma_f32_16x16x32_bf16 v[130:133], a[176:179], a[116:119], v[130:133]\n"
        "s_mov_b32 s31, 0x400000\n"
        "v_cvt_pk_bf16_f32 v123, v126, v127\n"
        "v_cvt_pk_bf16_f32 v124, v134, v135\n"
        "v_cvt_pk_bf16_f32 v125, v136, v137\n"
        "v_mfma_f32_16x16x32_bf16 v[126:129], a[188:191], a[116:119], 0\n"
        "s_mov_b32 s87, s49\n"
        "v_cvt_pk_bf16_f32 v46, v138, v139\n"
        "v_cvt_pk_bf16_f32 v47, v140, v141\n"
        "v_cvt_pk_bf16_f32 v48, v142, v143\n"
        "v_mfma_f32_16x16x32_bf16 v[126:129], a[184:187], a[112:115], v[126:129]\n"
        "s_mov_b32 s88, s50\n"
        "v_cvt_pk_bf16_f32 v49, v144, v145\n"
        "v_add_f32 v253, v253, v101\n"
        "v_add_f32 v253, v253, v102\n"
        "v_mfma_f32_16x16x32_bf16 v[126:129], a[180:183], a[108:111], v[126:129]\n"
        "s_mov_b64 s[44:45], s[40:41]\n"
        "buffer_load_dwordx4 v[62:65], v37, s[36:39], 0 offen offset:32\n"
        "v_add_f32 v253, v253, v103\n"
        "v_add_f32 v253, v253, v104\n"
        "v_mfma_f32_16x16x32_bf16 v[134:137], a[192:195], a[108:111], 0\n"
        "s_mov_b32 s46, 0\n"
        "v_add_f32 v253, v253, v105\n"
        "v_add_f32 v253, v253, v110\n"
        "v_add_f32 v253, v253, v111\n"
        "v_mfma_f32_16x16x32_bf16 v[134:137], a[196:199], a[112:115], v[134:137]\n"
        "s_mov_b32 s47, s43\n"
        "v_add_f32 v253, v253, v112\n"
        "v_add_f32 v253, v253, v113\n"
        "v_add_f32 v253, v253, v150\n"
        "v_mfma_f32_16x16x32_bf16 v[134:137], a[200:203], a[116:119], v[134:137]\n"
        "v_add_f32 v253, v253, v151\n"
        "v_add_f32 v253, v253, v152\n"
        "v_add_f32 v253, v253, v153\n"
        "v_mfma_f32_16x16x32_bf16 v[30:33], a[212:215], a[116:119], 0\n"
        "v_add_f32 v253, v253, v146\n"
        "v_add_f32 v253, v253, v147\n"
        "v_add_f32 v253, v253, v148\n"
        "v_mfma_f32_16x16x32_bf16 v[30:33], a[208:211], a[112:115], v[30:33]\n"
        "v_add_f32 v253, v253, v149\n"
        "v_mov_b32 v254, v253\n"
        "v_add_f32 v38, v106, v107\n"
        "v_mfma_f32_16x16x32_bf16 v[30:33], a[204:207], a[108:111], v[30:33]\n"
        "v_permlane32_swap_b32 v253, v254\n"
        "v_add_f32 v253, v253, v254\n"
        "v_mfma_f32_16x16x32_bf16 v[138:141], a[144:147], a[120:123], 0\n"
        "v_mov_b32 v254, v253\n"
        "v_add_f32 v38, v38, v108\n"
        "v_add_f32 v38, v38, v109\n"
        "v_mfma_f32_16x16x32_bf16 v[138:141], a[148:151], a[124:127], v[138:141]\n"
        "s_add_u32 m0, s52, 0\n"
        "v_permlane16_swap_b32 v253, v254\n"
        "v_add_f32 v253, v253, v254\n"
        "v_mfma_f32_16x16x32_bf16 v[138:141], a[152:155], a[128:131], v[138:141]\n"
        "buffer_load_dwordx4 v0, s[36:39], s49 offen lds\n"
        "v_mul_f32 v253, 0xbc2aaaab, v253\n"
        "v_add_f32 v90, v90, v253\n"
        "v_mfma_f32_16x16x32_bf16 v[34:37], a[164:167], a[128:131], 0\n"
        "v_add_f32 v91, v91, v253\n"
        "v_add_f32 v92, v92, v253\n"
        "v_add_f32 v93, v93, v253\n"
        "v_mfma_f32_16x16x32_bf16 v[34:37], a[160:163], a[124:127], v[34:37]\n"
        "v_add_f32 v98, v98, v253\n"
        "v_add_f32 v99, v99, v253\n"
        "v_add_f32 v100, v100, v253\n"
        "v_mfma_f32_16x16x32_bf16 v[34:37], a[156:159], a[120:123], v[34:37]\n"
        "v_add_f32 v101, v101, v253\n"
        "v_add_f32 v102, v102, v253\n"
        "v_add_f32 v103, v103, v253\n"
        "v_mfma_f32_16x16x32_bf16 v[172:175], a[168:171], a[120:123], 0\n"
        "v_add_f32 v104, v104, v253\n"
        "v_add_f32 v105, v105, v253\n"
        "v_add_f32 v110, v110, v253\n"
        "v_mfma_f32_16x16x32_bf16 v[172:175], a[172:175], a[124:127], v[172:175]\n"
        "v_add_f32 v111, v111, v253\n"
        "v_add_f32 v112, v112, v253\n"
        "v_add_f32 v113, v113, v253\n"
        "v_mfma_f32_16x16x32_bf16 v[172:175], a[176:179], a[128:131], v[172:175]\n"
        "buffer_load_dwordx4 v1, s[36:39], s49 offen offset:1024 lds\n"
        "v_add_f32 v150, v150, v253\n"
        "v_add_f32 v151, v151, v253\n"
        "v_mfma_f32_16x16x32_bf16 v[154:157], a[188:191], a[128:131], 0\n"
        "v_add_f32 v152, v152, v253\n"
        "v_add_f32 v153, v153, v253\n"
        "v_add_f32 v146, v146, v253\n"
        "v_mfma_f32_16x16x32_bf16 v[154:157], a[184:187], a[124:127], v[154:157]\n"
        "v_add_f32 v147, v147, v253\n"
        "v_add_f32 v148, v148, v253\n"
        "v_add_f32 v149, v149, v253\n"
        "v_mfma_f32_16x16x32_bf16 v[154:157], a[180:183], a[120:123], v[154:157]\n"
        "v_cvt_pk_bf16_f32 v142, v90, v91\n"
        "v_cvt_pk_bf16_f32 v143, v92, v93\n"
        "v_cvt_pk_bf16_f32 v144, v98, v99\n"
        "v_mfma_f32_16x16x32_bf16 v[158:161], a[192:195], a[120:123], 0\n"
        "v_cvt_pk_bf16_f32 v145, v100, v101\n"
        "v_cvt_pk_bf16_f32 v90, v102, v103\n"
        "v_cvt_pk_bf16_f32 v91, v104, v105\n"
        "v_mfma_f32_16x16x32_bf16 v[158:161], a[196:199], a[124:127], v[158:161]\n"
        "v_cvt_pk_bf16_f32 v92, v110, v111\n"
        "v_cvt_pk_bf16_f32 v93, v112, v113\n"
        "v_cvt_pk_bf16_f32 v98, v150, v151\n"
        "v_mfma_f32_16x16x32_bf16 v[158:161], a[200:203], a[128:131], v[158:161]\n"
        "buffer_load_dwordx4 v2, s[36:39], s49 offen offset:2048 lds\n"
        "v_cvt_pk_bf16_f32 v99, v152, v153\n"
        "v_cvt_pk_bf16_f32 v100, v146, v147\n"
        "v_mfma_f32_16x16x32_bf16 v[150:153], a[212:215], a[128:131], 0\n"
        "v_cvt_pk_bf16_f32 v101, v148, v149\n"
        "v_add_f32 v38, v38, v114\n"
        "v_add_f32 v38, v38, v115\n"
        "v_mfma_f32_16x16x32_bf16 v[150:153], a[208:211], a[124:127], v[150:153]\n"
        "v_add_f32 v38, v38, v116\n"
        "v_add_f32 v38, v38, v117\n"
        "v_add_f32 v38, v38, v130\n"
        "v_mfma_f32_16x16x32_bf16 v[150:153], a[204:207], a[120:123], v[150:153]\n"
        "v_add_f32 v38, v38, v131\n"
        "v_add_f32 v38, v38, v132\n"
        "v_add_f32 v38, v38, v133\n"
        "v_mfma_f32_16x16x32_bf16 v[102:105], a[144:147], a[132:135], 0\n"
        "v_add_f32 v38, v38, v126\n"
        "v_add_f32 v38, v38, v127\n"
        "v_add_f32 v38, v38, v128\n"
        "v_mfma_f32_16x16x32_bf16 v[102:105], a[148:151], a[136:139], v[102:105]\n"
        "v_add_f32 v38, v38, v129\n"
        "v_add_f32 v38, v38, v134\n"
        "v_add_f32 v38, v38, v135\n"
        "v_mfma_f32_16x16x32_bf16 v[102:105], a[152:155], a[140:143], v[102:105]\n"
        "buffer_load_dwordx4 v3, s[36:39], s49 offen offset:3072 lds\n"
        "v_add_f32 v38, v38, v136\n"
        "v_add_f32 v38, v38, v137\n"
        "v_mfma_f32_16x16x32_bf16 v[146:149], a[164:167], a[140:143], 0\n"
        "v_add_f32 v38, v38, v30\n"
        "v_add_f32 v38, v38, v31\n"
        "v_add_f32 v38, v38, v32\n"
        "v_mfma_f32_16x16x32_bf16 v[146:149], a[160:163], a[136:139], v[146:149]\n"
        "v_add_f32 v38, v38, v33\n"
        "v_mov_b32 v39, v38\n"
        "v_add_f32 v54, v138, v139\n"
        "v_mfma_f32_16x16x32_bf16 v[146:149], a[156:159], a[132:135], v[146:149]\n"
        "v_permlane32_swap_b32 v38, v39\n"
        "v_add_f32 v38, v38, v39\n"
        "v_mfma_f32_16x16x32_bf16 v[180:183], a[168:171], a[132:135], 0\n"
        "v_mov_b32 v39, v38\n"
        "v_add_f32 v54, v54, v140\n"
        "v_add_f32 v54, v54, v141\n"
        "v_mfma_f32_16x16x32_bf16 v[180:183], a[172:175], a[136:139], v[180:183]\n"
        "s_add_u32 m0, s52, 4096\n"
        "v_permlane16_swap_b32 v38, v39\n"
        "v_add_f32 v38, v38, v39\n"
        "v_mfma_f32_16x16x32_bf16 v[180:183], a[176:179], a[140:143], v[180:183]\n"
        "buffer_load_dwordx4 v4, s[36:39], s49 offen lds\n"
        "v_mul_f32 v38, 0xbc2aaaab, v38\n"
        "v_add_f32 v54, v54, v34\n"
        "v_mfma_f32_16x16x32_bf16 v[110:113], a[188:191], a[140:143], 0\n"
        "v_add_f32 v54, v54, v35\n"
        "v_add_f32 v54, v54, v36\n"
        "v_add_f32 v54, v54, v37\n"
        "v_mfma_f32_16x16x32_bf16 v[110:113], a[184:187], a[136:139], v[110:113]\n"
        "v_add_f32 v54, v54, v172\n"
        "v_add_f32 v54, v54, v173\n"
        "v_add_f32 v54, v54, v174\n"
        "v_mfma_f32_16x16x32_bf16 v[110:113], a[180:183], a[132:135], v[110:113]\n"
        "v_add_f32 v54, v54, v175\n"
        "v_add_f32 v54, v54, v154\n"
        "v_add_f32 v54, v54, v155\n"
        "v_mfma_f32_16x16x32_bf16 v[184:187], a[192:195], a[132:135], 0\n"
        "v_add_f32 v54, v54, v156\n"
        "v_add_f32 v54, v54, v157\n"
        "v_add_f32 v54, v54, v158\n"
        "v_mfma_f32_16x16x32_bf16 v[184:187], a[196:199], a[136:139], v[184:187]\n"
        "v_add_f32 v54, v54, v159\n"
        "v_add_f32 v54, v54, v160\n"
        "v_add_f32 v54, v54, v161\n"
        "v_mfma_f32_16x16x32_bf16 v[184:187], a[200:203], a[140:143], v[184:187]\n"
        "buffer_load_dwordx4 v5, s[36:39], s49 offen offset:1024 lds\n"
        "v_add_f32 v54, v54, v150\n"
        "v_add_f32 v54, v54, v151\n"
        "v_mfma_f32_16x16x32_bf16 v[176:179], a[212:215], a[140:143], 0\n"
        "v_add_f32 v54, v54, v152\n"
        "v_add_f32 v54, v54, v153\n"
        "v_mov_b32 v55, v54\n"
        "v_mfma_f32_16x16x32_bf16 v[176:179], a[208:211], a[136:139], v[176:179]\n"
        "ds_write_b128 v40, v[122:125] offset:1024\n"
        "v_add_f32 v60, v102, v103\n"
        "v_mfma_f32_16x16x32_bf16 v[176:179], a[204:207], a[132:135], v[176:179]\n"
        "ds_write_b128 v40, v[46:49] offset:2048\n"
        "v_add_f32 v60, v60, v104\n"
        "v_pk_add_f32 v[106:107], v[106:107], v[38:39] op_sel_hi:[1,0]\n"
        "v_cvt_pk_bf16_f32 v188, v106, v107\n"
        "v_pk_add_f32 v[108:109], v[108:109], v[38:39] op_sel_hi:[1,0]\n"
        "v_cvt_pk_bf16_f32 v189, v108, v109\n"
        "v_pk_add_f32 v[114:115], v[114:115], v[38:39] op_sel_hi:[1,0]\n"
        "v_cvt_pk_bf16_f32 v190, v114, v115\n"
        "v_pk_add_f32 v[116:117], v[116:117], v[38:39] op_sel_hi:[1,0]\n"
        "v_cvt_pk_bf16_f32 v191, v116, v117\n"
        "v_pk_add_f32 v[130:131], v[130:131], v[38:39] op_sel_hi:[1,0]\n"
        "v_cvt_pk_bf16_f32 v114, v130, v131\n"
        "v_pk_add_f32 v[132:133], v[132:133], v[38:39] op_sel_hi:[1,0]\n"
        "v_cvt_pk_bf16_f32 v115, v132, v133\n"
        "v_pk_add_f32 v[126:127], v[126:127], v[38:39] op_sel_hi:[1,0]\n"
        "v_cvt_pk_bf16_f32 v116, v126, v127\n"
        "v_pk_add_f32 v[128:129], v[128:129], v[38:39] op_sel_hi:[1,0]\n"
        "v_cvt_pk_bf16_f32 v117, v128, v129\n"
        "v_pk_add_f32 v[134:135], v[134:135], v[38:39] op_sel_hi:[1,0]\n"
        "v_cvt_pk_bf16_f32 v106, v134, v135\n"
        "v_pk_add_f32 v[136:137], v[136:137], v[38:39] op_sel_hi:[1,0]\n"
        "v_cvt_pk_bf16_f32 v107, v136, v137\n"
        "v_pk_add_f32 v[30:31], v[30:31], v[38:39] op_sel_hi:[1,0]\n"
        "v_cvt_pk_bf16_f32 v108, v30, v31\n"
        "v_pk_add_f32 v[32:33], v[32:33], v[38:39] op_sel_hi:[1,0]\n"
        "v_cvt_pk_bf16_f32 v109, v32, v33\n"
        "v_permlane32_swap_b32 v54, v55\n"
        "v_add_f32 v54, v54, v55\n"
        "v_mov_b32 v55, v54\n"
        "s_nop 1\n"
        "v_permlane16_swap_b32 v54, v55\n"
        "v_add_f32 v54, v54, v55\n"
        "v_mul_f32 v54, 0xbc2aaaab, v54\n"
        "v_pk_add_f32 v[138:139], v[138:139], v[54:55] op_sel_hi:[1,0]\n"
        "v_cvt_pk_bf16_f32 v126, v138, v139\n"
        "v_pk_add_f32 v[140:141], v[140:141], v[54:55] op_sel_hi:[1,0]\n"
        "v_cvt_pk_bf16_f32 v127, v140, v141\n"
        "v_pk_add_f32 v[34:35], v[34:35], v[54:55] op_sel_hi:[1,0]\n"
        "v_cvt_pk_bf16_f32 v128, v34, v35\n"
        "v_pk_add_f32 v[36:37], v[36:37], v[54:55] op_sel_hi:[1,0]\n"
        "v_cvt_pk_bf16_f32 v129, v36, v37\n"
        "v_pk_add_f32 v[172:173], v[172:173], v[54:55] op_sel_hi:[1,0]\n"
        "v_cvt_pk_bf16_f32 v30, v172, v173\n"
        "v_pk_add_f32 v[174:175], v[174:175], v[54:55] op_sel_hi:[1,0]\n"
        "v_cvt_pk_bf16_f32 v31, v174, v175\n"
        "v_pk_add_f32 v[154:155], v[154:155], v[54:55] op_sel_hi:[1,0]\n"
        "v_cvt_pk_bf16_f32 v32, v154, v155\n"
        "v_pk_add_f32 v[156:157], v[156:157], v[54:55] op_sel_hi:[1,0]\n"
        "v_cvt_pk_bf16_f32 v33, v156, v157\n"
        "v_pk_add_f32 v[158:159], v[158:159], v[54:55] op_sel_hi:[1,0]\n"
        "v_cvt_pk_bf16_f32 v34, v158, v159\n"
        "v_pk_add_f32 v[160:161], v[160:161], v[54:55] op_sel_hi:[1,0]\n"
        "v_cvt_pk_bf16_f32 v35, v160, v161\n"
        "v_pk_add_f32 v[150:151], v[150:151], v[54:55] op_sel_hi:[1,0]\n"
        "v_cvt_pk_bf16_f32 v36, v150, v151\n"
        "v_pk_add_f32 v[152:153], v[152:153], v[54:55] op_sel_hi:[1,0]\n"
        "v_cvt_pk_bf16_f32 v37, v152, v153\n"
        "v_add_f32 v60, v60, v105\n"
        "v_add_f32 v60, v60, v146\n"
        "v_add_f32 v60, v60, v147\n"
        "v_add_f32 v60, v60, v148\n"
        "v_add_f32 v60, v60, v149\n"
        "v_add_f32 v60, v60, v180\n"
        "v_add_f32 v60, v60, v181\n"
        "v_add_f32 v60, v60, v182\n"
        "v_add_f32 v60, v60, v183\n"
        "v_add_f32 v60, v60, v110\n"
        "v_add_f32 v60, v60, v111\n"
        "v_add_f32 v60, v60, v112\n"
        "v_add_f32 v60, v60, v113\n"
        "v_add_f32 v60, v60, v184\n"
        "v_add_f32 v60, v60, v185\n"
        "v_add_f32 v60, v60, v186\n"
        "v_add_f32 v60, v60, v187\n"
        "v_add_f32 v60, v60, v176\n"
        "v_add_f32 v60, v60, v177\n"
        "v_add_f32 v60, v60, v178\n"
        "v_add_f32 v60, v60, v179\n"
        "v_mov_b32 v61, v60\n"
        "s_nop 1\n"
        "v_permlane32_swap_b32 v60, v61\n"
        "v_add_f32 v60, v60, v61\n"
        "v_mov_b32 v61, v60\n"
        "s_nop 1\n"
        "v_permlane16_swap_b32 v60, v61\n"
        "v_add_f32 v60, v60, v61\n"
        "v_mul_f32 v60, 0xbc2aaaab, v60\n"
        "v_pk_add_f32 v[102:103], v[102:103], v[60:61] op_sel_hi:[1,0]\n"
        "v_cvt_pk_bf16_f32 v130, v102, v103\n"
        "v_pk_add_f32 v[104:105], v[104:105], v[60:61] op_sel_hi:[1,0]\n"
        "v_cvt_pk_bf16_f32 v131, v104, v105\n"
        "v_pk_add_f32 v[146:147], v[146:147], v[60:61] op_sel_hi:[1,0]\n"
        "v_cvt_pk_bf16_f32 v132, v146, v147\n"
        "v_pk_add_f32 v[148:149], v[148:149], v[60:61] op_sel_hi:[1,0]\n"
        "v_cvt_pk_bf16_f32 v133, v148, v149\n"
        "v_pk_add_f32 v[180:181], v[180:181], v[60:61] op_sel_hi:[1,0]\n"
        "v_cvt_pk_bf16_f32 v102, v180, v181\n"
        "v_pk_add_f32 v[182:183], v[182:183], v[60:61] op_sel_hi:[1,0]\n"
        "v_cvt_pk_bf16_f32 v103, v182, v183\n"
        "v_pk_add_f32 v[110:111], v[110:111], v[60:61] op_sel_hi:[1,0]\n"
        "v_cvt_pk_bf16_f32 v104, v110, v111\n"
        "v_pk_add_f32 v[112:113], v[112:113], v[60:61] op_sel_hi:[1,0]\n"
        "v_cvt_pk_bf16_f32 v105, v112, v113\n"
        "v_pk_add_f32 v[184:185], v[184:185], v[60:61] op_sel_hi:[1,0]\n"
        "v_cvt_pk_bf16_f32 v110, v184, v185\n"
        "v_pk_add_f32 v[186:187], v[186:187], v[60:61] op_sel_hi:[1,0]\n"
        "v_cvt_pk_bf16_f32 v111, v186, v187\n"
        "v_pk_add_f32 v[176:177], v[176:177], v[60:61] op_sel_hi:[1,0]\n"
        "v_cvt_pk_bf16_f32 v112, v176, v177\n"
        "v_pk_add_f32 v[178:179], v[178:179], v[60:61] op_sel_hi:[1,0]\n"
        "v_cvt_pk_bf16_f32 v113, v178, v179\n"
        "s_nop 1\n"
        "v_mfma_f32_16x16x32_bf16 v[134:137], v[70:73], v[70:73], 0\n"
        "ds_write_b128 v41, v[142:145]\n"
        "v_mfma_f32_16x16x32_bf16 v[134:137], v[94:97], v[94:97], v[134:137]\n"
        "ds_write_b128 v41, v[90:93] offset:1024\n"
        "v_mfma_f32_16x16x32_bf16 v[134:137], v[66:69], v[66:69], v[134:137]\n"
        "buffer_load_dwordx4 v6, s[36:39], s49 offen offset:2048 lds\n"
        "v_mfma_f32_16x16x32_bf16 v[134:137], v[118:121], v[118:121], v[134:137]\n"
        "ds_write_b128 v41, v[98:101] offset:2048\n"
        "v_mfma_f32_16x16x32_bf16 v[134:137], v[122:125], v[122:125], v[134:137]\n"
        "ds_write_b128 v42, v[188:191]\n"
        "v_mfma_f32_16x16x32_bf16 v[134:137], v[46:49], v[46:49], v[134:137]\n"
        "ds_write_b128 v42, v[114:117] offset:1024\n"
        "v_mfma_f32_16x16x32_bf16 v[134:137], v[142:145], v[142:145], v[134:137]\n"
        "ds_write_b128 v42, v[106:109] offset:2048\n"
        "v_mfma_f32_16x16x32_bf16 v[134:137], v[90:93], v[90:93], v[134:137]\n"
        "ds_write_b128 v43, v[126:129]\n"
        "v_mfma_f32_16x16x32_bf16 v[134:137], v[98:101], v[98:101], v[134:137]\n"
        "buffer_load_dwordx4 v7, s[36:39], s49 offen offset:3072 lds\n"
        "v_mfma_f32_16x16x32_bf16 v[134:137], v[188:191], v[188:191], v[134:137]\n"
        "ds_write_b128 v43, v[30:33] offset:1024\n"
        "v_mfma_f32_16x16x32_bf16 v[134:137], v[114:117], v[114:117], v[134:137]\n"
        "ds_write_b128 v43, v[34:37] offset:2048\n"
        "v_mfma_f32_16x16x32_bf16 v[134:137], v[106:109], v[106:109], v[134:137]\n"
        "ds_write_b128 v44, v[130:133]\n"
        "v_mfma_f32_16x16x32_bf16 v[134:137], v[126:129], v[126:129], v[134:137]\n"
        "ds_write_b128 v44, v[102:105] offset:1024\n"
        "v_mfma_f32_16x16x32_bf16 v[134:137], v[30:33], v[30:33], v[134:137]\n"
        "s_add_u32 m0, s52, 8192\n"
        "ds_write_b128 v44, v[110:113] offset:2048\n"
        "v_mfma_f32_16x16x32_bf16 v[134:137], v[34:37], v[34:37], v[134:137]\n"
        "buffer_load_dwordx4 v8, s[36:39], s49 offen lds\n"
        "v_mfma_f32_16x16x32_bf16 v[134:137], v[130:133], v[130:133], v[134:137]\n"
        "v_mfma_f32_16x16x32_bf16 v[134:137], v[102:105], v[102:105], v[134:137]\n"
        "v_mfma_f32_16x16x32_bf16 v[134:137], v[110:113], v[110:113], v[134:137]\n"
        "s_nop 2\n"
        "s_waitcnt vmcnt(9)\n"
        "s_nop 3\n"
        "v_max3_f32 v254, v134, v135, v136\n"
        "v_max_f32 v254, v254, v137\n"
        "v_cmp_nge_f32_e64 s[74:75], s24, v254\n"
        "v_mov_b32 v254, 161632\n"
        "s_nop 0\n"
        "v_cndmask_b32_e64 v254, v255, v254, s[74:75]\n"
        "v_mov_b32 v255, 0\n"
        "ds_write_b32 v254, v255\n"
        "s_waitcnt lgkmcnt(0)\n"
        "s_barrier\n"
        "FE_%=:\n"
        "s_mov_b64 s[16:17], s[54:55]\n"
        "s_cmp_eq_u32 s51, 0\n"
        "s_cselect_b32 s18, 128, 0\n"
        "s_mov_b32 s49, 161888\n"
        "s_cselect_b32 s49, 163424, s49\n"
        "s_movk_i32 s22, 1584\n"
        "s_movk_i32 s23, 1584\n"
        "s_mov_b32 s53, 19\n"
        "s_mov_b32 s54, 0x05040302\n"
        "s_mov_b32 s55, 0x07060302\n"
        "v_mov_b32 v28, s80\n"
        "ds_read_b128 v[96:99], v26\n"
        "ds_read_b128 v[68:71], v26 offset:64\n"
        "ds_read_b128 v[100:103], v26 offset:128\n"
        "v_xor_b32 v252, 64, v164\n"
        "ds_read_b128 a[0:3], v164\n"
        "ds_read_b128 a[4:7], v164 offset:1024\n"
        "ds_read_b128 a[8:11], v164 offset:2048\n"
        "ds_read_b128 a[12:15], v164 offset:3072\n"
        "ds_read_b128 a[16:19], v164 offset:4096\n"
        "ds_read_b128 a[20:23], v164 offset:5120\n"
        "ds_read_b128 a[24:27], v164 offset:6144\n"
        "ds_read_b128 a[28:31], v164 offset:7168\n"
        "ds_read_b128 a[32:35], v164 offset:8192\n"
        "v_mfma_f32_16x16x32_bf16 v[32:35], v[74:77], v[74:77], 0\n"
        "ds_read_b128 a[36:39], v164 offset:9216\n"
        "v_mov_b32 v250, 161632\n"
        "v_mfma_f32_16x16x32_bf16 v[32:35], v[80:83], v[80:83], v[32:35]\n"
        "ds_read_b128 a[40:43], v164 offset:10240\n"
        "v_lshrrev_b32 v253, 2, v164\n"
        "v_mfma_f32_16x16x32_bf16 v[32:35], v[86:89], v[86:89], v[32:35]\n"
        "ds_read_b128 a[44:47], v164 offset:11264\n"
        "v_and_b32 v253, 128, v253\n"
        "v_mfma_f32_16x16x32_bf16 v[38:41], v[50:53], v[50:53], 0\n"
        "ds_read_b128 a[48:51], v164 offset:12288\n"
        "v_xor_b32 v253, v253, v164\n"
        "v_mfma_f32_16x16x32_bf16 v[38:41], v[56:59], v[56:59], v[38:41]\n"
        "s_nop 0\n"
        "ds_read_b128 a[52:55], v164 offset:13312\n"
        "v_cndmask_b32_e64 v249, v32, v33, s[56:57]\n"
        "v_mfma_f32_16x16x32_bf16 v[38:41], v[62:65], v[62:65], v[38:41]\n"
        "s_waitcnt lgkmcnt(14)\n"
        "ds_read_b128 a[56:59], v164 offset:14336\n"
        "v_cndmask_b32_e64 v249, v249, v34, s[58:59]\n"
        "v_mfma_f32_16x16x32_bf16 v[30:33], v[100:103], v[62:65], 0\n"
        "ds_read_b128 a[60:63], v164 offset:15360\n"
        "v_cndmask_b32_e64 v249, v249, v35, s[60:61]\n"
        "v_mfma_f32_16x16x32_bf16 v[30:33], v[68:71], v[56:59], v[30:33]\n"
        "ds_read_b128 a[64:67], v164 offset:16384\n"
        "v_cndmask_b32_e64 v255, v38, v39, s[56:57]\n"
        "v_mfma_f32_16x16x32_bf16 v[30:33], v[96:99], v[50:53], v[30:33]\n"
        "s_waitcnt lgkmcnt(14)\n"
        "ds_read_b128 a[68:71], v164 offset:17408\n"
        "v_cndmask_b32_e64 v255, v255, v40, s[58:59]\n"
        "v_mfma_f32_16x16x32_bf16 v[36:39], v[96:99], v[74:77], 0\n"
        "s_waitcnt lgkmcnt(12)\n"
        "ds_read_b128 a[72:75], v164 offset:18432\n"
        "v_cndmask_b32_e64 v255, v255, v41, s[60:61]\n"
        "v_mfma_f32_16x16x32_bf16 v[36:39], v[68:71], v[80:83], v[36:39]\n"
        "s_waitcnt lgkmcnt(10)\n"
        "ds_read_b128 a[76:79], v164 offset:19456\n"
        "v_cndmask_b32_dpp v249, v255, v249, vcc row_ror:4 row_mask:0xf bank_mask:0xf\n"
        "v_mfma_f32_16x16x32_bf16 v[36:39], v[100:103], v[86:89], v[36:39]\n"
        "ds_read_b128 a[80:83], v164 offset:20480\n"
        "v_add_u32 v255, 73792, v164\n"
        "v_mfma_f32_16x16x32_bf16 v[40:43], a[8:11], v[86:89], 0\n"
        "ds_read_b128 a[84:87], v164 offset:21504\n"
        "v_add_u32 v253, 140896, v253\n"
        "v_mfma_f32_16x16x32_bf16 v[40:43], a[4:7], v[80:83], v[40:43]\n"
        "s_waitcnt lgkmcnt(10)\n"
        "ds_read_b128 a[88:91], v164 offset:22528\n"
        "v_cndmask_b32_dpp v254, v30, v36, vcc row_ror:4 row_mask:0xf bank_mask:0xf\n"
        "v_mfma_f32_16x16x32_bf16 v[40:43], a[0:3], v[74:77], v[40:43]\n"
        "ds_read_b128 a[92:95], v164 offset:23552\n"
        "v_mul_f32 v251, s80, v254\n"
        "v_mfma_f32_16x16x32_bf16 v[32:35], a[12:15], v[74:77], 0\n"
        "ds_read_b128 a[96:99], v252 offset:24576\n"
        "v_fma_f32 v251, -v251, v254, v249\n"
        "v_mfma_f32_16x16x32_bf16 v[32:35], a[16:19], v[80:83], v[32:35]\n"
        "s_waitcnt lgkmcnt(10)\n"
        "ds_read_b128 a[100:103], v252 offset:25600\n"
        "v_cvt_pk_bf16_f32 v44, v40, v41\n"
        "v_mfma_f32_16x16x32_bf16 v[32:35], a[20:23], v[86:89], v[32:35]\n"
        "ds_read_b128 a[104:107], v252 offset:26624\n"
        "v_cvt_pk_bf16_f32 v45, v42, v43\n"
        "v_mfma_f32_16x16x32_bf16 v[38:41], a[32:35], v[86:89], 0\n"
        "ds_read_b128 a[108:111], v252 offset:27648\n"
        "v_max_f32 v251, 0, v251\n"
        "v_mfma_f32_16x16x32_bf16 v[38:41], a[28:31], v[80:83], v[38:41]\n"
        "s_waitcnt lgkmcnt(10)\n"
        "ds_read_b128 a[112:115], v252 offset:28672\n"
        "v_cvt_pk_bf16_f32 v46, v32, v33\n"
        "v_mfma_f32_16x16x32_bf16 v[38:41], a[24:27], v[74:77], v[38:41]\n"
        "ds_read_b128 a[116:119], v252 offset:29696\n"
        "v_cvt_pk_bf16_f32 v47, v34, v35\n"
        "v_mfma_f32_16x16x32_bf16 v[104:107], a[36:39], v[74:77], 0\n"
        "ds_read_b128 a[120:123], v164 offset:30720\n"
        "v_fmaak_f32 v251, v28, v251, 0x3727c5ac\n"
        "v_mfma_f32_16x16x32_bf16 v[104:107], a[40:43], v[80:83], v[104:107]\n"
        "s_nop 0\n"
        "ds_read_b128 a[124:127], v164 offset:31744\n"
        "v_cvt_pk_bf16_f32 v32, v38, v39\n"
        "v_mfma_f32_16x16x32_bf16 v[104:107], a[44:47], v[86:89], v[104:107]\n"
        "ds_read_b128 a[128:131], v164 offset:32768\n"
        "v_cvt_pk_bf16_f32 v33, v40, v41\n"
        "v_mfma_f32_16x16x32_bf16 v[90:93], a[56:59], v[86:89], 0\n"
        "ds_read_b128 a[132:135], v164 offset:33792\n"
        "v_cndmask_b32_dpp v252, v31, v37, vcc row_ror:4 row_mask:0xf bank_mask:0xf\n"
        "v_mfma_f32_16x16x32_bf16 v[90:93], a[52:55], v[80:83], v[90:93]\n"
        "s_nop 0\n"
        "ds_read_b128 a[136:139], v164 offset:34816\n"
        "v_cvt_pk_bf16_f32 v34, v104, v105\n"
        "v_mfma_f32_16x16x32_bf16 v[90:93], a[48:51], v[74:77], v[90:93]\n"
        "s_nop 0\n"
        "ds_read_b128 a[140:143], v164 offset:35840\n"
        "v_cvt_pk_bf16_f32 v35, v106, v107\n"
        "v_mfma_f32_16x16x32_bf16 v[36:39], a[60:63], v[74:77], 0\n"
        "s_nop 0\n"
        "v_rsq_f32 v246, v251\n"
        "v_mfma_f32_16x16x32_bf16 v[36:39], a[64:67], v[80:83], v[36:39]\n"
        "s_nop 0\n"
        "v_cvt_pk_bf16_f32 v40, v90, v91\n"
        "v_cvt_pk_bf16_f32 v41, v92, v93\n"
        "v_mfma_f32_16x16x32_bf16 v[36:39], a[68:71], v[86:89], v[36:39]\n"
        "s_nop 1\n"
        "v_mfma_f32_16x16x32_bf16 v[90:93], a[8:11], v[62:65], 0\n"
        "s_nop 2\n"
        "v_mfma_f32_16x16x32_bf16 v[90:93], a[4:7], v[56:59], v[90:93]\n"
        "s_nop 0\n"
        "v_cvt_pk_bf16_f32 v42, v36, v37\n"
        "v_cvt_pk_bf16_f32 v43, v38, v39\n"
        "v_mfma_f32_16x16x32_bf16 v[90:93], a[0:3], v[50:53], v[90:93]\n"
        "s_nop 1\n"
        "v_mfma_f32_16x16x32_bf16 v[36:39], v[74:77], v[44:47], 0\n"
        "s_nop 2\n"
        "v_mfma_f32_16x16x32_bf16 v[36:39], v[80:83], v[32:35], v[36:39]\n"
        "s_nop 0\n"
        "v_cvt_pk_bf16_f32 v32, v90, v91\n"
        "v_cvt_pk_bf16_f32 v33, v92, v93\n"
        "v_mfma_f32_16x16x32_bf16 v[36:39], v[86:89], v[40:43], v[36:39]\n"
        "v_mfma_f32_16x16x32_bf16 v[40:43], a[20:23], v[62:65], 0\n"
        "s_waitcnt lgkmcnt(0)\n"
        "v_mfma_f32_16x16x32_bf16 v[40:43], a[16:19], v[56:59], v[40:43]\n"
        "s_barrier\n"
        "ds_read_b128 a[144:147], v253\n"
        "v_mfma_f32_16x16x32_bf16 v[40:43], a[12:15], v[50:53], v[40:43]\n"
        "s_nop 1\n"
        "v_mfma_f32_16x16x32_bf16 v[44:47], a[24:27], v[50:53], 0\n"
        "s_nop 0\n"
        "ds_read_b128 a[148:151], v253 offset:1024\n"
        "s_nop 0\n"
        "v_mfma_f32_16x16x32_bf16 v[44:47], a[28:31], v[56:59], v[44:47]\n"
        "s_nop 0\n"
        "v_cvt_pk_bf16_f32 v34, v40, v41\n"
        "v_cvt_pk_bf16_f32 v35, v42, v43\n"
        "v_mfma_f32_16x16x32_bf16 v[44:47], a[32:35], v[62:65], v[44:47]\n"
        "s_nop 0\n"
        "v_mfma_f32_16x16x32_bf16 v[90:93], a[44:47], v[62:65], 0\n"
        "s_nop 2\n"
        "v_mfma_f32_16x16x32_bf16 v[90:93], a[40:43], v[56:59], v[90:93]\n"
        "s_nop 0\n"
        "ds_read_b128 a[152:155], v253 offset:2048\n"
        "v_cvt_pk_bf16_f32 v40, v44, v45\n"
        "v_mfma_f32_16x16x32_bf16 v[90:93], a[36:39], v[50:53], v[90:93]\n"
        "s_nop 0\n"
        "v_cvt_pk_bf16_f32 v41, v46, v47\n"
        "v_mfma_f32_16x16x32_bf16 v[46:49], a[48:51], v[50:53], 0\n"
        "s_nop 2\n"
        "v_mfma_f32_16x16x32_bf16 v[46:49], a[52:55], v[56:59], v[46:49]\n"
        "s_nop 0\n"
        "v_cvt_pk_bf16_f32 v42, v90, v91\n"
        "v_cvt_pk_bf16_f32 v43, v92, v93\n"
        "v_mfma_f32_16x16x32_bf16 v[46:49], a[56:59], v[62:65], v[46:49]\n"
        "s_nop 0\n"
        "ds_read_b128 a[156:159], v253 offset:3072\n"
        "v_mfma_f32_16x16x32_bf16 v[90:93], a[68:71], v[62:65], 0\n"
        "s_nop 2\n"
        "v_mfma_f32_16x16x32_bf16 v[90:93], a[64:67], v[56:59], v[90:93]\n"
        "s_nop 0\n"
        "v_cvt_pk_bf16_f32 v44, v46, v47\n"
        "v_cvt_pk_bf16_f32 v45, v48, v49\n"
        "v_mfma_f32_16x16x32_bf16 v[90:93], a[60:63], v[50:53], v[90:93]\n"
        "s_nop 0\n"
        "ds_read_b128 a[160:163], v253 offset:4096\n"
        "v_mfma_f32_16x16x32_bf16 v[108:111], v[74:77], a[72:75], 0\n"
        "s_nop 2\n"
        "v_mfma_f32_16x16x32_bf16 v[108:111], v[80:83], a[76:79], v[108:111]\n"
        "s_nop 0\n"
        "v_cvt_pk_bf16_f32 v46, v90, v91\n"
        "v_cvt_pk_bf16_f32 v47, v92, v93\n"
        "v_mfma_f32_16x16x32_bf16 v[108:111], v[86:89], a[80:83], v[108:111]\n"
        "s_nop 1\n"
        "v_mfma_f32_16x16x32_bf16 v[104:107], v[86:89], a[92:95], 0\n"
        "s_nop 0\n"
        "ds_read_b128 a[164:167], v253 offset:5120\n"
        "s_nop 0\n"
        "v_mfma_f32_16x16x32_bf16 v[104:107], v[80:83], a[88:91], v[104:107]\n"
        "s_nop 0\n"
        "v_cvt_pk_bf16_f32 v30, v108, v109\n"
        "v_cvt_pk_bf16_f32 v31, v110, v111\n"
        "v_mfma_f32_16x16x32_bf16 v[104:107], v[74:77], a[84:87], v[104:107]\n"
        "s_nop 0\n"
        "v_mfma_f32_16x16x32_bf16 v[90:93], v[50:53], v[32:35], 0\n"
        "s_nop 2\n"
        "v_mfma_f32_16x16x32_bf16 v[90:93], v[56:59], v[40:43], v[90:93]\n"
        "s_nop 0\n"
        "ds_read_b128 a[168:171], v253 offset:6144\n"
        "v_cvt_pk_bf16_f32 v34, v104, v105\n"
        "v_mfma_f32_16x16x32_bf16 v[90:93], v[62:65], v[44:47], v[90:93]\n"
        "s_waitcnt vmcnt(0)\n"
        "v_cvt_pk_bf16_f32 v35, v106, v107\n"
        "v_mfma_f32_16x16x32_bf16 v[44:47], v[86:89], a[104:107], 0\n"
        "s_nop 0\n"
        "ds_read_b128 v[146:149], v9\n"
        "v_mfma_f32_16x16x32_bf16 v[44:47], v[80:83], a[100:103], v[44:47]\n"
        "s_nop 0\n"
        "ds_read_b128 v[152:155], v10\n"
        "v_cndmask_b32_dpp v249, v90, v36, vcc row_ror:4 row_mask:0xf bank_mask:0xf\n"
        "v_mfma_f32_16x16x32_bf16 v[44:47], v[74:77], a[96:99], v[44:47]\n"
        "ds_read_b128 v[158:161], v11\n"
        "v_cndmask_b32_dpp v251, v91, v37, vcc row_ror:4 row_mask:0xf bank_mask:0xf\n"
        "v_mfma_f32_16x16x32_bf16 v[188:191], v[74:77], a[108:111], 0\n"
        "ds_read_b128 v[126:129], v9 offset:3072\n"
        "v_cndmask_b32_dpp v248, v92, v38, vcc row_ror:4 row_mask:0xf bank_mask:0xf\n"
        "v_mfma_f32_16x16x32_bf16 v[188:191], v[80:83], a[112:115], v[188:191]\n"
        "ds_read_b128 v[132:135], v10 offset:3072\n"
        "v_cndmask_b32_dpp v247, v93, v39, vcc row_ror:4 row_mask:0xf bank_mask:0xf\n"
        "v_mfma_f32_16x16x32_bf16 v[188:191], v[86:89], a[116:119], v[188:191]\n"
        "ds_read_b128 v[138:141], v11 offset:3072\n"
        "v_mul_f32_dpp v249, v246, v249 quad_perm:[0,0,0,0] row_mask:0xf bank_mask:0xf\n"
        "v_mfma_f32_16x16x32_bf16 v[192:195], v[86:89], a[128:131], 0\n"
        "ds_read_b128 v[106:109], v9 offset:6144\n"
        "v_mul_f32_dpp v251, v246, v251 quad_perm:[1,1,1,1] row_mask:0xf bank_mask:0xf\n"
        "v_mfma_f32_16x16x32_bf16 v[192:195], v[80:83], a[124:127], v[192:195]\n"
        "ds_read_b128 v[112:115], v10 offset:6144\n"
        "v_mul_f32_dpp v248, v246, v248 quad_perm:[2,2,2,2] row_mask:0xf bank_mask:0xf\n"
        "v_mfma_f32_16x16x32_bf16 v[192:195], v[74:77], a[120:123], v[192:195]\n"
        "ds_read_b128 v[118:121], v11 offset:6144\n"
        "v_mul_f32_dpp v247, v246, v247 quad_perm:[3,3,3,3] row_mask:0xf bank_mask:0xf\n"
        "v_mfma_f32_16x16x32_bf16 v[180:183], v[74:77], a[132:135], 0\n"
        "ds_read_b128 a[172:175], v253 offset:7168\n"
        "v_max3_f32 v245, v249, v251, v248\n"
        "v_mfma_f32_16x16x32_bf16 v[180:183], v[80:83], a[136:139], v[180:183]\n"
        "s_waitcnt lgkmcnt(7)\n"
        "ds_read_b128 a[176:179], v253 offset:8192\n"
        "v_max_f32 v245, v245, v247\n"
        "v_mfma_f32_16x16x32_bf16 v[180:183], v[86:89], a[140:143], v[180:183]\n"
        "v_mul_f32 v245, v245, v246\n"
        "v_fma_f32 v249, v249, v246, -v245\n"
        "v_mfma_f32_16x16x32_bf16 v[142:145], v[146:149], v[146:149], 0\n"
        "s_mov_b32 s26, s89\n"
        "v_fma_f32 v251, v251, v246, -v245\n"
        "v_fma_f32 v248, v248, v246, -v245\n"
        "v_mfma_f32_16x16x32_bf16 v[142:145], v[152:155], v[152:155], v[142:145]\n"
        "s_add_u32 s89, s86, s51\n"
        "v_fma_f32 v247, v247, v246, -v245\n"
        "v_cvt_pk_bf16_f32 v42, v44, v45\n"
        "v_mfma_f32_16x16x32_bf16 v[142:145], v[158:161], v[158:161], v[142:145]\n"
        "s_mov_b32 s30, s29\n"
        "v_exp_f32 v249, v249\n"
        "v_mfma_f32_16x16x32_bf16 v[122:125], v[100:103], v[158:161], 0\n"
        "s_add_u32 s29, s29, 1\n"
        "v_exp_f32 v251, v251\n"
        "v_mfma_f32_16x16x32_bf16 v[122:125], v[68:71], v[152:155], v[122:125]\n"
        "s_mov_b32 s86, s87\n"
        "v_exp_f32 v248, v248\n"
        "v_mfma_f32_16x16x32_bf16 v[122:125], v[96:99], v[146:149], v[122:125]\n"
        "s_add_u32 s88, s88, 32\n"
        "v_exp_f32 v247, v247\n"
        "v_mfma_f32_16x16x32_bf16 v[196:199], a[0:3], v[146:149], 0\n"
        "s_add_u32 s87, s87, 43008\n"
        "v_add_f32 v245, v249, v251\n"
        "v_add_f32 v244, v248, v247\n"
        "v_mfma_f32_16x16x32_bf16 v[196:199], a[4:7], v[152:155], v[196:199]\n"
        "s_cmp_ge_u32 s88, 73\n"
        "v_add_f32 v245, v245, v244\n"
        "v_cvt_pk_bf16_f32 v43, v46, v47\n"
        "v_mfma_f32_16x16x32_bf16 v[196:199], a[8:11], v[158:161], v[196:199]\n"
        "s_cselect_b32 s92, 588672, 0\n"
        "v_rcp_f32 v245, v245\n"
        "v_mfma_f32_16x16x32_bf16 v[176:179], a[20:23], v[158:161], 0\n"
        "s_cselect_b32 s94, 73, 0\n"
        "ds_read_b64 v[32:33], v27\n"
        "ds_read_b64 v[44:45], v27\n"
        "v_mfma_f32_16x16x32_bf16 v[176:179], a[16:19], v[152:155], v[176:179]\n"
        "s_sub_u32 s88, s88, s94\n"
        "ds_read_b64 v[38:39], v27\n"
        "v_mul_f32 v249, v249, v245\n"
        "v_mfma_f32_16x16x32_bf16 v[176:179], a[12:15], v[146:149], v[176:179]\n"
        "s_add_u32 s87, s87, s92\n"
        "ds_read_b32 v250, v250\n"
        "v_mul_f32 v251, v251, v245\n"
        "v_mfma_f32_16x16x32_bf16 v[92:95], a[24:27], v[146:149], 0\n"
        "s_cmp_gt_i32 s85, 0\n"
        "v_mul_f32 v248, v248, v245\n"
        "v_mul_f32 v247, v247, v245\n"
        "v_mfma_f32_16x16x32_bf16 v[92:95], a[28:31], v[152:155], v[92:95]\n"
        "s_cselect_b32 s94, s87, s84\n"
        "v_mul_f32_dpp v249, v246, v249 quad_perm:[0,0,0,0] row_mask:0xf bank_mask:0xf\n"
        "v_mul_f32_dpp v251, v246, v251 quad_perm:[1,1,1,1] row_mask:0xf bank_mask:0xf\n"
        "v_mfma_f32_16x16x32_bf16 v[92:95], a[32:35], v[158:161], v[92:95]\n"
        "s_sub_u32 s85, s85, 1\n"
        "v_mul_f32_dpp v248, v246, v248 quad_perm:[2,2,2,2] row_mask:0xf bank_mask:0xf\n"
        "v_mul_f32_dpp v247, v246, v247 quad_perm:[3,3,3,3] row_mask:0xf bank_mask:0xf\n"
        "v_mfma_f32_16x16x32_bf16 v[172:175], a[44:47], v[158:161], 0\n"
        "s_waitcnt lgkmcnt(9)\n"
        "v_cvt_pk_bf16_f32 v245, v249, v251\n"
        "v_cvt_pk_bf16_f32 v246, v248, v247\n"
        "v_mfma_f32_16x16x32_bf16 v[172:175], a[40:43], v[152:155], v[172:175]\n"
        "s_waitcnt lgkmcnt(6)\n"
        "v_and_b32 v72, v25, v245\n"
        "v_and_b32 v73, v25, v246\n"
        "v_mfma_f32_16x16x32_bf16 v[172:175], a[36:39], v[146:149], v[172:175]\n"
        "v_mov_b64 v[78:79], v[72:73]\n"
        "v_mov_b64 v[84:85], v[72:73]\n"
        "v_mfma_f32_16x16x32_bf16 v[184:187], a[48:51], v[146:149], 0\n"
        "s_waitcnt lgkmcnt(1)\n"
        "v_cvt_pk_bf16_f32 v46, v188, v189\n"
        "v_cvt_pk_bf16_f32 v47, v190, v191\n"
        "v_mfma_f32_16x16x32_bf16 v[184:187], a[52:55], v[152:155], v[184:187]\n"
        "s_waitcnt lgkmcnt(0)\n"
        "v_readfirstlane_b32 s83, v250\n"
        "v_cvt_pk_bf16_f32 v36, v192, v193\n"
        "v_mfma_f32_16x16x32_bf16 v[184:187], a[56:59], v[158:161], v[184:187]\n"
        "v_cvt_pk_bf16_f32 v37, v194, v195\n"
        "v_mov_b64 v[90:91], v[72:73]\n"
        "v_mfma_f32_16x16x32_bf16 v[188:191], a[68:71], v[158:161], 0\n"
        "v_cvt_pk_bf16_f32 v40, v180, v181\n"
        "v_cvt_pk_bf16_f32 v41, v182, v183\n"
        "v_mfma_f32_16x16x32_bf16 v[188:191], a[64:67], v[152:155], v[188:191]\n"
        "ds_read_b128 a[180:183], v253 offset:9216\n"
        "v_and_b32_dpp v48, v245, v25 row_ror:12 row_mask:0xf bank_mask:0xf\n"
        "v_mfma_f32_16x16x32_bf16 v[188:191], a[60:63], v[146:149], v[188:191]\n"
        "ds_read_b128 a[184:187], v253 offset:10240\n"
        "v_and_b32_dpp v49, v246, v25 row_ror:12 row_mask:0xf bank_mask:0xf\n"
        "v_mfma_f32_16x16x32_bf16 v[180:183], v[30:33], v[72:75], 0\n"
        "ds_read_b128 a[188:191], v253 offset:11264\n"
        "v_mov_b64 v[54:55], v[48:49]\n"
        "v_mfma_f32_16x16x32_bf16 v[200:203], v[32:35], v[76:79], 0\n"
        "ds_read_b128 a[192:195], v253 offset:12288\n"
        "v_mov_b64 v[60:61], v[48:49]\n"
        "v_mfma_f32_16x16x32_bf16 v[72:75], v[50:53], a[72:75], 0\n"
        "s_nop 0\n"
        "ds_read_b128 a[196:199], v253 offset:13312\n"
        "v_cvt_pk_bf16_f32 v76, v180, v181\n"
        "v_mfma_f32_16x16x32_bf16 v[72:75], v[56:59], a[76:79], v[72:75]\n"
        "ds_read_b128 a[200:203], v253 offset:14336\n"
        "v_cvt_pk_bf16_f32 v192, v182, v183\n"
        "v_mfma_f32_16x16x32_bf16 v[72:75], v[62:65], a[80:83], v[72:75]\n"
        "s_add_u32 m0, s52, 0\n"
        "ds_read_b128 a[204:207], v253 offset:15360\n"
        "v_cvt_pk_bf16_f32 v180, v200, v201\n"
        "v_mfma_f32_16x16x32_bf16 v[204:207], v[62:65], a[92:95], 0\n"
        "ds_read_b128 a[208:211], v253 offset:16384\n"
        "v_cvt_pk_bf16_f32 v77, v202, v203\n"
        "v_mfma_f32_16x16x32_bf16 v[204:207], v[56:59], a[88:91], v[204:207]\n"
        "buffer_load_dwordx4 v0, s[36:39], s94 offen lds\n"
        "v_cvt_pk_bf16_f32 v30, v72, v73\n"
        "v_mfma_f32_16x16x32_bf16 v[204:207], v[50:53], a[84:87], v[204:207]\n"
        "ds_read_b128 a[212:215], v253 offset:17408\n"
        "v_cvt_pk_bf16_f32 v31, v74, v75\n"
        "v_mfma_f32_16x16x32_bf16 v[72:75], v[42:45], v[78:81], 0\n"
        "s_nop 0\n"
        "ds_read_b128 a[216:219], v255\n"
        "v_mov_b64 v[66:67], v[48:49]\n"
        "v_mfma_f32_16x16x32_bf16 v[200:203], v[44:47], v[82:85], 0\n"
        "s_nop 0\n"
        "v_cvt_pk_bf16_f32 v34, v204, v205\n"
        "v_cvt_pk_bf16_f32 v35, v206, v207\n"
        "v_mfma_f32_16x16x32_bf16 v[80:83], v[50:53], a[96:99], 0\n"
        "v_cvt_pk_bf16_f32 v193, v72, v73\n"
        "v_cvt_pk_bf16_f32 v181, v74, v75\n"
        "v_mfma_f32_16x16x32_bf16 v[80:83], v[56:59], a[100:103], v[80:83]\n"
        "s_nop 0\n"
        "v_cvt_pk_bf16_f32 v78, v200, v201\n"
        "v_cvt_pk_bf16_f32 v194, v202, v203\n"
        "v_mfma_f32_16x16x32_bf16 v[80:83], v[62:65], a[104:107], v[80:83]\n"
        "ds_read_b128 a[220:223], v255 offset:1024\n"
        "v_cvt_pk_bf16_f32 v72, v196, v197\n"
        "v_mfma_f32_16x16x32_bf16 v[200:203], v[62:65], a[116:119], 0\n"
        "v_cvt_pk_bf16_f32 v73, v198, v199\n"
        "v_cvt_pk_bf16_f32 v74, v176, v177\n"
        "v_mfma_f32_16x16x32_bf16 v[200:203], v[56:59], a[112:115], v[200:203]\n"
        "s_nop 0\n"
        "buffer_load_dwordx4 v1, s[36:39], s94 offen offset:1024 lds\n"
        "v_cvt_pk_bf16_f32 v42, v80, v81\n"
        "v_mfma_f32_16x16x32_bf16 v[200:203], v[50:53], a[108:111], v[200:203]\n"
        "v_cvt_pk_bf16_f32 v43, v82, v83\n"
        "v_cvt_pk_bf16_f32 v75, v178, v179\n"
        "v_mfma_f32_16x16x32_bf16 v[196:199], v[36:39], v[84:87], 0\n"
        "s_nop 0\n"
        "ds_read_b128 a[224:227], v255 offset:2048\n"
        "v_cvt_pk_bf16_f32 v176, v92, v93\n"
        "v_mfma_f32_16x16x32_bf16 v[84:87], v[38:41], v[88:91], 0\n"
        "s_nop 0\n"
        "v_cvt_pk_bf16_f32 v46, v200, v201\n"
        "v_cvt_pk_bf16_f32 v47, v202, v203\n"
        "v_mfma_f32_16x16x32_bf16 v[80:83], v[50:53], a[120:123], 0\n"
        "v_cvt_pk_bf16_f32 v182, v196, v197\n"
        "v_cvt_pk_bf16_f32 v79, v198, v199\n"
        "v_mfma_f32_16x16x32_bf16 v[80:83], v[56:59], a[124:127], v[80:83]\n"
        "ds_write_b128 v12, v[76:79]\n"
        "v_mfma_f32_16x16x32_bf16 v[80:83], v[62:65], a[128:131], v[80:83]\n"
        "v_cvt_pk_bf16_f32 v195, v84, v85\n"
        "v_cvt_pk_bf16_f32 v183, v86, v87\n"
        "v_mfma_f32_16x16x32_bf16 v[90:93], v[62:65], a[140:143], 0\n"
        "ds_write_b128 v12, v[192:195] offset:1024\n"
        "v_mfma_f32_16x16x32_bf16 v[90:93], v[56:59], a[136:139], v[90:93]\n"
        "ds_write_b128 v12, v[180:183] offset:2048\n"
        "v_mfma_f32_16x16x32_bf16 v[90:93], v[50:53], a[132:135], v[90:93]\n"
        "buffer_load_dwordx4 v2, s[36:39], s94 offen offset:2048 lds\n"
        "v_cvt_pk_bf16_f32 v177, v94, v95\n"
        "v_mfma_f32_16x16x32_bf16 v[196:199], v[76:79], v[76:79], 0\n"
        "v_cvt_pk_bf16_f32 v178, v172, v173\n"
        "v_cvt_pk_bf16_f32 v179, v174, v175\n"
        "v_mfma_f32_16x16x32_bf16 v[196:199], v[192:195], v[192:195], v[196:199]\n"
        "v_cvt_pk_bf16_f32 v76, v184, v185\n"
        "v_cvt_pk_bf16_f32 v77, v186, v187\n"
        "v_mfma_f32_16x16x32_bf16 v[196:199], v[180:183], v[180:183], v[196:199]\n"
        "v_cvt_pk_bf16_f32 v78, v188, v189\n"
        "v_cvt_pk_bf16_f32 v79, v190, v191\n"
        "v_mfma_f32_16x16x32_bf16 v[86:89], v[126:129], v[126:129], 0\n"
        "v_cvt_pk_bf16_f32 v36, v80, v81\n"
        "v_cvt_pk_bf16_f32 v37, v82, v83\n"
        "v_mfma_f32_16x16x32_bf16 v[86:89], v[132:135], v[132:135], v[86:89]\n"
        "v_cvt_pk_bf16_f32 v40, v90, v91\n"
        "v_cvt_pk_bf16_f32 v41, v92, v93\n"
        "v_mfma_f32_16x16x32_bf16 v[86:89], v[138:141], v[138:141], v[86:89]\n"
        "ds_read_b128 a[228:231], v255 offset:3088\n"
        "v_mul_f32_dpp v253, v252, v249 quad_perm:[0,0,0,0] row_mask:0xf bank_mask:0xf\n"
        "v_mfma_f32_16x16x32_bf16 v[80:83], v[100:103], v[138:141], 0\n"
        "buffer_load_dwordx4 v3, s[36:39], s94 offen offset:3072 lds\n"
        "v_fmac_f32_dpp v253, v252, v251 quad_perm:[1,1,1,1] row_mask:0xf bank_mask:0xf\n"
        "v_mfma_f32_16x16x32_bf16 v[80:83], v[68:71], v[132:135], v[80:83]\n"
        "ds_read_b128 a[232:235], v255 offset:4112\n"
        "v_fmac_f32_dpp v253, v252, v248 quad_perm:[2,2,2,2] row_mask:0xf bank_mask:0xf\n"
        "v_mfma_f32_16x16x32_bf16 v[80:83], v[96:99], v[126:129], v[80:83]\n"
        "ds_read_b128 a[236:239], v255 offset:5136\n"
        "v_cndmask_b32_e64 v251, v196, v197, s[56:57]\n"
        "v_mfma_f32_16x16x32_bf16 v[172:175], v[30:33], v[48:51], 0\n"
        "ds_read_b128 a[240:243], v255 offset:6176\n"
        "v_cndmask_b32_e64 v251, v251, v198, s[58:59]\n"
        "v_mfma_f32_16x16x32_bf16 v[90:93], v[32:35], v[52:55], 0\n"
        "s_nop 0\n"
        "ds_read_b128 a[244:247], v255 offset:7200\n"
        "v_cndmask_b32_e64 v251, v251, v199, s[60:61]\n"
        "v_mfma_f32_16x16x32_bf16 v[180:183], v[42:45], v[54:57], 0\n"
        "s_nop 0\n"
        "v_cvt_pk_bf16_f32 v34, v172, v173\n"
        "v_cvt_pk_bf16_f32 v50, v174, v175\n"
        "v_mfma_f32_16x16x32_bf16 v[82:85], v[44:47], v[58:61], 0\n"
        "s_add_u32 m0, s52, 4096\n"
        "v_cvt_pk_bf16_f32 v46, v90, v91\n"
        "v_cvt_pk_bf16_f32 v35, v92, v93\n"
        "v_mfma_f32_16x16x32_bf16 v[54:57], v[36:39], v[60:63], 0\n"
        "v_cvt_pk_bf16_f32 v51, v180, v181\n"
        "v_cvt_pk_bf16_f32 v47, v182, v183\n"
        "v_mfma_f32_16x16x32_bf16 v[58:61], v[38:41], v[64:67], 0\n"
        "buffer_load_dwordx4 v4, s[36:39], s94 offen lds\n"
        "v_cvt_pk_bf16_f32 v36, v82, v83\n"
        "v_mfma_f32_16x16x32_bf16 v[40:43], v[146:149], v[72:75], 0\n"
        "s_nop 0\n"
        "v_cvt_pk_bf16_f32 v52, v84, v85\n"
        "v_cvt_pk_bf16_f32 v48, v54, v55\n"
        "v_mfma_f32_16x16x32_bf16 v[40:43], v[152:155], v[176:179], v[40:43]\n"
        "v_cvt_pk_bf16_f32 v37, v56, v57\n"
        "v_cvt_pk_bf16_f32 v53, v58, v59\n"
        "v_mfma_f32_16x16x32_bf16 v[40:43], v[158:161], v[76:79], v[40:43]\n"
        "ds_write_b128 v12, v[34:37] offset:3072\n"
        "v_mfma_f32_16x16x32_bf16 v[54:57], a[8:11], v[138:141], 0\n"
        "ds_write_b128 v12, v[50:53] offset:4096\n"
        "v_mfma_f32_16x16x32_bf16 v[54:57], a[4:7], v[132:135], v[54:57]\n"
        "v_cvt_pk_bf16_f32 v49, v60, v61\n"
        "v_fmac_f32_dpp v253, v252, v247 quad_perm:[3,3,3,3] row_mask:0xf bank_mask:0xf\n"
        "v_mfma_f32_16x16x32_bf16 v[54:57], a[0:3], v[126:129], v[54:57]\n"
        "ds_write_b128 v12, v[46:49] offset:5120\n"
        "v_mfma_f32_16x16x32_bf16 v[58:61], v[34:37], v[34:37], 0\n"
        "s_nop 0\n"
        "v_add_f32 v253, v253, v254\n"
        "v_mul_f32 v252, s80, v253\n"
        "v_mfma_f32_16x16x32_bf16 v[58:61], v[50:53], v[50:53], v[58:61]\n"
        "s_nop 0\n"
        "buffer_load_dwordx4 v5, s[36:39], s94 offen offset:1024 lds\n"
        "v_cvt_pk_bf16_f32 v34, v54, v55\n"
        "v_mfma_f32_16x16x32_bf16 v[58:61], v[46:49], v[46:49], v[58:61]\n"
        "v_cvt_pk_bf16_f32 v35, v56, v57\n"
        "v_cndmask_b32_e64 v254, v86, v87, s[56:57]\n"
        "v_mfma_f32_16x16x32_bf16 v[46:49], a[20:23], v[138:141], 0\n"
        "s_nop 0\n"
        "ds_read_b128 a[248:251], v255 offset:8224\n"
        "ds_read_b128 a[252:255], v255 offset:9264\n"
        "v_mfma_f32_16x16x32_bf16 v[46:49], a[16:19], v[132:135], v[46:49]\n"
        "s_nop 0\n"
        "v_cndmask_b32_e64 v250, v58, v59, s[56:57]\n"
        "v_cndmask_b32_e64 v250, v250, v60, s[58:59]\n"
        "v_mfma_f32_16x16x32_bf16 v[46:49], a[12:15], v[126:129], v[46:49]\n"
        "v_cndmask_b32_e64 v250, v250, v61, s[60:61]\n"
        "v_cndmask_b32_e64 v254, v254, v88, s[58:59]\n"
        "v_mfma_f32_16x16x32_bf16 v[54:57], a[24:27], v[126:129], 0\n"
        "v_cndmask_b32_dpp v251, v250, v251, vcc row_ror:4 row_mask:0xf bank_mask:0xf\n"
        "v_fma_f32 v252, -v252, v253, v251\n"
        "v_mfma_f32_16x16x32_bf16 v[54:57], a[28:31], v[132:135], v[54:57]\n"
        "v_max_f32 v252, 0, v252\n"
        "v_fmaak_f32 v252, v28, v252, 0x3727c5ac\n"
        "v_mfma_f32_16x16x32_bf16 v[54:57], a[32:35], v[138:141], v[54:57]\n"
        "v_sub_f32 v251, 0x47c00000, v251\n"
        "v_bfi_b32 v252, s84, v251, v252\n"
        "v_mfma_f32_16x16x32_bf16 v[58:61], a[44:47], v[138:141], 0\n"
        "buffer_load_dwordx4 v6, s[36:39], s94 offen offset:2048 lds\n"
        "v_cvt_pk_bf16_f32 v36, v46, v47\n"
        "v_mfma_f32_16x16x32_bf16 v[58:61], a[40:43], v[132:135], v[58:61]\n"
        "v_rsq_f32 v253, v252\n"
        "v_mfma_f32_16x16x32_bf16 v[58:61], a[36:39], v[126:129], v[58:61]\n"
        "v_cvt_f16_f32 v253, v253\n"
        "v_cvt_pk_bf16_f32 v37, v48, v49\n"
        "v_mfma_f32_16x16x32_bf16 v[46:49], a[48:51], v[126:129], 0\n"
        "ds_write_b16 v18, v253\n"
        "v_mfma_f32_16x16x32_bf16 v[46:49], a[52:55], v[132:135], v[46:49]\n"
        "ds_write_b16 v19, v253\n"
        "v_mfma_f32_16x16x32_bf16 v[46:49], a[56:59], v[138:141], v[46:49]\n"
        "ds_read_b128 v[92:95], v255 offset:10288\n"
        "v_cvt_pk_bf16_f32 v172, v54, v55\n"
        "v_mfma_f32_16x16x32_bf16 v[180:183], a[68:71], v[138:141], 0\n"
        "ds_read_b128 v[52:55], v255 offset:11312\n"
        "v_cvt_pk_bf16_f32 v173, v56, v57\n"
        "v_mfma_f32_16x16x32_bf16 v[180:183], a[64:67], v[132:135], v[180:183]\n"
        "ds_read_b128 v[64:67], v255 offset:12352\n"
        "v_cvt_pk_bf16_f32 v174, v58, v59\n"
        "v_mfma_f32_16x16x32_bf16 v[180:183], a[60:63], v[126:129], v[180:183]\n"
        "buffer_load_dwordx4 v7, s[36:39], s94 offen offset:3072 lds\n"
        "v_cvt_pk_bf16_f32 v175, v60, v61\n"
        "v_mfma_f32_16x16x32_bf16 v[56:59], v[106:109], v[106:109], 0\n"
        "ds_read_b128 v[84:87], v255 offset:13376\n"
        "v_cvt_pk_bf16_f32 v176, v46, v47\n"
        "v_mfma_f32_16x16x32_bf16 v[56:59], v[112:115], v[112:115], v[56:59]\n"
        "ds_read_b128 v[76:79], v255 offset:14400\n"
        "v_cvt_pk_bf16_f32 v177, v48, v49\n"
        "v_mfma_f32_16x16x32_bf16 v[56:59], v[118:121], v[118:121], v[56:59]\n"
        "ds_read_b128 v[60:63], v255 offset:15440\n"
        "v_cvt_pk_bf16_f32 v178, v180, v181\n"
        "v_mfma_f32_16x16x32_bf16 v[184:187], v[100:103], v[118:121], 0\n"
        "s_waitcnt lgkmcnt(6)\n"
        "ds_read_b128 v[72:75], v255 offset:16464\n"
        "v_cvt_pk_bf16_f32 v179, v182, v183\n"
        "v_mfma_f32_16x16x32_bf16 v[184:187], v[68:71], v[112:115], v[184:187]\n"
        "s_barrier\n"
        "v_cndmask_b32_e64 v253, v56, v57, s[56:57]\n"
        "v_cndmask_b32_e64 v253, v253, v58, s[58:59]\n"
        "v_mfma_f32_16x16x32_bf16 v[184:187], v[96:99], v[106:109], v[184:187]\n"
        "s_add_u32 m0, s52, 8192\n"
        "v_cndmask_b32_e64 v254, v254, v89, s[60:61]\n"
        "v_cndmask_b32_e64 v253, v253, v59, s[60:61]\n"
        "v_mfma_f32_16x16x32_bf16 v[56:59], v[126:129], v[34:37], 0\n"
        "ds_read_b128 v[48:51], v255 offset:17488\n"
        "v_cndmask_b32_e64 v252, v142, v143, s[56:57]\n"
        "v_mfma_f32_16x16x32_bf16 v[56:59], v[132:135], v[172:175], v[56:59]\n"
        "buffer_load_dwordx4 v8, s[36:39], s94 offen lds\n"
        "v_cndmask_b32_e64 v252, v252, v144, s[58:59]\n"
        "v_mfma_f32_16x16x32_bf16 v[56:59], v[138:141], v[176:179], v[56:59]\n"
        "v_cndmask_b32_dpp v255, v184, v80, vcc row_ror:4 row_mask:0xf bank_mask:0xf\n"
        "v_cndmask_b32_e64 v252, v252, v145, s[60:61]\n"
        "v_mfma_f32_16x16x32_bf16 v[88:91], a[8:11], v[118:121], 0\n"
        "v_cndmask_b32_dpp v254, v253, v254, vcc row_ror:4 row_mask:0xf bank_mask:0xf\n"
        "v_cndmask_b32_dpp v255, v255, v122, vcc row_ror:4 row_mask:0xf bank_mask:0xf\n"
        "v_mfma_f32_16x16x32_bf16 v[88:91], a[4:7], v[112:115], v[88:91]\n"
        "v_cndmask_b32_dpp v252, v254, v252, vcc row_ror:4 row_mask:0xf bank_mask:0xf\n"
        "v_mul_f32 v253, s80, v255\n"
        "v_mfma_f32_16x16x32_bf16 v[88:91], a[0:3], v[106:109], v[88:91]\n"
        "v_fma_f32 v253, -v253, v255, v252\n"
        "v_max_f32 v253, 0, v253\n"
        "v_mfma_f32_16x16x32_bf16 v[34:37], a[12:15], v[106:109], 0\n"
        "s_nop 0\n"
        "v_fmaak_f32 v253, v28, v253, 0x3727c5ac\n"
        "v_cndmask_b32_dpp v254, v185, v81, vcc row_ror:4 row_mask:0xf bank_mask:0xf\n"
        "v_mfma_f32_16x16x32_bf16 v[34:37], a[16:19], v[112:115], v[34:37]\n"
        "s_nop 0\n"
        "v_cvt_pk_bf16_f32 v142, v88, v89\n"
        "v_cvt_pk_bf16_f32 v143, v90, v91\n"
        "v_mfma_f32_16x16x32_bf16 v[34:37], a[20:23], v[118:121], v[34:37]\n"
        "s_waitcnt lgkmcnt(0)\n"
        "v_rsq_f32 v249, v253\n"
        "v_mfma_f32_16x16x32_bf16 v[80:83], a[32:35], v[118:121], 0\n"
        "s_nop 0\n"
        "v_cndmask_b32_dpp v254, v254, v123, vcc row_ror:4 row_mask:0xf bank_mask:0xf\n"
        "s_nop 0\n"
        "v_mfma_f32_16x16x32_bf16 v[80:83], a[28:31], v[112:115], v[80:83]\n"
        "s_nop 0\n"
        "v_cvt_pk_bf16_f32 v144, v34, v35\n"
        "v_cvt_pk_bf16_f32 v145, v36, v37\n"
        "v_mfma_f32_16x16x32_bf16 v[80:83], a[24:27], v[106:109], v[80:83]\n"
        "s_nop 1\n"
        "v_mfma_f32_16x16x32_bf16 v[88:91], a[36:39], v[106:109], 0\n"
        "s_nop 2\n"
        "v_mfma_f32_16x16x32_bf16 v[88:91], a[40:43], v[112:115], v[88:91]\n"
        "s_nop 0\n"
        "v_cvt_pk_bf16_f32 v34, v80, v81\n"
        "v_cvt_pk_bf16_f32 v35, v82, v83\n"
        "v_mfma_f32_16x16x32_bf16 v[88:91], a[44:47], v[118:121], v[88:91]\n"
        "s_nop 0\n"
        "v_mfma_f32_16x16x32_bf16 v[80:83], a[56:59], v[118:121], 0\n"
        "s_nop 0\n"
        "ds_read_b128 v[188:191], v13\n"
        "ds_read_b128 v[184:187], v13 offset:1024\n"
        "v_mfma_f32_16x16x32_bf16 v[80:83], a[52:55], v[112:115], v[80:83]\n"
        "s_nop 0\n"
        "ds_read_b128 v[196:199], v13 offset:2048\n"
        "v_cvt_pk_bf16_f32 v36, v88, v89\n"
        "v_mfma_f32_16x16x32_bf16 v[80:83], a[48:51], v[106:109], v[80:83]\n"
        "v_mov_b64 v[200:201], 0\n"
        "s_mov_b64 exec, 1\n"
        "buffer_atomic_add v200, off, s[16:19], 0 sc0\n"
        "buffer_atomic_add v201, off, s[16:19], s15 offset:4 sc0\n"
        "s_mov_b64 exec, -1\n"
        "v_mfma_f32_16x16x32_bf16 v[212:215], a[60:63], v[106:109], 0\n"
        "v_cvt_pk_bf16_f32 v37, v90, v91\n"
        "v_mfma_f32_16x16x32_bf16 v[212:215], a[64:67], v[112:115], v[212:215]\n"
        "v_cvt_pk_bf16_f32 v204, v80, v81\n"
        "v_cvt_pk_bf16_f32 v205, v82, v83\n"
        "v_mfma_f32_16x16x32_bf16 v[212:215], a[68:71], v[118:121], v[212:215]\n"
        "v_mfma_f32_16x16x32_bf16 v[208:211], v[158:161], a[80:83], 0\n"
        "v_mfma_f32_16x16x32_bf16 v[208:211], v[152:155], a[76:79], v[208:211]\n"
        "ds_read_b64_tr_b16 v[122:123], v165\n"
        "ds_read_b64_tr_b16 v[124:125], v165 offset:3072\n"
        "v_mfma_f32_16x16x32_bf16 v[208:211], v[146:149], a[72:75], v[208:211]\n"
        "s_waitcnt lgkmcnt(2)\n"
        "ds_read_b64_tr_b16 v[88:89], v165 offset:6144\n"
        "ds_read_b64_tr_b16 v[90:91], v165 offset:9216\n"
        "v_mfma_f32_16x16x32_bf16 v[216:219], v[146:149], a[84:87], 0\n"
        "ds_read_b128 v[192:195], v13 offset:3072\n"
        "ds_read_b128 v[172:175], v13 offset:4096\n"
        "v_mfma_f32_16x16x32_bf16 v[216:219], v[152:155], a[88:91], v[216:219]\n"
        "ds_read_b128 v[180:183], v13 offset:5120\n"
        "ds_read_b128 v[176:179], v20\n"
        "v_mfma_f32_16x16x32_bf16 v[216:219], v[158:161], a[92:95], v[216:219]\n"
        "ds_read_b64 v[202:203], v21\n"
        "ds_read_b64_tr_b16 v[80:81], v165 offset:12288\n"
        "v_mfma_f32_16x16x32_bf16 v[224:227], v[106:109], v[142:145], 0\n"
        "ds_read_b64_tr_b16 v[82:83], v165 offset:15360\n"
        "v_cvt_pk_bf16_f32 v206, v212, v213\n"
        "v_mfma_f32_16x16x32_bf16 v[224:227], v[112:115], v[34:37], v[224:227]\n"
        "v_cvt_pk_bf16_f32 v207, v214, v215\n"
        "v_cvt_pk_bf16_f32 v30, v208, v209\n"
        "s_nop 0\n"
        "v_mfma_f32_16x16x32_bf16 v[224:227], v[118:121], v[204:207], v[224:227]\n"
        "s_nop 0\n"
        "v_cvt_pk_bf16_f32 v31, v210, v211\n"
        "v_cvt_pk_bf16_f32 v34, v216, v217\n"
        "v_mfma_f32_16x16x32_bf16 v[220:223], a[152:155], v[196:199], 0\n"
        "s_nop 0\n"
        "v_cvt_pk_bf16_f32 v35, v218, v219\n"
        "v_mfma_f32_16x16x32_bf16 v[220:223], a[148:151], v[184:187], v[220:223]\n"
        "s_nop 0\n"
        "v_cndmask_b32_dpp v251, v224, v56, vcc row_ror:4 row_mask:0xf bank_mask:0xf\n"
        "v_cndmask_b32_dpp v252, v225, v57, vcc row_ror:4 row_mask:0xf bank_mask:0xf\n"
        "v_mfma_f32_16x16x32_bf16 v[220:223], a[144:147], v[188:191], v[220:223]\n"
        "v_cndmask_b32_dpp v253, v226, v58, vcc row_ror:4 row_mask:0xf bank_mask:0xf\n"
        "v_cndmask_b32_dpp v250, v227, v59, vcc row_ror:4 row_mask:0xf bank_mask:0xf\n"
        "v_mfma_f32_16x16x32_bf16 v[208:211], a[156:159], v[188:191], 0\n"
        "v_cndmask_b32_dpp v251, v251, v40, vcc row_ror:4 row_mask:0xf bank_mask:0xf\n"
        "s_waitcnt lgkmcnt(2)\n"
        "v_mfma_f32_16x16x32_bf16 v[208:211], a[160:163], v[184:187], v[208:211]\n"
        "s_barrier\n"
        "ds_read_b64_tr_b16 v[212:213], v165 offset:8\n"
        "ds_read_b64_tr_b16 v[214:215], v165 offset:3080\n"
        "v_mfma_f32_16x16x32_bf16 v[208:211], a[164:167], v[196:199], v[208:211]\n"
        "s_waitcnt lgkmcnt(2)\n"
        "ds_read_b64_tr_b16 v[56:57], v165 offset:6152\n"
        "ds_read_b64_tr_b16 v[58:59], v165 offset:9224\n"
        "v_mfma_f32_16x16x32_bf16 v[216:219], v[80:83], v[196:199], 0\n"
        "ds_read_b64_tr_b16 v[142:143], v165 offset:12296\n"
        "ds_read_b64_tr_b16 v[144:145], v165 offset:15368\n"
        "v_mfma_f32_16x16x32_bf16 v[216:219], v[88:91], v[184:187], v[216:219]\n"
        "v_cvt_pk_bf16_f32 v204, v220, v221\n"
        "v_cvt_pk_bf16_f32 v205, v222, v223\n"
        "v_mfma_f32_16x16x32_bf16 v[216:219], v[122:125], v[188:191], v[216:219]\n"
        "s_nop 0\n"
        "v_cvt_pk_bf16_f32 v206, v208, v209\n"
        "v_cvt_pk_bf16_f32 v207, v210, v211\n"
        "v_mfma_f32_16x16x32_bf16 v[208:211], v[122:125], v[192:195], 0\n"
        "s_nop 0\n"
        "ds_write_b128 v166, v[204:207]\n"
        "v_mfma_f32_16x16x32_bf16 v[208:211], v[88:91], v[172:175], v[208:211]\n"
        "s_nop 0\n"
        "v_cvt_pk_bf16_f32 v232, v216, v217\n"
        "v_cvt_pk_bf16_f32 v233, v218, v219\n"
        "v_mfma_f32_16x16x32_bf16 v[208:211], v[80:83], v[180:183], v[208:211]\n"
        "v_cndmask_b32_dpp v252, v252, v41, vcc row_ror:4 row_mask:0xf bank_mask:0xf\n"
        "v_cndmask_b32_dpp v253, v253, v42, vcc row_ror:4 row_mask:0xf bank_mask:0xf\n"
        "v_mfma_f32_16x16x32_bf16 v[216:219], a[176:179], v[196:199], 0\n"
        "s_nop 0\n"
        "v_cndmask_b32_dpp v250, v250, v43, vcc row_ror:4 row_mask:0xf bank_mask:0xf\n"
        "v_mul_f32_dpp v251, v249, v251 quad_perm:[0,0,0,0] row_mask:0xf bank_mask:0xf\n"
        "v_mfma_f32_16x16x32_bf16 v[216:219], a[172:175], v[184:187], v[216:219]\n"
        "s_waitcnt lgkmcnt(1)\n"
        "v_cvt_pk_bf16_f32 v122, v208, v209\n"
        "v_cvt_pk_bf16_f32 v123, v210, v211\n"
        "v_mfma_f32_16x16x32_bf16 v[216:219], a[168:171], v[188:191], v[216:219]\n"
        "v_mul_f32_dpp v252, v249, v252 quad_perm:[1,1,1,1] row_mask:0xf bank_mask:0xf\n"
        "v_mul_f32_dpp v253, v249, v253 quad_perm:[2,2,2,2] row_mask:0xf bank_mask:0xf\n"
        "v_mfma_f32_16x16x32_bf16 v[88:91], a[180:183], v[188:191], 0\n"
        "v_mul_f32_dpp v250, v249, v250 quad_perm:[3,3,3,3] row_mask:0xf bank_mask:0xf\n"
        "v_max3_f32 v248, v251, v252, v253\n"
        "v_mfma_f32_16x16x32_bf16 v[88:91], a[184:187], v[184:187], v[88:91]\n"
        "ds_read_b64_tr_b16 v[40:41], v165 offset:1024\n"
        "v_max_f32 v248, v248, v250\n"
        "v_mfma_f32_16x16x32_bf16 v[88:91], a[188:191], v[196:199], v[88:91]\n"
        "ds_read_b64_tr_b16 v[42:43], v165 offset:4096\n"
        "v_mul_f32 v248, v248, v249\n"
        "v_mfma_f32_16x16x32_bf16 v[220:223], v[142:145], v[196:199], 0\n"
        "v_fma_f32 v251, v251, v249, -v248\n"
        "v_fma_f32 v252, v252, v249, -v248\n"
        "v_mfma_f32_16x16x32_bf16 v[220:223], v[56:59], v[184:187], v[220:223]\n"
        "ds_read_b64_tr_b16 v[80:81], v165 offset:7168\n"
        "v_fma_f32 v253, v253, v249, -v248\n"
        "v_mfma_f32_16x16x32_bf16 v[220:223], v[212:215], v[188:191], v[220:223]\n"
        "ds_read_b64_tr_b16 v[82:83], v165 offset:10240\n"
        "v_fma_f32 v250, v250, v249, -v248\n"
        "v_mfma_f32_16x16x32_bf16 v[208:211], v[212:215], v[192:195], 0\n"
        "ds_read_b64_tr_b16 v[234:235], v165 offset:13312\n"
        "v_cvt_pk_bf16_f32 v212, v216, v217\n"
        "v_mfma_f32_16x16x32_bf16 v[208:211], v[56:59], v[172:175], v[208:211]\n"
        "ds_read_b64_tr_b16 v[236:237], v165 offset:16384\n"
        "v_cvt_pk_bf16_f32 v213, v218, v219\n"
        "v_mfma_f32_16x16x32_bf16 v[208:211], v[142:145], v[180:183], v[208:211]\n"
        "v_exp_f32 v251, v251\n"
        "v_mfma_f32_16x16x32_bf16 v[224:227], a[200:203], v[196:199], 0\n"
        "v_exp_f32 v252, v252\n"
        "v_mfma_f32_16x16x32_bf16 v[224:227], a[196:199], v[184:187], v[224:227]\n"
        "v_exp_f32 v253, v253\n"
        "v_mfma_f32_16x16x32_bf16 v[224:227], a[192:195], v[188:191], v[224:227]\n"
        "v_exp_f32 v250, v250\n"
        "v_mfma_f32_16x16x32_bf16 v[216:219], a[204:207], v[188:191], 0\n"
        "v_add_f32 v248, v251, v252\n"
        "v_add_f32 v247, v253, v250\n"
        "v_mfma_f32_16x16x32_bf16 v[216:219], a[208:211], v[184:187], v[216:219]\n"
        "v_add_f32 v248, v248, v247\n"
        "v_cvt_pk_bf16_f32 v214, v88, v89\n"
        "v_mfma_f32_16x16x32_bf16 v[216:219], a[212:215], v[196:199], v[216:219]\n"
        "v_rcp_f32 v248, v248\n"
        "s_waitcnt lgkmcnt(0)\n"
        "v_mfma_f32_16x16x32_bf16 v[56:59], v[234:237], v[196:199], 0\n"
        "v_mul_f32 v251, v251, v248\n"
        "v_mul_f32 v252, v252, v248\n"
        "v_mfma_f32_16x16x32_bf16 v[56:59], v[80:83], v[184:187], v[56:59]\n"
        "ds_read_b64_tr_b16 v[240:241], v165 offset:1032\n"
        "v_mul_f32 v253, v253, v248\n"
        "v_mfma_f32_16x16x32_bf16 v[56:59], v[40:43], v[188:191], v[56:59]\n"
        "ds_read_b64_tr_b16 v[242:243], v165 offset:4104\n"
        "v_mul_f32 v250, v250, v248\n"
        "v_mfma_f32_16x16x32_bf16 v[228:231], v[40:43], v[192:195], 0\n"
        "ds_read_b64_tr_b16 v[40:41], v165 offset:7176\n"
        "v_mul_f32_dpp v251, v249, v251 quad_perm:[0,0,0,0] row_mask:0xf bank_mask:0xf\n"
        "v_mfma_f32_16x16x32_bf16 v[228:231], v[80:83], v[172:175], v[228:231]\n"
        "ds_read_b64_tr_b16 v[42:43], v165 offset:10248\n"
        "v_mul_f32_dpp v252, v249, v252 quad_perm:[1,1,1,1] row_mask:0xf bank_mask:0xf\n"
        "v_mfma_f32_16x16x32_bf16 v[228:231], v[234:237], v[180:183], v[228:231]\n"
        "ds_read_b64_tr_b16 v[80:81], v165 offset:13320\n"
        "v_mul_f32_dpp v253, v249, v253 quad_perm:[2,2,2,2] row_mask:0xf bank_mask:0xf\n"
        "v_mfma_f32_16x16x32_bf16 v[244:247], v[158:161], a[104:107], 0\n"
        "ds_read_b64_tr_b16 v[82:83], v165 offset:16392\n"
        "v_mul_f32_dpp v250, v249, v250 quad_perm:[3,3,3,3] row_mask:0xf bank_mask:0xf\n"
        "v_mfma_f32_16x16x32_bf16 v[244:247], v[152:155], a[100:103], v[244:247]\n"
        "v_cvt_pk_bf16_f32 v164, v251, v252\n"
        "v_cvt_pk_bf16_f32 v203, v253, v250\n"
        "v_mfma_f32_16x16x32_bf16 v[244:247], v[146:149], a[96:99], v[244:247]\n"
        "v_and_b32 v144, v25, v164\n"
        "v_and_b32 v145, v25, v203\n"
        "v_mfma_f32_16x16x32_bf16 v[236:239], v[146:149], a[108:111], 0\n"
        "v_mov_b64 v[150:151], v[144:145]\n"
        "v_cvt_pk_bf16_f32 v215, v90, v91\n"
        "v_mfma_f32_16x16x32_bf16 v[236:239], v[152:155], a[112:115], v[236:239]\n"
        "ds_write_b128 v166, v[212:215] offset:64\n"
        "v_mfma_f32_16x16x32_bf16 v[236:239], v[158:161], a[116:119], v[236:239]\n"
        "v_cvt_pk_bf16_f32 v234, v220, v221\n"
        "v_cvt_pk_bf16_f32 v235, v222, v223\n"
        "v_mfma_f32_16x16x32_bf16 v[220:223], v[30:33], v[144:147], 0\n"
        "ds_write_b128 v166, v[232:235] offset:192\n"
        "v_mfma_f32_16x16x32_bf16 v[88:91], v[32:35], v[148:151], 0\n"
        "s_waitcnt lgkmcnt(2)\n"
        "v_cvt_pk_bf16_f32 v124, v208, v209\n"
        "v_cvt_pk_bf16_f32 v125, v210, v211\n"
        "v_mfma_f32_16x16x32_bf16 v[34:37], v[80:83], v[196:199], 0\n"
        "ds_write_b128 v167, v[122:125] offset:192\n"
        "v_mfma_f32_16x16x32_bf16 v[34:37], v[40:43], v[184:187], v[34:37]\n"
        "ds_read_b64_tr_b16 v[122:123], v165 offset:2048\n"
        "v_cvt_pk_bf16_f32 v208, v224, v225\n"
        "v_mfma_f32_16x16x32_bf16 v[34:37], v[240:243], v[188:191], v[34:37]\n"
        "ds_read_b64_tr_b16 v[124:125], v165 offset:5120\n"
        "v_cvt_pk_bf16_f32 v209, v226, v227\n"
        "v_mfma_f32_16x16x32_bf16 v[232:235], v[240:243], v[192:195], 0\n"
        "ds_read_b64_tr_b16 v[224:225], v165 offset:8192\n"
        "v_cvt_pk_bf16_f32 v210, v216, v217\n"
        "v_mfma_f32_16x16x32_bf16 v[232:235], v[40:43], v[172:175], v[232:235]\n"
        "ds_read_b64_tr_b16 v[226:227], v165 offset:11264\n"
        "v_cvt_pk_bf16_f32 v211, v218, v219\n"
        "v_mfma_f32_16x16x32_bf16 v[232:235], v[80:83], v[180:183], v[232:235]\n"
        "ds_write_b128 v166, v[208:211] offset:128\n"
        "v_mfma_f32_16x16x32_bf16 v[216:219], v[158:161], a[128:131], 0\n"
        "ds_read_b64_tr_b16 v[240:241], v165 offset:14336\n"
        "v_mov_b64 v[156:157], v[144:145]\n"
        "v_mfma_f32_16x16x32_bf16 v[216:219], v[152:155], a[124:127], v[216:219]\n"
        "ds_read_b64_tr_b16 v[242:243], v165 offset:17408\n"
        "v_cvt_pk_bf16_f32 v80, v56, v57\n"
        "v_mfma_f32_16x16x32_bf16 v[216:219], v[146:149], a[120:123], v[216:219]\n"
        "v_cvt_pk_bf16_f32 v81, v58, v59\n"
        "v_cvt_pk_bf16_f32 v40, v228, v229\n"
        "v_mfma_f32_16x16x32_bf16 v[56:59], v[146:149], a[132:135], 0\n"
        "v_cvt_pk_bf16_f32 v41, v230, v231\n"
        "v_cvt_pk_bf16_f32 v42, v244, v245\n"
        "v_mfma_f32_16x16x32_bf16 v[56:59], v[152:155], a[136:139], v[56:59]\n"
        "v_cvt_pk_bf16_f32 v43, v246, v247\n"
        "v_cvt_pk_bf16_f32 v46, v236, v237\n"
        "v_mfma_f32_16x16x32_bf16 v[56:59], v[158:161], a[140:143], v[56:59]\n"
        "v_cvt_pk_bf16_f32 v47, v238, v239\n"
        "v_mov_b64 v[162:163], v[144:145]\n"
        "v_mfma_f32_16x16x32_bf16 v[142:145], v[42:45], v[150:153], 0\n"
        "v_cvt_pk_bf16_f32 v82, v34, v35\n"
        "v_cvt_pk_bf16_f32 v83, v36, v37\n"
        "v_mfma_f32_16x16x32_bf16 v[236:239], v[44:47], v[154:157], 0\n"
        "ds_write_b128 v166, v[80:83] offset:256\n"
        "s_waitcnt lgkmcnt(1)\n"
        "v_mfma_f32_16x16x32_bf16 v[228:231], v[240:243], v[196:199], 0\n"
        "ds_read_b64_tr_b16 v[80:81], v165 offset:2056\n"
        "v_cvt_pk_bf16_f32 v42, v232, v233\n"
        "v_mfma_f32_16x16x32_bf16 v[228:231], v[224:227], v[184:187], v[228:231]\n"
        "ds_read_b64_tr_b16 v[82:83], v165 offset:5128\n"
        "v_cvt_pk_bf16_f32 v43, v234, v235\n"
        "v_mfma_f32_16x16x32_bf16 v[228:231], v[122:125], v[188:191], v[228:231]\n"
        "ds_write_b128 v167, v[40:43] offset:256\n"
        "v_mfma_f32_16x16x32_bf16 v[244:247], v[122:125], v[192:195], 0\n"
        "ds_read_b64_tr_b16 v[232:233], v165 offset:8200\n"
        "v_cvt_pk_bf16_f32 v36, v216, v217\n"
        "v_mfma_f32_16x16x32_bf16 v[244:247], v[224:227], v[172:175], v[244:247]\n"
        "ds_read_b64_tr_b16 v[234:235], v165 offset:11272\n"
        "v_cvt_pk_bf16_f32 v37, v218, v219\n"
        "v_mfma_f32_16x16x32_bf16 v[244:247], v[240:243], v[180:183], v[244:247]\n"
        "ds_read_b64_tr_b16 v[152:153], v165 offset:14344\n"
        "v_cvt_pk_bf16_f32 v40, v56, v57\n"
        "v_mfma_f32_16x16x32_bf16 v[216:219], v[138:141], a[80:83], 0\n"
        "ds_read_b64_tr_b16 v[154:155], v165 offset:17416\n"
        "v_cvt_pk_bf16_f32 v41, v58, v59\n"
        "v_mfma_f32_16x16x32_bf16 v[216:219], v[132:135], a[76:79], v[216:219]\n"
        "v_cvt_pk_bf16_f32 v56, v220, v221\n"
        "v_cvt_pk_bf16_f32 v57, v90, v91\n"
        "v_mfma_f32_16x16x32_bf16 v[216:219], v[126:129], a[72:75], v[216:219]\n"
        "v_cvt_pk_bf16_f32 v58, v236, v237\n"
        "v_cvt_pk_bf16_f32 v148, v222, v223\n"
        "v_mfma_f32_16x16x32_bf16 v[220:223], v[126:129], a[84:87], 0\n"
        "v_cvt_pk_bf16_f32 v149, v142, v143\n"
        "v_cvt_pk_bf16_f32 v150, v238, v239\n"
        "v_mfma_f32_16x16x32_bf16 v[220:223], v[132:135], a[88:91], v[220:223]\n"
        "v_cvt_pk_bf16_f32 v142, v228, v229\n"
        "v_cvt_pk_bf16_f32 v143, v230, v231\n"
        "v_mfma_f32_16x16x32_bf16 v[220:223], v[138:141], a[92:95], v[220:223]\n"
        "v_cvt_pk_bf16_f32 v228, v244, v245\n"
        "v_cvt_pk_bf16_f32 v229, v246, v247\n"
        "v_mfma_f32_16x16x32_bf16 v[224:227], v[36:39], v[156:159], 0\n"
        "v_and_b32_dpp v124, v164, v25 row_ror:12 row_mask:0xf bank_mask:0xf\n"
        "v_and_b32_dpp v125, v203, v25 row_ror:12 row_mask:0xf bank_mask:0xf\n"
        "v_mfma_f32_16x16x32_bf16 v[156:159], v[38:41], v[160:163], 0\n"
        "v_cvt_pk_bf16_f32 v30, v216, v217\n"
        "v_cvt_pk_bf16_f32 v160, v88, v89\n"
        "s_waitcnt lgkmcnt(0)\n"
        "v_mfma_f32_16x16x32_bf16 v[40:43], v[152:155], v[196:199], 0\n"
        "s_barrier\n"
        "v_cvt_pk_bf16_f32 v161, v144, v145\n"
        "v_cvt_pk_bf16_f32 v31, v218, v219\n"
        "v_mfma_f32_16x16x32_bf16 v[40:43], v[232:235], v[184:187], v[40:43]\n"
        "v_mov_b64 v[130:131], v[124:125]\n"
        "v_cvt_pk_bf16_f32 v34, v220, v221\n"
        "v_mfma_f32_16x16x32_bf16 v[40:43], v[80:83], v[188:191], v[40:43]\n"
        "v_cvt_pk_bf16_f32 v35, v222, v223\n"
        "v_cvt_pk_bf16_f32 v162, v224, v225\n"
        "v_mfma_f32_16x16x32_bf16 v[236:239], v[80:83], v[192:195], 0\n"
        "v_cvt_pk_bf16_f32 v59, v226, v227\n"
        "v_cvt_pk_bf16_f32 v151, v156, v157\n"
        "v_mfma_f32_16x16x32_bf16 v[236:239], v[232:235], v[172:175], v[236:239]\n"
        "ds_write_b128 v12, v[56:59]\n"
        "v_mfma_f32_16x16x32_bf16 v[236:239], v[152:155], v[180:183], v[236:239]\n"
        "ds_write_b128 v12, v[148:151] offset:1024\n"
        "v_mfma_f32_16x16x32_bf16 v[216:219], v[138:141], a[104:107], 0\n"
        "v_cvt_pk_bf16_f32 v163, v158, v159\n"
        "v_cvt_pk_bf16_f32 v144, v40, v41\n"
        "v_mfma_f32_16x16x32_bf16 v[216:219], v[132:135], a[100:103], v[216:219]\n"
        "ds_write_b128 v12, v[160:163] offset:2048\n"
        "v_mfma_f32_16x16x32_bf16 v[216:219], v[126:129], a[96:99], v[216:219]\n"
        "ds_read_b128 v[88:91], v27 offset:64\n"
        "ds_read_b128 v[80:83], v27 offset:128\n"
        "v_mfma_f32_16x16x32_bf16 v[220:223], v[126:129], a[108:111], 0\n"
        "v_cvt_pk_bf16_f32 v145, v42, v43\n"
        "v_cvt_pk_bf16_f32 v230, v236, v237\n"
        "v_mfma_f32_16x16x32_bf16 v[220:223], v[132:135], a[112:115], v[220:223]\n"
        "ds_write_b128 v166, v[142:145] offset:320\n"
        "v_mfma_f32_16x16x32_bf16 v[220:223], v[138:141], a[116:119], v[220:223]\n"
        "v_cvt_pk_bf16_f32 v231, v238, v239\n"
        "v_cvt_pk_bf16_f32 v42, v216, v217\n"
        "v_mfma_f32_16x16x32_bf16 v[152:155], v[30:33], v[124:127], 0\n"
        "s_nop 0\n"
        "ds_write_b128 v167, v[228:231] offset:320\n"
        "v_mfma_f32_16x16x32_bf16 v[156:159], v[32:35], v[128:131], 0\n"
        "s_nop 0\n"
        "v_cvt_pk_bf16_f32 v43, v218, v219\n"
        "v_cvt_pk_bf16_f32 v46, v220, v221\n"
        "v_mfma_f32_16x16x32_bf16 v[34:37], v[188:191], v[204:207], 0\n"
        "s_waitcnt lgkmcnt(2)\n"
        "v_cvt_pk_bf16_f32 v47, v222, v223\n"
        "v_cvt_f32_f16_sdwa v247, v202 dst_sel:DWORD dst_unused:UNUSED_PAD src0_sel:WORD_0\n"
        "v_mfma_f32_16x16x32_bf16 v[34:37], v[184:187], v[212:215], v[34:37]\n"
        "v_lshrrev_b32 v31, 0, v202\n"
        "v_mov_b64 v[136:137], v[124:125]\n"
        "v_mfma_f32_16x16x32_bf16 v[34:37], v[196:199], v[208:211], v[34:37]\n"
        "v_cvt_f32_f16_sdwa v246, v176 dst_sel:DWORD dst_unused:UNUSED_PAD src0_sel:WORD_0\n"
        "v_cvt_f32_f16_sdwa v242, v176 dst_sel:DWORD dst_unused:UNUSED_PAD src0_sel:WORD_1\n"
        "v_mfma_f32_16x16x32_bf16 v[208:211], v[196:199], a[224:227], 0\n"
        "s_nop 0\n"
        "v_cvt_f32_f16_sdwa v249, v177 dst_sel:DWORD dst_unused:UNUSED_PAD src0_sel:WORD_0\n"
        "v_cvt_f32_f16_sdwa v248, v177 dst_sel:DWORD dst_unused:UNUSED_PAD src0_sel:WORD_1\n"
        "v_mfma_f32_16x16x32_bf16 v[208:211], v[184:187], a[220:223], v[208:211]\n"
        "s_nop 0\n"
        "v_cndmask_b32_e64 v30, v34, v35, s[56:57]\n"
        "v_cndmask_b32_e64 v30, v30, v36, s[58:59]\n"
        "v_mfma_f32_16x16x32_bf16 v[208:211], v[188:191], a[216:219], v[208:211]\n"
        "v_cndmask_b32_e64 v30, v30, v37, s[60:61]\n"
        "v_mul_f32 v30, v30, v247\n"
        "v_mfma_f32_16x16x32_bf16 v[34:37], v[188:191], a[228:231], 0\n"
        "v_mul_f32 v30, v30, v247\n"
        "v_mov_b64 v[142:143], v[124:125]\n"
        "v_mfma_f32_16x16x32_bf16 v[34:37], v[184:187], a[232:235], v[34:37]\n"
        "v_exp_f32 v30, v30\n"
        "v_mfma_f32_16x16x32_bf16 v[34:37], v[196:199], a[236:239], v[34:37]\n"
        "s_nop 0\n"
        "ds_write_b64 v168, v[30:31]\n"
        "v_mfma_f32_16x16x32_bf16 v[122:125], v[188:191], v[88:91], 0\n"
        "s_nop 0\n"
        "v_mul_f32 v208, v208, v246\n"
        "v_mul_f32 v209, v209, v242\n"
        "v_mfma_f32_16x16x32_bf16 v[204:207], v[188:191], v[80:83], 0\n"
        "s_nop 0\n"
        "v_mul_f32 v34, v34, v246\n"
        "v_cvt_pk_f16_f32 v208, v208, v34\n"
        "v_mfma_f32_16x16x32_bf16 v[144:147], v[56:59], v[56:59], 0\n"
        "v_perm_b32 v30, v208, v122, s54\n"
        "v_mul_f32 v35, v35, v242\n"
        "v_mfma_f32_16x16x32_bf16 v[144:147], v[148:151], v[148:151], v[144:147]\n"
        "v_cvt_pk_f16_f32 v209, v209, v35\n"
        "v_perm_b32 v31, v208, v204, s55\n"
        "v_mfma_f32_16x16x32_bf16 v[144:147], v[160:163], v[160:163], v[144:147]\n"
        "ds_write_b64 v170, v[30:31]\n"
        "v_mfma_f32_16x16x32_bf16 v[148:151], v[138:141], a[128:131], 0\n"
        "v_perm_b32 v30, v209, v123, s54\n"
        "v_perm_b32 v31, v209, v205, s55\n"
        "v_mfma_f32_16x16x32_bf16 v[148:151], v[132:135], a[124:127], v[148:151]\n"
        "ds_write_b64 v170, v[30:31] offset:6336\n"
        "v_mfma_f32_16x16x32_bf16 v[148:151], v[126:129], a[120:123], v[148:151]\n"
        "v_mul_f32 v210, v210, v249\n"
        "v_mul_f32 v36, v36, v249\n"
        "v_mfma_f32_16x16x32_bf16 v[56:59], v[126:129], a[132:135], 0\n"
        "v_cvt_pk_f16_f32 v210, v210, v36\n"
        "v_perm_b32 v30, v210, v124, s54\n"
        "v_mfma_f32_16x16x32_bf16 v[56:59], v[132:135], a[136:139], v[56:59]\n"
        "v_perm_b32 v31, v210, v206, s55\n"
        "v_mul_f32 v211, v211, v248\n"
        "v_mfma_f32_16x16x32_bf16 v[56:59], v[138:141], a[140:143], v[56:59]\n"
        "ds_write_b64 v170, v[30:31] offset:12672\n"
        "v_mfma_f32_16x16x32_bf16 v[160:163], v[42:45], v[130:133], 0\n"
        "v_mul_f32 v37, v37, v248\n"
        "v_cvt_pk_f16_f32 v211, v211, v37\n"
        "v_mfma_f32_16x16x32_bf16 v[130:133], v[44:47], v[134:137], 0\n"
        "v_perm_b32 v30, v211, v125, s54\n"
        "v_perm_b32 v31, v211, v207, s55\n"
        "v_mfma_f32_16x16x32_bf16 v[204:207], a[152:155], v[180:183], 0\n"
        "ds_write_b64 v170, v[30:31] offset:19008\n"
        "v_mfma_f32_16x16x32_bf16 v[204:207], a[148:151], v[172:175], v[204:207]\n"
        "v_cvt_pk_bf16_f32 v36, v148, v149\n"
        "v_cvt_pk_bf16_f32 v37, v150, v151\n"
        "v_mfma_f32_16x16x32_bf16 v[204:207], a[144:147], v[192:195], v[204:207]\n"
        "v_cvt_pk_bf16_f32 v40, v56, v57\n"
        "v_cvt_pk_bf16_f32 v41, v58, v59\n"
        "v_mfma_f32_16x16x32_bf16 v[122:125], a[156:159], v[192:195], 0\n"
        "s_nop 0\n"
        "v_cvt_pk_bf16_f32 v126, v152, v153\n"
        "v_cvt_pk_bf16_f32 v127, v158, v159\n"
        "v_mfma_f32_16x16x32_bf16 v[122:125], a[160:163], v[172:175], v[122:125]\n"
        "s_nop 0\n"
        "v_cvt_pk_bf16_f32 v56, v204, v205\n"
        "v_cvt_pk_bf16_f32 v57, v206, v207\n"
        "v_mfma_f32_16x16x32_bf16 v[122:125], a[164:167], v[180:183], v[122:125]\n"
        "v_cvt_pk_bf16_f32 v128, v130, v131\n"
        "v_cvt_pk_bf16_f32 v148, v154, v155\n"
        "v_mfma_f32_16x16x32_bf16 v[152:155], v[118:121], a[80:83], 0\n"
        "s_nop 0\n"
        "v_cvt_pk_bf16_f32 v149, v160, v161\n"
        "v_cvt_pk_bf16_f32 v150, v132, v133\n"
        "v_mfma_f32_16x16x32_bf16 v[152:155], v[112:115], a[76:79], v[152:155]\n"
        "s_nop 0\n"
        "v_cvt_pk_bf16_f32 v58, v122, v123\n"
        "v_cvt_pk_bf16_f32 v59, v124, v125\n"
        "v_mfma_f32_16x16x32_bf16 v[152:155], v[106:109], a[72:75], v[152:155]\n"
        "v_cvt_pk_bf16_f32 v130, v156, v157\n"
        "v_cvt_pk_bf16_f32 v131, v162, v163\n"
        "v_mfma_f32_16x16x32_bf16 v[122:125], v[106:109], a[84:87], 0\n"
        "s_nop 0\n"
        "v_and_b32_dpp v104, v164, v25 row_ror:8 row_mask:0xf bank_mask:0xf\n"
        "v_and_b32_dpp v105, v203, v25 row_ror:8 row_mask:0xf bank_mask:0xf\n"
        "v_mfma_f32_16x16x32_bf16 v[122:125], v[112:115], a[88:91], v[122:125]\n"
        "s_nop 0\n"
        "v_cvt_pk_bf16_f32 v30, v152, v153\n"
        "v_cvt_pk_bf16_f32 v31, v154, v155\n"
        "v_mfma_f32_16x16x32_bf16 v[122:125], v[118:121], a[92:95], v[122:125]\n"
        "s_nop 0\n"
        "v_mov_b64 v[110:111], v[104:105]\n"
        "v_mov_b64 v[116:117], v[104:105]\n"
        "v_mfma_f32_16x16x32_bf16 v[156:159], v[36:39], v[136:139], 0\n"
        "s_nop 0\n"
        "ds_write_b128 v167, v[56:59]\n"
        "v_mfma_f32_16x16x32_bf16 v[152:155], v[38:41], v[140:143], 0\n"
        "s_nop 0\n"
        "v_cvt_pk_bf16_f32 v34, v122, v123\n"
        "v_cvt_pk_bf16_f32 v35, v124, v125\n"
        "v_mfma_f32_16x16x32_bf16 v[136:139], v[196:199], a[248:251], 0\n"
        "s_nop 0\n"
        "v_cvt_pk_bf16_f32 v129, v158, v159\n"
        "v_cvt_pk_bf16_f32 v132, v156, v157\n"
        "v_mfma_f32_16x16x32_bf16 v[136:139], v[184:187], a[244:247], v[136:139]\n"
        "ds_write_b128 v12, v[126:129] offset:3072\n"
        "v_mfma_f32_16x16x32_bf16 v[136:139], v[188:191], a[240:243], v[136:139]\n"
        "v_cvt_pk_bf16_f32 v151, v152, v153\n"
        "v_cvt_pk_bf16_f32 v133, v154, v155\n"
        "v_mfma_f32_16x16x32_bf16 v[122:125], v[188:191], a[252:255], 0\n"
        "ds_write_b128 v12, v[148:151] offset:4096\n"
        "v_mfma_f32_16x16x32_bf16 v[122:125], v[184:187], v[92:95], v[122:125]\n"
        "ds_write_b128 v12, v[130:133] offset:5120\n"
        "v_mfma_f32_16x16x32_bf16 v[122:125], v[196:199], v[52:55], v[122:125]\n"
        "s_nop 0\n"
        "v_mul_f32 v136, v136, v246\n"
        "v_mul_f32 v137, v137, v242\n"
        "v_mfma_f32_16x16x32_bf16 v[158:161], v[184:187], v[88:91], 0\n"
        "v_mul_f32 v138, v138, v249\n"
        "v_mul_f32 v139, v139, v248\n"
        "v_mfma_f32_16x16x32_bf16 v[140:143], v[184:187], v[80:83], 0\n"
        "s_nop 0\n"
        "v_mul_f32 v122, v122, v246\n"
        "v_cvt_pk_f16_f32 v136, v136, v122\n"
        "v_mfma_f32_16x16x32_bf16 v[152:155], v[118:121], a[104:107], 0\n"
        "s_nop 0\n"
        "v_perm_b32 v36, v136, v158, s54\n"
        "v_mul_f32 v123, v123, v242\n"
        "v_mfma_f32_16x16x32_bf16 v[152:155], v[112:115], a[100:103], v[152:155]\n"
        "v_perm_b32 v37, v136, v140, s55\n"
        "v_cvt_pk_f16_f32 v137, v137, v123\n"
        "v_mfma_f32_16x16x32_bf16 v[152:155], v[106:109], a[96:99], v[152:155]\n"
        "ds_write_b64 v170, v[36:37] offset:8\n"
        "v_mfma_f32_16x16x32_bf16 v[162:165], v[106:109], a[108:111], 0\n"
        "v_perm_b32 v36, v137, v159, s54\n"
        "v_perm_b32 v37, v137, v141, s55\n"
        "v_mfma_f32_16x16x32_bf16 v[162:165], v[112:115], a[112:115], v[162:165]\n"
        "ds_write_b64 v170, v[36:37] offset:6344\n"
        "v_mfma_f32_16x16x32_bf16 v[162:165], v[118:121], a[116:119], v[162:165]\n"
        "v_mul_f32 v124, v124, v249\n"
        "v_cvt_pk_f16_f32 v138, v138, v124\n"
        "v_mfma_f32_16x16x32_bf16 v[134:137], v[30:33], v[104:107], 0\n"
        "v_cvt_pk_bf16_f32 v42, v152, v153\n"
        "v_perm_b32 v30, v138, v160, s54\n"
        "v_mfma_f32_16x16x32_bf16 v[156:159], v[32:35], v[108:111], 0\n"
        "v_perm_b32 v31, v138, v142, s55\n"
        "v_cvt_pk_bf16_f32 v43, v154, v155\n"
        "v_mfma_f32_16x16x32_bf16 v[34:37], a[176:179], v[180:183], 0\n"
        "ds_write_b64 v170, v[30:31] offset:12680\n"
        "v_mfma_f32_16x16x32_bf16 v[34:37], a[172:175], v[172:175], v[34:37]\n"
        "v_cvt_pk_bf16_f32 v46, v162, v163\n"
        "v_cvt_pk_bf16_f32 v47, v164, v165\n"
        "v_mfma_f32_16x16x32_bf16 v[34:37], a[168:171], v[192:195], v[34:37]\n"
        "v_mul_f32 v125, v125, v248\n"
        "v_cvt_pk_f16_f32 v139, v139, v125\n"
        "v_mfma_f32_16x16x32_bf16 v[152:155], a[180:183], v[192:195], 0\n"
        "v_perm_b32 v30, v139, v161, s54\n"
        "v_perm_b32 v31, v139, v143, s55\n"
        "v_mfma_f32_16x16x32_bf16 v[152:155], a[184:187], v[172:175], v[152:155]\n"
        "ds_write_b64 v170, v[30:31] offset:19016\n"
        "v_mfma_f32_16x16x32_bf16 v[152:155], a[188:191], v[180:183], v[152:155]\n"
        "v_cvt_pk_bf16_f32 v124, v34, v35\n"
        "v_cvt_pk_bf16_f32 v125, v36, v37\n"
        "v_mfma_f32_16x16x32_bf16 v[138:141], v[126:129], v[126:129], 0\n"
        "s_nop 0\n"
        "v_mov_b64 v[122:123], v[104:105]\n"
        "v_cvt_pk_bf16_f32 v104, v134, v135\n"
        "v_mfma_f32_16x16x32_bf16 v[138:141], v[148:151], v[148:151], v[138:141]\n"
        "s_nop 0\n"
        "v_cvt_pk_bf16_f32 v126, v152, v153\n"
        "v_cvt_pk_bf16_f32 v127, v154, v155\n"
        "v_mfma_f32_16x16x32_bf16 v[138:141], v[130:133], v[130:133], v[138:141]\n"
        "ds_write_b128 v167, v[124:127] offset:64\n"
        "v_mfma_f32_16x16x32_bf16 v[128:131], v[118:121], a[128:131], 0\n"
        "v_cvt_pk_bf16_f32 v105, v158, v159\n"
        "v_cvt_pk_bf16_f32 v132, v136, v137\n"
        "v_mfma_f32_16x16x32_bf16 v[128:131], v[112:115], a[124:127], v[128:131]\n"
        "v_cvt_pk_bf16_f32 v34, v156, v157\n"
        "v_cvt_f32_f16_sdwa v243, v178 dst_sel:DWORD dst_unused:UNUSED_PAD src0_sel:WORD_0\n"
        "v_mfma_f32_16x16x32_bf16 v[128:131], v[106:109], a[120:123], v[128:131]\n"
        "v_cvt_f32_f16_sdwa v244, v178 dst_sel:DWORD dst_unused:UNUSED_PAD src0_sel:WORD_1\n"
        "v_cvt_f32_f16_sdwa v247, v179 dst_sel:DWORD dst_unused:UNUSED_PAD src0_sel:WORD_0\n"
        "v_mfma_f32_16x16x32_bf16 v[134:137], v[106:109], a[132:135], 0\n"
        "s_nop 0\n"
        "v_cvt_f32_f16_sdwa v245, v202 dst_sel:DWORD dst_unused:UNUSED_PAD src0_sel:WORD_1\n"
        "v_lshrrev_b32 v31, 16, v202\n"
        "v_mfma_f32_16x16x32_bf16 v[134:137], v[112:115], a[136:139], v[134:137]\n"
        "s_nop 0\n"
        "v_cvt_pk_bf16_f32 v36, v128, v129\n"
        "v_cvt_pk_bf16_f32 v37, v130, v131\n"
        "v_mfma_f32_16x16x32_bf16 v[134:137], v[118:121], a[140:143], v[134:137]\n"
        "v_mul_f32_dpp v241, v254, v251 quad_perm:[0,0,0,0] row_mask:0xf bank_mask:0xf\n"
        "v_cndmask_b32_e64 v251, v138, v139, s[56:57]\n"
        "v_mfma_f32_16x16x32_bf16 v[148:151], v[42:45], v[110:113], 0\n"
        "s_nop 0\n"
        "v_fmac_f32_dpp v241, v254, v252 quad_perm:[1,1,1,1] row_mask:0xf bank_mask:0xf\n"
        "v_cndmask_b32_e64 v251, v251, v140, s[58:59]\n"
        "v_mfma_f32_16x16x32_bf16 v[128:131], v[44:47], v[114:117], 0\n"
        "s_nop 0\n"
        "v_cvt_pk_bf16_f32 v40, v134, v135\n"
        "v_cvt_pk_bf16_f32 v41, v136, v137\n"
        "v_mfma_f32_16x16x32_bf16 v[108:111], v[196:199], v[76:79], 0\n"
        "v_cvt_pk_bf16_f32 v133, v148, v149\n"
        "v_cvt_pk_bf16_f32 v35, v150, v151\n"
        "v_mfma_f32_16x16x32_bf16 v[108:111], v[184:187], v[84:87], v[108:111]\n"
        "s_nop 0\n"
        "v_cvt_pk_bf16_f32 v106, v128, v129\n"
        "v_cvt_pk_bf16_f32 v134, v130, v131\n"
        "v_mfma_f32_16x16x32_bf16 v[108:111], v[188:191], v[64:67], v[108:111]\n"
        "v_cndmask_b32_e64 v251, v251, v141, s[60:61]\n"
        "v_fmac_f32_dpp v241, v254, v253 quad_perm:[2,2,2,2] row_mask:0xf bank_mask:0xf\n"
        "v_mfma_f32_16x16x32_bf16 v[140:143], v[188:191], v[60:63], 0\n"
        "s_nop 0\n"
        "v_cndmask_b32_e64 v253, v144, v145, s[56:57]\n"
        "v_cndmask_b32_e64 v253, v253, v146, s[58:59]\n"
        "v_mfma_f32_16x16x32_bf16 v[140:143], v[184:187], v[72:75], v[140:143]\n"
        "s_nop 0\n"
        "v_mul_f32 v108, v108, v246\n"
        "v_mul_f32 v109, v109, v242\n"
        "v_mfma_f32_16x16x32_bf16 v[140:143], v[196:199], v[48:51], v[140:143]\n"
        "v_mul_f32 v110, v110, v249\n"
        "v_mul_f32 v111, v111, v248\n"
        "v_mfma_f32_16x16x32_bf16 v[136:139], v[196:199], v[88:91], 0\n"
        "s_nop 0\n"
        "v_cndmask_b32_e64 v253, v253, v147, s[60:61]\n"
        "v_fmac_f32_dpp v241, v254, v250 quad_perm:[3,3,3,3] row_mask:0xf bank_mask:0xf\n"
        "v_mfma_f32_16x16x32_bf16 v[128:131], v[196:199], v[80:83], 0\n"
        "s_nop 0\n"
        "v_mul_f32 v140, v140, v246\n"
        "v_cvt_pk_f16_f32 v108, v108, v140\n"
        "v_mfma_f32_16x16x32_bf16 v[144:147], a[200:203], v[180:183], 0\n"
        "v_perm_b32 v42, v108, v136, s54\n"
        "v_mul_f32 v141, v141, v242\n"
        "v_mfma_f32_16x16x32_bf16 v[144:147], a[196:199], v[172:175], v[144:147]\n"
        "s_nop 0\n"
        "v_perm_b32 v43, v108, v128, s55\n"
        "v_cvt_pk_f16_f32 v109, v109, v141\n"
        "v_mfma_f32_16x16x32_bf16 v[144:147], a[192:195], v[192:195], v[144:147]\n"
        "ds_write_b64 v170, v[42:43] offset:16\n"
        "v_mfma_f32_16x16x32_bf16 v[112:115], a[204:207], v[192:195], 0\n"
        "v_perm_b32 v42, v109, v137, s54\n"
        "v_perm_b32 v43, v109, v129, s55\n"
        "v_mfma_f32_16x16x32_bf16 v[112:115], a[208:211], v[172:175], v[112:115]\n"
        "ds_write_b64 v170, v[42:43] offset:6352\n"
        "v_mfma_f32_16x16x32_bf16 v[112:115], a[212:215], v[180:183], v[112:115]\n"
        "s_nop 0\n"
        "v_cvt_pk_bf16_f32 v152, v144, v145\n"
        "v_cvt_pk_bf16_f32 v153, v146, v147\n"
        "v_mfma_f32_16x16x32_bf16 v[148:151], v[36:39], v[116:119], 0\n"
        "v_mul_f32 v142, v142, v249\n"
        "v_cvt_pk_f16_f32 v110, v110, v142\n"
        "v_mfma_f32_16x16x32_bf16 v[156:159], v[38:41], v[120:123], 0\n"
        "s_nop 0\n"
        "v_cvt_pk_bf16_f32 v154, v112, v113\n"
        "v_cvt_pk_bf16_f32 v155, v114, v115\n"
        "v_mfma_f32_16x16x32_bf16 v[112:115], v[180:183], a[224:227], 0\n"
        "ds_write_b128 v167, v[152:155] offset:128\n"
        "v_mfma_f32_16x16x32_bf16 v[112:115], v[172:175], a[220:223], v[112:115]\n"
        "v_cvt_pk_bf16_f32 v107, v150, v151\n"
        "v_perm_b32 v36, v110, v138, s54\n"
        "v_mfma_f32_16x16x32_bf16 v[112:115], v[192:195], a[216:219], v[112:115]\n"
        "ds_write_b128 v12, v[104:107] offset:6144\n"
        "v_mfma_f32_16x16x32_bf16 v[116:119], v[192:195], a[228:231], 0\n"
        "v_perm_b32 v37, v110, v130, s55\n"
        "v_cvt_pk_bf16_f32 v135, v156, v157\n"
        "v_mfma_f32_16x16x32_bf16 v[116:119], v[172:175], a[232:235], v[116:119]\n"
        "ds_write_b64 v170, v[36:37] offset:12688\n"
        "v_mfma_f32_16x16x32_bf16 v[116:119], v[180:183], a[236:239], v[116:119]\n"
        "ds_write_b128 v12, v[132:135] offset:7168\n"
        "v_mfma_f32_16x16x32_bf16 v[144:147], v[192:195], v[88:91], 0\n"
        "v_mul_f32 v143, v143, v248\n"
        "v_cvt_pk_bf16_f32 v36, v148, v149\n"
        "v_mfma_f32_16x16x32_bf16 v[148:151], v[192:195], v[80:83], 0\n"
        "v_cvt_pk_bf16_f32 v37, v158, v159\n"
        "v_cvt_pk_f16_f32 v111, v111, v143\n"
        "v_mfma_f32_16x16x32_bf16 v[40:43], v[104:107], v[104:107], 0\n"
        "ds_write_b128 v12, v[34:37] offset:8192\n"
        "v_mfma_f32_16x16x32_bf16 v[40:43], v[132:135], v[132:135], v[40:43]\n"
        "v_perm_b32 v46, v111, v139, s54\n"
        "v_perm_b32 v47, v111, v131, s55\n"
        "v_mfma_f32_16x16x32_bf16 v[40:43], v[34:37], v[34:37], v[40:43]\n"
        "ds_write_b64 v170, v[46:47] offset:19024\n"
        "v_mfma_f32_16x16x32_bf16 v[104:107], v[192:195], v[56:59], 0\n"
        "v_mul_f32 v112, v112, v243\n"
        "v_mul_f32 v116, v116, v243\n"
        "v_mfma_f32_16x16x32_bf16 v[104:107], v[172:175], v[124:127], v[104:107]\n"
        "v_cvt_pk_f16_f32 v112, v112, v116\n"
        "v_perm_b32 v34, v112, v144, s54\n"
        "v_mfma_f32_16x16x32_bf16 v[104:107], v[180:183], v[152:155], v[104:107]\n"
        "v_perm_b32 v35, v112, v148, s55\n"
        "v_mul_f32 v113, v113, v244\n"
        "v_mfma_f32_16x16x32_bf16 v[122:125], v[180:183], a[248:251], 0\n"
        "ds_write_b64 v170, v[34:35] offset:25344\n"
        "v_mfma_f32_16x16x32_bf16 v[122:125], v[172:175], a[244:247], v[122:125]\n"
        "v_mul_f32 v117, v117, v244\n"
        "v_cvt_pk_f16_f32 v113, v113, v117\n"
        "v_mfma_f32_16x16x32_bf16 v[122:125], v[192:195], a[240:243], v[122:125]\n"
        "v_perm_b32 v34, v113, v145, s54\n"
        "v_perm_b32 v35, v113, v149, s55\n"
        "v_mfma_f32_16x16x32_bf16 v[162:165], v[192:195], a[252:255], 0\n"
        "ds_write_b64 v170, v[34:35] offset:31680\n"
        "v_mfma_f32_16x16x32_bf16 v[162:165], v[172:175], v[92:95], v[162:165]\n"
        "v_mul_f32 v114, v114, v247\n"
        "v_mul_f32 v118, v118, v247\n"
        "v_mfma_f32_16x16x32_bf16 v[162:165], v[180:183], v[52:55], v[162:165]\n"
        "s_waitcnt vmcnt(0)\n"
        "v_cvt_pk_f16_f32 v114, v114, v118\n"
        "v_perm_b32 v34, v114, v146, s54\n"
        "v_mfma_f32_16x16x32_bf16 v[176:179], v[172:175], v[88:91], 0\n"
        "ds_read_b128 v[146:149], v9\n"
        "v_perm_b32 v35, v114, v150, s55\n"
        "v_mfma_f32_16x16x32_bf16 v[142:145], v[172:175], v[80:83], 0\n"
        "ds_write_b64 v171, v[34:35]\n"
        "v_mfma_f32_16x16x32_bf16 v[34:37], v[180:183], v[76:79], 0\n"
        "ds_read_b128 v[152:155], v10\n"
        "v_cndmask_b32_e64 v30, v104, v105, s[56:57]\n"
        "v_mfma_f32_16x16x32_bf16 v[34:37], v[172:175], v[84:87], v[34:37]\n"
        "ds_read_b128 v[158:161], v11\n"
        "v_cndmask_b32_e64 v30, v30, v106, s[58:59]\n"
        "v_mfma_f32_16x16x32_bf16 v[34:37], v[192:195], v[64:67], v[34:37]\n"
        "ds_read_b128 v[126:129], v9 offset:3072\n"
        "v_cndmask_b32_e64 v30, v30, v107, s[60:61]\n"
        "v_mfma_f32_16x16x32_bf16 v[184:187], v[192:195], v[60:63], 0\n"
        "ds_read_b128 v[132:135], v10 offset:3072\n"
        "v_cndmask_b32_e64 v254, v40, v41, s[56:57]\n"
        "v_mfma_f32_16x16x32_bf16 v[184:187], v[172:175], v[72:75], v[184:187]\n"
        "ds_read_b128 v[138:141], v11 offset:3072\n"
        "v_cndmask_b32_e64 v254, v254, v42, s[58:59]\n"
        "v_mfma_f32_16x16x32_bf16 v[184:187], v[180:183], v[48:51], v[184:187]\n"
        "ds_read_b128 v[106:109], v9 offset:6144\n"
        "v_cndmask_b32_e64 v254, v254, v43, s[60:61]\n"
        "v_mfma_f32_16x16x32_bf16 v[172:175], v[180:183], v[88:91], 0\n"
        "ds_read_b128 v[112:115], v10 offset:6144\n"
        "v_mul_f32 v30, v30, v245\n"
        "v_mfma_f32_16x16x32_bf16 v[40:43], v[180:183], v[80:83], 0\n"
        "ds_read_b128 v[118:121], v11 offset:6144\n"
        "v_cndmask_b32_dpp v251, v254, v251, vcc row_ror:4 row_mask:0xf bank_mask:0xf\n"
        "v_add_f32 v241, v241, v255\n"
        "v_mul_f32 v255, s80, v241\n"
        "v_cndmask_b32_dpp v253, v251, v253, vcc row_ror:4 row_mask:0xf bank_mask:0xf\n"
        "v_fma_f32 v255, -v255, v241, v253\n"
        "v_max_f32 v255, 0, v255\n"
        "v_fmaak_f32 v255, v28, v255, 0x3727c5ac\n"
        "v_sub_f32 v253, 0x47c00000, v253\n"
        "v_mul_f32 v30, v30, v245\n"
        "v_bfi_b32 v255, s84, v253, v255\n"
        "v_rsq_f32 v254, v255\n"
        "v_exp_f32 v30, v30\n"
        "v_cvt_f16_f32 v254, v254\n"
        "ds_write_b16 v18, v254\n"
        "ds_write_b64 v169, v[30:31]\n"
        "ds_write_b16 v19, v254\n"
        "v_mul_f32 v122, v122, v243\n"
        "v_mul_f32 v123, v123, v244\n"
        "v_mul_f32 v162, v162, v243\n"
        "v_mul_f32 v124, v124, v247\n"
        "v_mul_f32 v163, v163, v244\n"
        "v_mov_b32 v254, 0x3c003c00\n"
        "v_mov_b32 v255, s29\n"
        "v_mul_f32 v164, v164, v247\n"
        "v_cndmask_b32_e64 v56, 0, v254, s[76:77]\n"
        "v_cndmask_b32_e64 v57, 0, v254, s[56:57]\n"
        "v_cndmask_b32_e64 v58, 0, v254, s[58:59]\n"
        "v_cndmask_b32_e64 v59, 0, v254, s[60:61]\n"
        "ds_write_b16 v22, v255 offset:3072\n"
        "v_readfirstlane_b32 s20, v200\n"
        "v_readfirstlane_b32 s21, v201\n"
        "v_cvt_pk_f16_f32 v122, v122, v162\n"
        "v_cvt_pk_f16_f32 v123, v123, v163\n"
        "v_cvt_pk_f16_f32 v124, v124, v164\n"
        "v_perm_b32 v30, v122, v176, s54\n"
        "v_perm_b32 v46, v123, v177, s54\n"
        "v_perm_b32 v104, v124, v178, s54\n"
        "v_perm_b32 v31, v122, v142, s55\n"
        "v_perm_b32 v47, v123, v143, s55\n"
        "v_perm_b32 v105, v124, v144, s55\n"
        "ds_write_b64 v170, v[30:31] offset:25352\n"
        "ds_write_b64 v170, v[46:47] offset:31688\n"
        "ds_write_b64 v171, v[104:105] offset:8\n"
        "v_mul_f32 v34, v34, v243\n"
        "v_mul_f32 v35, v35, v244\n"
        "v_mul_f32 v36, v36, v247\n"
        "v_mul_f32 v184, v184, v243\n"
        "v_mul_f32 v185, v185, v244\n"
        "v_mul_f32 v186, v186, v247\n"
        "v_cvt_pk_f16_f32 v34, v34, v184\n"
        "v_cvt_pk_f16_f32 v35, v35, v185\n"
        "v_cvt_pk_f16_f32 v36, v36, v186\n"
        "v_perm_b32 v30, v34, v172, s54\n"
        "v_perm_b32 v46, v35, v173, s54\n"
        "v_perm_b32 v104, v36, v174, s54\n"
        "v_perm_b32 v31, v34, v40, s55\n"
        "v_perm_b32 v47, v35, v41, s55\n"
        "v_perm_b32 v105, v36, v42, s55\n"
        "ds_write_b64 v170, v[30:31] offset:25360\n"
        "ds_write_b64 v170, v[46:47] offset:31696\n"
        "ds_write_b64 v171, v[104:105] offset:16\n"
        "s_waitcnt lgkmcnt(0)\n"
        "s_nop 7\n"
        ".p2align 6\n"
        "TOP_%=:\n"
        "v_mfma_f32_16x16x32_bf16 v[198:201], v[146:149], v[146:149], 0\n"
        "s_mov_b32 s26, s89\n"
        "ds_read_b128 v[218:221], v20 offset:512\n"
        "s_mov_b32 s30, s29\n"
        "v_mfma_f32_16x16x32_bf16 v[198:201], v[152:155], v[152:155], v[198:201]\n"
        "s_add_u32 s88, s88, 32\n"
        "v_add_u32 v23, s22, v23\n"
        "v_add_u32 v24, s22, v24\n"
        "v_mfma_f32_16x16x32_bf16 v[198:201], v[158:161], v[158:161], v[198:201]\n"
        "s_mov_b32 s22, s23\n"
        "ds_read_b128 v[230:233], v20 offset:528\n"
        "s_add_u32 s89, s86, s51\n"
        "v_mfma_f32_16x16x32_bf16 v[34:37], v[100:103], v[158:161], 0\n"
        "s_add_u32 s29, s29, 1\n"
        "s_cmp_ge_u32 s88, 73\n"
        "v_cvt_pk_bf16_f32 v216, v202, v206\n"
        "v_mfma_f32_16x16x32_bf16 v[34:37], v[68:71], v[152:155], v[34:37]\n"
        "s_movk_i32 s23, 1584\n"
        "v_cvt_pk_bf16_f32 v236, v203, v207\n"
        "v_lshlrev_b32 v226, 16, v124\n"
        "v_mfma_f32_16x16x32_bf16 v[34:37], v[96:99], v[146:149], v[34:37]\n"
        "s_mov_b32 s86, s87\n"
        "v_lshlrev_b32 v227, 16, v176\n"
        "v_lshlrev_b32 v228, 16, v196\n"
        "v_mfma_f32_16x16x32_bf16 v[210:213], a[0:3], v[146:149], 0\n"
        "s_cselect_b32 s92, 588672, 0\n"
        "v_cvt_pk_bf16_f32 v252, v204, v208\n"
        "v_lshlrev_b32 v229, 16, v192\n"
        "v_mfma_f32_16x16x32_bf16 v[210:213], a[4:7], v[152:155], v[210:213]\n"
        "s_cselect_b32 s94, 73, 0\n"
        "s_add_u32 s27, s26, 392448\n"
        "s_add_u32 s28, s26, 490560\n"
        "v_mfma_f32_16x16x32_bf16 v[210:213], a[8:11], v[158:161], v[210:213]\n"
        "buffer_store_dwordx3 v[214:216], v15, s[40:43], s27 offen offset:768 sc1 nt\n"
        "s_add_u32 s87, s87, 43008\n"
        "v_lshlrev_b32 v206, 16, v172\n"
        "v_lshlrev_b32 v207, 16, v188\n"
        "v_cvt_pk_bf16_f32 v248, v205, v209\n"
        "v_mfma_f32_16x16x32_bf16 v[222:225], a[20:23], v[158:161], 0\n"
        "buffer_store_dwordx3 v[234:236], v14, s[40:43], s28 offen offset:192 sc1 nt\n"
        "s_sub_u32 s88, s88, s94\n"
        "v_cvt_pk_bf16_f32 v208, v210, v211\n"
        "v_cvt_pk_bf16_f32 v209, v212, v213\n"
        "v_cndmask_b32_e64 v254, v198, v199, s[56:57]\n"
        "v_mfma_f32_16x16x32_bf16 v[222:225], a[16:19], v[152:155], v[222:225]\n"
        "buffer_store_dwordx3 v[250:252], v16, s[40:43], s28 offen offset:960 sc1 nt\n"
        "s_add_u32 s87, s87, s92\n"
        "s_cmp_le_i32 s85, 0\n"
        "v_cndmask_b32_e64 v254, v254, v200, s[58:59]\n"
        "v_cndmask_b32_e64 v254, v254, v201, s[60:61]\n"
        "v_mfma_f32_16x16x32_bf16 v[222:225], a[12:15], v[146:149], v[222:225]\n"
        "s_waitcnt lgkmcnt(0)\n"
        "v_pk_mul_f16 v198, v218, v142\n"
        "v_pk_mul_f16 v199, v218, v162\n"
        "v_mfma_f32_16x16x32_bf16 v[212:215], a[24:27], v[146:149], 0\n"
        "s_cbranch_scc1 DY1_%=\n"
        "DY1B_%=:\n"
        "s_add_u32 s27, s26, 588672\n"
        "v_pk_mul_f16 v200, v218, v178\n"
        "v_mfma_f32_16x16x32_bf16 v[212:215], a[28:31], v[152:155], v[212:215]\n"
        "buffer_store_dwordx3 v[246:248], v14, s[40:43], s27 offen offset:384 sc1 nt\n"
        "v_pk_mul_f16 v201, v218, v166\n"
        "v_pk_mul_f16 v202, v218, v182\n"
        "v_pk_mul_f16 v203, v218, v40\n"
        "v_pk_fma_f16 v198, v219, v143, v198\n"
        "v_mfma_f32_16x16x32_bf16 v[212:215], a[32:35], v[158:161], v[212:215]\n"
        "s_cmp_gt_i32 s85, 0\n"
        "v_pk_fma_f16 v199, v219, v163, v199\n"
        "v_pk_fma_f16 v200, v219, v179, v200\n"
        "v_mfma_f32_16x16x32_bf16 v[234:237], a[44:47], v[158:161], 0\n"
        "s_cselect_b32 s94, s87, s84\n"
        "v_pk_fma_f16 v201, v219, v167, v201\n"
        "v_pk_fma_f16 v202, v219, v183, v202\n"
        "v_mfma_f32_16x16x32_bf16 v[234:237], a[40:43], v[152:155], v[234:237]\n"
        "s_sub_u32 s85, s85, 1\n"
        "v_pk_fma_f16 v203, v219, v41, v203\n"
        "v_pk_fma_f16 v198, v220, v144, v198\n"
        "v_mfma_f32_16x16x32_bf16 v[234:237], a[36:39], v[146:149], v[234:237]\n"
        "s_waitcnt lgkmcnt(0)\n"
        "v_pk_fma_f16 v199, v220, v164, v199\n"
        "v_pk_fma_f16 v200, v220, v180, v200\n"
        "v_mfma_f32_16x16x32_bf16 v[216:219], a[48:51], v[146:149], 0\n"
        "s_waitcnt lgkmcnt(0)\n"
        "v_pk_fma_f16 v201, v220, v168, v201\n"
        "v_pk_fma_f16 v202, v220, v184, v202\n"
        "v_mfma_f32_16x16x32_bf16 v[216:219], a[52:55], v[152:155], v[216:219]\n"
        "v_pk_fma_f16 v203, v220, v42, v203\n"
        "v_pk_fma_f16 v198, v221, v145, v198\n"
        "v_mfma_f32_16x16x32_bf16 v[216:219], a[56:59], v[158:161], v[216:219]\n"
        "v_pk_fma_f16 v199, v221, v165, v199\n"
        "v_pk_fma_f16 v200, v221, v181, v200\n"
        "v_mfma_f32_16x16x32_bf16 v[162:165], a[68:71], v[158:161], 0\n"
        "v_pk_fma_f16 v201, v221, v169, v201\n"
        "v_pk_fma_f16 v202, v221, v185, v202\n"
        "v_mfma_f32_16x16x32_bf16 v[162:165], a[64:67], v[152:155], v[162:165]\n"
        "v_pk_fma_f16 v203, v221, v43, v203\n"
        "v_pk_fma_f16 v198, v230, v122, v198\n"
        "v_mfma_f32_16x16x32_bf16 v[162:165], a[60:63], v[146:149], v[162:165]\n"
        "v_pk_fma_f16 v199, v230, v174, v199\n"
        "v_cvt_pk_bf16_f32 v210, v222, v223\n"
        "v_mfma_f32_16x16x32_bf16 v[166:169], v[126:129], v[126:129], 0\n"
        "v_cvt_pk_bf16_f32 v211, v224, v225\n"
        "v_cvt_pk_bf16_f32 v178, v212, v213\n"
        "v_mfma_f32_16x16x32_bf16 v[166:169], v[132:135], v[132:135], v[166:169]\n"
        "v_cvt_pk_bf16_f32 v179, v214, v215\n"
        "v_cvt_pk_bf16_f32 v180, v234, v235\n"
        "v_mfma_f32_16x16x32_bf16 v[166:169], v[138:141], v[138:141], v[166:169]\n"
        "v_cvt_pk_bf16_f32 v181, v236, v237\n"
        "v_cvt_pk_bf16_f32 v182, v216, v217\n"
        "v_mfma_f32_16x16x32_bf16 v[142:145], v[100:103], v[138:141], 0\n"
        "v_cvt_pk_bf16_f32 v183, v218, v219\n"
        "v_cvt_pk_bf16_f32 v184, v162, v163\n"
        "v_mfma_f32_16x16x32_bf16 v[142:145], v[68:71], v[132:135], v[142:145]\n"
        "v_cvt_pk_bf16_f32 v185, v164, v165\n"
        "v_pk_fma_f16 v200, v230, v194, v200\n"
        "v_mfma_f32_16x16x32_bf16 v[142:145], v[96:99], v[126:129], v[142:145]\n"
        "v_pk_fma_f16 v201, v230, v190, v201\n"
        "v_pk_fma_f16 v202, v230, v170, v202\n"
        "v_mfma_f32_16x16x32_bf16 v[40:43], v[146:149], v[208:211], 0\n"
        "v_pk_fma_f16 v203, v230, v186, v203\n"
        "v_pk_fma_f16 v198, v231, v123, v198\n"
        "v_mfma_f32_16x16x32_bf16 v[40:43], v[152:155], v[178:181], v[40:43]\n"
        "v_pk_fma_f16 v199, v231, v175, v199\n"
        "v_pk_fma_f16 v200, v231, v195, v200\n"
        "v_mfma_f32_16x16x32_bf16 v[40:43], v[158:161], v[182:185], v[40:43]\n"
        "v_pk_fma_f16 v201, v231, v191, v201\n"
        "v_pk_fma_f16 v202, v231, v171, v202\n"
        "v_mfma_f32_16x16x32_bf16 v[162:165], a[8:11], v[138:141], 0\n"
        "v_pk_fma_f16 v203, v231, v187, v203\n"
        "v_pk_fma_f16 v198, v232, v124, v198\n"
        "v_mfma_f32_16x16x32_bf16 v[162:165], a[4:7], v[132:135], v[162:165]\n"
        "v_pk_fma_f16 v199, v232, v176, v199\n"
        "v_pk_fma_f16 v200, v232, v196, v200\n"
        "v_mfma_f32_16x16x32_bf16 v[162:165], a[0:3], v[126:129], v[162:165]\n"
        "v_pk_fma_f16 v201, v232, v192, v201\n"
        "v_pk_fma_f16 v202, v232, v172, v202\n"
        "v_mfma_f32_16x16x32_bf16 v[170:173], a[12:15], v[126:129], 0\n"
        "s_nop 0\n"
        "v_pk_fma_f16 v203, v232, v188, v203\n"
        "v_cndmask_b32_e64 v255, v166, v167, s[56:57]\n"
        "v_mfma_f32_16x16x32_bf16 v[170:173], a[16:19], v[132:135], v[170:173]\n"
        "s_add_u32 s46, s26, 590016\n"
        "v_cvt_pk_bf16_f32 v122, v162, v163\n"
        "v_cvt_pk_bf16_f32 v123, v164, v165\n"
        "v_mfma_f32_16x16x32_bf16 v[170:173], a[20:23], v[138:141], v[170:173]\n"
        "s_nop 0\n"
        "v_cndmask_b32_e64 v255, v255, v168, s[58:59]\n"
        "v_cndmask_b32_e64 v255, v255, v169, s[60:61]\n"
        "v_mfma_f32_16x16x32_f16 v[226:229], v[56:59], v[198:201], v[226:229]\n"
        "s_min_u32 s46, s46, s42\n"
        "s_nop 0\n"
        "v_mfma_f32_16x16x32_f16 v[204:207], v[56:59], v[200:203], v[204:207]\n"
        "s_nop 0\n"
        "v_cvt_pk_bf16_f32 v124, v170, v171\n"
        "v_cvt_pk_bf16_f32 v125, v172, v173\n"
        "v_mfma_f32_16x16x32_bf16 v[164:167], a[32:35], v[138:141], 0\n"
        "s_nop 0\n"
        "v_cvt_pk_bf16_f32 v174, v226, v227\n"
        "v_cvt_pk_bf16_f32 v175, v228, v229\n"
        "v_mfma_f32_16x16x32_bf16 v[164:167], a[28:31], v[132:135], v[164:167]\n"
        "v_cvt_pk_bf16_f32 v176, v206, v207\n"
        "v_mfma_f32_16x16x32_bf16 v[164:167], a[24:27], v[126:129], v[164:167]\n"
        "s_nop 1\n"
        "v_mfma_f32_16x16x32_bf16 v[170:173], a[36:39], v[126:129], 0\n"
        "s_nop 2\n"
        "v_mfma_f32_16x16x32_bf16 v[170:173], a[40:43], v[132:135], v[170:173]\n"
        "buffer_store_dwordx3 v[174:176], v17, s[44:47], s27 offen offset:1152 sc1 nt\n"
        "v_cvt_pk_bf16_f32 v162, v164, v165\n"
        "v_cvt_pk_bf16_f32 v163, v166, v167\n"
        "v_mfma_f32_16x16x32_bf16 v[170:173], a[44:47], v[138:141], v[170:173]\n"
        "s_nop 1\n"
        "v_mfma_f32_16x16x32_bf16 v[166:169], a[56:59], v[138:141], 0\n"
        "s_nop 2\n"
        "v_mfma_f32_16x16x32_bf16 v[166:169], a[52:55], v[132:135], v[166:169]\n"
        "s_nop 0\n"
        "v_cvt_pk_bf16_f32 v164, v170, v171\n"
        "v_cvt_pk_bf16_f32 v165, v172, v173\n"
        "v_mfma_f32_16x16x32_bf16 v[166:169], a[48:51], v[126:129], v[166:169]\n"
        "s_nop 1\n"
        "v_mfma_f32_16x16x32_bf16 v[170:173], a[60:63], v[126:129], 0\n"
        "s_nop 2\n"
        "v_mfma_f32_16x16x32_bf16 v[170:173], a[64:67], v[132:135], v[170:173]\n"
        "s_nop 0\n"
        "v_cvt_pk_bf16_f32 v178, v166, v167\n"
        "v_cvt_pk_bf16_f32 v179, v168, v169\n"
        "v_mfma_f32_16x16x32_bf16 v[170:173], a[68:71], v[138:141], v[170:173]\n"
        "s_nop 0\n"
        "ds_read_u16 v252, v19 offset:2048\n"
        "v_mfma_f32_16x16x32_bf16 v[166:169], v[106:109], v[106:109], 0\n"
        "s_nop 2\n"
        "v_mfma_f32_16x16x32_bf16 v[166:169], v[112:115], v[112:115], v[166:169]\n"
        "s_nop 0\n"
        "v_cvt_pk_bf16_f32 v180, v170, v171\n"
        "v_cvt_pk_bf16_f32 v181, v172, v173\n"
        "v_mfma_f32_16x16x32_bf16 v[166:169], v[118:121], v[118:121], v[166:169]\n"
        "s_nop 1\n"
        "v_mfma_f32_16x16x32_bf16 v[170:173], v[100:103], v[118:121], 0\n"
        "s_nop 2\n"
        "v_mfma_f32_16x16x32_bf16 v[170:173], v[68:71], v[112:115], v[170:173]\n"
        "s_nop 0\n"
        "v_cndmask_b32_e64 v253, v166, v167, s[56:57]\n"
        "v_cndmask_b32_e64 v253, v253, v168, s[58:59]\n"
        "v_mfma_f32_16x16x32_bf16 v[170:173], v[96:99], v[106:109], v[170:173]\n"
        "s_cmp_le_i32 s85, 1\n"
        "v_cndmask_b32_e64 v253, v253, v169, s[60:61]\n"
        "v_mfma_f32_16x16x32_bf16 v[174:177], v[126:129], v[122:125], 0\n"
        "s_cbranch_scc1 DY2_%=\n"
        "DY2B_%=:\n"
        "v_cndmask_b32_dpp v255, v253, v255, vcc row_ror:4 row_mask:0xf bank_mask:0xf\n"
        "v_mfma_f32_16x16x32_bf16 v[174:177], v[132:135], v[162:165], v[174:177]\n"
        "s_waitcnt lgkmcnt(0)\n"
        "v_cmp_ne_u32_e64 s[24:25], s30, v252\n"
        "v_cndmask_b32_dpp v254, v255, v254, vcc row_ror:4 row_mask:0xf bank_mask:0xf\n"
        "v_mfma_f32_16x16x32_bf16 v[174:177], v[138:141], v[178:181], v[174:177]\n"
        "v_cndmask_b32_dpp v253, v170, v142, vcc row_ror:4 row_mask:0xf bank_mask:0xf\n"
        "v_cndmask_b32_dpp v255, v171, v143, vcc row_ror:4 row_mask:0xf bank_mask:0xf\n"
        "v_mfma_f32_16x16x32_bf16 v[170:173], a[8:11], v[118:121], 0\n"
        "s_add_u32 m0, s52, 0\n"
        "v_cndmask_b32_dpp v253, v253, v34, vcc row_ror:4 row_mask:0xf bank_mask:0xf\n"
        "v_mul_f32 v251, s80, v253\n"
        "v_mfma_f32_16x16x32_bf16 v[170:173], a[4:7], v[112:115], v[170:173]\n"
        "s_cmp_lg_u64 s[24:25], 0\n"
        "ds_read_b128 v[166:169], v23\n"
        "v_fma_f32 v251, -v251, v253, v254\n"
        "v_mfma_f32_16x16x32_bf16 v[170:173], a[0:3], v[106:109], v[170:173]\n"
        "s_cbranch_scc1 SP6_%=\n"
        "BK6_%=:\n"
        "buffer_load_dwordx4 v0, s[36:39], s94 offen lds\n"
        "v_max_f32 v251, 0, v251\n"
        "v_mfma_f32_16x16x32_bf16 v[162:165], a[12:15], v[106:109], 0\n"
        "ds_read_b128 v[122:125], v23 offset:64\n"
        "ds_read_b128 v[142:145], v23 offset:128\n"
        "v_mfma_f32_16x16x32_bf16 v[162:165], a[16:19], v[112:115], v[162:165]\n"
        "ds_read_b64 v[36:37], v24 offset:360\n"
        "v_mov_b32 v254, s29\n"
        "v_mfma_f32_16x16x32_bf16 v[162:165], a[20:23], v[118:121], v[162:165]\n"
        "ds_read_b128 v[222:225], v13\n"
        "v_cvt_pk_bf16_f32 v194, v170, v171\n"
        "v_mfma_f32_16x16x32_bf16 v[178:181], a[32:35], v[118:121], 0\n"
        "ds_read_b128 v[206:209], v13 offset:1024\n"
        "ds_read_b128 v[218:221], v13 offset:2048\n"
        "v_mfma_f32_16x16x32_bf16 v[178:181], a[28:31], v[112:115], v[178:181]\n"
        "ds_read_b128 v[226:229], v13 offset:3072\n"
        "v_cvt_pk_bf16_f32 v195, v172, v173\n"
        "v_mfma_f32_16x16x32_bf16 v[178:181], a[24:27], v[106:109], v[178:181]\n"
        "buffer_load_dwordx4 v1, s[36:39], s94 offen offset:1024 lds\n"
        "v_cvt_pk_bf16_f32 v196, v162, v163\n"
        "v_mfma_f32_16x16x32_bf16 v[186:189], a[36:39], v[106:109], 0\n"
        "ds_read_b128 v[234:237], v13 offset:4096\n"
        "ds_read_b128 v[230:233], v13 offset:5120\n"
        "v_mfma_f32_16x16x32_bf16 v[186:189], a[40:43], v[112:115], v[186:189]\n"
        "ds_read_b128 v[202:205], v13 offset:6144\n"
        "ds_read_b128 v[210:213], v13 offset:7168\n"
        "v_mfma_f32_16x16x32_bf16 v[186:189], a[44:47], v[118:121], v[186:189]\n"
        "ds_read_b128 v[198:201], v13 offset:8192\n"
        "ds_read_b128 v[214:217], v20\n"
        "v_mfma_f32_16x16x32_bf16 v[182:185], a[56:59], v[118:121], 0\n"
        "s_waitcnt lgkmcnt(10)\n"
        "ds_read_b64 v[246:247], v20 offset:16\n"
        "ds_read_u16 v250, v22\n"
        "v_mfma_f32_16x16x32_bf16 v[182:185], a[52:55], v[112:115], v[182:185]\n"
        "ds_read_b64 v[172:173], v21\n"
        "v_readlane_b32 s93, v37, 1\n"
        "v_mfma_f32_16x16x32_bf16 v[182:185], a[48:51], v[106:109], v[182:185]\n"
        "s_waitcnt lgkmcnt(10)\n"
        "buffer_load_dwordx4 v2, s[36:39], s94 offen offset:2048 lds\n"
        "v_cvt_pk_bf16_f32 v197, v164, v165\n"
        "v_mfma_f32_16x16x32_bf16 v[162:165], a[60:63], v[106:109], 0\n"
        "ds_write_b16 v22, v254 offset:3584\n"
        "v_mfma_f32_16x16x32_bf16 v[162:165], a[64:67], v[112:115], v[162:165]\n"
        "v_cvt_pk_bf16_f32 v190, v178, v179\n"
        "v_cvt_pk_bf16_f32 v191, v180, v181\n"
        "v_mfma_f32_16x16x32_bf16 v[162:165], a[68:71], v[118:121], v[162:165]\n"
        "s_waitcnt lgkmcnt(8)\n"
        "v_cvt_pk_bf16_f32 v192, v186, v187\n"
        "v_cvt_pk_bf16_f32 v193, v188, v189\n"
        "v_mfma_f32_16x16x32_bf16 v[238:241], v[166:169], v[222:225], 0\n"
        "v_cvt_pk_bf16_f32 v186, v182, v183\n"
        "v_cvt_pk_bf16_f32 v187, v184, v185\n"
        "v_mfma_f32_16x16x32_bf16 v[238:241], v[122:125], v[206:209], v[238:241]\n"
        "s_nop 0\n"
        "v_cvt_pk_bf16_f32 v188, v162, v163\n"
        "v_cvt_pk_bf16_f32 v189, v164, v165\n"
        "v_mfma_f32_16x16x32_bf16 v[238:241], v[142:145], v[218:221], v[238:241]\n"
        "buffer_load_dwordx4 v3, s[36:39], s94 offen offset:3072 lds\n"
        "v_cndmask_b32_e64 v36, 0, v36, s[72:73]\n"
        "v_mfma_f32_16x16x32_bf16 v[162:165], v[142:145], v[230:233], 0\n"
        "s_waitcnt lgkmcnt(1)\n"
        "v_fmaak_f32 v251, v28, v251, 0x3727c5ac\n"
        "v_cndmask_b32_dpp v255, v255, v35, vcc row_ror:4 row_mask:0xf bank_mask:0xf\n"
        "v_mfma_f32_16x16x32_bf16 v[162:165], v[122:125], v[234:237], v[162:165]\n"
        "v_rsq_f32 v244, v251\n"
        "v_mfma_f32_16x16x32_bf16 v[162:165], v[166:169], v[226:229], v[162:165]\n"
        "s_nop 1\n"
        "v_mfma_f32_16x16x32_bf16 v[178:181], v[166:169], v[202:205], 0\n"
        "s_add_u32 m0, s52, 4096\n"
        "s_nop 1\n"
        "v_mfma_f32_16x16x32_bf16 v[178:181], v[122:125], v[210:213], v[178:181]\n"
        "s_nop 0\n"
        "v_cndmask_b32_e64 v245, v238, v162, s[64:65]\n"
        "v_cndmask_b32_e64 v251, v239, v163, s[64:65]\n"
        "v_mfma_f32_16x16x32_bf16 v[178:181], v[142:145], v[198:201], v[178:181]\n"
        "s_nop 0\n"
        "buffer_load_dwordx4 v4, s[36:39], s94 offen lds\n"
        "v_mfma_f32_16x16x32_bf16 v[142:145], v[106:109], v[194:197], 0\n"
        "s_nop 2\n"
        "v_mfma_f32_16x16x32_bf16 v[142:145], v[112:115], v[190:193], v[142:145]\n"
        "s_nop 0\n"
        "v_cndmask_b32_e64 v245, v245, v178, s[70:71]\n"
        "v_cndmask_b32_e64 v251, v251, v179, s[70:71]\n"
        "v_mfma_f32_16x16x32_bf16 v[142:145], v[118:121], v[186:189], v[142:145]\n"
        "v_cvt_pk_f16_f32 v245, v245, v251\n"
        "v_pk_mul_f16 v245, v245, v250 op_sel_hi:[1,0]\n"
        "v_mfma_f32_16x16x32_bf16 v[162:165], a[152:155], v[218:221], 0\n"
        "v_pk_mul_f16 v245, v245, s93 op_sel_hi:[1,0]\n"
        "v_mfma_f32_16x16x32_bf16 v[162:165], a[148:151], v[206:209], v[162:165]\n"
        "v_exp_f16_sdwa v245, v245 dst_sel:WORD_0 dst_unused:UNUSED_PRESERVE src0_sel:WORD_0\n"
        "v_mfma_f32_16x16x32_bf16 v[162:165], a[144:147], v[222:225], v[162:165]\n"
        "buffer_load_dwordx4 v5, s[36:39], s94 offen offset:1024 lds\n"
        "v_cndmask_b32_dpp v252, v142, v174, vcc row_ror:4 row_mask:0xf bank_mask:0xf\n"
        "v_mfma_f32_16x16x32_bf16 v[122:125], a[156:159], v[222:225], 0\n"
        "v_cndmask_b32_dpp v254, v143, v175, vcc row_ror:4 row_mask:0xf bank_mask:0xf\n"
        "v_cndmask_b32_dpp v248, v144, v176, vcc row_ror:4 row_mask:0xf bank_mask:0xf\n"
        "v_mfma_f32_16x16x32_bf16 v[122:125], a[160:163], v[206:209], v[122:125]\n"
        "v_cndmask_b32_dpp v249, v145, v177, vcc row_ror:4 row_mask:0xf bank_mask:0xf\n"
        "v_cndmask_b32_dpp v252, v252, v40, vcc row_ror:4 row_mask:0xf bank_mask:0xf\n"
        "v_mfma_f32_16x16x32_bf16 v[122:125], a[164:167], v[218:221], v[122:125]\n"
        "v_cndmask_b32_dpp v254, v254, v41, vcc row_ror:4 row_mask:0xf bank_mask:0xf\n"
        "v_cndmask_b32_dpp v248, v248, v42, vcc row_ror:4 row_mask:0xf bank_mask:0xf\n"
        "v_mfma_f32_16x16x32_bf16 v[176:179], a[176:179], v[218:221], 0\n"
        "v_cndmask_b32_dpp v249, v249, v43, vcc row_ror:4 row_mask:0xf bank_mask:0xf\n"
        "v_mul_f32_dpp v252, v244, v252 quad_perm:[0,0,0,0] row_mask:0xf bank_mask:0xf\n"
        "v_mfma_f32_16x16x32_bf16 v[176:179], a[172:175], v[206:209], v[176:179]\n"
        "v_mul_f32_dpp v254, v244, v254 quad_perm:[1,1,1,1] row_mask:0xf bank_mask:0xf\n"
        "v_mul_f32_dpp v248, v244, v248 quad_perm:[2,2,2,2] row_mask:0xf bank_mask:0xf\n"
        "v_mfma_f32_16x16x32_bf16 v[176:179], a[168:171], v[222:225], v[176:179]\n"
        "buffer_load_dwordx4 v6, s[36:39], s94 offen offset:2048 lds\n"
        "v_mul_f32_dpp v249, v244, v249 quad_perm:[3,3,3,3] row_mask:0xf bank_mask:0xf\n"
        "v_mfma_f32_16x16x32_bf16 v[142:145], a[180:183], v[222:225], 0\n"
        "v_max3_f32 v251, v252, v254, v248\n"
        "v_max_f32 v251, v251, v249\n"
        "v_mfma_f32_16x16x32_bf16 v[142:145], a[184:187], v[206:209], v[142:145]\n"
        "v_mul_f32 v251, v251, v244\n"
        "v_fma_f32 v252, v252, v244, -v251\n"
        "v_mfma_f32_16x16x32_bf16 v[142:145], a[188:191], v[218:221], v[142:145]\n"
        "v_fma_f32 v254, v254, v244, -v251\n"
        "v_fma_f32 v248, v248, v244, -v251\n"
        "v_mfma_f32_16x16x32_bf16 v[166:169], a[200:203], v[218:221], 0\n"
        "v_fma_f32 v249, v249, v244, -v251\n"
        "v_cvt_pk_bf16_f32 v40, v162, v163\n"
        "v_mfma_f32_16x16x32_bf16 v[166:169], a[196:199], v[206:209], v[166:169]\n"
        "v_exp_f32 v252, v252\n"
        "v_mfma_f32_16x16x32_bf16 v[166:169], a[192:195], v[222:225], v[166:169]\n"
        "buffer_load_dwordx4 v7, s[36:39], s94 offen offset:3072 lds\n"
        "v_cvt_pk_bf16_f32 v41, v164, v165\n"
        "v_mfma_f32_16x16x32_bf16 v[162:165], a[204:207], v[222:225], 0\n"
        "v_cvt_pk_bf16_f32 v42, v122, v123\n"
        "v_cvt_pk_bf16_f32 v43, v124, v125\n"
        "v_mfma_f32_16x16x32_bf16 v[162:165], a[208:211], v[206:209], v[162:165]\n"
        "v_cvt_pk_bf16_f32 v174, v176, v177\n"
        "v_cvt_pk_bf16_f32 v175, v178, v179\n"
        "v_mfma_f32_16x16x32_bf16 v[162:165], a[212:215], v[218:221], v[162:165]\n"
        "v_cvt_pk_bf16_f32 v176, v142, v143\n"
        "v_cvt_pk_bf16_f32 v177, v144, v145\n"
        "v_mfma_f32_16x16x32_bf16 v[178:181], v[158:161], a[80:83], 0\n"
        "s_add_u32 m0, s52, 8192\n"
        "v_cvt_pk_bf16_f32 v142, v166, v167\n"
        "v_cvt_pk_bf16_f32 v143, v168, v169\n"
        "v_mfma_f32_16x16x32_bf16 v[178:181], v[152:155], a[76:79], v[178:181]\n"
        "s_nop 0\n"
        "v_cvt_pk_bf16_f32 v144, v162, v163\n"
        "v_cvt_pk_bf16_f32 v145, v164, v165\n"
        "v_mfma_f32_16x16x32_bf16 v[178:181], v[146:149], a[72:75], v[178:181]\n"
        "buffer_load_dwordx4 v8, s[36:39], s94 offen lds\n"
        "v_mfma_f32_16x16x32_bf16 v[122:125], v[146:149], a[84:87], 0\n"
        "v_exp_f32 v254, v254\n"
        "v_mfma_f32_16x16x32_bf16 v[122:125], v[152:155], a[88:91], v[122:125]\n"
        "v_exp_f32 v248, v248\n"
        "v_mfma_f32_16x16x32_bf16 v[122:125], v[158:161], a[92:95], v[122:125]\n"
        "v_exp_f32 v249, v249\n"
        "v_mfma_f32_16x16x32_bf16 v[182:185], v[222:225], v[40:43], 0\n"
        "v_add_f32 v251, v252, v254\n"
        "v_add_f32 v243, v248, v249\n"
        "v_mfma_f32_16x16x32_bf16 v[182:185], v[206:209], v[174:177], v[182:185]\n"
        "v_add_f32 v251, v251, v243\n"
        "v_cvt_pk_bf16_f32 v30, v178, v179\n"
        "v_mfma_f32_16x16x32_bf16 v[182:185], v[218:221], v[142:145], v[182:185]\n"
        "v_rcp_f32 v251, v251\n"
        "v_mfma_f32_16x16x32_bf16 v[164:167], v[230:233], v[142:145], 0\n"
        "v_exp_f16_sdwa v245, v245 dst_sel:WORD_1 dst_unused:UNUSED_PRESERVE src0_sel:WORD_1\n"
        "v_mfma_f32_16x16x32_bf16 v[164:167], v[234:237], v[174:177], v[164:167]\n"
        "v_mul_f32 v252, v252, v251\n"
        "v_mul_f32 v254, v254, v251\n"
        "v_mfma_f32_16x16x32_bf16 v[164:167], v[226:229], v[40:43], v[164:167]\n"
        "v_mul_f32 v248, v248, v251\n"
        "v_mul_f32 v249, v249, v251\n"
        "v_mfma_f32_16x16x32_bf16 v[168:171], v[202:205], v[40:43], 0\n"
        "v_mul_f32_dpp v252, v244, v252 quad_perm:[0,0,0,0] row_mask:0xf bank_mask:0xf\n"
        "v_mul_f32_dpp v254, v244, v254 quad_perm:[1,1,1,1] row_mask:0xf bank_mask:0xf\n"
        "v_mfma_f32_16x16x32_bf16 v[168:171], v[210:213], v[174:177], v[168:171]\n"
        "v_mul_f32_dpp v248, v244, v248 quad_perm:[2,2,2,2] row_mask:0xf bank_mask:0xf\n"
        "v_mul_f32_dpp v249, v244, v249 quad_perm:[3,3,3,3] row_mask:0xf bank_mask:0xf\n"
        "v_mfma_f32_16x16x32_bf16 v[168:171], v[198:201], v[142:145], v[168:171]\n"
        "v_cvt_pk_bf16_f32 v197, v252, v254\n"
        "v_cvt_pk_bf16_f32 v243, v248, v249\n"
        "v_mfma_f32_16x16x32_bf16 v[174:177], v[158:161], a[104:107], 0\n"
        "v_and_b32 v144, v25, v197\n"
        "v_and_b32 v145, v25, v243\n"
        "v_mfma_f32_16x16x32_bf16 v[174:177], v[152:155], a[100:103], v[174:177]\n"
        "v_mov_b64 v[150:151], v[144:145]\n"
        "v_cvt_pk_bf16_f32 v31, v180, v181\n"
        "v_mfma_f32_16x16x32_bf16 v[174:177], v[146:149], a[96:99], v[174:177]\n"
        "v_cvt_pk_bf16_f32 v34, v122, v123\n"
        "v_cvt_pk_bf16_f32 v35, v124, v125\n"
        "v_mfma_f32_16x16x32_bf16 v[122:125], v[146:149], a[108:111], 0\n"
        "v_cvt_f32_f16 v242, v245\n"
        "v_cndmask_b32_e64 v242, v242, v36, s[68:69]\n"
        "v_mfma_f32_16x16x32_bf16 v[122:125], v[152:155], a[112:115], v[122:125]\n"
        "s_nop 0\n"
        "v_add_f32_dpp v244, v242, v242 row_ror:8 row_mask:0xf bank_mask:0xf\n"
        "v_mov_b64 v[156:157], v[144:145]\n"
        "v_mfma_f32_16x16x32_bf16 v[122:125], v[158:161], a[116:119], v[122:125]\n"
        "v_add_f32_dpp v244, v244, v244 row_ror:4 row_mask:0xf bank_mask:0xf\n"
        "v_cvt_pk_bf16_f32 v42, v174, v175\n"
        "v_mfma_f32_16x16x32_bf16 v[178:181], v[30:33], v[144:147], 0\n"
        "ds_read_u16 v251, v19 offset:2560\n"
        "v_add_f32_dpp v244, v244, v244 row_ror:2 row_mask:0xf bank_mask:0xf\n"
        "v_mfma_f32_16x16x32_bf16 v[186:189], v[32:35], v[148:151], 0\n"
        "s_nop 0\n"
        "v_add_f32_dpp v244, v244, v244 row_ror:1 row_mask:0xf bank_mask:0xf\n"
        "v_cvt_pk_bf16_f32 v43, v176, v177\n"
        "v_mfma_f32_16x16x32_bf16 v[192:195], v[158:161], a[128:131], 0\n"
        "v_add_f32_dpp v244, v244, v244 row_bcast:15 row_mask:0xa bank_mask:0xf\n"
        "v_cvt_pk_bf16_f32 v46, v122, v123\n"
        "v_mfma_f32_16x16x32_bf16 v[192:195], v[152:155], a[124:127], v[192:195]\n"
        "v_cvt_pk_bf16_f32 v47, v124, v125\n"
        "v_add_f32_dpp v244, v244, v244 row_bcast:31 row_mask:0xc bank_mask:0xf\n"
        "v_mfma_f32_16x16x32_bf16 v[192:195], v[146:149], a[120:123], v[192:195]\n"
        "v_readlane_b32 s95, v244, 63\n"
        "v_mov_b64 v[162:163], v[144:145]\n"
        "v_mfma_f32_16x16x32_bf16 v[142:145], v[146:149], a[132:135], 0\n"
        "v_rcp_f32 v191, s95\n"
        "v_mfma_f32_16x16x32_bf16 v[142:145], v[152:155], a[136:139], v[142:145]\n"
        "v_mul_f32 v242, v242, v191\n"
        "v_cvt_f16_f32 v242, v242\n"
        "v_mfma_f32_16x16x32_bf16 v[142:145], v[158:161], a[140:143], v[142:145]\n"
        "v_mul_f16 v242, v242, v250\n"
        "v_and_b32_dpp v124, v197, v25 row_ror:12 row_mask:0xf bank_mask:0xf\n"
        "v_mfma_f32_16x16x32_bf16 v[174:177], v[42:45], v[150:153], 0\n"
        "s_waitcnt lgkmcnt(0)\n"
        "ds_write_b16 v22, v242 offset:512\n"
        "v_mfma_f32_16x16x32_bf16 v[146:149], v[44:47], v[154:157], 0\n"
        "v_cmp_ne_u32_e64 s[24:25], s29, v251\n"
        "v_and_b32_dpp v125, v243, v25 row_ror:12 row_mask:0xf bank_mask:0xf\n"
        "v_mfma_f32_16x16x32_bf16 v[150:153], v[138:141], a[80:83], 0\n"
        "v_cvt_pk_bf16_f32 v36, v192, v193\n"
        "v_cvt_pk_bf16_f32 v37, v194, v195\n"
        "v_mfma_f32_16x16x32_bf16 v[150:153], v[132:135], a[76:79], v[150:153]\n"
        "s_cmp_lg_u64 s[24:25], 0\n"
        "v_cvt_pk_bf16_f32 v40, v142, v143\n"
        "v_cvt_pk_bf16_f32 v41, v144, v145\n"
        "v_mfma_f32_16x16x32_bf16 v[150:153], v[126:129], a[72:75], v[150:153]\n"
        "s_cbranch_scc1 SP10_%=\n"
        "BK10_%=:\n"
        "v_mov_b64 v[130:131], v[124:125]\n"
        "v_cvt_pk_bf16_f32 v238, v178, v179\n"
        "v_mfma_f32_16x16x32_bf16 v[192:195], v[126:129], a[84:87], 0\n"
        "v_cvt_pk_bf16_f32 v154, v180, v181\n"
        "v_cvt_pk_bf16_f32 v178, v186, v187\n"
        "v_mfma_f32_16x16x32_bf16 v[192:195], v[132:135], a[88:91], v[192:195]\n"
        "v_cvt_pk_bf16_f32 v239, v188, v189\n"
        "v_cvt_pk_bf16_f32 v155, v174, v175\n"
        "v_mfma_f32_16x16x32_bf16 v[192:195], v[138:141], a[92:95], v[192:195]\n"
        "v_cvt_pk_bf16_f32 v179, v176, v177\n"
        "v_cvt_pk_bf16_f32 v240, v146, v147\n"
        "v_mfma_f32_16x16x32_bf16 v[174:177], v[36:39], v[156:159], 0\n"
        "s_nop 0\n"
        "v_cvt_pk_bf16_f32 v30, v150, v151\n"
        "v_cvt_pk_bf16_f32 v31, v152, v153\n"
        "v_mfma_f32_16x16x32_bf16 v[142:145], v[38:41], v[160:163], 0\n"
        "s_nop 0\n"
        "v_cvt_pk_bf16_f32 v34, v192, v193\n"
        "v_cvt_pk_bf16_f32 v35, v194, v195\n"
        "v_mfma_f32_16x16x32_bf16 v[150:153], v[138:141], a[104:107], 0\n"
        "v_cvt_pk_bf16_f32 v241, v176, v177\n"
        "v_cvt_pk_bf16_f32 v156, v148, v149\n"
        "v_mfma_f32_16x16x32_bf16 v[150:153], v[132:135], a[100:103], v[150:153]\n"
        "ds_write_b128 v12, v[238:241]\n"
        "v_mfma_f32_16x16x32_bf16 v[150:153], v[126:129], a[96:99], v[150:153]\n"
        "v_cvt_pk_bf16_f32 v180, v174, v175\n"
        "v_cvt_pk_bf16_f32 v157, v142, v143\n"
        "v_mfma_f32_16x16x32_bf16 v[146:149], v[126:129], a[108:111], 0\n"
        "ds_write_b128 v12, v[154:157] offset:1024\n"
        "v_mfma_f32_16x16x32_bf16 v[146:149], v[132:135], a[112:115], v[146:149]\n"
        "v_cvt_pk_bf16_f32 v181, v144, v145\n"
        "v_and_b32 v251, 0xffff0000, v245\n"
        "v_mfma_f32_16x16x32_bf16 v[146:149], v[138:141], a[116:119], v[146:149]\n"
        "ds_write_b128 v12, v[178:181] offset:2048\n"
        "v_mfma_f32_16x16x32_bf16 v[158:161], v[30:33], v[124:127], 0\n"
        "s_nop 0\n"
        "v_mov_b64 v[136:137], v[124:125]\n"
        "v_cvt_pk_bf16_f32 v42, v150, v151\n"
        "v_mfma_f32_16x16x32_bf16 v[174:177], v[32:35], v[128:131], 0\n"
        "s_nop 0\n"
        "v_cvt_pk_bf16_f32 v43, v152, v153\n"
        "v_cvt_pk_bf16_f32 v46, v146, v147\n"
        "v_mfma_f32_16x16x32_bf16 v[150:153], v[238:241], v[238:241], 0\n"
        "v_cvt_pk_bf16_f32 v47, v148, v149\n"
        "v_mul_f32_dpp v250, v255, v252 quad_perm:[0,0,0,0] row_mask:0xf bank_mask:0xf\n"
        "v_mfma_f32_16x16x32_bf16 v[150:153], v[154:157], v[154:157], v[150:153]\n"
        "v_and_b32_dpp v104, v197, v25 row_ror:8 row_mask:0xf bank_mask:0xf\n"
        "v_and_b32_dpp v105, v243, v25 row_ror:8 row_mask:0xf bank_mask:0xf\n"
        "v_mfma_f32_16x16x32_bf16 v[150:153], v[178:181], v[178:181], v[150:153]\n"
        "v_mov_b64 v[142:143], v[124:125]\n"
        "v_mov_b64 v[110:111], v[104:105]\n"
        "v_mfma_f32_16x16x32_bf16 v[154:157], v[138:141], a[128:131], 0\n"
        "v_mov_b64 v[116:117], v[104:105]\n"
        "v_fmac_f32_dpp v250, v255, v254 quad_perm:[1,1,1,1] row_mask:0xf bank_mask:0xf\n"
        "v_mfma_f32_16x16x32_bf16 v[154:157], v[132:135], a[124:127], v[154:157]\n"
        "v_mov_b64 v[122:123], v[104:105]\n"
        "v_fmac_f32_dpp v250, v255, v248 quad_perm:[2,2,2,2] row_mask:0xf bank_mask:0xf\n"
        "v_mfma_f32_16x16x32_bf16 v[154:157], v[126:129], a[120:123], v[154:157]\n"
        "s_nop 0\n"
        "v_fmac_f32_dpp v250, v255, v249 quad_perm:[3,3,3,3] row_mask:0xf bank_mask:0xf\n"
        "v_add_f32 v250, v250, v253\n"
        "v_mfma_f32_16x16x32_bf16 v[178:181], v[126:129], a[132:135], 0\n"
        "v_cvt_pk_f16_f32 v146, v182, v183\n"
        "v_cvt_pk_f16_f32 v147, v184, v185\n"
        "v_mfma_f32_16x16x32_bf16 v[178:181], v[132:135], a[136:139], v[178:181]\n"
        "s_nop 0\n"
        "v_cvt_pk_bf16_f32 v36, v154, v155\n"
        "v_cvt_pk_bf16_f32 v37, v156, v157\n"
        "v_mfma_f32_16x16x32_bf16 v[178:181], v[138:141], a[140:143], v[178:181]\n"
        "v_cvt_pk_bf16_f32 v182, v158, v159\n"
        "v_cvt_pk_bf16_f32 v158, v160, v161\n"
        "v_mfma_f32_16x16x32_bf16 v[124:127], v[42:45], v[130:133], 0\n"
        "s_nop 0\n"
        "v_cvt_pk_bf16_f32 v128, v174, v175\n"
        "v_cvt_pk_bf16_f32 v183, v176, v177\n"
        "v_mfma_f32_16x16x32_bf16 v[130:133], v[44:47], v[134:137], 0\n"
        "s_nop 0\n"
        "v_cvt_pk_bf16_f32 v40, v178, v179\n"
        "v_cvt_pk_bf16_f32 v41, v180, v181\n"
        "v_mfma_f32_16x16x32_bf16 v[174:177], v[118:121], a[80:83], 0\n"
        "v_cvt_pk_bf16_f32 v159, v124, v125\n"
        "v_cvt_pk_bf16_f32 v129, v126, v127\n"
        "v_mfma_f32_16x16x32_bf16 v[174:177], v[112:115], a[76:79], v[174:177]\n"
        "s_nop 0\n"
        "v_cvt_pk_bf16_f32 v184, v130, v131\n"
        "v_cvt_pk_bf16_f32 v160, v132, v133\n"
        "v_mfma_f32_16x16x32_bf16 v[174:177], v[106:109], a[72:75], v[174:177]\n"
        "v_pk_mul_f16 v146, v146, v172 op_sel_hi:[1,0]\n"
        "v_pk_mul_f16 v147, v147, v172 op_sel_hi:[1,0]\n"
        "v_mfma_f32_16x16x32_bf16 v[154:157], v[106:109], a[84:87], 0\n"
        "s_nop 0\n"
        "v_pk_mul_f16 v146, v146, v214\n"
        "v_pk_mul_f16 v147, v147, v215\n"
        "v_mfma_f32_16x16x32_bf16 v[154:157], v[112:115], a[88:91], v[154:157]\n"
        "s_nop 0\n"
        "v_cvt_pk_bf16_f32 v30, v174, v175\n"
        "v_cvt_pk_bf16_f32 v31, v176, v177\n"
        "v_mfma_f32_16x16x32_bf16 v[154:157], v[118:121], a[92:95], v[154:157]\n"
        "v_exp_f16_sdwa v146, v146 dst_sel:WORD_0 dst_unused:UNUSED_PRESERVE src0_sel:WORD_0\n"
        "v_mfma_f32_16x16x32_bf16 v[132:135], v[36:39], v[136:139], 0\n"
        "v_exp_f16_sdwa v147, v147 dst_sel:WORD_0 dst_unused:UNUSED_PRESERVE src0_sel:WORD_0\n"
        "v_mfma_f32_16x16x32_bf16 v[124:127], v[38:41], v[140:143], 0\n"
        "s_nop 0\n"
        "v_exp_f16_sdwa v146, v146 dst_sel:WORD_1 dst_unused:UNUSED_PRESERVE src0_sel:WORD_1\n"
        "v_mfma_f32_16x16x32_bf16 v[140:143], v[118:121], a[104:107], 0\n"
        "s_nop 0\n"
        "v_cvt_pk_bf16_f32 v34, v154, v155\n"
        "v_cvt_pk_bf16_f32 v35, v156, v157\n"
        "v_mfma_f32_16x16x32_bf16 v[140:143], v[112:115], a[100:103], v[140:143]\n"
        "v_cvt_pk_bf16_f32 v185, v134, v135\n"
        "v_cvt_pk_bf16_f32 v130, v132, v133\n"
        "v_mfma_f32_16x16x32_bf16 v[140:143], v[106:109], a[96:99], v[140:143]\n"
        "ds_write_b128 v12, v[182:185] offset:3072\n"
        "v_mfma_f32_16x16x32_bf16 v[132:135], v[106:109], a[108:111], 0\n"
        "v_cvt_pk_bf16_f32 v161, v124, v125\n"
        "v_cvt_pk_bf16_f32 v131, v126, v127\n"
        "v_mfma_f32_16x16x32_bf16 v[132:135], v[112:115], a[112:115], v[132:135]\n"
        "ds_write_b128 v12, v[158:161] offset:4096\n"
        "v_mfma_f32_16x16x32_bf16 v[132:135], v[118:121], a[116:119], v[132:135]\n"
        "s_nop 0\n"
        "ds_write_b128 v12, v[128:131] offset:5120\n"
        "v_mfma_f32_16x16x32_bf16 v[136:139], v[30:33], v[104:107], 0\n"
        "s_nop 0\n"
        "v_cvt_pk_bf16_f32 v42, v140, v141\n"
        "v_cvt_pk_bf16_f32 v43, v142, v143\n"
        "v_mfma_f32_16x16x32_bf16 v[140:143], v[32:35], v[108:111], 0\n"
        "s_nop 0\n"
        "v_cvt_pk_bf16_f32 v46, v132, v133\n"
        "v_cvt_pk_bf16_f32 v47, v134, v135\n"
        "v_mfma_f32_16x16x32_bf16 v[132:135], v[182:185], v[182:185], 0\n"
        "v_exp_f16_sdwa v147, v147 dst_sel:WORD_1 dst_unused:UNUSED_PRESERVE src0_sel:WORD_1\n"
        "v_mfma_f32_16x16x32_bf16 v[132:135], v[158:161], v[158:161], v[132:135]\n"
        "v_mul_f32 v255, s80, v250\n"
        "v_pk_add_f16 v126, v146, v147\n"
        "v_mfma_f32_16x16x32_bf16 v[132:135], v[128:131], v[128:131], v[132:135]\n"
        "v_cvt_pk_f16_f32 v148, v164, v165\n"
        "v_cvt_pk_f16_f32 v149, v166, v167\n"
        "v_mfma_f32_16x16x32_bf16 v[158:161], v[118:121], a[128:131], 0\n"
        "v_pk_mul_f16 v148, v148, v172 op_sel_hi:[1,0]\n"
        "v_pk_mul_f16 v149, v149, v172 op_sel_hi:[1,0]\n"
        "v_mfma_f32_16x16x32_bf16 v[158:161], v[112:115], a[124:127], v[158:161]\n"
        "v_pk_mul_f16 v148, v148, v216\n"
        "v_pk_mul_f16 v149, v149, v217\n"
        "v_mfma_f32_16x16x32_bf16 v[158:161], v[106:109], a[120:123], v[158:161]\n"
        "s_nop 0\n"
        "v_exp_f16_sdwa v148, v148 dst_sel:WORD_0 dst_unused:UNUSED_PRESERVE src0_sel:WORD_0\n"
        "v_mfma_f32_16x16x32_bf16 v[154:157], v[106:109], a[132:135], 0\n"
        "s_nop 0\n"
        "v_cvt_pk_bf16_f32 v104, v136, v137\n"
        "v_cvt_pk_bf16_f32 v128, v138, v139\n"
        "v_mfma_f32_16x16x32_bf16 v[154:157], v[112:115], a[136:139], v[154:157]\n"
        "s_nop 0\n"
        "v_cvt_pk_bf16_f32 v36, v158, v159\n"
        "v_cvt_pk_bf16_f32 v37, v160, v161\n"
        "v_mfma_f32_16x16x32_bf16 v[154:157], v[118:121], a[140:143], v[154:157]\n"
        "s_nop 0\n"
        "v_cvt_pk_bf16_f32 v136, v140, v141\n"
        "v_cvt_pk_bf16_f32 v105, v142, v143\n"
        "v_mfma_f32_16x16x32_bf16 v[106:109], v[42:45], v[110:113], 0\n"
        "s_nop 0\n"
        "v_exp_f16_sdwa v149, v149 dst_sel:WORD_0 dst_unused:UNUSED_PRESERVE src0_sel:WORD_0\n"
        "v_mfma_f32_16x16x32_bf16 v[138:141], v[44:47], v[114:117], 0\n"
        "s_nop 0\n"
        "v_cvt_pk_bf16_f32 v40, v154, v155\n"
        "v_cvt_pk_bf16_f32 v41, v156, v157\n"
        "v_mfma_f32_16x16x32_bf16 v[112:115], a[152:155], v[230:233], 0\n"
        "s_nop 0\n"
        "v_cvt_pk_bf16_f32 v129, v106, v107\n"
        "v_cvt_pk_bf16_f32 v137, v108, v109\n"
        "v_mfma_f32_16x16x32_bf16 v[112:115], a[148:151], v[234:237], v[112:115]\n"
        "v_cvt_pk_bf16_f32 v106, v138, v139\n"
        "v_cvt_pk_bf16_f32 v130, v140, v141\n"
        "v_mfma_f32_16x16x32_bf16 v[112:115], a[144:147], v[226:229], v[112:115]\n"
        "v_exp_f16_sdwa v148, v148 dst_sel:WORD_1 dst_unused:UNUSED_PRESERVE src0_sel:WORD_1\n"
        "v_mfma_f32_16x16x32_bf16 v[108:111], v[36:39], v[116:119], 0\n"
        "s_nop 0\n"
        "v_exp_f16_sdwa v149, v149 dst_sel:WORD_1 dst_unused:UNUSED_PRESERVE src0_sel:WORD_1\n"
        "v_mfma_f32_16x16x32_bf16 v[34:37], v[38:41], v[120:123], 0\n"
        "s_nop 0\n"
        "v_pk_add_f16 v126, v126, v148\n"
        "v_cndmask_b32_e64 v254, v150, v151, s[56:57]\n"
        "v_mfma_f32_16x16x32_bf16 v[40:43], a[156:159], v[226:229], 0\n"
        "s_nop 0\n"
        "v_cvt_pk_bf16_f32 v107, v110, v111\n"
        "v_cvt_pk_bf16_f32 v138, v108, v109\n"
        "v_mfma_f32_16x16x32_bf16 v[40:43], a[160:163], v[234:237], v[40:43]\n"
        "ds_write_b128 v12, v[104:107] offset:6144\n"
        "v_mfma_f32_16x16x32_bf16 v[40:43], a[164:167], v[230:233], v[40:43]\n"
        "v_cvt_pk_bf16_f32 v131, v34, v35\n"
        "v_cvt_pk_bf16_f32 v139, v36, v37\n"
        "v_mfma_f32_16x16x32_bf16 v[108:111], a[176:179], v[230:233], 0\n"
        "ds_write_b128 v12, v[128:131] offset:7168\n"
        "v_mfma_f32_16x16x32_bf16 v[108:111], a[172:175], v[234:237], v[108:111]\n"
        "ds_write_b128 v12, v[136:139] offset:8192\n"
        "v_mfma_f32_16x16x32_bf16 v[108:111], a[168:171], v[226:229], v[108:111]\n"
        "v_cndmask_b32_e64 v254, v254, v152, s[58:59]\n"
        "v_cndmask_b32_e64 v252, v132, v133, s[56:57]\n"
        "v_mfma_f32_16x16x32_bf16 v[116:119], v[104:107], v[104:107], 0\n"
        "v_cndmask_b32_e64 v252, v252, v134, s[58:59]\n"
        "v_cndmask_b32_e64 v252, v252, v135, s[60:61]\n"
        "v_mfma_f32_16x16x32_bf16 v[116:119], v[128:131], v[128:131], v[116:119]\n"
        "v_cvt_pk_bf16_f32 v34, v112, v113\n"
        "v_cvt_pk_bf16_f32 v35, v114, v115\n"
        "v_mfma_f32_16x16x32_bf16 v[116:119], v[136:139], v[136:139], v[116:119]\n"
        "v_cvt_pk_bf16_f32 v36, v40, v41\n"
        "v_cvt_pk_bf16_f32 v37, v42, v43\n"
        "v_mfma_f32_16x16x32_bf16 v[104:107], a[188:191], v[230:233], 0\n"
        "s_nop 0\n"
        "v_cvt_pk_bf16_f32 v40, v108, v109\n"
        "v_cvt_pk_bf16_f32 v41, v110, v111\n"
        "v_mfma_f32_16x16x32_bf16 v[104:107], a[184:187], v[234:237], v[104:107]\n"
        "s_nop 0\n"
        "v_cndmask_b32_e64 v253, v116, v117, s[56:57]\n"
        "v_cndmask_b32_e64 v253, v253, v118, s[58:59]\n"
        "v_mfma_f32_16x16x32_bf16 v[104:107], a[180:183], v[226:229], v[104:107]\n"
        "v_cndmask_b32_e64 v253, v253, v119, s[60:61]\n"
        "v_cndmask_b32_e64 v254, v254, v153, s[60:61]\n"
        "v_mfma_f32_16x16x32_bf16 v[110:113], a[192:195], v[226:229], 0\n"
        "s_nop 0\n"
        "v_cndmask_b32_dpp v252, v253, v252, vcc row_ror:4 row_mask:0xf bank_mask:0xf\n"
        "v_pk_add_f16 v126, v126, v149\n"
        "v_mfma_f32_16x16x32_bf16 v[110:113], a[196:199], v[234:237], v[110:113]\n"
        "s_nop 0\n"
        "v_cvt_pk_bf16_f32 v42, v104, v105\n"
        "v_cvt_pk_bf16_f32 v43, v106, v107\n"
        "v_mfma_f32_16x16x32_bf16 v[110:113], a[200:203], v[230:233], v[110:113]\n"
        "v_cndmask_b32_dpp v254, v252, v254, vcc row_ror:4 row_mask:0xf bank_mask:0xf\n"
        "v_fma_f32 v255, -v255, v250, v254\n"
        "v_mfma_f32_16x16x32_bf16 v[116:119], a[212:215], v[230:233], 0\n"
        "s_nop 0\n"
        "v_mov_b32 v253, s29\n"
        "v_max_f32 v255, 0, v255\n"
        "v_mfma_f32_16x16x32_bf16 v[116:119], a[208:211], v[234:237], v[116:119]\n"
        "s_nop 0\n"
        "v_cvt_pk_bf16_f32 v108, v110, v111\n"
        "v_cvt_pk_bf16_f32 v109, v112, v113\n"
        "v_mfma_f32_16x16x32_bf16 v[116:119], a[204:207], v[226:229], v[116:119]\n"
        "v_sub_f32 v254, 0x47c00000, v254\n"
        "v_fmaak_f32 v255, v28, v255, 0x3727c5ac\n"
        "v_mfma_f32_16x16x32_bf16 v[104:107], a[144:147], v[202:205], 0\n"
        "s_nop 0\n"
        "v_bfi_b32 v255, s84, v254, v255\n"
        "v_cvt_pk_f16_f32 v114, v168, v169\n"
        "v_mfma_f32_16x16x32_bf16 v[104:107], a[148:151], v[210:213], v[104:107]\n"
        "s_nop 0\n"
        "v_cvt_pk_bf16_f32 v110, v116, v117\n"
        "v_cvt_pk_bf16_f32 v111, v118, v119\n"
        "v_mfma_f32_16x16x32_bf16 v[104:107], a[152:155], v[198:201], v[104:107]\n"
        "v_rsq_f32 v254, v255\n"
        "v_mfma_f32_16x16x32_bf16 v[136:139], v[222:225], v[34:37], 0\n"
        "v_cvt_f16_f32 v254, v254\n"
        "v_cvt_pk_f16_f32 v115, v170, v171\n"
        "v_mfma_f32_16x16x32_bf16 v[136:139], v[206:209], v[40:43], v[136:139]\n"
        "ds_write_b16 v18, v254\n"
        "v_mfma_f32_16x16x32_bf16 v[136:139], v[218:221], v[108:111], v[136:139]\n"
        "ds_write_b16 v19, v254\n"
        "v_mfma_f32_16x16x32_bf16 v[116:119], v[230:233], v[108:111], 0\n"
        "ds_write_b16 v22, v253 offset:3072\n"
        "v_mfma_f32_16x16x32_bf16 v[116:119], v[234:237], v[40:43], v[116:119]\n"
        "v_pk_mul_f16 v114, v114, v172 op_sel_hi:[1,0]\n"
        "v_pk_mul_f16 v115, v115, v172 op_sel_hi:[1,0]\n"
        "v_mfma_f32_16x16x32_bf16 v[116:119], v[226:229], v[34:37], v[116:119]\n"
        "v_pk_mul_f16 v114, v114, v246\n"
        "v_pk_mul_f16 v115, v115, v247\n"
        "v_mfma_f32_16x16x32_bf16 v[120:123], v[202:205], v[34:37], 0\n"
        "v_exp_f16_sdwa v114, v114 dst_sel:WORD_0 dst_unused:UNUSED_PRESERVE src0_sel:WORD_0\n"
        "v_mfma_f32_16x16x32_bf16 v[120:123], v[210:213], v[40:43], v[120:123]\n"
        "v_exp_f16_sdwa v115, v115 dst_sel:WORD_0 dst_unused:UNUSED_PRESERVE src0_sel:WORD_0\n"
        "v_mfma_f32_16x16x32_bf16 v[120:123], v[198:201], v[108:111], v[120:123]\n"
        "v_exp_f16_sdwa v114, v114 dst_sel:WORD_1 dst_unused:UNUSED_PRESERVE src0_sel:WORD_1\n"
        "v_mfma_f32_16x16x32_bf16 v[34:37], a[164:167], v[198:201], 0\n"
        "v_exp_f16_sdwa v115, v115 dst_sel:WORD_1 dst_unused:UNUSED_PRESERVE src0_sel:WORD_1\n"
        "v_mfma_f32_16x16x32_bf16 v[34:37], a[160:163], v[210:213], v[34:37]\n"
        "v_pk_add_f16 v126, v126, v114\n"
        "v_pk_add_f16 v126, v126, v115\n"
        "v_mfma_f32_16x16x32_bf16 v[34:37], a[156:159], v[202:205], v[34:37]\n"
        "v_cvt_pk_bf16_f32 v108, v104, v105\n"
        "v_cvt_pk_f16_f32 v130, v136, v137\n"
        "v_mfma_f32_16x16x32_bf16 v[134:137], a[168:171], v[202:205], 0\n"
        "v_cvt_pk_f16_f32 v131, v138, v139\n"
        "v_pk_mul_f16 v130, v130, v172 op_sel:[0,1]\n"
        "v_mfma_f32_16x16x32_bf16 v[134:137], a[172:175], v[210:213], v[134:137]\n"
        "v_pk_mul_f16 v131, v131, v172 op_sel:[0,1]\n"
        "v_pk_mul_f16 v130, v130, v214\n"
        "v_mfma_f32_16x16x32_bf16 v[134:137], a[176:179], v[198:201], v[134:137]\n"
        "v_pk_mul_f16 v131, v131, v215\n"
        "v_cvt_pk_bf16_f32 v109, v106, v107\n"
        "v_mfma_f32_16x16x32_bf16 v[104:107], a[188:191], v[198:201], 0\n"
        "v_exp_f16_sdwa v130, v130 dst_sel:WORD_0 dst_unused:UNUSED_PRESERVE src0_sel:WORD_0\n"
        "v_mfma_f32_16x16x32_bf16 v[104:107], a[184:187], v[210:213], v[104:107]\n"
        "v_exp_f16_sdwa v131, v131 dst_sel:WORD_0 dst_unused:UNUSED_PRESERVE src0_sel:WORD_0\n"
        "v_mfma_f32_16x16x32_bf16 v[104:107], a[180:183], v[202:205], v[104:107]\n"
        "v_exp_f16_sdwa v130, v130 dst_sel:WORD_1 dst_unused:UNUSED_PRESERVE src0_sel:WORD_1\n"
        "v_mfma_f32_16x16x32_bf16 v[40:43], a[192:195], v[202:205], 0\n"
        "v_exp_f16_sdwa v131, v131 dst_sel:WORD_1 dst_unused:UNUSED_PRESERVE src0_sel:WORD_1\n"
        "v_mfma_f32_16x16x32_bf16 v[40:43], a[196:199], v[210:213], v[40:43]\n"
        "v_pk_add_f16 v127, v130, v131\n"
        "v_cvt_pk_f16_f32 v132, v116, v117\n"
        "v_mfma_f32_16x16x32_bf16 v[40:43], a[200:203], v[198:201], v[40:43]\n"
        "v_cvt_pk_f16_f32 v133, v118, v119\n"
        "v_pk_mul_f16 v132, v132, v172 op_sel:[0,1]\n"
        "v_mfma_f32_16x16x32_bf16 v[116:119], a[212:215], v[198:201], 0\n"
        "v_pk_mul_f16 v133, v133, v172 op_sel:[0,1]\n"
        "v_cvt_pk_bf16_f32 v110, v34, v35\n"
        "v_mfma_f32_16x16x32_bf16 v[116:119], a[208:211], v[210:213], v[116:119]\n"
        "v_cvt_pk_bf16_f32 v111, v36, v37\n"
        "v_cvt_pk_bf16_f32 v34, v134, v135\n"
        "v_mfma_f32_16x16x32_bf16 v[116:119], a[204:207], v[202:205], v[116:119]\n"
        "v_cvt_pk_bf16_f32 v35, v136, v137\n"
        "v_cvt_pk_bf16_f32 v36, v104, v105\n"
        "v_mfma_f32_16x16x32_bf16 v[134:137], v[222:225], a[216:219], 0\n"
        "v_cvt_pk_bf16_f32 v37, v106, v107\n"
        "v_cvt_pk_bf16_f32 v104, v40, v41\n"
        "v_mfma_f32_16x16x32_bf16 v[134:137], v[206:209], a[220:223], v[134:137]\n"
        "s_nop 0\n"
        "v_cvt_pk_bf16_f32 v105, v42, v43\n"
        "v_cvt_pk_bf16_f32 v106, v116, v117\n"
        "v_mfma_f32_16x16x32_bf16 v[134:137], v[218:221], a[224:227], v[134:137]\n"
        "v_cvt_pk_bf16_f32 v107, v118, v119\n"
        "v_pk_mul_f16 v132, v132, v216\n"
        "v_mfma_f32_16x16x32_bf16 v[40:43], v[230:233], a[224:227], 0\n"
        "v_pk_mul_f16 v133, v133, v217\n"
        "v_cvt_pk_f16_f32 v118, v120, v121\n"
        "v_mfma_f32_16x16x32_bf16 v[40:43], v[234:237], a[220:223], v[40:43]\n"
        "v_exp_f16_sdwa v132, v132 dst_sel:WORD_0 dst_unused:UNUSED_PRESERVE src0_sel:WORD_0\n"
        "v_mfma_f32_16x16x32_bf16 v[40:43], v[226:229], a[216:219], v[40:43]\n"
        "v_exp_f16_sdwa v133, v133 dst_sel:WORD_0 dst_unused:UNUSED_PRESERVE src0_sel:WORD_0\n"
        "v_mfma_f32_16x16x32_bf16 v[138:141], v[202:205], a[216:219], 0\n"
        "v_exp_f16_sdwa v132, v132 dst_sel:WORD_1 dst_unused:UNUSED_PRESERVE src0_sel:WORD_1\n"
        "v_mfma_f32_16x16x32_bf16 v[138:141], v[210:213], a[220:223], v[138:141]\n"
        "v_exp_f16_sdwa v133, v133 dst_sel:WORD_1 dst_unused:UNUSED_PRESERVE src0_sel:WORD_1\n"
        "v_mfma_f32_16x16x32_bf16 v[138:141], v[198:201], a[224:227], v[138:141]\n"
        "v_pk_add_f16 v127, v127, v132\n"
        "v_pk_add_f16 v127, v127, v133\n"
        "v_mfma_f32_16x16x32_bf16 v[150:153], v[222:225], v[108:111], 0\n"
        "v_cvt_pk_f16_f32 v119, v122, v123\n"
        "v_pk_mul_f16 v118, v118, v172 op_sel:[0,1]\n"
        "v_mfma_f32_16x16x32_bf16 v[150:153], v[206:209], v[34:37], v[150:153]\n"
        "v_pk_mul_f16 v119, v119, v172 op_sel:[0,1]\n"
        "v_pk_mul_f16 v118, v118, v246\n"
        "v_mfma_f32_16x16x32_bf16 v[150:153], v[218:221], v[104:107], v[150:153]\n"
        "v_pk_mul_f16 v119, v119, v247\n"
        "v_cvt_pk_f16_f32 v142, v134, v135\n"
        "v_mfma_f32_16x16x32_bf16 v[154:157], v[230:233], v[104:107], 0\n"
        "v_cndmask_b32_e64 v116, 0, v251, s[62:63]\n"
        "v_cndmask_b32_e64 v120, 0, v251, s[64:65]\n"
        "v_mfma_f32_16x16x32_bf16 v[154:157], v[234:237], v[34:37], v[154:157]\n"
        "v_pk_add_f16 v126, v126, v116\n"
        "v_cvt_pk_f16_f32 v143, v136, v137\n"
        "v_mfma_f32_16x16x32_bf16 v[154:157], v[226:229], v[108:111], v[154:157]\n"
        "v_exp_f16_sdwa v118, v118 dst_sel:WORD_0 dst_unused:UNUSED_PRESERVE src0_sel:WORD_0\n"
        "v_mfma_f32_16x16x32_bf16 v[134:137], v[202:205], v[108:111], 0\n"
        "v_mov_b64 v[128:129], 0\n"
        "v_cvt_pk_f16_f32 v144, v40, v41\n"
        "v_mfma_f32_16x16x32_bf16 v[134:137], v[210:213], v[34:37], v[134:137]\n"
        "v_exp_f16_sdwa v119, v119 dst_sel:WORD_0 dst_unused:UNUSED_PRESERVE src0_sel:WORD_0\n"
        "v_mfma_f32_16x16x32_bf16 v[134:137], v[198:201], v[104:107], v[134:137]\n"
        "v_exp_f16_sdwa v118, v118 dst_sel:WORD_1 dst_unused:UNUSED_PRESERVE src0_sel:WORD_1\n"
        "v_mfma_f32_16x16x32_bf16 v[108:111], v[198:201], a[236:239], 0\n"
        "v_exp_f16_sdwa v119, v119 dst_sel:WORD_1 dst_unused:UNUSED_PRESERVE src0_sel:WORD_1\n"
        "v_mfma_f32_16x16x32_bf16 v[108:111], v[210:213], a[232:235], v[108:111]\n"
        "v_pk_add_f16 v127, v127, v118\n"
        "v_pk_add_f16 v127, v127, v119\n"
        "v_mfma_f32_16x16x32_bf16 v[108:111], v[202:205], a[228:231], v[108:111]\n"
        "v_pk_add_f16 v127, v127, v120\n"
        "v_cvt_pk_f16_f32 v145, v42, v43\n"
        "v_mfma_f32_16x16x32_bf16 v[104:107], v[226:229], a[228:231], 0\n"
        "v_cvt_pk_f16_f32 v122, v138, v139\n"
        "v_cvt_pk_f16_f32 v123, v140, v141\n"
        "v_mfma_f32_16x16x32_bf16 v[104:107], v[234:237], a[232:235], v[104:107]\n"
        "v_cvt_pk_f16_f32 v238, v150, v151\n"
        "v_cvt_pk_f16_f32 v239, v152, v153\n"
        "v_mfma_f32_16x16x32_bf16 v[104:107], v[230:233], a[236:239], v[104:107]\n"
        "v_pk_mul_f16 v238, v238, v173 op_sel_hi:[1,0]\n"
        "v_pk_mul_f16 v239, v239, v173 op_sel_hi:[1,0]\n"
        "v_mfma_f32_16x16x32_bf16 v[34:37], v[218:221], a[236:239], 0\n"
        "v_pk_mul_f16 v238, v238, v214\n"
        "v_pk_mul_f16 v239, v239, v215\n"
        "v_mfma_f32_16x16x32_bf16 v[34:37], v[206:209], a[232:235], v[34:37]\n"
        "v_exp_f16_sdwa v238, v238 dst_sel:WORD_0 dst_unused:UNUSED_PRESERVE src0_sel:WORD_0\n"
        "v_mfma_f32_16x16x32_bf16 v[34:37], v[222:225], a[228:231], v[34:37]\n"
        "v_exp_f16_sdwa v239, v239 dst_sel:WORD_0 dst_unused:UNUSED_PRESERVE src0_sel:WORD_0\n"
        "v_mfma_f32_16x16x32_bf16 v[166:169], v[222:225], a[240:243], 0\n"
        "v_exp_f16_sdwa v238, v238 dst_sel:WORD_1 dst_unused:UNUSED_PRESERVE src0_sel:WORD_1\n"
        "v_mfma_f32_16x16x32_bf16 v[166:169], v[206:209], a[244:247], v[166:169]\n"
        "v_exp_f16_sdwa v239, v239 dst_sel:WORD_1 dst_unused:UNUSED_PRESERVE src0_sel:WORD_1\n"
        "v_mfma_f32_16x16x32_bf16 v[166:169], v[218:221], a[248:251], v[166:169]\n"
        "v_cvt_pk_f16_f32 v240, v154, v155\n"
        "v_cvt_pk_f16_f32 v241, v156, v157\n"
        "v_mfma_f32_16x16x32_bf16 v[40:43], v[230:233], a[248:251], 0\n"
        "v_pk_mul_f16 v240, v240, v173 op_sel_hi:[1,0]\n"
        "v_pk_mul_f16 v241, v241, v173 op_sel_hi:[1,0]\n"
        "v_mfma_f32_16x16x32_bf16 v[40:43], v[234:237], a[244:247], v[40:43]\n"
        "v_pk_mul_f16 v240, v240, v216\n"
        "v_pk_mul_f16 v241, v241, v217\n"
        "v_mfma_f32_16x16x32_bf16 v[40:43], v[226:229], a[240:243], v[40:43]\n"
        "v_exp_f16_sdwa v240, v240 dst_sel:WORD_0 dst_unused:UNUSED_PRESERVE src0_sel:WORD_0\n"
        "v_mfma_f32_16x16x32_bf16 v[158:161], v[202:205], a[240:243], 0\n"
        "v_exp_f16_sdwa v241, v241 dst_sel:WORD_0 dst_unused:UNUSED_PRESERVE src0_sel:WORD_0\n"
        "v_mfma_f32_16x16x32_bf16 v[158:161], v[210:213], a[244:247], v[158:161]\n"
        "v_exp_f16_sdwa v240, v240 dst_sel:WORD_1 dst_unused:UNUSED_PRESERVE src0_sel:WORD_1\n"
        "v_mfma_f32_16x16x32_bf16 v[158:161], v[198:201], a[248:251], v[158:161]\n"
        "v_exp_f16_sdwa v241, v241 dst_sel:WORD_1 dst_unused:UNUSED_PRESERVE src0_sel:WORD_1\n"
        "v_mfma_f32_16x16x32_f16 v[150:153], v[56:59], v[126:129], 0\n"
        "v_cvt_pk_f16_f32 v242, v134, v135\n"
        "v_cvt_pk_f16_f32 v243, v136, v137\n"
        "v_mfma_f32_16x16x32_bf16 v[154:157], v[198:201], v[52:55], 0\n"
        "v_pk_mul_f16 v242, v242, v173 op_sel_hi:[1,0]\n"
        "v_pk_mul_f16 v243, v243, v173 op_sel_hi:[1,0]\n"
        "v_mfma_f32_16x16x32_bf16 v[154:157], v[210:213], v[92:95], v[154:157]\n"
        "v_pk_mul_f16 v242, v242, v246\n"
        "v_pk_mul_f16 v243, v243, v247\n"
        "v_mfma_f32_16x16x32_bf16 v[154:157], v[202:205], a[252:255], v[154:157]\n"
        "v_exp_f16_sdwa v242, v242 dst_sel:WORD_0 dst_unused:UNUSED_PRESERVE src0_sel:WORD_0\n"
        "v_mfma_f32_16x16x32_bf16 v[134:137], v[226:229], a[252:255], 0\n"
        "v_cndmask_b32_e64 v244, 0, v251, s[66:67]\n"
        "v_cvt_pk_f16_f32 v174, v108, v109\n"
        "v_mfma_f32_16x16x32_bf16 v[134:137], v[234:237], v[92:95], v[134:137]\n"
        "ds_read2_b32 v[124:125], v24 offset0:0 offset1:0\n"
        "ds_read2_b32 v[176:177], v24 offset0:1 offset1:1\n"
        "v_mfma_f32_16x16x32_bf16 v[134:137], v[230:233], v[52:55], v[134:137]\n"
        "ds_read2_b32 v[196:197], v24 offset0:2 offset1:2\n"
        "ds_read2_b32 v[192:193], v24 offset0:3 offset1:3\n"
        "v_mfma_f32_16x16x32_bf16 v[138:141], v[218:221], v[52:55], 0\n"
        "ds_read2_b32 v[172:173], v24 offset0:4 offset1:4\n"
        "v_mov_b32 v117, 0\n"
        "v_mfma_f32_16x16x32_bf16 v[138:141], v[206:209], v[92:95], v[138:141]\n"
        "ds_read2_b32 v[188:189], v24 offset0:5 offset1:5\n"
        "v_cvt_pk_f16_f32 v175, v110, v111\n"
        "v_mfma_f32_16x16x32_bf16 v[138:141], v[222:225], a[252:255], v[138:141]\n"
        "v_exp_f16_sdwa v243, v243 dst_sel:WORD_0 dst_unused:UNUSED_PRESERVE src0_sel:WORD_0\n"
        "v_mfma_f32_16x16x32_bf16 v[184:187], v[222:225], v[64:67], 0\n"
        "v_exp_f16_sdwa v242, v242 dst_sel:WORD_1 dst_unused:UNUSED_PRESERVE src0_sel:WORD_1\n"
        "v_mfma_f32_16x16x32_bf16 v[184:187], v[206:209], v[84:87], v[184:187]\n"
        "v_exp_f16_sdwa v243, v243 dst_sel:WORD_1 dst_unused:UNUSED_PRESERVE src0_sel:WORD_1\n"
        "v_mfma_f32_16x16x32_bf16 v[184:187], v[218:221], v[76:79], v[184:187]\n"
        "v_cvt_pk_f16_f32 v164, v104, v105\n"
        "v_cvt_pk_f16_f32 v165, v106, v107\n"
        "v_mfma_f32_16x16x32_bf16 v[248:251], v[230:233], v[76:79], 0\n"
        "v_cvt_pk_f16_f32 v162, v34, v35\n"
        "v_cvt_pk_f16_f32 v163, v36, v37\n"
        "v_mfma_f32_16x16x32_bf16 v[248:251], v[234:237], v[84:87], v[248:251]\n"
        "v_cvt_pk_f16_f32 v178, v166, v167\n"
        "v_cvt_pk_f16_f32 v179, v168, v169\n"
        "v_mfma_f32_16x16x32_bf16 v[248:251], v[226:229], v[64:67], v[248:251]\n"
        "v_cvt_pk_f16_f32 v180, v40, v41\n"
        "v_cvt_pk_f16_f32 v181, v42, v43\n"
        "v_mfma_f32_16x16x32_bf16 v[252:255], v[202:205], v[64:67], 0\n"
        "s_waitcnt lgkmcnt(0)\n"
        "v_pk_add_f16 v128, v238, v239\n"
        "v_pk_add_f16 v128, v128, v240\n"
        "v_mfma_f32_16x16x32_bf16 v[252:255], v[210:213], v[84:87], v[252:255]\n"
        "v_pk_add_f16 v128, v128, v241\n"
        "v_pk_add_f16 v128, v128, v242\n"
        "v_mfma_f32_16x16x32_bf16 v[252:255], v[198:201], v[76:79], v[252:255]\n"
        "v_cvt_pk_f16_f32 v194, v158, v159\n"
        "v_cvt_pk_f16_f32 v195, v160, v161\n"
        "v_mfma_f32_16x16x32_bf16 v[40:43], v[198:201], v[48:51], 0\n"
        "v_pk_add_f16 v128, v128, v243\n"
        "v_pk_add_f16 v128, v128, v244\n"
        "v_mfma_f32_16x16x32_bf16 v[40:43], v[210:213], v[72:75], v[40:43]\n"
        "v_rcp_f32 v190, v150\n"
        "v_mfma_f32_16x16x32_bf16 v[40:43], v[202:205], v[60:63], v[40:43]\n"
        "v_cvt_f16_f32 v245, v190\n"
        "v_pk_mul_f16 v146, v146, v245 op_sel_hi:[1,0]\n"
        "v_mfma_f32_16x16x32_bf16 v[104:107], v[226:229], v[60:63], 0\n"
        "v_pk_mul_f16 v147, v147, v245 op_sel_hi:[1,0]\n"
        "v_pk_mul_f16 v148, v148, v245 op_sel_hi:[1,0]\n"
        "v_mfma_f32_16x16x32_bf16 v[104:107], v[234:237], v[72:75], v[104:107]\n"
        "v_pk_mul_f16 v149, v149, v245 op_sel_hi:[1,0]\n"
        "v_pk_mul_f16 v114, v114, v245 op_sel_hi:[1,0]\n"
        "v_mfma_f32_16x16x32_bf16 v[104:107], v[230:233], v[48:51], v[104:107]\n"
        "v_pk_mul_f16 v115, v115, v245 op_sel_hi:[1,0]\n"
        "v_pk_mul_f16 v116, v116, v245 op_sel_hi:[1,0]\n"
        "v_mfma_f32_16x16x32_bf16 v[34:37], v[218:221], v[48:51], 0\n"
        "v_mov_b32 v121, 0\n"
        "v_pk_mul_f16 v146, v146, v214\n"
        "v_mfma_f32_16x16x32_bf16 v[34:37], v[206:209], v[72:75], v[34:37]\n"
        "v_pk_mul_f16 v147, v147, v215\n"
        "v_pk_mul_f16 v148, v148, v216\n"
        "v_mfma_f32_16x16x32_bf16 v[34:37], v[222:225], v[60:63], v[34:37]\n"
        "v_pk_mul_f16 v149, v149, v217\n"
        "v_pk_mul_f16 v114, v114, v246\n"
        "v_mfma_f32_16x16x32_f16 v[110:113], v[56:59], v[126:129], 0\n"
        "v_pk_mul_f16 v115, v115, v247\n"
        "v_max3_f32 v245, s95, v150, v151\n"
        "v_mfma_f32_16x16x32_f16 v[126:129], v[146:149], v[142:145], 0\n"
        "v_rcp_f32 v183, v151\n"
        "v_mfma_f32_16x16x32_f16 v[126:129], v[114:117], v[122:125], v[126:129]\n"
        "v_add_f32 v47, v191, v190\n"
        "v_cvt_f16_f32 v191, v183\n"
        "v_mfma_f32_16x16x32_bf16 v[126:129], v[222:225], v[88:91], v[126:129]\n"
        "v_add_f32 v47, v47, v183\n"
        "v_pk_mul_f16 v130, v130, v191 op_sel_hi:[1,0]\n"
        "v_mfma_f32_16x16x32_bf16 v[158:161], v[222:225], v[80:83], 0\n"
        "v_pk_mul_f16 v131, v131, v191 op_sel_hi:[1,0]\n"
        "v_pk_mul_f16 v132, v132, v191 op_sel_hi:[1,0]\n"
        "v_mfma_f32_16x16x32_f16 v[158:161], v[114:117], v[174:177], v[158:161]\n"
        "v_pk_mul_f16 v133, v133, v191 op_sel_hi:[1,0]\n"
        "v_pk_mul_f16 v118, v118, v191 op_sel_hi:[1,0]\n"
        "v_mfma_f32_16x16x32_f16 v[158:161], v[146:149], v[162:165], v[158:161]\n"
        "v_pk_mul_f16 v119, v119, v191 op_sel_hi:[1,0]\n"
        "v_pk_mul_f16 v120, v120, v191 op_sel_hi:[1,0]\n"
        "v_mfma_f32_16x16x32_f16 v[108:111], v[146:149], v[178:181], 0\n"
        "v_cvt_pk_f16_f32 v190, v154, v155\n"
        "v_cvt_pk_f16_f32 v191, v156, v157\n"
        "v_mfma_f32_16x16x32_f16 v[108:111], v[114:117], v[194:197], v[108:111]\n"
        "v_cvt_pk_f16_f32 v168, v134, v135\n"
        "v_cvt_pk_f16_f32 v169, v136, v137\n"
        "v_mfma_f32_16x16x32_bf16 v[108:111], v[206:209], v[88:91], v[108:111]\n"
        "v_cvt_pk_f16_f32 v166, v138, v139\n"
        "v_cvt_pk_f16_f32 v167, v140, v141\n"
        "v_mfma_f32_16x16x32_bf16 v[150:153], v[206:209], v[80:83], 0\n"
        "v_cvt_pk_f16_f32 v182, v184, v185\n"
        "v_cvt_pk_f16_f32 v183, v186, v187\n"
        "v_mfma_f32_16x16x32_f16 v[150:153], v[114:117], v[190:193], v[150:153]\n"
        "v_cvt_pk_f16_f32 v184, v248, v249\n"
        "v_cvt_pk_f16_f32 v185, v250, v251\n"
        "v_mfma_f32_16x16x32_f16 v[150:153], v[146:149], v[166:169], v[150:153]\n"
        "v_cvt_pk_f16_f32 v170, v252, v253\n"
        "v_cvt_pk_f16_f32 v171, v254, v255\n"
        "v_mfma_f32_16x16x32_f16 v[134:137], v[146:149], v[182:185], 0\n"
        "v_pk_mul_f16 v130, v130, v214\n"
        "v_pk_mul_f16 v131, v131, v215\n"
        "v_mfma_f32_16x16x32_f16 v[134:137], v[114:117], v[170:173], v[134:137]\n"
        "v_cvt_pk_f16_f32 v186, v40, v41\n"
        "v_cvt_pk_f16_f32 v187, v42, v43\n"
        "v_mfma_f32_16x16x32_bf16 v[134:137], v[218:221], v[88:91], v[134:137]\n"
        "v_cvt_pk_f16_f32 v42, v104, v105\n"
        "v_cvt_pk_f16_f32 v43, v106, v107\n"
        "v_mfma_f32_16x16x32_bf16 v[138:141], v[218:221], v[80:83], 0\n"
        "v_cvt_pk_f16_f32 v40, v34, v35\n"
        "v_cvt_pk_f16_f32 v41, v36, v37\n"
        "v_mfma_f32_16x16x32_f16 v[138:141], v[114:117], v[186:189], v[138:141]\n"
        "v_pk_mul_f16 v132, v132, v216\n"
        "v_pk_mul_f16 v133, v133, v217\n"
        "v_mfma_f32_16x16x32_f16 v[138:141], v[146:149], v[40:43], v[138:141]\n"
        "s_add_u32 s90, s89, 0\n"
        "v_pk_mul_f16 v118, v118, v246\n"
        "v_pk_mul_f16 v119, v119, v247\n"
        "v_mfma_f32_16x16x32_f16 v[34:37], v[130:133], v[142:145], 0\n"
        "v_rcp_f32 v255, v112\n"
        "v_mfma_f32_16x16x32_f16 v[34:37], v[118:121], v[122:125], v[34:37]\n"
        "v_max_f32 v245, v245, v112\n"
        "v_add_f32 v47, v47, v255\n"
        "v_mfma_f32_16x16x32_bf16 v[34:37], v[226:229], v[88:91], v[34:37]\n"
        "v_fma_f32 v245, v47, s82, v245\n"
        "v_cmp_nlt_f32_e64 s[74:75], v245, s83\n"
        "v_mfma_f32_16x16x32_bf16 v[112:115], v[226:229], v[80:83], 0\n"
        "v_cvt_pk_bf16_f32 v154, v126, v158\n"
        "v_cvt_pk_bf16_f32 v155, v108, v150\n"
        "v_mfma_f32_16x16x32_f16 v[112:115], v[118:121], v[174:177], v[112:115]\n"
        "s_cmp_lg_u64 s[74:75], 0\n"
        "v_cvt_pk_bf16_f32 v156, v134, v138\n"
        "v_mov_b32 v245, 0\n"
        "v_mfma_f32_16x16x32_f16 v[112:115], v[130:133], v[162:165], v[112:115]\n"
        "s_cbranch_scc1 SLOW_%=\n"
        "v_cvt_pk_bf16_f32 v146, v127, v159\n"
        "v_cvt_pk_bf16_f32 v147, v109, v151\n"
        "v_mfma_f32_16x16x32_f16 v[104:107], v[130:133], v[178:181], 0\n"
        "buffer_store_dwordx3 v[154:156], v14, s[40:43], s90 offen sc1 nt\n"
        "v_cvt_pk_bf16_f32 v148, v135, v139\n"
        "v_cvt_f16_f32 v254, v255\n"
        "v_cvt_pk_bf16_f32 v126, v128, v160\n"
        "v_cvt_pk_bf16_f32 v127, v110, v152\n"
        "v_mfma_f32_16x16x32_f16 v[104:107], v[118:121], v[194:197], v[104:107]\n"
        "s_add_u32 s91, s89, 98112\n"
        "v_cvt_pk_bf16_f32 v128, v136, v140\n"
        "v_pk_mul_f16 v238, v238, v254 op_sel_hi:[1,0]\n"
        "v_mfma_f32_16x16x32_bf16 v[104:107], v[234:237], v[88:91], v[104:107]\n"
        "buffer_store_dwordx3 v[146:148], v15, s[40:43], s90 offen offset:768 sc1 nt\n"
        "v_cvt_pk_bf16_f32 v134, v129, v161\n"
        "v_cvt_pk_bf16_f32 v135, v111, v153\n"
        "v_cvt_pk_bf16_f32 v136, v137, v141\n"
        "v_pk_mul_f16 v239, v239, v254 op_sel_hi:[1,0]\n"
        "v_mfma_f32_16x16x32_bf16 v[108:111], v[234:237], v[80:83], 0\n"
        "v_pk_mul_f16 v240, v240, v254 op_sel_hi:[1,0]\n"
        "v_pk_mul_f16 v241, v241, v254 op_sel_hi:[1,0]\n"
        "v_mfma_f32_16x16x32_f16 v[108:111], v[118:121], v[190:193], v[108:111]\n"
        "buffer_store_dwordx3 v[126:128], v14, s[40:43], s91 offen offset:192 sc1 nt\n"
        "v_pk_mul_f16 v242, v242, v254 op_sel_hi:[1,0]\n"
        "v_pk_mul_f16 v243, v243, v254 op_sel_hi:[1,0]\n"
        "v_pk_mul_f16 v244, v244, v254 op_sel_hi:[1,0]\n"
        "v_pk_mul_f16 v238, v238, v214\n"
        "v_mfma_f32_16x16x32_f16 v[108:111], v[130:133], v[166:169], v[108:111]\n"
        "v_pk_mul_f16 v239, v239, v215\n"
        "v_pk_mul_f16 v240, v240, v216\n"
        "v_mfma_f32_16x16x32_f16 v[126:129], v[130:133], v[182:185], 0\n"
        "buffer_store_dwordx3 v[134:136], v16, s[40:43], s91 offen offset:960 sc1 nt\n"
        "v_pk_mul_f16 v241, v241, v217\n"
        "v_pk_mul_f16 v242, v242, v246\n"
        "v_pk_mul_f16 v243, v243, v247\n"
        "v_cvt_pk_bf16_f32 v138, v34, v112\n"
        "v_mfma_f32_16x16x32_f16 v[126:129], v[118:121], v[170:173], v[126:129]\n"
        "v_cvt_pk_bf16_f32 v112, v35, v113\n"
        "v_cvt_pk_bf16_f32 v116, v36, v114\n"
        "v_mfma_f32_16x16x32_bf16 v[126:129], v[230:233], v[88:91], v[126:129]\n"
        "v_cvt_pk_bf16_f32 v139, v104, v108\n"
        "v_cvt_pk_bf16_f32 v113, v105, v109\n"
        "v_mfma_f32_16x16x32_bf16 v[134:137], v[230:233], v[80:83], 0\n"
        "v_cvt_pk_bf16_f32 v117, v106, v110\n"
        "v_cvt_pk_bf16_f32 v216, v37, v115\n"
        "v_mfma_f32_16x16x32_f16 v[134:137], v[118:121], v[186:189], v[134:137]\n"
        "v_cvt_pk_bf16_f32 v217, v107, v111\n"
        "v_mfma_f32_16x16x32_f16 v[134:137], v[130:133], v[40:43], v[134:137]\n"
        "s_nop 1\n"
        "v_mfma_f32_16x16x32_f16 v[34:37], v[238:241], v[142:145], 0\n"
        "s_nop 2\n"
        "v_mfma_f32_16x16x32_f16 v[34:37], v[242:245], v[122:125], v[34:37]\n"
        "s_waitcnt vmcnt(4)\n"
        "v_cvt_pk_bf16_f32 v140, v126, v134\n"
        "v_cvt_pk_bf16_f32 v114, v127, v135\n"
        "v_mfma_f32_16x16x32_bf16 v[34:37], v[202:205], v[88:91], v[34:37]\n"
        "s_add_u32 s90, s89, 196224\n"
        "ds_read_b128 v[146:149], v9\n"
        "v_cvt_pk_bf16_f32 v118, v128, v136\n"
        "v_mfma_f32_16x16x32_bf16 v[104:107], v[202:205], v[80:83], 0\n"
        "s_cmp_le_i32 s85, 1\n"
        "ds_read_b128 v[152:155], v10\n"
        "v_cvt_pk_bf16_f32 v218, v129, v137\n"
        "v_mfma_f32_16x16x32_f16 v[104:107], v[242:245], v[174:177], v[104:107]\n"
        "s_cbranch_scc1 DY3_%=\n"
        "DY3B_%=:\n"
        "ds_read_b128 v[158:161], v11\n"
        "v_mfma_f32_16x16x32_f16 v[104:107], v[238:241], v[162:165], v[104:107]\n"
        "s_add_u32 s91, s89, 294336\n"
        "ds_read_b128 v[126:129], v9 offset:3072\n"
        "v_mfma_f32_16x16x32_f16 v[206:209], v[238:241], v[178:181], 0\n"
        "buffer_store_dwordx3 v[138:140], v14, s[40:43], s90 offen offset:384 sc1 nt\n"
        "v_mfma_f32_16x16x32_f16 v[206:209], v[242:245], v[194:197], v[206:209]\n"
        "ds_read_b128 v[132:135], v10 offset:3072\n"
        "ds_read_b128 v[138:141], v11 offset:3072\n"
        "v_mfma_f32_16x16x32_bf16 v[206:209], v[210:213], v[88:91], v[206:209]\n"
        "buffer_store_dwordx3 v[112:114], v17, s[40:43], s90 offen offset:1152 sc1 nt\n"
        "v_cvt_pk_bf16_f32 v214, v34, v104\n"
        "v_cvt_pk_bf16_f32 v234, v35, v105\n"
        "v_cvt_pk_bf16_f32 v250, v36, v106\n"
        "v_cvt_pk_bf16_f32 v246, v37, v107\n"
        "v_mfma_f32_16x16x32_bf16 v[34:37], v[210:213], v[80:83], 0\n"
        "s_add_u32 s90, s89, 392448\n"
        "ds_read_b128 v[106:109], v9 offset:6144\n"
        "ds_read_b128 v[112:115], v10 offset:6144\n"
        "v_mfma_f32_16x16x32_f16 v[34:37], v[242:245], v[190:193], v[34:37]\n"
        "buffer_store_dwordx3 v[116:118], v14, s[40:43], s91 offen offset:576 sc1 nt\n"
        "v_mfma_f32_16x16x32_f16 v[34:37], v[238:241], v[166:169], v[34:37]\n"
        "s_waitcnt lgkmcnt(5)\n"
        "ds_read_b128 v[118:121], v11 offset:6144\n"
        "v_mfma_f32_16x16x32_f16 v[202:205], v[238:241], v[182:185], 0\n"
        "buffer_store_dwordx3 v[216:218], v14, s[40:43], s90 offen sc1 nt\n"
        "s_nop 1\n"
        "v_mfma_f32_16x16x32_f16 v[202:205], v[242:245], v[170:173], v[202:205]\n"
        "s_nop 0\n"
        "v_cvt_pk_bf16_f32 v215, v206, v34\n"
        "v_cvt_pk_bf16_f32 v235, v207, v35\n"
        "v_mfma_f32_16x16x32_bf16 v[202:205], v[198:201], v[88:91], v[202:205]\n"
        "v_cvt_pk_bf16_f32 v251, v208, v36\n"
        "v_cvt_pk_bf16_f32 v247, v209, v37\n"
        "v_mfma_f32_16x16x32_bf16 v[206:209], v[198:201], v[80:83], 0\n"
        "s_cmp_ge_i32 s85, 0\n"
        "v_mfma_f32_16x16x32_f16 v[206:209], v[242:245], v[186:189], v[206:209]\n"
        "v_mfma_f32_16x16x32_f16 v[206:209], v[238:241], v[40:43], v[206:209]\n"
        "s_cbranch_scc1 TOP_%=\n"
        "LAST_%=:\n"
        "s_waitcnt lgkmcnt(0)\n"
        "s_nop 7\n"
        "s_mov_b32 s26, s89\n"
        "s_add_u32 s27, s26, 392448\n"
        "v_cvt_pk_bf16_f32 v216, v202, v206\n"
        "buffer_store_dwordx3 v[214:216], v15, s[40:43], s27 offen offset:768 sc1 nt\n"
        "v_cvt_pk_bf16_f32 v236, v203, v207\n"
        "s_add_u32 s28, s26, 490560\n"
        "buffer_store_dwordx3 v[234:236], v14, s[40:43], s28 offen offset:192 sc1 nt\n"
        "v_cvt_pk_bf16_f32 v252, v204, v208\n"
        "buffer_store_dwordx3 v[250:252], v16, s[40:43], s28 offen offset:960 sc1 nt\n"
        "v_cvt_pk_bf16_f32 v248, v205, v209\n"
        "s_add_u32 s27, s26, 588672\n"
        "buffer_store_dwordx3 v[246:248], v14, s[40:43], s27 offen offset:384 sc1 nt\n"
        "ds_read_u16 v255, v19 offset:2048\n"
        "s_waitcnt lgkmcnt(0)\n"
        "s_mov_b32 s30, s29\n"
        "v_cmp_ne_u32_e64 s[24:25], s30, v255\n"
        "s_nop 3\n"
        "s_cmp_lg_u64 s[24:25], 0\n"
        "s_cbranch_scc1 SP20_%=\n"
        "BK20_%=:\n"
        "ds_read_b128 v[222:225], v13\n"
        "ds_read_b128 v[206:209], v13 offset:1024\n"
        "ds_read_b128 v[218:221], v13 offset:2048\n"
        "ds_read_b128 v[226:229], v13 offset:3072\n"
        "ds_read_b128 v[234:237], v13 offset:4096\n"
        "ds_read_b128 v[230:233], v13 offset:5120\n"
        "ds_read_b128 v[202:205], v13 offset:6144\n"
        "ds_read_b128 v[210:213], v13 offset:7168\n"
        "ds_read_b128 v[198:201], v13 offset:8192\n"
        "ds_read_b128 v[152:155], v20\n"
        "ds_read_b64 v[30:31], v20 offset:16\n"
        "ds_read_u16 v255, v22\n"
        "ds_read_b64 v[34:35], v21\n"
        "v_mov_b32 v26, 0\n"
        "s_mov_b64 exec, 1\n"
        "buffer_atomic_add v26, off, s[16:19], s15 offset:4 sc0\n"
        "s_mov_b64 exec, -1\n"
        "s_waitcnt lgkmcnt(10)\n"
        "v_mfma_f32_16x16x32_bf16 v[104:107], a[152:155], v[218:221], 0\n"
        "s_add_u32 s89, s86, s51\n"
        "ds_read_b128 v[126:129], v20 offset:512\n"
        "v_add_u32 v23, s22, v23\n"
        "v_mfma_f32_16x16x32_bf16 v[104:107], a[148:151], v[206:209], v[104:107]\n"
        "s_add_u32 s29, s29, 1\n"
        "ds_read_b128 v[146:149], v20 offset:528\n"
        "v_add_u32 v24, s22, v24\n"
        "v_mfma_f32_16x16x32_bf16 v[104:107], a[144:147], v[222:225], v[104:107]\n"
        "s_mov_b32 s22, s23\n"
        "ds_read_b128 v[118:121], v23\n"
        "v_lshlrev_b32 v114, 16, v124\n"
        "v_mfma_f32_16x16x32_bf16 v[108:111], a[156:159], v[222:225], 0\n"
        "s_movk_i32 s23, 1584\n"
        "ds_read_b128 v[238:241], v23 offset:64\n"
        "v_lshlrev_b32 v115, 16, v176\n"
        "v_mfma_f32_16x16x32_bf16 v[108:111], a[160:163], v[206:209], v[108:111]\n"
        "s_mov_b32 s86, s87\n"
        "ds_read_b128 v[130:133], v23 offset:128\n"
        "v_lshlrev_b32 v116, 16, v196\n"
        "v_mfma_f32_16x16x32_bf16 v[108:111], a[164:167], v[218:221], v[108:111]\n"
        "s_add_u32 s88, s88, 32\n"
        "ds_read_b64 v[36:37], v24 offset:360\n"
        "v_lshlrev_b32 v117, 16, v192\n"
        "v_mfma_f32_16x16x32_bf16 v[242:245], a[176:179], v[218:221], 0\n"
        "s_add_u32 s87, s87, 43008\n"
        "v_lshlrev_b32 v112, 16, v172\n"
        "v_lshlrev_b32 v113, 16, v188\n"
        "v_mfma_f32_16x16x32_bf16 v[242:245], a[172:175], v[206:209], v[242:245]\n"
        "s_cmp_ge_u32 s88, 73\n"
        "v_cvt_pk_bf16_f32 v134, v104, v105\n"
        "v_cvt_pk_bf16_f32 v135, v106, v107\n"
        "v_mfma_f32_16x16x32_bf16 v[242:245], a[168:171], v[222:225], v[242:245]\n"
        "s_cselect_b32 s92, 588672, 0\n"
        "v_cvt_pk_bf16_f32 v136, v108, v109\n"
        "v_cvt_pk_bf16_f32 v137, v110, v111\n"
        "v_mfma_f32_16x16x32_bf16 v[138:141], a[180:183], v[222:225], 0\n"
        "s_waitcnt lgkmcnt(4)\n"
        "v_pk_mul_f16 v104, v126, v142\n"
        "v_pk_mul_f16 v105, v126, v162\n"
        "v_mfma_f32_16x16x32_bf16 v[138:141], a[184:187], v[206:209], v[138:141]\n"
        "s_cselect_b32 s94, 73, 0\n"
        "v_pk_mul_f16 v106, v126, v178\n"
        "v_pk_mul_f16 v107, v126, v166\n"
        "v_mfma_f32_16x16x32_bf16 v[138:141], a[188:191], v[218:221], v[138:141]\n"
        "s_sub_u32 s88, s88, s94\n"
        "v_pk_mul_f16 v108, v126, v182\n"
        "v_pk_mul_f16 v109, v126, v40\n"
        "v_mfma_f32_16x16x32_bf16 v[214:217], a[200:203], v[218:221], 0\n"
        "s_add_u32 s87, s87, s92\n"
        "v_pk_fma_f16 v104, v127, v143, v104\n"
        "v_pk_fma_f16 v105, v127, v163, v105\n"
        "v_mfma_f32_16x16x32_bf16 v[214:217], a[196:199], v[206:209], v[214:217]\n"
        "s_waitcnt lgkmcnt(0)\n"
        "v_pk_fma_f16 v106, v127, v179, v106\n"
        "v_pk_fma_f16 v107, v127, v167, v107\n"
        "v_mfma_f32_16x16x32_bf16 v[214:217], a[192:195], v[222:225], v[214:217]\n"
        "s_cmp_gt_i32 s85, 0\n"
        "v_pk_fma_f16 v108, v127, v183, v108\n"
        "v_pk_fma_f16 v109, v127, v41, v109\n"
        "v_mfma_f32_16x16x32_bf16 v[156:159], a[204:207], v[222:225], 0\n"
        "s_cselect_b32 s94, s87, s84\n"
        "v_readlane_b32 s93, v37, 1\n"
        "v_cndmask_b32_e64 v36, 0, v36, s[72:73]\n"
        "v_mfma_f32_16x16x32_bf16 v[156:159], a[208:211], v[206:209], v[156:159]\n"
        "s_sub_u32 s85, s85, 1\n"
        "v_pk_fma_f16 v104, v128, v144, v104\n"
        "v_pk_fma_f16 v105, v128, v164, v105\n"
        "v_mfma_f32_16x16x32_bf16 v[156:159], a[212:215], v[218:221], v[156:159]\n"
        "s_waitcnt lgkmcnt(0)\n"
        "v_pk_fma_f16 v106, v128, v180, v106\n"
        "v_pk_fma_f16 v107, v128, v168, v107\n"
        "v_mfma_f32_16x16x32_bf16 v[160:163], v[118:121], v[222:225], 0\n"
        "s_waitcnt lgkmcnt(0)\n"
        "v_pk_fma_f16 v108, v128, v184, v108\n"
        "v_pk_fma_f16 v109, v128, v42, v109\n"
        "v_mfma_f32_16x16x32_bf16 v[160:163], v[238:241], v[206:209], v[160:163]\n"
        "v_pk_fma_f16 v104, v129, v145, v104\n"
        "v_pk_fma_f16 v105, v129, v165, v105\n"
        "v_mfma_f32_16x16x32_bf16 v[160:163], v[130:133], v[218:221], v[160:163]\n"
        "v_pk_fma_f16 v106, v129, v181, v106\n"
        "v_pk_fma_f16 v107, v129, v169, v107\n"
        "v_mfma_f32_16x16x32_bf16 v[142:145], v[130:133], v[230:233], 0\n"
        "v_pk_fma_f16 v108, v129, v185, v108\n"
        "v_pk_fma_f16 v109, v129, v43, v109\n"
        "v_mfma_f32_16x16x32_bf16 v[142:145], v[238:241], v[234:237], v[142:145]\n"
        "v_pk_fma_f16 v104, v146, v122, v104\n"
        "v_pk_fma_f16 v105, v146, v174, v105\n"
        "v_mfma_f32_16x16x32_bf16 v[142:145], v[118:121], v[226:229], v[142:145]\n"
        "v_pk_fma_f16 v106, v146, v194, v106\n"
        "v_pk_fma_f16 v107, v146, v190, v107\n"
        "v_mfma_f32_16x16x32_bf16 v[40:43], v[118:121], v[202:205], 0\n"
        "v_pk_fma_f16 v108, v146, v170, v108\n"
        "v_pk_fma_f16 v109, v146, v186, v109\n"
        "v_mfma_f32_16x16x32_bf16 v[40:43], v[238:241], v[210:213], v[40:43]\n"
        "v_pk_fma_f16 v104, v147, v123, v104\n"
        "v_pk_fma_f16 v105, v147, v175, v105\n"
        "v_mfma_f32_16x16x32_bf16 v[40:43], v[130:133], v[198:201], v[40:43]\n"
        "v_pk_fma_f16 v106, v147, v195, v106\n"
        "v_pk_fma_f16 v107, v147, v191, v107\n"
        "v_mfma_f32_16x16x32_bf16 v[118:121], v[222:225], v[134:137], 0\n"
        "v_pk_fma_f16 v108, v147, v171, v108\n"
        "v_pk_fma_f16 v109, v147, v187, v109\n"
        "v_cvt_pk_bf16_f32 v126, v242, v243\n"
        "v_cvt_pk_bf16_f32 v127, v244, v245\n"
        "v_cvt_pk_bf16_f32 v128, v138, v139\n"
        "v_cvt_pk_bf16_f32 v129, v140, v141\n"
        "s_nop 1\n"
        "v_mfma_f32_16x16x32_bf16 v[118:121], v[206:209], v[126:129], v[118:121]\n"
        "v_pk_fma_f16 v104, v148, v124, v104\n"
        "v_pk_fma_f16 v105, v148, v176, v105\n"
        "v_cvt_pk_bf16_f32 v130, v214, v215\n"
        "v_cvt_pk_bf16_f32 v131, v216, v217\n"
        "v_cvt_pk_bf16_f32 v132, v156, v157\n"
        "v_cvt_pk_bf16_f32 v133, v158, v159\n"
        "s_nop 1\n"
        "v_mfma_f32_16x16x32_bf16 v[118:121], v[218:221], v[130:133], v[118:121]\n"
        "v_pk_fma_f16 v106, v148, v196, v106\n"
        "v_pk_fma_f16 v107, v148, v192, v107\n"
        "v_mfma_f32_16x16x32_bf16 v[122:125], v[230:233], v[130:133], 0\n"
        "v_pk_fma_f16 v108, v148, v172, v108\n"
        "v_pk_fma_f16 v109, v148, v188, v109\n"
        "v_mfma_f32_16x16x32_bf16 v[122:125], v[234:237], v[126:129], v[122:125]\n"
        "v_cndmask_b32_e64 v254, v160, v142, s[64:65]\n"
        "v_cndmask_b32_e64 v253, v161, v143, s[64:65]\n"
        "v_mfma_f32_16x16x32_bf16 v[122:125], v[226:229], v[134:137], v[122:125]\n"
        "v_cndmask_b32_e64 v254, v254, v40, s[70:71]\n"
        "v_cndmask_b32_e64 v253, v253, v41, s[70:71]\n"
        "v_mfma_f32_16x16x32_bf16 v[40:43], v[202:205], v[134:137], 0\n"
        "v_cvt_pk_f16_f32 v254, v254, v253\n"
        "v_pk_mul_f16 v254, v254, v255 op_sel_hi:[1,0]\n"
        "v_mfma_f32_16x16x32_bf16 v[40:43], v[210:213], v[126:129], v[40:43]\n"
        "s_add_u32 s46, s26, 590016\n"
        "v_pk_mul_f16 v254, v254, s93 op_sel_hi:[1,0]\n"
        "v_cvt_pk_f16_f32 v214, v118, v119\n"
        "v_mfma_f32_16x16x32_bf16 v[40:43], v[198:201], v[130:133], v[40:43]\n"
        "v_exp_f16_sdwa v254, v254 dst_sel:WORD_0 dst_unused:UNUSED_PRESERVE src0_sel:WORD_0\n"
        "v_mfma_f32_16x16x32_f16 v[114:117], v[56:59], v[104:107], v[114:117]\n"
        "s_min_u32 s46, s46, s42\n"
        "v_exp_f16_sdwa v254, v254 dst_sel:WORD_1 dst_unused:UNUSED_PRESERVE src0_sel:WORD_1\n"
        "v_mfma_f32_16x16x32_f16 v[110:113], v[56:59], v[106:109], v[110:113]\n"
        "v_and_b32 v253, 0xffff0000, v254\n"
        "v_cvt_f32_f16 v252, v254\n"
        "v_mfma_f32_16x16x32_bf16 v[104:107], a[152:155], v[230:233], 0\n"
        "v_cndmask_b32_e64 v252, v252, v36, s[68:69]\n"
        "v_cvt_pk_f16_f32 v215, v120, v121\n"
        "v_mfma_f32_16x16x32_bf16 v[104:107], a[148:151], v[234:237], v[104:107]\n"
        "v_add_f32_dpp v254, v252, v252 row_ror:8 row_mask:0xf bank_mask:0xf\n"
        "v_pk_mul_f16 v214, v214, v34 op_sel_hi:[1,0]\n"
        "v_mfma_f32_16x16x32_bf16 v[104:107], a[144:147], v[226:229], v[104:107]\n"
        "v_pk_mul_f16 v215, v215, v34 op_sel_hi:[1,0]\n"
        "v_pk_mul_f16 v214, v214, v152\n"
        "v_mfma_f32_16x16x32_bf16 v[134:137], a[156:159], v[226:229], 0\n"
        "v_pk_mul_f16 v215, v215, v153\n"
        "v_add_f32_dpp v254, v254, v254 row_ror:4 row_mask:0xf bank_mask:0xf\n"
        "v_mfma_f32_16x16x32_bf16 v[134:137], a[160:163], v[234:237], v[134:137]\n"
        "v_exp_f16_sdwa v214, v214 dst_sel:WORD_0 dst_unused:UNUSED_PRESERVE src0_sel:WORD_0\n"
        "v_mfma_f32_16x16x32_bf16 v[134:137], a[164:167], v[230:233], v[134:137]\n"
        "v_exp_f16_sdwa v215, v215 dst_sel:WORD_0 dst_unused:UNUSED_PRESERVE src0_sel:WORD_0\n"
        "v_mfma_f32_16x16x32_bf16 v[126:129], a[176:179], v[230:233], 0\n"
        "v_exp_f16_sdwa v214, v214 dst_sel:WORD_1 dst_unused:UNUSED_PRESERVE src0_sel:WORD_1\n"
        "v_mfma_f32_16x16x32_bf16 v[126:129], a[172:175], v[234:237], v[126:129]\n"
        "v_exp_f16_sdwa v215, v215 dst_sel:WORD_1 dst_unused:UNUSED_PRESERVE src0_sel:WORD_1\n"
        "v_mfma_f32_16x16x32_bf16 v[126:129], a[168:171], v[226:229], v[126:129]\n"
        "v_add_f32_dpp v254, v254, v254 row_ror:2 row_mask:0xf bank_mask:0xf\n"
        "v_cvt_pk_bf16_f32 v108, v114, v115\n"
        "v_mfma_f32_16x16x32_bf16 v[130:133], a[188:191], v[230:233], 0\n"
        "v_cvt_pk_bf16_f32 v109, v116, v117\n"
        "v_cvt_pk_bf16_f32 v110, v112, v113\n"
        "v_mfma_f32_16x16x32_bf16 v[130:133], a[184:187], v[234:237], v[130:133]\n"
        "buffer_store_dwordx3 v[108:110], v17, s[44:47], s27 offen offset:1152 sc1 nt\n"
        "v_pk_add_f16 v112, v214, v215\n"
        "v_add_f32_dpp v254, v254, v254 row_ror:1 row_mask:0xf bank_mask:0xf\n"
        "v_cvt_pk_f16_f32 v216, v122, v123\n"
        "v_cvt_pk_f16_f32 v217, v124, v125\n"
        "v_mfma_f32_16x16x32_bf16 v[130:133], a[180:183], v[226:229], v[130:133]\n"
        "v_add_f32_dpp v254, v254, v254 row_bcast:15 row_mask:0xa bank_mask:0xf\n"
        "v_pk_mul_f16 v216, v216, v34 op_sel_hi:[1,0]\n"
        "v_mfma_f32_16x16x32_bf16 v[108:111], a[192:195], v[226:229], 0\n"
        "v_pk_mul_f16 v217, v217, v34 op_sel_hi:[1,0]\n"
        "v_pk_mul_f16 v216, v216, v154\n"
        "v_mfma_f32_16x16x32_bf16 v[108:111], a[196:199], v[234:237], v[108:111]\n"
        "v_pk_mul_f16 v217, v217, v155\n"
        "v_add_f32_dpp v254, v254, v254 row_bcast:31 row_mask:0xc bank_mask:0xf\n"
        "v_mfma_f32_16x16x32_bf16 v[108:111], a[200:203], v[230:233], v[108:111]\n"
        "v_exp_f16_sdwa v216, v216 dst_sel:WORD_0 dst_unused:UNUSED_PRESERVE src0_sel:WORD_0\n"
        "v_mfma_f32_16x16x32_bf16 v[114:117], a[212:215], v[230:233], 0\n"
        "v_exp_f16_sdwa v217, v217 dst_sel:WORD_0 dst_unused:UNUSED_PRESERVE src0_sel:WORD_0\n"
        "v_mfma_f32_16x16x32_bf16 v[114:117], a[208:211], v[234:237], v[114:117]\n"
        "v_readlane_b32 s95, v254, 63\n"
        "v_cvt_pk_f16_f32 v164, v40, v41\n"
        "v_mfma_f32_16x16x32_bf16 v[114:117], a[204:207], v[226:229], v[114:117]\n"
        "v_exp_f16_sdwa v216, v216 dst_sel:WORD_1 dst_unused:UNUSED_PRESERVE src0_sel:WORD_1\n"
        "v_mfma_f32_16x16x32_bf16 v[122:125], v[222:225], a[216:219], 0\n"
        "v_exp_f16_sdwa v217, v217 dst_sel:WORD_1 dst_unused:UNUSED_PRESERVE src0_sel:WORD_1\n"
        "v_mfma_f32_16x16x32_bf16 v[122:125], v[206:209], a[220:223], v[122:125]\n"
        "v_pk_add_f16 v112, v112, v216\n"
        "v_pk_add_f16 v112, v112, v217\n"
        "v_mfma_f32_16x16x32_bf16 v[122:125], v[218:221], a[224:227], v[122:125]\n"
        "v_rcp_f32 v254, s95\n"
        "v_mfma_f32_16x16x32_bf16 v[118:121], v[230:233], a[224:227], 0\n"
        "v_mul_f32 v252, v252, v254\n"
        "v_cvt_f16_f32 v252, v252\n"
        "v_mfma_f32_16x16x32_bf16 v[118:121], v[234:237], a[220:223], v[118:121]\n"
        "v_mul_f16 v252, v252, v255\n"
        "v_cvt_pk_f16_f32 v165, v42, v43\n"
        "v_mfma_f32_16x16x32_bf16 v[118:121], v[226:229], a[216:219], v[118:121]\n"
        "ds_write_b16 v22, v252 offset:512\n"
        "v_mfma_f32_16x16x32_bf16 v[40:43], v[202:205], a[216:219], 0\n"
        "v_pk_mul_f16 v164, v164, v34 op_sel_hi:[1,0]\n"
        "v_pk_mul_f16 v165, v165, v34 op_sel_hi:[1,0]\n"
        "v_mfma_f32_16x16x32_bf16 v[40:43], v[210:213], a[220:223], v[40:43]\n"
        "v_pk_mul_f16 v164, v164, v30\n"
        "v_pk_mul_f16 v165, v165, v31\n"
        "v_mfma_f32_16x16x32_bf16 v[40:43], v[198:201], a[224:227], v[40:43]\n"
        "v_exp_f16_sdwa v164, v164 dst_sel:WORD_0 dst_unused:UNUSED_PRESERVE src0_sel:WORD_0\n"
        "v_cvt_pk_bf16_f32 v138, v104, v105\n"
        "v_cvt_pk_bf16_f32 v139, v106, v107\n"
        "v_cvt_pk_bf16_f32 v140, v134, v135\n"
        "v_cvt_pk_bf16_f32 v141, v136, v137\n"
        "s_nop 1\n"
        "v_mfma_f32_16x16x32_bf16 v[134:137], v[222:225], v[138:141], 0\n"
        "v_cvt_pk_bf16_f32 v142, v126, v127\n"
        "v_cvt_pk_bf16_f32 v143, v128, v129\n"
        "v_cvt_pk_bf16_f32 v144, v130, v131\n"
        "v_cvt_pk_bf16_f32 v145, v132, v133\n"
        "s_nop 1\n"
        "v_mfma_f32_16x16x32_bf16 v[134:137], v[206:209], v[142:145], v[134:137]\n"
        "v_cvt_pk_bf16_f32 v104, v108, v109\n"
        "v_cvt_pk_bf16_f32 v105, v110, v111\n"
        "v_cvt_pk_bf16_f32 v106, v114, v115\n"
        "v_cvt_pk_bf16_f32 v107, v116, v117\n"
        "s_nop 1\n"
        "v_mfma_f32_16x16x32_bf16 v[134:137], v[218:221], v[104:107], v[134:137]\n"
        "v_exp_f16_sdwa v165, v165 dst_sel:WORD_0 dst_unused:UNUSED_PRESERVE src0_sel:WORD_0\n"
        "v_mfma_f32_16x16x32_bf16 v[108:111], v[230:233], v[104:107], 0\n"
        "v_exp_f16_sdwa v164, v164 dst_sel:WORD_1 dst_unused:UNUSED_PRESERVE src0_sel:WORD_1\n"
        "v_mfma_f32_16x16x32_bf16 v[108:111], v[234:237], v[142:145], v[108:111]\n"
        "v_exp_f16_sdwa v165, v165 dst_sel:WORD_1 dst_unused:UNUSED_PRESERVE src0_sel:WORD_1\n"
        "v_mfma_f32_16x16x32_bf16 v[108:111], v[226:229], v[138:141], v[108:111]\n"
        "v_pk_add_f16 v112, v112, v164\n"
        "v_pk_add_f16 v112, v112, v165\n"
        "v_mfma_f32_16x16x32_bf16 v[114:117], v[202:205], v[138:141], 0\n"
        "v_cvt_pk_f16_f32 v160, v122, v123\n"
        "v_cvt_pk_f16_f32 v161, v124, v125\n"
        "v_mfma_f32_16x16x32_bf16 v[114:117], v[210:213], v[142:145], v[114:117]\n"
        "v_cvt_pk_f16_f32 v162, v118, v119\n"
        "v_cvt_pk_f16_f32 v163, v120, v121\n"
        "v_mfma_f32_16x16x32_bf16 v[114:117], v[198:201], v[104:107], v[114:117]\n"
        "v_cvt_pk_f16_f32 v176, v40, v41\n"
        "v_cvt_pk_f16_f32 v177, v42, v43\n"
        "v_mfma_f32_16x16x32_bf16 v[40:43], a[144:147], v[202:205], 0\n"
        "v_cvt_pk_f16_f32 v156, v134, v135\n"
        "v_cvt_pk_f16_f32 v157, v136, v137\n"
        "v_mfma_f32_16x16x32_bf16 v[40:43], a[148:151], v[210:213], v[40:43]\n"
        "v_pk_mul_f16 v156, v156, v34 op_sel:[0,1]\n"
        "v_pk_mul_f16 v157, v157, v34 op_sel:[0,1]\n"
        "v_mfma_f32_16x16x32_bf16 v[40:43], a[152:155], v[198:201], v[40:43]\n"
        "v_pk_mul_f16 v156, v156, v152\n"
        "v_pk_mul_f16 v157, v157, v153\n"
        "v_mfma_f32_16x16x32_bf16 v[104:107], a[164:167], v[198:201], 0\n"
        "v_exp_f16_sdwa v156, v156 dst_sel:WORD_0 dst_unused:UNUSED_PRESERVE src0_sel:WORD_0\n"
        "v_mfma_f32_16x16x32_bf16 v[104:107], a[160:163], v[210:213], v[104:107]\n"
        "v_exp_f16_sdwa v157, v157 dst_sel:WORD_0 dst_unused:UNUSED_PRESERVE src0_sel:WORD_0\n"
        "v_mfma_f32_16x16x32_bf16 v[104:107], a[156:159], v[202:205], v[104:107]\n"
        "v_exp_f16_sdwa v156, v156 dst_sel:WORD_1 dst_unused:UNUSED_PRESERVE src0_sel:WORD_1\n"
        "v_mfma_f32_16x16x32_bf16 v[122:125], a[168:171], v[202:205], 0\n"
        "v_exp_f16_sdwa v157, v157 dst_sel:WORD_1 dst_unused:UNUSED_PRESERVE src0_sel:WORD_1\n"
        "v_mfma_f32_16x16x32_bf16 v[122:125], a[172:175], v[210:213], v[122:125]\n"
        "v_pk_add_f16 v113, v156, v157\n"
        "v_cvt_pk_f16_f32 v158, v108, v109\n"
        "v_mfma_f32_16x16x32_bf16 v[122:125], a[176:179], v[198:201], v[122:125]\n"
        "v_cvt_pk_f16_f32 v159, v110, v111\n"
        "v_pk_mul_f16 v158, v158, v34 op_sel:[0,1]\n"
        "v_mfma_f32_16x16x32_bf16 v[108:111], a[188:191], v[198:201], 0\n"
        "v_pk_mul_f16 v159, v159, v34 op_sel:[0,1]\n"
        "v_pk_mul_f16 v158, v158, v154\n"
        "v_mfma_f32_16x16x32_bf16 v[108:111], a[184:187], v[210:213], v[108:111]\n"
        "v_pk_mul_f16 v159, v159, v155\n"
        "v_cvt_pk_f16_f32 v242, v114, v115\n"
        "v_mfma_f32_16x16x32_bf16 v[108:111], a[180:183], v[202:205], v[108:111]\n"
        "v_exp_f16_sdwa v158, v158 dst_sel:WORD_0 dst_unused:UNUSED_PRESERVE src0_sel:WORD_0\n"
        "v_mfma_f32_16x16x32_bf16 v[118:121], a[192:195], v[202:205], 0\n"
        "v_exp_f16_sdwa v159, v159 dst_sel:WORD_0 dst_unused:UNUSED_PRESERVE src0_sel:WORD_0\n"
        "v_mfma_f32_16x16x32_bf16 v[118:121], a[196:199], v[210:213], v[118:121]\n"
        "v_exp_f16_sdwa v158, v158 dst_sel:WORD_1 dst_unused:UNUSED_PRESERVE src0_sel:WORD_1\n"
        "v_mfma_f32_16x16x32_bf16 v[118:121], a[200:203], v[198:201], v[118:121]\n"
        "v_exp_f16_sdwa v159, v159 dst_sel:WORD_1 dst_unused:UNUSED_PRESERVE src0_sel:WORD_1\n"
        "v_mfma_f32_16x16x32_bf16 v[130:133], a[212:215], v[198:201], 0\n"
        "v_pk_add_f16 v113, v113, v158\n"
        "v_pk_add_f16 v113, v113, v159\n"
        "v_mfma_f32_16x16x32_bf16 v[130:133], a[208:211], v[210:213], v[130:133]\n"
        "v_cvt_pk_f16_f32 v243, v116, v117\n"
        "v_pk_mul_f16 v242, v242, v34 op_sel:[0,1]\n"
        "v_mfma_f32_16x16x32_bf16 v[130:133], a[204:207], v[202:205], v[130:133]\n"
        "v_pk_mul_f16 v243, v243, v34 op_sel:[0,1]\n"
        "v_pk_mul_f16 v242, v242, v30\n"
        "v_mfma_f32_16x16x32_bf16 v[138:141], v[198:201], a[236:239], 0\n"
        "v_pk_mul_f16 v243, v243, v31\n"
        "v_cvt_pk_bf16_f32 v126, v40, v41\n"
        "v_mfma_f32_16x16x32_bf16 v[138:141], v[210:213], a[232:235], v[138:141]\n"
        "v_exp_f16_sdwa v242, v242 dst_sel:WORD_0 dst_unused:UNUSED_PRESERVE src0_sel:WORD_0\n"
        "v_mfma_f32_16x16x32_bf16 v[138:141], v[202:205], a[228:231], v[138:141]\n"
        "v_cvt_pk_bf16_f32 v127, v42, v43\n"
        "v_cvt_pk_bf16_f32 v128, v104, v105\n"
        "v_mfma_f32_16x16x32_bf16 v[134:137], v[226:229], a[228:231], 0\n"
        "v_cndmask_b32_e64 v166, 0, v253, s[62:63]\n"
        "v_cndmask_b32_e64 v244, 0, v253, s[64:65]\n"
        "v_mfma_f32_16x16x32_bf16 v[134:137], v[234:237], a[232:235], v[134:137]\n"
        "v_pk_add_f16 v112, v112, v166\n"
        "v_cvt_pk_bf16_f32 v129, v106, v107\n"
        "v_mfma_f32_16x16x32_bf16 v[134:137], v[230:233], a[236:239], v[134:137]\n"
        "v_cvt_pk_bf16_f32 v40, v122, v123\n"
        "v_cvt_pk_bf16_f32 v41, v124, v125\n"
        "v_mfma_f32_16x16x32_bf16 v[122:125], v[218:221], a[236:239], 0\n"
        "v_mov_b64 v[114:115], 0\n"
        "v_cvt_pk_bf16_f32 v42, v108, v109\n"
        "v_mfma_f32_16x16x32_bf16 v[122:125], v[206:209], a[232:235], v[122:125]\n"
        "v_cvt_pk_bf16_f32 v43, v110, v111\n"
        "v_cvt_pk_bf16_f32 v142, v118, v119\n"
        "v_mfma_f32_16x16x32_bf16 v[122:125], v[222:225], a[228:231], v[122:125]\n"
        "v_cvt_pk_bf16_f32 v143, v120, v121\n"
        "v_cvt_pk_bf16_f32 v144, v130, v131\n"
        "v_mfma_f32_16x16x32_bf16 v[108:111], v[222:225], v[126:129], 0\n"
        "v_cvt_pk_bf16_f32 v145, v132, v133\n"
        "v_cvt_pk_f16_f32 v246, v138, v139\n"
        "v_mfma_f32_16x16x32_bf16 v[108:111], v[206:209], v[40:43], v[108:111]\n"
        "v_exp_f16_sdwa v243, v243 dst_sel:WORD_0 dst_unused:UNUSED_PRESERVE src0_sel:WORD_0\n"
        "v_mfma_f32_16x16x32_bf16 v[108:111], v[218:221], v[142:145], v[108:111]\n"
        "v_exp_f16_sdwa v242, v242 dst_sel:WORD_1 dst_unused:UNUSED_PRESERVE src0_sel:WORD_1\n"
        "v_mfma_f32_16x16x32_bf16 v[104:107], v[230:233], v[142:145], 0\n"
        "v_exp_f16_sdwa v243, v243 dst_sel:WORD_1 dst_unused:UNUSED_PRESERVE src0_sel:WORD_1\n"
        "v_mfma_f32_16x16x32_bf16 v[104:107], v[234:237], v[40:43], v[104:107]\n"
        "v_pk_add_f16 v113, v113, v242\n"
        "v_pk_add_f16 v113, v113, v243\n"
        "v_mfma_f32_16x16x32_bf16 v[104:107], v[226:229], v[126:129], v[104:107]\n"
        "v_pk_add_f16 v113, v113, v244\n"
        "v_cvt_pk_f16_f32 v247, v140, v141\n"
        "v_mfma_f32_16x16x32_bf16 v[116:119], v[202:205], v[126:129], 0\n"
        "v_cvt_pk_f16_f32 v174, v134, v135\n"
        "v_cvt_pk_f16_f32 v175, v136, v137\n"
        "v_mfma_f32_16x16x32_bf16 v[116:119], v[210:213], v[40:43], v[116:119]\n"
        "v_cvt_pk_f16_f32 v172, v122, v123\n"
        "v_cvt_pk_f16_f32 v173, v124, v125\n"
        "v_mfma_f32_16x16x32_bf16 v[116:119], v[198:201], v[142:145], v[116:119]\n"
        "v_cvt_pk_f16_f32 v180, v108, v109\n"
        "v_cvt_pk_f16_f32 v181, v110, v111\n"
        "v_mfma_f32_16x16x32_bf16 v[124:127], v[222:225], a[240:243], 0\n"
        "v_pk_mul_f16 v180, v180, v35 op_sel_hi:[1,0]\n"
        "v_pk_mul_f16 v181, v181, v35 op_sel_hi:[1,0]\n"
        "v_mfma_f32_16x16x32_bf16 v[124:127], v[206:209], a[244:247], v[124:127]\n"
        "v_pk_mul_f16 v180, v180, v152\n"
        "v_pk_mul_f16 v181, v181, v153\n"
        "v_mfma_f32_16x16x32_bf16 v[124:127], v[218:221], a[248:251], v[124:127]\n"
        "v_exp_f16_sdwa v180, v180 dst_sel:WORD_0 dst_unused:UNUSED_PRESERVE src0_sel:WORD_0\n"
        "v_mfma_f32_16x16x32_bf16 v[120:123], v[230:233], a[248:251], 0\n"
        "v_exp_f16_sdwa v181, v181 dst_sel:WORD_0 dst_unused:UNUSED_PRESERVE src0_sel:WORD_0\n"
        "v_mfma_f32_16x16x32_bf16 v[120:123], v[234:237], a[244:247], v[120:123]\n"
        "v_exp_f16_sdwa v180, v180 dst_sel:WORD_1 dst_unused:UNUSED_PRESERVE src0_sel:WORD_1\n"
        "v_mfma_f32_16x16x32_bf16 v[120:123], v[226:229], a[240:243], v[120:123]\n"
        "v_exp_f16_sdwa v181, v181 dst_sel:WORD_1 dst_unused:UNUSED_PRESERVE src0_sel:WORD_1\n"
        "v_mfma_f32_16x16x32_bf16 v[108:111], v[202:205], a[240:243], 0\n"
        "v_cvt_pk_f16_f32 v182, v104, v105\n"
        "v_cvt_pk_f16_f32 v183, v106, v107\n"
        "v_mfma_f32_16x16x32_bf16 v[108:111], v[210:213], a[244:247], v[108:111]\n"
        "v_pk_mul_f16 v182, v182, v35 op_sel_hi:[1,0]\n"
        "v_pk_mul_f16 v183, v183, v35 op_sel_hi:[1,0]\n"
        "v_mfma_f32_16x16x32_bf16 v[108:111], v[198:201], a[248:251], v[108:111]\n"
        "v_pk_mul_f16 v182, v182, v154\n"
        "v_pk_mul_f16 v183, v183, v155\n"
        "v_mfma_f32_16x16x32_f16 v[144:147], v[56:59], v[112:115], 0\n"
        "v_exp_f16_sdwa v182, v182 dst_sel:WORD_0 dst_unused:UNUSED_PRESERVE src0_sel:WORD_0\n"
        "v_mfma_f32_16x16x32_bf16 v[40:43], v[198:201], v[52:55], 0\n"
        "v_exp_f16_sdwa v183, v183 dst_sel:WORD_0 dst_unused:UNUSED_PRESERVE src0_sel:WORD_0\n"
        "v_mfma_f32_16x16x32_bf16 v[40:43], v[210:213], v[92:95], v[40:43]\n"
        "v_exp_f16_sdwa v182, v182 dst_sel:WORD_1 dst_unused:UNUSED_PRESERVE src0_sel:WORD_1\n"
        "v_mfma_f32_16x16x32_bf16 v[40:43], v[202:205], a[252:255], v[40:43]\n"
        "v_exp_f16_sdwa v183, v183 dst_sel:WORD_1 dst_unused:UNUSED_PRESERVE src0_sel:WORD_1\n"
        "v_mfma_f32_16x16x32_bf16 v[148:151], v[226:229], a[252:255], 0\n"
        "v_cndmask_b32_e64 v252, 0, v253, s[66:67]\n"
        "v_cvt_pk_f16_f32 v250, v116, v117\n"
        "v_mfma_f32_16x16x32_bf16 v[148:151], v[234:237], v[92:95], v[148:151]\n"
        "ds_read2_b32 v[178:179], v24 offset0:0 offset1:0\n"
        "ds_read2_b32 v[248:249], v24 offset0:1 offset1:1\n"
        "v_mfma_f32_16x16x32_bf16 v[148:151], v[230:233], v[52:55], v[148:151]\n"
        "ds_read2_b32 v[240:241], v24 offset0:2 offset1:2\n"
        "ds_read2_b32 v[194:195], v24 offset0:3 offset1:3\n"
        "v_mfma_f32_16x16x32_bf16 v[104:107], v[218:221], v[52:55], 0\n"
        "ds_read2_b32 v[170:171], v24 offset0:4 offset1:4\n"
        "v_mov_b32 v167, 0\n"
        "v_mfma_f32_16x16x32_bf16 v[104:107], v[206:209], v[92:95], v[104:107]\n"
        "ds_read2_b32 v[190:191], v24 offset0:5 offset1:5\n"
        "v_cvt_pk_f16_f32 v251, v118, v119\n"
        "v_mfma_f32_16x16x32_bf16 v[104:107], v[222:225], a[252:255], v[104:107]\n"
        "v_pk_mul_f16 v250, v250, v35 op_sel_hi:[1,0]\n"
        "v_pk_mul_f16 v251, v251, v35 op_sel_hi:[1,0]\n"
        "v_mfma_f32_16x16x32_bf16 v[116:119], v[222:225], v[64:67], 0\n"
        "v_pk_mul_f16 v250, v250, v30\n"
        "v_pk_mul_f16 v251, v251, v31\n"
        "v_mfma_f32_16x16x32_bf16 v[116:119], v[206:209], v[84:87], v[116:119]\n"
        "v_exp_f16_sdwa v250, v250 dst_sel:WORD_0 dst_unused:UNUSED_PRESERVE src0_sel:WORD_0\n"
        "v_mfma_f32_16x16x32_bf16 v[116:119], v[218:221], v[76:79], v[116:119]\n"
        "v_exp_f16_sdwa v251, v251 dst_sel:WORD_0 dst_unused:UNUSED_PRESERVE src0_sel:WORD_0\n"
        "v_mfma_f32_16x16x32_bf16 v[140:143], v[230:233], v[76:79], 0\n"
        "v_exp_f16_sdwa v250, v250 dst_sel:WORD_1 dst_unused:UNUSED_PRESERVE src0_sel:WORD_1\n"
        "v_mfma_f32_16x16x32_bf16 v[140:143], v[234:237], v[84:87], v[140:143]\n"
        "v_exp_f16_sdwa v251, v251 dst_sel:WORD_1 dst_unused:UNUSED_PRESERVE src0_sel:WORD_1\n"
        "v_mfma_f32_16x16x32_bf16 v[140:143], v[226:229], v[64:67], v[140:143]\n"
        "v_cvt_pk_f16_f32 v34, v124, v125\n"
        "v_cvt_pk_f16_f32 v35, v126, v127\n"
        "v_mfma_f32_16x16x32_bf16 v[128:131], v[202:205], v[64:67], 0\n"
        "s_waitcnt lgkmcnt(0)\n"
        "v_lshlrev_b32 v184, 16, v178\n"
        "v_lshlrev_b32 v185, 16, v248\n"
        "v_mfma_f32_16x16x32_bf16 v[128:131], v[210:213], v[84:87], v[128:131]\n"
        "v_lshlrev_b32 v186, 16, v240\n"
        "v_lshlrev_b32 v187, 16, v194\n"
        "v_mfma_f32_16x16x32_bf16 v[128:131], v[198:201], v[76:79], v[128:131]\n"
        "v_lshlrev_b32 v196, 16, v170\n"
        "v_lshlrev_b32 v197, 16, v190\n"
        "v_mfma_f32_16x16x32_bf16 v[136:139], v[198:201], v[48:51], 0\n"
        "v_cvt_pk_f16_f32 v36, v120, v121\n"
        "v_cvt_pk_f16_f32 v37, v122, v123\n"
        "v_mfma_f32_16x16x32_bf16 v[136:139], v[210:213], v[72:75], v[136:139]\n"
        "v_pk_add_f16 v114, v180, v181\n"
        "v_pk_add_f16 v114, v114, v182\n"
        "v_mfma_f32_16x16x32_bf16 v[136:139], v[202:205], v[60:63], v[136:139]\n"
        "s_waitcnt vmcnt(1)\n"
        "v_pk_add_f16 v114, v114, v183\n"
        "v_pk_add_f16 v114, v114, v250\n"
        "v_mfma_f32_16x16x32_bf16 v[132:135], v[226:229], v[60:63], 0\n"
        "v_readfirstlane_b32 s50, v26\n"
        "s_lshl_b32 s92, s15, 3\n"
        "s_add_u32 s92, s92, s48\n"
        "v_mov_b32 v27, s49\n"
        "s_nop 0\n"
        "s_cmp_eq_u32 s50, s21\n"
        "s_cselect_b32 s92, s92, 0x10000\n"
        "v_mov_b32 v26, s92\n"
        "ds_write_b32 v27, v26 offset:8\n"
        "v_mfma_f32_16x16x32_bf16 v[132:135], v[234:237], v[72:75], v[132:135]\n"
        "v_cvt_pk_f16_f32 v238, v108, v109\n"
        "v_cvt_pk_f16_f32 v239, v110, v111\n"
        "v_mfma_f32_16x16x32_bf16 v[132:135], v[230:233], v[48:51], v[132:135]\n"
        "v_pk_add_f16 v114, v114, v251\n"
        "v_pk_add_f16 v114, v114, v252\n"
        "v_mfma_f32_16x16x32_bf16 v[124:127], v[218:221], v[48:51], 0\n"
        "v_mov_b32 v245, 0\n"
        "v_max3_f32 v255, s95, v144, v145\n"
        "v_mfma_f32_16x16x32_bf16 v[124:127], v[206:209], v[72:75], v[124:127]\n"
        "v_rcp_f32 v253, v144\n"
        "v_mfma_f32_16x16x32_bf16 v[124:127], v[222:225], v[60:63], v[124:127]\n"
        "v_cvt_f16_f32 v193, v253\n"
        "v_pk_mul_f16 v214, v214, v193 op_sel_hi:[1,0]\n"
        "v_mfma_f32_16x16x32_f16 v[108:111], v[56:59], v[112:115], 0\n"
        "v_pk_mul_f16 v215, v215, v193 op_sel_hi:[1,0]\n"
        "v_pk_mul_f16 v216, v216, v193 op_sel_hi:[1,0]\n"
        "v_pk_mul_f16 v214, v214, v152\n"
        "v_pk_mul_f16 v215, v215, v153\n"
        "v_pk_mul_f16 v216, v216, v154\n"
        "v_pk_mul_f16 v217, v217, v193 op_sel_hi:[1,0]\n"
        "v_pk_mul_f16 v217, v217, v155\n"
        "s_nop 1\n"
        "v_mfma_f32_16x16x32_f16 v[120:123], v[214:217], v[160:163], 0\n"
        "v_pk_mul_f16 v164, v164, v193 op_sel_hi:[1,0]\n"
        "v_pk_mul_f16 v165, v165, v193 op_sel_hi:[1,0]\n"
        "v_pk_mul_f16 v166, v166, v193 op_sel_hi:[1,0]\n"
        "v_pk_mul_f16 v164, v164, v30\n"
        "v_pk_mul_f16 v165, v165, v31\n"
        "s_nop 1\n"
        "v_mfma_f32_16x16x32_f16 v[120:123], v[164:167], v[176:179], v[120:123]\n"
        "v_rcp_f32 v193, v145\n"
        "v_mfma_f32_16x16x32_bf16 v[120:123], v[222:225], v[88:91], v[120:123]\n"
        "v_add_f32 v111, v254, v253\n"
        "v_cvt_f16_f32 v254, v193\n"
        "v_mfma_f32_16x16x32_bf16 v[112:115], v[222:225], v[80:83], 0\n"
        "v_add_f32 v111, v111, v193\n"
        "v_pk_mul_f16 v156, v156, v254 op_sel_hi:[1,0]\n"
        "v_mfma_f32_16x16x32_f16 v[112:115], v[164:167], v[246:249], v[112:115]\n"
        "v_pk_mul_f16 v157, v157, v254 op_sel_hi:[1,0]\n"
        "v_pk_mul_f16 v158, v158, v254 op_sel_hi:[1,0]\n"
        "v_mfma_f32_16x16x32_f16 v[112:115], v[214:217], v[172:175], v[112:115]\n"
        "v_pk_mul_f16 v159, v159, v254 op_sel_hi:[1,0]\n"
        "v_pk_mul_f16 v242, v242, v254 op_sel_hi:[1,0]\n"
        "v_mfma_f32_16x16x32_f16 v[144:147], v[214:217], v[34:37], 0\n"
        "v_pk_mul_f16 v243, v243, v254 op_sel_hi:[1,0]\n"
        "v_pk_mul_f16 v244, v244, v254 op_sel_hi:[1,0]\n"
        "v_mfma_f32_16x16x32_f16 v[144:147], v[164:167], v[238:241], v[144:147]\n"
        "v_cvt_pk_f16_f32 v192, v40, v41\n"
        "v_cvt_pk_f16_f32 v193, v42, v43\n"
        "v_mfma_f32_16x16x32_bf16 v[144:147], v[206:209], v[88:91], v[144:147]\n"
        "v_cvt_pk_f16_f32 v42, v148, v149\n"
        "v_cvt_pk_f16_f32 v43, v150, v151\n"
        "v_mfma_f32_16x16x32_bf16 v[148:151], v[206:209], v[80:83], 0\n"
        "v_cvt_pk_f16_f32 v40, v104, v105\n"
        "v_cvt_pk_f16_f32 v41, v106, v107\n"
        "v_mfma_f32_16x16x32_f16 v[148:151], v[164:167], v[192:195], v[148:151]\n"
        "v_cvt_pk_f16_f32 v104, v116, v117\n"
        "v_cvt_pk_f16_f32 v105, v118, v119\n"
        "v_mfma_f32_16x16x32_f16 v[148:151], v[214:217], v[40:43], v[148:151]\n"
        "v_cvt_pk_f16_f32 v106, v140, v141\n"
        "v_cvt_pk_f16_f32 v107, v142, v143\n"
        "s_nop 1\n"
        "v_mfma_f32_16x16x32_f16 v[116:119], v[214:217], v[104:107], 0\n"
        "v_cvt_pk_f16_f32 v168, v128, v129\n"
        "v_cvt_pk_f16_f32 v169, v130, v131\n"
        "s_nop 1\n"
        "v_mfma_f32_16x16x32_f16 v[116:119], v[164:167], v[168:171], v[116:119]\n"
        "v_pk_mul_f16 v156, v156, v152\n"
        "v_pk_mul_f16 v157, v157, v153\n"
        "v_mfma_f32_16x16x32_bf16 v[116:119], v[218:221], v[88:91], v[116:119]\n"
        "v_mov_b32 v253, 0\n"
        "v_cvt_pk_f16_f32 v188, v136, v137\n"
        "v_mfma_f32_16x16x32_bf16 v[128:131], v[218:221], v[80:83], 0\n"
        "v_cvt_pk_f16_f32 v189, v138, v139\n"
        "v_cvt_pk_f16_f32 v138, v132, v133\n"
        "s_nop 0\n"
        "v_mfma_f32_16x16x32_f16 v[128:131], v[164:167], v[188:191], v[128:131]\n"
        "v_cvt_pk_f16_f32 v139, v134, v135\n"
        "v_cvt_pk_f16_f32 v136, v124, v125\n"
        "v_cvt_pk_f16_f32 v137, v126, v127\n"
        "s_nop 1\n"
        "v_mfma_f32_16x16x32_f16 v[128:131], v[214:217], v[136:139], v[128:131]\n"
        "s_add_u32 s90, s89, 0\n"
        "v_pk_mul_f16 v158, v158, v154\n"
        "v_pk_mul_f16 v159, v159, v155\n"
        "s_nop 1\n"
        "v_mfma_f32_16x16x32_f16 v[164:167], v[156:159], v[160:163], 0\n"
        "v_pk_mul_f16 v242, v242, v30\n"
        "v_pk_mul_f16 v243, v243, v31\n"
        "s_nop 1\n"
        "v_mfma_f32_16x16x32_f16 v[164:167], v[242:245], v[176:179], v[164:167]\n"
        "v_rcp_f32 v254, v110\n"
        "v_mfma_f32_16x16x32_bf16 v[164:167], v[226:229], v[88:91], v[164:167]\n"
        "v_max_f32 v255, v255, v110\n"
        "v_add_f32 v111, v111, v254\n"
        "v_mfma_f32_16x16x32_bf16 v[140:143], v[226:229], v[80:83], 0\n"
        "v_fma_f32 v255, v111, s82, v255\n"
        "v_cmp_nlt_f32_e64 s[74:75], v255, s83\n"
        "v_mfma_f32_16x16x32_f16 v[140:143], v[242:245], v[246:249], v[140:143]\n"
        "v_cvt_pk_bf16_f32 v110, v120, v112\n"
        "v_cvt_pk_bf16_f32 v111, v144, v148\n"
        "v_mfma_f32_16x16x32_f16 v[140:143], v[156:159], v[172:175], v[140:143]\n"
        "s_cmp_lg_u64 s[74:75], 0\n"
        "s_cbranch_scc1 SLOW_%=\n"
        "v_cvt_pk_bf16_f32 v112, v116, v128\n"
        "v_mfma_f32_16x16x32_f16 v[124:127], v[156:159], v[34:37], 0\n"
        "buffer_store_dwordx3 v[110:112], v14, s[40:43], s90 offen sc1 nt\n"
        "v_cvt_pk_bf16_f32 v108, v121, v113\n"
        "v_cvt_pk_bf16_f32 v109, v145, v149\n"
        "v_cvt_pk_bf16_f32 v110, v117, v129\n"
        "v_cvt_f16_f32 v255, v254\n"
        "v_mfma_f32_16x16x32_f16 v[124:127], v[242:245], v[238:241], v[124:127]\n"
        "s_add_u32 s91, s89, 98112\n"
        "v_cvt_pk_bf16_f32 v132, v122, v114\n"
        "v_cvt_pk_bf16_f32 v133, v146, v150\n"
        "v_mfma_f32_16x16x32_bf16 v[124:127], v[234:237], v[88:91], v[124:127]\n"
        "buffer_store_dwordx3 v[108:110], v15, s[40:43], s90 offen offset:768 sc1 nt\n"
        "v_cvt_pk_bf16_f32 v134, v118, v130\n"
        "v_pk_mul_f16 v180, v180, v255 op_sel_hi:[1,0]\n"
        "v_cvt_pk_bf16_f32 v108, v123, v115\n"
        "v_cvt_pk_bf16_f32 v109, v147, v151\n"
        "v_mfma_f32_16x16x32_bf16 v[112:115], v[234:237], v[80:83], 0\n"
        "ds_read_b128 v[206:209], v20 offset:512\n"
        "ds_read_b128 v[120:123], v20 offset:528\n"
        "v_mfma_f32_16x16x32_f16 v[112:115], v[242:245], v[192:195], v[112:115]\n"
        "buffer_store_dwordx3 v[132:134], v14, s[40:43], s91 offen offset:192 sc1 nt\n"
        "v_cvt_pk_bf16_f32 v110, v119, v131\n"
        "v_pk_mul_f16 v181, v181, v255 op_sel_hi:[1,0]\n"
        "v_pk_mul_f16 v182, v182, v255 op_sel_hi:[1,0]\n"
        "v_pk_mul_f16 v183, v183, v255 op_sel_hi:[1,0]\n"
        "v_mfma_f32_16x16x32_f16 v[112:115], v[156:159], v[40:43], v[112:115]\n"
        "v_pk_mul_f16 v250, v250, v255 op_sel_hi:[1,0]\n"
        "v_pk_mul_f16 v251, v251, v255 op_sel_hi:[1,0]\n"
        "v_mfma_f32_16x16x32_f16 v[116:119], v[156:159], v[104:107], 0\n"
        "buffer_store_dwordx3 v[108:110], v16, s[40:43], s91 offen offset:960 sc1 nt\n"
        "v_pk_mul_f16 v252, v252, v255 op_sel_hi:[1,0]\n"
        "v_pk_mul_f16 v180, v180, v152\n"
        "v_pk_mul_f16 v181, v181, v153\n"
        "v_pk_mul_f16 v182, v182, v154\n"
        "v_mfma_f32_16x16x32_f16 v[116:119], v[242:245], v[168:171], v[116:119]\n"
        "v_pk_mul_f16 v183, v183, v155\n"
        "v_pk_mul_f16 v250, v250, v30\n"
        "v_mfma_f32_16x16x32_bf16 v[116:119], v[230:233], v[88:91], v[116:119]\n"
        "v_pk_mul_f16 v251, v251, v31\n"
        "v_cvt_pk_bf16_f32 v214, v164, v140\n"
        "v_mfma_f32_16x16x32_bf16 v[128:131], v[230:233], v[80:83], 0\n"
        "v_cvt_pk_bf16_f32 v215, v124, v112\n"
        "v_cvt_pk_bf16_f32 v218, v165, v141\n"
        "v_mfma_f32_16x16x32_f16 v[128:131], v[242:245], v[188:191], v[128:131]\n"
        "s_waitcnt lgkmcnt(0)\n"
        "v_pk_mul_f16 v108, v206, v160\n"
        "v_pk_fma_f16 v108, v207, v161, v108\n"
        "v_mfma_f32_16x16x32_f16 v[128:131], v[156:159], v[136:139], v[128:131]\n"
        "v_pk_fma_f16 v108, v208, v162, v108\n"
        "v_pk_fma_f16 v108, v209, v163, v108\n"
        "v_mfma_f32_16x16x32_f16 v[148:151], v[180:183], v[160:163], 0\n"
        "v_pk_fma_f16 v108, v120, v176, v108\n"
        "v_pk_fma_f16 v108, v121, v177, v108\n"
        "v_mfma_f32_16x16x32_f16 v[148:151], v[250:253], v[176:179], v[148:151]\n"
        "s_add_u32 s90, s89, 196224\n"
        "v_pk_fma_f16 v108, v122, v178, v108\n"
        "v_pk_mul_f16 v109, v206, v172\n"
        "v_mfma_f32_16x16x32_bf16 v[148:151], v[202:205], v[88:91], v[148:151]\n"
        "v_pk_fma_f16 v109, v207, v173, v109\n"
        "v_pk_fma_f16 v109, v208, v174, v109\n"
        "v_mfma_f32_16x16x32_bf16 v[144:147], v[202:205], v[80:83], 0\n"
        "v_pk_fma_f16 v109, v209, v175, v109\n"
        "v_pk_fma_f16 v109, v120, v246, v109\n"
        "v_mfma_f32_16x16x32_f16 v[144:147], v[250:253], v[246:249], v[144:147]\n"
        "v_pk_fma_f16 v109, v121, v247, v109\n"
        "v_pk_fma_f16 v109, v122, v248, v109\n"
        "v_mfma_f32_16x16x32_f16 v[144:147], v[180:183], v[172:175], v[144:147]\n"
        "s_add_u32 s91, s89, 294336\n"
        "v_pk_mul_f16 v110, v206, v34\n"
        "v_pk_fma_f16 v110, v207, v35, v110\n"
        "v_mfma_f32_16x16x32_f16 v[132:135], v[180:183], v[34:37], 0\n"
        "v_pk_fma_f16 v110, v208, v36, v110\n"
        "v_pk_fma_f16 v110, v209, v37, v110\n"
        "v_mfma_f32_16x16x32_f16 v[132:135], v[250:253], v[238:241], v[132:135]\n"
        "v_pk_fma_f16 v110, v120, v238, v110\n"
        "v_pk_fma_f16 v110, v121, v239, v110\n"
        "v_mfma_f32_16x16x32_bf16 v[132:135], v[210:213], v[88:91], v[132:135]\n"
        "v_pk_fma_f16 v110, v122, v240, v110\n"
        "v_pk_mul_f16 v111, v206, v40\n"
        "v_mfma_f32_16x16x32_bf16 v[152:155], v[210:213], v[80:83], 0\n"
        "v_pk_fma_f16 v111, v207, v41, v111\n"
        "v_pk_fma_f16 v111, v208, v42, v111\n"
        "v_mfma_f32_16x16x32_f16 v[152:155], v[250:253], v[192:195], v[152:155]\n"
        "v_pk_fma_f16 v111, v209, v43, v111\n"
        "v_pk_fma_f16 v111, v120, v192, v111\n"
        "v_mfma_f32_16x16x32_f16 v[152:155], v[180:183], v[40:43], v[152:155]\n"
        "v_pk_fma_f16 v111, v121, v193, v111\n"
        "v_cvt_pk_bf16_f32 v216, v116, v128\n"
        "v_mfma_f32_16x16x32_f16 v[34:37], v[180:183], v[104:107], 0\n"
        "buffer_store_dwordx3 v[214:216], v14, s[40:43], s90 offen offset:384 sc1 nt\n"
        "v_pk_fma_f16 v111, v122, v194, v111\n"
        "v_cvt_pk_bf16_f32 v219, v125, v113\n"
        "v_cvt_pk_bf16_f32 v220, v117, v129\n"
        "v_pk_mul_f16 v112, v206, v104\n"
        "v_mfma_f32_16x16x32_f16 v[34:37], v[250:253], v[168:171], v[34:37]\n"
        "buffer_store_dwordx3 v[218:220], v17, s[40:43], s90 offen offset:1152 sc1 nt\n"
        "v_cvt_pk_bf16_f32 v40, v166, v142\n"
        "v_cvt_pk_bf16_f32 v41, v126, v114\n"
        "v_cvt_pk_bf16_f32 v42, v118, v130\n"
        "v_pk_fma_f16 v112, v207, v105, v112\n"
        "v_mfma_f32_16x16x32_bf16 v[34:37], v[198:201], v[88:91], v[34:37]\n"
        "buffer_store_dwordx3 v[40:42], v14, s[40:43], s91 offen offset:576 sc1 nt\n"
        "v_cvt_pk_bf16_f32 v116, v167, v143\n"
        "v_cvt_pk_bf16_f32 v117, v127, v115\n"
        "s_add_u32 s90, s89, 392448\n"
        "v_cvt_pk_bf16_f32 v118, v119, v131\n"
        "v_mfma_f32_16x16x32_bf16 v[40:43], v[198:201], v[80:83], 0\n"
        "buffer_store_dwordx3 v[116:118], v14, s[40:43], s90 offen sc1 nt\n"
        "v_pk_fma_f16 v112, v208, v106, v112\n"
        "v_pk_fma_f16 v112, v209, v107, v112\n"
        "v_pk_fma_f16 v112, v120, v168, v112\n"
        "v_pk_fma_f16 v112, v121, v169, v112\n"
        "v_mfma_f32_16x16x32_f16 v[40:43], v[250:253], v[188:191], v[40:43]\n"
        "v_pk_fma_f16 v112, v122, v170, v112\n"
        "v_pk_mul_f16 v113, v206, v136\n"
        "v_mfma_f32_16x16x32_f16 v[40:43], v[180:183], v[136:139], v[40:43]\n"
        "v_pk_fma_f16 v113, v207, v137, v113\n"
        "v_pk_fma_f16 v113, v208, v138, v113\n"
        "v_mfma_f32_16x16x32_f16 v[184:187], v[56:59], v[108:111], v[184:187]\n"
        "s_add_u32 s46, s89, 590016\n"
        "v_pk_fma_f16 v113, v209, v139, v113\n"
        "v_pk_fma_f16 v113, v120, v188, v113\n"
        "v_pk_fma_f16 v113, v121, v189, v113\n"
        "v_pk_fma_f16 v113, v122, v190, v113\n"
        "s_nop 1\n"
        "v_mfma_f32_16x16x32_f16 v[194:197], v[56:59], v[110:113], v[194:197]\n"
        "v_cvt_pk_bf16_f32 v110, v148, v144\n"
        "v_cvt_pk_bf16_f32 v114, v149, v145\n"
        "v_cvt_pk_bf16_f32 v104, v150, v146\n"
        "v_cvt_pk_bf16_f32 v108, v151, v147\n"
        "v_cvt_pk_bf16_f32 v111, v132, v152\n"
        "v_cvt_pk_bf16_f32 v115, v133, v153\n"
        "v_cvt_pk_bf16_f32 v112, v34, v40\n"
        "buffer_store_dwordx3 v[110:112], v15, s[40:43], s90 offen offset:768 sc1 nt\n"
        "v_cvt_pk_bf16_f32 v105, v134, v154\n"
        "v_cvt_pk_bf16_f32 v109, v135, v155\n"
        "v_cvt_pk_bf16_f32 v116, v35, v41\n"
        "s_add_u32 s91, s89, 490560\n"
        "v_cvt_pk_bf16_f32 v106, v36, v42\n"
        "buffer_store_dwordx3 v[114:116], v14, s[40:43], s91 offen offset:192 sc1 nt\n"
        "s_min_u32 s46, s46, s42\n"
        "v_cvt_pk_bf16_f32 v110, v37, v43\n"
        "v_cvt_pk_bf16_f32 v34, v184, v185\n"
        "v_cvt_pk_bf16_f32 v35, v186, v187\n"
        "v_cvt_pk_bf16_f32 v36, v196, v197\n"
        "buffer_store_dwordx3 v[104:106], v16, s[40:43], s91 offen offset:960 sc1 nt\n"
        "s_add_u32 s90, s89, 588672\n"
        "buffer_store_dwordx3 v[108:110], v14, s[40:43], s90 offen offset:384 sc1 nt\n"
        "buffer_store_dwordx3 v[34:36], v17, s[44:47], s90 offen offset:1152 sc1 nt\n"
        "FIN_%=:\n"
        "s_waitcnt lgkmcnt(0)\n"
        "s_barrier\n"
        "v_mov_b32 v27, 163432\n"
        "ds_read_b32 v27, v27\n"
        "s_waitcnt lgkmcnt(0)\n"
        "v_readfirstlane_b32 s50, v27\n"
        "s_nop 3\n"
        "s_cmp_lt_u32 s50, 5329\n"
        "s_cbranch_scc1 POST_%=\n"
        ".Lswin_fin:\n"
        "s_endpgm\n"
        "POST_%=:\n"
        "s_barrier\n"
        "s_mul_i32 s87, s50, 1344\n"
        "s_mul_i32 s92, s50, 57457\n"
        "s_lshr_b32 s92, s92, 22\n"
        "s_mul_i32 s92, s92, 588672\n"
        "s_add_u32 s87, s87, s92\n"
        "s_lshr_b32 s23, s15, 2\n"
        "s_add_u32 s23, s23, 20\n"
        "s_sub_u32 s23, s23, s53\n"
        "s_mul_i32 s23, s23, 1584\n"
        "s_mov_b32 s22, 0\n"
        "s_mov_b32 s48, 0x10000\n"
        "s_mov_b32 s18, 0\n"
        "s_add_u32 m0, s52, 0\n"
        "s_nop 0\n"
        "buffer_load_dwordx4 v0, s[36:39], s87 offen lds\n"
        "buffer_load_dwordx4 v1, s[36:39], s87 offen offset:1024 lds\n"
        "buffer_load_dwordx4 v2, s[36:39], s87 offen offset:2048 lds\n"
        "buffer_load_dwordx4 v3, s[36:39], s87 offen offset:3072 lds\n"
        "s_add_u32 m0, s52, 4096\n"
        "s_nop 0\n"
        "buffer_load_dwordx4 v4, s[36:39], s87 offen lds\n"
        "buffer_load_dwordx4 v5, s[36:39], s87 offen offset:1024 lds\n"
        "buffer_load_dwordx4 v6, s[36:39], s87 offen offset:2048 lds\n"
        "buffer_load_dwordx4 v7, s[36:39], s87 offen offset:3072 lds\n"
        "s_add_u32 m0, s52, 8192\n"
        "s_nop 0\n"
        "buffer_load_dwordx4 v8, s[36:39], s87 offen lds\n"
        "s_waitcnt vmcnt(0)\n"
        "ds_read_b128 v[146:149], v9\n"
        "ds_read_b128 v[152:155], v10\n"
        "ds_read_b128 v[158:161], v11\n"
        "ds_read_b128 v[126:129], v9 offset:3072\n"
        "ds_read_b128 v[132:135], v10 offset:3072\n"
        "ds_read_b128 v[138:141], v11 offset:3072\n"
        "ds_read_b128 v[106:109], v9 offset:6144\n"
        "ds_read_b128 v[112:115], v10 offset:6144\n"
        "ds_read_b128 v[118:121], v11 offset:6144\n"
        "s_mov_b32 s86, 0x80000000\n"
        "s_mov_b32 s89, 0x80000000\n"
        "s_mov_b32 s85, 0\n"
        "s_sub_u32 s29, s29, 1\n"
        "s_waitcnt lgkmcnt(0)\n"
        "s_nop 7\n"
        "s_branch TOP_%=\n"
        "SLOW_%=:\n"
        "s_waitcnt lgkmcnt(0)\n"
        "s_nop 15\n"
        "v_mbcnt_lo_u32_b32 v255, -1, 0\n"
        "v_mbcnt_hi_u32_b32 v255, -1, v255\n"
        "v_and_b32 v254, 15, v255\n"
        "v_mul_u32_u24 v252, 24, v254\n"
        "v_sub_u32 v253, v24, v252\n"
        "v_lshrrev_b32 v250, 4, v255\n"
        "v_mul_u32_u24 v252, 96, v250\n"
        "v_add_u32 v253, v253, v252\n"
        "ds_read_b64 v[132:133], v253\n"
        "ds_read_b64 v[30:31], v253 offset:24\n"
        "ds_read_b64 v[34:35], v253 offset:48\n"
        "ds_read_b64 v[46:47], v253 offset:72\n"
        "ds_read_b64 v[40:41], v253 offset:8\n"
        "ds_read_b64 v[136:137], v253 offset:32\n"
        "ds_read_b64 v[104:105], v253 offset:56\n"
        "ds_read_b64 v[144:145], v253 offset:80\n"
        "ds_read_b64 v[124:125], v253 offset:16\n"
        "ds_read_b64 v[116:117], v253 offset:40\n"
        "ds_read_b64 v[108:109], v253 offset:64\n"
        "ds_read_b64 v[140:141], v253 offset:88\n"
        "v_mfma_f32_16x16x32_bf16 v[120:123], v[222:225], v[222:225], 0\n"
        "v_mov_b32 v253, 0x05040100\n"
        "v_subrev_u32 v248, 159328, v20\n"
        "v_mfma_f32_16x16x32_bf16 v[120:123], v[206:209], v[206:209], v[120:123]\n"
        "v_mov_b32 v249, 161888\n"
        "v_mov_b32 v166, 0x3f803f80\n"
        "v_mfma_f32_16x16x32_bf16 v[120:123], v[218:221], v[218:221], v[120:123]\n"
        "v_mov_b32 v167, 0x3f803f80\n"
        "v_mov_b32 v168, 0x3f803f80\n"
        "v_mov_b32 v169, 0x3f803f80\n"
        "s_nop 1\n"
        "v_mfma_f32_16x16x32_bf16 v[112:115], v[166:169], v[218:221], 0\n"
        "v_mov_b32 v255, 0xff800000\n"
        "v_mov_b32 v139, 0\n"
        "v_mfma_f32_16x16x32_bf16 v[112:115], v[166:169], v[206:209], v[112:115]\n"
        "s_waitcnt lgkmcnt(11)\n"
        "v_mov_b32 v43, 0\n"
        "v_mov_b32 v143, 0\n"
        "v_mfma_f32_16x16x32_bf16 v[112:115], v[166:169], v[222:225], v[112:115]\n"
        "s_waitcnt lgkmcnt(10)\n"
        "v_mov_b32 v119, 0\n"
        "v_mov_b32 v107, 0\n"
        "v_mfma_f32_16x16x32_bf16 v[128:131], v[226:229], v[226:229], 0\n"
        "s_waitcnt lgkmcnt(9)\n"
        "v_mov_b32 v37, 0\n"
        "v_mov_b32 v111, 0\n"
        "v_mfma_f32_16x16x32_bf16 v[128:131], v[234:237], v[234:237], v[128:131]\n"
        "s_waitcnt lgkmcnt(8)\n"
        "v_mov_b32 v147, 0\n"
        "v_mov_b32 v135, 0\n"
        "v_mfma_f32_16x16x32_bf16 v[128:131], v[230:233], v[230:233], v[128:131]\n"
        "s_waitcnt lgkmcnt(7)\n"
        "v_mov_b32 v127, 0\n"
        "v_and_b32 v248, 384, v248\n"
        "v_mfma_f32_16x16x32_bf16 v[162:165], v[166:169], v[230:233], 0\n"
        "s_waitcnt lgkmcnt(6)\n"
        "v_lshlrev_b32 v248, 1, v248\n"
        "v_add_u32 v248, 160864, v248\n"
        "v_mfma_f32_16x16x32_bf16 v[162:165], v[166:169], v[234:237], v[162:165]\n"
        "s_waitcnt lgkmcnt(5)\n"
        "v_lshlrev_b32 v251, 2, v254\n"
        "v_lshlrev_b32 v252, 4, v250\n"
        "v_mfma_f32_16x16x32_bf16 v[162:165], v[166:169], v[226:229], v[162:165]\n"
        "s_waitcnt lgkmcnt(4)\n"
        "v_add_u32 v251, v251, v248\n"
        "v_add_u32 v252, v252, v248\n"
        "v_mfma_f32_16x16x32_bf16 v[152:155], v[202:205], v[202:205], 0\n"
        "s_waitcnt lgkmcnt(3)\n"
        "v_cndmask_b32 v249, v249, v251, vcc\n"
        "v_cndmask_b32_e64 v254, v120, v121, s[56:57]\n"
        "v_mfma_f32_16x16x32_bf16 v[152:155], v[210:213], v[210:213], v[152:155]\n"
        "s_waitcnt lgkmcnt(2)\n"
        "v_cndmask_b32_e64 v254, v254, v122, s[58:59]\n"
        "v_cndmask_b32_e64 v254, v254, v123, s[60:61]\n"
        "v_mfma_f32_16x16x32_bf16 v[152:155], v[198:201], v[198:201], v[152:155]\n"
        "s_waitcnt lgkmcnt(1)\n"
        "v_mul_f32 v254, s80, v254\n"
        "v_mul_f32 v250, s80, v112\n"
        "v_mfma_f32_16x16x32_bf16 v[156:159], v[166:169], v[198:201], 0\n"
        "s_waitcnt lgkmcnt(0)\n"
        "v_fma_f32 v250, -v250, v250, v254\n"
        "v_max_f32 v250, 0, v250\n"
        "v_mfma_f32_16x16x32_bf16 v[156:159], v[166:169], v[210:213], v[156:159]\n"
        "v_add_f32 v250, s81, v250\n"
        "v_perm_b32 v120, v133, v132, v253\n"
        "v_mfma_f32_16x16x32_bf16 v[156:159], v[166:169], v[202:205], v[156:159]\n"
        "v_rsq_f32 v250, v250\n"
        "v_perm_b32 v121, v31, v30, v253\n"
        "v_perm_b32 v122, v35, v34, v253\n"
        "v_perm_b32 v123, v47, v46, v253\n"
        "s_nop 1\n"
        "v_mfma_f32_16x16x32_bf16 v[170:173], v[120:123], v[120:123], 0\n"
        "ds_write_b32 v249, v250\n"
        "v_perm_b32 v112, v41, v40, v253\n"
        "v_perm_b32 v113, v137, v136, v253\n"
        "v_perm_b32 v114, v105, v104, v253\n"
        "v_perm_b32 v115, v145, v144, v253\n"
        "s_nop 1\n"
        "v_mfma_f32_16x16x32_bf16 v[170:173], v[112:115], v[112:115], v[170:173]\n"
        "v_perm_b32 v148, v125, v124, v253\n"
        "v_perm_b32 v149, v117, v116, v253\n"
        "v_perm_b32 v150, v109, v108, v253\n"
        "v_perm_b32 v151, v141, v140, v253\n"
        "s_nop 1\n"
        "v_mfma_f32_16x16x32_bf16 v[170:173], v[148:151], v[148:151], v[170:173]\n"
        "v_cndmask_b32_e64 v254, v128, v129, s[56:57]\n"
        "v_cndmask_b32_e64 v254, v254, v130, s[58:59]\n"
        "v_mfma_f32_16x16x32_bf16 v[158:161], v[166:169], v[148:151], 0\n"
        "v_cndmask_b32_e64 v254, v254, v131, s[60:61]\n"
        "v_mul_f32 v254, s80, v254\n"
        "v_mfma_f32_16x16x32_bf16 v[158:161], v[166:169], v[112:115], v[158:161]\n"
        "v_mul_f32 v253, s80, v162\n"
        "v_fma_f32 v253, -v253, v253, v254\n"
        "v_mfma_f32_16x16x32_bf16 v[158:161], v[166:169], v[120:123], v[158:161]\n"
        "v_max_f32 v253, 0, v253\n"
        "v_add_f32 v253, s81, v253\n"
        "v_mfma_f32_16x16x32_bf16 v[130:133], v[222:225], a[216:219], 0\n"
        "v_rsq_f32 v253, v253\n"
        "v_mfma_f32_16x16x32_bf16 v[130:133], v[206:209], a[220:223], v[130:133]\n"
        "ds_write_b32 v249, v253 offset:64\n"
        "v_mfma_f32_16x16x32_bf16 v[130:133], v[218:221], a[224:227], v[130:133]\n"
        "v_cndmask_b32_e64 v254, v152, v153, s[56:57]\n"
        "v_cndmask_b32_e64 v254, v254, v154, s[58:59]\n"
        "v_mfma_f32_16x16x32_bf16 v[164:167], v[230:233], a[224:227], 0\n"
        "v_cndmask_b32_e64 v254, v254, v155, s[60:61]\n"
        "v_mul_f32 v254, s80, v254\n"
        "v_mfma_f32_16x16x32_bf16 v[164:167], v[234:237], a[220:223], v[164:167]\n"
        "v_mul_f32 v253, s80, v156\n"
        "v_fma_f32 v253, -v253, v253, v254\n"
        "v_mfma_f32_16x16x32_bf16 v[164:167], v[226:229], a[216:219], v[164:167]\n"
        "v_max_f32 v253, 0, v253\n"
        "v_add_f32 v253, s81, v253\n"
        "v_mfma_f32_16x16x32_bf16 v[160:163], v[202:205], a[216:219], 0\n"
        "v_rsq_f32 v253, v253\n"
        "v_mfma_f32_16x16x32_bf16 v[160:163], v[210:213], a[220:223], v[160:163]\n"
        "ds_write_b32 v249, v253 offset:128\n"
        "v_mfma_f32_16x16x32_bf16 v[160:163], v[198:201], a[224:227], v[160:163]\n"
        "v_cndmask_b32_e64 v253, v170, v171, s[56:57]\n"
        "v_cndmask_b32_e64 v253, v253, v172, s[58:59]\n"
        "v_mfma_f32_16x16x32_bf16 v[152:155], v[148:151], a[224:227], 0\n"
        "v_cndmask_b32_e64 v253, v253, v173, s[60:61]\n"
        "v_mul_f32 v253, s80, v253\n"
        "v_mfma_f32_16x16x32_bf16 v[152:155], v[112:115], a[220:223], v[152:155]\n"
        "v_mul_f32 v254, s80, v158\n"
        "v_fma_f32 v254, -v254, v254, v253\n"
        "v_mfma_f32_16x16x32_bf16 v[152:155], v[120:123], a[216:219], v[152:155]\n"
        "v_max_f32 v254, 0, v254\n"
        "v_add_f32 v254, s81, v254\n"
        "v_mfma_f32_16x16x32_bf16 v[156:159], v[202:205], a[228:231], 0\n"
        "v_rsq_f32 v254, v254\n"
        "v_mfma_f32_16x16x32_bf16 v[156:159], v[210:213], a[232:235], v[156:159]\n"
        "ds_write_b32 v249, v254 offset:192\n"
        "v_mfma_f32_16x16x32_bf16 v[156:159], v[198:201], a[236:239], v[156:159]\n"
        "v_cvt_pk_bf16_f32 v128, v130, v131\n"
        "v_cvt_pk_bf16_f32 v129, v132, v133\n"
        "v_mfma_f32_16x16x32_bf16 v[168:171], v[230:233], a[236:239], 0\n"
        "v_cvt_pk_bf16_f32 v130, v164, v165\n"
        "v_cvt_pk_bf16_f32 v131, v166, v167\n"
        "v_mfma_f32_16x16x32_bf16 v[168:171], v[234:237], a[232:235], v[168:171]\n"
        "v_cvt_pk_bf16_f32 v136, v160, v161\n"
        "v_cvt_pk_bf16_f32 v137, v162, v163\n"
        "v_mfma_f32_16x16x32_bf16 v[168:171], v[226:229], a[228:231], v[168:171]\n"
        "s_nop 0\n"
        "v_cvt_pk_bf16_f32 v138, v152, 0\n"
        "v_cvt_pk_bf16_f32 v40, v156, v157\n"
        "v_mfma_f32_16x16x32_bf16 v[160:163], v[222:225], a[228:231], 0\n"
        "s_nop 0\n"
        "v_cvt_pk_bf16_f32 v41, v158, v159\n"
        "v_mfma_f32_16x16x32_bf16 v[160:163], v[206:209], a[232:235], v[160:163]\n"
        "s_nop 0\n"
        "v_cvt_pk_bf16_f32 v154, v168, v169\n"
        "v_cvt_pk_bf16_f32 v155, v170, v171\n"
        "v_mfma_f32_16x16x32_bf16 v[160:163], v[218:221], a[236:239], v[160:163]\n"
        "s_nop 1\n"
        "v_mfma_f32_16x16x32_bf16 v[156:159], v[148:151], a[236:239], 0\n"
        "s_nop 2\n"
        "v_mfma_f32_16x16x32_bf16 v[156:159], v[112:115], a[232:235], v[156:159]\n"
        "s_nop 0\n"
        "v_cvt_pk_bf16_f32 v152, v160, v161\n"
        "v_cvt_pk_bf16_f32 v153, v162, v163\n"
        "v_mfma_f32_16x16x32_bf16 v[156:159], v[120:123], a[228:231], v[156:159]\n"
        "s_nop 1\n"
        "v_mfma_f32_16x16x32_bf16 v[162:165], v[222:225], a[240:243], 0\n"
        "s_nop 2\n"
        "v_mfma_f32_16x16x32_bf16 v[162:165], v[206:209], a[244:247], v[162:165]\n"
        "s_nop 0\n"
        "v_cvt_pk_bf16_f32 v42, v156, 0\n"
        "v_mfma_f32_16x16x32_bf16 v[162:165], v[218:221], a[248:251], v[162:165]\n"
        "s_nop 1\n"
        "v_mfma_f32_16x16x32_bf16 v[166:169], v[230:233], a[248:251], 0\n"
        "s_nop 2\n"
        "v_mfma_f32_16x16x32_bf16 v[166:169], v[234:237], a[244:247], v[166:169]\n"
        "s_nop 0\n"
        "v_cvt_pk_bf16_f32 v160, v162, v163\n"
        "v_cvt_pk_bf16_f32 v161, v164, v165\n"
        "v_mfma_f32_16x16x32_bf16 v[166:169], v[226:229], a[240:243], v[166:169]\n"
        "s_nop 1\n"
        "v_mfma_f32_16x16x32_bf16 v[156:159], v[202:205], a[240:243], 0\n"
        "s_nop 2\n"
        "v_mfma_f32_16x16x32_bf16 v[156:159], v[210:213], a[244:247], v[156:159]\n"
        "s_nop 0\n"
        "v_cvt_pk_bf16_f32 v162, v166, v167\n"
        "v_cvt_pk_bf16_f32 v163, v168, v169\n"
        "v_mfma_f32_16x16x32_bf16 v[156:159], v[198:201], a[248:251], v[156:159]\n"
        "s_nop 1\n"
        "v_mfma_f32_16x16x32_bf16 v[164:167], v[148:151], a[248:251], 0\n"
        "s_nop 2\n"
        "v_mfma_f32_16x16x32_bf16 v[164:167], v[112:115], a[244:247], v[164:167]\n"
        "s_nop 0\n"
        "v_cvt_pk_bf16_f32 v140, v156, v157\n"
        "v_cvt_pk_bf16_f32 v141, v158, v159\n"
        "v_mfma_f32_16x16x32_bf16 v[164:167], v[120:123], a[240:243], v[164:167]\n"
        "s_nop 1\n"
        "v_mfma_f32_16x16x32_bf16 v[156:159], v[202:205], a[252:255], 0\n"
        "s_nop 2\n"
        "v_mfma_f32_16x16x32_bf16 v[156:159], v[210:213], v[92:95], v[156:159]\n"
        "s_nop 0\n"
        "v_cvt_pk_bf16_f32 v142, v164, 0\n"
        "v_mfma_f32_16x16x32_bf16 v[156:159], v[198:201], v[52:55], v[156:159]\n"
        "s_nop 1\n"
        "v_mfma_f32_16x16x32_bf16 v[164:167], v[230:233], v[52:55], 0\n"
        "s_nop 2\n"
        "v_mfma_f32_16x16x32_bf16 v[164:167], v[234:237], v[92:95], v[164:167]\n"
        "s_nop 0\n"
        "v_cvt_pk_bf16_f32 v116, v156, v157\n"
        "v_cvt_pk_bf16_f32 v117, v158, v159\n"
        "v_mfma_f32_16x16x32_bf16 v[164:167], v[226:229], a[252:255], v[164:167]\n"
        "s_nop 1\n"
        "v_mfma_f32_16x16x32_bf16 v[168:171], v[222:225], a[252:255], 0\n"
        "s_nop 2\n"
        "v_mfma_f32_16x16x32_bf16 v[168:171], v[206:209], v[92:95], v[168:171]\n"
        "s_nop 0\n"
        "v_cvt_pk_bf16_f32 v158, v164, v165\n"
        "v_cvt_pk_bf16_f32 v159, v166, v167\n"
        "v_mfma_f32_16x16x32_bf16 v[168:171], v[218:221], v[52:55], v[168:171]\n"
        "s_nop 1\n"
        "v_mfma_f32_16x16x32_bf16 v[164:167], v[148:151], v[52:55], 0\n"
        "s_nop 2\n"
        "v_mfma_f32_16x16x32_bf16 v[164:167], v[112:115], v[92:95], v[164:167]\n"
        "s_nop 0\n"
        "v_cvt_pk_bf16_f32 v156, v168, v169\n"
        "v_cvt_pk_bf16_f32 v157, v170, v171\n"
        "v_mfma_f32_16x16x32_bf16 v[164:167], v[120:123], a[252:255], v[164:167]\n"
        "s_nop 1\n"
        "v_mfma_f32_16x16x32_bf16 v[168:171], v[222:225], v[64:67], 0\n"
        "s_nop 2\n"
        "v_mfma_f32_16x16x32_bf16 v[168:171], v[206:209], v[84:87], v[168:171]\n"
        "s_nop 0\n"
        "v_cvt_pk_bf16_f32 v118, v164, 0\n"
        "v_mfma_f32_16x16x32_bf16 v[168:171], v[218:221], v[76:79], v[168:171]\n"
        "s_nop 1\n"
        "v_mfma_f32_16x16x32_bf16 v[176:179], v[230:233], v[76:79], 0\n"
        "s_nop 2\n"
        "v_mfma_f32_16x16x32_bf16 v[176:179], v[234:237], v[84:87], v[176:179]\n"
        "s_nop 0\n"
        "v_cvt_pk_bf16_f32 v164, v168, v169\n"
        "v_cvt_pk_bf16_f32 v165, v170, v171\n"
        "v_mfma_f32_16x16x32_bf16 v[176:179], v[226:229], v[64:67], v[176:179]\n"
        "s_nop 1\n"
        "v_mfma_f32_16x16x32_bf16 v[172:175], v[202:205], v[64:67], 0\n"
        "s_nop 2\n"
        "v_mfma_f32_16x16x32_bf16 v[172:175], v[210:213], v[84:87], v[172:175]\n"
        "s_nop 0\n"
        "v_cvt_pk_bf16_f32 v166, v176, v177\n"
        "v_cvt_pk_bf16_f32 v167, v178, v179\n"
        "v_mfma_f32_16x16x32_bf16 v[172:175], v[198:201], v[76:79], v[172:175]\n"
        "s_nop 1\n"
        "v_mfma_f32_16x16x32_bf16 v[168:171], v[148:151], v[76:79], 0\n"
        "s_nop 2\n"
        "v_mfma_f32_16x16x32_bf16 v[168:171], v[112:115], v[84:87], v[168:171]\n"
        "s_nop 0\n"
        "v_cvt_pk_bf16_f32 v104, v172, v173\n"
        "v_cvt_pk_bf16_f32 v105, v174, v175\n"
        "v_mfma_f32_16x16x32_bf16 v[168:171], v[120:123], v[64:67], v[168:171]\n"
        "s_nop 1\n"
        "v_mfma_f32_16x16x32_bf16 v[172:175], v[202:205], v[60:63], 0\n"
        "s_nop 2\n"
        "v_mfma_f32_16x16x32_bf16 v[172:175], v[210:213], v[72:75], v[172:175]\n"
        "s_nop 0\n"
        "v_cvt_pk_bf16_f32 v106, v168, 0\n"
        "v_mfma_f32_16x16x32_bf16 v[172:175], v[198:201], v[48:51], v[172:175]\n"
        "v_mfma_f32_16x16x32_bf16 v[192:195], v[230:233], v[48:51], 0\n"
        "v_mfma_f32_16x16x32_bf16 v[192:195], v[234:237], v[72:75], v[192:195]\n"
        "ds_read_b128 v[168:171], v252\n"
        "ds_read_b128 v[176:179], v252 offset:64\n"
        "v_mfma_f32_16x16x32_bf16 v[192:195], v[226:229], v[60:63], v[192:195]\n"
        "ds_read_b128 v[180:183], v252 offset:128\n"
        "ds_read_b32 v250, v251\n"
        "v_mfma_f32_16x16x32_bf16 v[188:191], v[222:225], v[60:63], 0\n"
        "ds_read_b32 v252, v251 offset:64\n"
        "ds_read_b32 v254, v251 offset:128\n"
        "v_mfma_f32_16x16x32_bf16 v[188:191], v[206:209], v[72:75], v[188:191]\n"
        "ds_read_b32 v253, v251 offset:192\n"
        "v_cvt_pk_bf16_f32 v34, v172, v173\n"
        "v_mfma_f32_16x16x32_bf16 v[188:191], v[218:221], v[48:51], v[188:191]\n"
        "s_nop 0\n"
        "v_cvt_pk_bf16_f32 v35, v174, v175\n"
        "v_cvt_pk_bf16_f32 v174, v192, v193\n"
        "v_mfma_f32_16x16x32_bf16 v[184:187], v[148:151], v[48:51], 0\n"
        "s_nop 0\n"
        "v_cvt_pk_bf16_f32 v175, v194, v195\n"
        "v_mfma_f32_16x16x32_bf16 v[184:187], v[112:115], v[72:75], v[184:187]\n"
        "s_nop 0\n"
        "v_cvt_pk_bf16_f32 v172, v188, v189\n"
        "v_cvt_pk_bf16_f32 v173, v190, v191\n"
        "v_mfma_f32_16x16x32_bf16 v[184:187], v[120:123], v[60:63], v[184:187]\n"
        "s_nop 1\n"
        "v_mfma_f32_16x16x32_bf16 v[188:191], a[144:147], v[222:225], 0\n"
        "s_nop 2\n"
        "v_mfma_f32_16x16x32_bf16 v[188:191], a[148:151], v[206:209], v[188:191]\n"
        "s_nop 0\n"
        "v_cvt_pk_bf16_f32 v36, v184, 0\n"
        "v_mfma_f32_16x16x32_bf16 v[188:191], a[152:155], v[218:221], v[188:191]\n"
        "s_nop 1\n"
        "v_mfma_f32_16x16x32_bf16 v[184:187], a[164:167], v[218:221], 0\n"
        "s_waitcnt lgkmcnt(0)\n"
        "s_nop 1\n"
        "v_mfma_f32_16x16x32_bf16 v[184:187], a[160:163], v[206:209], v[184:187]\n"
        "s_nop 0\n"
        "v_cvt_pk_bf16_f32 v238, v188, v189\n"
        "v_cvt_pk_bf16_f32 v239, v190, v191\n"
        "v_mfma_f32_16x16x32_bf16 v[184:187], a[156:159], v[222:225], v[184:187]\n"
        "s_nop 1\n"
        "v_mfma_f32_16x16x32_bf16 v[188:191], a[168:171], v[222:225], 0\n"
        "s_nop 2\n"
        "v_mfma_f32_16x16x32_bf16 v[188:191], a[172:175], v[206:209], v[188:191]\n"
        "s_nop 0\n"
        "v_cvt_pk_bf16_f32 v240, v184, v185\n"
        "v_cvt_pk_bf16_f32 v241, v186, v187\n"
        "v_mfma_f32_16x16x32_bf16 v[188:191], a[176:179], v[218:221], v[188:191]\n"
        "s_nop 1\n"
        "v_mfma_f32_16x16x32_bf16 v[184:187], a[188:191], v[218:221], 0\n"
        "s_nop 2\n"
        "v_mfma_f32_16x16x32_bf16 v[184:187], a[184:187], v[206:209], v[184:187]\n"
        "s_nop 0\n"
        "v_cvt_pk_bf16_f32 v214, v188, v189\n"
        "v_cvt_pk_bf16_f32 v215, v190, v191\n"
        "v_mfma_f32_16x16x32_bf16 v[184:187], a[180:183], v[222:225], v[184:187]\n"
        "s_nop 1\n"
        "v_mfma_f32_16x16x32_bf16 v[188:191], a[192:195], v[222:225], 0\n"
        "s_nop 2\n"
        "v_mfma_f32_16x16x32_bf16 v[188:191], a[196:199], v[206:209], v[188:191]\n"
        "s_nop 0\n"
        "v_cvt_pk_bf16_f32 v216, v184, v185\n"
        "v_cvt_pk_bf16_f32 v217, v186, v187\n"
        "v_mfma_f32_16x16x32_bf16 v[188:191], a[200:203], v[218:221], v[188:191]\n"
        "s_nop 1\n"
        "v_mfma_f32_16x16x32_bf16 v[192:195], a[212:215], v[218:221], 0\n"
        "s_nop 2\n"
        "v_mfma_f32_16x16x32_bf16 v[192:195], a[208:211], v[206:209], v[192:195]\n"
        "s_nop 0\n"
        "v_cvt_pk_bf16_f32 v184, v188, v189\n"
        "v_cvt_pk_bf16_f32 v185, v190, v191\n"
        "v_mfma_f32_16x16x32_bf16 v[192:195], a[204:207], v[222:225], v[192:195]\n"
        "s_nop 1\n"
        "v_mfma_f32_16x16x32_bf16 v[188:191], v[222:225], v[238:241], 0\n"
        "s_nop 2\n"
        "v_mfma_f32_16x16x32_bf16 v[188:191], v[206:209], v[214:217], v[188:191]\n"
        "s_nop 0\n"
        "v_cvt_pk_bf16_f32 v186, v192, v193\n"
        "v_cvt_pk_bf16_f32 v187, v194, v195\n"
        "s_nop 1\n"
        "v_mfma_f32_16x16x32_bf16 v[188:191], v[218:221], v[184:187], v[188:191]\n"
        "s_nop 1\n"
        "v_mfma_f32_16x16x32_bf16 v[192:195], v[230:233], v[184:187], 0\n"
        "s_nop 2\n"
        "v_mfma_f32_16x16x32_bf16 v[192:195], v[234:237], v[214:217], v[192:195]\n"
        "s_nop 0\n"
        "v_mul_f32 v188, v188, v168\n"
        "v_mul_f32 v189, v189, v169\n"
        "v_mfma_f32_16x16x32_bf16 v[192:195], v[226:229], v[238:241], v[192:195]\n"
        "s_nop 0\n"
        "v_mul_f32 v190, v190, v170\n"
        "v_mul_f32 v191, v191, v171\n"
        "v_mfma_f32_16x16x32_bf16 v[242:245], v[202:205], v[238:241], 0\n"
        "s_nop 0\n"
        "v_max3_f32 v251, v188, v189, v190\n"
        "v_mfma_f32_16x16x32_bf16 v[242:245], v[210:213], v[214:217], v[242:245]\n"
        "s_nop 0\n"
        "v_mul_f32 v192, v192, v176\n"
        "v_mul_f32 v193, v193, v177\n"
        "v_mfma_f32_16x16x32_bf16 v[242:245], v[198:201], v[184:187], v[242:245]\n"
        "v_mul_f32 v194, v194, v178\n"
        "v_mul_f32 v195, v195, v179\n"
        "v_mfma_f32_16x16x32_bf16 v[246:249], v[148:151], v[184:187], 0\n"
        "s_nop 0\n"
        "v_max3_f32 v251, v251, v191, v192\n"
        "v_max3_f32 v251, v251, v193, v194\n"
        "v_mfma_f32_16x16x32_bf16 v[246:249], v[112:115], v[214:217], v[246:249]\n"
        "s_nop 0\n"
        "v_mul_f32 v242, v242, v180\n"
        "v_mul_f32 v243, v243, v181\n"
        "v_mfma_f32_16x16x32_bf16 v[246:249], v[120:123], v[238:241], v[246:249]\n"
        "s_nop 0\n"
        "v_mul_f32 v244, v244, v182\n"
        "v_mul_f32 v245, v245, v183\n"
        "v_max3_f32 v251, v251, v195, v242\n"
        "v_max3_f32 v251, v251, v243, v244\n"
        "s_nop 2\n"
        "v_mul_f32 v246, v246, v253\n"
        "v_cndmask_b32_e64 v246, v255, v246, s[62:63]\n"
        "v_max3_f32 v251, v251, v245, v246\n"
        "v_mov_b32 v249, v251\n"
        "s_nop 1\n"
        "v_permlane32_swap_b32 v251, v249\n"
        "v_max_f32 v251, v251, v249\n"
        "v_mov_b32 v249, v251\n"
        "s_nop 1\n"
        "v_permlane16_swap_b32 v251, v249\n"
        "v_max_f32 v251, v251, v249\n"
        "v_mul_f32 v251, v251, v250\n"
        "v_fma_f32 v188, v188, v250, -v251\n"
        "v_exp_f32 v188, v188\n"
        "v_fma_f32 v189, v189, v250, -v251\n"
        "v_exp_f32 v189, v189\n"
        "s_nop 0\n"
        "v_add_f32 v249, v188, v189\n"
        "v_mul_f32 v188, v188, v168\n"
        "v_fma_f32 v246, v246, v250, -v251\n"
        "v_exp_f32 v246, v246\n"
        "v_fma_f32 v245, v245, v250, -v251\n"
        "v_exp_f32 v245, v245\n"
        "v_fma_f32 v244, v244, v250, -v251\n"
        "v_exp_f32 v244, v244\n"
        "v_fma_f32 v243, v243, v250, -v251\n"
        "v_exp_f32 v243, v243\n"
        "v_fma_f32 v242, v242, v250, -v251\n"
        "v_exp_f32 v242, v242\n"
        "v_fma_f32 v195, v195, v250, -v251\n"
        "v_exp_f32 v195, v195\n"
        "v_fma_f32 v194, v194, v250, -v251\n"
        "v_exp_f32 v194, v194\n"
        "v_fma_f32 v193, v193, v250, -v251\n"
        "v_exp_f32 v193, v193\n"
        "v_fma_f32 v192, v192, v250, -v251\n"
        "v_exp_f32 v192, v192\n"
        "v_fma_f32 v191, v191, v250, -v251\n"
        "v_exp_f32 v191, v191\n"
        "v_fma_f32 v190, v190, v250, -v251\n"
        "v_exp_f32 v190, v190\n"
        "s_nop 0\n"
        "v_add_f32 v249, v249, v190\n"
        "v_add_f32 v249, v249, v191\n"
        "v_add_f32 v249, v249, v192\n"
        "v_add_f32 v249, v249, v193\n"
        "v_add_f32 v249, v249, v194\n"
        "v_add_f32 v249, v249, v195\n"
        "v_add_f32 v249, v249, v242\n"
        "v_add_f32 v249, v249, v243\n"
        "v_add_f32 v249, v249, v244\n"
        "v_add_f32 v249, v249, v245\n"
        "v_add_f32 v249, v249, v246\n"
        "v_mov_b32 v251, v249\n"
        "s_nop 1\n"
        "v_permlane32_swap_b32 v249, v251\n"
        "v_add_f32 v249, v249, v251\n"
        "v_mov_b32 v251, v249\n"
        "s_nop 1\n"
        "v_permlane16_swap_b32 v249, v251\n"
        "v_add_f32 v249, v249, v251\n"
        "v_rcp_f32 v249, v249\n"
        "s_nop 0\n"
        "v_mul_f32 v188, v188, v249\n"
        "v_mul_f32 v189, v189, v169\n"
        "v_mul_f32 v189, v189, v249\n"
        "v_cvt_pk_bf16_f32 v214, v188, v189\n"
        "v_mul_f32 v190, v190, v170\n"
        "v_mul_f32 v190, v190, v249\n"
        "v_mul_f32 v191, v191, v171\n"
        "v_mul_f32 v191, v191, v249\n"
        "v_cvt_pk_bf16_f32 v215, v190, v191\n"
        "v_mul_f32 v192, v192, v176\n"
        "v_mul_f32 v192, v192, v249\n"
        "v_mul_f32 v193, v193, v177\n"
        "v_mul_f32 v193, v193, v249\n"
        "v_cvt_pk_bf16_f32 v216, v192, v193\n"
        "v_mul_f32 v194, v194, v178\n"
        "v_mul_f32 v194, v194, v249\n"
        "v_mul_f32 v195, v195, v179\n"
        "v_mul_f32 v195, v195, v249\n"
        "v_cvt_pk_bf16_f32 v217, v194, v195\n"
        "s_nop 1\n"
        "v_mfma_f32_16x16x32_bf16 v[184:187], v[214:217], v[128:131], 0\n"
        "v_mul_f32 v242, v242, v180\n"
        "v_mul_f32 v243, v243, v181\n"
        "v_mul_f32 v242, v242, v249\n"
        "v_mul_f32 v243, v243, v249\n"
        "v_cvt_pk_bf16_f32 v108, v242, v243\n"
        "v_mul_f32 v244, v244, v182\n"
        "v_mul_f32 v244, v244, v249\n"
        "v_mul_f32 v245, v245, v183\n"
        "v_mul_f32 v245, v245, v249\n"
        "v_cvt_pk_bf16_f32 v109, v244, v245\n"
        "v_mul_f32 v246, v246, v253\n"
        "v_mul_f32 v246, v246, v249\n"
        "v_cvt_pk_bf16_f32 v110, v246, 0\n"
        "s_nop 1\n"
        "v_mfma_f32_16x16x32_bf16 v[184:187], v[108:111], v[136:139], v[184:187]\n"
        "v_mfma_f32_16x16x32_bf16 v[184:187], v[222:225], v[88:91], v[184:187]\n"
        "v_mfma_f32_16x16x32_bf16 v[188:191], v[222:225], v[80:83], 0\n"
        "v_mfma_f32_16x16x32_bf16 v[188:191], v[108:111], v[40:43], v[188:191]\n"
        "v_mfma_f32_16x16x32_bf16 v[188:191], v[214:217], v[152:155], v[188:191]\n"
        "s_add_u32 s90, s89, 0\n"
        "s_nop 0\n"
        "v_mfma_f32_16x16x32_bf16 v[192:195], v[214:217], v[160:163], 0\n"
        "s_nop 2\n"
        "v_mfma_f32_16x16x32_bf16 v[192:195], v[108:111], v[140:143], v[192:195]\n"
        "s_nop 0\n"
        "v_cvt_pk_bf16_f32 v124, v184, v188\n"
        "v_cvt_pk_bf16_f32 v238, v185, v189\n"
        "v_mfma_f32_16x16x32_bf16 v[192:195], v[206:209], v[88:91], v[192:195]\n"
        "v_cvt_pk_bf16_f32 v132, v186, v190\n"
        "v_cvt_pk_bf16_f32 v144, v187, v191\n"
        "v_mfma_f32_16x16x32_bf16 v[184:187], v[206:209], v[80:83], 0\n"
        "s_add_u32 s91, s89, 98112\n"
        "v_mfma_f32_16x16x32_bf16 v[184:187], v[108:111], v[116:119], v[184:187]\n"
        "v_mfma_f32_16x16x32_bf16 v[184:187], v[214:217], v[156:159], v[184:187]\n"
        "s_nop 1\n"
        "v_mfma_f32_16x16x32_bf16 v[188:191], v[214:217], v[164:167], 0\n"
        "s_nop 2\n"
        "v_mfma_f32_16x16x32_bf16 v[188:191], v[108:111], v[104:107], v[188:191]\n"
        "s_nop 0\n"
        "v_cvt_pk_bf16_f32 v125, v192, v184\n"
        "v_cvt_pk_bf16_f32 v239, v193, v185\n"
        "v_mfma_f32_16x16x32_bf16 v[188:191], v[218:221], v[88:91], v[188:191]\n"
        "v_cvt_pk_bf16_f32 v133, v194, v186\n"
        "v_cvt_pk_bf16_f32 v145, v195, v187\n"
        "v_mfma_f32_16x16x32_bf16 v[184:187], v[218:221], v[80:83], 0\n"
        "v_mfma_f32_16x16x32_bf16 v[184:187], v[108:111], v[34:37], v[184:187]\n"
        "v_mfma_f32_16x16x32_bf16 v[184:187], v[214:217], v[172:175], v[184:187]\n"
        "s_nop 1\n"
        "v_mfma_f32_16x16x32_bf16 v[108:111], a[144:147], v[226:229], 0\n"
        "s_nop 2\n"
        "v_mfma_f32_16x16x32_bf16 v[108:111], a[148:151], v[234:237], v[108:111]\n"
        "s_nop 0\n"
        "v_cvt_pk_bf16_f32 v126, v188, v184\n"
        "v_cvt_pk_bf16_f32 v240, v189, v185\n"
        "v_mfma_f32_16x16x32_bf16 v[108:111], a[152:155], v[230:233], v[108:111]\n"
        "s_nop 0\n"
        "v_cvt_pk_bf16_f32 v134, v190, v186\n"
        "v_cvt_pk_bf16_f32 v146, v191, v187\n"
        "v_mfma_f32_16x16x32_bf16 v[188:191], a[164:167], v[230:233], 0\n"
        "s_nop 1\n"
        "v_mfma_f32_16x16x32_bf16 v[188:191], a[160:163], v[234:237], v[188:191]\n"
        "s_nop 0\n"
        "v_cvt_pk_bf16_f32 v184, v108, v109\n"
        "v_cvt_pk_bf16_f32 v185, v110, v111\n"
        "v_mfma_f32_16x16x32_bf16 v[188:191], a[156:159], v[226:229], v[188:191]\n"
        "s_nop 0\n"
        "v_mfma_f32_16x16x32_bf16 v[108:111], a[168:171], v[226:229], 0\n"
        "buffer_store_dwordx3 v[124:126], v14, s[40:43], s90 offen sc1 nt\n"
        "s_nop 2\n"
        "v_mfma_f32_16x16x32_bf16 v[108:111], a[172:175], v[234:237], v[108:111]\n"
        "s_nop 0\n"
        "v_cvt_pk_bf16_f32 v186, v188, v189\n"
        "v_cvt_pk_bf16_f32 v187, v190, v191\n"
        "v_mfma_f32_16x16x32_bf16 v[108:111], a[176:179], v[230:233], v[108:111]\n"
        "buffer_store_dwordx3 v[238:240], v15, s[40:43], s90 offen offset:768 sc1 nt\n"
        "s_nop 0\n"
        "v_mfma_f32_16x16x32_bf16 v[214:217], a[188:191], v[230:233], 0\n"
        "s_nop 2\n"
        "v_mfma_f32_16x16x32_bf16 v[214:217], a[184:187], v[234:237], v[214:217]\n"
        "buffer_store_dwordx3 v[132:134], v14, s[40:43], s91 offen offset:192 sc1 nt\n"
        "v_cvt_pk_bf16_f32 v188, v108, v109\n"
        "v_cvt_pk_bf16_f32 v189, v110, v111\n"
        "v_mfma_f32_16x16x32_bf16 v[214:217], a[180:183], v[226:229], v[214:217]\n"
        "s_nop 0\n"
        "v_mfma_f32_16x16x32_bf16 v[192:195], a[192:195], v[226:229], 0\n"
        "buffer_store_dwordx3 v[144:146], v16, s[40:43], s91 offen offset:960 sc1 nt\n"
        "s_nop 2\n"
        "v_mfma_f32_16x16x32_bf16 v[192:195], a[196:199], v[234:237], v[192:195]\n"
        "s_nop 0\n"
        "v_cvt_pk_bf16_f32 v190, v214, v215\n"
        "v_cvt_pk_bf16_f32 v191, v216, v217\n"
        "v_mfma_f32_16x16x32_bf16 v[192:195], a[200:203], v[230:233], v[192:195]\n"
        "s_nop 1\n"
        "v_mfma_f32_16x16x32_bf16 v[108:111], a[212:215], v[230:233], 0\n"
        "s_nop 2\n"
        "v_mfma_f32_16x16x32_bf16 v[108:111], a[208:211], v[234:237], v[108:111]\n"
        "s_nop 0\n"
        "v_cvt_pk_bf16_f32 v214, v192, v193\n"
        "v_cvt_pk_bf16_f32 v215, v194, v195\n"
        "v_mfma_f32_16x16x32_bf16 v[108:111], a[204:207], v[226:229], v[108:111]\n"
        "s_nop 1\n"
        "v_mfma_f32_16x16x32_bf16 v[238:241], v[222:225], v[184:187], 0\n"
        "s_nop 2\n"
        "v_mfma_f32_16x16x32_bf16 v[238:241], v[206:209], v[188:191], v[238:241]\n"
        "s_nop 0\n"
        "v_cvt_pk_bf16_f32 v216, v108, v109\n"
        "v_cvt_pk_bf16_f32 v217, v110, v111\n"
        "s_nop 1\n"
        "v_mfma_f32_16x16x32_bf16 v[238:241], v[218:221], v[214:217], v[238:241]\n"
        "s_nop 1\n"
        "v_mfma_f32_16x16x32_bf16 v[108:111], v[230:233], v[214:217], 0\n"
        "s_nop 2\n"
        "v_mfma_f32_16x16x32_bf16 v[108:111], v[234:237], v[188:191], v[108:111]\n"
        "s_nop 0\n"
        "v_mul_f32 v238, v238, v168\n"
        "v_mul_f32 v239, v239, v169\n"
        "v_mfma_f32_16x16x32_bf16 v[108:111], v[226:229], v[184:187], v[108:111]\n"
        "s_nop 0\n"
        "v_mul_f32 v240, v240, v170\n"
        "v_mul_f32 v241, v241, v171\n"
        "v_mfma_f32_16x16x32_bf16 v[192:195], v[202:205], v[184:187], 0\n"
        "s_nop 0\n"
        "v_max3_f32 v250, v238, v239, v240\n"
        "v_mfma_f32_16x16x32_bf16 v[192:195], v[210:213], v[188:191], v[192:195]\n"
        "s_nop 0\n"
        "v_mul_f32 v108, v108, v176\n"
        "v_mul_f32 v109, v109, v177\n"
        "v_mfma_f32_16x16x32_bf16 v[192:195], v[198:201], v[214:217], v[192:195]\n"
        "v_mul_f32 v110, v110, v178\n"
        "v_mul_f32 v111, v111, v179\n"
        "v_mfma_f32_16x16x32_bf16 v[242:245], v[148:151], v[214:217], 0\n"
        "s_nop 0\n"
        "v_max3_f32 v250, v250, v241, v108\n"
        "v_max3_f32 v250, v250, v109, v110\n"
        "v_mfma_f32_16x16x32_bf16 v[242:245], v[112:115], v[188:191], v[242:245]\n"
        "s_nop 0\n"
        "v_mul_f32 v192, v192, v180\n"
        "v_mul_f32 v193, v193, v181\n"
        "v_mfma_f32_16x16x32_bf16 v[242:245], v[120:123], v[184:187], v[242:245]\n"
        "s_nop 0\n"
        "v_mul_f32 v194, v194, v182\n"
        "v_mul_f32 v195, v195, v183\n"
        "v_max3_f32 v250, v250, v111, v192\n"
        "v_max3_f32 v250, v250, v193, v194\n"
        "s_nop 2\n"
        "v_mul_f32 v242, v242, v253\n"
        "v_cndmask_b32_e64 v242, v255, v242, s[62:63]\n"
        "v_max3_f32 v250, v250, v195, v242\n"
        "v_mov_b32 v251, v250\n"
        "s_nop 1\n"
        "v_permlane32_swap_b32 v250, v251\n"
        "v_max_f32 v250, v250, v251\n"
        "v_mov_b32 v251, v250\n"
        "s_nop 1\n"
        "v_permlane16_swap_b32 v250, v251\n"
        "v_max_f32 v250, v250, v251\n"
        "v_mul_f32 v250, v250, v252\n"
        "v_fma_f32 v238, v238, v252, -v250\n"
        "v_exp_f32 v238, v238\n"
        "v_fma_f32 v239, v239, v252, -v250\n"
        "v_exp_f32 v239, v239\n"
        "s_nop 0\n"
        "v_add_f32 v251, v238, v239\n"
        "v_mul_f32 v238, v238, v168\n"
        "v_fma_f32 v242, v242, v252, -v250\n"
        "v_exp_f32 v242, v242\n"
        "v_fma_f32 v195, v195, v252, -v250\n"
        "v_exp_f32 v195, v195\n"
        "v_fma_f32 v194, v194, v252, -v250\n"
        "v_exp_f32 v194, v194\n"
        "v_fma_f32 v193, v193, v252, -v250\n"
        "v_exp_f32 v193, v193\n"
        "v_fma_f32 v192, v192, v252, -v250\n"
        "v_exp_f32 v192, v192\n"
        "v_fma_f32 v111, v111, v252, -v250\n"
        "v_exp_f32 v111, v111\n"
        "v_fma_f32 v110, v110, v252, -v250\n"
        "v_exp_f32 v110, v110\n"
        "v_fma_f32 v109, v109, v252, -v250\n"
        "v_exp_f32 v109, v109\n"
        "v_fma_f32 v108, v108, v252, -v250\n"
        "v_exp_f32 v108, v108\n"
        "v_fma_f32 v241, v241, v252, -v250\n"
        "v_exp_f32 v241, v241\n"
        "v_fma_f32 v240, v240, v252, -v250\n"
        "v_exp_f32 v240, v240\n"
        "s_nop 0\n"
        "v_add_f32 v251, v251, v240\n"
        "v_add_f32 v251, v251, v241\n"
        "v_add_f32 v251, v251, v108\n"
        "v_add_f32 v251, v251, v109\n"
        "v_add_f32 v251, v251, v110\n"
        "v_add_f32 v251, v251, v111\n"
        "v_add_f32 v251, v251, v192\n"
        "v_add_f32 v251, v251, v193\n"
        "v_add_f32 v251, v251, v194\n"
        "v_add_f32 v251, v251, v195\n"
        "v_add_f32 v251, v251, v242\n"
        "v_mov_b32 v252, v251\n"
        "s_nop 1\n"
        "v_permlane32_swap_b32 v251, v252\n"
        "v_add_f32 v251, v251, v252\n"
        "v_mov_b32 v252, v251\n"
        "s_nop 1\n"
        "v_permlane16_swap_b32 v251, v252\n"
        "v_add_f32 v251, v251, v252\n"
        "v_rcp_f32 v251, v251\n"
        "s_nop 0\n"
        "v_mul_f32 v238, v238, v251\n"
        "v_mul_f32 v239, v239, v169\n"
        "v_mul_f32 v239, v239, v251\n"
        "v_cvt_pk_bf16_f32 v188, v238, v239\n"
        "v_mul_f32 v240, v240, v170\n"
        "v_mul_f32 v240, v240, v251\n"
        "v_mul_f32 v241, v241, v171\n"
        "v_mul_f32 v241, v241, v251\n"
        "v_cvt_pk_bf16_f32 v189, v240, v241\n"
        "v_mul_f32 v108, v108, v176\n"
        "v_mul_f32 v108, v108, v251\n"
        "v_mul_f32 v109, v109, v177\n"
        "v_mul_f32 v109, v109, v251\n"
        "v_cvt_pk_bf16_f32 v190, v108, v109\n"
        "v_mul_f32 v110, v110, v178\n"
        "v_mul_f32 v110, v110, v251\n"
        "v_mul_f32 v111, v111, v179\n"
        "v_mul_f32 v111, v111, v251\n"
        "v_cvt_pk_bf16_f32 v191, v110, v111\n"
        "s_nop 1\n"
        "v_mfma_f32_16x16x32_bf16 v[108:111], v[188:191], v[128:131], 0\n"
        "v_mul_f32 v192, v192, v180\n"
        "v_mul_f32 v193, v193, v181\n"
        "v_mul_f32 v192, v192, v251\n"
        "v_mul_f32 v193, v193, v251\n"
        "v_cvt_pk_bf16_f32 v144, v192, v193\n"
        "v_mul_f32 v194, v194, v182\n"
        "v_mul_f32 v194, v194, v251\n"
        "v_mul_f32 v195, v195, v183\n"
        "v_mul_f32 v195, v195, v251\n"
        "v_cvt_pk_bf16_f32 v145, v194, v195\n"
        "v_mul_f32 v242, v242, v253\n"
        "v_mul_f32 v242, v242, v251\n"
        "v_cvt_pk_bf16_f32 v146, v242, 0\n"
        "s_nop 1\n"
        "v_mfma_f32_16x16x32_bf16 v[108:111], v[144:147], v[136:139], v[108:111]\n"
        "v_mfma_f32_16x16x32_bf16 v[108:111], v[226:229], v[88:91], v[108:111]\n"
        "v_mfma_f32_16x16x32_bf16 v[192:195], v[226:229], v[80:83], 0\n"
        "v_mfma_f32_16x16x32_bf16 v[192:195], v[144:147], v[40:43], v[192:195]\n"
        "v_mfma_f32_16x16x32_bf16 v[192:195], v[188:191], v[152:155], v[192:195]\n"
        "s_add_u32 s90, s89, 196224\n"
        "s_nop 0\n"
        "v_mfma_f32_16x16x32_bf16 v[184:187], v[188:191], v[160:163], 0\n"
        "s_nop 2\n"
        "v_mfma_f32_16x16x32_bf16 v[184:187], v[144:147], v[140:143], v[184:187]\n"
        "s_nop 0\n"
        "v_cvt_pk_bf16_f32 v238, v108, v192\n"
        "v_cvt_pk_bf16_f32 v214, v109, v193\n"
        "v_mfma_f32_16x16x32_bf16 v[184:187], v[234:237], v[88:91], v[184:187]\n"
        "v_cvt_pk_bf16_f32 v132, v110, v194\n"
        "v_cvt_pk_bf16_f32 v124, v111, v195\n"
        "v_mfma_f32_16x16x32_bf16 v[192:195], v[234:237], v[80:83], 0\n"
        "s_add_u32 s91, s89, 294336\n"
        "v_mfma_f32_16x16x32_bf16 v[192:195], v[144:147], v[116:119], v[192:195]\n"
        "v_mfma_f32_16x16x32_bf16 v[192:195], v[188:191], v[156:159], v[192:195]\n"
        "s_nop 1\n"
        "v_mfma_f32_16x16x32_bf16 v[108:111], v[188:191], v[164:167], 0\n"
        "s_nop 2\n"
        "v_mfma_f32_16x16x32_bf16 v[108:111], v[144:147], v[104:107], v[108:111]\n"
        "s_nop 0\n"
        "v_cvt_pk_bf16_f32 v239, v184, v192\n"
        "v_cvt_pk_bf16_f32 v215, v185, v193\n"
        "v_mfma_f32_16x16x32_bf16 v[108:111], v[230:233], v[88:91], v[108:111]\n"
        "v_cvt_pk_bf16_f32 v133, v186, v194\n"
        "v_cvt_pk_bf16_f32 v125, v187, v195\n"
        "v_mfma_f32_16x16x32_bf16 v[184:187], v[230:233], v[80:83], 0\n"
        "v_mfma_f32_16x16x32_bf16 v[184:187], v[144:147], v[34:37], v[184:187]\n"
        "v_mfma_f32_16x16x32_bf16 v[184:187], v[188:191], v[172:175], v[184:187]\n"
        "s_nop 1\n"
        "v_mfma_f32_16x16x32_bf16 v[144:147], a[144:147], v[202:205], 0\n"
        "s_nop 2\n"
        "v_mfma_f32_16x16x32_bf16 v[144:147], a[148:151], v[210:213], v[144:147]\n"
        "s_nop 0\n"
        "v_cvt_pk_bf16_f32 v240, v108, v184\n"
        "v_cvt_pk_bf16_f32 v216, v109, v185\n"
        "v_mfma_f32_16x16x32_bf16 v[144:147], a[152:155], v[198:201], v[144:147]\n"
        "s_nop 0\n"
        "v_cvt_pk_bf16_f32 v134, v110, v186\n"
        "v_cvt_pk_bf16_f32 v126, v111, v187\n"
        "v_mfma_f32_16x16x32_bf16 v[184:187], a[164:167], v[198:201], 0\n"
        "s_nop 1\n"
        "v_mfma_f32_16x16x32_bf16 v[184:187], a[160:163], v[210:213], v[184:187]\n"
        "s_nop 0\n"
        "v_cvt_pk_bf16_f32 v188, v144, v145\n"
        "v_cvt_pk_bf16_f32 v189, v146, v147\n"
        "v_mfma_f32_16x16x32_bf16 v[184:187], a[156:159], v[202:205], v[184:187]\n"
        "s_nop 0\n"
        "v_mfma_f32_16x16x32_bf16 v[108:111], a[168:171], v[202:205], 0\n"
        "buffer_store_dwordx3 v[238:240], v14, s[40:43], s90 offen offset:384 sc1 nt\n"
        "s_nop 2\n"
        "v_mfma_f32_16x16x32_bf16 v[108:111], a[172:175], v[210:213], v[108:111]\n"
        "s_nop 0\n"
        "v_cvt_pk_bf16_f32 v190, v184, v185\n"
        "v_cvt_pk_bf16_f32 v191, v186, v187\n"
        "v_mfma_f32_16x16x32_bf16 v[108:111], a[176:179], v[198:201], v[108:111]\n"
        "buffer_store_dwordx3 v[214:216], v17, s[40:43], s90 offen offset:1152 sc1 nt\n"
        "s_add_u32 s90, s89, 392448\n"
        "v_mfma_f32_16x16x32_bf16 v[184:187], a[188:191], v[198:201], 0\n"
        "s_nop 2\n"
        "v_mfma_f32_16x16x32_bf16 v[184:187], a[184:187], v[210:213], v[184:187]\n"
        "buffer_store_dwordx3 v[132:134], v14, s[40:43], s91 offen offset:576 sc1 nt\n"
        "v_cvt_pk_bf16_f32 v144, v108, v109\n"
        "v_cvt_pk_bf16_f32 v145, v110, v111\n"
        "v_mfma_f32_16x16x32_bf16 v[184:187], a[180:183], v[202:205], v[184:187]\n"
        "s_nop 0\n"
        "v_mfma_f32_16x16x32_bf16 v[108:111], a[192:195], v[202:205], 0\n"
        "buffer_store_dwordx3 v[124:126], v14, s[40:43], s90 offen sc1 nt\n"
        "s_nop 2\n"
        "v_mfma_f32_16x16x32_bf16 v[108:111], a[196:199], v[210:213], v[108:111]\n"
        "s_nop 0\n"
        "v_cvt_pk_bf16_f32 v146, v184, v185\n"
        "v_cvt_pk_bf16_f32 v147, v186, v187\n"
        "v_mfma_f32_16x16x32_bf16 v[108:111], a[200:203], v[198:201], v[108:111]\n"
        "s_nop 1\n"
        "v_mfma_f32_16x16x32_bf16 v[184:187], a[212:215], v[198:201], 0\n"
        "s_nop 2\n"
        "v_mfma_f32_16x16x32_bf16 v[184:187], a[208:211], v[210:213], v[184:187]\n"
        "s_nop 0\n"
        "v_cvt_pk_bf16_f32 v192, v108, v109\n"
        "v_cvt_pk_bf16_f32 v193, v110, v111\n"
        "v_mfma_f32_16x16x32_bf16 v[184:187], a[204:207], v[202:205], v[184:187]\n"
        "s_nop 1\n"
        "v_mfma_f32_16x16x32_bf16 v[108:111], v[222:225], v[188:191], 0\n"
        "s_nop 2\n"
        "v_mfma_f32_16x16x32_bf16 v[108:111], v[206:209], v[144:147], v[108:111]\n"
        "s_nop 0\n"
        "v_cvt_pk_bf16_f32 v194, v184, v185\n"
        "v_cvt_pk_bf16_f32 v195, v186, v187\n"
        "s_nop 1\n"
        "v_mfma_f32_16x16x32_bf16 v[108:111], v[218:221], v[192:195], v[108:111]\n"
        "s_nop 1\n"
        "v_mfma_f32_16x16x32_bf16 v[214:217], v[230:233], v[192:195], 0\n"
        "s_nop 2\n"
        "v_mfma_f32_16x16x32_bf16 v[214:217], v[234:237], v[144:147], v[214:217]\n"
        "s_nop 0\n"
        "v_mul_f32 v108, v108, v168\n"
        "v_mul_f32 v109, v109, v169\n"
        "v_mfma_f32_16x16x32_bf16 v[214:217], v[226:229], v[188:191], v[214:217]\n"
        "s_nop 0\n"
        "v_mul_f32 v110, v110, v170\n"
        "v_mul_f32 v111, v111, v171\n"
        "v_mfma_f32_16x16x32_bf16 v[238:241], v[202:205], v[188:191], 0\n"
        "s_nop 0\n"
        "v_max3_f32 v251, v108, v109, v110\n"
        "v_mfma_f32_16x16x32_bf16 v[238:241], v[210:213], v[144:147], v[238:241]\n"
        "s_nop 0\n"
        "v_mul_f32 v214, v214, v176\n"
        "v_mul_f32 v215, v215, v177\n"
        "v_mfma_f32_16x16x32_bf16 v[238:241], v[198:201], v[192:195], v[238:241]\n"
        "v_mul_f32 v216, v216, v178\n"
        "v_mul_f32 v217, v217, v179\n"
        "v_mfma_f32_16x16x32_bf16 v[184:187], v[148:151], v[192:195], 0\n"
        "s_nop 0\n"
        "v_max3_f32 v251, v251, v111, v214\n"
        "v_max3_f32 v251, v251, v215, v216\n"
        "v_mfma_f32_16x16x32_bf16 v[184:187], v[112:115], v[144:147], v[184:187]\n"
        "s_nop 0\n"
        "v_mul_f32 v238, v238, v180\n"
        "v_mul_f32 v239, v239, v181\n"
        "v_mfma_f32_16x16x32_bf16 v[184:187], v[120:123], v[188:191], v[184:187]\n"
        "s_nop 0\n"
        "v_mul_f32 v240, v240, v182\n"
        "v_mul_f32 v241, v241, v183\n"
        "v_max3_f32 v251, v251, v217, v238\n"
        "v_max3_f32 v251, v251, v239, v240\n"
        "s_nop 2\n"
        "v_mul_f32 v184, v184, v253\n"
        "v_cndmask_b32_e64 v184, v255, v184, s[62:63]\n"
        "v_max3_f32 v251, v251, v241, v184\n"
        "v_mov_b32 v252, v251\n"
        "s_nop 1\n"
        "v_permlane32_swap_b32 v251, v252\n"
        "v_max_f32 v251, v251, v252\n"
        "v_mov_b32 v252, v251\n"
        "s_nop 1\n"
        "v_permlane16_swap_b32 v251, v252\n"
        "v_max_f32 v251, v251, v252\n"
        "v_mul_f32 v251, v251, v254\n"
        "v_fma_f32 v108, v108, v254, -v251\n"
        "v_exp_f32 v108, v108\n"
        "v_fma_f32 v109, v109, v254, -v251\n"
        "v_exp_f32 v109, v109\n"
        "s_nop 0\n"
        "v_add_f32 v252, v108, v109\n"
        "v_mul_f32 v108, v108, v168\n"
        "v_fma_f32 v184, v184, v254, -v251\n"
        "v_exp_f32 v184, v184\n"
        "v_fma_f32 v241, v241, v254, -v251\n"
        "v_exp_f32 v241, v241\n"
        "v_fma_f32 v240, v240, v254, -v251\n"
        "v_exp_f32 v240, v240\n"
        "v_fma_f32 v239, v239, v254, -v251\n"
        "v_exp_f32 v239, v239\n"
        "v_fma_f32 v238, v238, v254, -v251\n"
        "v_exp_f32 v238, v238\n"
        "v_fma_f32 v217, v217, v254, -v251\n"
        "v_exp_f32 v217, v217\n"
        "v_fma_f32 v216, v216, v254, -v251\n"
        "v_exp_f32 v216, v216\n"
        "v_fma_f32 v215, v215, v254, -v251\n"
        "v_exp_f32 v215, v215\n"
        "v_fma_f32 v214, v214, v254, -v251\n"
        "v_exp_f32 v214, v214\n"
        "v_fma_f32 v111, v111, v254, -v251\n"
        "v_exp_f32 v111, v111\n"
        "v_fma_f32 v110, v110, v254, -v251\n"
        "v_exp_f32 v110, v110\n"
        "s_nop 0\n"
        "v_add_f32 v252, v252, v110\n"
        "v_add_f32 v252, v252, v111\n"
        "v_add_f32 v252, v252, v214\n"
        "v_add_f32 v252, v252, v215\n"
        "v_add_f32 v252, v252, v216\n"
        "v_add_f32 v252, v252, v217\n"
        "v_add_f32 v252, v252, v238\n"
        "v_add_f32 v252, v252, v239\n"
        "v_add_f32 v252, v252, v240\n"
        "v_add_f32 v252, v252, v241\n"
        "v_add_f32 v252, v252, v184\n"
        "v_mov_b32 v254, v252\n"
        "s_nop 1\n"
        "v_permlane32_swap_b32 v252, v254\n"
        "v_add_f32 v252, v252, v254\n"
        "v_mov_b32 v254, v252\n"
        "s_nop 1\n"
        "v_permlane16_swap_b32 v252, v254\n"
        "v_add_f32 v252, v252, v254\n"
        "v_rcp_f32 v252, v252\n"
        "s_nop 0\n"
        "v_mul_f32 v108, v108, v252\n"
        "v_mul_f32 v109, v109, v169\n"
        "v_mul_f32 v109, v109, v252\n"
        "v_cvt_pk_bf16_f32 v188, v108, v109\n"
        "v_mul_f32 v110, v110, v170\n"
        "v_mul_f32 v110, v110, v252\n"
        "v_mul_f32 v111, v111, v171\n"
        "v_mul_f32 v111, v111, v252\n"
        "v_cvt_pk_bf16_f32 v189, v110, v111\n"
        "v_mul_f32 v214, v214, v176\n"
        "v_mul_f32 v214, v214, v252\n"
        "v_mul_f32 v215, v215, v177\n"
        "v_mul_f32 v215, v215, v252\n"
        "v_cvt_pk_bf16_f32 v190, v214, v215\n"
        "v_mul_f32 v216, v216, v178\n"
        "v_mul_f32 v216, v216, v252\n"
        "v_mul_f32 v217, v217, v179\n"
        "v_mul_f32 v217, v217, v252\n"
        "v_cvt_pk_bf16_f32 v191, v216, v217\n"
        "s_nop 1\n"
        "v_mfma_f32_16x16x32_bf16 v[108:111], v[188:191], v[128:131], 0\n"
        "v_mul_f32 v238, v238, v180\n"
        "v_mul_f32 v239, v239, v181\n"
        "v_mul_f32 v238, v238, v252\n"
        "v_mul_f32 v239, v239, v252\n"
        "v_cvt_pk_bf16_f32 v132, v238, v239\n"
        "v_mul_f32 v240, v240, v182\n"
        "v_mul_f32 v240, v240, v252\n"
        "v_mul_f32 v241, v241, v183\n"
        "v_mul_f32 v241, v241, v252\n"
        "v_cvt_pk_bf16_f32 v133, v240, v241\n"
        "v_mul_f32 v184, v184, v253\n"
        "v_mul_f32 v184, v184, v252\n"
        "v_cvt_pk_bf16_f32 v134, v184, 0\n"
        "s_nop 1\n"
        "v_mfma_f32_16x16x32_bf16 v[108:111], v[132:135], v[136:139], v[108:111]\n"
        "v_mfma_f32_16x16x32_bf16 v[108:111], v[202:205], v[88:91], v[108:111]\n"
        "v_mfma_f32_16x16x32_bf16 v[184:187], v[202:205], v[80:83], 0\n"
        "v_mfma_f32_16x16x32_bf16 v[184:187], v[132:135], v[40:43], v[184:187]\n"
        "v_mfma_f32_16x16x32_bf16 v[184:187], v[188:191], v[152:155], v[184:187]\n"
        "s_nop 1\n"
        "v_mfma_f32_16x16x32_bf16 v[144:147], v[188:191], v[160:163], 0\n"
        "s_nop 2\n"
        "v_mfma_f32_16x16x32_bf16 v[144:147], v[132:135], v[140:143], v[144:147]\n"
        "s_add_u32 s91, s89, 490560\n"
        "v_cvt_pk_bf16_f32 v238, v108, v184\n"
        "v_cvt_pk_bf16_f32 v124, v109, v185\n"
        "v_mfma_f32_16x16x32_bf16 v[144:147], v[210:213], v[88:91], v[144:147]\n"
        "v_cvt_pk_bf16_f32 v192, v110, v186\n"
        "v_cvt_pk_bf16_f32 v214, v111, v187\n"
        "v_mfma_f32_16x16x32_bf16 v[108:111], v[210:213], v[80:83], 0\n"
        "v_mfma_f32_16x16x32_bf16 v[108:111], v[132:135], v[116:119], v[108:111]\n"
        "v_mfma_f32_16x16x32_bf16 v[108:111], v[188:191], v[156:159], v[108:111]\n"
        "s_nop 1\n"
        "v_mfma_f32_16x16x32_bf16 v[184:187], v[188:191], v[164:167], 0\n"
        "s_nop 2\n"
        "v_mfma_f32_16x16x32_bf16 v[184:187], v[132:135], v[104:107], v[184:187]\n"
        "s_nop 0\n"
        "v_cvt_pk_bf16_f32 v239, v144, v108\n"
        "v_cvt_pk_bf16_f32 v125, v145, v109\n"
        "v_mfma_f32_16x16x32_bf16 v[184:187], v[198:201], v[88:91], v[184:187]\n"
        "v_cvt_pk_bf16_f32 v193, v146, v110\n"
        "v_cvt_pk_bf16_f32 v215, v147, v111\n"
        "v_mfma_f32_16x16x32_bf16 v[108:111], v[198:201], v[80:83], 0\n"
        "v_mfma_f32_16x16x32_bf16 v[108:111], v[132:135], v[34:37], v[108:111]\n"
        "v_mfma_f32_16x16x32_bf16 v[108:111], v[188:191], v[172:175], v[108:111]\n"
        "s_nop 1\n"
        "v_mfma_f32_16x16x32_bf16 v[144:147], a[144:147], v[120:123], 0\n"
        "s_nop 2\n"
        "v_mfma_f32_16x16x32_bf16 v[144:147], a[148:151], v[112:115], v[144:147]\n"
        "s_nop 0\n"
        "v_cvt_pk_bf16_f32 v240, v184, v108\n"
        "v_cvt_pk_bf16_f32 v126, v185, v109\n"
        "v_mfma_f32_16x16x32_bf16 v[144:147], a[152:155], v[148:151], v[144:147]\n"
        "s_nop 0\n"
        "v_cvt_pk_bf16_f32 v194, v186, v110\n"
        "v_cvt_pk_bf16_f32 v216, v187, v111\n"
        "v_mfma_f32_16x16x32_bf16 v[132:135], a[164:167], v[148:151], 0\n"
        "s_nop 1\n"
        "v_mfma_f32_16x16x32_bf16 v[132:135], a[160:163], v[112:115], v[132:135]\n"
        "s_nop 0\n"
        "v_cvt_pk_bf16_f32 v108, v144, v145\n"
        "v_cvt_pk_bf16_f32 v109, v146, v147\n"
        "v_mfma_f32_16x16x32_bf16 v[132:135], a[156:159], v[120:123], v[132:135]\n"
        "s_nop 0\n"
        "v_mfma_f32_16x16x32_bf16 v[184:187], a[168:171], v[120:123], 0\n"
        "buffer_store_dwordx3 v[238:240], v15, s[40:43], s90 offen offset:768 sc1 nt\n"
        "s_add_u32 s90, s89, 588672\n"
        "s_nop 1\n"
        "v_mfma_f32_16x16x32_bf16 v[184:187], a[172:175], v[112:115], v[184:187]\n"
        "s_nop 0\n"
        "v_cvt_pk_bf16_f32 v110, v132, v133\n"
        "v_cvt_pk_bf16_f32 v111, v134, v135\n"
        "v_mfma_f32_16x16x32_bf16 v[184:187], a[176:179], v[148:151], v[184:187]\n"
        "buffer_store_dwordx3 v[124:126], v14, s[40:43], s91 offen offset:192 sc1 nt\n"
        "s_nop 0\n"
        "v_mfma_f32_16x16x32_bf16 v[132:135], a[188:191], v[148:151], 0\n"
        "s_nop 2\n"
        "v_mfma_f32_16x16x32_bf16 v[132:135], a[184:187], v[112:115], v[132:135]\n"
        "buffer_store_dwordx3 v[192:194], v16, s[40:43], s91 offen offset:960 sc1 nt\n"
        "v_cvt_pk_bf16_f32 v144, v184, v185\n"
        "v_cvt_pk_bf16_f32 v145, v186, v187\n"
        "v_mfma_f32_16x16x32_bf16 v[132:135], a[180:183], v[120:123], v[132:135]\n"
        "s_nop 0\n"
        "v_mfma_f32_16x16x32_bf16 v[184:187], a[192:195], v[120:123], 0\n"
        "buffer_store_dwordx3 v[214:216], v14, s[40:43], s90 offen offset:384 sc1 nt\n"
        "s_nop 2\n"
        "v_mfma_f32_16x16x32_bf16 v[184:187], a[196:199], v[112:115], v[184:187]\n"
        "s_nop 0\n"
        "v_cvt_pk_bf16_f32 v146, v132, v133\n"
        "v_cvt_pk_bf16_f32 v147, v134, v135\n"
        "v_mfma_f32_16x16x32_bf16 v[184:187], a[200:203], v[148:151], v[184:187]\n"
        "s_nop 1\n"
        "v_mfma_f32_16x16x32_bf16 v[132:135], a[212:215], v[148:151], 0\n"
        "s_nop 2\n"
        "v_mfma_f32_16x16x32_bf16 v[132:135], a[208:211], v[112:115], v[132:135]\n"
        "s_nop 0\n"
        "v_cvt_pk_bf16_f32 v214, v184, v185\n"
        "v_cvt_pk_bf16_f32 v215, v186, v187\n"
        "v_mfma_f32_16x16x32_bf16 v[132:135], a[204:207], v[120:123], v[132:135]\n"
        "s_nop 1\n"
        "v_mfma_f32_16x16x32_bf16 v[184:187], v[222:225], v[108:111], 0\n"
        "s_nop 2\n"
        "v_mfma_f32_16x16x32_bf16 v[184:187], v[206:209], v[144:147], v[184:187]\n"
        "s_nop 0\n"
        "v_cvt_pk_bf16_f32 v216, v132, v133\n"
        "v_cvt_pk_bf16_f32 v217, v134, v135\n"
        "s_nop 1\n"
        "v_mfma_f32_16x16x32_bf16 v[184:187], v[218:221], v[214:217], v[184:187]\n"
        "s_nop 1\n"
        "v_mfma_f32_16x16x32_bf16 v[192:195], v[230:233], v[214:217], 0\n"
        "s_nop 2\n"
        "v_mfma_f32_16x16x32_bf16 v[192:195], v[234:237], v[144:147], v[192:195]\n"
        "s_nop 0\n"
        "v_mul_f32 v184, v184, v168\n"
        "v_mul_f32 v185, v185, v169\n"
        "v_mfma_f32_16x16x32_bf16 v[192:195], v[226:229], v[108:111], v[192:195]\n"
        "s_nop 0\n"
        "v_mul_f32 v186, v186, v170\n"
        "v_mul_f32 v187, v187, v171\n"
        "v_mfma_f32_16x16x32_bf16 v[188:191], v[202:205], v[108:111], 0\n"
        "s_nop 0\n"
        "v_max3_f32 v254, v184, v185, v186\n"
        "v_mfma_f32_16x16x32_bf16 v[188:191], v[210:213], v[144:147], v[188:191]\n"
        "s_nop 0\n"
        "v_mul_f32 v192, v192, v176\n"
        "v_mul_f32 v193, v193, v177\n"
        "v_mfma_f32_16x16x32_bf16 v[188:191], v[198:201], v[214:217], v[188:191]\n"
        "v_mul_f32 v194, v194, v178\n"
        "v_mul_f32 v195, v195, v179\n"
        "v_mfma_f32_16x16x32_bf16 v[132:135], v[148:151], v[214:217], 0\n"
        "s_nop 0\n"
        "v_max3_f32 v254, v254, v187, v192\n"
        "v_max3_f32 v254, v254, v193, v194\n"
        "v_mfma_f32_16x16x32_bf16 v[132:135], v[112:115], v[144:147], v[132:135]\n"
        "s_nop 0\n"
        "v_mul_f32 v188, v188, v180\n"
        "v_mul_f32 v189, v189, v181\n"
        "v_mfma_f32_16x16x32_bf16 v[132:135], v[120:123], v[108:111], v[132:135]\n"
        "s_nop 0\n"
        "v_mul_f32 v190, v190, v182\n"
        "v_mul_f32 v191, v191, v183\n"
        "v_max3_f32 v254, v254, v195, v188\n"
        "v_max3_f32 v254, v254, v189, v190\n"
        "s_nop 2\n"
        "v_mul_f32 v132, v132, v253\n"
        "v_cndmask_b32_e64 v132, v255, v132, s[62:63]\n"
        "v_max3_f32 v254, v254, v191, v132\n"
        "v_mov_b32 v255, v254\n"
        "s_nop 1\n"
        "v_permlane32_swap_b32 v254, v255\n"
        "v_max_f32 v254, v254, v255\n"
        "v_mov_b32 v255, v254\n"
        "s_nop 1\n"
        "v_permlane16_swap_b32 v254, v255\n"
        "v_max_f32 v254, v254, v255\n"
        "v_mul_f32 v254, v254, v253\n"
        "v_fma_f32 v184, v184, v253, -v254\n"
        "v_exp_f32 v184, v184\n"
        "v_fma_f32 v185, v185, v253, -v254\n"
        "v_exp_f32 v185, v185\n"
        "s_nop 0\n"
        "v_add_f32 v255, v184, v185\n"
        "v_mul_f32 v184, v184, v168\n"
        "v_fma_f32 v132, v132, v253, -v254\n"
        "v_exp_f32 v132, v132\n"
        "v_fma_f32 v191, v191, v253, -v254\n"
        "v_exp_f32 v191, v191\n"
        "v_fma_f32 v190, v190, v253, -v254\n"
        "v_exp_f32 v190, v190\n"
        "v_fma_f32 v189, v189, v253, -v254\n"
        "v_exp_f32 v189, v189\n"
        "v_fma_f32 v188, v188, v253, -v254\n"
        "v_exp_f32 v188, v188\n"
        "v_fma_f32 v195, v195, v253, -v254\n"
        "v_exp_f32 v195, v195\n"
        "v_fma_f32 v194, v194, v253, -v254\n"
        "v_exp_f32 v194, v194\n"
        "v_fma_f32 v193, v193, v253, -v254\n"
        "v_exp_f32 v193, v193\n"
        "v_fma_f32 v192, v192, v253, -v254\n"
        "v_exp_f32 v192, v192\n"
        "v_fma_f32 v187, v187, v253, -v254\n"
        "v_exp_f32 v187, v187\n"
        "v_fma_f32 v186, v186, v253, -v254\n"
        "v_exp_f32 v186, v186\n"
        "s_nop 0\n"
        "v_add_f32 v255, v255, v186\n"
        "v_add_f32 v255, v255, v187\n"
        "v_add_f32 v255, v255, v192\n"
        "v_add_f32 v255, v255, v193\n"
        "v_add_f32 v255, v255, v194\n"
        "v_add_f32 v255, v255, v195\n"
        "v_add_f32 v255, v255, v188\n"
        "v_add_f32 v255, v255, v189\n"
        "v_add_f32 v255, v255, v190\n"
        "v_add_f32 v255, v255, v191\n"
        "v_add_f32 v255, v255, v132\n"
        "v_mov_b32 v254, v255\n"
        "s_nop 1\n"
        "v_permlane32_swap_b32 v255, v254\n"
        "v_add_f32 v255, v255, v254\n"
        "v_mov_b32 v254, v255\n"
        "s_nop 1\n"
        "v_permlane16_swap_b32 v255, v254\n"
        "v_add_f32 v255, v255, v254\n"
        "v_rcp_f32 v255, v255\n"
        "s_nop 0\n"
        "v_mul_f32 v184, v184, v255\n"
        "v_mul_f32 v185, v185, v169\n"
        "v_mul_f32 v185, v185, v255\n"
        "v_cvt_pk_bf16_f32 v144, v184, v185\n"
        "v_mul_f32 v186, v186, v170\n"
        "v_mul_f32 v186, v186, v255\n"
        "v_mul_f32 v187, v187, v171\n"
        "v_mul_f32 v187, v187, v255\n"
        "v_cvt_pk_bf16_f32 v145, v186, v187\n"
        "v_mul_f32 v192, v192, v176\n"
        "v_mul_f32 v192, v192, v255\n"
        "v_mul_f32 v193, v193, v177\n"
        "v_mul_f32 v193, v193, v255\n"
        "v_cvt_pk_bf16_f32 v146, v192, v193\n"
        "v_mul_f32 v194, v194, v178\n"
        "v_mul_f32 v194, v194, v255\n"
        "v_mul_f32 v195, v195, v179\n"
        "v_mul_f32 v195, v195, v255\n"
        "v_cvt_pk_bf16_f32 v147, v194, v195\n"
        "s_nop 1\n"
        "v_mfma_f32_16x16x32_bf16 v[108:111], v[144:147], v[128:131], 0\n"
        "v_mul_f32 v188, v188, v180\n"
        "v_mul_f32 v189, v189, v181\n"
        "v_mul_f32 v188, v188, v255\n"
        "v_mul_f32 v189, v189, v255\n"
        "v_cvt_pk_bf16_f32 v124, v188, v189\n"
        "v_mul_f32 v190, v190, v182\n"
        "v_mul_f32 v190, v190, v255\n"
        "v_mul_f32 v191, v191, v183\n"
        "v_mul_f32 v191, v191, v255\n"
        "v_cvt_pk_bf16_f32 v125, v190, v191\n"
        "v_mul_f32 v132, v132, v253\n"
        "v_mul_f32 v132, v132, v255\n"
        "v_cvt_pk_bf16_f32 v126, v132, 0\n"
        "s_nop 1\n"
        "v_mfma_f32_16x16x32_bf16 v[108:111], v[124:127], v[136:139], v[108:111]\n"
        "v_mfma_f32_16x16x32_bf16 v[108:111], v[120:123], v[88:91], v[108:111]\n"
        "v_mfma_f32_16x16x32_bf16 v[128:131], v[120:123], v[80:83], 0\n"
        "v_mfma_f32_16x16x32_bf16 v[128:131], v[124:127], v[40:43], v[128:131]\n"
        "v_mfma_f32_16x16x32_bf16 v[128:131], v[144:147], v[152:155], v[128:131]\n"
        "s_nop 1\n"
        "v_mfma_f32_16x16x32_bf16 v[120:123], v[144:147], v[160:163], 0\n"
        "s_nop 2\n"
        "v_mfma_f32_16x16x32_bf16 v[120:123], v[124:127], v[140:143], v[120:123]\n"
        "s_nop 0\n"
        "v_cvt_pk_bf16_f32 v130, v108, v128\n"
        "v_mfma_f32_16x16x32_bf16 v[120:123], v[112:115], v[88:91], v[120:123]\n"
        "v_mfma_f32_16x16x32_bf16 v[108:111], v[112:115], v[80:83], 0\n"
        "v_mfma_f32_16x16x32_bf16 v[108:111], v[124:127], v[116:119], v[108:111]\n"
        "v_mfma_f32_16x16x32_bf16 v[108:111], v[144:147], v[156:159], v[108:111]\n"
        "s_nop 1\n"
        "v_mfma_f32_16x16x32_bf16 v[40:43], v[144:147], v[164:167], 0\n"
        "s_nop 2\n"
        "v_mfma_f32_16x16x32_bf16 v[40:43], v[124:127], v[104:107], v[40:43]\n"
        "s_nop 0\n"
        "v_cvt_pk_bf16_f32 v131, v120, v108\n"
        "v_mfma_f32_16x16x32_bf16 v[40:43], v[148:151], v[88:91], v[40:43]\n"
        "v_mfma_f32_16x16x32_bf16 v[104:107], v[148:151], v[80:83], 0\n"
        "v_mfma_f32_16x16x32_bf16 v[104:107], v[124:127], v[34:37], v[104:107]\n"
        "s_add_u32 s46, s89, 590016\n"
        "v_mfma_f32_16x16x32_bf16 v[104:107], v[144:147], v[172:175], v[104:107]\n"
        "s_nop 2\n"
        "s_min_u32 s46, s46, s42\n"
        "s_nop 3\n"
        "v_cvt_pk_bf16_f32 v132, v40, v104\n"
        "buffer_store_dwordx3 v[130:132], v17, s[44:47], s90 offen offset:1152 sc1 nt\n"
        "s_nop 15\n"
        "s_cmp_lt_i32 s85, -1\n"
        "s_cbranch_scc1 FIN_%=\n"
        "s_mov_b32 s89, 0x80000000\n"
        "s_waitcnt vmcnt(0)\n"
        "ds_read_b128 v[146:149], v9\n"
        "ds_read_b128 v[152:155], v10\n"
        "ds_read_b128 v[158:161], v11\n"
        "ds_read_b128 v[126:129], v9 offset:3072\n"
        "ds_read_b128 v[132:135], v10 offset:3072\n"
        "ds_read_b128 v[138:141], v11 offset:3072\n"
        "ds_read_b128 v[106:109], v9 offset:6144\n"
        "ds_read_b128 v[112:115], v10 offset:6144\n"
        "ds_read_b128 v[118:121], v11 offset:6144\n"
        "s_cmp_gt_i32 s85, 1\n"
        "s_cbranch_scc1 SLX_%=\n"
        "v_readfirstlane_b32 s50, v26\n"
        "s_nop 3\n"
        "s_sub_u32 s50, s50, s20\n"
        "s_min_u32 s50, s50, 31\n"
        "s_and_b32 s92, s29, 1\n"
        "s_lshl_b32 s92, s92, 2\n"
        "s_add_u32 s92, s92, s49\n"
        "v_mov_b32 v27, s92\n"
        "v_mov_b32 v26, s50\n"
        "ds_write_b32 v27, v26\n"
        "s_lshl_b32 s50, s50, 2\n"
        "s_waitcnt lgkmcnt(0)\n"
        "v_mov_b32 v27, s50\n"
        "v_mov_b32 v26, 1\n"
        "s_mov_b64 exec, 1\n"
        "buffer_atomic_add v26, v27, s[16:19], 0 offen offset:4\n"
        "s_mov_b64 exec, -1\n"
        "s_nop 1\n"
        "SLX_%=:\n"
        "s_waitcnt lgkmcnt(0)\n"
        "s_nop 7\n"
        "s_cmp_ge_i32 s85, 0\n"
        "s_cbranch_scc1 TOP_%=\n"
        "s_branch LAST_%=\n"
        "DY1_%=:\n"
        "s_and_b32 s50, s29, 1\n"
        "s_lshl_b32 s50, s50, 2\n"
        "s_add_u32 s50, s50, 163424\n"
        "v_mov_b32 v27, s50\n"
        "ds_read_b32 v27, v27\n"
        "s_waitcnt lgkmcnt(0)\n"
        "v_readfirstlane_b32 s50, v27\n"
        "s_nop 3\n"
        "s_lshl_b32 s92, s50, 5\n"
        "s_add_u32 s92, s92, s48\n"
        "s_cmp_ge_u32 s92, 5329\n"
        "s_cbranch_scc1 DY1B_%=\n"
        "s_mul_i32 s87, s92, 1344\n"
        "s_mul_i32 s92, s92, 57457\n"
        "s_lshr_b32 s92, s92, 22\n"
        "s_mul_i32 s92, s92, 588672\n"
        "s_add_u32 s87, s87, s92\n"
        "s_add_u32 s85, s85, 1\n"
        "s_add_u32 s50, s50, 20\n"
        "s_sub_u32 s23, s50, s53\n"
        "s_mul_i32 s23, s23, 1584\n"
        "s_mov_b32 s53, s50\n"
        "s_branch DY1B_%=\n"
        "DY2_%=:\n"
        "v_mov_b32 v26, 1\n"
        "s_mov_b64 exec, 1\n"
        "buffer_atomic_add v26, off, s[16:19], 0 sc0\n"
        "s_mov_b64 exec, -1\n"
        "s_branch DY2B_%=\n"
        "SP6_%=:\n"
        "s_sleep 1\n"
        "ds_read_u16 v252, v19 offset:2048\n"
        "s_waitcnt lgkmcnt(0)\n"
        "v_cmp_ne_u32_e64 s[24:25], s30, v252\n"
        "s_nop 4\n"
        "s_cmp_eq_u64 s[24:25], 0\n"
        "s_cbranch_scc1 BK6_%=\n"
        "s_sub_u32 s31, s31, 1\n"
        "s_cbranch_scc0 SP6_%=\n"
        "s_endpgm\n"
        "SP10_%=:\n"
        "s_sleep 1\n"
        "ds_read_u16 v251, v19 offset:2560\n"
        "s_waitcnt lgkmcnt(0)\n"
        "v_cmp_ne_u32_e64 s[24:25], s29, v251\n"
        "s_nop 4\n"
        "s_cmp_eq_u64 s[24:25], 0\n"
        "s_cbranch_scc1 BK10_%=\n"
        "s_sub_u32 s31, s31, 1\n"
        "s_cbranch_scc0 SP10_%=\n"
        "s_endpgm\n"
        "DY3_%=:\n"
        "v_readfirstlane_b32 s50, v26\n"
        "s_nop 3\n"
        "s_sub_u32 s50, s50, s20\n"
        "s_min_u32 s50, s50, 31\n"
        "s_and_b32 s92, s29, 1\n"
        "s_lshl_b32 s92, s92, 2\n"
        "s_add_u32 s92, s92, s49\n"
        "v_mov_b32 v27, s92\n"
        "v_mov_b32 v26, s50\n"
        "ds_write_b32 v27, v26\n"
        "s_lshl_b32 s50, s50, 2\n"
        "s_waitcnt lgkmcnt(0)\n"
        "v_mov_b32 v27, s50\n"
        "v_mov_b32 v26, 1\n"
        "s_mov_b64 exec, 1\n"
        "buffer_atomic_add v26, v27, s[16:19], 0 offen offset:4\n"
        "s_mov_b64 exec, -1\n"
        "s_nop 1\n"
        "s_branch DY3B_%=\n"
        "SP20_%=:\n"
        "s_sleep 1\n"
        "ds_read_u16 v255, v19 offset:2048\n"
        "s_waitcnt lgkmcnt(0)\n"
        "v_cmp_ne_u32_e64 s[24:25], s30, v255\n"
        "s_nop 4\n"
        "s_cmp_eq_u64 s[24:25], 0\n"
        "s_cbranch_scc1 BK20_%=\n"
        "s_sub_u32 s31, s31, 1\n"
        "s_cbranch_scc0 SP20_%=\n"
        "s_endpgm\n"
        :: "{v[0:15]}"(c0), "{v[16:23]}"(c1), "{v[24:27]}"(c2), "{v[164:171]}"(sp), "{v[30:45]}"(f0), "{v[46:49]}"(f1), "{s15}"(own4), "{s[16:19]}"(srdl), "{s[20:23]}"(sol), "{s[36:39]}"(srdx), "{s[40:43]}"(srdo), "{s[48:55]}"(si)
        : "v28", "v29", "v50", "v51", "v52", "v53", "v54", "v55", "v56", "v57", "v58", "v59", "v60", "v61", "v62", "v63", "v64", "v65", "v66", "v67", "v68", "v69", "v70", "v71", "v72", "v73", "v74", "v75", "v76", "v77", "v78", "v79", "v80", "v81", "v82", "v83", "v84", "v85", "v86", "v87", "v88", "v89", "v90", "v91", "v92", "v93", "v94", "v95", "v96", "v97", "v98", "v99", "v100", "v101", "v102", "v103", "v104", "v105", "v106", "v107", "v108", "v109", "v110", "v111", "v112", "v113", "v114", "v115", "v116", "v117", "v118", "v119", "v120", "v121", "v122", "v123", "v124", "v125", "v126", "v127", "v128", "v129", "v130", "v131", "v132", "v133", "v134", "v135", "v136", "v137", "v138", "v139", "v140", "v141", "v142", "v143", "v144", "v145", "v146", "v147", "v148", "v149", "v150", "v151", "v152", "v153", "v154", "v155", "v156", "v157", "v158", "v159", "v160", "v161", "v162", "v163", "v172", "v173", "v174", "v175", "v176", "v177", "v178", "v179", "v180", "v181", "v182", "v183", "v184", "v185", "v186", "v187", "v188", "v189", "v190", "v191", "v192", "v193", "v194", "v195", "v196", "v197", "v198", "v199", "v200", "v201", "v202", "v203", "v204", "v205", "v206", "v207", "v208", "v209", "v210", "v211", "v212", "v213", "v214", "v215", "v216", "v217", "v218", "v219", "v220", "v221", "v222", "v223", "v224", "v225", "v226", "v227", "v228", "v229", "v230", "v231", "v232", "v233", "v234", "v235", "v236", "v237", "v238", "v239", "v240", "v241", "v242", "v243", "v244", "v245", "v246", "v247", "v248", "v249", "v250", "v251", "v252", "v253", "v254", "v255", "a0", "a1", "a2", "a3", "a4", "a5", "a6", "a7", "a8", "a9", "a10", "a11", "a12", "a13", "a14", "a15", "a16", "a17", "a18", "a19", "a20", "a21", "a22", "a23", "a24", "a25", "a26", "a27", "a28", "a29", "a30", "a31", "a32", "a33", "a34", "a35", "a36", "a37", "a38", "a39", "a40", "a41", "a42", "a43", "a44", "a45", "a46", "a47", "a48", "a49", "a50", "a51", "a52", "a53", "a54", "a55", "a56", "a57", "a58", "a59", "a60", "a61", "a62", "a63", "a64", "a65", "a66", "a67", "a68", "a69", "a70", "a71", "a72", "a73", "a74", "a75", "a76", "a77", "a78", "a79", "a80", "a81", "a82", "a83", "a84", "a85", "a86", "a87", "a88", "a89", "a90", "a91", "a92", "a93", "a94", "a95", "a96", "a97", "a98", "a99", "a100", "a101", "a102", "a103", "a104", "a105", "a106", "a107", "a108", "a109", "a110", "a111", "a112", "a113", "a114", "a115", "a116", "a117", "a118", "a119", "a120", "a121", "a122", "a123", "a124", "a125", "a126", "a127", "a128", "a129", "a130", "a131", "a132", "a133", "a134", "a135", "a136", "a137", "a138", "a139", "a140", "a141", "a142", "a143", "a144", "a145", "a146", "a147", "a148", "a149", "a150", "a151", "a152", "a153", "a154", "a155", "a156", "a157", "a158", "a159", "a160", "a161", "a162", "a163", "a164", "a165", "a166", "a167", "a168", "a169", "a170", "a171", "a172", "a173", "a174", "a175", "a176", "a177", "a178", "a179", "a180", "a181", "a182", "a183", "a184", "a185", "a186", "a187", "a188", "a189", "a190", "a191", "a192", "a193", "a194", "a195", "a196", "a197", "a198", "a199", "a200", "a201", "a202", "a203", "a204", "a205", "a206", "a207", "a208", "a209", "a210", "a211", "a212", "a213", "a214", "a215", "a216", "a217", "a218", "a219", "a220", "a221", "a222", "a223", "a224", "a225", "a226", "a227", "a228", "a229", "a230", "a231", "a232", "a233", "a234", "a235", "a236", "a237", "a238", "a239", "a240", "a241", "a242", "a243", "a244", "a245", "a246", "a247", "a248", "a249", "a250", "a251", "a252", "a253", "a254", "a255", "s24", "s25", "s26", "s27", "s28", "s29", "s30", "s31", "s44", "s45", "s46", "s47", "s56", "s57", "s58", "s59", "s60", "s61", "s62", "s63", "s64", "s65", "s66", "s67", "s68", "s69", "s70", "s71", "s72", "s73", "s74", "s75", "s76", "s77", "s78", "s79", "s80", "s81", "s82", "s83", "s84", "s85", "s86", "s87", "s88", "s89", "s90", "s91", "s92", "s93", "s94", "s95", "vcc", "scc", "memory");
}

extern "C" void kernel_launch(void* const* d_in, const int* in_sizes, int n_in, void* d_out, int out_size, void* d_ws, size_t ws_size, hipStream_t stream) {
    (void)in_sizes; (void)n_in; (void)out_size; (void)ws_size;
    (void)hipFuncSetAttribute((const void*)swin_kernel, hipFuncAttributeMaxDynamicSharedMemorySize, LDS_BYTES);
    swin_kernel<<<GRID, 256, LDS_BYTES, stream>>>((const char*)d_in[0], (const char*)d_in[1], (const char*)d_in[2], (const char*)d_in[3], (const char*)d_in[4], (char*)d_out, (const char*)d_ws);
}
```

```cpp
#define NLC 5
#define WSTEP 32
#define CPL 8
#define SMASK 31914
#define ODDS 1
#define DYN 1
#define EARLY 1
#define TLBW 0
#include <hip/hip_runtime.h>
#include <hip/hip_bf16.h>

#define DI __device__ __forceinline__

using u32x2 = __attribute__((ext_vector_type(2))) unsigned;
using u32x4 = __attribute__((ext_vector_type(4))) unsigned;
using u32x8 = __attribute__((ext_vector_type(8))) unsigned;
using u32x16 = __attribute__((ext_vector_type(16))) unsigned;

constexpr int HH = 511, WW = 511, NWX = 73, NWIN = NWX * NWX, GRID = 256;
constexpr int TOKB = 192, ROWB = WW * TOKB;
constexpr unsigned FRAMEB = (unsigned)HH * ROWB, TOTALB = FRAMEB * 4;
constexpr unsigned FS = 9232, L_BUF = 0, L_STG = 4 * FS, REC = 1584, NREC = DYN ? 27 : 21, L_EXT = L_STG + 4 * 9216, L_MS = L_EXT + 2 * NREC * REC - (DYN ? 18432 : 0), L_STA = L_MS + 18432, L_PSC = L_STA + 512,
                   L_STB = L_PSC + 512, L_AUX = L_STB + 512, L_DUM = L_AUX + 1024, L_FA = L_DUM + 512, LDS_BYTES = 163840;
constexpr unsigned RAWM = 18432, L_GT = L_AUX + 768;
static_assert(L_EXT + 3 * RAWM <= L_MS && ((L_STG >> 10) & 3) == 0 && (L_STG & 1023) < 512 && L_FA + 1024 + 16 <= LDS_BYTES && (NWIN + GRID - 1) / GRID <= NREC, "LDS");

#define LP(T, a) ((__attribute__((address_space(3))) T*)(size_t)(unsigned)(a))
#define SB() __builtin_amdgcn_sched_barrier(0)
#define PIN(v) asm volatile("" : "+v"(v))
DI unsigned raw_lane(unsigned lane, unsigned m, unsigned A, unsigned B, unsigned C) {
    const unsigned n = 64 * m + lane, row = (n * 171) >> 11, pc = n - 12 * row;
    return (row >> 3) * (A & 0xffff) + ((row >> 1) & 3) * (B & 0xffff) + (row & 1) * (C & 0xffff) + (pc ^ ((row >> 2) & 3)) * 16 - 1024 * m;
}
DI u32x4 make_srd(unsigned lo, unsigned hi, unsigned bytes) { return u32x4{lo, hi & 0xffffu, bytes, 0x00020000u}; }
DI void raw_dma(unsigned v0, unsigned v1, unsigned v2, const u32x4& srd, unsigned so, unsigned dst) {
    asm volatile("s_mov_b32 m0, %5\n\ts_nop 0\n\tbuffer_load_dwordx4 %0, %3, %4 offen lds\n\tbuffer_load_dwordx4 %1, %3, %4 offen offset:1024 lds\n\tbuffer_load_dwordx4 %2, %3, %4 offen offset:2048 lds"
                 :: "v"(v0), "v"(v1), "v"(v2), "s"(srd), "s"(so), "s"(dst) : "memory");
}

__global__ __launch_bounds__(256, 1) void swin_kernel(const char* __restrict__ x_, const char* __restrict__ wqkv_t, const char* __restrict__ wo_t, const char* __restrict__ wqkv_s,
                                                           const char* __restrict__ wo_s, char* __restrict__ out_, const char* wf) {
    const unsigned k = __builtin_amdgcn_readfirstlane(threadIdx.x >> 6) & 3, lane = threadIdx.x & 63, k1 = k & 1, k2 = k >> 1;
    u32x2 kx, kl, kr; u32x4 ko;
    const unsigned long long kap = (unsigned long long)__builtin_amdgcn_kernarg_segment_ptr();
    asm volatile("s_load_dwordx2 %0, %4, %5\n\ts_load_dwordx2 %1, %4, %6\n\ts_load_dwordx2 %2, %4, 0x0\n\ts_load_dwordx4 %3, %4, 0x28"
                 : "=&s"(kr), "=&s"(kl), "=&s"(kx), "=&s"(ko) : "s"(kap), "s"(8 + 8 * k), "s"(8 + 16 * k2));
    SB();
    {
        unsigned long long pc; unsigned co = ((((blockIdx.x >> 3) * 4 + k) * CPL + lane / (64 / CPL)) & 511) * 128;
        asm volatile("s_getpc_b64 %0\n.Lswin_pc:\n\ts_mov_b32 m0, %2\n\tv_min_u32 %1, .Lswin_fin-.Lswin_pc, %1\n\tglobal_load_lds_dword %1, %0" : "=&s"(pc), "+v"(co) : "s"(L_DUM + 256) : "memory");
    }
    SB();
#define TS(i)
    const unsigned Bl = 1152 + 2304 * k2, Ql = 4608 - 3456 * k2;
    const unsigned Ar = 13824 - 12288 * k1, Br = k1 ? 384 : Bl, Cr = 576 - 384 * k1, Pr = 27648 - 24576 * k1, Qr = k1 ? 6144 : Ql, cr = 192 - 192 * k1, szr = 55296 - 36864 * k1;
    const unsigned ldr = L_STG + k * RAWM + k2 * RAWM, ldl = k1 * RAWM + k2 * (L_MS - k1 * (L_MS - L_EXT + RAWM));
    static_assert(L_STG + 2 * RAWM == L_EXT, "");
    unsigned vW = (((blockIdx.x >> 3) & 31) * 14 + lane) * 128 + (lane >= 14) * 0x40000000u; PIN(vW);
    unsigned vr[3];
#pragma unroll
    for (int m = 0; m < EARLY; ++m) { vr[m] = raw_lane(lane, m, Ar, Br, Cr); PIN(vr[m]); }
    SB();
    asm volatile("s_waitcnt lgkmcnt(0)" : "+s"(kr), "+s"(kl), "+s"(kx), "+s"(ko));
    const u32x4 srdr = make_srd(kr[0], kr[1], szr);
    asm volatile("s_mov_b32 m0, %2\n\ts_nop 0\n\tbuffer_load_dword %0, %1, 0 offen lds" :: "v"(vW), "s"(srdr), "s"(L_DUM) : "memory");
    SB();
#pragma unroll
    for (int m = EARLY; m < 3; ++m) { vr[m] = raw_lane(lane, m, Ar, Br, Cr); PIN(vr[m]); }
#define RDMA(n) do { SB(); raw_dma(vr[0], vr[1], vr[2], srdr, cr + (n & 1) * Pr + (n >> 1) * Qr, ldr + n * 3072); SB(); } while (0)
    RDMA(0); RDMA(1);
    TS(1);
    const u32x4 srdl = make_srd(kl[0], kl[1], 55296), srdx = make_srd(kx[0], kx[1], TOTALB), srdo = make_srd(ko[0], ko[1], TOTALB);
    const unsigned vb = (blockIdx.x & 7) * (GRID / 8) + (blockIdx.x >> 3);
    const unsigned w0 = (blockIdx.x & 7) * 640 + (blockIdx.x >> 3), nwin = 5120 + (blockIdx.x >> 3), wy0 = w0 / NWX, wx0 = w0 - wy0 * NWX, own4 = (blockIdx.x & 7) * 4;
    static_assert(WSTEP == 32, "");
    const unsigned base0 = wy0 * 7 * ROWB + wx0 * 7 * TOKB;
#define SLOT(n) do { constexpr int a_ = __builtin_popcount(SMASK & ((1 << (n)) - 1)); if ((SMASK >> (n)) & 1) { if (a_ < 4) RDMA((a_ + 2)); else LDMA((a_ - 4)); } } while (0)
    static_assert(__builtin_popcount(SMASK) == 4 + NLC, "");
    const unsigned i = lane & 15, g = lane >> 4, p = i >> 2, t = i & 3, cl = 384 * k1;
    u32x16 c0, f0; u32x8 c1, sp; u32x4 c2;
    const unsigned stg = L_STG + k * 9216, ext = L_EXT + k * 384, exv = L_EXT + NREC * REC + k * 392;
    unsigned vl[3];
#pragma unroll
    for (int m = 0; m < 3; ++m) { vl[m] = raw_lane(lane, m, 13824, Bl, 576); PIN(vl[m]); }
#define LDMA(n) do { SB(); raw_dma(vl[0], vl[1], vl[2], srdl, cl + (n & 1) * 27648 + (n >> 1) * Ql, ldl + n * 3072); SB(); } while (0)
    SLOT(0);
#pragma unroll
    for (int a = 0; a < 2; ++a) {
        const unsigned ra = (8 * p + 4 * a + t) * 192 + ((g ^ ((2 * p + a) & 3)) * 16);
        f0[a] = ldl + ra; f0[2 + a] = ldr + ra; f0[4 + a] = ldr + (8 * g + 4 * a + p) * 192 + ((t ^ ((2 * g + a) & 3)) * 16);
        PIN(f0[a]); PIN(f0[2 + a]); PIN(f0[4 + a]);
    }
    SLOT(1);
#pragma unroll
    for (int a = 0; a < 2; ++a) {
        const unsigned n = 16 * a + 4 * k + p;
        const unsigned w = (DYN && n >= 20 ? nwin - 20 * WSTEP : w0) + n * WSTEP;
        f0[6 + a] = (DYN ? w < NWIN : n < nwin) ? t * FRAMEB + w * (7 * TOKB) + ((w * 57457) >> 22) * (7 * ROWB - NWX * 7 * TOKB) + 6 * ROWB + 6 * TOKB + g * 48 : 0x80000000u; PIN(f0[6 + a]);
        if (a == 0) SLOT(2);
    }
    f0[8] = i == 0 ? L_AUX + 192 + g * 16 : L_DUM; f0[15] = 0;
    SLOT(3);
    {
        const unsigned sh = 2 - 2 * k2, ts = 3072 + 16 * k2, ml = 64 - 48 * k2, xm = 64 * (k == 1), a0 = ldl + g * 256 + (k == 1 ? t * 16 : k1 ? 0 : (i ^ ((g & k & 2) << 2)) * 16);
#pragma unroll
        for (int n = 0; n < 6; ++n) {
            const unsigned z = (32 * (n >> 1) + 4 * (n & 1) + 8 * p + t) >> sh;
            const unsigned z6 = (z * 43) >> 8, tl = z - 6 * z6;
            f0[9 + n] = a0 + (tl * ts + ((z6 * ml) ^ ((tl << 5) & xm))) * k1 + n * 3072 * (1 - k1); PIN(f0[9 + n]);
            if (n == 0) SLOT(4);
            if (n == 2) SLOT(5);
            if (n == 4) SLOT(6);
        }
    }
    SLOT(7);
#pragma unroll
    for (int m = 0; m < 3; ++m) {
        const unsigned n = 64 * m + lane, tf = (n * 171) >> 13, r = n - 48 * tf, q = (r * 43) >> 9;
        const unsigned c = tf * FRAMEB + ((r - 12 * q) ^ tf) * 16;
#pragma unroll
        for (int u = 0; u < 3; ++u) {
            const unsigned px = 16 * u + 4 * k + q;
            c0[3 * u + m] = c + px * TOKB + ((px * 37) >> 8) * (ROWB - 7 * TOKB) - 1024 * ((3 * u + m) % 4); PIN(c0[3 * u + m]);
        }
        if (m == 0) SLOT(8);
        if (m == 1) SLOT(9);
        if (m == 2) SLOT(10);
    }
#pragma unroll
    for (int S = 0; S < 3; ++S) c0[9 + S] = stg + t * 768 + p * TOKB + ((3 * g + S) ^ t) * 16;
    c0[12] = L_BUF + t * FS + g * 256 + p * 64 + k * 16;
    c0[13] = L_BUF + k * FS + lane * 16;
    const unsigned vs = g * TOKB + 12 * i, brk = ROWB - 7 * TOKB, u = (p - g) & 3;
    c0[14] = vs; c0[15] = vs + (g >= 3 ? brk : 0);
    PIN(c0);
    SLOT(11);
    c1[0] = vs + (g >= 2 ? brk : 0); c1[1] = vs + (g >= 1 ? brk : 0);
    c1[2] = u < 3 ? L_STA + t * 128 + g * 32 + u * 8 + k * 2 : L_DUM;
    c1[3] = L_STB + t * 128 + (4 * g + k) * 8 + u * 2;
    c1[4] = L_STA + k * 128 + g * 32;
    c1[5] = L_STB + k * 128 + i * 8;
    c1[6] = L_STA + k * 128 + p * 32 + g * 8 + t * 2;
    c1[7] = ext - REC + (i & 1) * 192 + g * 16;
    c2[0] = exv - REC + 24 * i;
    c2[1] = p == g ? ~0u : 0u;
    c2[2] = L_AUX + (t < 2 ? t : 2) * 192 + g * 16;
    c2[3] = p == g ? L_AUX + 576 + t * 16 : L_AUX + 384;
    PIN(c1); PIN(c2);
    SLOT(12);
    sp[0] = lane * 16; sp[1] = L_MS + (16 * t + ((4 * g + p) ^ ((t & 2) << 2))) * 16;
#pragma unroll
    for (int T = 0; T < 2; ++T) {
        const unsigned n = 16 * T + 4 * t + p;
        sp[2 + T] = n < NREC ? ext + n * REC + g * 16 : L_DUM + (i & 7) * 16; sp[4 + T] = n < NREC && p == g ? exv + n * REC + 384 : L_DUM;
    }
    sp[6] = exv + g * REC + 24 * i; sp[7] = g < (DYN ? 3 : 1) ? exv + ((DYN ? 24 : 20) + g) * REC + 24 * i : L_DUM + 24 * i;
    PIN(sp);
    SLOT(13);
    {
        const unsigned e = 64 * k + lane, y = e - 144, one = (y < 16 ? (y >> 2) & 1 : y >> 5) ? 0x3f800000u : 0x3f80u;
        *LP(unsigned, L_AUX + e * 4) = e < 48 ? 0x3f803f80u : e == 192 ? 0x46800000u : y < 48 && t == (y < 16 ? y >> 3 : (y >> 2) & 3) ? one : 0u;
        *LP(unsigned, L_STA + k * 128 + (lane & 6) * 16 + 24 + (lane & 1) * 4) = 0x3c003c00u;
        *LP(unsigned, L_FA + e * 4) = 0u;
    }
    SLOT(14);
    const u32x4 f1 = {vl[0], vl[1], vl[2], 0u}, sol = {cl + Ql, cl + Ql + 27648, cl + 2 * Ql, cl + 2 * Ql + 27648};
    const unsigned long long gl = (((unsigned long long)ko[3] << 32) | ko[2]) + (blockIdx.x >> 3) * 128;
    const u32x8 si = {(unsigned)nwin, base0, (unsigned)wx0, k * FRAMEB, stg, ldl, (unsigned)gl, (unsigned)(gl >> 32) & 0xffffu};
    asm volatile(
        "s_bitcmp1_b32 s52, 10\n"
        "s_cbranch_scc1 FV_%=\n"
        "s_mov_b32 s24, 0x3e16c740\n"
        "v_mov_b32 v50, 0x3f803f80\n"
        "v_mov_b32 v51, 0x3f803f80\n"
        "v_mov_b32 v52, 0x3f803f80\n"
        "v_mov_b32 v53, 0x3f803f80\n"
        "s_waitcnt vmcnt(15)\n"
        "ds_read_b64_tr_b16 a[72:73], v34 offset:0\n"
        "ds_read_b64_tr_b16 a[74:75], v35 offset:0\n"
        "ds_read_b64_tr_b16 a[76:77], v34 offset:6144\n"
        "ds_read_b64_tr_b16 a[78:79], v35 offset:6144\n"
        "ds_read_b64_tr_b16 a[80:81], v34 offset:12288\n"
        "ds_read_b64_tr_b16 a[82:83], v35 offset:12288\n"
        "s_add_u32 m0, s53, 15360\n"
        "s_nop 0\n"
        "buffer_load_dwordx4 v46, s[16:19], s23 offen lds\n"
        "ds_read_b64_tr_b16 a[84:85], v34 offset:8\n"
        "ds_read_b64_tr_b16 a[86:87], v35 offset:8\n"
        "ds_read_b64_tr_b16 a[88:89], v34 offset:6152\n"
        "ds_read_b64_tr_b16 a[90:91], v35 offset:6152\n"
        "ds_read_b64_tr_b16 a[92:93], v34 offset:12296\n"
        "ds_read_b64_tr_b16 a[94:95], v35 offset:12296\n"
        "buffer_load_dwordx4 v47, s[16:19], s23 offen offset:1024 lds\n"
        "ds_read_b64_tr_b16 a[96:97], v34 offset:64\n"
        "ds_read_b64_tr_b16 a[98:99], v35 offset:64\n"
        "ds_read_b64_tr_b16 a[100:101], v34 offset:6208\n"
        "ds_read_b64_tr_b16 a[102:103], v35 offset:6208\n"
        "ds_read_b64_tr_b16 a[104:105], v34 offset:12352\n"
        "ds_read_b64_tr_b16 a[106:107], v35 offset:12352\n"
        "buffer_load_dwordx4 v48, s[16:19], s23 offen offset:2048 lds\n"
        "ds_read_b64_tr_b16 a[108:109], v34 offset:72\n"
        "ds_read_b64_tr_b16 a[110:111], v35 offset:72\n"
        "ds_read_b64_tr_b16 a[112:113], v34 offset:6216\n"
        "ds_read_b64_tr_b16 a[114:115], v35 offset:6216\n"
        "ds_read_b64_tr_b16 a[116:117], v34 offset:12360\n"
        "ds_read_b64_tr_b16 a[118:119], v35 offset:12360\n"
        "ds_read_b64_tr_b16 a[120:121], v34 offset:128\n"
        "ds_read_b64_tr_b16 a[122:123], v35 offset:128\n"
        "ds_read_b64_tr_b16 a[124:125], v34 offset:6272\n"
        "ds_read_b64_tr_b16 a[126:127], v35 offset:6272\n"
        "ds_read_b64_tr_b16 a[128:129], v34 offset:12416\n"
        "ds_read_b64_tr_b16 a[130:131], v35 offset:12416\n"
        "ds_read_b64_tr_b16 a[132:133], v34 offset:136\n"
        "ds_read_b64_tr_b16 a[134:135], v35 offset:136\n"
        "ds_read_b64_tr_b16 a[136:137], v34 offset:6280\n"
        "ds_read_b64_tr_b16 a[138:139], v35 offset:6280\n"
        "ds_read_b64_tr_b16 a[140:141], v34 offset:12424\n"
        "ds_read_b64_tr_b16 a[142:143], v35 offset:12424\n"
        "s_waitcnt lgkmcnt(15)\n"
        "v_mfma_f32_16x16x32_bf16 v[46:49], a[72:75], v[50:53], 0\n"
        "s_waitcnt vmcnt(12)\n"
        "s_waitcnt vmcnt(6)\n"
        "v_mfma_f32_16x16x32_bf16 v[46:49], a[76:79], v[50:53], v[46:49]\n"
        "v_mfma_f32_16x16x32_bf16 v[46:49], a[80:83], v[50:53], v[46:49]\n"
        "s_waitcnt lgkmcnt(15)\n"
        "s_nop 0\n"
        "v_mfma_f32_16x16x32_bf16 v[54:57], a[92:95], v[50:53], 0\n"
        "s_nop 2\n"
        "v_mfma_f32_16x16x32_bf16 v[54:57], a[88:91], v[50:53], v[54:57]\n"
        "s_waitcnt lgkmcnt(15)\n"
        "v_mul_f32 v46, 0xbc2aaaab, v46\n"
        "v_mul_f32 v47, 0xbc2aaaab, v47\n"
        "v_mul_f32 v48, 0xbc2aaaab, v48\n"
        "v_mfma_f32_16x16x32_bf16 v[54:57], a[84:87], v[50:53], v[54:57]\n"
        "s_waitcnt vmcnt(0)\n"
        "v_mul_f32 v49, 0xbc2aaaab, v49\n"
        "v_cvt_pk_bf16_f32 v70, v46, v47\n"
        "v_cvt_pk_bf16_f32 v71, v48, v49\n"
        "v_mfma_f32_16x16x32_bf16 v[46:49], a[96:99], v[50:53], 0\n"
        "s_waitcnt lgkmcnt(12)\n"
        "ds_read_b128 a[0:3], v32\n"
        "v_mfma_f32_16x16x32_bf16 v[46:49], a[100:103], v[50:53], v[46:49]\n"
        "s_waitcnt lgkmcnt(7)\n"
        "ds_read_b128 a[4:7], v32 offset:64\n"
        "v_mul_f32 v54, 0xbc2aaaab, v54\n"
        "v_mul_f32 v55, 0xbc2aaaab, v55\n"
        "v_mfma_f32_16x16x32_bf16 v[46:49], a[104:107], v[50:53], v[46:49]\n"
        "s_waitcnt lgkmcnt(2)\n"
        "ds_read_b128 a[8:11], v32 offset:128\n"
        "v_mul_f32 v56, 0xbc2aaaab, v56\n"
        "v_mul_f32 v57, 0xbc2aaaab, v57\n"
        "v_mfma_f32_16x16x32_bf16 v[58:61], a[116:119], v[50:53], 0\n"
        "ds_read_b128 a[144:147], v30\n"
        "v_cvt_pk_bf16_f32 v72, v54, v55\n"
        "v_cvt_pk_bf16_f32 v73, v56, v57\n"
        "v_mfma_f32_16x16x32_bf16 v[58:61], a[112:115], v[50:53], v[58:61]\n"
        "ds_read_b128 a[148:151], v30 offset:64\n"
        "v_mul_f32 v46, 0xbc2aaaab, v46\n"
        "v_mul_f32 v47, 0xbc2aaaab, v47\n"
        "v_mfma_f32_16x16x32_bf16 v[58:61], a[108:111], v[50:53], v[58:61]\n"
        "ds_read_b128 a[152:155], v30 offset:128\n"
        "v_mul_f32 v48, 0xbc2aaaab, v48\n"
        "v_mul_f32 v49, 0xbc2aaaab, v49\n"
        "v_mfma_f32_16x16x32_bf16 v[54:57], a[120:123], v[50:53], 0\n"
        "ds_read_b128 a[156:159], v31\n"
        "v_cvt_pk_bf16_f32 v74, v46, v47\n"
        "v_cvt_pk_bf16_f32 v75, v48, v49\n"
        "v_mfma_f32_16x16x32_bf16 v[54:57], a[124:127], v[50:53], v[54:57]\n"
        "ds_read_b128 a[160:163], v31 offset:64\n"
        "v_mul_f32 v58, 0xbc2aaaab, v58\n"
        "v_mul_f32 v59, 0xbc2aaaab, v59\n"
        "v_mfma_f32_16x16x32_bf16 v[54:57], a[128:131], v[50:53], v[54:57]\n"
        "ds_read_b128 a[164:167], v31 offset:128\n"
        "v_mul_f32 v60, 0xbc2aaaab, v60\n"
        "v_mul_f32 v61, 0xbc2aaaab, v61\n"
        "v_mfma_f32_16x16x32_bf16 v[62:65], a[140:143], v[50:53], 0\n"
        "ds_read_b128 a[168:171], v30 offset:6144\n"
        "v_cvt_pk_bf16_f32 v76, v58, v59\n"
        "v_cvt_pk_bf16_f32 v77, v60, v61\n"
        "v_mfma_f32_16x16x32_bf16 v[62:65], a[136:139], v[50:53], v[62:65]\n"
        "ds_read_b128 a[172:175], v30 offset:6208\n"
        "v_mul_f32 v54, 0xbc2aaaab, v54\n"
        "v_mul_f32 v55, 0xbc2aaaab, v55\n"
        "v_mfma_f32_16x16x32_bf16 v[62:65], a[132:135], v[50:53], v[62:65]\n"
        "s_waitcnt lgkmcnt(8)\n"
        "ds_read_b128 a[176:179], v30 offset:6272\n"
        "v_mul_f32 v56, 0xbc2aaaab, v56\n"
        "v_mul_f32 v57, 0xbc2aaaab, v57\n"
        "s_waitcnt lgkmcnt(6)\n"
        "v_mfma_f32_16x16x32_bf16 v[46:49], a[144:147], v[70:73], 0\n"
        "ds_read_b128 a[180:183], v31 offset:6144\n"
        "v_cvt_pk_bf16_f32 v78, v54, v55\n"
        "v_cvt_pk_bf16_f32 v79, v56, v57\n"
        "v_mfma_f32_16x16x32_bf16 v[46:49], a[148:151], v[74:77], v[46:49]\n"
        "ds_read_b128 a[184:187], v31 offset:6208\n"
        "v_mul_f32 v62, 0xbc2aaaab, v62\n"
        "v_mul_f32 v63, 0xbc2aaaab, v63\n"
        "v_cvt_pk_bf16_f32 v80, v62, v63\n"
        "v_mul_f32 v64, 0xbc2aaaab, v64\n"
        "v_mul_f32 v65, 0xbc2aaaab, v65\n"
        "v_cvt_pk_bf16_f32 v81, v64, v65\n"
        "s_nop 1\n"
        "v_mfma_f32_16x16x32_bf16 v[46:49], a[152:155], v[78:81], v[46:49]\n"
        "s_waitcnt lgkmcnt(5)\n"
        "ds_read_b128 a[188:191], v31 offset:6272\n"
        "v_mfma_f32_16x16x32_bf16 v[54:57], a[152:155], a[8:11], v[46:49]\n"
        "buffer_load_dwordx4 v[164:167], v36, s[36:39], 0 offen\n"
        "v_mfma_f32_16x16x32_bf16 v[54:57], a[148:151], a[4:7], v[54:57]\n"
        "ds_read_b128 a[192:195], v30 offset:12288\n"
        "ds_read_b128 a[196:199], v30 offset:12352\n"
        "v_mfma_f32_16x16x32_bf16 v[54:57], a[144:147], a[0:3], v[54:57]\n"
        "s_waitcnt lgkmcnt(5)\n"
        "ds_read_b128 a[200:203], v30 offset:12416\n"
        "v_mfma_f32_16x16x32_bf16 v[50:53], a[156:159], v[70:73], 0\n"
        "s_nop 0\n"
        "ds_read_b128 a[204:207], v31 offset:12288\n"
        "v_mfma_f32_16x16x32_bf16 v[50:53], a[160:163], v[74:77], v[50:53]\n"
        "s_nop 0\n"
        "ds_read_b128 a[208:211], v31 offset:12352\n"
        "v_add_f32 v255, v54, v55\n"
        "v_add_f32 v255, v255, v56\n"
        "v_mfma_f32_16x16x32_bf16 v[50:53], a[164:167], v[78:81], v[50:53]\n"
        "ds_read_b128 a[212:215], v31 offset:12416\n"
        "v_add_f32 v255, v255, v57\n"
        "v_mfma_f32_16x16x32_bf16 v[82:85], a[164:167], a[8:11], v[50:53]\n"
        "ds_read_b128 a[12:15], v33\n"
        "v_mfma_f32_16x16x32_bf16 v[82:85], a[160:163], a[4:7], v[82:85]\n"
        "ds_read_b128 a[16:19], v33 offset:64\n"
        "v_mfma_f32_16x16x32_bf16 v[82:85], a[156:159], a[0:3], v[82:85]\n"
        "ds_read_b128 a[20:23], v33 offset:128\n"
        "v_mfma_f32_16x16x32_bf16 v[58:61], a[168:171], v[70:73], 0\n"
        "s_waitcnt lgkmcnt(9)\n"
        "buffer_load_dwordx4 v[170:173], v36, s[36:39], 0 offen offset:16\n"
        "v_mfma_f32_16x16x32_bf16 v[58:61], a[172:175], v[74:77], v[58:61]\n"
        "s_mov_b32 s78, 0xf0000f\n"
        "ds_read_b128 a[24:27], v32 offset:6144\n"
        "ds_read_b128 a[28:31], v32 offset:6208\n"
        "v_add_f32 v255, v255, v82\n"
        "v_mfma_f32_16x16x32_bf16 v[58:61], a[176:179], v[78:81], v[58:61]\n"
        "s_mov_b32 s79, 0xf0000f00\n"
        "ds_read_b128 a[32:35], v32 offset:6272\n"
        "v_add_f32 v255, v255, v83\n"
        "v_add_f32 v255, v255, v84\n"
        "v_mfma_f32_16x16x32_bf16 v[86:89], a[176:179], a[8:11], v[58:61]\n"
        "s_waitcnt lgkmcnt(9)\n"
        "ds_read_b128 a[36:39], v33 offset:6144\n"
        "v_add_f32 v255, v255, v85\n"
        "v_mfma_f32_16x16x32_bf16 v[86:89], a[172:175], a[4:7], v[86:89]\n"
        "s_mov_b32 s76, 0x11111111\n"
        "ds_read_b128 a[40:43], v33 offset:6208\n"
        "v_mfma_f32_16x16x32_bf16 v[86:89], a[168:171], a[0:3], v[86:89]\n"
        "s_mov_b32 s77, 0x11111111\n"
        "ds_read_b128 a[44:47], v33 offset:6272\n"
        "v_mfma_f32_16x16x32_bf16 v[66:69], a[180:183], v[70:73], 0\n"
        "s_waitcnt lgkmcnt(9)\n"
        "ds_read_b128 a[48:51], v32 offset:12288\n"
        "v_mfma_f32_16x16x32_bf16 v[66:69], a[184:187], v[74:77], v[66:69]\n"
        "s_mov_b32 s56, 0x22222222\n"
        "ds_read_b128 a[52:55], v32 offset:12352\n"
        "v_add_f32 v255, v255, v86\n"
        "v_add_f32 v255, v255, v87\n"
        "v_mfma_f32_16x16x32_bf16 v[66:69], a[188:191], v[78:81], v[66:69]\n"
        "s_mov_b32 s57, 0x22222222\n"
        "ds_read_b128 a[56:59], v32 offset:12416\n"
        "v_add_f32 v255, v255, v88\n"
        "v_add_f32 v255, v255, v89\n"
        "v_mfma_f32_16x16x32_bf16 v[90:93], a[188:191], a[8:11], v[66:69]\n"
        "s_mov_b32 s58, 0x44444444\n"
        "buffer_load_dwordx4 v[176:179], v36, s[36:39], 0 offen offset:32\n"
        "v_mfma_f32_16x16x32_bf16 v[90:93], a[184:187], a[4:7], v[90:93]\n"
        "s_waitcnt lgkmcnt(9)\n"
        "ds_read_b128 a[60:63], v33 offset:12288\n"
        "ds_read_b128 a[64:67], v33 offset:12352\n"
        "v_mfma_f32_16x16x32_bf16 v[90:93], a[180:183], a[0:3], v[90:93]\n"
        "s_mov_b32 s59, 0x44444444\n"
        "ds_read_b128 a[68:71], v33 offset:12416\n"
        "v_mfma_f32_16x16x32_bf16 v[62:65], a[192:195], v[70:73], 0\n"
        "s_mov_b32 s60, 0x88888888\n"
        "s_nop 1\n"
        "v_mfma_f32_16x16x32_bf16 v[62:65], a[196:199], v[74:77], v[62:65]\n"
        "s_mov_b32 s61, 0x88888888\n"
        "v_add_f32 v255, v255, v90\n"
        "v_add_f32 v255, v255, v91\n"
        "v_add_f32 v255, v255, v92\n"
        "v_mfma_f32_16x16x32_bf16 v[62:65], a[200:203], v[78:81], v[62:65]\n"
        "s_mov_b32 s62, 0xffff\n"
        "v_add_f32 v255, v255, v93\n"
        "v_mfma_f32_16x16x32_bf16 v[94:97], a[200:203], a[8:11], v[62:65]\n"
        "s_mov_b32 s63, 0x0\n"
        "v_mfma_f32_16x16x32_bf16 v[94:97], a[196:199], a[4:7], v[94:97]\n"
        "s_waitcnt lgkmcnt(9)\n"
        "v_mfma_f32_16x16x32_bf16 v[94:97], a[192:195], a[0:3], v[94:97]\n"
        "s_mov_b32 s64, 0xffff0000\n"
        "buffer_load_dwordx4 v[184:187], v37, s[36:39], 0 offen\n"
        "v_mfma_f32_16x16x32_bf16 v[30:33], a[204:207], v[70:73], 0\n"
        "s_mov_b32 s65, 0x0\n"
        "s_nop 1\n"
        "v_mfma_f32_16x16x32_bf16 v[30:33], a[208:211], v[74:77], v[30:33]\n"
        "s_mov_b32 s66, 0x0\n"
        "v_add_f32 v255, v255, v94\n"
        "v_add_f32 v255, v255, v95\n"
        "v_add_f32 v255, v255, v96\n"
        "v_mfma_f32_16x16x32_bf16 v[30:33], a[212:215], v[78:81], v[30:33]\n"
        "s_mov_b32 s67, 0xffff\n"
        "v_add_f32 v255, v255, v97\n"
        "v_mfma_f32_16x16x32_bf16 v[70:73], a[212:215], a[8:11], v[30:33]\n"
        "s_waitcnt lgkmcnt(0)\n"
        "v_mfma_f32_16x16x32_bf16 v[70:73], a[208:211], a[4:7], v[70:73]\n"
        "s_mov_b32 s68, 0x0\n"
        "v_mfma_f32_16x16x32_bf16 v[70:73], a[204:207], a[0:3], v[70:73]\n"
        "s_mov_b32 s69, 0xffff0000\n"
        "s_nop 0\n"
        "v_mfma_f32_16x16x32_bf16 v[98:101], a[144:147], a[12:15], v[46:49]\n"
        "s_mov_b32 s70, 0x0\n"
        "s_nop 1\n"
        "v_mfma_f32_16x16x32_bf16 v[98:101], a[148:151], a[16:19], v[98:101]\n"
        "s_mov_b32 s71, 0xffffffff\n"
        "v_add_f32 v255, v255, v70\n"
        "v_add_f32 v255, v255, v71\n"
        "v_add_f32 v255, v255, v72\n"
        "v_mfma_f32_16x16x32_bf16 v[98:101], a[152:155], a[20:23], v[98:101]\n"
        "s_barrier\n"
        "buffer_load_dwordx4 v[190:193], v37, s[36:39], 0 offen offset:16\n"
        "v_add_f32 v255, v255, v73\n"
        "v_mov_b32 v253, v255\n"
        "v_mfma_f32_16x16x32_bf16 v[106:109], a[164:167], a[20:23], v[50:53]\n"
        "s_mov_b32 s72, 0x0\n"
        "v_permlane32_swap_b32 v255, v253\n"
        "v_add_f32 v255, v255, v253\n"
        "v_mfma_f32_16x16x32_bf16 v[106:109], a[160:163], a[16:19], v[106:109]\n"
        "s_mov_b32 s73, 0x20000\n"
        "v_mov_b32 v253, v255\n"
        "v_add_f32 v254, v98, v99\n"
        "v_add_f32 v254, v254, v100\n"
        "v_mfma_f32_16x16x32_bf16 v[106:109], a[156:159], a[12:15], v[106:109]\n"
        "s_mov_b64 vcc, s[78:79]\n"
        "v_permlane16_swap_b32 v255, v253\n"
        "v_add_f32 v255, v255, v253\n"
        "v_mfma_f32_16x16x32_bf16 v[110:113], a[168:171], a[12:15], v[58:61]\n"
        "s_mov_b32 s80, 0x3c2aaaab\n"
        "v_mul_f32 v255, 0xbac909aa, v255\n"
        "v_add_f32 v254, v254, v101\n"
        "v_fma_f32 v54, v54, s24, v255\n"
        "v_mfma_f32_16x16x32_bf16 v[110:113], a[172:175], a[16:19], v[110:113]\n"
        "s_mov_b32 s81, 0x3727c5ac\n"
        "v_fma_f32 v55, v55, s24, v255\n"
        "v_fma_f32 v56, v56, s24, v255\n"
        "v_fma_f32 v57, v57, s24, v255\n"
        "v_mfma_f32_16x16x32_bf16 v[110:113], a[176:179], a[20:23], v[110:113]\n"
        "s_mov_b32 s82, 0x43800000\n"
        "v_fma_f32 v82, v82, s24, v255\n"
        "v_fma_f32 v83, v83, s24, v255\n"
        "v_fma_f32 v84, v84, s24, v255\n"
        "v_mfma_f32_16x16x32_bf16 v[102:105], a[188:191], a[20:23], v[66:69]\n"
        "s_mov_b32 s84, 0x80000000\n"
        "v_fma_f32 v85, v85, s24, v255\n"
        "v_fma_f32 v86, v86, s24, v255\n"
        "v_fma_f32 v87, v87, s24, v255\n"
        "v_mfma_f32_16x16x32_bf16 v[102:105], a[184:187], a[16:19], v[102:105]\n"
        "s_mov_b32 s85, 19\n"
        "v_fma_f32 v88, v88, s24, v255\n"
        "v_fma_f32 v89, v89, s24, v255\n"
        "v_fma_f32 v90, v90, s24, v255\n"
        "v_mfma_f32_16x16x32_bf16 v[102:105], a[180:183], a[12:15], v[102:105]\n"
        "s_mov_b32 s86, 0x80000000\n"
        "buffer_load_dwordx4 v[196:199], v37, s[36:39], 0 offen offset:32\n"
        "v_fma_f32 v91, v91, s24, v255\n"
        "v_fma_f32 v92, v92, s24, v255\n"
        "v_mfma_f32_16x16x32_bf16 v[118:121], a[192:195], a[12:15], v[62:65]\n"
        "s_mov_b32 s89, 0x80000000\n"
        "v_fma_f32 v93, v93, s24, v255\n"
        "v_fma_f32 v94, v94, s24, v255\n"
        "v_fma_f32 v95, v95, s24, v255\n"
        "v_mfma_f32_16x16x32_bf16 v[118:121], a[196:199], a[16:19], v[118:121]\n"
        "s_mov_b32 s29, 1\n"
        "v_fma_f32 v96, v96, s24, v255\n"
        "v_fma_f32 v97, v97, s24, v255\n"
        "v_fma_f32 v70, v70, s24, v255\n"
        "v_mfma_f32_16x16x32_bf16 v[118:121], a[200:203], a[20:23], v[118:121]\n"
        "s_mov_b32 s31, 0x400000\n"
        "v_fma_f32 v71, v71, s24, v255\n"
        "v_fma_f32 v72, v72, s24, v255\n"
        "v_fma_f32 v73, v73, s24, v255\n"
        "v_mfma_f32_16x16x32_bf16 v[122:125], a[212:215], a[20:23], v[30:33]\n"
        "s_mov_b32 s87, s49\n"
        "v_cvt_pk_bf16_f32 v34, v54, v55\n"
        "v_cvt_pk_bf16_f32 v35, v56, v57\n"
        "v_cvt_pk_bf16_f32 v36, v82, v83\n"
        "v_mfma_f32_16x16x32_bf16 v[122:125], a[208:211], a[16:19], v[122:125]\n"
        "s_mov_b32 s88, s50\n"
        "v_cvt_pk_bf16_f32 v37, v84, v85\n"
        "v_cvt_pk_bf16_f32 v54, v86, v87\n"
        "v_cvt_pk_bf16_f32 v55, v88, v89\n"
        "v_mfma_f32_16x16x32_bf16 v[122:125], a[204:207], a[12:15], v[122:125]\n"
        "s_mov_b64 s[44:45], s[40:41]\n"
        "ds_write_b128 v39, v[34:37]\n"
        "v_cvt_pk_bf16_f32 v56, v90, v91\n"
        "v_mfma_f32_16x16x32_bf16 v[34:37], a[144:147], a[24:27], v[46:49]\n"
        "s_mov_b32 s46, 0\n"
        "v_cvt_pk_bf16_f32 v57, v92, v93\n"
        "v_cvt_pk_bf16_f32 v74, v94, v95\n"
        "v_cvt_pk_bf16_f32 v75, v96, v97\n"
        "v_mfma_f32_16x16x32_bf16 v[34:37], a[148:151], a[28:31], v[34:37]\n"
        "s_add_u32 m0, s52, 0\n"
        "ds_write_b128 v39, v[54:57] offset:1024\n"
        "v_cvt_pk_bf16_f32 v76, v70, v71\n"
        "v_mfma_f32_16x16x32_bf16 v[34:37], a[152:155], a[32:35], v[34:37]\n"
        "s_mov_b32 s47, s43\n"
        "buffer_load_dwordx4 v0, s[36:39], s49 offen lds\n"
        "v_cvt_pk_bf16_f32 v77, v72, v73\n"
        "v_add_f32 v254, v254, v106\n"
        "v_mfma_f32_16x16x32_bf16 v[54:57], a[164:167], a[32:35], v[50:53]\n"
        "s_waitcnt lgkmcnt(2)\n"
        "ds_write_b128 v39, v[74:77] offset:2048\n"
        "v_add_f32 v254, v254, v107\n"
        "v_mfma_f32_16x16x32_bf16 v[54:57], a[160:163], a[28:31], v[54:57]\n"
        "s_waitcnt lgkmcnt(3)\n"
        "v_add_f32 v254, v254, v108\n"
        "v_add_f32 v254, v254, v109\n"
        "v_add_f32 v254, v254, v110\n"
        "v_mfma_f32_16x16x32_bf16 v[54:57], a[156:159], a[24:27], v[54:57]\n"
        "s_waitcnt lgkmcnt(3)\n"
        "v_add_f32 v254, v254, v111\n"
        "v_add_f32 v254, v254, v112\n"
        "v_add_f32 v254, v254, v113\n"
        "v_mfma_f32_16x16x32_bf16 v[70:73], a[168:171], a[24:27], v[58:61]\n"
        "v_add_f32 v254, v254, v102\n"
        "v_add_f32 v254, v254, v103\n"
        "v_add_f32 v254, v254, v104\n"
        "v_mfma_f32_16x16x32_bf16 v[70:73], a[172:175], a[28:31], v[70:73]\n"
        "v_add_f32 v254, v254, v105\n"
        "v_add_f32 v254, v254, v118\n"
        "v_add_f32 v254, v254, v119\n"
        "v_mfma_f32_16x16x32_bf16 v[70:73], a[176:179], a[32:35], v[70:73]\n"
        "buffer_load_dwordx4 v1, s[36:39], s49 offen offset:1024 lds\n"
        "v_add_f32 v254, v254, v120\n"
        "v_add_f32 v254, v254, v121\n"
        "v_mfma_f32_16x16x32_bf16 v[114:117], a[188:191], a[32:35], v[66:69]\n"
        "v_add_f32 v254, v254, v122\n"
        "v_add_f32 v254, v254, v123\n"
        "v_add_f32 v254, v254, v124\n"
        "v_mfma_f32_16x16x32_bf16 v[114:117], a[184:187], a[28:31], v[114:117]\n"
        "v_add_f32 v254, v254, v125\n"
        "v_mov_b32 v253, v254\n"
        "v_add_f32 v255, v34, v35\n"
        "v_mfma_f32_16x16x32_bf16 v[114:117], a[180:183], a[24:27], v[114:117]\n"
        "v_permlane32_swap_b32 v254, v253\n"
        "v_add_f32 v254, v254, v253\n"
        "v_mfma_f32_16x16x32_bf16 v[126:129], a[192:195], a[24:27], v[62:65]\n"
        "v_mov_b32 v253, v254\n"
        "v_add_f32 v255, v255, v36\n"
        "v_add_f32 v255, v255, v37\n"
        "v_mfma_f32_16x16x32_bf16 v[126:129], a[196:199], a[28:31], v[126:129]\n"
        "v_permlane16_swap_b32 v254, v253\n"
        "v_add_f32 v254, v254, v253\n"
        "v_mfma_f32_16x16x32_bf16 v[126:129], a[200:203], a[32:35], v[126:129]\n"
        "buffer_load_dwordx4 v2, s[36:39], s49 offen offset:2048 lds\n"
        "v_mul_f32 v254, 0xbac909aa, v254\n"
        "v_fma_f32 v98, v98, s24, v254\n"
        "v_mfma_f32_16x16x32_bf16 v[130:133], a[212:215], a[32:35], v[30:33]\n"
        "v_fma_f32 v99, v99, s24, v254\n"
        "v_fma_f32 v100, v100, s24, v254\n"
        "v_fma_f32 v101, v101, s24, v254\n"
        "v_mfma_f32_16x16x32_bf16 v[130:133], a[208:211], a[28:31], v[130:133]\n"
        "v_fma_f32 v106, v106, s24, v254\n"
        "v_fma_f32 v107, v107, s24, v254\n"
        "v_fma_f32 v108, v108, s24, v254\n"
        "v_mfma_f32_16x16x32_bf16 v[130:133], a[204:207], a[24:27], v[130:133]\n"
        "v_fma_f32 v109, v109, s24, v254\n"
        "v_fma_f32 v110, v110, s24, v254\n"
        "v_fma_f32 v111, v111, s24, v254\n"
        "v_mfma_f32_16x16x32_bf16 v[74:77], a[144:147], a[36:39], v[46:49]\n"
        "v_fma_f32 v112, v112, s24, v254\n"
        "v_fma_f32 v113, v113, s24, v254\n"
        "v_fma_f32 v102, v102, s24, v254\n"
        "v_mfma_f32_16x16x32_bf16 v[74:77], a[148:151], a[40:43], v[74:77]\n"
        "v_fma_f32 v103, v103, s24, v254\n"
        "v_fma_f32 v104, v104, s24, v254\n"
        "v_fma_f32 v105, v105, s24, v254\n"
        "v_mfma_f32_16x16x32_bf16 v[74:77], a[152:155], a[44:47], v[74:77]\n"
        "buffer_load_dwordx4 v3, s[36:39], s49 offen offset:3072 lds\n"
        "v_fma_f32 v118, v118, s24, v254\n"
        "v_fma_f32 v119, v119, s24, v254\n"
        "v_mfma_f32_16x16x32_bf16 v[78:81], a[164:167], a[44:47], v[50:53]\n"
        "v_fma_f32 v120, v120, s24, v254\n"
        "v_fma_f32 v121, v121, s24, v254\n"
        "v_fma_f32 v122, v122, s24, v254\n"
        "v_mfma_f32_16x16x32_bf16 v[78:81], a[160:163], a[40:43], v[78:81]\n"
        "v_fma_f32 v123, v123, s24, v254\n"
        "v_fma_f32 v124, v124, s24, v254\n"
        "v_fma_f32 v125, v125, s24, v254\n"
        "v_mfma_f32_16x16x32_bf16 v[78:81], a[156:159], a[36:39], v[78:81]\n"
        "v_cvt_pk_bf16_f32 v84, v98, v99\n"
        "v_cvt_pk_bf16_f32 v85, v100, v101\n"
        "v_cvt_pk_bf16_f32 v86, v106, v107\n"
        "v_mfma_f32_16x16x32_bf16 v[90:93], a[168:171], a[36:39], v[58:61]\n"
        "v_cvt_pk_bf16_f32 v87, v108, v109\n"
        "v_cvt_pk_bf16_f32 v82, v110, v111\n"
        "v_cvt_pk_bf16_f32 v83, v112, v113\n"
        "v_mfma_f32_16x16x32_bf16 v[90:93], a[172:175], a[40:43], v[90:93]\n"
        "s_add_u32 m0, s52, 4096\n"
        "ds_write_b128 v40, v[84:87]\n"
        "v_cvt_pk_bf16_f32 v84, v102, v103\n"
        "v_mfma_f32_16x16x32_bf16 v[90:93], a[176:179], a[44:47], v[90:93]\n"
        "buffer_load_dwordx4 v4, s[36:39], s49 offen lds\n"
        "v_cvt_pk_bf16_f32 v85, v104, v105\n"
        "v_cvt_pk_bf16_f32 v94, v118, v119\n"
        "v_mfma_f32_16x16x32_bf16 v[86:89], a[188:191], a[44:47], v[66:69]\n"
        "ds_write_b128 v40, v[82:85] offset:1024\n"
        "v_cvt_pk_bf16_f32 v95, v120, v121\n"
        "v_mfma_f32_16x16x32_bf16 v[86:89], a[184:187], a[40:43], v[86:89]\n"
        "v_cvt_pk_bf16_f32 v96, v122, v123\n"
        "v_cvt_pk_bf16_f32 v97, v124, v125\n"
        "v_add_f32 v255, v255, v54\n"
        "v_mfma_f32_16x16x32_bf16 v[86:89], a[180:183], a[36:39], v[86:89]\n"
        "ds_write_b128 v40, v[94:97] offset:2048\n"
        "v_add_f32 v255, v255, v55\n"
        "v_mfma_f32_16x16x32_bf16 v[94:97], a[192:195], a[36:39], v[62:65]\n"
        "v_add_f32 v255, v255, v56\n"
        "v_add_f32 v255, v255, v57\n"
        "v_add_f32 v255, v255, v70\n"
        "v_mfma_f32_16x16x32_bf16 v[94:97], a[196:199], a[40:43], v[94:97]\n"
        "v_add_f32 v255, v255, v71\n"
        "v_add_f32 v255, v255, v72\n"
        "v_add_f32 v255, v255, v73\n"
        "v_mfma_f32_16x16x32_bf16 v[94:97], a[200:203], a[44:47], v[94:97]\n"
        "buffer_load_dwordx4 v5, s[36:39], s49 offen offset:1024 lds\n"
        "v_add_f32 v255, v255, v114\n"
        "v_add_f32 v255, v255, v115\n"
        "v_mfma_f32_16x16x32_bf16 v[82:85], a[212:215], a[44:47], v[30:33]\n"
        "v_add_f32 v255, v255, v116\n"
        "v_add_f32 v255, v255, v117\n"
        "v_add_f32 v255, v255, v126\n"
        "v_mfma_f32_16x16x32_bf16 v[82:85], a[208:211], a[40:43], v[82:85]\n"
        "v_add_f32 v255, v255, v127\n"
        "v_add_f32 v255, v255, v128\n"
        "v_add_f32 v255, v255, v129\n"
        "v_mfma_f32_16x16x32_bf16 v[82:85], a[204:207], a[36:39], v[82:85]\n"
        "v_add_f32 v255, v255, v130\n"
        "v_add_f32 v255, v255, v131\n"
        "v_add_f32 v255, v255, v132\n"
        "v_mfma_f32_16x16x32_bf16 v[102:105], a[144:147], a[48:51], v[46:49]\n"
        "v_add_f32 v255, v255, v133\n"
        "v_mov_b32 v254, v255\n"
        "v_add_f32 v134, v74, v75\n"
        "v_mfma_f32_16x16x32_bf16 v[102:105], a[148:151], a[52:55], v[102:105]\n"
        "v_permlane32_swap_b32 v255, v254\n"
        "v_add_f32 v255, v255, v254\n"
        "v_mfma_f32_16x16x32_bf16 v[102:105], a[152:155], a[56:59], v[102:105]\n"
        "buffer_load_dwordx4 v6, s[36:39], s49 offen offset:2048 lds\n"
        "v_mov_b32 v254, v255\n"
        "v_add_f32 v134, v134, v76\n"
        "v_mfma_f32_16x16x32_bf16 v[106:109], a[164:167], a[56:59], v[50:53]\n"
        "v_permlane16_swap_b32 v255, v254\n"
        "v_add_f32 v255, v255, v254\n"
        "v_mfma_f32_16x16x32_bf16 v[106:109], a[160:163], a[52:55], v[106:109]\n"
        "v_mul_f32 v255, 0xbac909aa, v255\n"
        "v_add_f32 v134, v134, v77\n"
        "v_fma_f32 v34, v34, s24, v255\n"
        "v_mfma_f32_16x16x32_bf16 v[106:109], a[156:159], a[48:51], v[106:109]\n"
        "v_fma_f32 v35, v35, s24, v255\n"
        "v_fma_f32 v36, v36, s24, v255\n"
        "v_fma_f32 v37, v37, s24, v255\n"
        "v_mfma_f32_16x16x32_bf16 v[98:101], a[168:171], a[48:51], v[58:61]\n"
        "v_fma_f32 v54, v54, s24, v255\n"
        "v_fma_f32 v55, v55, s24, v255\n"
        "v_fma_f32 v56, v56, s24, v255\n"
        "v_mfma_f32_16x16x32_bf16 v[98:101], a[172:175], a[52:55], v[98:101]\n"
        "v_fma_f32 v57, v57, s24, v255\n"
        "v_fma_f32 v70, v70, s24, v255\n"
        "v_fma_f32 v71, v71, s24, v255\n"
        "v_mfma_f32_16x16x32_bf16 v[98:101], a[176:179], a[56:59], v[98:101]\n"
        "buffer_load_dwordx4 v7, s[36:39], s49 offen offset:3072 lds\n"
        "v_fma_f32 v72, v72, s24, v255\n"
        "v_fma_f32 v73, v73, s24, v255\n"
        "v_mfma_f32_16x16x32_bf16 v[110:113], a[188:191], a[56:59], v[66:69]\n"
        "v_fma_f32 v114, v114, s24, v255\n"
        "v_fma_f32 v115, v115, s24, v255\n"
        "v_fma_f32 v116, v116, s24, v255\n"
        "v_mfma_f32_16x16x32_bf16 v[110:113], a[184:187], a[52:55], v[110:113]\n"
        "v_fma_f32 v117, v117, s24, v255\n"
        "v_fma_f32 v126, v126, s24, v255\n"
        "v_fma_f32 v127, v127, s24, v255\n"
        "v_mfma_f32_16x16x32_bf16 v[110:113], a[180:183], a[48:51], v[110:113]\n"
        "v_fma_f32 v128, v128, s24, v255\n"
        "v_fma_f32 v129, v129, s24, v255\n"
        "v_fma_f32 v130, v130, s24, v255\n"
        "v_mfma_f32_16x16x32_bf16 v[118:121], a[192:195], a[48:51], v[62:65]\n"
        "v_fma_f32 v131, v131, s24, v255\n"
        "v_fma_f32 v132, v132, s24, v255\n"
        "v_fma_f32 v133, v133, s24, v255\n"
        "v_mfma_f32_16x16x32_bf16 v[118:121], a[196:199], a[52:55], v[118:121]\n"
        "s_add_u32 m0, s52, 8192\n"
        "v_cvt_pk_bf16_f32 v122, v34, v35\n"
        "v_cvt_pk_bf16_f32 v123, v36, v37\n"
        "v_cvt_pk_bf16_f32 v124, v54, v55\n"
        "v_mfma_f32_16x16x32_bf16 v[118:121], a[200:203], a[56:59], v[118:121]\n"
        "buffer_load_dwordx4 v8, s[36:39], s49 offen lds\n"
        "v_cvt_pk_bf16_f32 v125, v56, v57\n"
        "v_cvt_pk_bf16_f32 v34, v70, v71\n"
        "v_mfma_f32_16x16x32_bf16 v[54:57], a[212:215], a[56:59], v[30:33]\n"
        "ds_write_b128 v41, v[122:125]\n"
        "v_cvt_pk_bf16_f32 v35, v72, v73\n"
        "v_mfma_f32_16x16x32_bf16 v[54:57], a[208:211], a[52:55], v[54:57]\n"
        "v_cvt_pk_bf16_f32 v36, v114, v115\n"
        "v_cvt_pk_bf16_f32 v37, v116, v117\n"
        "v_cvt_pk_bf16_f32 v70, v126, v127\n"
        "v_mfma_f32_16x16x32_bf16 v[54:57], a[204:207], a[48:51], v[54:57]\n"
        "ds_write_b128 v41, v[34:37] offset:1024\n"
        "v_cvt_pk_bf16_f32 v71, v128, v129\n"
        "v_mfma_f32_16x16x32_bf16 v[34:37], a[144:147], a[60:63], v[46:49]\n"
        "v_cvt_pk_bf16_f32 v72, v130, v131\n"
        "v_cvt_pk_bf16_f32 v73, v132, v133\n"
        "v_add_f32 v134, v134, v78\n"
        "v_mfma_f32_16x16x32_bf16 v[34:37], a[148:151], a[64:67], v[34:37]\n"
        "ds_write_b128 v41, v[70:73] offset:2048\n"
        "v_add_f32 v134, v134, v79\n"
        "v_mfma_f32_16x16x32_bf16 v[34:37], a[152:155], a[68:71], v[34:37]\n"
        "v_add_f32 v134, v134, v80\n"
        "v_add_f32 v134, v134, v81\n"
        "v_add_f32 v134, v134, v90\n"
        "v_mfma_f32_16x16x32_bf16 v[46:49], a[164:167], a[68:71], v[50:53]\n"
        "v_add_f32 v134, v134, v91\n"
        "v_add_f32 v134, v134, v92\n"
        "v_add_f32 v134, v134, v93\n"
        "v_mfma_f32_16x16x32_bf16 v[46:49], a[160:163], a[64:67], v[46:49]\n"
        "v_add_f32 v134, v134, v86\n"
        "v_add_f32 v134, v134, v87\n"
        "v_add_f32 v134, v134, v88\n"
        "v_mfma_f32_16x16x32_bf16 v[46:49], a[156:159], a[60:63], v[46:49]\n"
        "v_add_f32 v134, v134, v89\n"
        "v_add_f32 v134, v134, v94\n"
        "v_add_f32 v134, v134, v95\n"
        "v_mfma_f32_16x16x32_bf16 v[50:53], a[168:171], a[60:63], v[58:61]\n"
        "v_add_f32 v134, v134, v96\n"
        "v_add_f32 v134, v134, v97\n"
        "v_add_f32 v134, v134, v82\n"
        "v_mfma_f32_16x16x32_bf16 v[50:53], a[172:175], a[64:67], v[50:53]\n"
        "v_add_f32 v134, v134, v83\n"
        "v_add_f32 v134, v134, v84\n"
        "v_add_f32 v134, v134, v85\n"
        "v_mfma_f32_16x16x32_bf16 v[50:53], a[176:179], a[68:71], v[50:53]\n"
        "v_mov_b32 v135, v134\n"
        "v_add_f32 v70, v102, v103\n"
        "v_add_f32 v70, v70, v104\n"
        "v_mfma_f32_16x16x32_bf16 v[58:61], a[188:191], a[68:71], v[66:69]\n"
        "v_permlane32_swap_b32 v134, v135\n"
        "v_add_f32 v134, v134, v135\n"
        "v_mfma_f32_16x16x32_bf16 v[58:61], a[184:187], a[64:67], v[58:61]\n"
        "v_mov_b32 v135, v134\n"
        "v_add_f32 v70, v70, v105\n"
        "v_add_f32 v70, v70, v106\n"
        "v_mfma_f32_16x16x32_bf16 v[58:61], a[180:183], a[60:63], v[58:61]\n"
        "v_permlane16_swap_b32 v134, v135\n"
        "v_add_f32 v134, v134, v135\n"
        "v_mfma_f32_16x16x32_bf16 v[38:41], a[192:195], a[60:63], v[62:65]\n"
        "v_mul_f32 v134, 0xbac909aa, v134\n"
        "v_add_f32 v70, v70, v107\n"
        "v_add_f32 v70, v70, v108\n"
        "v_mfma_f32_16x16x32_bf16 v[38:41], a[196:199], a[64:67], v[38:41]\n"
        "v_add_f32 v70, v70, v109\n"
        "v_add_f32 v70, v70, v98\n"
        "v_add_f32 v70, v70, v99\n"
        "v_mfma_f32_16x16x32_bf16 v[38:41], a[200:203], a[68:71], v[38:41]\n"
        "v_add_f32 v70, v70, v100\n"
        "v_add_f32 v70, v70, v101\n"
        "v_add_f32 v70, v70, v110\n"
        "v_mfma_f32_16x16x32_bf16 v[62:65], a[212:215], a[68:71], v[30:33]\n"
        "v_add_f32 v70, v70, v111\n"
        "v_add_f32 v70, v70, v112\n"
        "v_add_f32 v70, v70, v113\n"
        "v_mfma_f32_16x16x32_bf16 v[62:65], a[208:211], a[64:67], v[62:65]\n"
        "v_add_f32 v70, v70, v118\n"
        "v_add_f32 v70, v70, v119\n"
        "v_add_f32 v70, v70, v120\n"
        "v_mfma_f32_16x16x32_bf16 v[62:65], a[204:207], a[60:63], v[62:65]\n"
        "v_add_f32 v70, v70, v121\n"
        "v_add_f32 v70, v70, v54\n"
        "v_add_f32 v70, v70, v55\n"
        "v_add_f32 v70, v70, v56\n"
        "v_add_f32 v70, v70, v57\n"
        "v_mov_b32 v71, v70\n"
        "v_add_f32 v114, v34, v35\n"
        "v_add_f32 v114, v114, v36\n"
        "v_permlane32_swap_b32 v70, v71\n"
        "v_add_f32 v70, v70, v71\n"
        "v_mov_b32 v71, v70\n"
        "v_add_f32 v114, v114, v37\n"
        "v_add_f32 v114, v114, v46\n"
        "v_permlane16_swap_b32 v70, v71\n"
        "v_add_f32 v70, v70, v71\n"
        "v_mul_f32 v70, 0xbac909aa, v70\n"
        "v_add_f32 v114, v114, v47\n"
        "v_add_f32 v114, v114, v48\n"
        "v_add_f32 v114, v114, v49\n"
        "v_add_f32 v114, v114, v50\n"
        "v_add_f32 v114, v114, v51\n"
        "v_add_f32 v114, v114, v52\n"
        "v_add_f32 v114, v114, v53\n"
        "v_add_f32 v114, v114, v58\n"
        "v_add_f32 v114, v114, v59\n"
        "v_add_f32 v114, v114, v60\n"
        "v_add_f32 v114, v114, v61\n"
        "v_add_f32 v114, v114, v38\n"
        "v_add_f32 v114, v114, v39\n"
        "v_add_f32 v114, v114, v40\n"
        "v_add_f32 v114, v114, v41\n"
        "v_pk_fma_f32 v[74:75], v[74:75], s[24:25], v[134:135] op_sel_hi:[1,0,0]\n"
        "v_pk_fma_f32 v[76:77], v[76:77], s[24:25], v[134:135] op_sel_hi:[1,0,0]\n"
        "v_pk_fma_f32 v[78:79], v[78:79], s[24:25], v[134:135] op_sel_hi:[1,0,0]\n"
        "v_pk_fma_f32 v[80:81], v[80:81], s[24:25], v[134:135] op_sel_hi:[1,0,0]\n"
        "v_pk_fma_f32 v[90:91], v[90:91], s[24:25], v[134:135] op_sel_hi:[1,0,0]\n"
        "v_pk_fma_f32 v[92:93], v[92:93], s[24:25], v[134:135] op_sel_hi:[1,0,0]\n"
        "v_pk_fma_f32 v[86:87], v[86:87], s[24:25], v[134:135] op_sel_hi:[1,0,0]\n"
        "v_pk_fma_f32 v[88:89], v[88:89], s[24:25], v[134:135] op_sel_hi:[1,0,0]\n"
        "v_pk_fma_f32 v[94:95], v[94:95], s[24:25], v[134:135] op_sel_hi:[1,0,0]\n"
        "v_pk_fma_f32 v[96:97], v[96:97], s[24:25], v[134:135] op_sel_hi:[1,0,0]\n"
        "v_pk_fma_f32 v[82:83], v[82:83], s[24:25], v[134:135] op_sel_hi:[1,0,0]\n"
        "v_pk_fma_f32 v[84:85], v[84:85], s[24:25], v[134:135] op_sel_hi:[1,0,0]\n"
        "v_pk_fma_f32 v[102:103], v[102:103], s[24:25], v[70:71] op_sel_hi:[1,0,0]\n"
        "v_pk_fma_f32 v[104:105], v[104:105], s[24:25], v[70:71] op_sel_hi:[1,0,0]\n"
        "v_pk_fma_f32 v[106:107], v[106:107], s[24:25], v[70:71] op_sel_hi:[1,0,0]\n"
        "v_pk_fma_f32 v[108:109], v[108:109], s[24:25], v[70:71] op_sel_hi:[1,0,0]\n"
        "v_pk_fma_f32 v[98:99], v[98:99], s[24:25], v[70:71] op_sel_hi:[1,0,0]\n"
        "v_pk_fma_f32 v[100:101], v[100:101], s[24:25], v[70:71] op_sel_hi:[1,0,0]\n"
        "v_pk_fma_f32 v[110:111], v[110:111], s[24:25], v[70:71] op_sel_hi:[1,0,0]\n"
        "v_pk_fma_f32 v[112:113], v[112:113], s[24:25], v[70:71] op_sel_hi:[1,0,0]\n"
        "v_pk_fma_f32 v[118:119], v[118:119], s[24:25], v[70:71] op_sel_hi:[1,0,0]\n"
        "v_pk_fma_f32 v[120:121], v[120:121], s[24:25], v[70:71] op_sel_hi:[1,0,0]\n"
        "v_pk_fma_f32 v[54:55], v[54:55], s[24:25], v[70:71] op_sel_hi:[1,0,0]\n"
        "v_pk_fma_f32 v[56:57], v[56:57], s[24:25], v[70:71] op_sel_hi:[1,0,0]\n"
        "v_cvt_pk_bf16_f32 v30, v74, v75\n"
        "v_cvt_pk_bf16_f32 v31, v76, v77\n"
        "v_cvt_pk_bf16_f32 v32, v78, v79\n"
        "v_cvt_pk_bf16_f32 v33, v80, v81\n"
        "v_cvt_pk_bf16_f32 v66, v90, v91\n"
        "v_cvt_pk_bf16_f32 v67, v92, v93\n"
        "v_cvt_pk_bf16_f32 v68, v86, v87\n"
        "v_cvt_pk_bf16_f32 v69, v88, v89\n"
        "v_cvt_pk_bf16_f32 v70, v94, v95\n"
        "v_cvt_pk_bf16_f32 v71, v96, v97\n"
        "v_cvt_pk_bf16_f32 v72, v82, v83\n"
        "v_cvt_pk_bf16_f32 v73, v84, v85\n"
        "v_cvt_pk_bf16_f32 v78, v102, v103\n"
        "v_cvt_pk_bf16_f32 v79, v104, v105\n"
        "v_cvt_pk_bf16_f32 v80, v106, v107\n"
        "v_cvt_pk_bf16_f32 v81, v108, v109\n"
        "v_cvt_pk_bf16_f32 v74, v98, v99\n"
        "v_cvt_pk_bf16_f32 v75, v100, v101\n"
        "v_cvt_pk_bf16_f32 v76, v110, v111\n"
        "v_cvt_pk_bf16_f32 v77, v112, v113\n"
        "v_cvt_pk_bf16_f32 v82, v118, v119\n"
        "v_cvt_pk_bf16_f32 v83, v120, v121\n"
        "v_cvt_pk_bf16_f32 v84, v54, v55\n"
        "v_cvt_pk_bf16_f32 v85, v56, v57\n"
        "v_add_f32 v114, v114, v62\n"
        "ds_write_b128 v42, v[30:33]\n"
        "ds_write_b128 v42, v[66:69] offset:1024\n"
        "ds_write_b128 v42, v[70:73] offset:2048\n"
        "ds_write_b128 v43, v[78:81]\n"
        "ds_write_b128 v43, v[74:77] offset:1024\n"
        "ds_write_b128 v43, v[82:85] offset:2048\n"
        "v_add_f32 v114, v114, v63\n"
        "v_add_f32 v114, v114, v64\n"
        "v_add_f32 v114, v114, v65\n"
        "v_mov_b32 v115, v114\n"
        "s_waitcnt vmcnt(9)\n"
        "s_nop 0\n"
        "v_permlane32_swap_b32 v114, v115\n"
        "v_add_f32 v114, v114, v115\n"
        "v_mov_b32 v115, v114\n"
        "s_nop 1\n"
        "v_permlane16_swap_b32 v114, v115\n"
        "v_add_f32 v114, v114, v115\n"
        "v_mul_f32 v114, 0xbac909aa, v114\n"
        "v_pk_fma_f32 v[34:35], v[34:35], s[24:25], v[114:115] op_sel_hi:[1,0,0]\n"
        "v_pk_fma_f32 v[36:37], v[36:37], s[24:25], v[114:115] op_sel_hi:[1,0,0]\n"
        "v_pk_fma_f32 v[46:47], v[46:47], s[24:25], v[114:115] op_sel_hi:[1,0,0]\n"
        "v_pk_fma_f32 v[48:49], v[48:49], s[24:25], v[114:115] op_sel_hi:[1,0,0]\n"
        "v_pk_fma_f32 v[50:51], v[50:51], s[24:25], v[114:115] op_sel_hi:[1,0,0]\n"
        "v_pk_fma_f32 v[52:53], v[52:53], s[24:25], v[114:115] op_sel_hi:[1,0,0]\n"
        "v_pk_fma_f32 v[58:59], v[58:59], s[24:25], v[114:115] op_sel_hi:[1,0,0]\n"
        "v_pk_fma_f32 v[60:61], v[60:61], s[24:25], v[114:115] op_sel_hi:[1,0,0]\n"
        "v_pk_fma_f32 v[38:39], v[38:39], s[24:25], v[114:115] op_sel_hi:[1,0,0]\n"
        "v_pk_fma_f32 v[40:41], v[40:41], s[24:25], v[114:115] op_sel_hi:[1,0,0]\n"
        "v_pk_fma_f32 v[62:63], v[62:63], s[24:25], v[114:115] op_sel_hi:[1,0,0]\n"
        "v_pk_fma_f32 v[64:65], v[64:65], s[24:25], v[114:115] op_sel_hi:[1,0,0]\n"
        "v_cvt_pk_bf16_f32 v54, v34, v35\n"
        "v_cvt_pk_bf16_f32 v55, v36, v37\n"
        "v_cvt_pk_bf16_f32 v56, v46, v47\n"
        "v_cvt_pk_bf16_f32 v57, v48, v49\n"
        "v_cvt_pk_bf16_f32 v34, v50, v51\n"
        "v_cvt_pk_bf16_f32 v35, v52, v53\n"
        "v_cvt_pk_bf16_f32 v36, v58, v59\n"
        "v_cvt_pk_bf16_f32 v37, v60, v61\n"
        "v_cvt_pk_bf16_f32 v30, v38, v39\n"
        "v_cvt_pk_bf16_f32 v31, v40, v41\n"
        "v_cvt_pk_bf16_f32 v32, v62, v63\n"
        "v_cvt_pk_bf16_f32 v33, v64, v65\n"
        "ds_write_b128 v44, v[54:57]\n"
        "ds_write_b128 v44, v[34:37] offset:1024\n"
        "ds_write_b128 v44, v[30:33] offset:2048\n"
        "s_waitcnt lgkmcnt(0)\n"
        "s_barrier\n"
        "s_branch FE_%=\n"
        "FV_%=:\n"
        "s_bitcmp1_b32 s52, 11\n"
        "s_cbranch_scc1 FG_%=\n"
        "v_mov_b32 v50, 0x3f803f80\n"
        "v_mov_b32 v51, 0x3f803f80\n"
        "v_mov_b32 v52, 0x3f803f80\n"
        "v_mov_b32 v53, 0x3f803f80\n"
        "s_waitcnt vmcnt(15)\n"
        "ds_read_b128 a[0:3], v32\n"
        "ds_read_b128 a[4:7], v32 offset:64\n"
        "ds_read_b128 a[8:11], v32 offset:128\n"
        "s_add_u32 m0, s53, 15360\n"
        "s_nop 0\n"
        "buffer_load_dwordx4 v46, s[16:19], s23 offen lds\n"
        "ds_read_b128 a[12:15], v33\n"
        "ds_read_b128 a[16:19], v33 offset:64\n"
        "ds_read_b128 a[20:23], v33 offset:128\n"
        "buffer_load_dwordx4 v47, s[16:19], s23 offen offset:1024 lds\n"
        "ds_read_b128 a[24:27], v32 offset:6144\n"
        "ds_read_b128 a[28:31], v32 offset:6208\n"
        "ds_read_b128 a[32:35], v32 offset:6272\n"
        "buffer_load_dwordx4 v48, s[16:19], s23 offen offset:2048 lds\n"
        "ds_read_b128 a[36:39], v33 offset:6144\n"
        "ds_read_b128 a[40:43], v33 offset:6208\n"
        "ds_read_b128 a[44:47], v33 offset:6272\n"
        "ds_read_b128 a[48:51], v32 offset:12288\n"
        "ds_read_b128 a[52:55], v32 offset:12352\n"
        "ds_read_b128 a[56:59], v32 offset:12416\n"
        "ds_read_b128 a[60:63], v33 offset:12288\n"
        "ds_read_b128 a[64:67], v33 offset:12352\n"
        "ds_read_b128 a[68:71], v33 offset:12416\n"
        "ds_read_b64_tr_b16 a[72:73], v34 offset:0\n"
        "ds_read_b64_tr_b16 a[74:75], v35 offset:0\n"
        "ds_read_b64_tr_b16 a[76:77], v34 offset:6144\n"
        "ds_read_b64_tr_b16 a[78:79], v35 offset:6144\n"
        "ds_read_b64_tr_b16 a[80:81], v34 offset:12288\n"
        "ds_read_b64_tr_b16 a[82:83], v35 offset:12288\n"
        "s_waitcnt lgkmcnt(15)\n"
        "v_mfma_f32_16x16x32_bf16 v[54:57], a[0:3], v[50:53], 0\n"
        "s_waitcnt vmcnt(12)\n"
        "s_waitcnt vmcnt(6)\n"
        "v_mfma_f32_16x16x32_bf16 v[54:57], a[4:7], v[50:53], v[54:57]\n"
        "s_waitcnt lgkmcnt(15)\n"
        "v_mfma_f32_16x16x32_bf16 v[54:57], a[8:11], v[50:53], v[54:57]\n"
        "s_waitcnt vmcnt(0)\n"
        "s_nop 0\n"
        "v_mfma_f32_16x16x32_bf16 v[58:61], a[20:23], v[50:53], 0\n"
        "s_waitcnt lgkmcnt(15)\n"
        "s_nop 1\n"
        "v_mfma_f32_16x16x32_bf16 v[58:61], a[16:19], v[50:53], v[58:61]\n"
        "s_waitcnt lgkmcnt(12)\n"
        "v_cvt_pk_bf16_f32 v46, v54, v55\n"
        "v_cvt_pk_bf16_f32 v47, v56, v57\n"
        "v_mfma_f32_16x16x32_bf16 v[58:61], a[12:15], v[50:53], v[58:61]\n"
        "s_waitcnt lgkmcnt(9)\n"
        "v_mfma_f32_16x16x32_bf16 v[54:57], a[24:27], v[50:53], 0\n"
        "s_waitcnt lgkmcnt(6)\n"
        "ds_read_b128 a[144:147], v30\n"
        "s_nop 0\n"
        "v_mfma_f32_16x16x32_bf16 v[54:57], a[28:31], v[50:53], v[54:57]\n"
        "s_waitcnt lgkmcnt(1)\n"
        "ds_read_b128 a[148:151], v30 offset:64\n"
        "v_cvt_pk_bf16_f32 v48, v58, v59\n"
        "v_cvt_pk_bf16_f32 v49, v60, v61\n"
        "v_mfma_f32_16x16x32_bf16 v[54:57], a[32:35], v[50:53], v[54:57]\n"
        "s_nop 0\n"
        "ds_read_b128 a[152:155], v30 offset:128\n"
        "v_mfma_f32_16x16x32_bf16 v[58:61], a[44:47], v[50:53], 0\n"
        "s_nop 0\n"
        "ds_read_b128 a[156:159], v31\n"
        "v_mfma_f32_16x16x32_bf16 v[58:61], a[40:43], v[50:53], v[58:61]\n"
        "s_nop 0\n"
        "ds_read_b128 a[160:163], v31 offset:64\n"
        "v_cvt_pk_bf16_f32 v66, v54, v55\n"
        "v_cvt_pk_bf16_f32 v67, v56, v57\n"
        "v_mfma_f32_16x16x32_bf16 v[58:61], a[36:39], v[50:53], v[58:61]\n"
        "s_nop 0\n"
        "ds_read_b128 a[164:167], v31 offset:128\n"
        "v_mfma_f32_16x16x32_bf16 v[54:57], a[48:51], v[50:53], 0\n"
        "s_nop 0\n"
        "ds_read_b128 a[168:171], v30 offset:6144\n"
        "v_mfma_f32_16x16x32_bf16 v[54:57], a[52:55], v[50:53], v[54:57]\n"
        "s_nop 0\n"
        "ds_read_b128 a[172:175], v30 offset:6208\n"
        "v_cvt_pk_bf16_f32 v68, v58, v59\n"
        "v_cvt_pk_bf16_f32 v69, v60, v61\n"
        "v_mfma_f32_16x16x32_bf16 v[54:57], a[56:59], v[50:53], v[54:57]\n"
        "s_nop 0\n"
        "ds_read_b128 a[176:179], v30 offset:6272\n"
        "v_mfma_f32_16x16x32_bf16 v[58:61], a[68:71], v[50:53], 0\n"
        "s_nop 0\n"
        "ds_read_b128 a[180:183], v31 offset:6144\n"
        "v_mfma_f32_16x16x32_bf16 v[58:61], a[64:67], v[50:53], v[58:61]\n"
        "s_nop 0\n"
        "ds_read_b128 a[184:187], v31 offset:6208\n"
        "v_cvt_pk_bf16_f32 v70, v54, v55\n"
        "v_cvt_pk_bf16_f32 v71, v56, v57\n"
        "v_mfma_f32_16x16x32_bf16 v[58:61], a[60:63], v[50:53], v[58:61]\n"
        "ds_read_b128 a[188:191], v31 offset:6272\n"
        "s_waitcnt lgkmcnt(9)\n"
        "v_mfma_f32_16x16x32_bf16 v[50:53], a[144:147], a[72:75], 0\n"
        "s_nop 0\n"
        "ds_read_b128 a[192:195], v30 offset:12288\n"
        "v_mfma_f32_16x16x32_bf16 v[50:53], a[148:151], a[76:79], v[50:53]\n"
        "s_nop 0\n"
        "ds_read_b128 a[196:199], v30 offset:12352\n"
        "v_cvt_pk_bf16_f32 v72, v58, v59\n"
        "v_cvt_pk_bf16_f32 v73, v60, v61\n"
        "v_mfma_f32_16x16x32_bf16 v[50:53], a[152:155], a[80:83], v[50:53]\n"
        "ds_read_b128 a[200:203], v30 offset:12416\n"
        "v_mfma_f32_16x16x32_bf16 v[54:57], a[152:155], v[70:73], 0\n"
        "s_waitcnt lgkmcnt(9)\n"
        "buffer_load_dwordx4 v[164:167], v36, s[36:39], 0 offen\n"
        "v_mfma_f32_16x16x32_bf16 v[54:57], a[148:151], v[66:69], v[54:57]\n"
        "s_nop 0\n"
        "ds_read_b128 a[204:207], v31 offset:12288\n"
        "ds_read_b128 a[208:211], v31 offset:12352\n"
        "v_add_f32 v254, v50, v51\n"
        "v_mfma_f32_16x16x32_bf16 v[54:57], a[144:147], v[46:49], v[54:57]\n"
        "ds_read_b128 a[212:215], v31 offset:12416\n"
        "v_add_f32 v254, v254, v52\n"
        "v_add_f32 v254, v254, v53\n"
        "v_mfma_f32_16x16x32_bf16 v[82:85], a[156:159], a[72:75], 0\n"
        "s_waitcnt lgkmcnt(9)\n"
        "ds_read_b64_tr_b16 a[84:85], v34 offset:8\n"
        "v_mfma_f32_16x16x32_bf16 v[82:85], a[160:163], a[76:79], v[82:85]\n"
        "ds_read_b64_tr_b16 a[86:87], v35 offset:8\n"
        "v_add_f32 v255, v54, v55\n"
        "v_add_f32 v255, v255, v56\n"
        "v_mfma_f32_16x16x32_bf16 v[82:85], a[164:167], a[80:83], v[82:85]\n"
        "ds_read_b64_tr_b16 a[88:89], v34 offset:6152\n"
        "v_add_f32 v255, v255, v57\n"
        "v_mfma_f32_16x16x32_bf16 v[30:33], a[164:167], v[70:73], 0\n"
        "s_waitcnt lgkmcnt(9)\n"
        "ds_read_b64_tr_b16 a[90:91], v35 offset:6152\n"
        "v_mfma_f32_16x16x32_bf16 v[30:33], a[160:163], v[66:69], v[30:33]\n"
        "s_nop 0\n"
        "ds_read_b64_tr_b16 a[92:93], v34 offset:12296\n"
        "v_add_f32 v254, v254, v82\n"
        "v_add_f32 v254, v254, v83\n"
        "v_mfma_f32_16x16x32_bf16 v[30:33], a[156:159], v[46:49], v[30:33]\n"
        "ds_read_b64_tr_b16 a[94:95], v35 offset:12296\n"
        "v_add_f32 v254, v254, v84\n"
        "v_add_f32 v254, v254, v85\n"
        "v_mfma_f32_16x16x32_bf16 v[88:91], a[168:171], a[72:75], 0\n"
        "s_waitcnt lgkmcnt(9)\n"
        "buffer_load_dwordx4 v[170:173], v36, s[36:39], 0 offen offset:16\n"
        "v_mfma_f32_16x16x32_bf16 v[88:91], a[172:175], a[76:79], v[88:91]\n"
        "s_mov_b32 s78, 0xf0000f\n"
        "ds_read_b64_tr_b16 a[96:97], v34 offset:64\n"
        "ds_read_b64_tr_b16 a[98:99], v35 offset:64\n"
        "v_add_f32 v255, v255, v30\n"
        "v_mfma_f32_16x16x32_bf16 v[88:91], a[176:179], a[80:83], v[88:91]\n"
        "s_mov_b32 s79, 0xf0000f00\n"
        "ds_read_b64_tr_b16 a[100:101], v34 offset:6208\n"
        "v_add_f32 v255, v255, v31\n"
        "v_add_f32 v255, v255, v32\n"
        "v_mfma_f32_16x16x32_bf16 v[58:61], a[176:179], v[70:73], 0\n"
        "s_mov_b32 s76, 0x11111111\n"
        "ds_read_b64_tr_b16 a[102:103], v35 offset:6208\n"
        "v_add_f32 v255, v255, v33\n"
        "v_add_f32 v254, v254, v88\n"
        "v_mfma_f32_16x16x32_bf16 v[58:61], a[172:175], v[66:69], v[58:61]\n"
        "s_waitcnt lgkmcnt(10)\n"
        "ds_read_b64_tr_b16 a[104:105], v34 offset:12352\n"
        "v_add_f32 v254, v254, v89\n"
        "v_add_f32 v254, v254, v90\n"
        "v_mfma_f32_16x16x32_bf16 v[58:61], a[168:171], v[46:49], v[58:61]\n"
        "s_mov_b32 s77, 0x11111111\n"
        "ds_read_b64_tr_b16 a[106:107], v35 offset:12352\n"
        "v_add_f32 v254, v254, v91\n"
        "v_mfma_f32_16x16x32_bf16 v[92:95], a[180:183], a[72:75], 0\n"
        "s_mov_b32 s56, 0x22222222\n"
        "ds_read_b64_tr_b16 a[108:109], v34 offset:72\n"
        "v_mfma_f32_16x16x32_bf16 v[92:95], a[184:187], a[76:79], v[92:95]\n"
        "s_mov_b32 s57, 0x22222222\n"
        "ds_read_b64_tr_b16 a[110:111], v35 offset:72\n"
        "v_add_f32 v255, v255, v58\n"
        "v_add_f32 v255, v255, v59\n"
        "v_mfma_f32_16x16x32_bf16 v[92:95], a[188:191], a[80:83], v[92:95]\n"
        "s_mov_b32 s58, 0x44444444\n"
        "ds_read_b64_tr_b16 a[112:113], v34 offset:6216\n"
        "v_add_f32 v255, v255, v60\n"
        "v_add_f32 v255, v255, v61\n"
        "v_mfma_f32_16x16x32_bf16 v[62:65], a[188:191], v[70:73], 0\n"
        "s_waitcnt lgkmcnt(9)\n"
        "buffer_load_dwordx4 v[176:179], v36, s[36:39], 0 offen offset:32\n"
        "v_mfma_f32_16x16x32_bf16 v[62:65], a[184:187], v[66:69], v[62:65]\n"
        "s_mov_b32 s59, 0x44444444\n"
        "ds_read_b64_tr_b16 a[114:115], v35 offset:6216\n"
        "ds_read_b64_tr_b16 a[116:117], v34 offset:12360\n"
        "v_add_f32 v254, v254, v92\n"
        "v_mfma_f32_16x16x32_bf16 v[62:65], a[180:183], v[46:49], v[62:65]\n"
        "s_mov_b32 s60, 0x88888888\n"
        "ds_read_b64_tr_b16 a[118:119], v35 offset:12360\n"
        "v_add_f32 v254, v254, v93\n"
        "v_add_f32 v254, v254, v94\n"
        "v_mfma_f32_16x16x32_bf16 v[96:99], a[192:195], a[72:75], 0\n"
        "s_mov_b32 s61, 0x88888888\n"
        "ds_read_b64_tr_b16 a[120:121], v34 offset:128\n"
        "v_add_f32 v254, v254, v95\n"
        "v_add_f32 v255, v255, v62\n"
        "v_mfma_f32_16x16x32_bf16 v[96:99], a[196:199], a[76:79], v[96:99]\n"
        "s_mov_b32 s62, 0xffff\n"
        "ds_read_b64_tr_b16 a[122:123], v35 offset:128\n"
        "v_add_f32 v255, v255, v63\n"
        "v_add_f32 v255, v255, v64\n"
        "v_mfma_f32_16x16x32_bf16 v[96:99], a[200:203], a[80:83], v[96:99]\n"
        "s_mov_b32 s63, 0x0\n"
        "ds_read_b64_tr_b16 a[124:125], v34 offset:6272\n"
        "v_add_f32 v255, v255, v65\n"
        "v_mfma_f32_16x16x32_bf16 v[110:113], a[200:203], v[70:73], 0\n"
        "s_mov_b32 s64, 0xffff0000\n"
        "ds_read_b64_tr_b16 a[126:127], v35 offset:6272\n"
        "v_mfma_f32_16x16x32_bf16 v[110:113], a[196:199], v[66:69], v[110:113]\n"
        "s_mov_b32 s65, 0x0\n"
        "ds_read_b64_tr_b16 a[128:129], v34 offset:12416\n"
        "v_add_f32 v254, v254, v96\n"
        "v_add_f32 v254, v254, v97\n"
        "v_mfma_f32_16x16x32_bf16 v[110:113], a[192:195], v[46:49], v[110:113]\n"
        "s_mov_b32 s66, 0x0\n"
        "ds_read_b64_tr_b16 a[130:131], v35 offset:12416\n"
        "v_add_f32 v254, v254, v98\n"
        "v_add_f32 v254, v254, v99\n"
        "v_mfma_f32_16x16x32_bf16 v[106:109], a[204:207], a[72:75], 0\n"
        "s_waitcnt lgkmcnt(12)\n"
        "buffer_load_dwordx4 v[184:187], v37, s[36:39], 0 offen\n"
        "v_mfma_f32_16x16x32_bf16 v[106:109], a[208:211], a[76:79], v[106:109]\n"
        "s_mov_b32 s67, 0xffff\n"
        "ds_read_b64_tr_b16 a[132:133], v34 offset:136\n"
        "ds_read_b64_tr_b16 a[134:135], v35 offset:136\n"
        "v_add_f32 v255, v255, v110\n"
        "v_mfma_f32_16x16x32_bf16 v[106:109], a[212:215], a[80:83], v[106:109]\n"
        "s_mov_b32 s68, 0x0\n"
        "ds_read_b64_tr_b16 a[136:137], v34 offset:6280\n"
        "v_add_f32 v255, v255, v111\n"
        "v_add_f32 v255, v255, v112\n"
        "v_mfma_f32_16x16x32_bf16 v[102:105], a[212:215], v[70:73], 0\n"
        "s_mov_b32 s69, 0xffff0000\n"
        "ds_read_b64_tr_b16 a[138:139], v35 offset:6280\n"
        "v_add_f32 v255, v255, v113\n"
        "v_add_f32 v254, v254, v106\n"
        "v_mfma_f32_16x16x32_bf16 v[102:105], a[208:211], v[66:69], v[102:105]\n"
        "s_mov_b32 s70, 0x0\n"
        "ds_read_b64_tr_b16 a[140:141], v34 offset:12424\n"
        "v_add_f32 v254, v254, v107\n"
        "v_add_f32 v254, v254, v108\n"
        "v_mfma_f32_16x16x32_bf16 v[102:105], a[204:207], v[46:49], v[102:105]\n"
        "s_mov_b32 s71, 0xffffffff\n"
        "ds_read_b64_tr_b16 a[142:143], v35 offset:12424\n"
        "v_add_f32 v254, v254, v109\n"
        "v_mov_b32 v253, v254\n"
        "v_mfma_f32_16x16x32_bf16 v[46:49], a[144:147], a[84:87], 0\n"
        "s_mov_b32 s72, 0x0\n"
        "v_permlane32_swap_b32 v254, v253\n"
        "v_add_f32 v254, v254, v253\n"
        "v_mfma_f32_16x16x32_bf16 v[46:49], a[148:151], a[88:91], v[46:49]\n"
        "s_mov_b32 s73, 0x20000\n"
        "v_mov_b32 v253, v254\n"
        "v_add_f32 v255, v255, v102\n"
        "v_add_f32 v255, v255, v103\n"
        "v_mfma_f32_16x16x32_bf16 v[46:49], a[152:155], a[92:95], v[46:49]\n"
        "s_mov_b64 vcc, s[78:79]\n"
        "buffer_load_dwordx4 v[190:193], v37, s[36:39], 0 offen offset:16\n"
        "v_add_f32 v255, v255, v104\n"
        "v_add_f32 v255, v255, v105\n"
        "v_mfma_f32_16x16x32_bf16 v[70:73], a[164:167], a[92:95], 0\n"
        "s_mov_b32 s80, 0x3c2aaaab\n"
        "v_permlane16_swap_b32 v254, v253\n"
        "v_add_f32 v254, v254, v253\n"
        "v_mfma_f32_16x16x32_bf16 v[70:73], a[160:163], a[88:91], v[70:73]\n"
        "s_mov_b32 s81, 0x3727c5ac\n"
        "v_mul_f32 v254, 0xbc2aaaab, v254\n"
        "v_add_f32 v50, v50, v254\n"
        "v_add_f32 v51, v51, v254\n"
        "v_mfma_f32_16x16x32_bf16 v[70:73], a[156:159], a[84:87], v[70:73]\n"
        "s_mov_b32 s82, 0x43800000\n"
        "v_add_f32 v52, v52, v254\n"
        "v_add_f32 v53, v53, v254\n"
        "v_add_f32 v82, v82, v254\n"
        "v_mfma_f32_16x16x32_bf16 v[74:77], a[168:171], a[84:87], 0\n"
        "s_mov_b32 s84, 0x80000000\n"
        "v_add_f32 v83, v83, v254\n"
        "v_add_f32 v84, v84, v254\n"
        "v_add_f32 v85, v85, v254\n"
        "v_mfma_f32_16x16x32_bf16 v[74:77], a[172:175], a[88:91], v[74:77]\n"
        "s_mov_b32 s85, 19\n"
        "v_add_f32 v88, v88, v254\n"
        "v_add_f32 v89, v89, v254\n"
        "v_add_f32 v90, v90, v254\n"
        "v_mfma_f32_16x16x32_bf16 v[74:77], a[176:179], a[92:95], v[74:77]\n"
        "s_waitcnt lgkmcnt(0)\n"
        "v_add_f32 v91, v91, v254\n"
        "v_add_f32 v92, v92, v254\n"
        "v_add_f32 v93, v93, v254\n"
        "v_mfma_f32_16x16x32_bf16 v[66:69], a[188:191], a[92:95], 0\n"
        "s_barrier\n"
        "v_add_f32 v94, v94, v254\n"
        "v_add_f32 v95, v95, v254\n"
        "v_add_f32 v96, v96, v254\n"
        "v_mfma_f32_16x16x32_bf16 v[66:69], a[184:187], a[88:91], v[66:69]\n"
        "s_mov_b32 s86, 0x80000000\n"
        "v_add_f32 v97, v97, v254\n"
        "v_add_f32 v98, v98, v254\n"
        "v_add_f32 v99, v99, v254\n"
        "v_mfma_f32_16x16x32_bf16 v[66:69], a[180:183], a[84:87], v[66:69]\n"
        "s_mov_b32 s89, 0x80000000\n"
        "buffer_load_dwordx4 v[196:199], v37, s[36:39], 0 offen offset:32\n"
        "v_add_f32 v106, v106, v254\n"
        "v_add_f32 v107, v107, v254\n"
        "v_mfma_f32_16x16x32_bf16 v[78:81], a[192:195], a[84:87], 0\n"
        "s_mov_b32 s29, 1\n"
        "v_add_f32 v108, v108, v254\n"
        "v_add_f32 v109, v109, v254\n"
        "v_cvt_pk_bf16_f32 v34, v50, v51\n"
        "v_mfma_f32_16x16x32_bf16 v[78:81], a[196:199], a[88:91], v[78:81]\n"
        "s_mov_b32 s31, 0x400000\n"
        "v_cvt_pk_bf16_f32 v35, v52, v53\n"
        "v_cvt_pk_bf16_f32 v36, v82, v83\n"
        "v_cvt_pk_bf16_f32 v37, v84, v85\n"
        "v_mfma_f32_16x16x32_bf16 v[78:81], a[200:203], a[92:95], v[78:81]\n"
        "s_mov_b32 s87, s49\n"
        "ds_write_b128 v39, v[34:37]\n"
        "v_cvt_pk_bf16_f32 v34, v88, v89\n"
        "v_mfma_f32_16x16x32_bf16 v[86:89], a[212:215], a[92:95], 0\n"
        "s_mov_b32 s88, s50\n"
        "v_cvt_pk_bf16_f32 v35, v90, v91\n"
        "v_cvt_pk_bf16_f32 v36, v92, v93\n"
        "v_cvt_pk_bf16_f32 v37, v94, v95\n"
        "v_mfma_f32_16x16x32_bf16 v[86:89], a[208:211], a[88:91], v[86:89]\n"
        "s_mov_b64 s[44:45], s[40:41]\n"
        "ds_write_b128 v39, v[34:37] offset:1024\n"
        "v_cvt_pk_bf16_f32 v34, v96, v97\n"
        "v_mfma_f32_16x16x32_bf16 v[86:89], a[204:207], a[84:87], v[86:89]\n"
        "s_mov_b32 s46, 0\n"
        "v_cvt_pk_bf16_f32 v35, v98, v99\n"
        "v_cvt_pk_bf16_f32 v36, v106, v107\n"
        "v_cvt_pk_bf16_f32 v37, v108, v109\n"
        "v_mfma_f32_16x16x32_bf16 v[50:53], a[144:147], a[96:99], 0\n"
        "s_mov_b32 s47, s43\n"
        "ds_write_b128 v39, v[34:37] offset:2048\n"
        "v_mov_b32 v254, v255\n"
        "v_mfma_f32_16x16x32_bf16 v[50:53], a[148:151], a[100:103], v[50:53]\n"
        "s_add_u32 m0, s52, 0\n"
        "v_permlane32_swap_b32 v255, v254\n"
        "v_add_f32 v255, v255, v254\n"
        "v_mfma_f32_16x16x32_bf16 v[50:53], a[152:155], a[104:107], v[50:53]\n"
        "s_waitcnt lgkmcnt(3)\n"
        "buffer_load_dwordx4 v0, s[36:39], s49 offen lds\n"
        "v_mov_b32 v254, v255\n"
        "v_add_f32 v253, v46, v47\n"
        "v_mfma_f32_16x16x32_bf16 v[82:85], a[164:167], a[104:107], 0\n"
        "s_waitcnt lgkmcnt(3)\n"
        "v_permlane16_swap_b32 v255, v254\n"
        "v_add_f32 v255, v255, v254\n"
        "v_mfma_f32_16x16x32_bf16 v[82:85], a[160:163], a[100:103], v[82:85]\n"
        "s_waitcnt lgkmcnt(3)\n"
        "v_add_f32 v253, v253, v48\n"
        "v_mul_f32 v255, 0xbc2aaaab, v255\n"
        "v_add_f32 v253, v253, v49\n"
        "v_mfma_f32_16x16x32_bf16 v[82:85], a[156:159], a[96:99], v[82:85]\n"
        "v_add_f32 v54, v54, v255\n"
        "v_add_f32 v55, v55, v255\n"
        "v_add_f32 v56, v56, v255\n"
        "v_mfma_f32_16x16x32_bf16 v[94:97], a[168:171], a[96:99], 0\n"
        "v_add_f32 v57, v57, v255\n"
        "v_add_f32 v30, v30, v255\n"
        "v_add_f32 v31, v31, v255\n"
        "v_mfma_f32_16x16x32_bf16 v[94:97], a[172:175], a[100:103], v[94:97]\n"
        "v_add_f32 v32, v32, v255\n"
        "v_add_f32 v33, v33, v255\n"
        "v_add_f32 v58, v58, v255\n"
        "v_mfma_f32_16x16x32_bf16 v[94:97], a[176:179], a[104:107], v[94:97]\n"
        "buffer_load_dwordx4 v1, s[36:39], s49 offen offset:1024 lds\n"
        "v_add_f32 v59, v59, v255\n"
        "v_add_f32 v60, v60, v255\n"
        "v_mfma_f32_16x16x32_bf16 v[34:37], a[188:191], a[104:107], 0\n"
        "v_add_f32 v61, v61, v255\n"
        "v_add_f32 v62, v62, v255\n"
        "v_add_f32 v63, v63, v255\n"
        "v_mfma_f32_16x16x32_bf16 v[34:37], a[184:187], a[100:103], v[34:37]\n"
        "v_add_f32 v64, v64, v255\n"
        "v_add_f32 v65, v65, v255\n"
        "v_add_f32 v110, v110, v255\n"
        "v_mfma_f32_16x16x32_bf16 v[34:37], a[180:183], a[96:99], v[34:37]\n"
        "v_add_f32 v111, v111, v255\n"
        "v_add_f32 v112, v112, v255\n"
        "v_add_f32 v113, v113, v255\n"
        "v_mfma_f32_16x16x32_bf16 v[90:93], a[192:195], a[96:99], 0\n"
        "v_add_f32 v102, v102, v255\n"
        "v_add_f32 v103, v103, v255\n"
        "v_add_f32 v104, v104, v255\n"
        "v_mfma_f32_16x16x32_bf16 v[90:93], a[196:199], a[100:103], v[90:93]\n"
        "v_add_f32 v105, v105, v255\n"
        "v_cvt_pk_bf16_f32 v106, v54, v55\n"
        "v_cvt_pk_bf16_f32 v107, v56, v57\n"
        "v_mfma_f32_16x16x32_bf16 v[90:93], a[200:203], a[104:107], v[90:93]\n"
        "buffer_load_dwordx4 v2, s[36:39], s49 offen offset:2048 lds\n"
        "v_cvt_pk_bf16_f32 v108, v30, v31\n"
        "v_cvt_pk_bf16_f32 v109, v32, v33\n"
        "v_mfma_f32_16x16x32_bf16 v[98:101], a[212:215], a[104:107], 0\n"
        "ds_write_b128 v38, v[106:109]\n"
        "v_cvt_pk_bf16_f32 v30, v58, v59\n"
        "v_mfma_f32_16x16x32_bf16 v[98:101], a[208:211], a[100:103], v[98:101]\n"
        "v_cvt_pk_bf16_f32 v31, v60, v61\n"
        "v_cvt_pk_bf16_f32 v32, v62, v63\n"
        "v_cvt_pk_bf16_f32 v33, v64, v65\n"
        "v_mfma_f32_16x16x32_bf16 v[98:101], a[204:207], a[96:99], v[98:101]\n"
        "ds_write_b128 v38, v[30:33] offset:64\n"
        "v_cvt_pk_bf16_f32 v54, v110, v111\n"
        "v_mfma_f32_16x16x32_bf16 v[30:33], a[144:147], a[108:111], 0\n"
        "v_cvt_pk_bf16_f32 v55, v112, v113\n"
        "v_cvt_pk_bf16_f32 v56, v102, v103\n"
        "v_cvt_pk_bf16_f32 v57, v104, v105\n"
        "v_mfma_f32_16x16x32_bf16 v[30:33], a[148:151], a[112:115], v[30:33]\n"
        "ds_write_b128 v38, v[54:57] offset:128\n"
        "v_add_f32 v253, v253, v70\n"
        "v_mfma_f32_16x16x32_bf16 v[30:33], a[152:155], a[116:119], v[30:33]\n"
        "buffer_load_dwordx4 v3, s[36:39], s49 offen offset:3072 lds\n"
        "v_add_f32 v253, v253, v71\n"
        "v_add_f32 v253, v253, v72\n"
        "v_mfma_f32_16x16x32_bf16 v[54:57], a[164:167], a[116:119], 0\n"
        "v_add_f32 v253, v253, v73\n"
        "v_add_f32 v253, v253, v74\n"
        "v_add_f32 v253, v253, v75\n"
        "v_mfma_f32_16x16x32_bf16 v[54:57], a[160:163], a[112:115], v[54:57]\n"
        "v_add_f32 v253, v253, v76\n"
        "v_add_f32 v253, v253, v77\n"
        "v_add_f32 v253, v253, v66\n"
        "v_mfma_f32_16x16x32_bf16 v[54:57], a[156:159], a[108:111], v[54:57]\n"
        "v_add_f32 v253, v253, v67\n"
        "v_add_f32 v253, v253, v68\n"
        "v_add_f32 v253, v253, v69\n"
        "v_mfma_f32_16x16x32_bf16 v[102:105], a[168:171], a[108:111], 0\n"
        "v_add_f32 v253, v253, v78\n"
        "v_add_f32 v253, v253, v79\n"
        "v_add_f32 v253, v253, v80\n"
        "v_mfma_f32_16x16x32_bf16 v[102:105], a[172:175], a[112:115], v[102:105]\n"
        "s_add_u32 m0, s52, 4096\n"
        "v_add_f32 v253, v253, v81\n"
        "v_add_f32 v253, v253, v86\n"
        "v_add_f32 v253, v253, v87\n"
        "v_mfma_f32_16x16x32_bf16 v[102:105], a[176:179], a[116:119], v[102:105]\n"
        "buffer_load_dwordx4 v4, s[36:39], s49 offen lds\n"
        "v_add_f32 v253, v253, v88\n"
        "v_add_f32 v253, v253, v89\n"
        "v_mfma_f32_16x16x32_bf16 v[62:65], a[188:191], a[116:119], 0\n"
        "v_mov_b32 v254, v253\n"
        "v_add_f32 v255, v50, v51\n"
        "v_add_f32 v255, v255, v52\n"
        "v_mfma_f32_16x16x32_bf16 v[62:65], a[184:187], a[112:115], v[62:65]\n"
        "v_permlane32_swap_b32 v253, v254\n"
        "v_add_f32 v253, v253, v254\n"
        "v_mfma_f32_16x16x32_bf16 v[62:65], a[180:183], a[108:111], v[62:65]\n"
        "v_mov_b32 v254, v253\n"
        "v_add_f32 v255, v255, v53\n"
        "v_add_f32 v255, v255, v82\n"
        "v_mfma_f32_16x16x32_bf16 v[106:109], a[192:195], a[108:111], 0\n"
        "v_permlane16_swap_b32 v253, v254\n"
        "v_add_f32 v253, v253, v254\n"
        "v_mfma_f32_16x16x32_bf16 v[106:109], a[196:199], a[112:115], v[106:109]\n"
        "v_mul_f32 v253, 0xbc2aaaab, v253\n"
        "v_add_f32 v46, v46, v253\n"
        "v_add_f32 v47, v47, v253\n"
        "v_mfma_f32_16x16x32_bf16 v[106:109], a[200:203], a[116:119], v[106:109]\n"
        "buffer_load_dwordx4 v5, s[36:39], s49 offen offset:1024 lds\n"
        "v_add_f32 v48, v48, v253\n"
        "v_add_f32 v49, v49, v253\n"
        "v_mfma_f32_16x16x32_bf16 v[58:61], a[212:215], a[116:119], 0\n"
        "v_add_f32 v70, v70, v253\n"
        "v_add_f32 v71, v71, v253\n"
        "v_add_f32 v72, v72, v253\n"
        "v_mfma_f32_16x16x32_bf16 v[58:61], a[208:211], a[112:115], v[58:61]\n"
        "v_add_f32 v73, v73, v253\n"
        "v_add_f32 v74, v74, v253\n"
        "v_add_f32 v75, v75, v253\n"
        "v_mfma_f32_16x16x32_bf16 v[58:61], a[204:207], a[108:111], v[58:61]\n"
        "v_add_f32 v76, v76, v253\n"
        "v_add_f32 v77, v77, v253\n"
        "v_add_f32 v66, v66, v253\n"
        "v_mfma_f32_16x16x32_bf16 v[110:113], a[144:147], a[120:123], 0\n"
        "v_add_f32 v67, v67, v253\n"
        "v_add_f32 v68, v68, v253\n"
        "v_add_f32 v69, v69, v253\n"
        "v_mfma_f32_16x16x32_bf16 v[110:113], a[148:151], a[124:127], v[110:113]\n"
        "v_add_f32 v78, v78, v253\n"
        "v_add_f32 v79, v79, v253\n"
        "v_add_f32 v80, v80, v253\n"
        "v_mfma_f32_16x16x32_bf16 v[110:113], a[152:155], a[128:131], v[110:113]\n"
        "buffer_load_dwordx4 v6, s[36:39], s49 offen offset:2048 lds\n"
        "v_add_f32 v81, v81, v253\n"
        "v_add_f32 v86, v86, v253\n"
        "v_mfma_f32_16x16x32_bf16 v[114:117], a[164:167], a[128:131], 0\n"
        "v_add_f32 v87, v87, v253\n"
        "v_add_f32 v88, v88, v253\n"
        "v_add_f32 v89, v89, v253\n"
        "v_mfma_f32_16x16x32_bf16 v[114:117], a[160:163], a[124:127], v[114:117]\n"
        "v_cvt_pk_bf16_f32 v118, v46, v47\n"
        "v_cvt_pk_bf16_f32 v119, v48, v49\n"
        "v_cvt_pk_bf16_f32 v120, v70, v71\n"
        "v_mfma_f32_16x16x32_bf16 v[114:117], a[156:159], a[120:123], v[114:117]\n"
        "v_cvt_pk_bf16_f32 v121, v72, v73\n"
        "v_cvt_pk_bf16_f32 v70, v74, v75\n"
        "v_cvt_pk_bf16_f32 v71, v76, v77\n"
        "v_mfma_f32_16x16x32_bf16 v[46:49], a[168:171], a[120:123], 0\n"
        "ds_write_b128 v40, v[118:121]\n"
        "v_cvt_pk_bf16_f32 v72, v66, v67\n"
        "v_mfma_f32_16x16x32_bf16 v[46:49], a[172:175], a[124:127], v[46:49]\n"
        "v_cvt_pk_bf16_f32 v73, v68, v69\n"
        "v_cvt_pk_bf16_f32 v74, v78, v79\n"
        "v_cvt_pk_bf16_f32 v75, v80, v81\n"
        "v_mfma_f32_16x16x32_bf16 v[46:49], a[176:179], a[128:131], v[46:49]\n"
        "ds_write_b128 v40, v[70:73] offset:1024\n"
        "v_cvt_pk_bf16_f32 v76, v86, v87\n"
        "v_mfma_f32_16x16x32_bf16 v[66:69], a[188:191], a[128:131], 0\n"
        "buffer_load_dwordx4 v7, s[36:39], s49 offen offset:3072 lds\n"
        "v_cvt_pk_bf16_f32 v77, v88, v89\n"
        "v_add_f32 v255, v255, v83\n"
        "v_mfma_f32_16x16x32_bf16 v[66:69], a[184:187], a[124:127], v[66:69]\n"
        "ds_write_b128 v40, v[74:77] offset:2048\n"
        "v_add_f32 v255, v255, v84\n"
        "v_mfma_f32_16x16x32_bf16 v[66:69], a[180:183], a[120:123], v[66:69]\n"
        "v_add_f32 v255, v255, v85\n"
        "v_add_f32 v255, v255, v94\n"
        "v_add_f32 v255, v255, v95\n"
        "v_mfma_f32_16x16x32_bf16 v[70:73], a[192:195], a[120:123], 0\n"
        "v_add_f32 v255, v255, v96\n"
        "v_add_f32 v255, v255, v97\n"
        "v_add_f32 v255, v255, v34\n"
        "v_mfma_f32_16x16x32_bf16 v[70:73], a[196:199], a[124:127], v[70:73]\n"
        "s_add_u32 m0, s52, 8192\n"
        "v_add_f32 v255, v255, v35\n"
        "v_add_f32 v255, v255, v36\n"
        "v_add_f32 v255, v255, v37\n"
        "v_mfma_f32_16x16x32_bf16 v[70:73], a[200:203], a[128:131], v[70:73]\n"
        "buffer_load_dwordx4 v8, s[36:39], s49 offen lds\n"
        "v_add_f32 v255, v255, v90\n"
        "v_add_f32 v255, v255, v91\n"
        "v_mfma_f32_16x16x32_bf16 v[78:81], a[212:215], a[128:131], 0\n"
        "v_add_f32 v255, v255, v92\n"
        "v_add_f32 v255, v255, v93\n"
        "v_add_f32 v255, v255, v98\n"
        "v_mfma_f32_16x16x32_bf16 v[78:81], a[208:211], a[124:127], v[78:81]\n"
        "v_add_f32 v255, v255, v99\n"
        "v_add_f32 v255, v255, v100\n"
        "v_add_f32 v255, v255, v101\n"
        "v_mfma_f32_16x16x32_bf16 v[78:81], a[204:207], a[120:123], v[78:81]\n"
        "v_mov_b32 v254, v255\n"
        "v_add_f32 v38, v30, v31\n"
        "v_add_f32 v38, v38, v32\n"
        "v_mfma_f32_16x16x32_bf16 v[74:77], a[144:147], a[132:135], 0\n"
        "v_permlane32_swap_b32 v255, v254\n"
        "v_add_f32 v255, v255, v254\n"
        "v_mfma_f32_16x16x32_bf16 v[74:77], a[148:151], a[136:139], v[74:77]\n"
        "v_mov_b32 v254, v255\n"
        "v_add_f32 v38, v38, v33\n"
        "v_add_f32 v38, v38, v54\n"
        "v_mfma_f32_16x16x32_bf16 v[74:77], a[152:155], a[140:143], v[74:77]\n"
        "v_permlane16_swap_b32 v255, v254\n"
        "v_add_f32 v255, v255, v254\n"
        "v_mfma_f32_16x16x32_bf16 v[86:89], a[164:167], a[140:143], 0\n"
        "v_mul_f32 v255, 0xbc2aaaab, v255\n"
        "v_add_f32 v50, v50, v255\n"
        "v_add_f32 v51, v51, v255\n"
        "v_mfma_f32_16x16x32_bf16 v[86:89], a[160:163], a[136:139], v[86:89]\n"
        "v_add_f32 v52, v52, v255\n"
        "v_add_f32 v53, v53, v255\n"
        "v_add_f32 v82, v82, v255\n"
        "v_mfma_f32_16x16x32_bf16 v[86:89], a[156:159], a[132:135], v[86:89]\n"
        "v_add_f32 v83, v83, v255\n"
        "v_add_f32 v84, v84, v255\n"
        "v_add_f32 v85, v85, v255\n"
        "v_mfma_f32_16x16x32_bf16 v[118:121], a[168:171], a[132:135], 0\n"
        "v_add_f32 v94, v94, v255\n"
        "v_add_f32 v95, v95, v255\n"
        "v_add_f32 v96, v96, v255\n"
        "v_mfma_f32_16x16x32_bf16 v[118:121], a[172:175], a[136:139], v[118:121]\n"
        "v_add_f32 v97, v97, v255\n"
        "v_add_f32 v34, v34, v255\n"
        "v_add_f32 v35, v35, v255\n"
        "v_mfma_f32_16x16x32_bf16 v[118:121], a[176:179], a[140:143], v[118:121]\n"
        "v_add_f32 v36, v36, v255\n"
        "v_add_f32 v37, v37, v255\n"
        "v_add_f32 v90, v90, v255\n"
        "v_mfma_f32_16x16x32_bf16 v[126:129], a[188:191], a[140:143], 0\n"
        "v_add_f32 v91, v91, v255\n"
        "v_add_f32 v92, v92, v255\n"
        "v_add_f32 v93, v93, v255\n"
        "v_mfma_f32_16x16x32_bf16 v[126:129], a[184:187], a[136:139], v[126:129]\n"
        "v_add_f32 v98, v98, v255\n"
        "v_add_f32 v99, v99, v255\n"
        "v_add_f32 v100, v100, v255\n"
        "v_mfma_f32_16x16x32_bf16 v[126:129], a[180:183], a[132:135], v[126:129]\n"
        "v_add_f32 v101, v101, v255\n"
        "v_cvt_pk_bf16_f32 v122, v50, v51\n"
        "v_cvt_pk_bf16_f32 v123, v52, v53\n"
        "v_mfma_f32_16x16x32_bf16 v[50:53], a[192:195], a[132:135], 0\n"
        "v_cvt_pk_bf16_f32 v124, v82, v83\n"
        "v_cvt_pk_bf16_f32 v125, v84, v85\n"
        "v_cvt_pk_bf16_f32 v82, v94, v95\n"
        "v_mfma_f32_16x16x32_bf16 v[50:53], a[196:199], a[136:139], v[50:53]\n"
        "ds_write_b128 v41, v[122:125]\n"
        "v_cvt_pk_bf16_f32 v83, v96, v97\n"
        "v_mfma_f32_16x16x32_bf16 v[50:53], a[200:203], a[140:143], v[50:53]\n"
        "v_cvt_pk_bf16_f32 v84, v34, v35\n"
        "v_cvt_pk_bf16_f32 v85, v36, v37\n"
        "v_cvt_pk_bf16_f32 v34, v90, v91\n"
        "v_mfma_f32_16x16x32_bf16 v[94:97], a[212:215], a[140:143], 0\n"
        "ds_write_b128 v41, v[82:85] offset:1024\n"
        "v_cvt_pk_bf16_f32 v35, v92, v93\n"
        "v_mfma_f32_16x16x32_bf16 v[94:97], a[208:211], a[136:139], v[94:97]\n"
        "v_cvt_pk_bf16_f32 v36, v98, v99\n"
        "v_cvt_pk_bf16_f32 v37, v100, v101\n"
        "v_add_f32 v38, v38, v55\n"
        "v_mfma_f32_16x16x32_bf16 v[94:97], a[204:207], a[132:135], v[94:97]\n"
        "ds_write_b128 v41, v[34:37] offset:2048\n"
        "v_add_f32 v38, v38, v56\n"
        "v_add_f32 v38, v38, v57\n"
        "v_add_f32 v38, v38, v102\n"
        "v_add_f32 v38, v38, v103\n"
        "v_add_f32 v38, v38, v104\n"
        "v_add_f32 v38, v38, v105\n"
        "v_add_f32 v38, v38, v62\n"
        "v_add_f32 v38, v38, v63\n"
        "v_add_f32 v38, v38, v64\n"
        "v_add_f32 v38, v38, v65\n"
        "v_add_f32 v38, v38, v106\n"
        "v_add_f32 v38, v38, v107\n"
        "v_add_f32 v38, v38, v108\n"
        "v_add_f32 v38, v38, v109\n"
        "v_add_f32 v38, v38, v58\n"
        "v_add_f32 v38, v38, v59\n"
        "v_add_f32 v38, v38, v60\n"
        "v_add_f32 v38, v38, v61\n"
        "v_mov_b32 v39, v38\n"
        "v_add_f32 v34, v110, v111\n"
        "v_add_f32 v34, v34, v112\n"
        "v_permlane32_swap_b32 v38, v39\n"
        "v_add_f32 v38, v38, v39\n"
        "v_mov_b32 v39, v38\n"
        "v_add_f32 v34, v34, v113\n"
        "v_add_f32 v34, v34, v114\n"
        "v_permlane16_swap_b32 v38, v39\n"
        "v_add_f32 v38, v38, v39\n"
        "v_mul_f32 v38, 0xbc2aaaab, v38\n"
        "v_add_f32 v34, v34, v115\n"
        "v_add_f32 v34, v34, v116\n"
        "v_add_f32 v34, v34, v117\n"
        "v_add_f32 v34, v34, v46\n"
        "v_add_f32 v34, v34, v47\n"
        "v_add_f32 v34, v34, v48\n"
        "v_add_f32 v34, v34, v49\n"
        "v_add_f32 v34, v34, v66\n"
        "v_add_f32 v34, v34, v67\n"
        "v_add_f32 v34, v34, v68\n"
        "v_add_f32 v34, v34, v69\n"
        "v_add_f32 v34, v34, v70\n"
        "v_add_f32 v34, v34, v71\n"
        "v_add_f32 v34, v34, v72\n"
        "v_add_f32 v34, v34, v73\n"
        "v_add_f32 v34, v34, v78\n"
        "v_add_f32 v34, v34, v79\n"
        "v_add_f32 v34, v34, v80\n"
        "v_add_f32 v34, v34, v81\n"
        "v_mov_b32 v35, v34\n"
        "v_add_f32 v82, v74, v75\n"
        "v_add_f32 v82, v82, v76\n"
        "v_permlane32_swap_b32 v34, v35\n"
        "v_add_f32 v34, v34, v35\n"
        "v_mov_b32 v35, v34\n"
        "v_add_f32 v82, v82, v77\n"
        "v_add_f32 v82, v82, v86\n"
        "v_permlane16_swap_b32 v34, v35\n"
        "v_add_f32 v34, v34, v35\n"
        "v_mul_f32 v34, 0xbc2aaaab, v34\n"
        "v_add_f32 v82, v82, v87\n"
        "v_add_f32 v82, v82, v88\n"
        "v_add_f32 v82, v82, v89\n"
        "v_add_f32 v82, v82, v118\n"
        "v_add_f32 v82, v82, v119\n"
        "v_add_f32 v82, v82, v120\n"
        "v_add_f32 v82, v82, v121\n"
        "v_add_f32 v82, v82, v126\n"
        "v_add_f32 v82, v82, v127\n"
        "v_add_f32 v82, v82, v128\n"
        "v_add_f32 v82, v82, v129\n"
        "v_add_f32 v82, v82, v50\n"
        "v_add_f32 v82, v82, v51\n"
        "v_add_f32 v82, v82, v52\n"
        "v_add_f32 v82, v82, v53\n"
        "v_pk_add_f32 v[30:31], v[30:31], v[38:39] op_sel_hi:[1,0]\n"
        "v_pk_add_f32 v[32:33], v[32:33], v[38:39] op_sel_hi:[1,0]\n"
        "v_pk_add_f32 v[54:55], v[54:55], v[38:39] op_sel_hi:[1,0]\n"
        "v_pk_add_f32 v[56:57], v[56:57], v[38:39] op_sel_hi:[1,0]\n"
        "v_pk_add_f32 v[102:103], v[102:103], v[38:39] op_sel_hi:[1,0]\n"
        "v_pk_add_f32 v[104:105], v[104:105], v[38:39] op_sel_hi:[1,0]\n"
        "v_pk_add_f32 v[62:63], v[62:63], v[38:39] op_sel_hi:[1,0]\n"
        "v_pk_add_f32 v[64:65], v[64:65], v[38:39] op_sel_hi:[1,0]\n"
        "v_pk_add_f32 v[106:107], v[106:107], v[38:39] op_sel_hi:[1,0]\n"
        "v_pk_add_f32 v[108:109], v[108:109], v[38:39] op_sel_hi:[1,0]\n"
        "v_pk_add_f32 v[58:59], v[58:59], v[38:39] op_sel_hi:[1,0]\n"
        "v_pk_add_f32 v[60:61], v[60:61], v[38:39] op_sel_hi:[1,0]\n"
        "v_pk_add_f32 v[110:111], v[110:111], v[34:35] op_sel_hi:[1,0]\n"
        "v_pk_add_f32 v[112:113], v[112:113], v[34:35] op_sel_hi:[1,0]\n"
        "v_pk_add_f32 v[114:115], v[114:115], v[34:35] op_sel_hi:[1,0]\n"
        "v_pk_add_f32 v[116:117], v[116:117], v[34:35] op_sel_hi:[1,0]\n"
        "v_pk_add_f32 v[46:47], v[46:47], v[34:35] op_sel_hi:[1,0]\n"
        "v_pk_add_f32 v[48:49], v[48:49], v[34:35] op_sel_hi:[1,0]\n"
        "v_pk_add_f32 v[66:67], v[66:67], v[34:35] op_sel_hi:[1,0]\n"
        "v_pk_add_f32 v[68:69], v[68:69], v[34:35] op_sel_hi:[1,0]\n"
        "v_pk_add_f32 v[70:71], v[70:71], v[34:35] op_sel_hi:[1,0]\n"
        "v_pk_add_f32 v[72:73], v[72:73], v[34:35] op_sel_hi:[1,0]\n"
        "v_pk_add_f32 v[78:79], v[78:79], v[34:35] op_sel_hi:[1,0]\n"
        "v_pk_add_f32 v[80:81], v[80:81], v[34:35] op_sel_hi:[1,0]\n"
        "v_cvt_pk_bf16_f32 v34, v30, v31\n"
        "v_cvt_pk_bf16_f32 v35, v32, v33\n"
        "v_cvt_pk_bf16_f32 v36, v54, v55\n"
        "v_cvt_pk_bf16_f32 v37, v56, v57\n"
        "v_cvt_pk_bf16_f32 v54, v102, v103\n"
        "v_cvt_pk_bf16_f32 v55, v104, v105\n"
        "v_cvt_pk_bf16_f32 v56, v62, v63\n"
        "v_cvt_pk_bf16_f32 v57, v64, v65\n"
        "v_cvt_pk_bf16_f32 v38, v106, v107\n"
        "v_cvt_pk_bf16_f32 v39, v108, v109\n"
        "v_cvt_pk_bf16_f32 v40, v58, v59\n"
        "v_cvt_pk_bf16_f32 v41, v60, v61\n"
        "v_cvt_pk_bf16_f32 v58, v110, v111\n"
        "v_cvt_pk_bf16_f32 v59, v112, v113\n"
        "v_cvt_pk_bf16_f32 v60, v114, v115\n"
        "v_cvt_pk_bf16_f32 v61, v116, v117\n"
        "v_cvt_pk_bf16_f32 v62, v46, v47\n"
        "v_cvt_pk_bf16_f32 v63, v48, v49\n"
        "v_cvt_pk_bf16_f32 v64, v66, v67\n"
        "v_cvt_pk_bf16_f32 v65, v68, v69\n"
        "v_cvt_pk_bf16_f32 v30, v70, v71\n"
        "v_cvt_pk_bf16_f32 v31, v72, v73\n"
        "v_cvt_pk_bf16_f32 v32, v78, v79\n"
        "v_cvt_pk_bf16_f32 v33, v80, v81\n"
        "v_add_f32 v82, v82, v94\n"
        "ds_write_b128 v42, v[34:37]\n"
        "ds_write_b128 v42, v[54:57] offset:1024\n"
        "ds_write_b128 v42, v[38:41] offset:2048\n"
        "ds_write_b128 v43, v[58:61]\n"
        "ds_write_b128 v43, v[62:65] offset:1024\n"
        "ds_write_b128 v43, v[30:33] offset:2048\n"
        "v_add_f32 v82, v82, v95\n"
        "v_add_f32 v82, v82, v96\n"
        "v_add_f32 v82, v82, v97\n"
        "v_mov_b32 v83, v82\n"
        "s_waitcnt vmcnt(9)\n"
        "s_nop 0\n"
        "v_permlane32_swap_b32 v82, v83\n"
        "v_add_f32 v82, v82, v83\n"
        "v_mov_b32 v83, v82\n"
        "s_nop 1\n"
        "v_permlane16_swap_b32 v82, v83\n"
        "v_add_f32 v82, v82, v83\n"
        "v_mul_f32 v82, 0xbc2aaaab, v82\n"
        "v_pk_add_f32 v[74:75], v[74:75], v[82:83] op_sel_hi:[1,0]\n"
        "v_pk_add_f32 v[76:77], v[76:77], v[82:83] op_sel_hi:[1,0]\n"
        "v_pk_add_f32 v[86:87], v[86:87], v[82:83] op_sel_hi:[1,0]\n"
        "v_pk_add_f32 v[88:89], v[88:89], v[82:83] op_sel_hi:[1,0]\n"
        "v_pk_add_f32 v[118:119], v[118:119], v[82:83] op_sel_hi:[1,0]\n"
        "v_pk_add_f32 v[120:121], v[120:121], v[82:83] op_sel_hi:[1,0]\n"
        "v_pk_add_f32 v[126:127], v[126:127], v[82:83] op_sel_hi:[1,0]\n"
        "v_pk_add_f32 v[128:129], v[128:129], v[82:83] op_sel_hi:[1,0]\n"
        "v_pk_add_f32 v[50:51], v[50:51], v[82:83] op_sel_hi:[1,0]\n"
        "v_pk_add_f32 v[52:53], v[52:53], v[82:83] op_sel_hi:[1,0]\n"
        "v_pk_add_f32 v[94:95], v[94:95], v[82:83] op_sel_hi:[1,0]\n"
        "v_pk_add_f32 v[96:97], v[96:97], v[82:83] op_sel_hi:[1,0]\n"
        "v_cvt_pk_bf16_f32 v34, v74, v75\n"
        "v_cvt_pk_bf16_f32 v35, v76, v77\n"
        "v_cvt_pk_bf16_f32 v36, v86, v87\n"
        "v_cvt_pk_bf16_f32 v37, v88, v89\n"
        "v_cvt_pk_bf16_f32 v38, v118, v119\n"
        "v_cvt_pk_bf16_f32 v39, v120, v121\n"
        "v_cvt_pk_bf16_f32 v40, v126, v127\n"
        "v_cvt_pk_bf16_f32 v41, v128, v129\n"
        "v_cvt_pk_bf16_f32 v30, v50, v51\n"
        "v_cvt_pk_bf16_f32 v31, v52, v53\n"
        "v_cvt_pk_bf16_f32 v32, v94, v95\n"
        "v_cvt_pk_bf16_f32 v33, v96, v97\n"
        "ds_write_b128 v44, v[34:37]\n"
        "ds_write_b128 v44, v[38:41] offset:1024\n"
        "ds_write_b128 v44, v[30:33] offset:2048\n"
        "s_waitcnt lgkmcnt(0)\n"
        "s_barrier\n"
        "s_branch FE_%=\n"
        "FG_%=:\n"
        "s_mov_b32 s24, 0x471c4000\n"
        "v_mov_b32 v255, 0x3f803f80\n"
        "v_mov_b32 v255, 0x3f803f80\n"
        "v_mov_b32 v255, 0x3f803f80\n"
        "v_mov_b32 v255, 0x3f803f80\n"
        "s_waitcnt vmcnt(15)\n"
        "ds_read_b64_tr_b16 a[72:73], v34 offset:0\n"
        "ds_read_b64_tr_b16 a[74:75], v35 offset:0\n"
        "ds_read_b64_tr_b16 a[76:77], v34 offset:6144\n"
        "ds_read_b64_tr_b16 a[78:79], v35 offset:6144\n"
        "ds_read_b64_tr_b16 a[80:81], v34 offset:12288\n"
        "ds_read_b64_tr_b16 a[82:83], v35 offset:12288\n"
        "s_add_u32 m0, s53, 15360\n"
        "s_nop 0\n"
        "buffer_load_dwordx4 v46, s[16:19], s23 offen lds\n"
        "ds_read_b64_tr_b16 a[84:85], v34 offset:8\n"
        "ds_read_b64_tr_b16 a[86:87], v35 offset:8\n"
        "ds_read_b64_tr_b16 a[88:89], v34 offset:6152\n"
        "ds_read_b64_tr_b16 a[90:91], v35 offset:6152\n"
        "ds_read_b64_tr_b16 a[92:93], v34 offset:12296\n"
        "ds_read_b64_tr_b16 a[94:95], v35 offset:12296\n"
        "buffer_load_dwordx4 v47, s[16:19], s23 offen offset:1024 lds\n"
        "ds_read_b64_tr_b16 a[96:97], v34 offset:64\n"
        "ds_read_b64_tr_b16 a[98:99], v35 offset:64\n"
        "ds_read_b64_tr_b16 a[100:101], v34 offset:6208\n"
        "ds_read_b64_tr_b16 a[102:103], v35 offset:6208\n"
        "ds_read_b64_tr_b16 a[104:105], v34 offset:12352\n"
        "ds_read_b64_tr_b16 a[106:107], v35 offset:12352\n"
        "buffer_load_dwordx4 v48, s[16:19], s23 offen offset:2048 lds\n"
        "ds_read_b64_tr_b16 a[108:109], v34 offset:72\n"
        "ds_read_b64_tr_b16 a[110:111], v35 offset:72\n"
        "ds_read_b64_tr_b16 a[112:113], v34 offset:6216\n"
        "ds_read_b64_tr_b16 a[114:115], v35 offset:6216\n"
        "ds_read_b64_tr_b16 a[116:117], v34 offset:12360\n"
        "ds_read_b64_tr_b16 a[118:119], v35 offset:12360\n"
        "ds_read_b64_tr_b16 a[120:121], v34 offset:128\n"
        "ds_read_b64_tr_b16 a[122:123], v35 offset:128\n"
        "ds_read_b64_tr_b16 a[124:125], v34 offset:6272\n"
        "ds_read_b64_tr_b16 a[126:127], v35 offset:6272\n"
        "ds_read_b64_tr_b16 a[128:129], v34 offset:12416\n"
        "ds_read_b64_tr_b16 a[130:131], v35 offset:12416\n"
        "ds_read_b64_tr_b16 a[132:133], v34 offset:136\n"
        "ds_read_b64_tr_b16 a[134:135], v35 offset:136\n"
        "ds_read_b64_tr_b16 a[136:137], v34 offset:6280\n"
        "ds_read_b64_tr_b16 a[138:139], v35 offset:6280\n"
        "ds_read_b64_tr_b16 a[140:141], v34 offset:12424\n"
        "ds_read_b64_tr_b16 a[142:143], v35 offset:12424\n"
        "s_waitcnt vmcnt(12)\n"
        "ds_read_b128 a[144:147], v30\n"
        "ds_read_b128 a[148:151], v30 offset:64\n"
        "ds_read_b128 a[152:155], v30 offset:128\n"
        "ds_read_b128 a[156:159], v31\n"
        "ds_read_b128 a[160:163], v31 offset:64\n"
        "ds_read_b128 a[164:167], v31 offset:128\n"
        "s_waitcnt lgkmcnt(15)\n"
        "s_waitcnt lgkmcnt(3)\n"
        "v_mfma_f32_16x16x32_bf16 v[50:53], a[144:147], a[72:75], 0\n"
        "s_waitcnt vmcnt(6)\n"
        "ds_read_b128 a[168:171], v30 offset:6144\n"
        "v_mov_b32 v255, 161888\n"
        "v_mfma_f32_16x16x32_bf16 v[50:53], a[148:151], a[76:79], v[50:53]\n"
        "s_waitcnt vmcnt(0)\n"
        "ds_read_b128 a[172:175], v30 offset:6208\n"
        "ds_read_b128 a[176:179], v30 offset:6272\n"
        "v_mfma_f32_16x16x32_bf16 v[50:53], a[152:155], a[80:83], v[50:53]\n"
        "s_waitcnt lgkmcnt(6)\n"
        "ds_read_b128 a[180:183], v31 offset:6144\n"
        "ds_read_b128 a[184:187], v31 offset:6208\n"
        "s_waitcnt lgkmcnt(5)\n"
        "v_mfma_f32_16x16x32_bf16 v[58:61], a[164:167], a[80:83], 0\n"
        "s_waitcnt lgkmcnt(5)\n"
        "ds_read_b128 a[188:191], v31 offset:6272\n"
        "ds_read_b128 a[192:195], v30 offset:12288\n"
        "v_add_f32 v254, v50, v51\n"
        "v_mfma_f32_16x16x32_bf16 v[58:61], a[160:163], a[76:79], v[58:61]\n"
        "s_waitcnt lgkmcnt(7)\n"
        "ds_read_b128 a[196:199], v30 offset:12352\n"
        "ds_read_b128 a[200:203], v30 offset:12416\n"
        "v_add_f32 v254, v254, v52\n"
        "v_mfma_f32_16x16x32_bf16 v[58:61], a[156:159], a[72:75], v[58:61]\n"
        "s_waitcnt lgkmcnt(9)\n"
        "ds_read_b128 a[204:207], v31 offset:12288\n"
        "v_add_f32 v254, v254, v53\n"
        "s_waitcnt lgkmcnt(7)\n"
        "v_mfma_f32_16x16x32_bf16 v[70:73], a[168:171], a[72:75], 0\n"
        "s_waitcnt lgkmcnt(7)\n"
        "ds_read_b128 a[208:211], v31 offset:12352\n"
        "ds_read_b128 a[212:215], v31 offset:12416\n"
        "v_add_f32 v254, v254, v58\n"
        "v_mfma_f32_16x16x32_bf16 v[70:73], a[172:175], a[76:79], v[70:73]\n"
        "s_waitcnt lgkmcnt(9)\n"
        "v_add_f32 v254, v254, v59\n"
        "v_add_f32 v254, v254, v60\n"
        "v_add_f32 v254, v254, v61\n"
        "v_mfma_f32_16x16x32_bf16 v[70:73], a[176:179], a[80:83], v[70:73]\n"
        "s_waitcnt lgkmcnt(6)\n"
        "s_nop 0\n"
        "v_mfma_f32_16x16x32_bf16 v[66:69], a[188:191], a[80:83], 0\n"
        "s_waitcnt lgkmcnt(3)\n"
        "s_nop 1\n"
        "v_mfma_f32_16x16x32_bf16 v[66:69], a[184:187], a[76:79], v[66:69]\n"
        "s_nop 0\n"
        "v_add_f32 v254, v254, v70\n"
        "v_add_f32 v254, v254, v71\n"
        "v_add_f32 v254, v254, v72\n"
        "v_mfma_f32_16x16x32_bf16 v[66:69], a[180:183], a[72:75], v[66:69]\n"
        "s_nop 0\n"
        "v_add_f32 v254, v254, v73\n"
        "v_mfma_f32_16x16x32_bf16 v[78:81], a[192:195], a[72:75], 0\n"
        "s_nop 2\n"
        "v_mfma_f32_16x16x32_bf16 v[78:81], a[196:199], a[76:79], v[78:81]\n"
        "s_nop 0\n"
        "v_add_f32 v254, v254, v66\n"
        "v_add_f32 v254, v254, v67\n"
        "v_add_f32 v254, v254, v68\n"
        "v_mfma_f32_16x16x32_bf16 v[78:81], a[200:203], a[80:83], v[78:81]\n"
        "s_nop 0\n"
        "v_add_f32 v254, v254, v69\n"
        "s_waitcnt lgkmcnt(0)\n"
        "v_mfma_f32_16x16x32_bf16 v[74:77], a[212:215], a[80:83], 0\n"
        "s_nop 1\n"
        "v_mfma_f32_16x16x32_bf16 v[74:77], a[208:211], a[76:79], v[74:77]\n"
        "s_nop 0\n"
        "v_add_f32 v254, v254, v78\n"
        "v_add_f32 v254, v254, v79\n"
        "v_add_f32 v254, v254, v80\n"
        "v_mfma_f32_16x16x32_bf16 v[74:77], a[204:207], a[72:75], v[74:77]\n"
        "s_nop 0\n"
        "v_add_f32 v254, v254, v81\n"
        "v_mfma_f32_16x16x32_bf16 v[30:33], a[144:147], a[84:87], 0\n"
        "s_nop 2\n"
        "v_mfma_f32_16x16x32_bf16 v[30:33], a[148:151], a[88:91], v[30:33]\n"
        "s_nop 0\n"
        "v_add_f32 v254, v254, v74\n"
        "v_add_f32 v254, v254, v75\n"
        "v_add_f32 v254, v254, v76\n"
        "v_mfma_f32_16x16x32_bf16 v[30:33], a[152:155], a[92:95], v[30:33]\n"
        "buffer_load_dwordx4 v[164:167], v36, s[36:39], 0 offen\n"
        "v_add_f32 v254, v254, v77\n"
        "v_mov_b32 v253, v254\n"
        "v_mfma_f32_16x16x32_bf16 v[46:49], a[164:167], a[92:95], 0\n"
        "s_nop 0\n"
        "v_permlane32_swap_b32 v254, v253\n"
        "v_add_f32 v254, v254, v253\n"
        "v_mfma_f32_16x16x32_bf16 v[46:49], a[160:163], a[88:91], v[46:49]\n"
        "v_mov_b32 v253, v254\n"
        "v_add_f32 v252, v30, v31\n"
        "v_add_f32 v252, v252, v32\n"
        "v_mfma_f32_16x16x32_bf16 v[46:49], a[156:159], a[84:87], v[46:49]\n"
        "v_permlane16_swap_b32 v254, v253\n"
        "v_add_f32 v254, v254, v253\n"
        "v_mfma_f32_16x16x32_bf16 v[92:95], a[168:171], a[84:87], 0\n"
        "v_mul_f32 v254, 0xbc2aaaab, v254\n"
        "v_add_f32 v252, v252, v33\n"
        "v_add_f32 v50, v50, v254\n"
        "v_mfma_f32_16x16x32_bf16 v[92:95], a[172:175], a[88:91], v[92:95]\n"
        "v_add_f32 v51, v51, v254\n"
        "v_add_f32 v52, v52, v254\n"
        "v_add_f32 v53, v53, v254\n"
        "v_mfma_f32_16x16x32_bf16 v[92:95], a[176:179], a[92:95], v[92:95]\n"
        "v_add_f32 v58, v58, v254\n"
        "v_add_f32 v59, v59, v254\n"
        "v_add_f32 v60, v60, v254\n"
        "v_mfma_f32_16x16x32_bf16 v[102:105], a[188:191], a[92:95], 0\n"
        "v_add_f32 v61, v61, v254\n"
        "v_add_f32 v70, v70, v254\n"
        "v_add_f32 v71, v71, v254\n"
        "v_mfma_f32_16x16x32_bf16 v[102:105], a[184:187], a[88:91], v[102:105]\n"
        "v_add_f32 v72, v72, v254\n"
        "v_add_f32 v73, v73, v254\n"
        "v_add_f32 v66, v66, v254\n"
        "v_mfma_f32_16x16x32_bf16 v[102:105], a[180:183], a[84:87], v[102:105]\n"
        "buffer_load_dwordx4 v[170:173], v36, s[36:39], 0 offen offset:16\n"
        "v_add_f32 v67, v67, v254\n"
        "v_add_f32 v68, v68, v254\n"
        "v_mfma_f32_16x16x32_bf16 v[106:109], a[192:195], a[84:87], 0\n"
        "s_mov_b32 s78, 0xf0000f\n"
        "v_add_f32 v69, v69, v254\n"
        "v_add_f32 v78, v78, v254\n"
        "v_add_f32 v79, v79, v254\n"
        "v_mfma_f32_16x16x32_bf16 v[106:109], a[196:199], a[88:91], v[106:109]\n"
        "s_mov_b32 s79, 0xf0000f00\n"
        "v_add_f32 v80, v80, v254\n"
        "v_add_f32 v81, v81, v254\n"
        "v_add_f32 v74, v74, v254\n"
        "v_mfma_f32_16x16x32_bf16 v[106:109], a[200:203], a[92:95], v[106:109]\n"
        "s_mov_b32 s76, 0x11111111\n"
        "v_add_f32 v75, v75, v254\n"
        "v_add_f32 v76, v76, v254\n"
        "v_add_f32 v77, v77, v254\n"
        "v_mfma_f32_16x16x32_bf16 v[110:113], a[212:215], a[92:95], 0\n"
        "s_mov_b32 s77, 0x11111111\n"
        "v_cvt_pk_bf16_f32 v54, v50, v51\n"
        "v_cvt_pk_bf16_f32 v55, v52, v53\n"
        "v_cvt_pk_bf16_f32 v56, v58, v59\n"
        "v_mfma_f32_16x16x32_bf16 v[110:113], a[208:211], a[88:91], v[110:113]\n"
        "s_mov_b32 s56, 0x22222222\n"
        "v_cvt_pk_bf16_f32 v57, v60, v61\n"
        "v_cvt_pk_bf16_f32 v62, v70, v71\n"
        "v_cvt_pk_bf16_f32 v63, v72, v73\n"
        "v_mfma_f32_16x16x32_bf16 v[110:113], a[204:207], a[84:87], v[110:113]\n"
        "s_mov_b32 s57, 0x22222222\n"
        "v_cvt_pk_bf16_f32 v64, v66, v67\n"
        "v_cvt_pk_bf16_f32 v65, v68, v69\n"
        "v_cvt_pk_bf16_f32 v50, v78, v79\n"
        "v_mfma_f32_16x16x32_bf16 v[58:61], a[144:147], a[96:99], 0\n"
        "s_mov_b32 s58, 0x44444444\n"
        "v_cvt_pk_bf16_f32 v51, v80, v81\n"
        "v_cvt_pk_bf16_f32 v52, v74, v75\n"
        "v_cvt_pk_bf16_f32 v53, v76, v77\n"
        "v_mfma_f32_16x16x32_bf16 v[58:61], a[148:151], a[100:103], v[58:61]\n"
        "s_mov_b32 s59, 0x44444444\n"
        "v_add_f32 v252, v252, v46\n"
        "v_add_f32 v252, v252, v47\n"
        "v_add_f32 v252, v252, v48\n"
        "v_mfma_f32_16x16x32_bf16 v[58:61], a[152:155], a[104:107], v[58:61]\n"
        "s_mov_b32 s60, 0x88888888\n"
        "buffer_load_dwordx4 v[176:179], v36, s[36:39], 0 offen offset:32\n"
        "v_add_f32 v252, v252, v49\n"
        "v_add_f32 v252, v252, v92\n"
        "v_mfma_f32_16x16x32_bf16 v[66:69], a[164:167], a[104:107], 0\n"
        "s_mov_b32 s61, 0x88888888\n"
        "v_add_f32 v252, v252, v93\n"
        "v_add_f32 v252, v252, v94\n"
        "v_add_f32 v252, v252, v95\n"
        "v_mfma_f32_16x16x32_bf16 v[66:69], a[160:163], a[100:103], v[66:69]\n"
        "s_mov_b32 s62, 0xffff\n"
        "v_add_f32 v252, v252, v102\n"
        "v_add_f32 v252, v252, v103\n"
        "v_add_f32 v252, v252, v104\n"
        "v_mfma_f32_16x16x32_bf16 v[66:69], a[156:159], a[96:99], v[66:69]\n"
        "s_mov_b32 s63, 0x0\n"
        "v_add_f32 v252, v252, v105\n"
        "v_add_f32 v252, v252, v106\n"
        "v_add_f32 v252, v252, v107\n"
        "v_mfma_f32_16x16x32_bf16 v[70:73], a[168:171], a[96:99], 0\n"
        "s_mov_b32 s64, 0xffff0000\n"
        "v_add_f32 v252, v252, v108\n"
        "v_add_f32 v252, v252, v109\n"
        "v_add_f32 v252, v252, v110\n"
        "v_mfma_f32_16x16x32_bf16 v[70:73], a[172:175], a[100:103], v[70:73]\n"
        "s_mov_b32 s65, 0x0\n"
        "v_add_f32 v252, v252, v111\n"
        "v_add_f32 v252, v252, v112\n"
        "v_add_f32 v252, v252, v113\n"
        "v_mfma_f32_16x16x32_bf16 v[70:73], a[176:179], a[104:107], v[70:73]\n"
        "s_barrier\n"
        "ds_write_b128 v39, v[54:57]\n"
        "v_mov_b32 v254, v252\n"
        "v_mfma_f32_16x16x32_bf16 v[78:81], a[188:191], a[104:107], 0\n"
        "s_mov_b32 s66, 0x0\n"
        "ds_write_b128 v39, v[62:65] offset:1024\n"
        "v_add_f32 v253, v58, v59\n"
        "v_mfma_f32_16x16x32_bf16 v[78:81], a[184:187], a[100:103], v[78:81]\n"
        "s_mov_b32 s67, 0xffff\n"
        "ds_write_b128 v39, v[50:53] offset:2048\n"
        "v_add_f32 v253, v253, v60\n"
        "v_mfma_f32_16x16x32_bf16 v[78:81], a[180:183], a[96:99], v[78:81]\n"
        "s_mov_b32 s68, 0x0\n"
        "buffer_load_dwordx4 v[184:187], v37, s[36:39], 0 offen\n"
        "v_add_f32 v253, v253, v61\n"
        "v_add_f32 v253, v253, v66\n"
        "v_mfma_f32_16x16x32_bf16 v[118:121], a[192:195], a[96:99], 0\n"
        "s_mov_b32 s69, 0xffff0000\n"
        "v_permlane32_swap_b32 v252, v254\n"
        "v_add_f32 v252, v252, v254\n"
        "v_mfma_f32_16x16x32_bf16 v[118:121], a[196:199], a[100:103], v[118:121]\n"
        "s_mov_b32 s70, 0x0\n"
        "v_mov_b32 v254, v252\n"
        "v_add_f32 v253, v253, v67\n"
        "v_add_f32 v253, v253, v68\n"
        "v_mfma_f32_16x16x32_bf16 v[118:121], a[200:203], a[104:107], v[118:121]\n"
        "s_mov_b32 s71, 0xffffffff\n"
        "v_permlane16_swap_b32 v252, v254\n"
        "v_add_f32 v252, v252, v254\n"
        "v_mfma_f32_16x16x32_bf16 v[114:117], a[212:215], a[104:107], 0\n"
        "s_mov_b32 s72, 0x0\n"
        "v_mul_f32 v252, 0xbc2aaaab, v252\n"
        "v_add_f32 v30, v30, v252\n"
        "v_add_f32 v31, v31, v252\n"
        "v_mfma_f32_16x16x32_bf16 v[114:117], a[208:211], a[100:103], v[114:117]\n"
        "s_mov_b32 s73, 0x20000\n"
        "v_add_f32 v32, v32, v252\n"
        "v_add_f32 v33, v33, v252\n"
        "v_add_f32 v46, v46, v252\n"
        "v_mfma_f32_16x16x32_bf16 v[114:117], a[204:207], a[96:99], v[114:117]\n"
        "s_mov_b64 vcc, s[78:79]\n"
        "v_add_f32 v47, v47, v252\n"
        "v_add_f32 v48, v48, v252\n"
        "v_add_f32 v49, v49, v252\n"
        "v_mfma_f32_16x16x32_bf16 v[74:77], a[144:147], a[108:111], 0\n"
        "s_mov_b32 s80, 0x3c2aaaab\n"
        "v_add_f32 v92, v92, v252\n"
        "v_add_f32 v93, v93, v252\n"
        "v_add_f32 v94, v94, v252\n"
        "v_mfma_f32_16x16x32_bf16 v[74:77], a[148:151], a[112:115], v[74:77]\n"
        "s_mov_b32 s81, 0x3727c5ac\n"
        "v_add_f32 v95, v95, v252\n"
        "v_add_f32 v102, v102, v252\n"
        "v_add_f32 v103, v103, v252\n"
        "v_mfma_f32_16x16x32_bf16 v[74:77], a[152:155], a[116:119], v[74:77]\n"
        "s_mov_b32 s82, 0x43800000\n"
        "buffer_load_dwordx4 v[190:193], v37, s[36:39], 0 offen offset:16\n"
        "v_add_f32 v104, v104, v252\n"
        "v_add_f32 v105, v105, v252\n"
        "v_mfma_f32_16x16x32_bf16 v[82:85], a[164:167], a[116:119], 0\n"
        "s_mov_b32 s84, 0x80000000\n"
        "v_add_f32 v106, v106, v252\n"
        "v_add_f32 v107, v107, v252\n"
        "v_add_f32 v108, v108, v252\n"
        "v_mfma_f32_16x16x32_bf16 v[82:85], a[160:163], a[112:115], v[82:85]\n"
        "s_mov_b32 s85, 19\n"
        "v_add_f32 v109, v109, v252\n"
        "v_add_f32 v110, v110, v252\n"
        "v_add_f32 v111, v111, v252\n"
        "v_mfma_f32_16x16x32_bf16 v[82:85], a[156:159], a[108:111], v[82:85]\n"
        "s_mov_b32 s86, 0x80000000\n"
        "v_add_f32 v112, v112, v252\n"
        "v_add_f32 v113, v113, v252\n"
        "v_cvt_pk_bf16_f32 v86, v30, v31\n"
        "v_mfma_f32_16x16x32_bf16 v[98:101], a[168:171], a[108:111], 0\n"
        "s_mov_b32 s89, 0x80000000\n"
        "v_cvt_pk_bf16_f32 v87, v32, v33\n"
        "v_cvt_pk_bf16_f32 v88, v46, v47\n"
        "v_cvt_pk_bf16_f32 v89, v48, v49\n"
        "v_mfma_f32_16x16x32_bf16 v[98:101], a[172:175], a[112:115], v[98:101]\n"
        "s_mov_b32 s29, 1\n"
        "ds_write_b128 v40, v[86:89]\n"
        "v_cvt_pk_bf16_f32 v90, v92, v93\n"
        "v_mfma_f32_16x16x32_bf16 v[98:101], a[176:179], a[116:119], v[98:101]\n"
        "s_mov_b32 s31, 0x400000\n"
        "v_cvt_pk_bf16_f32 v91, v94, v95\n"
        "v_cvt_pk_bf16_f32 v92, v102, v103\n"
        "v_cvt_pk_bf16_f32 v93, v104, v105\n"
        "v_mfma_f32_16x16x32_bf16 v[94:97], a[188:191], a[116:119], 0\n"
        "s_mov_b32 s87, s49\n"
        "v_cvt_pk_bf16_f32 v46, v106, v107\n"
        "v_cvt_pk_bf16_f32 v47, v108, v109\n"
        "v_cvt_pk_bf16_f32 v48, v110, v111\n"
        "v_mfma_f32_16x16x32_bf16 v[94:97], a[184:187], a[112:115], v[94:97]\n"
        "s_mov_b32 s88, s50\n"
        "v_cvt_pk_bf16_f32 v49, v112, v113\n"
        "v_add_f32 v253, v253, v69\n"
        "v_add_f32 v253, v253, v70\n"
        "v_mfma_f32_16x16x32_bf16 v[94:97], a[180:183], a[108:111], v[94:97]\n"
        "s_mov_b64 s[44:45], s[40:41]\n"
        "buffer_load_dwordx4 v[196:199], v37, s[36:39], 0 offen offset:32\n"
        "v_add_f32 v253, v253, v71\n"
        "v_add_f32 v253, v253, v72\n"
        "v_mfma_f32_16x16x32_bf16 v[102:105], a[192:195], a[108:111], 0\n"
        "s_mov_b32 s46, 0\n"
        "v_add_f32 v253, v253, v73\n"
        "v_add_f32 v253, v253, v78\n"
        "v_add_f32 v253, v253, v79\n"
        "v_mfma_f32_16x16x32_bf16 v[102:105], a[196:199], a[112:115], v[102:105]\n"
        "s_mov_b32 s47, s43\n"
        "v_add_f32 v253, v253, v80\n"
        "v_add_f32 v253, v253, v81\n"
        "v_add_f32 v253, v253, v118\n"
        "v_mfma_f32_16x16x32_bf16 v[102:105], a[200:203], a[116:119], v[102:105]\n"
        "v_add_f32 v253, v253, v119\n"
        "v_add_f32 v253, v253, v120\n"
        "v_add_f32 v253, v253, v121\n"
        "v_mfma_f32_16x16x32_bf16 v[30:33], a[212:215], a[116:119], 0\n"
        "v_add_f32 v253, v253, v114\n"
        "v_add_f32 v253, v253, v115\n"
        "v_add_f32 v253, v253, v116\n"
        "v_mfma_f32_16x16x32_bf16 v[30:33], a[208:211], a[112:115], v[30:33]\n"
        "v_add_f32 v253, v253, v117\n"
        "v_mov_b32 v254, v253\n"
        "v_add_f32 v38, v74, v75\n"
        "v_mfma_f32_16x16x32_bf16 v[30:33], a[204:207], a[108:111], v[30:33]\n"
        "v_permlane32_swap_b32 v253, v254\n"
        "v_add_f32 v253, v253, v254\n"
        "v_mfma_f32_16x16x32_bf16 v[106:109], a[144:147], a[120:123], 0\n"
        "v_mov_b32 v254, v253\n"
        "v_add_f32 v38, v38, v76\n"
        "v_add_f32 v38, v38, v77\n"
        "v_mfma_f32_16x16x32_bf16 v[106:109], a[148:151], a[124:127], v[106:109]\n"
        "s_add_u32 m0, s52, 0\n"
        "v_permlane16_swap_b32 v253, v254\n"
        "v_add_f32 v253, v253, v254\n"
        "v_mfma_f32_16x16x32_bf16 v[106:109], a[152:155], a[128:131], v[106:109]\n"
        "buffer_load_dwordx4 v0, s[36:39], s49 offen lds\n"
        "v_mul_f32 v253, 0xbc2aaaab, v253\n"
        "v_add_f32 v58, v58, v253\n"
        "v_mfma_f32_16x16x32_bf16 v[34:37], a[164:167], a[128:131], 0\n"
        "v_add_f32 v59, v59, v253\n"
        "v_add_f32 v60, v60, v253\n"
        "v_add_f32 v61, v61, v253\n"
        "v_mfma_f32_16x16x32_bf16 v[34:37], a[160:163], a[124:127], v[34:37]\n"
        "v_add_f32 v66, v66, v253\n"
        "v_add_f32 v67, v67, v253\n"
        "v_add_f32 v68, v68, v253\n"
        "v_mfma_f32_16x16x32_bf16 v[34:37], a[156:159], a[120:123], v[34:37]\n"
        "v_add_f32 v69, v69, v253\n"
        "v_add_f32 v70, v70, v253\n"
        "v_add_f32 v71, v71, v253\n"
        "v_mfma_f32_16x16x32_bf16 v[130:133], a[168:171], a[120:123], 0\n"
        "v_add_f32 v72, v72, v253\n"
        "v_add_f32 v73, v73, v253\n"
        "v_add_f32 v78, v78, v253\n"
        "v_mfma_f32_16x16x32_bf16 v[130:133], a[172:175], a[124:127], v[130:133]\n"
        "v_add_f32 v79, v79, v253\n"
        "v_add_f32 v80, v80, v253\n"
        "v_add_f32 v81, v81, v253\n"
        "v_mfma_f32_16x16x32_bf16 v[130:133], a[176:179], a[128:131], v[130:133]\n"
        "buffer_load_dwordx4 v1, s[36:39], s49 offen offset:1024 lds\n"
        "v_add_f32 v118, v118, v253\n"
        "v_add_f32 v119, v119, v253\n"
        "v_mfma_f32_16x16x32_bf16 v[122:125], a[188:191], a[128:131], 0\n"
        "v_add_f32 v120, v120, v253\n"
        "v_add_f32 v121, v121, v253\n"
        "v_add_f32 v114, v114, v253\n"
        "v_mfma_f32_16x16x32_bf16 v[122:125], a[184:187], a[124:127], v[122:125]\n"
        "v_add_f32 v115, v115, v253\n"
        "v_add_f32 v116, v116, v253\n"
        "v_add_f32 v117, v117, v253\n"
        "v_mfma_f32_16x16x32_bf16 v[122:125], a[180:183], a[120:123], v[122:125]\n"
        "v_cvt_pk_bf16_f32 v110, v58, v59\n"
        "v_cvt_pk_bf16_f32 v111, v60, v61\n"
        "v_cvt_pk_bf16_f32 v112, v66, v67\n"
        "v_mfma_f32_16x16x32_bf16 v[126:129], a[192:195], a[120:123], 0\n"
        "v_cvt_pk_bf16_f32 v113, v68, v69\n"
        "v_cvt_pk_bf16_f32 v58, v70, v71\n"
        "v_cvt_pk_bf16_f32 v59, v72, v73\n"
        "v_mfma_f32_16x16x32_bf16 v[126:129], a[196:199], a[124:127], v[126:129]\n"
        "v_cvt_pk_bf16_f32 v60, v78, v79\n"
        "v_cvt_pk_bf16_f32 v61, v80, v81\n"
        "v_cvt_pk_bf16_f32 v66, v118, v119\n"
        "v_mfma_f32_16x16x32_bf16 v[126:129], a[200:203], a[128:131], v[126:129]\n"
        "buffer_load_dwordx4 v2, s[36:39], s49 offen offset:2048 lds\n"
        "v_cvt_pk_bf16_f32 v67, v120, v121\n"
        "v_cvt_pk_bf16_f32 v68, v114, v115\n"
        "v_mfma_f32_16x16x32_bf16 v[118:121], a[212:215], a[128:131], 0\n"
        "v_cvt_pk_bf16_f32 v69, v116, v117\n"
        "v_add_f32 v38, v38, v82\n"
        "v_add_f32 v38, v38, v83\n"
        "v_mfma_f32_16x16x32_bf16 v[118:121], a[208:211], a[124:127], v[118:121]\n"
        "v_add_f32 v38, v38, v84\n"
        "v_add_f32 v38, v38, v85\n"
        "v_add_f32 v38, v38, v98\n"
        "v_mfma_f32_16x16x32_bf16 v[118:121], a[204:207], a[120:123], v[118:121]\n"
        "v_add_f32 v38, v38, v99\n"
        "v_add_f32 v38, v38, v100\n"
        "v_add_f32 v38, v38, v101\n"
        "v_mfma_f32_16x16x32_bf16 v[70:73], a[144:147], a[132:135], 0\n"
        "v_add_f32 v38, v38, v94\n"
        "v_add_f32 v38, v38, v95\n"
        "v_add_f32 v38, v38, v96\n"
        "v_mfma_f32_16x16x32_bf16 v[70:73], a[148:151], a[136:139], v[70:73]\n"
        "v_add_f32 v38, v38, v97\n"
        "v_add_f32 v38, v38, v102\n"
        "v_add_f32 v38, v38, v103\n"
        "v_mfma_f32_16x16x32_bf16 v[70:73], a[152:155], a[140:143], v[70:73]\n"
        "buffer_load_dwordx4 v3, s[36:39], s49 offen offset:3072 lds\n"
        "v_add_f32 v38, v38, v104\n"
        "v_add_f32 v38, v38, v105\n"
        "v_mfma_f32_16x16x32_bf16 v[114:117], a[164:167], a[140:143], 0\n"
        "v_add_f32 v38, v38, v30\n"
        "v_add_f32 v38, v38, v31\n"
        "v_add_f32 v38, v38, v32\n"
        "v_mfma_f32_16x16x32_bf16 v[114:117], a[160:163], a[136:139], v[114:117]\n"
        "v_add_f32 v38, v38, v33\n"
        "v_mov_b32 v39, v38\n"
        "v_add_f32 v150, v106, v107\n"
        "v_mfma_f32_16x16x32_bf16 v[114:117], a[156:159], a[132:135], v[114:117]\n"
        "v_permlane32_swap_b32 v38, v39\n"
        "v_add_f32 v38, v38, v39\n"
        "v_mfma_f32_16x16x32_bf16 v[138:141], a[168:171], a[132:135], 0\n"
        "v_mov_b32 v39, v38\n"
        "v_add_f32 v150, v150, v108\n"
        "v_add_f32 v150, v150, v109\n"
        "v_mfma_f32_16x16x32_bf16 v[138:141], a[172:175], a[136:139], v[138:141]\n"
        "s_add_u32 m0, s52, 4096\n"
        "v_permlane16_swap_b32 v38, v39\n"
        "v_add_f32 v38, v38, v39\n"
        "v_mfma_f32_16x16x32_bf16 v[138:141], a[176:179], a[140:143], v[138:141]\n"
        "buffer_load_dwordx4 v4, s[36:39], s49 offen lds\n"
        "v_mul_f32 v38, 0xbc2aaaab, v38\n"
        "v_add_f32 v150, v150, v34\n"
        "v_mfma_f32_16x16x32_bf16 v[78:81], a[188:191], a[140:143], 0\n"
        "v_add_f32 v150, v150, v35\n"
        "v_add_f32 v150, v150, v36\n"
        "v_add_f32 v150, v150, v37\n"
        "v_mfma_f32_16x16x32_bf16 v[78:81], a[184:187], a[136:139], v[78:81]\n"
        "v_add_f32 v150, v150, v130\n"
        "v_add_f32 v150, v150, v131\n"
        "v_add_f32 v150, v150, v132\n"
        "v_mfma_f32_16x16x32_bf16 v[78:81], a[180:183], a[132:135], v[78:81]\n"
        "v_add_f32 v150, v150, v133\n"
        "v_add_f32 v150, v150, v122\n"
        "v_add_f32 v150, v150, v123\n"
        "v_mfma_f32_16x16x32_bf16 v[142:145], a[192:195], a[132:135], 0\n"
        "v_add_f32 v150, v150, v124\n"
        "v_add_f32 v150, v150, v125\n"
        "v_add_f32 v150, v150, v126\n"
        "v_mfma_f32_16x16x32_bf16 v[142:145], a[196:199], a[136:139], v[142:145]\n"
        "v_add_f32 v150, v150, v127\n"
        "v_add_f32 v150, v150, v128\n"
        "v_add_f32 v150, v150, v129\n"
        "v_mfma_f32_16x16x32_bf16 v[142:145], a[200:203], a[140:143], v[142:145]\n"
        "buffer_load_dwordx4 v5, s[36:39], s49 offen offset:1024 lds\n"
        "v_add_f32 v150, v150, v118\n"
        "v_add_f32 v150, v150, v119\n"
        "v_mfma_f32_16x16x32_bf16 v[134:137], a[212:215], a[140:143], 0\n"
        "v_add_f32 v150, v150, v120\n"
        "v_add_f32 v150, v150, v121\n"
        "v_mov_b32 v151, v150\n"
        "v_mfma_f32_16x16x32_bf16 v[134:137], a[208:211], a[136:139], v[134:137]\n"
        "ds_write_b128 v40, v[90:93] offset:1024\n"
        "v_add_f32 v152, v70, v71\n"
        "v_mfma_f32_16x16x32_bf16 v[134:137], a[204:207], a[132:135], v[134:137]\n"
        "ds_write_b128 v40, v[46:49] offset:2048\n"
        "v_add_f32 v152, v152, v72\n"
        "v_pk_add_f32 v[74:75], v[74:75], v[38:39] op_sel_hi:[1,0]\n"
        "v_cvt_pk_bf16_f32 v146, v74, v75\n"
        "v_pk_add_f32 v[76:77], v[76:77], v[38:39] op_sel_hi:[1,0]\n"
        "v_cvt_pk_bf16_f32 v147, v76, v77\n"
        "v_pk_add_f32 v[82:83], v[82:83], v[38:39] op_sel_hi:[1,0]\n"
        "v_cvt_pk_bf16_f32 v148, v82, v83\n"
        "v_pk_add_f32 v[84:85], v[84:85], v[38:39] op_sel_hi:[1,0]\n"
        "v_cvt_pk_bf16_f32 v149, v84, v85\n"
        "v_pk_add_f32 v[98:99], v[98:99], v[38:39] op_sel_hi:[1,0]\n"
        "v_cvt_pk_bf16_f32 v82, v98, v99\n"
        "v_pk_add_f32 v[100:101], v[100:101], v[38:39] op_sel_hi:[1,0]\n"
        "v_cvt_pk_bf16_f32 v83, v100, v101\n"
        "v_pk_add_f32 v[94:95], v[94:95], v[38:39] op_sel_hi:[1,0]\n"
        "v_cvt_pk_bf16_f32 v84, v94, v95\n"
        "v_pk_add_f32 v[96:97], v[96:97], v[38:39] op_sel_hi:[1,0]\n"
        "v_cvt_pk_bf16_f32 v85, v96, v97\n"
        "v_pk_add_f32 v[102:103], v[102:103], v[38:39] op_sel_hi:[1,0]\n"
        "v_cvt_pk_bf16_f32 v74, v102, v103\n"
        "v_pk_add_f32 v[104:105], v[104:105], v[38:39] op_sel_hi:[1,0]\n"
        "v_cvt_pk_bf16_f32 v75, v104, v105\n"
        "v_pk_add_f32 v[30:31], v[30:31], v[38:39] op_sel_hi:[1,0]\n"
        "v_cvt_pk_bf16_f32 v76, v30, v31\n"
        "v_pk_add_f32 v[32:33], v[32:33], v[38:39] op_sel_hi:[1,0]\n"
        "v_cvt_pk_bf16_f32 v77, v32, v33\n"
        "v_permlane32_swap_b32 v150, v151\n"
        "v_add_f32 v150, v150, v151\n"
        "v_mov_b32 v151, v150\n"
        "s_nop 1\n"
        "v_permlane16_swap_b32 v150, v151\n"
        "v_add_f32 v150, v150, v151\n"
        "v_mul_f32 v150, 0xbc2aaaab, v150\n"
        "v_pk_add_f32 v[106:107], v[106:107], v[150:151] op_sel_hi:[1,0]\n"
        "v_cvt_pk_bf16_f32 v94, v106, v107\n"
        "v_pk_add_f32 v[108:109], v[108:109], v[150:151] op_sel_hi:[1,0]\n"
        "v_cvt_pk_bf16_f32 v95, v108, v109\n"
        "v_pk_add_f32 v[34:35], v[34:35], v[150:151] op_sel_hi:[1,0]\n"
        "v_cvt_pk_bf16_f32 v96, v34, v35\n"
        "v_pk_add_f32 v[36:37], v[36:37], v[150:151] op_sel_hi:[1,0]\n"
        "v_cvt_pk_bf16_f32 v97, v36, v37\n"
        "v_pk_add_f32 v[130:131], v[130:131], v[150:151] op_sel_hi:[1,0]\n"
        "v_cvt_pk_bf16_f32 v30, v130, v131\n"
        "v_pk_add_f32 v[132:133], v[132:133], v[150:151] op_sel_hi:[1,0]\n"
        "v_cvt_pk_bf16_f32 v31, v132, v133\n"
        "v_pk_add_f32 v[122:123], v[122:123], v[150:151] op_sel_hi:[1,0]\n"
        "v_cvt_pk_bf16_f32 v32, v122, v123\n"
        "v_pk_add_f32 v[124:125], v[124:125], v[150:151] op_sel_hi:[1,0]\n"
        "v_cvt_pk_bf16_f32 v33, v124, v125\n"
        "v_pk_add_f32 v[126:127], v[126:127], v[150:151] op_sel_hi:[1,0]\n"
        "v_cvt_pk_bf16_f32 v34, v126, v127\n"
        "v_pk_add_f32 v[128:129], v[128:129], v[150:151] op_sel_hi:[1,0]\n"
        "v_cvt_pk_bf16_f32 v35, v128, v129\n"
        "v_pk_add_f32 v[118:119], v[118:119], v[150:151] op_sel_hi:[1,0]\n"
        "v_cvt_pk_bf16_f32 v36, v118, v119\n"
        "v_pk_add_f32 v[120:121], v[120:121], v[150:151] op_sel_hi:[1,0]\n"
        "v_cvt_pk_bf16_f32 v37, v120, v121\n"
        "v_add_f32 v152, v152, v73\n"
        "v_add_f32 v152, v152, v114\n"
        "v_add_f32 v152, v152, v115\n"
        "v_add_f32 v152, v152, v116\n"
        "v_add_f32 v152, v152, v117\n"
        "v_add_f32 v152, v152, v138\n"
        "v_add_f32 v152, v152, v139\n"
        "v_add_f32 v152, v152, v140\n"
        "v_add_f32 v152, v152, v141\n"
        "v_add_f32 v152, v152, v78\n"
        "v_add_f32 v152, v152, v79\n"
        "v_add_f32 v152, v152, v80\n"
        "v_add_f32 v152, v152, v81\n"
        "v_add_f32 v152, v152, v142\n"
        "v_add_f32 v152, v152, v143\n"
        "v_add_f32 v152, v152, v144\n"
        "v_add_f32 v152, v152, v145\n"
        "v_add_f32 v152, v152, v134\n"
        "v_add_f32 v152, v152, v135\n"
        "v_add_f32 v152, v152, v136\n"
        "v_add_f32 v152, v152, v137\n"
        "v_mov_b32 v153, v152\n"
        "s_nop 1\n"
        "v_permlane32_swap_b32 v152, v153\n"
        "v_add_f32 v152, v152, v153\n"
        "v_mov_b32 v153, v152\n"
        "s_nop 1\n"
        "v_permlane16_swap_b32 v152, v153\n"
        "v_add_f32 v152, v152, v153\n"
        "v_mul_f32 v152, 0xbc2aaaab, v152\n"
        "v_pk_add_f32 v[70:71], v[70:71], v[152:153] op_sel_hi:[1,0]\n"
        "v_cvt_pk_bf16_f32 v98, v70, v71\n"
        "v_pk_add_f32 v[72:73], v[72:73], v[152:153] op_sel_hi:[1,0]\n"
        "v_cvt_pk_bf16_f32 v99, v72, v73\n"
        "v_pk_add_f32 v[114:115], v[114:115], v[152:153] op_sel_hi:[1,0]\n"
        "v_cvt_pk_bf16_f32 v100, v114, v115\n"
        "v_pk_add_f32 v[116:117], v[116:117], v[152:153] op_sel_hi:[1,0]\n"
        "v_cvt_pk_bf16_f32 v101, v116, v117\n"
        "v_pk_add_f32 v[138:139], v[138:139], v[152:153] op_sel_hi:[1,0]\n"
        "v_cvt_pk_bf16_f32 v70, v138, v139\n"
        "v_pk_add_f32 v[140:141], v[140:141], v[152:153] op_sel_hi:[1,0]\n"
        "v_cvt_pk_bf16_f32 v71, v140, v141\n"
        "v_pk_add_f32 v[78:79], v[78:79], v[152:153] op_sel_hi:[1,0]\n"
        "v_cvt_pk_bf16_f32 v72, v78, v79\n"
        "v_pk_add_f32 v[80:81], v[80:81], v[152:153] op_sel_hi:[1,0]\n"
        "v_cvt_pk_bf16_f32 v73, v80, v81\n"
        "v_pk_add_f32 v[142:143], v[142:143], v[152:153] op_sel_hi:[1,0]\n"
        "v_cvt_pk_bf16_f32 v78, v142, v143\n"
        "v_pk_add_f32 v[144:145], v[144:145], v[152:153] op_sel_hi:[1,0]\n"
        "v_cvt_pk_bf16_f32 v79, v144, v145\n"
        "v_pk_add_f32 v[134:135], v[134:135], v[152:153] op_sel_hi:[1,0]\n"
        "v_cvt_pk_bf16_f32 v80, v134, v135\n"
        "v_pk_add_f32 v[136:137], v[136:137], v[152:153] op_sel_hi:[1,0]\n"
        "v_cvt_pk_bf16_f32 v81, v136, v137\n"
        "s_nop 1\n"
        "v_mfma_f32_16x16x32_bf16 v[102:105], v[54:57], v[54:57], 0\n"
        "ds_write_b128 v41, v[110:113]\n"
        "v_mfma_f32_16x16x32_bf16 v[102:105], v[62:65], v[62:65], v[102:105]\n"
        "ds_write_b128 v41, v[58:61] offset:1024\n"
        "v_mfma_f32_16x16x32_bf16 v[102:105], v[50:53], v[50:53], v[102:105]\n"
        "buffer_load_dwordx4 v6, s[36:39], s49 offen offset:2048 lds\n"
        "v_mfma_f32_16x16x32_bf16 v[102:105], v[86:89], v[86:89], v[102:105]\n"
        "ds_write_b128 v41, v[66:69] offset:2048\n"
        "v_mfma_f32_16x16x32_bf16 v[102:105], v[90:93], v[90:93], v[102:105]\n"
        "ds_write_b128 v42, v[146:149]\n"
        "v_mfma_f32_16x16x32_bf16 v[102:105], v[46:49], v[46:49], v[102:105]\n"
        "ds_write_b128 v42, v[82:85] offset:1024\n"
        "v_mfma_f32_16x16x32_bf16 v[102:105], v[110:113], v[110:113], v[102:105]\n"
        "ds_write_b128 v42, v[74:77] offset:2048\n"
        "v_mfma_f32_16x16x32_bf16 v[102:105], v[58:61], v[58:61], v[102:105]\n"
        "ds_write_b128 v43, v[94:97]\n"
        "v_mfma_f32_16x16x32_bf16 v[102:105], v[66:69], v[66:69], v[102:105]\n"
        "buffer_load_dwordx4 v7, s[36:39], s49 offen offset:3072 lds\n"
        "v_mfma_f32_16x16x32_bf16 v[102:105], v[146:149], v[146:149], v[102:105]\n"
        "ds_write_b128 v43, v[30:33] offset:1024\n"
        "v_mfma_f32_16x16x32_bf16 v[102:105], v[82:85], v[82:85], v[102:105]\n"
        "ds_write_b128 v43, v[34:37] offset:2048\n"
        "v_mfma_f32_16x16x32_bf16 v[102:105], v[74:77], v[74:77], v[102:105]\n"
        "ds_write_b128 v44, v[98:101]\n"
        "v_mfma_f32_16x16x32_bf16 v[102:105], v[94:97], v[94:97], v[102:105]\n"
        "ds_write_b128 v44, v[70:73] offset:1024\n"
        "v_mfma_f32_16x16x32_bf16 v[102:105], v[30:33], v[30:33], v[102:105]\n"
        "s_add_u32 m0, s52, 8192\n"
        "ds_write_b128 v44, v[78:81] offset:2048\n"
        "v_mfma_f32_16x16x32_bf16 v[102:105], v[34:37], v[34:37], v[102:105]\n"
        "buffer_load_dwordx4 v8, s[36:39], s49 offen lds\n"
        "v_mfma_f32_16x16x32_bf16 v[102:105], v[98:101], v[98:101], v[102:105]\n"
        "v_mfma_f32_16x16x32_bf16 v[102:105], v[70:73], v[70:73], v[102:105]\n"
        "v_mfma_f32_16x16x32_bf16 v[102:105], v[78:81], v[78:81], v[102:105]\n"
        "s_nop 2\n"
        "s_waitcnt vmcnt(9)\n"
        "s_nop 3\n"
        "v_max3_f32 v254, v102, v103, v104\n"
        "v_max_f32 v254, v254, v105\n"
        "v_cmp_nge_f32_e64 s[74:75], s24, v254\n"
        "v_mov_b32 v254, 161632\n"
        "s_nop 0\n"
        "v_cndmask_b32_e64 v254, v255, v254, s[74:75]\n"
        "v_mov_b32 v255, 0\n"
        "ds_write_b32 v254, v255\n"
        "s_waitcnt lgkmcnt(0)\n"
        "s_barrier\n"
        "FE_%=:\n"
        "s_mov_b64 s[16:17], s[54:55]\n"
        "s_cmp_eq_u32 s51, 0\n"
        "s_cselect_b32 s18, 128, 0\n"
        "s_mov_b32 s49, 161888\n"
        "s_cselect_b32 s49, 163424, s49\n"
        "s_movk_i32 s22, 1584\n"
        "s_movk_i32 s23, 1584\n"
        "s_mov_b32 s53, 19\n"
        "s_mov_b32 s54, 0x05040302\n"
        "s_mov_b32 s55, 0x07060302\n"
        "v_mov_b32 v28, s80\n"
        "ds_read_b128 v[72:75], v26\n"
        "ds_read_b128 v[60:63], v26 offset:64\n"
        "ds_read_b128 v[88:91], v26 offset:128\n"
        "v_xor_b32 v250, 64, v206\n"
        "ds_read_b128 a[0:3], v206\n"
        "ds_read_b128 a[4:7], v206 offset:1024\n"
        "ds_read_b128 a[8:11], v206 offset:2048\n"
        "ds_read_b128 a[12:15], v206 offset:3072\n"
        "ds_read_b128 a[16:19], v206 offset:4096\n"
        "ds_read_b128 a[20:23], v206 offset:5120\n"
        "ds_read_b128 a[24:27], v206 offset:6144\n"
        "ds_read_b128 a[28:31], v206 offset:7168\n"
        "ds_read_b128 a[32:35], v206 offset:8192\n"
        "v_mfma_f32_16x16x32_bf16 v[30:33], v[164:167], v[164:167], 0\n"
        "ds_read_b128 a[36:39], v206 offset:9216\n"
        "v_mov_b32 v255, 161632\n"
        "v_mfma_f32_16x16x32_bf16 v[30:33], v[170:173], v[170:173], v[30:33]\n"
        "ds_read_b128 a[40:43], v206 offset:10240\n"
        "v_lshrrev_b32 v254, 2, v206\n"
        "v_mfma_f32_16x16x32_bf16 v[30:33], v[176:179], v[176:179], v[30:33]\n"
        "ds_read_b128 a[44:47], v206 offset:11264\n"
        "v_and_b32 v254, 128, v254\n"
        "v_mfma_f32_16x16x32_bf16 v[34:37], v[184:187], v[184:187], 0\n"
        "ds_read_b128 a[48:51], v206 offset:12288\n"
        "v_xor_b32 v254, v254, v206\n"
        "v_mfma_f32_16x16x32_bf16 v[34:37], v[190:193], v[190:193], v[34:37]\n"
        "s_nop 0\n"
        "ds_read_b128 a[52:55], v206 offset:13312\n"
        "v_cndmask_b32_e64 v249, v30, v31, s[56:57]\n"
        "v_mfma_f32_16x16x32_bf16 v[34:37], v[196:199], v[196:199], v[34:37]\n"
        "s_waitcnt lgkmcnt(14)\n"
        "ds_read_b128 a[56:59], v206 offset:14336\n"
        "v_cndmask_b32_e64 v249, v249, v32, s[58:59]\n"
        "v_mfma_f32_16x16x32_bf16 v[38:41], v[88:91], v[196:199], 0\n"
        "ds_read_b128 a[60:63], v206 offset:15360\n"
        "v_cndmask_b32_e64 v249, v249, v33, s[60:61]\n"
        "v_mfma_f32_16x16x32_bf16 v[38:41], v[60:63], v[190:193], v[38:41]\n"
        "ds_read_b128 a[64:67], v206 offset:16384\n"
        "v_cndmask_b32_e64 v253, v34, v35, s[56:57]\n"
        "v_mfma_f32_16x16x32_bf16 v[38:41], v[72:75], v[184:187], v[38:41]\n"
        "s_waitcnt lgkmcnt(14)\n"
        "ds_read_b128 a[68:71], v206 offset:17408\n"
        "v_cndmask_b32_e64 v253, v253, v36, s[58:59]\n"
        "v_mfma_f32_16x16x32_bf16 v[30:33], v[72:75], v[164:167], 0\n"
        "s_waitcnt lgkmcnt(12)\n"
        "ds_read_b128 a[72:75], v206 offset:18432\n"
        "v_cndmask_b32_e64 v253, v253, v37, s[60:61]\n"
        "v_mfma_f32_16x16x32_bf16 v[30:33], v[60:63], v[170:173], v[30:33]\n"
        "s_waitcnt lgkmcnt(10)\n"
        "ds_read_b128 a[76:79], v206 offset:19456\n"
        "v_cndmask_b32_dpp v249, v253, v249, vcc row_ror:4 row_mask:0xf bank_mask:0xf\n"
        "v_mfma_f32_16x16x32_bf16 v[30:33], v[88:91], v[176:179], v[30:33]\n"
        "ds_read_b128 a[80:83], v206 offset:20480\n"
        "v_add_u32 v252, 73792, v206\n"
        "v_mfma_f32_16x16x32_bf16 v[40:43], a[8:11], v[176:179], 0\n"
        "ds_read_b128 a[84:87], v206 offset:21504\n"
        "v_add_u32 v254, 140896, v254\n"
        "v_mfma_f32_16x16x32_bf16 v[40:43], a[4:7], v[170:173], v[40:43]\n"
        "s_waitcnt lgkmcnt(10)\n"
        "ds_read_b128 a[88:91], v206 offset:22528\n"
        "v_cndmask_b32_dpp v251, v38, v30, vcc row_ror:4 row_mask:0xf bank_mask:0xf\n"
        "v_mfma_f32_16x16x32_bf16 v[40:43], a[0:3], v[164:167], v[40:43]\n"
        "ds_read_b128 a[92:95], v206 offset:23552\n"
        "v_mul_f32 v248, s80, v251\n"
        "v_mfma_f32_16x16x32_bf16 v[32:35], a[12:15], v[164:167], 0\n"
        "ds_read_b128 a[96:99], v250 offset:24576\n"
        "v_fma_f32 v248, -v248, v251, v249\n"
        "v_mfma_f32_16x16x32_bf16 v[32:35], a[16:19], v[170:173], v[32:35]\n"
        "s_waitcnt lgkmcnt(10)\n"
        "ds_read_b128 a[100:103], v250 offset:25600\n"
        "v_cvt_pk_bf16_f32 v44, v40, v41\n"
        "v_mfma_f32_16x16x32_bf16 v[32:35], a[20:23], v[176:179], v[32:35]\n"
        "ds_read_b128 a[104:107], v250 offset:26624\n"
        "v_cvt_pk_bf16_f32 v45, v42, v43\n"
        "v_mfma_f32_16x16x32_bf16 v[48:51], a[32:35], v[176:179], 0\n"
        "ds_read_b128 a[108:111], v250 offset:27648\n"
        "v_max_f32 v248, 0, v248\n"
        "v_mfma_f32_16x16x32_bf16 v[48:51], a[28:31], v[170:173], v[48:51]\n"
        "s_waitcnt lgkmcnt(10)\n"
        "ds_read_b128 a[112:115], v250 offset:28672\n"
        "v_cvt_pk_bf16_f32 v46, v32, v33\n"
        "v_mfma_f32_16x16x32_bf16 v[48:51], a[24:27], v[164:167], v[48:51]\n"
        "ds_read_b128 a[116:119], v250 offset:29696\n"
        "v_cvt_pk_bf16_f32 v47, v34, v35\n"
        "v_mfma_f32_16x16x32_bf16 v[40:43], a[36:39], v[164:167], 0\n"
        "ds_read_b128 a[120:123], v206 offset:30720\n"
        "v_fmaak_f32 v248, v28, v248, 0x3727c5ac\n"
        "v_mfma_f32_16x16x32_bf16 v[40:43], a[40:43], v[170:173], v[40:43]\n"
        "s_nop 0\n"
        "ds_read_b128 a[124:127], v206 offset:31744\n"
        "v_cvt_pk_bf16_f32 v52, v48, v49\n"
        "v_mfma_f32_16x16x32_bf16 v[40:43], a[44:47], v[176:179], v[40:43]\n"
        "ds_read_b128 a[128:131], v206 offset:32768\n"
        "v_cvt_pk_bf16_f32 v53, v50, v51\n"
        "v_mfma_f32_16x16x32_bf16 v[32:35], a[56:59], v[176:179], 0\n"
        "ds_read_b128 a[132:135], v206 offset:33792\n"
        "v_cndmask_b32_dpp v253, v39, v31, vcc row_ror:4 row_mask:0xf bank_mask:0xf\n"
        "v_mfma_f32_16x16x32_bf16 v[32:35], a[52:55], v[170:173], v[32:35]\n"
        "s_nop 0\n"
        "ds_read_b128 a[136:139], v206 offset:34816\n"
        "v_cvt_pk_bf16_f32 v54, v40, v41\n"
        "v_mfma_f32_16x16x32_bf16 v[32:35], a[48:51], v[164:167], v[32:35]\n"
        "s_nop 0\n"
        "ds_read_b128 a[140:143], v206 offset:35840\n"
        "v_cvt_pk_bf16_f32 v55, v42, v43\n"
        "v_mfma_f32_16x16x32_bf16 v[36:39], a[60:63], v[164:167], 0\n"
        "s_nop 0\n"
        "v_rsq_f32 v249, v248\n"
        "v_mfma_f32_16x16x32_bf16 v[36:39], a[64:67], v[170:173], v[36:39]\n"
        "s_nop 0\n"
        "v_cvt_pk_bf16_f32 v40, v32, v33\n"
        "v_cvt_pk_bf16_f32 v41, v34, v35\n"
        "v_mfma_f32_16x16x32_bf16 v[36:39], a[68:71], v[176:179], v[36:39]\n"
        "s_nop 1\n"
        "v_mfma_f32_16x16x32_bf16 v[30:33], a[8:11], v[196:199], 0\n"
        "s_nop 2\n"
        "v_mfma_f32_16x16x32_bf16 v[30:33], a[4:7], v[190:193], v[30:33]\n"
        "s_nop 0\n"
        "v_cvt_pk_bf16_f32 v42, v36, v37\n"
        "v_cvt_pk_bf16_f32 v43, v38, v39\n"
        "v_mfma_f32_16x16x32_bf16 v[30:33], a[0:3], v[184:187], v[30:33]\n"
        "s_nop 1\n"
        "v_mfma_f32_16x16x32_bf16 v[56:59], v[164:167], v[44:47], 0\n"
        "s_nop 2\n"
        "v_mfma_f32_16x16x32_bf16 v[56:59], v[170:173], v[52:55], v[56:59]\n"
        "s_nop 0\n"
        "v_cvt_pk_bf16_f32 v34, v30, v31\n"
        "v_cvt_pk_bf16_f32 v35, v32, v33\n"
        "v_mfma_f32_16x16x32_bf16 v[56:59], v[176:179], v[40:43], v[56:59]\n"
        "v_mfma_f32_16x16x32_bf16 v[30:33], a[20:23], v[196:199], 0\n"
        "s_waitcnt lgkmcnt(0)\n"
        "v_mfma_f32_16x16x32_bf16 v[30:33], a[16:19], v[190:193], v[30:33]\n"
        "s_barrier\n"
        "ds_read_b128 a[144:147], v254\n"
        "v_mfma_f32_16x16x32_bf16 v[30:33], a[12:15], v[184:187], v[30:33]\n"
        "s_nop 1\n"
        "v_mfma_f32_16x16x32_bf16 v[38:41], a[24:27], v[184:187], 0\n"
        "s_nop 0\n"
        "ds_read_b128 a[148:151], v254 offset:1024\n"
        "s_nop 0\n"
        "v_mfma_f32_16x16x32_bf16 v[38:41], a[28:31], v[190:193], v[38:41]\n"
        "s_nop 0\n"
        "v_cvt_pk_bf16_f32 v36, v30, v31\n"
        "v_cvt_pk_bf16_f32 v37, v32, v33\n"
        "v_mfma_f32_16x16x32_bf16 v[38:41], a[32:35], v[196:199], v[38:41]\n"
        "s_nop 1\n"
        "v_mfma_f32_16x16x32_bf16 v[30:33], a[44:47], v[196:199], 0\n"
        "s_nop 2\n"
        "v_mfma_f32_16x16x32_bf16 v[30:33], a[40:43], v[190:193], v[30:33]\n"
        "s_nop 0\n"
        "v_cvt_pk_bf16_f32 v42, v38, v39\n"
        "v_cvt_pk_bf16_f32 v43, v40, v41\n"
        "v_mfma_f32_16x16x32_bf16 v[30:33], a[36:39], v[184:187], v[30:33]\n"
        "s_nop 0\n"
        "ds_read_b128 a[152:155], v254 offset:2048\n"
        "v_mfma_f32_16x16x32_bf16 v[46:49], a[48:51], v[184:187], 0\n"
        "s_nop 2\n"
        "v_mfma_f32_16x16x32_bf16 v[46:49], a[52:55], v[190:193], v[46:49]\n"
        "s_nop 0\n"
        "v_cvt_pk_bf16_f32 v44, v30, v31\n"
        "v_cvt_pk_bf16_f32 v45, v32, v33\n"
        "v_mfma_f32_16x16x32_bf16 v[46:49], a[56:59], v[196:199], v[46:49]\n"
        "s_nop 0\n"
        "ds_read_b128 a[156:159], v254 offset:3072\n"
        "v_mfma_f32_16x16x32_bf16 v[30:33], a[68:71], v[196:199], 0\n"
        "s_nop 2\n"
        "v_mfma_f32_16x16x32_bf16 v[30:33], a[64:67], v[190:193], v[30:33]\n"
        "s_nop 0\n"
        "v_cvt_pk_bf16_f32 v38, v46, v47\n"
        "v_cvt_pk_bf16_f32 v39, v48, v49\n"
        "v_mfma_f32_16x16x32_bf16 v[30:33], a[60:63], v[184:187], v[30:33]\n"
        "s_nop 0\n"
        "ds_read_b128 a[160:163], v254 offset:4096\n"
        "v_mfma_f32_16x16x32_bf16 v[46:49], v[164:167], a[72:75], 0\n"
        "s_nop 2\n"
        "v_mfma_f32_16x16x32_bf16 v[46:49], v[170:173], a[76:79], v[46:49]\n"
        "s_nop 0\n"
        "v_cvt_pk_bf16_f32 v40, v30, v31\n"
        "v_cvt_pk_bf16_f32 v41, v32, v33\n"
        "v_mfma_f32_16x16x32_bf16 v[46:49], v[176:179], a[80:83], v[46:49]\n"
        "s_nop 1\n"
        "v_mfma_f32_16x16x32_bf16 v[50:53], v[176:179], a[92:95], 0\n"
        "s_nop 0\n"
        "ds_read_b128 a[164:167], v254 offset:5120\n"
        "s_nop 0\n"
        "v_mfma_f32_16x16x32_bf16 v[50:53], v[170:173], a[88:91], v[50:53]\n"
        "s_nop 0\n"
        "v_cvt_pk_bf16_f32 v30, v46, v47\n"
        "v_cvt_pk_bf16_f32 v31, v48, v49\n"
        "v_mfma_f32_16x16x32_bf16 v[50:53], v[164:167], a[84:87], v[50:53]\n"
        "s_nop 0\n"
        "v_mfma_f32_16x16x32_bf16 v[64:67], v[184:187], v[34:37], 0\n"
        "s_nop 2\n"
        "v_mfma_f32_16x16x32_bf16 v[64:67], v[190:193], v[42:45], v[64:67]\n"
        "s_nop 0\n"
        "ds_read_b128 a[168:171], v254 offset:6144\n"
        "v_cvt_pk_bf16_f32 v34, v50, v51\n"
        "v_mfma_f32_16x16x32_bf16 v[64:67], v[196:199], v[38:41], v[64:67]\n"
        "s_waitcnt vmcnt(0)\n"
        "v_cvt_pk_bf16_f32 v35, v52, v53\n"
        "v_mfma_f32_16x16x32_bf16 v[36:39], v[176:179], a[104:107], 0\n"
        "s_nop 0\n"
        "ds_read_b128 v[146:149], v9\n"
        "v_mfma_f32_16x16x32_bf16 v[36:39], v[170:173], a[100:103], v[36:39]\n"
        "s_nop 0\n"
        "ds_read_b128 v[152:155], v10\n"
        "v_cndmask_b32_dpp v246, v64, v56, vcc row_ror:4 row_mask:0xf bank_mask:0xf\n"
        "v_mfma_f32_16x16x32_bf16 v[36:39], v[164:167], a[96:99], v[36:39]\n"
        "ds_read_b128 v[158:161], v11\n"
        "v_cndmask_b32_dpp v248, v65, v57, vcc row_ror:4 row_mask:0xf bank_mask:0xf\n"
        "v_mfma_f32_16x16x32_bf16 v[48:51], v[164:167], a[108:111], 0\n"
        "ds_read_b128 v[126:129], v9 offset:3072\n"
        "v_cndmask_b32_dpp v247, v66, v58, vcc row_ror:4 row_mask:0xf bank_mask:0xf\n"
        "v_mfma_f32_16x16x32_bf16 v[48:51], v[170:173], a[112:115], v[48:51]\n"
        "ds_read_b128 v[132:135], v10 offset:3072\n"
        "v_cndmask_b32_dpp v250, v67, v59, vcc row_ror:4 row_mask:0xf bank_mask:0xf\n"
        "v_mfma_f32_16x16x32_bf16 v[48:51], v[176:179], a[116:119], v[48:51]\n"
        "ds_read_b128 v[138:141], v11 offset:3072\n"
        "v_mul_f32_dpp v246, v249, v246 quad_perm:[0,0,0,0] row_mask:0xf bank_mask:0xf\n"
        "v_mfma_f32_16x16x32_bf16 v[84:87], v[176:179], a[128:131], 0\n"
        "ds_read_b128 v[106:109], v9 offset:6144\n"
        "v_mul_f32_dpp v248, v249, v248 quad_perm:[1,1,1,1] row_mask:0xf bank_mask:0xf\n"
        "v_mfma_f32_16x16x32_bf16 v[84:87], v[170:173], a[124:127], v[84:87]\n"
        "ds_read_b128 v[112:115], v10 offset:6144\n"
        "v_mul_f32_dpp v247, v249, v247 quad_perm:[2,2,2,2] row_mask:0xf bank_mask:0xf\n"
        "v_mfma_f32_16x16x32_bf16 v[84:87], v[164:167], a[120:123], v[84:87]\n"
        "ds_read_b128 v[118:121], v11 offset:6144\n"
        "v_mul_f32_dpp v250, v249, v250 quad_perm:[3,3,3,3] row_mask:0xf bank_mask:0xf\n"
        "v_mfma_f32_16x16x32_bf16 v[64:67], v[164:167], a[132:135], 0\n"
        "ds_read_b128 a[172:175], v254 offset:7168\n"
        "v_max3_f32 v245, v246, v248, v247\n"
        "v_mfma_f32_16x16x32_bf16 v[64:67], v[170:173], a[136:139], v[64:67]\n"
        "s_waitcnt lgkmcnt(7)\n"
        "ds_read_b128 a[176:179], v254 offset:8192\n"
        "v_max_f32 v245, v245, v250\n"
        "v_mfma_f32_16x16x32_bf16 v[64:67], v[176:179], a[140:143], v[64:67]\n"
        "v_mul_f32 v245, v245, v249\n"
        "v_fma_f32 v246, v246, v249, -v245\n"
        "v_mfma_f32_16x16x32_bf16 v[52:55], v[146:149], v[146:149], 0\n"
        "s_mov_b32 s26, s89\n"
        "v_fma_f32 v248, v248, v249, -v245\n"
        "v_fma_f32 v247, v247, v249, -v245\n"
        "v_mfma_f32_16x16x32_bf16 v[52:55], v[152:155], v[152:155], v[52:55]\n"
        "s_add_u32 s89, s86, s51\n"
        "v_fma_f32 v250, v250, v249, -v245\n"
        "v_cvt_pk_bf16_f32 v42, v36, v37\n"
        "v_mfma_f32_16x16x32_bf16 v[52:55], v[158:161], v[158:161], v[52:55]\n"
        "s_mov_b32 s30, s29\n"
        "v_exp_f32 v246, v246\n"
        "v_mfma_f32_16x16x32_bf16 v[56:59], v[88:91], v[158:161], 0\n"
        "s_add_u32 s29, s29, 1\n"
        "v_exp_f32 v248, v248\n"
        "v_mfma_f32_16x16x32_bf16 v[56:59], v[60:63], v[152:155], v[56:59]\n"
        "s_mov_b32 s86, s87\n"
        "v_exp_f32 v247, v247\n"
        "v_mfma_f32_16x16x32_bf16 v[56:59], v[72:75], v[146:149], v[56:59]\n"
        "s_add_u32 s88, s88, 32\n"
        "v_exp_f32 v250, v250\n"
        "v_mfma_f32_16x16x32_bf16 v[80:83], a[0:3], v[146:149], 0\n"
        "s_add_u32 s87, s87, 43008\n"
        "v_add_f32 v244, v246, v248\n"
        "v_add_f32 v245, v247, v250\n"
        "v_mfma_f32_16x16x32_bf16 v[80:83], a[4:7], v[152:155], v[80:83]\n"
        "s_cmp_ge_u32 s88, 73\n"
        "v_add_f32 v244, v244, v245\n"
        "v_cvt_pk_bf16_f32 v43, v38, v39\n"
        "v_mfma_f32_16x16x32_bf16 v[80:83], a[8:11], v[158:161], v[80:83]\n"
        "s_cselect_b32 s92, 588672, 0\n"
        "v_rcp_f32 v244, v244\n"
        "v_mfma_f32_16x16x32_bf16 v[76:79], a[20:23], v[158:161], 0\n"
        "s_cselect_b32 s94, 73, 0\n"
        "ds_read_b64 v[32:33], v27\n"
        "ds_read_b64 v[44:45], v27\n"
        "v_mfma_f32_16x16x32_bf16 v[76:79], a[16:19], v[152:155], v[76:79]\n"
        "s_sub_u32 s88, s88, s94\n"
        "ds_read_b64 v[38:39], v27\n"
        "v_mul_f32 v246, v246, v244\n"
        "v_mfma_f32_16x16x32_bf16 v[76:79], a[12:15], v[146:149], v[76:79]\n"
        "s_add_u32 s87, s87, s92\n"
        "ds_read_b32 v255, v255\n"
        "v_mul_f32 v248, v248, v244\n"
        "v_mfma_f32_16x16x32_bf16 v[96:99], a[24:27], v[146:149], 0\n"
        "s_cmp_gt_i32 s85, 0\n"
        "v_mul_f32 v247, v247, v244\n"
        "v_mul_f32 v250, v250, v244\n"
        "v_mfma_f32_16x16x32_bf16 v[96:99], a[28:31], v[152:155], v[96:99]\n"
        "s_cselect_b32 s94, s87, s84\n"
        "v_mul_f32_dpp v246, v249, v246 quad_perm:[0,0,0,0] row_mask:0xf bank_mask:0xf\n"
        "v_mul_f32_dpp v248, v249, v248 quad_perm:[1,1,1,1] row_mask:0xf bank_mask:0xf\n"
        "v_mfma_f32_16x16x32_bf16 v[96:99], a[32:35], v[158:161], v[96:99]\n"
        "s_sub_u32 s85, s85, 1\n"
        "v_mul_f32_dpp v247, v249, v247 quad_perm:[2,2,2,2] row_mask:0xf bank_mask:0xf\n"
        "v_mul_f32_dpp v250, v249, v250 quad_perm:[3,3,3,3] row_mask:0xf bank_mask:0xf\n"
        "v_mfma_f32_16x16x32_bf16 v[92:95], a[44:47], v[158:161], 0\n"
        "s_waitcnt lgkmcnt(9)\n"
        "v_cvt_pk_bf16_f32 v249, v246, v248\n"
        "v_cvt_pk_bf16_f32 v245, v247, v250\n"
        "v_mfma_f32_16x16x32_bf16 v[92:95], a[40:43], v[152:155], v[92:95]\n"
        "s_waitcnt lgkmcnt(6)\n"
        "v_and_b32 v162, v25, v249\n"
        "v_and_b32 v163, v25, v245\n"
        "v_mfma_f32_16x16x32_bf16 v[92:95], a[36:39], v[146:149], v[92:95]\n"
        "v_mov_b64 v[168:169], v[162:163]\n"
        "v_mov_b64 v[174:175], v[162:163]\n"
        "v_mfma_f32_16x16x32_bf16 v[68:71], a[48:51], v[146:149], 0\n"
        "s_waitcnt lgkmcnt(1)\n"
        "v_cvt_pk_bf16_f32 v46, v48, v49\n"
        "v_cvt_pk_bf16_f32 v47, v50, v51\n"
        "v_mfma_f32_16x16x32_bf16 v[68:71], a[52:55], v[152:155], v[68:71]\n"
        "s_waitcnt lgkmcnt(0)\n"
        "v_readfirstlane_b32 s83, v255\n"
        "v_cvt_pk_bf16_f32 v36, v84, v85\n"
        "v_mfma_f32_16x16x32_bf16 v[68:71], a[56:59], v[158:161], v[68:71]\n"
        "v_cvt_pk_bf16_f32 v37, v86, v87\n"
        "v_mov_b64 v[180:181], v[162:163]\n"
        "v_mfma_f32_16x16x32_bf16 v[122:125], a[68:71], v[158:161], 0\n"
        "v_cvt_pk_bf16_f32 v40, v64, v65\n"
        "v_cvt_pk_bf16_f32 v41, v66, v67\n"
        "v_mfma_f32_16x16x32_bf16 v[122:125], a[64:67], v[152:155], v[122:125]\n"
        "ds_read_b128 a[180:183], v254 offset:9216\n"
        "v_and_b32_dpp v182, v249, v25 row_ror:12 row_mask:0xf bank_mask:0xf\n"
        "v_mfma_f32_16x16x32_bf16 v[122:125], a[60:63], v[146:149], v[122:125]\n"
        "ds_read_b128 a[184:187], v254 offset:10240\n"
        "v_and_b32_dpp v183, v245, v25 row_ror:12 row_mask:0xf bank_mask:0xf\n"
        "v_mfma_f32_16x16x32_bf16 v[48:51], v[30:33], v[162:165], 0\n"
        "ds_read_b128 a[188:191], v254 offset:11264\n"
        "v_mov_b64 v[188:189], v[182:183]\n"
        "v_mfma_f32_16x16x32_bf16 v[64:67], v[32:35], v[166:169], 0\n"
        "s_nop 0\n"
        "ds_read_b128 a[192:195], v254 offset:12288\n"
        "v_mov_b64 v[194:195], v[182:183]\n"
        "v_mfma_f32_16x16x32_bf16 v[84:87], v[184:187], a[72:75], 0\n"
        "s_nop 0\n"
        "v_cvt_pk_bf16_f32 v142, v48, v49\n"
        "v_cvt_pk_bf16_f32 v162, v50, v51\n"
        "v_mfma_f32_16x16x32_bf16 v[84:87], v[190:193], a[76:79], v[84:87]\n"
        "v_cvt_pk_bf16_f32 v143, v66, v67\n"
        "v_cvt_pk_bf16_f32 v166, v64, v65\n"
        "v_mfma_f32_16x16x32_bf16 v[84:87], v[196:199], a[80:83], v[84:87]\n"
        "s_add_u32 m0, s52, 0\n"
        "ds_read_b128 a[196:199], v254 offset:13312\n"
        "v_mov_b64 v[200:201], v[182:183]\n"
        "v_mfma_f32_16x16x32_bf16 v[100:103], v[196:199], a[92:95], 0\n"
        "ds_read_b128 a[200:203], v254 offset:14336\n"
        "v_cvt_pk_bf16_f32 v64, v80, v81\n"
        "v_mfma_f32_16x16x32_bf16 v[100:103], v[190:193], a[88:91], v[100:103]\n"
        "buffer_load_dwordx4 v0, s[36:39], s94 offen lds\n"
        "v_cvt_pk_bf16_f32 v30, v84, v85\n"
        "v_mfma_f32_16x16x32_bf16 v[100:103], v[184:187], a[84:87], v[100:103]\n"
        "ds_read_b128 a[204:207], v254 offset:15360\n"
        "v_cvt_pk_bf16_f32 v31, v86, v87\n"
        "v_mfma_f32_16x16x32_bf16 v[84:87], v[42:45], v[168:171], 0\n"
        "ds_read_b128 a[208:211], v254 offset:16384\n"
        "v_cvt_pk_bf16_f32 v65, v82, v83\n"
        "v_mfma_f32_16x16x32_bf16 v[48:51], v[44:47], v[172:175], 0\n"
        "s_nop 0\n"
        "ds_read_b128 a[212:215], v254 offset:17408\n"
        "v_cvt_pk_bf16_f32 v34, v100, v101\n"
        "v_mfma_f32_16x16x32_bf16 v[80:83], v[184:187], a[96:99], 0\n"
        "s_nop 0\n"
        "v_cvt_pk_bf16_f32 v163, v84, v85\n"
        "v_cvt_pk_bf16_f32 v167, v86, v87\n"
        "v_mfma_f32_16x16x32_bf16 v[80:83], v[190:193], a[100:103], v[80:83]\n"
        "v_cvt_pk_bf16_f32 v144, v48, v49\n"
        "v_cvt_pk_bf16_f32 v164, v50, v51\n"
        "v_mfma_f32_16x16x32_bf16 v[80:83], v[196:199], a[104:107], v[80:83]\n"
        "v_cvt_pk_bf16_f32 v35, v102, v103\n"
        "v_cvt_pk_bf16_f32 v66, v76, v77\n"
        "v_mfma_f32_16x16x32_bf16 v[100:103], v[196:199], a[116:119], 0\n"
        "ds_read_b128 a[216:219], v252\n"
        "v_cvt_pk_bf16_f32 v67, v78, v79\n"
        "v_mfma_f32_16x16x32_bf16 v[100:103], v[190:193], a[112:115], v[100:103]\n"
        "s_nop 0\n"
        "buffer_load_dwordx4 v1, s[36:39], s94 offen offset:1024 lds\n"
        "v_cvt_pk_bf16_f32 v42, v80, v81\n"
        "v_mfma_f32_16x16x32_bf16 v[100:103], v[184:187], a[108:111], v[100:103]\n"
        "v_cvt_pk_bf16_f32 v43, v82, v83\n"
        "v_cvt_pk_bf16_f32 v80, v96, v97\n"
        "v_mfma_f32_16x16x32_bf16 v[48:51], v[36:39], v[174:177], 0\n"
        "s_nop 0\n"
        "ds_read_b128 a[220:223], v252 offset:1024\n"
        "v_cvt_pk_bf16_f32 v81, v98, v99\n"
        "v_mfma_f32_16x16x32_bf16 v[82:85], v[38:41], v[178:181], 0\n"
        "s_nop 0\n"
        "v_cvt_pk_bf16_f32 v46, v100, v101\n"
        "v_cvt_pk_bf16_f32 v47, v102, v103\n"
        "v_mfma_f32_16x16x32_bf16 v[76:79], v[184:187], a[120:123], 0\n"
        "v_cvt_pk_bf16_f32 v168, v48, v49\n"
        "v_cvt_pk_bf16_f32 v145, v50, v51\n"
        "v_mfma_f32_16x16x32_bf16 v[76:79], v[190:193], a[124:127], v[76:79]\n"
        "ds_write_b128 v12, v[142:145]\n"
        "v_mfma_f32_16x16x32_bf16 v[76:79], v[196:199], a[128:131], v[76:79]\n"
        "v_cvt_pk_bf16_f32 v165, v82, v83\n"
        "v_cvt_pk_bf16_f32 v169, v84, v85\n"
        "v_mfma_f32_16x16x32_bf16 v[48:51], v[196:199], a[140:143], 0\n"
        "ds_write_b128 v12, v[162:165] offset:1024\n"
        "v_mfma_f32_16x16x32_bf16 v[48:51], v[190:193], a[136:139], v[48:51]\n"
        "ds_write_b128 v12, v[166:169] offset:2048\n"
        "v_mfma_f32_16x16x32_bf16 v[48:51], v[184:187], a[132:135], v[48:51]\n"
        "buffer_load_dwordx4 v2, s[36:39], s94 offen offset:2048 lds\n"
        "v_cvt_pk_bf16_f32 v36, v76, v77\n"
        "v_mfma_f32_16x16x32_bf16 v[96:99], v[142:145], v[142:145], 0\n"
        "v_cvt_pk_bf16_f32 v82, v92, v93\n"
        "v_cvt_pk_bf16_f32 v83, v94, v95\n"
        "v_mfma_f32_16x16x32_bf16 v[96:99], v[162:165], v[162:165], v[96:99]\n"
        "v_cvt_pk_bf16_f32 v37, v78, v79\n"
        "v_cvt_pk_bf16_f32 v100, v68, v69\n"
        "v_mfma_f32_16x16x32_bf16 v[96:99], v[166:169], v[166:169], v[96:99]\n"
        "v_cvt_pk_bf16_f32 v101, v70, v71\n"
        "v_cvt_pk_bf16_f32 v102, v122, v123\n"
        "v_mfma_f32_16x16x32_bf16 v[76:79], v[126:129], v[126:129], 0\n"
        "v_cvt_pk_bf16_f32 v103, v124, v125\n"
        "v_cvt_pk_bf16_f32 v40, v48, v49\n"
        "v_mfma_f32_16x16x32_bf16 v[76:79], v[132:135], v[132:135], v[76:79]\n"
        "v_cvt_pk_bf16_f32 v41, v50, v51\n"
        "v_mul_f32_dpp v255, v253, v246 quad_perm:[0,0,0,0] row_mask:0xf bank_mask:0xf\n"
        "v_mfma_f32_16x16x32_bf16 v[76:79], v[138:141], v[138:141], v[76:79]\n"
        "ds_read_b128 a[224:227], v252 offset:2048\n"
        "v_fmac_f32_dpp v255, v253, v248 quad_perm:[1,1,1,1] row_mask:0xf bank_mask:0xf\n"
        "v_mfma_f32_16x16x32_bf16 v[68:71], v[88:91], v[138:141], 0\n"
        "buffer_load_dwordx4 v3, s[36:39], s94 offen offset:3072 lds\n"
        "v_fmac_f32_dpp v255, v253, v247 quad_perm:[2,2,2,2] row_mask:0xf bank_mask:0xf\n"
        "v_mfma_f32_16x16x32_bf16 v[68:71], v[60:63], v[132:135], v[68:71]\n"
        "ds_read_b128 a[228:231], v252 offset:3088\n"
        "v_cndmask_b32_e64 v254, v96, v97, s[56:57]\n"
        "v_mfma_f32_16x16x32_bf16 v[68:71], v[72:75], v[126:129], v[68:71]\n"
        "ds_read_b128 a[232:235], v252 offset:4112\n"
        "v_cndmask_b32_e64 v254, v254, v98, s[58:59]\n"
        "v_mfma_f32_16x16x32_bf16 v[84:87], v[30:33], v[182:185], 0\n"
        "ds_read_b128 a[236:239], v252 offset:5136\n"
        "v_cndmask_b32_e64 v254, v254, v99, s[60:61]\n"
        "v_mfma_f32_16x16x32_bf16 v[94:97], v[32:35], v[186:189], 0\n"
        "s_nop 0\n"
        "ds_read_b128 a[240:243], v252 offset:6176\n"
        "v_fmac_f32_dpp v255, v253, v250 quad_perm:[3,3,3,3] row_mask:0xf bank_mask:0xf\n"
        "v_mfma_f32_16x16x32_bf16 v[48:51], v[42:45], v[188:191], 0\n"
        "s_nop 0\n"
        "v_cvt_pk_bf16_f32 v92, v84, v85\n"
        "v_cvt_pk_bf16_f32 v34, v86, v87\n"
        "v_mfma_f32_16x16x32_bf16 v[122:125], v[44:47], v[192:195], 0\n"
        "s_add_u32 m0, s52, 4096\n"
        "v_cvt_pk_bf16_f32 v46, v94, v95\n"
        "v_cvt_pk_bf16_f32 v93, v96, v97\n"
        "v_mfma_f32_16x16x32_bf16 v[84:87], v[36:39], v[194:197], 0\n"
        "v_cvt_pk_bf16_f32 v35, v48, v49\n"
        "v_cvt_pk_bf16_f32 v47, v50, v51\n"
        "v_mfma_f32_16x16x32_bf16 v[96:99], v[38:41], v[198:201], 0\n"
        "buffer_load_dwordx4 v4, s[36:39], s94 offen lds\n"
        "v_cvt_pk_bf16_f32 v94, v122, v123\n"
        "v_mfma_f32_16x16x32_bf16 v[40:43], v[146:149], v[64:67], 0\n"
        "s_nop 0\n"
        "v_cvt_pk_bf16_f32 v36, v124, v125\n"
        "v_cvt_pk_bf16_f32 v48, v84, v85\n"
        "v_mfma_f32_16x16x32_bf16 v[40:43], v[152:155], v[80:83], v[40:43]\n"
        "v_cvt_pk_bf16_f32 v95, v86, v87\n"
        "v_cvt_pk_bf16_f32 v37, v96, v97\n"
        "v_mfma_f32_16x16x32_bf16 v[40:43], v[158:161], v[100:103], v[40:43]\n"
        "ds_write_b128 v12, v[92:95] offset:3072\n"
        "v_mfma_f32_16x16x32_bf16 v[64:67], a[8:11], v[138:141], 0\n"
        "ds_write_b128 v12, v[34:37] offset:4096\n"
        "v_mfma_f32_16x16x32_bf16 v[64:67], a[4:7], v[132:135], v[64:67]\n"
        "v_cvt_pk_bf16_f32 v49, v98, v99\n"
        "v_add_f32 v255, v255, v251\n"
        "v_mfma_f32_16x16x32_bf16 v[64:67], a[0:3], v[126:129], v[64:67]\n"
        "ds_write_b128 v12, v[46:49] offset:5120\n"
        "v_mfma_f32_16x16x32_bf16 v[82:85], v[92:95], v[92:95], 0\n"
        "s_nop 0\n"
        "v_mul_f32 v251, s80, v255\n"
        "v_cndmask_b32_e64 v253, v76, v77, s[56:57]\n"
        "v_mfma_f32_16x16x32_bf16 v[82:85], v[34:37], v[34:37], v[82:85]\n"
        "s_nop 0\n"
        "buffer_load_dwordx4 v5, s[36:39], s94 offen offset:1024 lds\n"
        "v_cvt_pk_bf16_f32 v80, v64, v65\n"
        "v_mfma_f32_16x16x32_bf16 v[82:85], v[46:49], v[46:49], v[82:85]\n"
        "v_cvt_pk_bf16_f32 v81, v66, v67\n"
        "v_cndmask_b32_e64 v253, v253, v78, s[58:59]\n"
        "v_mfma_f32_16x16x32_bf16 v[46:49], a[20:23], v[138:141], 0\n"
        "s_nop 0\n"
        "ds_read_b128 a[244:247], v252 offset:7200\n"
        "ds_read_b128 a[248:251], v252 offset:8224\n"
        "v_mfma_f32_16x16x32_bf16 v[46:49], a[16:19], v[132:135], v[46:49]\n"
        "s_nop 0\n"
        "v_cndmask_b32_e64 v250, v82, v83, s[56:57]\n"
        "v_cndmask_b32_e64 v250, v250, v84, s[58:59]\n"
        "v_mfma_f32_16x16x32_bf16 v[46:49], a[12:15], v[126:129], v[46:49]\n"
        "v_cndmask_b32_e64 v250, v250, v85, s[60:61]\n"
        "v_cndmask_b32_e64 v253, v253, v79, s[60:61]\n"
        "v_mfma_f32_16x16x32_bf16 v[34:37], a[24:27], v[126:129], 0\n"
        "v_cndmask_b32_dpp v254, v250, v254, vcc row_ror:4 row_mask:0xf bank_mask:0xf\n"
        "v_fma_f32 v251, -v251, v255, v254\n"
        "v_mfma_f32_16x16x32_bf16 v[34:37], a[28:31], v[132:135], v[34:37]\n"
        "v_max_f32 v251, 0, v251\n"
        "v_fmaak_f32 v251, v28, v251, 0x3727c5ac\n"
        "v_mfma_f32_16x16x32_bf16 v[34:37], a[32:35], v[138:141], v[34:37]\n"
        "v_sub_f32 v254, 0x47c00000, v254\n"
        "v_bfi_b32 v251, s84, v254, v251\n"
        "v_mfma_f32_16x16x32_bf16 v[76:79], a[44:47], v[138:141], 0\n"
        "buffer_load_dwordx4 v6, s[36:39], s94 offen offset:2048 lds\n"
        "v_cvt_pk_bf16_f32 v82, v46, v47\n"
        "v_mfma_f32_16x16x32_bf16 v[76:79], a[40:43], v[132:135], v[76:79]\n"
        "v_rsq_f32 v255, v251\n"
        "v_mfma_f32_16x16x32_bf16 v[76:79], a[36:39], v[126:129], v[76:79]\n"
        "v_cvt_f16_f32 v255, v255\n"
        "v_cvt_pk_bf16_f32 v83, v48, v49\n"
        "v_mfma_f32_16x16x32_bf16 v[64:67], a[48:51], v[126:129], 0\n"
        "ds_write_b16 v18, v255\n"
        "v_mfma_f32_16x16x32_bf16 v[64:67], a[52:55], v[132:135], v[64:67]\n"
        "ds_write_b16 v19, v255\n"
        "v_mfma_f32_16x16x32_bf16 v[64:67], a[56:59], v[138:141], v[64:67]\n"
        "ds_read_b128 a[252:255], v252 offset:9264\n"
        "v_cvt_pk_bf16_f32 v122, v34, v35\n"
        "v_mfma_f32_16x16x32_bf16 v[92:95], a[68:71], v[138:141], 0\n"
        "ds_read_b128 v[100:103], v252 offset:10288\n"
        "v_cvt_pk_bf16_f32 v123, v36, v37\n"
        "v_mfma_f32_16x16x32_bf16 v[92:95], a[64:67], v[132:135], v[92:95]\n"
        "ds_read_b128 v[48:51], v252 offset:11312\n"
        "v_cvt_pk_bf16_f32 v124, v76, v77\n"
        "v_mfma_f32_16x16x32_bf16 v[92:95], a[60:63], v[126:129], v[92:95]\n"
        "buffer_load_dwordx4 v7, s[36:39], s94 offen offset:3072 lds\n"
        "v_cvt_pk_bf16_f32 v125, v78, v79\n"
        "v_mfma_f32_16x16x32_bf16 v[34:37], v[106:109], v[106:109], 0\n"
        "ds_read_b128 v[84:87], v252 offset:12352\n"
        "v_cvt_pk_bf16_f32 v162, v64, v65\n"
        "v_mfma_f32_16x16x32_bf16 v[34:37], v[112:115], v[112:115], v[34:37]\n"
        "ds_read_b128 v[96:99], v252 offset:13376\n"
        "v_cvt_pk_bf16_f32 v163, v66, v67\n"
        "v_mfma_f32_16x16x32_bf16 v[34:37], v[118:121], v[118:121], v[34:37]\n"
        "ds_read_b128 v[76:79], v252 offset:14400\n"
        "v_cvt_pk_bf16_f32 v164, v92, v93\n"
        "v_mfma_f32_16x16x32_bf16 v[142:145], v[88:91], v[118:121], 0\n"
        "s_waitcnt lgkmcnt(6)\n"
        "ds_read_b128 v[64:67], v252 offset:15440\n"
        "v_cvt_pk_bf16_f32 v165, v94, v95\n"
        "v_mfma_f32_16x16x32_bf16 v[142:145], v[60:63], v[112:115], v[142:145]\n"
        "s_barrier\n"
        "ds_read_b128 v[92:95], v252 offset:16464\n"
        "v_cndmask_b32_e64 v254, v34, v35, s[56:57]\n"
        "v_mfma_f32_16x16x32_bf16 v[142:145], v[72:75], v[106:109], v[142:145]\n"
        "s_add_u32 m0, s52, 8192\n"
        "v_cndmask_b32_e64 v254, v254, v36, s[58:59]\n"
        "v_cndmask_b32_e64 v254, v254, v37, s[60:61]\n"
        "v_mfma_f32_16x16x32_bf16 v[166:169], v[126:129], v[80:83], 0\n"
        "ds_read_b128 v[80:83], v252 offset:17488\n"
        "v_cndmask_b32_e64 v251, v52, v53, s[56:57]\n"
        "v_mfma_f32_16x16x32_bf16 v[166:169], v[132:135], v[122:125], v[166:169]\n"
        "buffer_load_dwordx4 v8, s[36:39], s94 offen lds\n"
        "v_cndmask_b32_e64 v251, v251, v54, s[58:59]\n"
        "v_mfma_f32_16x16x32_bf16 v[166:169], v[138:141], v[162:165], v[166:169]\n"
        "v_cndmask_b32_dpp v255, v142, v68, vcc row_ror:4 row_mask:0xf bank_mask:0xf\n"
        "v_cndmask_b32_e64 v251, v251, v55, s[60:61]\n"
        "v_mfma_f32_16x16x32_bf16 v[34:37], a[8:11], v[118:121], 0\n"
        "v_cndmask_b32_dpp v253, v254, v253, vcc row_ror:4 row_mask:0xf bank_mask:0xf\n"
        "v_cndmask_b32_dpp v255, v255, v56, vcc row_ror:4 row_mask:0xf bank_mask:0xf\n"
        "v_mfma_f32_16x16x32_bf16 v[34:37], a[4:7], v[112:115], v[34:37]\n"
        "v_cndmask_b32_dpp v251, v253, v251, vcc row_ror:4 row_mask:0xf bank_mask:0xf\n"
        "v_mul_f32 v252, s80, v255\n"
        "v_mfma_f32_16x16x32_bf16 v[34:37], a[0:3], v[106:109], v[34:37]\n"
        "v_fma_f32 v252, -v252, v255, v251\n"
        "v_max_f32 v252, 0, v252\n"
        "v_mfma_f32_16x16x32_bf16 v[122:125], a[12:15], v[106:109], 0\n"
        "s_nop 0\n"
        "v_fmaak_f32 v252, v28, v252, 0x3727c5ac\n"
        "v_cndmask_b32_dpp v254, v143, v69, vcc row_ror:4 row_mask:0xf bank_mask:0xf\n"
        "v_mfma_f32_16x16x32_bf16 v[122:125], a[16:19], v[112:115], v[122:125]\n"
        "s_nop 0\n"
        "v_cvt_pk_bf16_f32 v52, v34, v35\n"
        "v_cvt_pk_bf16_f32 v53, v36, v37\n"
        "v_mfma_f32_16x16x32_bf16 v[122:125], a[20:23], v[118:121], v[122:125]\n"
        "s_waitcnt lgkmcnt(0)\n"
        "v_rsq_f32 v253, v252\n"
        "v_mfma_f32_16x16x32_bf16 v[34:37], a[32:35], v[118:121], 0\n"
        "s_nop 0\n"
        "v_cndmask_b32_dpp v254, v254, v57, vcc row_ror:4 row_mask:0xf bank_mask:0xf\n"
        "s_nop 0\n"
        "v_mfma_f32_16x16x32_bf16 v[34:37], a[28:31], v[112:115], v[34:37]\n"
        "s_nop 0\n"
        "v_cvt_pk_bf16_f32 v54, v122, v123\n"
        "v_cvt_pk_bf16_f32 v55, v124, v125\n"
        "v_mfma_f32_16x16x32_bf16 v[34:37], a[24:27], v[106:109], v[34:37]\n"
        "s_nop 1\n"
        "v_mfma_f32_16x16x32_bf16 v[56:59], a[36:39], v[106:109], 0\n"
        "s_nop 2\n"
        "v_mfma_f32_16x16x32_bf16 v[56:59], a[40:43], v[112:115], v[56:59]\n"
        "s_nop 0\n"
        "v_cvt_pk_bf16_f32 v122, v34, v35\n"
        "v_cvt_pk_bf16_f32 v123, v36, v37\n"
        "v_mfma_f32_16x16x32_bf16 v[56:59], a[44:47], v[118:121], v[56:59]\n"
        "s_nop 0\n"
        "v_mfma_f32_16x16x32_bf16 v[34:37], a[56:59], v[118:121], 0\n"
        "s_nop 0\n"
        "ds_read_b128 v[218:221], v13\n"
        "ds_read_b128 v[214:217], v13 offset:1024\n"
        "v_mfma_f32_16x16x32_bf16 v[34:37], a[52:55], v[112:115], v[34:37]\n"
        "s_nop 0\n"
        "ds_read_b128 v[234:237], v13 offset:2048\n"
        "v_cvt_pk_bf16_f32 v124, v56, v57\n"
        "v_mfma_f32_16x16x32_bf16 v[34:37], a[48:51], v[106:109], v[34:37]\n"
        "v_mov_b64 v[246:247], 0\n"
        "s_mov_b64 exec, 1\n"
        "buffer_atomic_add v246, off, s[16:19], 0 sc0\n"
        "buffer_atomic_add v247, off, s[16:19], s15 offset:4 sc0\n"
        "s_mov_b64 exec, -1\n"
        "v_mfma_f32_16x16x32_bf16 v[178:181], a[60:63], v[106:109], 0\n"
        "v_cvt_pk_bf16_f32 v125, v58, v59\n"
        "v_mfma_f32_16x16x32_bf16 v[178:181], a[64:67], v[112:115], v[178:181]\n"
        "v_cvt_pk_bf16_f32 v174, v34, v35\n"
        "v_cvt_pk_bf16_f32 v175, v36, v37\n"
        "v_mfma_f32_16x16x32_bf16 v[178:181], a[68:71], v[118:121], v[178:181]\n"
        "v_mfma_f32_16x16x32_bf16 v[34:37], v[158:161], a[80:83], 0\n"
        "v_mfma_f32_16x16x32_bf16 v[34:37], v[152:155], a[76:79], v[34:37]\n"
        "ds_read_b64_tr_b16 v[170:171], v207\n"
        "ds_read_b64_tr_b16 v[172:173], v207 offset:3072\n"
        "v_mfma_f32_16x16x32_bf16 v[34:37], v[146:149], a[72:75], v[34:37]\n"
        "s_waitcnt lgkmcnt(2)\n"
        "ds_read_b64_tr_b16 v[142:143], v207 offset:6144\n"
        "ds_read_b64_tr_b16 v[144:145], v207 offset:9216\n"
        "v_mfma_f32_16x16x32_bf16 v[68:71], v[146:149], a[84:87], 0\n"
        "ds_read_b64_tr_b16 v[56:57], v207 offset:12288\n"
        "ds_read_b128 v[238:241], v13 offset:3072\n"
        "v_mfma_f32_16x16x32_bf16 v[68:71], v[152:155], a[88:91], v[68:71]\n"
        "ds_read_b128 v[222:225], v13 offset:4096\n"
        "ds_read_b128 v[230:233], v13 offset:5120\n"
        "v_mfma_f32_16x16x32_bf16 v[68:71], v[158:161], a[92:95], v[68:71]\n"
        "ds_read_b128 v[226:229], v20\n"
        "ds_read_b64 v[248:249], v21\n"
        "v_mfma_f32_16x16x32_bf16 v[162:165], v[106:109], v[52:55], 0\n"
        "ds_read_b64_tr_b16 v[58:59], v207 offset:15360\n"
        "v_cvt_pk_bf16_f32 v176, v178, v179\n"
        "v_mfma_f32_16x16x32_bf16 v[162:165], v[112:115], v[122:125], v[162:165]\n"
        "v_cvt_pk_bf16_f32 v177, v180, v181\n"
        "v_cvt_pk_bf16_f32 v30, v34, v35\n"
        "s_nop 0\n"
        "v_mfma_f32_16x16x32_bf16 v[162:165], v[118:121], v[174:177], v[162:165]\n"
        "s_nop 0\n"
        "v_cvt_pk_bf16_f32 v31, v36, v37\n"
        "v_cvt_pk_bf16_f32 v34, v68, v69\n"
        "v_mfma_f32_16x16x32_bf16 v[122:125], a[152:155], v[234:237], 0\n"
        "s_nop 0\n"
        "v_cvt_pk_bf16_f32 v35, v70, v71\n"
        "v_mfma_f32_16x16x32_bf16 v[122:125], a[148:151], v[214:217], v[122:125]\n"
        "s_nop 0\n"
        "v_cndmask_b32_dpp v252, v162, v166, vcc row_ror:4 row_mask:0xf bank_mask:0xf\n"
        "v_cndmask_b32_dpp v251, v163, v167, vcc row_ror:4 row_mask:0xf bank_mask:0xf\n"
        "v_mfma_f32_16x16x32_bf16 v[122:125], a[144:147], v[218:221], v[122:125]\n"
        "v_cndmask_b32_dpp v206, v164, v168, vcc row_ror:4 row_mask:0xf bank_mask:0xf\n"
        "v_cndmask_b32_dpp v250, v165, v169, vcc row_ror:4 row_mask:0xf bank_mask:0xf\n"
        "v_mfma_f32_16x16x32_bf16 v[52:55], a[156:159], v[218:221], 0\n"
        "v_cndmask_b32_dpp v252, v252, v40, vcc row_ror:4 row_mask:0xf bank_mask:0xf\n"
        "v_cndmask_b32_dpp v251, v251, v41, vcc row_ror:4 row_mask:0xf bank_mask:0xf\n"
        "v_mfma_f32_16x16x32_bf16 v[52:55], a[160:163], v[214:217], v[52:55]\n"
        "s_waitcnt lgkmcnt(1)\n"
        "ds_read_b64_tr_b16 v[174:175], v207 offset:8\n"
        "ds_read_b64_tr_b16 v[176:177], v207 offset:3080\n"
        "v_mfma_f32_16x16x32_bf16 v[52:55], a[164:167], v[234:237], v[52:55]\n"
        "s_waitcnt lgkmcnt(2)\n"
        "ds_read_b64_tr_b16 v[178:179], v207 offset:6152\n"
        "ds_read_b64_tr_b16 v[180:181], v207 offset:9224\n"
        "v_mfma_f32_16x16x32_bf16 v[182:185], v[56:59], v[234:237], 0\n"
        "s_barrier\n"
        "ds_read_b64_tr_b16 v[162:163], v207 offset:12296\n"
        "ds_read_b64_tr_b16 v[164:165], v207 offset:15368\n"
        "v_mfma_f32_16x16x32_bf16 v[182:185], v[142:145], v[214:217], v[182:185]\n"
        "v_cvt_pk_bf16_f32 v68, v122, v123\n"
        "v_cvt_pk_bf16_f32 v69, v124, v125\n"
        "v_mfma_f32_16x16x32_bf16 v[182:185], v[170:173], v[218:221], v[182:185]\n"
        "s_nop 0\n"
        "v_cvt_pk_bf16_f32 v70, v52, v53\n"
        "v_cvt_pk_bf16_f32 v71, v54, v55\n"
        "v_mfma_f32_16x16x32_bf16 v[52:55], v[170:173], v[238:241], 0\n"
        "s_nop 0\n"
        "ds_write_b128 v208, v[68:71]\n"
        "v_mfma_f32_16x16x32_bf16 v[52:55], v[142:145], v[222:225], v[52:55]\n"
        "s_nop 0\n"
        "v_cvt_pk_bf16_f32 v166, v182, v183\n"
        "v_cvt_pk_bf16_f32 v167, v184, v185\n"
        "v_mfma_f32_16x16x32_bf16 v[52:55], v[56:59], v[230:233], v[52:55]\n"
        "v_cndmask_b32_dpp v206, v206, v42, vcc row_ror:4 row_mask:0xf bank_mask:0xf\n"
        "v_cndmask_b32_dpp v250, v250, v43, vcc row_ror:4 row_mask:0xf bank_mask:0xf\n"
        "v_mfma_f32_16x16x32_bf16 v[168:171], a[176:179], v[234:237], 0\n"
        "s_nop 0\n"
        "v_mul_f32_dpp v252, v253, v252 quad_perm:[0,0,0,0] row_mask:0xf bank_mask:0xf\n"
        "v_mul_f32_dpp v251, v253, v251 quad_perm:[1,1,1,1] row_mask:0xf bank_mask:0xf\n"
        "v_mfma_f32_16x16x32_bf16 v[168:171], a[172:175], v[214:217], v[168:171]\n"
        "s_waitcnt lgkmcnt(1)\n"
        "v_cvt_pk_bf16_f32 v40, v52, v53\n"
        "v_cvt_pk_bf16_f32 v41, v54, v55\n"
        "v_mfma_f32_16x16x32_bf16 v[168:171], a[168:171], v[218:221], v[168:171]\n"
        "v_mul_f32_dpp v206, v253, v206 quad_perm:[2,2,2,2] row_mask:0xf bank_mask:0xf\n"
        "v_mul_f32_dpp v250, v253, v250 quad_perm:[3,3,3,3] row_mask:0xf bank_mask:0xf\n"
        "v_mfma_f32_16x16x32_bf16 v[52:55], a[180:183], v[218:221], 0\n"
        "v_max3_f32 v249, v252, v251, v206\n"
        "v_max_f32 v249, v249, v250\n"
        "v_mfma_f32_16x16x32_bf16 v[52:55], a[184:187], v[214:217], v[52:55]\n"
        "ds_read_b64_tr_b16 v[142:143], v207 offset:1024\n"
        "v_mul_f32 v249, v249, v253\n"
        "v_mfma_f32_16x16x32_bf16 v[52:55], a[188:191], v[234:237], v[52:55]\n"
        "ds_read_b64_tr_b16 v[144:145], v207 offset:4096\n"
        "v_fma_f32 v252, v252, v253, -v249\n"
        "v_mfma_f32_16x16x32_bf16 v[122:125], v[162:165], v[234:237], 0\n"
        "v_fma_f32 v251, v251, v253, -v249\n"
        "v_fma_f32 v206, v206, v253, -v249\n"
        "v_mfma_f32_16x16x32_bf16 v[122:125], v[178:181], v[214:217], v[122:125]\n"
        "ds_read_b64_tr_b16 v[56:57], v207 offset:7168\n"
        "v_fma_f32 v250, v250, v253, -v249\n"
        "v_mfma_f32_16x16x32_bf16 v[122:125], v[174:177], v[218:221], v[122:125]\n"
        "ds_read_b64_tr_b16 v[58:59], v207 offset:10240\n"
        "v_cvt_pk_bf16_f32 v242, v168, v169\n"
        "v_mfma_f32_16x16x32_bf16 v[196:199], v[174:177], v[238:241], 0\n"
        "ds_read_b64_tr_b16 v[186:187], v207 offset:13312\n"
        "v_cvt_pk_bf16_f32 v243, v170, v171\n"
        "v_mfma_f32_16x16x32_bf16 v[196:199], v[178:181], v[222:225], v[196:199]\n"
        "ds_read_b64_tr_b16 v[188:189], v207 offset:16384\n"
        "v_cvt_pk_bf16_f32 v244, v52, v53\n"
        "v_mfma_f32_16x16x32_bf16 v[196:199], v[162:165], v[230:233], v[196:199]\n"
        "v_exp_f32 v252, v252\n"
        "v_mfma_f32_16x16x32_bf16 v[162:165], a[200:203], v[234:237], 0\n"
        "v_exp_f32 v251, v251\n"
        "v_mfma_f32_16x16x32_bf16 v[162:165], a[196:199], v[214:217], v[162:165]\n"
        "v_exp_f32 v206, v206\n"
        "v_mfma_f32_16x16x32_bf16 v[162:165], a[192:195], v[218:221], v[162:165]\n"
        "v_exp_f32 v250, v250\n"
        "v_mfma_f32_16x16x32_bf16 v[174:177], a[204:207], v[218:221], 0\n"
        "v_add_f32 v249, v252, v251\n"
        "v_add_f32 v245, v206, v250\n"
        "v_mfma_f32_16x16x32_bf16 v[174:177], a[208:211], v[214:217], v[174:177]\n"
        "v_add_f32 v249, v249, v245\n"
        "v_cvt_pk_bf16_f32 v245, v54, v55\n"
        "v_mfma_f32_16x16x32_bf16 v[174:177], a[212:215], v[234:237], v[174:177]\n"
        "v_rcp_f32 v249, v249\n"
        "s_waitcnt lgkmcnt(0)\n"
        "v_mfma_f32_16x16x32_bf16 v[178:181], v[186:189], v[234:237], 0\n"
        "v_mul_f32 v252, v252, v249\n"
        "v_mul_f32 v251, v251, v249\n"
        "v_mfma_f32_16x16x32_bf16 v[178:181], v[56:59], v[214:217], v[178:181]\n"
        "ds_read_b64_tr_b16 v[170:171], v207 offset:1032\n"
        "v_mul_f32 v206, v206, v249\n"
        "v_mfma_f32_16x16x32_bf16 v[178:181], v[142:145], v[218:221], v[178:181]\n"
        "ds_read_b64_tr_b16 v[172:173], v207 offset:4104\n"
        "v_mul_f32 v250, v250, v249\n"
        "v_mfma_f32_16x16x32_bf16 v[52:55], v[142:145], v[238:241], 0\n"
        "ds_read_b64_tr_b16 v[182:183], v207 offset:7176\n"
        "v_mul_f32_dpp v252, v253, v252 quad_perm:[0,0,0,0] row_mask:0xf bank_mask:0xf\n"
        "v_mfma_f32_16x16x32_bf16 v[52:55], v[56:59], v[222:225], v[52:55]\n"
        "ds_read_b64_tr_b16 v[184:185], v207 offset:10248\n"
        "v_mul_f32_dpp v251, v253, v251 quad_perm:[1,1,1,1] row_mask:0xf bank_mask:0xf\n"
        "v_mfma_f32_16x16x32_bf16 v[52:55], v[186:189], v[230:233], v[52:55]\n"
        "ds_read_b64_tr_b16 v[190:191], v207 offset:13320\n"
        "v_mul_f32_dpp v206, v253, v206 quad_perm:[2,2,2,2] row_mask:0xf bank_mask:0xf\n"
        "v_mfma_f32_16x16x32_bf16 v[186:189], v[158:161], a[104:107], 0\n"
        "ds_read_b64_tr_b16 v[192:193], v207 offset:16392\n"
        "v_mul_f32_dpp v250, v253, v250 quad_perm:[3,3,3,3] row_mask:0xf bank_mask:0xf\n"
        "v_mfma_f32_16x16x32_bf16 v[186:189], v[152:155], a[100:103], v[186:189]\n"
        "ds_write_b128 v208, v[242:245] offset:64\n"
        "v_mfma_f32_16x16x32_bf16 v[186:189], v[146:149], a[96:99], v[186:189]\n"
        "v_cvt_pk_bf16_f32 v253, v252, v251\n"
        "v_cvt_pk_bf16_f32 v249, v206, v250\n"
        "v_mfma_f32_16x16x32_bf16 v[56:59], v[146:149], a[108:111], 0\n"
        "v_and_b32 v144, v25, v253\n"
        "v_and_b32 v145, v25, v249\n"
        "v_mfma_f32_16x16x32_bf16 v[56:59], v[152:155], a[112:115], v[56:59]\n"
        "v_mov_b64 v[150:151], v[144:145]\n"
        "v_cvt_pk_bf16_f32 v168, v122, v123\n"
        "v_mfma_f32_16x16x32_bf16 v[56:59], v[158:161], a[116:119], v[56:59]\n"
        "v_cvt_pk_bf16_f32 v169, v124, v125\n"
        "v_cvt_pk_bf16_f32 v42, v196, v197\n"
        "v_mfma_f32_16x16x32_bf16 v[194:197], v[30:33], v[144:147], 0\n"
        "ds_write_b128 v208, v[166:169] offset:192\n"
        "v_mfma_f32_16x16x32_bf16 v[166:169], v[32:35], v[148:151], 0\n"
        "s_waitcnt lgkmcnt(2)\n"
        "v_cvt_pk_bf16_f32 v43, v198, v199\n"
        "v_cvt_pk_bf16_f32 v122, v162, v163\n"
        "v_mfma_f32_16x16x32_bf16 v[34:37], v[190:193], v[234:237], 0\n"
        "ds_write_b128 v209, v[40:43] offset:192\n"
        "v_mfma_f32_16x16x32_bf16 v[34:37], v[182:185], v[214:217], v[34:37]\n"
        "ds_read_b64_tr_b16 v[162:163], v207 offset:2048\n"
        "v_cvt_pk_bf16_f32 v123, v164, v165\n"
        "v_mfma_f32_16x16x32_bf16 v[34:37], v[170:173], v[218:221], v[34:37]\n"
        "ds_read_b64_tr_b16 v[164:165], v207 offset:5120\n"
        "v_cvt_pk_bf16_f32 v124, v174, v175\n"
        "v_mfma_f32_16x16x32_bf16 v[198:201], v[170:173], v[238:241], 0\n"
        "ds_read_b64_tr_b16 v[174:175], v207 offset:8192\n"
        "v_cvt_pk_bf16_f32 v125, v176, v177\n"
        "v_mfma_f32_16x16x32_bf16 v[198:201], v[182:185], v[222:225], v[198:201]\n"
        "ds_write_b128 v208, v[122:125] offset:128\n"
        "v_mfma_f32_16x16x32_bf16 v[198:201], v[190:193], v[230:233], v[198:201]\n"
        "ds_read_b64_tr_b16 v[176:177], v207 offset:11264\n"
        "v_mov_b64 v[156:157], v[144:145]\n"
        "v_mfma_f32_16x16x32_bf16 v[202:205], v[158:161], a[128:131], 0\n"
        "ds_read_b64_tr_b16 v[170:171], v207 offset:14336\n"
        "v_cvt_pk_bf16_f32 v40, v178, v179\n"
        "v_mfma_f32_16x16x32_bf16 v[202:205], v[152:155], a[124:127], v[202:205]\n"
        "ds_read_b64_tr_b16 v[172:173], v207 offset:17408\n"
        "v_cvt_pk_bf16_f32 v41, v180, v181\n"
        "v_mfma_f32_16x16x32_bf16 v[202:205], v[146:149], a[120:123], v[202:205]\n"
        "ds_read_b64_tr_b16 v[190:191], v207 offset:2056\n"
        "v_cvt_pk_bf16_f32 v178, v52, v53\n"
        "v_mfma_f32_16x16x32_bf16 v[182:185], v[146:149], a[132:135], 0\n"
        "ds_read_b64_tr_b16 v[192:193], v207 offset:5128\n"
        "v_cvt_pk_bf16_f32 v179, v54, v55\n"
        "v_mfma_f32_16x16x32_bf16 v[182:185], v[152:155], a[136:139], v[182:185]\n"
        "ds_read_b64_tr_b16 v[52:53], v207 offset:8200\n"
        "v_cvt_pk_bf16_f32 v42, v186, v187\n"
        "v_mfma_f32_16x16x32_bf16 v[182:185], v[158:161], a[140:143], v[182:185]\n"
        "ds_read_b64_tr_b16 v[54:55], v207 offset:11272\n"
        "v_cvt_pk_bf16_f32 v43, v188, v189\n"
        "s_nop 1\n"
        "v_mfma_f32_16x16x32_bf16 v[186:189], v[42:45], v[150:153], 0\n"
        "ds_read_b64_tr_b16 v[150:151], v207 offset:14344\n"
        "v_cvt_pk_bf16_f32 v46, v56, v57\n"
        "v_cvt_pk_bf16_f32 v47, v58, v59\n"
        "s_nop 1\n"
        "v_mfma_f32_16x16x32_bf16 v[56:59], v[44:47], v[154:157], 0\n"
        "ds_read_b64_tr_b16 v[152:153], v207 offset:17416\n"
        "v_cvt_pk_bf16_f32 v42, v34, v35\n"
        "s_waitcnt lgkmcnt(6)\n"
        "v_mfma_f32_16x16x32_bf16 v[146:149], v[170:173], v[234:237], 0\n"
        "v_cvt_pk_bf16_f32 v43, v36, v37\n"
        "v_cvt_pk_bf16_f32 v180, v198, v199\n"
        "v_mfma_f32_16x16x32_bf16 v[146:149], v[174:177], v[214:217], v[146:149]\n"
        "ds_write_b128 v208, v[40:43] offset:256\n"
        "v_mfma_f32_16x16x32_bf16 v[146:149], v[162:165], v[218:221], v[146:149]\n"
        "v_cvt_pk_bf16_f32 v181, v200, v201\n"
        "v_cvt_pk_bf16_f32 v36, v202, v203\n"
        "v_mfma_f32_16x16x32_bf16 v[198:201], v[162:165], v[238:241], 0\n"
        "ds_write_b128 v209, v[178:181] offset:256\n"
        "v_mfma_f32_16x16x32_bf16 v[198:201], v[174:177], v[222:225], v[198:201]\n"
        "v_cvt_pk_bf16_f32 v37, v204, v205\n"
        "v_mov_b64 v[162:163], v[144:145]\n"
        "v_mfma_f32_16x16x32_bf16 v[198:201], v[170:173], v[230:233], v[198:201]\n"
        "s_waitcnt lgkmcnt(2)\n"
        "v_cvt_pk_bf16_f32 v40, v182, v183\n"
        "v_cvt_pk_bf16_f32 v41, v184, v185\n"
        "v_mfma_f32_16x16x32_bf16 v[170:173], v[150:153], v[234:237], 0\n"
        "s_barrier\n"
        "v_cvt_pk_bf16_f32 v180, v146, v147\n"
        "v_cvt_pk_bf16_f32 v181, v148, v149\n"
        "v_mfma_f32_16x16x32_bf16 v[170:173], v[52:55], v[214:217], v[170:173]\n"
        "v_cvt_pk_bf16_f32 v178, v198, v199\n"
        "v_cvt_pk_bf16_f32 v179, v200, v201\n"
        "v_mfma_f32_16x16x32_bf16 v[170:173], v[190:193], v[218:221], v[170:173]\n"
        "v_cvt_pk_bf16_f32 v146, v194, v195\n"
        "v_cvt_pk_bf16_f32 v147, v168, v169\n"
        "v_mfma_f32_16x16x32_bf16 v[174:177], v[190:193], v[238:241], 0\n"
        "s_nop 0\n"
        "v_cvt_pk_bf16_f32 v148, v56, v57\n"
        "v_cvt_pk_bf16_f32 v142, v196, v197\n"
        "v_mfma_f32_16x16x32_bf16 v[174:177], v[52:55], v[222:225], v[174:177]\n"
        "s_nop 0\n"
        "v_cvt_pk_bf16_f32 v182, v170, v171\n"
        "v_cvt_pk_bf16_f32 v183, v172, v173\n"
        "v_mfma_f32_16x16x32_bf16 v[174:177], v[150:153], v[230:233], v[174:177]\n"
        "s_nop 0\n"
        "ds_write_b128 v208, v[180:183] offset:320\n"
        "v_mfma_f32_16x16x32_bf16 v[168:171], v[138:141], a[80:83], 0\n"
        "s_nop 0\n"
        "v_cvt_pk_bf16_f32 v143, v186, v187\n"
        "v_cvt_pk_bf16_f32 v144, v58, v59\n"
        "v_mfma_f32_16x16x32_bf16 v[168:171], v[132:135], a[76:79], v[168:171]\n"
        "s_nop 0\n"
        "v_cvt_pk_bf16_f32 v180, v174, v175\n"
        "v_cvt_pk_bf16_f32 v181, v176, v177\n"
        "v_mfma_f32_16x16x32_bf16 v[168:171], v[126:129], a[72:75], v[168:171]\n"
        "s_nop 0\n"
        "ds_read_b128 v[52:55], v27 offset:64\n"
        "ds_read_b128 v[56:59], v27 offset:128\n"
        "v_mfma_f32_16x16x32_bf16 v[150:153], v[126:129], a[84:87], 0\n"
        "s_nop 0\n"
        "ds_write_b128 v209, v[178:181] offset:320\n"
        "v_mfma_f32_16x16x32_bf16 v[150:153], v[132:135], a[88:91], v[150:153]\n"
        "s_nop 0\n"
        "v_cvt_pk_bf16_f32 v30, v168, v169\n"
        "v_cvt_pk_bf16_f32 v31, v170, v171\n"
        "v_mfma_f32_16x16x32_bf16 v[150:153], v[138:141], a[92:95], v[150:153]\n"
        "v_cvt_pk_bf16_f32 v154, v166, v167\n"
        "v_cvt_pk_bf16_f32 v155, v188, v189\n"
        "v_mfma_f32_16x16x32_bf16 v[164:167], v[36:39], v[156:159], 0\n"
        "s_nop 0\n"
        "v_cvt_f32_f16_sdwa v208, v248 dst_sel:DWORD dst_unused:UNUSED_PAD src0_sel:WORD_0\n"
        "v_lshrrev_b32 v37, 0, v248\n"
        "v_mfma_f32_16x16x32_bf16 v[168:171], v[38:41], v[160:163], 0\n"
        "s_nop 0\n"
        "v_cvt_pk_bf16_f32 v34, v150, v151\n"
        "v_cvt_pk_bf16_f32 v35, v152, v153\n"
        "v_mfma_f32_16x16x32_bf16 v[150:153], v[218:221], v[68:71], 0\n"
        "v_cvt_pk_bf16_f32 v149, v166, v167\n"
        "v_cvt_pk_bf16_f32 v156, v164, v165\n"
        "v_mfma_f32_16x16x32_bf16 v[150:153], v[214:217], v[242:245], v[150:153]\n"
        "s_waitcnt lgkmcnt(1)\n"
        "ds_write_b128 v12, v[146:149]\n"
        "v_mfma_f32_16x16x32_bf16 v[150:153], v[234:237], v[122:125], v[150:153]\n"
        "v_cvt_pk_bf16_f32 v145, v168, v169\n"
        "v_cvt_pk_bf16_f32 v157, v170, v171\n"
        "v_mfma_f32_16x16x32_bf16 v[162:165], v[234:237], a[224:227], 0\n"
        "ds_write_b128 v12, v[142:145] offset:1024\n"
        "v_mfma_f32_16x16x32_bf16 v[162:165], v[214:217], a[220:223], v[162:165]\n"
        "ds_write_b128 v12, v[154:157] offset:2048\n"
        "v_mfma_f32_16x16x32_bf16 v[162:165], v[218:221], a[216:219], v[162:165]\n"
        "s_nop 0\n"
        "v_cndmask_b32_e64 v36, v150, v151, s[56:57]\n"
        "v_cndmask_b32_e64 v36, v36, v152, s[58:59]\n"
        "v_mfma_f32_16x16x32_bf16 v[40:43], v[218:221], a[228:231], 0\n"
        "v_cndmask_b32_e64 v36, v36, v153, s[60:61]\n"
        "v_mul_f32 v36, v36, v208\n"
        "v_mfma_f32_16x16x32_bf16 v[40:43], v[214:217], a[232:235], v[40:43]\n"
        "v_mul_f32 v36, v36, v208\n"
        "v_and_b32_dpp v124, v253, v25 row_ror:12 row_mask:0xf bank_mask:0xf\n"
        "v_mfma_f32_16x16x32_bf16 v[40:43], v[234:237], a[236:239], v[40:43]\n"
        "v_and_b32_dpp v125, v249, v25 row_ror:12 row_mask:0xf bank_mask:0xf\n"
        "v_mov_b64 v[130:131], v[124:125]\n"
        "v_mfma_f32_16x16x32_bf16 v[176:179], v[218:221], v[52:55], 0\n"
        "v_exp_f32 v36, v36\n"
        "v_mfma_f32_16x16x32_bf16 v[180:183], v[218:221], v[56:59], 0\n"
        "ds_write_b64 v210, v[36:37]\n"
        "v_mfma_f32_16x16x32_bf16 v[158:161], v[138:141], a[104:107], 0\n"
        "v_cvt_f32_f16_sdwa v244, v226 dst_sel:DWORD dst_unused:UNUSED_PAD src0_sel:WORD_0\n"
        "v_mul_f32 v162, v162, v244\n"
        "v_mfma_f32_16x16x32_bf16 v[158:161], v[132:135], a[100:103], v[158:161]\n"
        "v_mul_f32 v40, v40, v244\n"
        "v_cvt_pk_f16_f32 v162, v162, v40\n"
        "v_mfma_f32_16x16x32_bf16 v[158:161], v[126:129], a[96:99], v[158:161]\n"
        "v_cvt_f32_f16_sdwa v245, v226 dst_sel:DWORD dst_unused:UNUSED_PAD src0_sel:WORD_1\n"
        "v_mul_f32 v163, v163, v245\n"
        "v_mfma_f32_16x16x32_bf16 v[166:169], v[126:129], a[108:111], 0\n"
        "v_mul_f32 v41, v41, v245\n"
        "v_cvt_pk_f16_f32 v163, v163, v41\n"
        "v_mfma_f32_16x16x32_bf16 v[166:169], v[132:135], a[112:115], v[166:169]\n"
        "v_perm_b32 v40, v162, v176, s54\n"
        "v_perm_b32 v46, v163, v177, s54\n"
        "v_mfma_f32_16x16x32_bf16 v[166:169], v[138:141], a[116:119], v[166:169]\n"
        "v_cvt_f32_f16_sdwa v243, v227 dst_sel:DWORD dst_unused:UNUSED_PAD src0_sel:WORD_0\n"
        "v_mul_f32 v164, v164, v243\n"
        "v_mfma_f32_16x16x32_bf16 v[150:153], v[30:33], v[124:127], 0\n"
        "v_mul_f32 v42, v42, v243\n"
        "v_cvt_pk_f16_f32 v164, v164, v42\n"
        "v_mfma_f32_16x16x32_bf16 v[68:71], v[32:35], v[128:131], 0\n"
        "v_perm_b32 v41, v162, v180, s55\n"
        "v_perm_b32 v30, v164, v178, s54\n"
        "v_mfma_f32_16x16x32_bf16 v[34:37], v[234:237], a[248:251], 0\n"
        "ds_write_b64 v212, v[40:41]\n"
        "v_mfma_f32_16x16x32_bf16 v[34:37], v[214:217], a[244:247], v[34:37]\n"
        "v_perm_b32 v47, v163, v181, s55\n"
        "v_perm_b32 v31, v164, v182, s55\n"
        "v_mfma_f32_16x16x32_bf16 v[34:37], v[218:221], a[240:243], v[34:37]\n"
        "ds_write_b64 v212, v[46:47] offset:6336\n"
        "v_mfma_f32_16x16x32_bf16 v[174:177], v[218:221], a[252:255], 0\n"
        "ds_write_b64 v212, v[30:31] offset:12672\n"
        "v_mfma_f32_16x16x32_bf16 v[174:177], v[214:217], v[100:103], v[174:177]\n"
        "v_cvt_f32_f16_sdwa v242, v227 dst_sel:DWORD dst_unused:UNUSED_PAD src0_sel:WORD_1\n"
        "v_mul_f32 v165, v165, v242\n"
        "v_mfma_f32_16x16x32_bf16 v[174:177], v[234:237], v[48:51], v[174:177]\n"
        "v_mul_f32 v43, v43, v242\n"
        "v_cvt_pk_f16_f32 v165, v165, v43\n"
        "v_mfma_f32_16x16x32_bf16 v[170:173], v[214:217], v[52:55], 0\n"
        "v_perm_b32 v30, v165, v179, s54\n"
        "v_perm_b32 v31, v165, v183, s55\n"
        "v_mfma_f32_16x16x32_bf16 v[178:181], v[214:217], v[56:59], 0\n"
        "ds_write_b64 v212, v[30:31] offset:19008\n"
        "v_mfma_f32_16x16x32_bf16 v[162:165], v[146:149], v[146:149], 0\n"
        "v_cvt_pk_bf16_f32 v42, v158, v159\n"
        "v_cvt_pk_bf16_f32 v43, v160, v161\n"
        "v_mfma_f32_16x16x32_bf16 v[162:165], v[142:145], v[142:145], v[162:165]\n"
        "v_cvt_pk_bf16_f32 v46, v166, v167\n"
        "v_cvt_pk_bf16_f32 v47, v168, v169\n"
        "v_mfma_f32_16x16x32_bf16 v[162:165], v[154:157], v[154:157], v[162:165]\n"
        "v_mul_f32 v34, v34, v244\n"
        "v_mul_f32 v174, v174, v244\n"
        "v_mfma_f32_16x16x32_bf16 v[166:169], a[152:155], v[230:233], 0\n"
        "v_mul_f32 v35, v35, v245\n"
        "v_mul_f32 v175, v175, v245\n"
        "v_mfma_f32_16x16x32_bf16 v[166:169], a[148:151], v[222:225], v[166:169]\n"
        "v_cvt_pk_f16_f32 v34, v34, v174\n"
        "v_cvt_pk_f16_f32 v35, v35, v175\n"
        "v_mfma_f32_16x16x32_bf16 v[166:169], a[144:147], v[238:241], v[166:169]\n"
        "v_mul_f32 v36, v36, v243\n"
        "v_mul_f32 v176, v176, v243\n"
        "v_mfma_f32_16x16x32_bf16 v[158:161], a[156:159], v[238:241], 0\n"
        "v_mov_b64 v[136:137], v[124:125]\n"
        "v_cvt_pk_f16_f32 v36, v36, v176\n"
        "v_mfma_f32_16x16x32_bf16 v[158:161], a[160:163], v[222:225], v[158:161]\n"
        "v_perm_b32 v104, v34, v170, s54\n"
        "v_perm_b32 v40, v35, v171, s54\n"
        "v_mfma_f32_16x16x32_bf16 v[158:161], a[164:167], v[230:233], v[158:161]\n"
        "v_perm_b32 v30, v36, v172, s54\n"
        "v_mul_f32 v37, v37, v242\n"
        "v_mfma_f32_16x16x32_bf16 v[182:185], v[138:141], a[128:131], 0\n"
        "v_mul_f32 v177, v177, v242\n"
        "v_cvt_pk_f16_f32 v37, v37, v177\n"
        "v_mfma_f32_16x16x32_bf16 v[182:185], v[132:135], a[124:127], v[182:185]\n"
        "v_perm_b32 v105, v34, v178, s55\n"
        "v_perm_b32 v34, v37, v173, s54\n"
        "v_mfma_f32_16x16x32_bf16 v[182:185], v[126:129], a[120:123], v[182:185]\n"
        "ds_write_b64 v212, v[104:105] offset:8\n"
        "v_mfma_f32_16x16x32_bf16 v[142:145], v[126:129], a[132:135], 0\n"
        "v_perm_b32 v41, v35, v179, s55\n"
        "v_perm_b32 v31, v36, v180, s55\n"
        "v_mfma_f32_16x16x32_bf16 v[142:145], v[132:135], a[136:139], v[142:145]\n"
        "ds_write_b64 v212, v[40:41] offset:6344\n"
        "v_mfma_f32_16x16x32_bf16 v[142:145], v[138:141], a[140:143], v[142:145]\n"
        "ds_write_b64 v212, v[30:31] offset:12680\n"
        "v_mfma_f32_16x16x32_bf16 v[146:149], v[42:45], v[130:133], 0\n"
        "v_perm_b32 v35, v37, v181, s55\n"
        "v_cvt_pk_bf16_f32 v130, v166, v167\n"
        "v_mfma_f32_16x16x32_bf16 v[170:173], v[44:47], v[134:137], 0\n"
        "ds_write_b64 v212, v[34:35] offset:19016\n"
        "v_mfma_f32_16x16x32_bf16 v[154:157], v[234:237], v[76:79], 0\n"
        "v_cvt_pk_bf16_f32 v131, v168, v169\n"
        "v_cvt_pk_bf16_f32 v132, v158, v159\n"
        "v_mfma_f32_16x16x32_bf16 v[154:157], v[214:217], v[96:99], v[154:157]\n"
        "v_cvt_pk_bf16_f32 v133, v160, v161\n"
        "v_cvt_pk_bf16_f32 v36, v182, v183\n"
        "v_mfma_f32_16x16x32_bf16 v[154:157], v[218:221], v[84:87], v[154:157]\n"
        "v_cvt_pk_bf16_f32 v37, v184, v185\n"
        "v_cvt_pk_bf16_f32 v40, v142, v143\n"
        "v_mfma_f32_16x16x32_bf16 v[126:129], v[218:221], v[64:67], 0\n"
        "s_nop 0\n"
        "v_cvt_pk_bf16_f32 v41, v144, v145\n"
        "v_mov_b64 v[142:143], v[124:125]\n"
        "v_mfma_f32_16x16x32_bf16 v[126:129], v[214:217], v[92:95], v[126:129]\n"
        "s_nop 0\n"
        "v_mul_f32 v154, v154, v244\n"
        "v_mul_f32 v155, v155, v245\n"
        "v_mfma_f32_16x16x32_bf16 v[126:129], v[234:237], v[80:83], v[126:129]\n"
        "v_cvt_pk_bf16_f32 v174, v150, v151\n"
        "v_cvt_pk_bf16_f32 v175, v70, v71\n"
        "v_mfma_f32_16x16x32_bf16 v[122:125], v[234:237], v[52:55], 0\n"
        "s_nop 0\n"
        "v_cvt_pk_bf16_f32 v176, v170, v171\n"
        "v_mul_f32 v156, v156, v243\n"
        "v_mfma_f32_16x16x32_bf16 v[166:169], v[234:237], v[56:59], 0\n"
        "s_nop 0\n"
        "v_mul_f32 v126, v126, v244\n"
        "v_cvt_pk_f16_f32 v154, v154, v126\n"
        "v_mfma_f32_16x16x32_bf16 v[158:161], v[118:121], a[80:83], 0\n"
        "v_perm_b32 v30, v154, v122, s54\n"
        "v_mul_f32 v127, v127, v245\n"
        "v_mfma_f32_16x16x32_bf16 v[158:161], v[112:115], a[76:79], v[158:161]\n"
        "v_cvt_pk_f16_f32 v155, v155, v127\n"
        "v_perm_b32 v34, v155, v123, s54\n"
        "v_mfma_f32_16x16x32_bf16 v[158:161], v[106:109], a[72:75], v[158:161]\n"
        "v_perm_b32 v31, v154, v166, s55\n"
        "v_perm_b32 v35, v155, v167, s55\n"
        "v_mfma_f32_16x16x32_bf16 v[182:185], v[106:109], a[84:87], 0\n"
        "ds_write_b64 v212, v[30:31] offset:16\n"
        "v_mfma_f32_16x16x32_bf16 v[182:185], v[112:115], a[88:91], v[182:185]\n"
        "ds_write_b64 v212, v[34:35] offset:6352\n"
        "v_mfma_f32_16x16x32_bf16 v[182:185], v[118:121], a[92:95], v[182:185]\n"
        "v_mul_f32 v128, v128, v243\n"
        "v_cvt_pk_f16_f32 v156, v156, v128\n"
        "v_mfma_f32_16x16x32_bf16 v[178:181], v[36:39], v[136:139], 0\n"
        "v_perm_b32 v30, v156, v124, s54\n"
        "v_perm_b32 v31, v156, v168, s55\n"
        "v_mfma_f32_16x16x32_bf16 v[134:137], v[38:41], v[140:143], 0\n"
        "ds_write_b64 v212, v[30:31] offset:12688\n"
        "v_mfma_f32_16x16x32_bf16 v[40:43], a[176:179], v[230:233], 0\n"
        "v_cvt_pk_bf16_f32 v150, v152, v153\n"
        "v_cvt_pk_bf16_f32 v151, v146, v147\n"
        "v_mfma_f32_16x16x32_bf16 v[40:43], a[172:175], v[222:225], v[40:43]\n"
        "v_cvt_pk_bf16_f32 v152, v172, v173\n"
        "v_mul_f32 v157, v157, v242\n"
        "v_mfma_f32_16x16x32_bf16 v[40:43], a[168:171], v[238:241], v[40:43]\n"
        "v_mul_f32 v129, v129, v242\n"
        "v_cvt_pk_f16_f32 v157, v157, v129\n"
        "v_mfma_f32_16x16x32_bf16 v[144:147], a[180:183], v[238:241], 0\n"
        "v_perm_b32 v30, v157, v125, s54\n"
        "v_perm_b32 v31, v157, v169, s55\n"
        "v_mfma_f32_16x16x32_bf16 v[144:147], a[184:187], v[222:225], v[144:147]\n"
        "ds_write_b64 v212, v[30:31] offset:19024\n"
        "v_mfma_f32_16x16x32_bf16 v[144:147], a[188:191], v[230:233], v[144:147]\n"
        "v_and_b32_dpp v104, v253, v25 row_ror:8 row_mask:0xf bank_mask:0xf\n"
        "v_and_b32_dpp v105, v249, v25 row_ror:8 row_mask:0xf bank_mask:0xf\n"
        "v_mfma_f32_16x16x32_bf16 v[140:143], v[118:121], a[104:107], 0\n"
        "v_cvt_pk_bf16_f32 v122, v68, v69\n"
        "v_cvt_pk_bf16_f32 v123, v148, v149\n"
        "v_mfma_f32_16x16x32_bf16 v[140:143], v[112:115], a[100:103], v[140:143]\n"
        "v_mov_b64 v[110:111], v[104:105]\n"
        "v_cvt_pk_bf16_f32 v30, v158, v159\n"
        "v_mfma_f32_16x16x32_bf16 v[140:143], v[106:109], a[96:99], v[140:143]\n"
        "v_cvt_pk_bf16_f32 v31, v160, v161\n"
        "v_cvt_pk_bf16_f32 v34, v182, v183\n"
        "v_mfma_f32_16x16x32_bf16 v[68:71], v[106:109], a[108:111], 0\n"
        "v_cvt_pk_bf16_f32 v35, v184, v185\n"
        "v_cvt_pk_bf16_f32 v124, v178, v179\n"
        "v_mfma_f32_16x16x32_bf16 v[68:71], v[112:115], a[112:115], v[68:71]\n"
        "v_cvt_pk_bf16_f32 v177, v180, v181\n"
        "v_cvt_pk_bf16_f32 v153, v134, v135\n"
        "v_mfma_f32_16x16x32_bf16 v[68:71], v[118:121], a[116:119], v[68:71]\n"
        "ds_write_b128 v12, v[174:177] offset:3072\n"
        "v_mfma_f32_16x16x32_bf16 v[126:129], v[30:33], v[104:107], 0\n"
        "ds_write_b128 v12, v[150:153] offset:4096\n"
        "v_mfma_f32_16x16x32_bf16 v[154:157], v[32:35], v[108:111], 0\n"
        "v_cvt_pk_bf16_f32 v125, v136, v137\n"
        "v_cvt_pk_bf16_f32 v136, v40, v41\n"
        "v_mfma_f32_16x16x32_bf16 v[34:37], v[230:233], a[224:227], 0\n"
        "ds_write_b128 v12, v[122:125] offset:5120\n"
        "v_mfma_f32_16x16x32_bf16 v[34:37], v[222:225], a[220:223], v[34:37]\n"
        "v_cvt_pk_bf16_f32 v137, v42, v43\n"
        "v_cvt_pk_bf16_f32 v138, v144, v145\n"
        "v_mfma_f32_16x16x32_bf16 v[34:37], v[238:241], a[216:219], v[34:37]\n"
        "v_cvt_pk_bf16_f32 v139, v146, v147\n"
        "v_cvt_pk_bf16_f32 v42, v140, v141\n"
        "v_mfma_f32_16x16x32_bf16 v[158:161], v[238:241], a[228:231], 0\n"
        "v_cvt_pk_bf16_f32 v43, v142, v143\n"
        "v_cvt_pk_bf16_f32 v46, v68, v69\n"
        "v_mfma_f32_16x16x32_bf16 v[158:161], v[222:225], a[232:235], v[158:161]\n"
        "v_cvt_pk_bf16_f32 v47, v70, v71\n"
        "v_mov_b64 v[116:117], v[104:105]\n"
        "v_mfma_f32_16x16x32_bf16 v[158:161], v[230:233], a[236:239], v[158:161]\n"
        "v_cvt_f32_f16_sdwa v245, v228 dst_sel:DWORD dst_unused:UNUSED_PAD src0_sel:WORD_0\n"
        "v_mul_f32 v34, v34, v245\n"
        "v_mfma_f32_16x16x32_bf16 v[140:143], v[238:241], v[52:55], 0\n"
        "s_nop 0\n"
        "v_cvt_f32_f16_sdwa v253, v228 dst_sel:DWORD dst_unused:UNUSED_PAD src0_sel:WORD_1\n"
        "v_mul_f32 v35, v35, v253\n"
        "v_mfma_f32_16x16x32_bf16 v[144:147], v[238:241], v[56:59], 0\n"
        "s_nop 0\n"
        "v_mul_f32 v158, v158, v245\n"
        "v_cvt_pk_f16_f32 v34, v34, v158\n"
        "v_mfma_f32_16x16x32_bf16 v[68:71], v[174:177], v[174:177], 0\n"
        "v_perm_b32 v30, v34, v140, s54\n"
        "v_mul_f32 v159, v159, v253\n"
        "v_mfma_f32_16x16x32_bf16 v[68:71], v[150:153], v[150:153], v[68:71]\n"
        "s_nop 0\n"
        "v_perm_b32 v31, v34, v144, s55\n"
        "v_cvt_pk_f16_f32 v35, v35, v159\n"
        "v_mfma_f32_16x16x32_bf16 v[68:71], v[122:125], v[122:125], v[68:71]\n"
        "ds_write_b64 v212, v[30:31] offset:25344\n"
        "v_mfma_f32_16x16x32_bf16 v[122:125], v[118:121], a[128:131], 0\n"
        "ds_write_b128 v209, v[130:133]\n"
        "v_mfma_f32_16x16x32_bf16 v[122:125], v[112:115], a[124:127], v[122:125]\n"
        "v_perm_b32 v30, v35, v141, s54\n"
        "v_perm_b32 v31, v35, v145, s55\n"
        "v_mfma_f32_16x16x32_bf16 v[122:125], v[106:109], a[120:123], v[122:125]\n"
        "ds_write_b64 v212, v[30:31] offset:31680\n"
        "v_mfma_f32_16x16x32_bf16 v[148:151], v[106:109], a[132:135], 0\n"
        "ds_write_b128 v209, v[136:139] offset:64\n"
        "v_mfma_f32_16x16x32_bf16 v[148:151], v[112:115], a[136:139], v[148:151]\n"
        "v_cvt_f32_f16_sdwa v249, v229 dst_sel:DWORD dst_unused:UNUSED_PAD src0_sel:WORD_0\n"
        "v_mul_f32 v36, v36, v249\n"
        "v_mfma_f32_16x16x32_bf16 v[148:151], v[118:121], a[140:143], v[148:151]\n"
        "v_mul_f32 v160, v160, v249\n"
        "v_cvt_pk_f16_f32 v36, v36, v160\n"
        "v_mfma_f32_16x16x32_bf16 v[158:161], v[42:45], v[110:113], 0\n"
        "v_perm_b32 v30, v36, v142, s54\n"
        "v_perm_b32 v31, v36, v146, s55\n"
        "v_mfma_f32_16x16x32_bf16 v[106:109], v[44:47], v[114:117], 0\n"
        "ds_write_b64 v213, v[30:31]\n"
        "v_mfma_f32_16x16x32_bf16 v[110:113], a[200:203], v[230:233], 0\n"
        "v_cvt_pk_bf16_f32 v36, v122, v123\n"
        "v_cvt_pk_bf16_f32 v37, v124, v125\n"
        "v_mfma_f32_16x16x32_bf16 v[110:113], a[196:199], v[222:225], v[110:113]\n"
        "v_cvt_pk_bf16_f32 v40, v148, v149\n"
        "v_cvt_pk_bf16_f32 v41, v150, v151\n"
        "v_mfma_f32_16x16x32_bf16 v[110:113], a[192:195], v[238:241], v[110:113]\n"
        "v_mov_b64 v[122:123], v[104:105]\n"
        "v_cvt_pk_bf16_f32 v174, v126, v127\n"
        "v_mfma_f32_16x16x32_bf16 v[124:127], a[204:207], v[238:241], 0\n"
        "s_nop 0\n"
        "v_cvt_pk_bf16_f32 v175, v156, v157\n"
        "v_cvt_pk_bf16_f32 v176, v106, v107\n"
        "v_mfma_f32_16x16x32_bf16 v[124:127], a[208:211], v[222:225], v[124:127]\n"
        "s_nop 0\n"
        "v_cvt_pk_bf16_f32 v170, v110, v111\n"
        "v_cvt_pk_bf16_f32 v171, v112, v113\n"
        "v_mfma_f32_16x16x32_bf16 v[124:127], a[212:215], v[230:233], v[124:127]\n"
        "v_cvt_pk_bf16_f32 v104, v128, v129\n"
        "v_cvt_pk_bf16_f32 v105, v158, v159\n"
        "v_mfma_f32_16x16x32_bf16 v[110:113], v[230:233], a[248:251], 0\n"
        "s_nop 0\n"
        "v_cvt_pk_bf16_f32 v106, v108, v109\n"
        "v_cvt_pk_bf16_f32 v114, v154, v155\n"
        "v_mfma_f32_16x16x32_bf16 v[110:113], v[222:225], a[244:247], v[110:113]\n"
        "s_nop 0\n"
        "v_cvt_pk_bf16_f32 v172, v124, v125\n"
        "v_cvt_pk_bf16_f32 v173, v126, v127\n"
        "v_mfma_f32_16x16x32_bf16 v[110:113], v[238:241], a[240:243], v[110:113]\n"
        "s_nop 0\n"
        "ds_write_b128 v209, v[170:173] offset:128\n"
        "v_mfma_f32_16x16x32_bf16 v[140:143], v[238:241], a[252:255], 0\n"
        "s_nop 0\n"
        "v_cvt_pk_bf16_f32 v115, v160, v161\n"
        "v_mul_f32_dpp v244, v254, v252 quad_perm:[0,0,0,0] row_mask:0xf bank_mask:0xf\n"
        "v_mfma_f32_16x16x32_bf16 v[140:143], v[222:225], v[100:103], v[140:143]\n"
        "s_nop 0\n"
        "v_mul_f32 v110, v110, v245\n"
        "v_mul_f32 v111, v111, v253\n"
        "v_mfma_f32_16x16x32_bf16 v[140:143], v[230:233], v[48:51], v[140:143]\n"
        "v_mul_f32 v112, v112, v249\n"
        "v_fmac_f32_dpp v244, v254, v251 quad_perm:[1,1,1,1] row_mask:0xf bank_mask:0xf\n"
        "v_mfma_f32_16x16x32_bf16 v[154:157], v[222:225], v[52:55], 0\n"
        "s_nop 0\n"
        "v_cndmask_b32_e64 v252, v68, v69, s[56:57]\n"
        "v_cndmask_b32_e64 v252, v252, v70, s[58:59]\n"
        "v_mfma_f32_16x16x32_bf16 v[166:169], v[222:225], v[56:59], 0\n"
        "s_nop 0\n"
        "v_mul_f32 v140, v140, v245\n"
        "v_cvt_pk_f16_f32 v110, v110, v140\n"
        "v_mfma_f32_16x16x32_bf16 v[144:147], v[36:39], v[116:119], 0\n"
        "v_perm_b32 v30, v110, v154, s54\n"
        "v_mul_f32 v141, v141, v253\n"
        "v_mfma_f32_16x16x32_bf16 v[124:127], v[38:41], v[120:123], 0\n"
        "s_nop 0\n"
        "v_perm_b32 v31, v110, v166, s55\n"
        "v_cvt_pk_f16_f32 v111, v111, v141\n"
        "v_mfma_f32_16x16x32_bf16 v[34:37], v[230:233], v[76:79], 0\n"
        "ds_write_b64 v212, v[30:31] offset:25352\n"
        "v_mfma_f32_16x16x32_bf16 v[34:37], v[222:225], v[96:99], v[34:37]\n"
        "v_cvt_pk_bf16_f32 v177, v146, v147\n"
        "v_cvt_pk_bf16_f32 v116, v144, v145\n"
        "v_mfma_f32_16x16x32_bf16 v[34:37], v[238:241], v[84:87], v[34:37]\n"
        "ds_write_b128 v12, v[174:177] offset:6144\n"
        "v_mfma_f32_16x16x32_bf16 v[178:181], v[238:241], v[64:67], 0\n"
        "s_waitcnt vmcnt(0)\n"
        "v_cvt_pk_bf16_f32 v107, v124, v125\n"
        "v_cvt_pk_bf16_f32 v117, v126, v127\n"
        "v_mfma_f32_16x16x32_bf16 v[178:181], v[222:225], v[92:95], v[178:181]\n"
        "ds_write_b128 v12, v[104:107] offset:7168\n"
        "v_mfma_f32_16x16x32_bf16 v[178:181], v[230:233], v[80:83], v[178:181]\n"
        "ds_write_b128 v12, v[114:117] offset:8192\n"
        "v_mfma_f32_16x16x32_bf16 v[118:121], v[230:233], v[52:55], 0\n"
        "ds_read_b128 v[146:149], v9\n"
        "v_perm_b32 v30, v111, v155, s54\n"
        "v_mfma_f32_16x16x32_bf16 v[182:185], v[230:233], v[56:59], 0\n"
        "ds_read_b128 v[152:155], v10\n"
        "v_perm_b32 v31, v111, v167, s55\n"
        "v_mfma_f32_16x16x32_bf16 v[122:125], v[174:177], v[174:177], 0\n"
        "ds_write_b64 v212, v[30:31] offset:31688\n"
        "v_mfma_f32_16x16x32_bf16 v[122:125], v[104:107], v[104:107], v[122:125]\n"
        "ds_read_b128 v[158:161], v11\n"
        "v_mul_f32 v142, v142, v249\n"
        "v_mfma_f32_16x16x32_bf16 v[122:125], v[114:117], v[114:117], v[122:125]\n"
        "ds_read_b128 v[126:129], v9 offset:3072\n"
        "v_cvt_pk_f16_f32 v112, v112, v142\n"
        "v_mfma_f32_16x16x32_bf16 v[40:43], v[238:241], v[130:133], 0\n"
        "ds_read_b128 v[132:135], v10 offset:3072\n"
        "v_perm_b32 v30, v112, v156, s54\n"
        "v_mfma_f32_16x16x32_bf16 v[40:43], v[222:225], v[136:139], v[40:43]\n"
        "ds_read_b128 v[138:141], v11 offset:3072\n"
        "v_perm_b32 v31, v112, v168, s55\n"
        "v_mfma_f32_16x16x32_bf16 v[40:43], v[230:233], v[170:173], v[40:43]\n"
        "ds_write_b64 v213, v[30:31] offset:8\n"
        "v_cndmask_b32_e64 v252, v252, v71, s[60:61]\n"
        "v_fmac_f32_dpp v244, v254, v206 quad_perm:[2,2,2,2] row_mask:0xf bank_mask:0xf\n"
        "v_cndmask_b32_e64 v251, v162, v163, s[56:57]\n"
        "v_cndmask_b32_e64 v251, v251, v164, s[58:59]\n"
        "v_cndmask_b32_e64 v251, v251, v165, s[60:61]\n"
        "v_fmac_f32_dpp v244, v254, v250 quad_perm:[3,3,3,3] row_mask:0xf bank_mask:0xf\n"
        "v_mul_f32 v34, v34, v245\n"
        "v_mul_f32 v178, v178, v245\n"
        "v_add_f32 v244, v244, v255\n"
        "v_cvt_pk_f16_f32 v34, v34, v178\n"
        "v_mul_f32 v254, s80, v244\n"
        "v_cvt_f32_f16_sdwa v250, v248 dst_sel:DWORD dst_unused:UNUSED_PAD src0_sel:WORD_1\n"
        "v_perm_b32 v46, v34, v118, s54\n"
        "v_mul_f32 v35, v35, v253\n"
        "v_mul_f32 v179, v179, v253\n"
        "v_mul_f32 v36, v36, v249\n"
        "v_mul_f32 v180, v180, v249\n"
        "v_cvt_pk_f16_f32 v35, v35, v179\n"
        "v_cvt_pk_f16_f32 v36, v36, v180\n"
        "v_mov_b32 v255, 0x3c003c00\n"
        "v_perm_b32 v30, v35, v119, s54\n"
        "v_lshrrev_b32 v37, 16, v248\n"
        "v_mov_b32 v253, s29\n"
        "v_perm_b32 v104, v36, v120, s54\n"
        "v_cndmask_b32_e64 v68, 0, v255, s[76:77]\n"
        "v_cndmask_b32_e64 v69, 0, v255, s[56:57]\n"
        "v_cndmask_b32_e64 v70, 0, v255, s[58:59]\n"
        "v_cndmask_b32_e64 v71, 0, v255, s[60:61]\n"
        "v_readfirstlane_b32 s20, v246\n"
        "v_readfirstlane_b32 s21, v247\n"
        "v_perm_b32 v47, v34, v182, s55\n"
        "v_perm_b32 v31, v35, v183, s55\n"
        "v_perm_b32 v105, v36, v184, s55\n"
        "ds_write_b64 v212, v[46:47] offset:25360\n"
        "ds_write_b64 v212, v[30:31] offset:31696\n"
        "ds_write_b64 v213, v[104:105] offset:16\n"
        "ds_read_b128 v[106:109], v9 offset:6144\n"
        "v_cndmask_b32_e64 v255, v122, v123, s[56:57]\n"
        "v_cndmask_b32_e64 v255, v255, v124, s[58:59]\n"
        "v_cndmask_b32_e64 v255, v255, v125, s[60:61]\n"
        "ds_read_b128 v[112:115], v10 offset:6144\n"
        "ds_read_b128 v[118:121], v11 offset:6144\n"
        "v_cndmask_b32_dpp v252, v255, v252, vcc row_ror:4 row_mask:0xf bank_mask:0xf\n"
        "v_cndmask_b32_e64 v36, v40, v41, s[56:57]\n"
        "v_cndmask_b32_e64 v36, v36, v42, s[58:59]\n"
        "v_cndmask_b32_dpp v251, v252, v251, vcc row_ror:4 row_mask:0xf bank_mask:0xf\n"
        "v_fma_f32 v254, -v254, v244, v251\n"
        "v_max_f32 v254, 0, v254\n"
        "v_sub_f32 v251, 0x47c00000, v251\n"
        "v_fmaak_f32 v254, v28, v254, 0x3727c5ac\n"
        "v_bfi_b32 v254, s84, v251, v254\n"
        "v_rsq_f32 v255, v254\n"
        "v_cndmask_b32_e64 v36, v36, v43, s[60:61]\n"
        "v_cvt_f16_f32 v255, v255\n"
        "v_mul_f32 v36, v36, v250\n"
        "ds_write_b16 v18, v255\n"
        "ds_write_b16 v19, v255\n"
        "v_mul_f32 v36, v36, v250\n"
        "v_exp_f32 v36, v36\n"
        "ds_write_b16 v22, v253 offset:3072\n"
        "ds_write_b64 v211, v[36:37]\n"
        "s_waitcnt lgkmcnt(0)\n"
        "s_nop 7\n"
        ".p2align 6\n"
        "TOP_%=:\n"
        "v_mfma_f32_16x16x32_bf16 v[122:125], v[146:149], v[146:149], 0\n"
        "s_mov_b32 s26, s89\n"
        "ds_read_b128 v[182:185], v20 offset:512\n"
        "s_mov_b32 s30, s29\n"
        "v_mfma_f32_16x16x32_bf16 v[122:125], v[152:155], v[152:155], v[122:125]\n"
        "s_add_u32 s88, s88, 32\n"
        "v_add_u32 v23, s22, v23\n"
        "v_add_u32 v24, s22, v24\n"
        "v_mfma_f32_16x16x32_bf16 v[122:125], v[158:161], v[158:161], v[122:125]\n"
        "s_mov_b32 s22, s23\n"
        "ds_read_b128 v[168:171], v20 offset:528\n"
        "s_add_u32 s89, s86, s51\n"
        "v_mfma_f32_16x16x32_bf16 v[34:37], v[88:91], v[158:161], 0\n"
        "s_add_u32 s29, s29, 1\n"
        "s_cmp_ge_u32 s88, 73\n"
        "v_cvt_pk_bf16_f32 v194, v162, v178\n"
        "v_mfma_f32_16x16x32_bf16 v[34:37], v[60:63], v[152:155], v[34:37]\n"
        "s_movk_i32 s23, 1584\n"
        "v_cvt_pk_bf16_f32 v198, v163, v179\n"
        "v_lshlrev_b32 v172, 16, v234\n"
        "v_mfma_f32_16x16x32_bf16 v[34:37], v[72:75], v[146:149], v[34:37]\n"
        "s_mov_b32 s86, s87\n"
        "v_lshlrev_b32 v173, 16, v210\n"
        "v_lshlrev_b32 v174, 16, v242\n"
        "v_mfma_f32_16x16x32_bf16 v[186:189], a[0:3], v[146:149], 0\n"
        "s_cselect_b32 s92, 588672, 0\n"
        "v_cvt_pk_bf16_f32 v202, v164, v180\n"
        "v_lshlrev_b32 v175, 16, v218\n"
        "v_mfma_f32_16x16x32_bf16 v[186:189], a[4:7], v[152:155], v[186:189]\n"
        "s_cselect_b32 s94, 73, 0\n"
        "s_add_u32 s27, s26, 392448\n"
        "s_add_u32 s28, s26, 490560\n"
        "v_mfma_f32_16x16x32_bf16 v[186:189], a[8:11], v[158:161], v[186:189]\n"
        "buffer_store_dwordx3 v[192:194], v15, s[40:43], s27 offen offset:768 sc1 nt\n"
        "s_add_u32 s87, s87, 43008\n"
        "v_lshlrev_b32 v144, 16, v206\n"
        "v_lshlrev_b32 v145, 16, v222\n"
        "v_cvt_pk_bf16_f32 v178, v165, v181\n"
        "v_mfma_f32_16x16x32_bf16 v[192:195], a[20:23], v[158:161], 0\n"
        "s_sub_u32 s88, s88, s94\n"
        "v_cndmask_b32_e64 v255, v122, v123, s[56:57]\n"
        "v_cvt_pk_bf16_f32 v180, v186, v187\n"
        "v_mfma_f32_16x16x32_bf16 v[192:195], a[16:19], v[152:155], v[192:195]\n"
        "s_add_u32 s87, s87, s92\n"
        "s_cmp_le_i32 s85, 0\n"
        "v_cvt_pk_bf16_f32 v181, v188, v189\n"
        "v_mfma_f32_16x16x32_bf16 v[192:195], a[12:15], v[146:149], v[192:195]\n"
        "buffer_store_dwordx3 v[196:198], v14, s[40:43], s28 offen offset:192 sc1 nt\n"
        "s_waitcnt lgkmcnt(0)\n"
        "v_pk_mul_f16 v162, v182, v224\n"
        "v_pk_mul_f16 v163, v182, v244\n"
        "v_pk_mul_f16 v164, v182, v212\n"
        "v_mfma_f32_16x16x32_bf16 v[196:199], a[24:27], v[146:149], 0\n"
        "s_cbranch_scc1 DY1_%=\n"
        "DY1B_%=:\n"
        "s_add_u32 s27, s26, 588672\n"
        "v_pk_mul_f16 v165, v182, v228\n"
        "v_mfma_f32_16x16x32_bf16 v[196:199], a[28:31], v[152:155], v[196:199]\n"
        "s_cmp_gt_i32 s85, 0\n"
        "v_pk_mul_f16 v166, v182, v40\n"
        "v_pk_mul_f16 v167, v182, v236\n"
        "v_mfma_f32_16x16x32_bf16 v[196:199], a[32:35], v[158:161], v[196:199]\n"
        "buffer_store_dwordx3 v[200:202], v16, s[40:43], s28 offen offset:960 sc1 nt\n"
        "v_pk_fma_f16 v162, v183, v225, v162\n"
        "v_pk_fma_f16 v163, v183, v245, v163\n"
        "v_pk_fma_f16 v164, v183, v213, v164\n"
        "v_pk_fma_f16 v165, v183, v229, v165\n"
        "v_mfma_f32_16x16x32_bf16 v[188:191], a[44:47], v[158:161], 0\n"
        "s_cselect_b32 s94, s87, s84\n"
        "v_pk_fma_f16 v166, v183, v41, v166\n"
        "v_pk_fma_f16 v167, v183, v237, v167\n"
        "v_mfma_f32_16x16x32_bf16 v[188:191], a[40:43], v[152:155], v[188:191]\n"
        "s_sub_u32 s85, s85, 1\n"
        "v_pk_fma_f16 v162, v184, v226, v162\n"
        "v_pk_fma_f16 v163, v184, v246, v163\n"
        "v_mfma_f32_16x16x32_bf16 v[188:191], a[36:39], v[146:149], v[188:191]\n"
        "buffer_store_dwordx3 v[176:178], v14, s[40:43], s27 offen offset:384 sc1 nt\n"
        "v_pk_fma_f16 v164, v184, v214, v164\n"
        "v_pk_fma_f16 v165, v184, v230, v165\n"
        "v_pk_fma_f16 v166, v184, v42, v166\n"
        "v_pk_fma_f16 v167, v184, v238, v167\n"
        "v_mfma_f32_16x16x32_bf16 v[176:179], a[48:51], v[146:149], 0\n"
        "s_waitcnt lgkmcnt(0)\n"
        "v_pk_fma_f16 v162, v185, v227, v162\n"
        "v_pk_fma_f16 v163, v185, v247, v163\n"
        "v_mfma_f32_16x16x32_bf16 v[176:179], a[52:55], v[152:155], v[176:179]\n"
        "s_waitcnt lgkmcnt(0)\n"
        "v_pk_fma_f16 v164, v185, v215, v164\n"
        "v_pk_fma_f16 v165, v185, v231, v165\n"
        "v_mfma_f32_16x16x32_bf16 v[176:179], a[56:59], v[158:161], v[176:179]\n"
        "v_pk_fma_f16 v166, v185, v43, v166\n"
        "v_pk_fma_f16 v167, v185, v239, v167\n"
        "v_mfma_f32_16x16x32_bf16 v[40:43], a[68:71], v[158:161], 0\n"
        "v_pk_fma_f16 v162, v168, v232, v162\n"
        "v_pk_fma_f16 v163, v168, v208, v163\n"
        "v_mfma_f32_16x16x32_bf16 v[40:43], a[64:67], v[152:155], v[40:43]\n"
        "v_cvt_pk_bf16_f32 v182, v192, v193\n"
        "v_cvt_pk_bf16_f32 v183, v194, v195\n"
        "v_mfma_f32_16x16x32_bf16 v[40:43], a[60:63], v[146:149], v[40:43]\n"
        "v_cvt_pk_bf16_f32 v192, v196, v197\n"
        "v_cvt_pk_bf16_f32 v193, v198, v199\n"
        "v_mfma_f32_16x16x32_bf16 v[184:187], v[126:129], v[126:129], 0\n"
        "v_cvt_pk_bf16_f32 v194, v188, v189\n"
        "v_cvt_pk_bf16_f32 v195, v190, v191\n"
        "v_mfma_f32_16x16x32_bf16 v[184:187], v[132:135], v[132:135], v[184:187]\n"
        "v_cvt_pk_bf16_f32 v188, v176, v177\n"
        "v_cvt_pk_bf16_f32 v189, v178, v179\n"
        "v_mfma_f32_16x16x32_bf16 v[184:187], v[138:141], v[138:141], v[184:187]\n"
        "v_cvt_pk_bf16_f32 v190, v40, v41\n"
        "v_cvt_pk_bf16_f32 v191, v42, v43\n"
        "v_mfma_f32_16x16x32_bf16 v[176:179], v[88:91], v[138:141], 0\n"
        "v_pk_fma_f16 v164, v168, v240, v164\n"
        "v_pk_fma_f16 v165, v168, v216, v165\n"
        "v_mfma_f32_16x16x32_bf16 v[176:179], v[60:63], v[132:135], v[176:179]\n"
        "v_pk_fma_f16 v166, v168, v204, v166\n"
        "v_pk_fma_f16 v167, v168, v220, v167\n"
        "v_mfma_f32_16x16x32_bf16 v[176:179], v[72:75], v[126:129], v[176:179]\n"
        "v_pk_fma_f16 v162, v169, v233, v162\n"
        "v_pk_fma_f16 v163, v169, v209, v163\n"
        "v_mfma_f32_16x16x32_bf16 v[40:43], v[146:149], v[180:183], 0\n"
        "v_pk_fma_f16 v164, v169, v241, v164\n"
        "v_pk_fma_f16 v165, v169, v217, v165\n"
        "v_mfma_f32_16x16x32_bf16 v[40:43], v[152:155], v[192:195], v[40:43]\n"
        "v_pk_fma_f16 v166, v169, v205, v166\n"
        "v_pk_fma_f16 v167, v169, v221, v167\n"
        "v_mfma_f32_16x16x32_bf16 v[40:43], v[158:161], v[188:191], v[40:43]\n"
        "v_pk_fma_f16 v162, v170, v234, v162\n"
        "v_pk_fma_f16 v163, v170, v210, v163\n"
        "v_mfma_f32_16x16x32_bf16 v[178:181], a[8:11], v[138:141], 0\n"
        "v_pk_fma_f16 v164, v170, v242, v164\n"
        "v_pk_fma_f16 v165, v170, v218, v165\n"
        "v_mfma_f32_16x16x32_bf16 v[178:181], a[4:7], v[132:135], v[178:181]\n"
        "v_pk_fma_f16 v166, v170, v206, v166\n"
        "v_pk_fma_f16 v167, v170, v222, v167\n"
        "v_mfma_f32_16x16x32_bf16 v[178:181], a[0:3], v[126:129], v[178:181]\n"
        "v_cndmask_b32_e64 v255, v255, v124, s[58:59]\n"
        "v_cndmask_b32_e64 v255, v255, v125, s[60:61]\n"
        "v_mfma_f32_16x16x32_bf16 v[122:125], a[12:15], v[126:129], 0\n"
        "s_nop 0\n"
        "v_cndmask_b32_e64 v254, v184, v185, s[56:57]\n"
        "v_cndmask_b32_e64 v254, v254, v186, s[58:59]\n"
        "v_mfma_f32_16x16x32_bf16 v[122:125], a[16:19], v[132:135], v[122:125]\n"
        "s_add_u32 s46, s26, 590016\n"
        "v_cvt_pk_bf16_f32 v168, v178, v179\n"
        "v_cvt_pk_bf16_f32 v169, v180, v181\n"
        "v_mfma_f32_16x16x32_bf16 v[122:125], a[20:23], v[138:141], v[122:125]\n"
        "s_nop 0\n"
        "v_cndmask_b32_e64 v254, v254, v187, s[60:61]\n"
        "v_mfma_f32_16x16x32_f16 v[172:175], v[68:71], v[162:165], v[172:175]\n"
        "s_min_u32 s46, s46, s42\n"
        "s_nop 1\n"
        "v_mfma_f32_16x16x32_f16 v[142:145], v[68:71], v[164:167], v[142:145]\n"
        "s_nop 0\n"
        "v_cvt_pk_bf16_f32 v170, v122, v123\n"
        "v_cvt_pk_bf16_f32 v171, v124, v125\n"
        "v_mfma_f32_16x16x32_bf16 v[162:165], a[32:35], v[138:141], 0\n"
        "v_cvt_pk_bf16_f32 v178, v172, v173\n"
        "v_cvt_pk_bf16_f32 v179, v174, v175\n"
        "v_mfma_f32_16x16x32_bf16 v[162:165], a[28:31], v[132:135], v[162:165]\n"
        "s_nop 0\n"
        "v_cvt_pk_bf16_f32 v180, v144, v145\n"
        "v_mfma_f32_16x16x32_bf16 v[162:165], a[24:27], v[126:129], v[162:165]\n"
        "s_nop 1\n"
        "v_mfma_f32_16x16x32_bf16 v[122:125], a[36:39], v[126:129], 0\n"
        "s_nop 2\n"
        "v_mfma_f32_16x16x32_bf16 v[122:125], a[40:43], v[132:135], v[122:125]\n"
        "s_nop 0\n"
        "v_cvt_pk_bf16_f32 v142, v162, v163\n"
        "v_cvt_pk_bf16_f32 v143, v164, v165\n"
        "v_mfma_f32_16x16x32_bf16 v[122:125], a[44:47], v[138:141], v[122:125]\n"
        "buffer_store_dwordx3 v[178:180], v17, s[44:47], s27 offen offset:1152 sc1 nt\n"
        "s_nop 0\n"
        "v_mfma_f32_16x16x32_bf16 v[164:167], a[56:59], v[138:141], 0\n"
        "s_nop 2\n"
        "v_mfma_f32_16x16x32_bf16 v[164:167], a[52:55], v[132:135], v[164:167]\n"
        "s_nop 0\n"
        "v_cvt_pk_bf16_f32 v144, v122, v123\n"
        "v_cvt_pk_bf16_f32 v145, v124, v125\n"
        "v_mfma_f32_16x16x32_bf16 v[164:167], a[48:51], v[126:129], v[164:167]\n"
        "s_nop 1\n"
        "v_mfma_f32_16x16x32_bf16 v[172:175], a[60:63], v[126:129], 0\n"
        "s_nop 2\n"
        "v_mfma_f32_16x16x32_bf16 v[172:175], a[64:67], v[132:135], v[172:175]\n"
        "s_nop 0\n"
        "v_cvt_pk_bf16_f32 v162, v164, v165\n"
        "v_cvt_pk_bf16_f32 v163, v166, v167\n"
        "v_mfma_f32_16x16x32_bf16 v[172:175], a[68:71], v[138:141], v[172:175]\n"
        "s_nop 0\n"
        "ds_read_u16 v253, v19 offset:2048\n"
        "v_mfma_f32_16x16x32_bf16 v[122:125], v[106:109], v[106:109], 0\n"
        "s_nop 2\n"
        "v_mfma_f32_16x16x32_bf16 v[122:125], v[112:115], v[112:115], v[122:125]\n"
        "s_nop 0\n"
        "v_cvt_pk_bf16_f32 v164, v172, v173\n"
        "v_cvt_pk_bf16_f32 v165, v174, v175\n"
        "v_mfma_f32_16x16x32_bf16 v[122:125], v[118:121], v[118:121], v[122:125]\n"
        "s_nop 1\n"
        "v_mfma_f32_16x16x32_bf16 v[172:175], v[88:91], v[118:121], 0\n"
        "s_nop 2\n"
        "v_mfma_f32_16x16x32_bf16 v[172:175], v[60:63], v[112:115], v[172:175]\n"
        "s_nop 0\n"
        "v_cndmask_b32_e64 v252, v122, v123, s[56:57]\n"
        "v_cndmask_b32_e64 v252, v252, v124, s[58:59]\n"
        "v_mfma_f32_16x16x32_bf16 v[172:175], v[72:75], v[106:109], v[172:175]\n"
        "s_cmp_le_i32 s85, 1\n"
        "v_cndmask_b32_e64 v252, v252, v125, s[60:61]\n"
        "v_mfma_f32_16x16x32_bf16 v[122:125], v[126:129], v[168:171], 0\n"
        "s_cbranch_scc1 DY2_%=\n"
        "DY2B_%=:\n"
        "v_cndmask_b32_dpp v254, v252, v254, vcc row_ror:4 row_mask:0xf bank_mask:0xf\n"
        "v_mfma_f32_16x16x32_bf16 v[122:125], v[132:135], v[142:145], v[122:125]\n"
        "s_waitcnt lgkmcnt(0)\n"
        "v_cmp_ne_u32_e64 s[24:25], s30, v253\n"
        "v_cndmask_b32_dpp v255, v254, v255, vcc row_ror:4 row_mask:0xf bank_mask:0xf\n"
        "v_mfma_f32_16x16x32_bf16 v[122:125], v[138:141], v[162:165], v[122:125]\n"
        "v_cndmask_b32_dpp v245, v172, v176, vcc row_ror:4 row_mask:0xf bank_mask:0xf\n"
        "v_cndmask_b32_dpp v248, v173, v177, vcc row_ror:4 row_mask:0xf bank_mask:0xf\n"
        "v_mfma_f32_16x16x32_bf16 v[162:165], a[8:11], v[118:121], 0\n"
        "s_add_u32 m0, s52, 0\n"
        "v_cndmask_b32_dpp v245, v245, v34, vcc row_ror:4 row_mask:0xf bank_mask:0xf\n"
        "v_mul_f32 v254, s80, v245\n"
        "v_mfma_f32_16x16x32_bf16 v[162:165], a[4:7], v[112:115], v[162:165]\n"
        "s_cmp_lg_u64 s[24:25], 0\n"
        "ds_read_b128 v[212:215], v23\n"
        "v_fma_f32 v254, -v254, v245, v255\n"
        "v_mfma_f32_16x16x32_bf16 v[162:165], a[0:3], v[106:109], v[162:165]\n"
        "s_cbranch_scc1 SP6_%=\n"
        "BK6_%=:\n"
        "buffer_load_dwordx4 v0, s[36:39], s94 offen lds\n"
        "v_max_f32 v254, 0, v254\n"
        "v_mfma_f32_16x16x32_bf16 v[224:227], a[12:15], v[106:109], 0\n"
        "ds_read_b128 v[218:221], v23 offset:64\n"
        "ds_read_b128 v[142:145], v23 offset:128\n"
        "v_mfma_f32_16x16x32_bf16 v[224:227], a[16:19], v[112:115], v[224:227]\n"
        "ds_read_b64 v[36:37], v24 offset:360\n"
        "v_mov_b32 v255, s29\n"
        "v_mfma_f32_16x16x32_bf16 v[224:227], a[20:23], v[118:121], v[224:227]\n"
        "ds_read_b128 v[180:183], v13\n"
        "v_cvt_pk_bf16_f32 v204, v162, v163\n"
        "v_mfma_f32_16x16x32_bf16 v[208:211], a[32:35], v[118:121], 0\n"
        "ds_read_b128 v[192:195], v13 offset:1024\n"
        "ds_read_b128 v[184:187], v13 offset:2048\n"
        "v_mfma_f32_16x16x32_bf16 v[208:211], a[28:31], v[112:115], v[208:211]\n"
        "ds_read_b128 v[200:203], v13 offset:3072\n"
        "v_cvt_pk_bf16_f32 v205, v164, v165\n"
        "v_mfma_f32_16x16x32_bf16 v[208:211], a[24:27], v[106:109], v[208:211]\n"
        "buffer_load_dwordx4 v1, s[36:39], s94 offen offset:1024 lds\n"
        "v_cvt_pk_bf16_f32 v206, v224, v225\n"
        "v_mfma_f32_16x16x32_bf16 v[230:233], a[36:39], v[106:109], 0\n"
        "ds_read_b128 v[168:171], v13 offset:4096\n"
        "ds_read_b128 v[176:179], v13 offset:5120\n"
        "v_mfma_f32_16x16x32_bf16 v[230:233], a[40:43], v[112:115], v[230:233]\n"
        "ds_read_b128 v[196:199], v13 offset:6144\n"
        "ds_read_b128 v[188:191], v13 offset:7168\n"
        "v_mfma_f32_16x16x32_bf16 v[230:233], a[44:47], v[118:121], v[230:233]\n"
        "ds_read_b128 v[172:175], v13 offset:8192\n"
        "ds_read_b128 v[164:167], v20\n"
        "v_mfma_f32_16x16x32_bf16 v[222:225], a[56:59], v[118:121], 0\n"
        "s_waitcnt lgkmcnt(10)\n"
        "ds_read_b64 v[252:253], v20 offset:16\n"
        "ds_read_u16 v244, v22\n"
        "v_mfma_f32_16x16x32_bf16 v[222:225], a[52:55], v[112:115], v[222:225]\n"
        "ds_read_b64 v[216:217], v21\n"
        "v_readlane_b32 s93, v37, 1\n"
        "v_mfma_f32_16x16x32_bf16 v[222:225], a[48:51], v[106:109], v[222:225]\n"
        "s_waitcnt lgkmcnt(10)\n"
        "buffer_load_dwordx4 v2, s[36:39], s94 offen offset:2048 lds\n"
        "v_cvt_pk_bf16_f32 v207, v226, v227\n"
        "v_mfma_f32_16x16x32_bf16 v[234:237], a[60:63], v[106:109], 0\n"
        "ds_write_b16 v22, v255 offset:3584\n"
        "v_mfma_f32_16x16x32_bf16 v[234:237], a[64:67], v[112:115], v[234:237]\n"
        "v_cvt_pk_bf16_f32 v226, v208, v209\n"
        "v_cvt_pk_bf16_f32 v227, v210, v211\n"
        "v_mfma_f32_16x16x32_bf16 v[234:237], a[68:71], v[118:121], v[234:237]\n"
        "s_waitcnt lgkmcnt(8)\n"
        "v_cvt_pk_bf16_f32 v228, v230, v231\n"
        "v_cvt_pk_bf16_f32 v229, v232, v233\n"
        "v_mfma_f32_16x16x32_bf16 v[230:233], v[212:215], v[180:183], 0\n"
        "v_cvt_pk_bf16_f32 v208, v222, v223\n"
        "v_cvt_pk_bf16_f32 v209, v224, v225\n"
        "v_mfma_f32_16x16x32_bf16 v[230:233], v[218:221], v[192:195], v[230:233]\n"
        "s_nop 0\n"
        "v_cvt_pk_bf16_f32 v210, v234, v235\n"
        "v_cvt_pk_bf16_f32 v211, v236, v237\n"
        "v_mfma_f32_16x16x32_bf16 v[230:233], v[142:145], v[184:187], v[230:233]\n"
        "buffer_load_dwordx4 v3, s[36:39], s94 offen offset:3072 lds\n"
        "v_cndmask_b32_e64 v36, 0, v36, s[72:73]\n"
        "v_mfma_f32_16x16x32_bf16 v[234:237], v[142:145], v[176:179], 0\n"
        "s_waitcnt lgkmcnt(1)\n"
        "v_fmaak_f32 v254, v28, v254, 0x3727c5ac\n"
        "v_cndmask_b32_dpp v248, v248, v35, vcc row_ror:4 row_mask:0xf bank_mask:0xf\n"
        "v_mfma_f32_16x16x32_bf16 v[234:237], v[218:221], v[168:171], v[234:237]\n"
        "v_rsq_f32 v255, v254\n"
        "v_mfma_f32_16x16x32_bf16 v[234:237], v[212:215], v[200:203], v[234:237]\n"
        "s_nop 1\n"
        "v_mfma_f32_16x16x32_bf16 v[222:225], v[212:215], v[196:199], 0\n"
        "s_add_u32 m0, s52, 4096\n"
        "s_nop 1\n"
        "v_mfma_f32_16x16x32_bf16 v[222:225], v[218:221], v[188:191], v[222:225]\n"
        "s_nop 0\n"
        "v_cndmask_b32_e64 v251, v230, v234, s[64:65]\n"
        "v_cndmask_b32_e64 v254, v231, v235, s[64:65]\n"
        "v_mfma_f32_16x16x32_bf16 v[222:225], v[142:145], v[172:175], v[222:225]\n"
        "s_nop 0\n"
        "buffer_load_dwordx4 v4, s[36:39], s94 offen lds\n"
        "v_mfma_f32_16x16x32_bf16 v[212:215], v[106:109], v[204:207], 0\n"
        "s_nop 2\n"
        "v_mfma_f32_16x16x32_bf16 v[212:215], v[112:115], v[226:229], v[212:215]\n"
        "s_nop 0\n"
        "v_cndmask_b32_e64 v251, v251, v222, s[70:71]\n"
        "v_cndmask_b32_e64 v254, v254, v223, s[70:71]\n"
        "v_mfma_f32_16x16x32_bf16 v[212:215], v[118:121], v[208:211], v[212:215]\n"
        "v_cvt_pk_f16_f32 v251, v251, v254\n"
        "v_pk_mul_f16 v251, v251, v244 op_sel_hi:[1,0]\n"
        "v_mfma_f32_16x16x32_bf16 v[142:145], a[152:155], v[184:187], 0\n"
        "v_pk_mul_f16 v251, v251, s93 op_sel_hi:[1,0]\n"
        "v_mfma_f32_16x16x32_bf16 v[142:145], a[148:151], v[192:195], v[142:145]\n"
        "v_exp_f16_sdwa v251, v251 dst_sel:WORD_0 dst_unused:UNUSED_PRESERVE src0_sel:WORD_0\n"
        "v_mfma_f32_16x16x32_bf16 v[142:145], a[144:147], v[180:183], v[142:145]\n"
        "buffer_load_dwordx4 v5, s[36:39], s94 offen offset:1024 lds\n"
        "v_cndmask_b32_dpp v249, v212, v122, vcc row_ror:4 row_mask:0xf bank_mask:0xf\n"
        "v_mfma_f32_16x16x32_bf16 v[204:207], a[156:159], v[180:183], 0\n"
        "v_cndmask_b32_dpp v246, v213, v123, vcc row_ror:4 row_mask:0xf bank_mask:0xf\n"
        "v_cndmask_b32_dpp v247, v214, v124, vcc row_ror:4 row_mask:0xf bank_mask:0xf\n"
        "v_mfma_f32_16x16x32_bf16 v[204:207], a[160:163], v[192:195], v[204:207]\n"
        "v_cndmask_b32_dpp v250, v215, v125, vcc row_ror:4 row_mask:0xf bank_mask:0xf\n"
        "v_cndmask_b32_dpp v249, v249, v40, vcc row_ror:4 row_mask:0xf bank_mask:0xf\n"
        "v_mfma_f32_16x16x32_bf16 v[204:207], a[164:167], v[184:187], v[204:207]\n"
        "v_cndmask_b32_dpp v246, v246, v41, vcc row_ror:4 row_mask:0xf bank_mask:0xf\n"
        "v_cndmask_b32_dpp v247, v247, v42, vcc row_ror:4 row_mask:0xf bank_mask:0xf\n"
        "v_mfma_f32_16x16x32_bf16 v[208:211], a[176:179], v[184:187], 0\n"
        "v_cndmask_b32_dpp v250, v250, v43, vcc row_ror:4 row_mask:0xf bank_mask:0xf\n"
        "v_mul_f32_dpp v249, v255, v249 quad_perm:[0,0,0,0] row_mask:0xf bank_mask:0xf\n"
        "v_mfma_f32_16x16x32_bf16 v[208:211], a[172:175], v[192:195], v[208:211]\n"
        "v_mul_f32_dpp v246, v255, v246 quad_perm:[1,1,1,1] row_mask:0xf bank_mask:0xf\n"
        "v_mul_f32_dpp v247, v255, v247 quad_perm:[2,2,2,2] row_mask:0xf bank_mask:0xf\n"
        "v_mfma_f32_16x16x32_bf16 v[208:211], a[168:171], v[180:183], v[208:211]\n"
        "buffer_load_dwordx4 v6, s[36:39], s94 offen offset:2048 lds\n"
        "v_mul_f32_dpp v250, v255, v250 quad_perm:[3,3,3,3] row_mask:0xf bank_mask:0xf\n"
        "v_mfma_f32_16x16x32_bf16 v[212:215], a[180:183], v[180:183], 0\n"
        "v_max3_f32 v254, v249, v246, v247\n"
        "v_max_f32 v254, v254, v250\n"
        "v_mfma_f32_16x16x32_bf16 v[212:215], a[184:187], v[192:195], v[212:215]\n"
        "v_mul_f32 v254, v254, v255\n"
        "v_fma_f32 v249, v249, v255, -v254\n"
        "v_mfma_f32_16x16x32_bf16 v[212:215], a[188:191], v[184:187], v[212:215]\n"
        "v_fma_f32 v246, v246, v255, -v254\n"
        "v_fma_f32 v247, v247, v255, -v254\n"
        "v_mfma_f32_16x16x32_bf16 v[226:229], a[200:203], v[184:187], 0\n"
        "v_fma_f32 v250, v250, v255, -v254\n"
        "v_cvt_pk_bf16_f32 v222, v142, v143\n"
        "v_mfma_f32_16x16x32_bf16 v[226:229], a[196:199], v[192:195], v[226:229]\n"
        "v_exp_f32 v249, v249\n"
        "v_mfma_f32_16x16x32_bf16 v[226:229], a[192:195], v[180:183], v[226:229]\n"
        "buffer_load_dwordx4 v7, s[36:39], s94 offen offset:3072 lds\n"
        "v_cvt_pk_bf16_f32 v223, v144, v145\n"
        "v_mfma_f32_16x16x32_bf16 v[122:125], a[204:207], v[180:183], 0\n"
        "v_cvt_pk_bf16_f32 v224, v204, v205\n"
        "v_cvt_pk_bf16_f32 v225, v206, v207\n"
        "v_mfma_f32_16x16x32_bf16 v[122:125], a[208:211], v[192:195], v[122:125]\n"
        "v_cvt_pk_bf16_f32 v40, v208, v209\n"
        "v_cvt_pk_bf16_f32 v41, v210, v211\n"
        "v_mfma_f32_16x16x32_bf16 v[122:125], a[212:215], v[184:187], v[122:125]\n"
        "v_cvt_pk_bf16_f32 v42, v212, v213\n"
        "v_cvt_pk_bf16_f32 v43, v214, v215\n"
        "v_mfma_f32_16x16x32_bf16 v[218:221], v[158:161], a[80:83], 0\n"
        "s_add_u32 m0, s52, 8192\n"
        "v_cvt_pk_bf16_f32 v142, v226, v227\n"
        "v_cvt_pk_bf16_f32 v143, v228, v229\n"
        "v_mfma_f32_16x16x32_bf16 v[218:221], v[152:155], a[76:79], v[218:221]\n"
        "s_nop 0\n"
        "v_cvt_pk_bf16_f32 v144, v122, v123\n"
        "v_cvt_pk_bf16_f32 v145, v124, v125\n"
        "v_mfma_f32_16x16x32_bf16 v[218:221], v[146:149], a[72:75], v[218:221]\n"
        "buffer_load_dwordx4 v8, s[36:39], s94 offen lds\n"
        "v_mfma_f32_16x16x32_bf16 v[122:125], v[146:149], a[84:87], 0\n"
        "v_exp_f32 v246, v246\n"
        "v_mfma_f32_16x16x32_bf16 v[122:125], v[152:155], a[88:91], v[122:125]\n"
        "v_exp_f32 v247, v247\n"
        "v_mfma_f32_16x16x32_bf16 v[122:125], v[158:161], a[92:95], v[122:125]\n"
        "v_exp_f32 v250, v250\n"
        "v_mfma_f32_16x16x32_bf16 v[212:215], v[180:183], v[222:225], 0\n"
        "v_add_f32 v254, v249, v246\n"
        "v_add_f32 v243, v247, v250\n"
        "v_mfma_f32_16x16x32_bf16 v[212:215], v[192:195], v[40:43], v[212:215]\n"
        "v_add_f32 v254, v254, v243\n"
        "v_cvt_pk_bf16_f32 v30, v218, v219\n"
        "v_mfma_f32_16x16x32_bf16 v[212:215], v[184:187], v[142:145], v[212:215]\n"
        "v_rcp_f32 v254, v254\n"
        "v_mfma_f32_16x16x32_bf16 v[208:211], v[176:179], v[142:145], 0\n"
        "v_exp_f16_sdwa v251, v251 dst_sel:WORD_1 dst_unused:UNUSED_PRESERVE src0_sel:WORD_1\n"
        "v_mfma_f32_16x16x32_bf16 v[208:211], v[168:171], v[40:43], v[208:211]\n"
        "v_mul_f32 v249, v249, v254\n"
        "v_mul_f32 v246, v246, v254\n"
        "v_mfma_f32_16x16x32_bf16 v[208:211], v[200:203], v[222:225], v[208:211]\n"
        "v_mul_f32 v247, v247, v254\n"
        "v_mul_f32 v250, v250, v254\n"
        "v_mfma_f32_16x16x32_bf16 v[204:207], v[196:199], v[222:225], 0\n"
        "v_mul_f32_dpp v249, v255, v249 quad_perm:[0,0,0,0] row_mask:0xf bank_mask:0xf\n"
        "v_mul_f32_dpp v246, v255, v246 quad_perm:[1,1,1,1] row_mask:0xf bank_mask:0xf\n"
        "v_mfma_f32_16x16x32_bf16 v[204:207], v[188:191], v[40:43], v[204:207]\n"
        "v_mul_f32_dpp v247, v255, v247 quad_perm:[2,2,2,2] row_mask:0xf bank_mask:0xf\n"
        "v_mul_f32_dpp v250, v255, v250 quad_perm:[3,3,3,3] row_mask:0xf bank_mask:0xf\n"
        "v_mfma_f32_16x16x32_bf16 v[204:207], v[172:175], v[142:145], v[204:207]\n"
        "v_cvt_pk_bf16_f32 v242, v249, v246\n"
        "v_cvt_pk_bf16_f32 v243, v247, v250\n"
        "v_mfma_f32_16x16x32_bf16 v[222:225], v[158:161], a[104:107], 0\n"
        "v_and_b32 v144, v25, v242\n"
        "v_and_b32 v145, v25, v243\n"
        "v_mfma_f32_16x16x32_bf16 v[222:225], v[152:155], a[100:103], v[222:225]\n"
        "v_mov_b64 v[150:151], v[144:145]\n"
        "v_cvt_pk_bf16_f32 v31, v220, v221\n"
        "v_mfma_f32_16x16x32_bf16 v[222:225], v[146:149], a[96:99], v[222:225]\n"
        "v_cvt_pk_bf16_f32 v34, v122, v123\n"
        "v_cvt_pk_bf16_f32 v35, v124, v125\n"
        "v_mfma_f32_16x16x32_bf16 v[122:125], v[146:149], a[108:111], 0\n"
        "v_cvt_f32_f16 v254, v251\n"
        "v_cndmask_b32_e64 v254, v254, v36, s[68:69]\n"
        "v_mfma_f32_16x16x32_bf16 v[122:125], v[152:155], a[112:115], v[122:125]\n"
        "s_nop 0\n"
        "v_add_f32_dpp v255, v254, v254 row_ror:8 row_mask:0xf bank_mask:0xf\n"
        "v_mov_b64 v[156:157], v[144:145]\n"
        "v_mfma_f32_16x16x32_bf16 v[122:125], v[158:161], a[116:119], v[122:125]\n"
        "v_add_f32_dpp v255, v255, v255 row_ror:4 row_mask:0xf bank_mask:0xf\n"
        "v_cvt_pk_bf16_f32 v42, v222, v223\n"
        "v_mfma_f32_16x16x32_bf16 v[218:221], v[30:33], v[144:147], 0\n"
        "ds_read_u16 v137, v19 offset:2560\n"
        "v_add_f32_dpp v255, v255, v255 row_ror:2 row_mask:0xf bank_mask:0xf\n"
        "v_mfma_f32_16x16x32_bf16 v[230:233], v[32:35], v[148:151], 0\n"
        "s_nop 0\n"
        "v_add_f32_dpp v255, v255, v255 row_ror:1 row_mask:0xf bank_mask:0xf\n"
        "v_cvt_pk_bf16_f32 v43, v224, v225\n"
        "v_mfma_f32_16x16x32_bf16 v[226:229], v[158:161], a[128:131], 0\n"
        "v_add_f32_dpp v255, v255, v255 row_bcast:15 row_mask:0xa bank_mask:0xf\n"
        "v_cvt_pk_bf16_f32 v46, v122, v123\n"
        "v_mfma_f32_16x16x32_bf16 v[226:229], v[152:155], a[124:127], v[226:229]\n"
        "v_cvt_pk_bf16_f32 v47, v124, v125\n"
        "v_add_f32_dpp v255, v255, v255 row_bcast:31 row_mask:0xc bank_mask:0xf\n"
        "v_mfma_f32_16x16x32_bf16 v[226:229], v[146:149], a[120:123], v[226:229]\n"
        "v_readlane_b32 s95, v255, 63\n"
        "v_mov_b64 v[162:163], v[144:145]\n"
        "v_mfma_f32_16x16x32_bf16 v[222:225], v[146:149], a[132:135], 0\n"
        "v_rcp_f32 v255, s95\n"
        "v_mfma_f32_16x16x32_bf16 v[222:225], v[152:155], a[136:139], v[222:225]\n"
        "v_mul_f32 v254, v254, v255\n"
        "v_cvt_f16_f32 v254, v254\n"
        "v_mfma_f32_16x16x32_bf16 v[222:225], v[158:161], a[140:143], v[222:225]\n"
        "v_mul_f16 v254, v254, v244\n"
        "v_and_b32_dpp v124, v242, v25 row_ror:12 row_mask:0xf bank_mask:0xf\n"
        "v_mfma_f32_16x16x32_bf16 v[234:237], v[42:45], v[150:153], 0\n"
        "s_waitcnt lgkmcnt(0)\n"
        "ds_write_b16 v22, v254 offset:512\n"
        "v_mfma_f32_16x16x32_bf16 v[142:145], v[44:47], v[154:157], 0\n"
        "v_cmp_ne_u32_e64 s[24:25], s29, v137\n"
        "v_and_b32_dpp v125, v243, v25 row_ror:12 row_mask:0xf bank_mask:0xf\n"
        "v_mfma_f32_16x16x32_bf16 v[238:241], v[138:141], a[80:83], 0\n"
        "v_cvt_pk_bf16_f32 v36, v226, v227\n"
        "v_cvt_pk_bf16_f32 v37, v228, v229\n"
        "v_mfma_f32_16x16x32_bf16 v[238:241], v[132:135], a[76:79], v[238:241]\n"
        "s_cmp_lg_u64 s[24:25], 0\n"
        "v_cvt_pk_bf16_f32 v40, v222, v223\n"
        "v_cvt_pk_bf16_f32 v41, v224, v225\n"
        "v_mfma_f32_16x16x32_bf16 v[238:241], v[126:129], a[72:75], v[238:241]\n"
        "s_cbranch_scc1 SP10_%=\n"
        "BK10_%=:\n"
        "v_mov_b64 v[130:131], v[124:125]\n"
        "v_cvt_pk_bf16_f32 v152, v218, v219\n"
        "v_mfma_f32_16x16x32_bf16 v[222:225], v[126:129], a[84:87], 0\n"
        "v_cvt_pk_bf16_f32 v148, v220, v221\n"
        "v_cvt_pk_bf16_f32 v218, v230, v231\n"
        "v_mfma_f32_16x16x32_bf16 v[222:225], v[132:135], a[88:91], v[222:225]\n"
        "v_cvt_pk_bf16_f32 v153, v232, v233\n"
        "v_cvt_pk_bf16_f32 v149, v234, v235\n"
        "v_mfma_f32_16x16x32_bf16 v[222:225], v[138:141], a[92:95], v[222:225]\n"
        "v_cvt_pk_bf16_f32 v219, v236, v237\n"
        "v_cvt_pk_bf16_f32 v154, v142, v143\n"
        "v_mfma_f32_16x16x32_bf16 v[230:233], v[36:39], v[156:159], 0\n"
        "s_nop 0\n"
        "v_cvt_pk_bf16_f32 v30, v238, v239\n"
        "v_cvt_pk_bf16_f32 v31, v240, v241\n"
        "v_mfma_f32_16x16x32_bf16 v[156:159], v[38:41], v[160:163], 0\n"
        "s_nop 0\n"
        "v_cvt_pk_bf16_f32 v34, v222, v223\n"
        "v_cvt_pk_bf16_f32 v35, v224, v225\n"
        "v_mfma_f32_16x16x32_bf16 v[226:229], v[138:141], a[104:107], 0\n"
        "v_cvt_pk_bf16_f32 v155, v232, v233\n"
        "v_cvt_pk_bf16_f32 v150, v144, v145\n"
        "v_mfma_f32_16x16x32_bf16 v[226:229], v[132:135], a[100:103], v[226:229]\n"
        "v_cvt_pk_bf16_f32 v220, v230, v231\n"
        "v_cvt_pk_bf16_f32 v151, v156, v157\n"
        "v_mfma_f32_16x16x32_bf16 v[226:229], v[126:129], a[96:99], v[226:229]\n"
        "ds_write_b128 v12, v[152:155]\n"
        "v_mfma_f32_16x16x32_bf16 v[222:225], v[126:129], a[108:111], 0\n"
        "s_nop 0\n"
        "v_cvt_pk_bf16_f32 v221, v158, v159\n"
        "v_and_b32 v254, 0xffff0000, v251\n"
        "v_mfma_f32_16x16x32_bf16 v[222:225], v[132:135], a[112:115], v[222:225]\n"
        "s_nop 0\n"
        "v_mov_b64 v[136:137], v[124:125]\n"
        "v_cvt_pk_bf16_f32 v42, v226, v227\n"
        "v_mfma_f32_16x16x32_bf16 v[222:225], v[138:141], a[116:119], v[222:225]\n"
        "s_nop 0\n"
        "ds_write_b128 v12, v[148:151] offset:1024\n"
        "v_mfma_f32_16x16x32_bf16 v[156:159], v[30:33], v[124:127], 0\n"
        "s_nop 0\n"
        "ds_write_b128 v12, v[218:221] offset:2048\n"
        "v_mfma_f32_16x16x32_bf16 v[160:163], v[32:35], v[128:131], 0\n"
        "s_nop 0\n"
        "v_cvt_pk_bf16_f32 v43, v228, v229\n"
        "v_cvt_pk_bf16_f32 v46, v222, v223\n"
        "v_mfma_f32_16x16x32_bf16 v[144:147], v[152:155], v[152:155], 0\n"
        "v_cvt_pk_bf16_f32 v47, v224, v225\n"
        "v_mul_f32_dpp v251, v248, v249 quad_perm:[0,0,0,0] row_mask:0xf bank_mask:0xf\n"
        "v_mfma_f32_16x16x32_bf16 v[144:147], v[148:151], v[148:151], v[144:147]\n"
        "v_and_b32_dpp v104, v242, v25 row_ror:8 row_mask:0xf bank_mask:0xf\n"
        "v_and_b32_dpp v105, v243, v25 row_ror:8 row_mask:0xf bank_mask:0xf\n"
        "v_mfma_f32_16x16x32_bf16 v[144:147], v[218:221], v[218:221], v[144:147]\n"
        "v_mov_b64 v[142:143], v[124:125]\n"
        "v_mov_b64 v[110:111], v[104:105]\n"
        "v_mfma_f32_16x16x32_bf16 v[150:153], v[138:141], a[128:131], 0\n"
        "v_mov_b64 v[116:117], v[104:105]\n"
        "v_fmac_f32_dpp v251, v248, v246 quad_perm:[1,1,1,1] row_mask:0xf bank_mask:0xf\n"
        "v_mfma_f32_16x16x32_bf16 v[150:153], v[132:135], a[124:127], v[150:153]\n"
        "v_mov_b64 v[122:123], v[104:105]\n"
        "v_fmac_f32_dpp v251, v248, v247 quad_perm:[2,2,2,2] row_mask:0xf bank_mask:0xf\n"
        "v_mfma_f32_16x16x32_bf16 v[150:153], v[126:129], a[120:123], v[150:153]\n"
        "s_nop 0\n"
        "v_fmac_f32_dpp v251, v248, v250 quad_perm:[3,3,3,3] row_mask:0xf bank_mask:0xf\n"
        "v_add_f32 v251, v251, v245\n"
        "v_mfma_f32_16x16x32_bf16 v[218:221], v[126:129], a[132:135], 0\n"
        "v_cvt_pk_f16_f32 v148, v212, v213\n"
        "v_cvt_pk_f16_f32 v149, v214, v215\n"
        "v_mfma_f32_16x16x32_bf16 v[218:221], v[132:135], a[136:139], v[218:221]\n"
        "s_nop 0\n"
        "v_cvt_pk_bf16_f32 v36, v150, v151\n"
        "v_cvt_pk_bf16_f32 v37, v152, v153\n"
        "v_mfma_f32_16x16x32_bf16 v[218:221], v[138:141], a[140:143], v[218:221]\n"
        "v_cvt_pk_bf16_f32 v150, v156, v157\n"
        "v_cvt_pk_bf16_f32 v128, v158, v159\n"
        "v_mfma_f32_16x16x32_bf16 v[212:215], v[42:45], v[130:133], 0\n"
        "s_nop 0\n"
        "v_cvt_pk_bf16_f32 v124, v160, v161\n"
        "v_cvt_pk_bf16_f32 v151, v162, v163\n"
        "v_mfma_f32_16x16x32_bf16 v[154:157], v[44:47], v[134:137], 0\n"
        "s_nop 0\n"
        "v_cvt_pk_bf16_f32 v40, v218, v219\n"
        "v_cvt_pk_bf16_f32 v41, v220, v221\n"
        "v_mfma_f32_16x16x32_bf16 v[132:135], v[118:121], a[80:83], 0\n"
        "v_cvt_pk_bf16_f32 v129, v212, v213\n"
        "v_cvt_pk_bf16_f32 v125, v214, v215\n"
        "v_mfma_f32_16x16x32_bf16 v[132:135], v[112:115], a[76:79], v[132:135]\n"
        "s_nop 0\n"
        "v_cvt_pk_bf16_f32 v152, v154, v155\n"
        "v_cvt_pk_bf16_f32 v130, v156, v157\n"
        "v_mfma_f32_16x16x32_bf16 v[132:135], v[106:109], a[72:75], v[132:135]\n"
        "v_pk_mul_f16 v148, v148, v216 op_sel_hi:[1,0]\n"
        "v_pk_mul_f16 v149, v149, v216 op_sel_hi:[1,0]\n"
        "v_mfma_f32_16x16x32_bf16 v[158:161], v[106:109], a[84:87], 0\n"
        "s_nop 0\n"
        "v_pk_mul_f16 v148, v148, v164\n"
        "v_pk_mul_f16 v149, v149, v165\n"
        "v_mfma_f32_16x16x32_bf16 v[158:161], v[112:115], a[88:91], v[158:161]\n"
        "s_nop 0\n"
        "v_cvt_pk_bf16_f32 v30, v132, v133\n"
        "v_cvt_pk_bf16_f32 v31, v134, v135\n"
        "v_mfma_f32_16x16x32_bf16 v[158:161], v[118:121], a[92:95], v[158:161]\n"
        "v_exp_f16_sdwa v148, v148 dst_sel:WORD_0 dst_unused:UNUSED_PRESERVE src0_sel:WORD_0\n"
        "v_mfma_f32_16x16x32_bf16 v[154:157], v[36:39], v[136:139], 0\n"
        "v_exp_f16_sdwa v149, v149 dst_sel:WORD_0 dst_unused:UNUSED_PRESERVE src0_sel:WORD_0\n"
        "v_mfma_f32_16x16x32_bf16 v[136:139], v[38:41], v[140:143], 0\n"
        "s_nop 0\n"
        "v_exp_f16_sdwa v148, v148 dst_sel:WORD_1 dst_unused:UNUSED_PRESERVE src0_sel:WORD_1\n"
        "v_mfma_f32_16x16x32_bf16 v[132:135], v[118:121], a[104:107], 0\n"
        "s_nop 0\n"
        "v_cvt_pk_bf16_f32 v34, v158, v159\n"
        "v_cvt_pk_bf16_f32 v35, v160, v161\n"
        "v_mfma_f32_16x16x32_bf16 v[132:135], v[112:115], a[100:103], v[132:135]\n"
        "v_cvt_pk_bf16_f32 v153, v156, v157\n"
        "v_cvt_pk_bf16_f32 v126, v154, v155\n"
        "v_mfma_f32_16x16x32_bf16 v[132:135], v[106:109], a[96:99], v[132:135]\n"
        "s_nop 0\n"
        "ds_write_b128 v12, v[150:153] offset:3072\n"
        "v_mfma_f32_16x16x32_bf16 v[140:143], v[106:109], a[108:111], 0\n"
        "s_nop 0\n"
        "v_cvt_pk_bf16_f32 v131, v136, v137\n"
        "v_cvt_pk_bf16_f32 v127, v138, v139\n"
        "v_mfma_f32_16x16x32_bf16 v[140:143], v[112:115], a[112:115], v[140:143]\n"
        "s_nop 0\n"
        "v_cvt_pk_bf16_f32 v42, v132, v133\n"
        "v_cvt_pk_bf16_f32 v43, v134, v135\n"
        "v_mfma_f32_16x16x32_bf16 v[140:143], v[118:121], a[116:119], v[140:143]\n"
        "ds_write_b128 v12, v[128:131] offset:4096\n"
        "v_mfma_f32_16x16x32_bf16 v[136:139], v[30:33], v[104:107], 0\n"
        "ds_write_b128 v12, v[124:127] offset:5120\n"
        "v_mfma_f32_16x16x32_bf16 v[154:157], v[32:35], v[108:111], 0\n"
        "s_nop 0\n"
        "v_exp_f16_sdwa v149, v149 dst_sel:WORD_1 dst_unused:UNUSED_PRESERVE src0_sel:WORD_1\n"
        "v_mfma_f32_16x16x32_bf16 v[132:135], v[150:153], v[150:153], 0\n"
        "s_nop 0\n"
        "v_cvt_pk_bf16_f32 v46, v140, v141\n"
        "v_cvt_pk_bf16_f32 v47, v142, v143\n"
        "v_mfma_f32_16x16x32_bf16 v[132:135], v[128:131], v[128:131], v[132:135]\n"
        "v_mul_f32 v250, s80, v251\n"
        "v_pk_add_f16 v104, v148, v149\n"
        "v_mfma_f32_16x16x32_bf16 v[132:135], v[124:127], v[124:127], v[132:135]\n"
        "v_cvt_pk_f16_f32 v150, v208, v209\n"
        "v_cvt_pk_f16_f32 v151, v210, v211\n"
        "v_mfma_f32_16x16x32_bf16 v[124:127], v[118:121], a[128:131], 0\n"
        "v_pk_mul_f16 v150, v150, v216 op_sel_hi:[1,0]\n"
        "v_pk_mul_f16 v151, v151, v216 op_sel_hi:[1,0]\n"
        "v_mfma_f32_16x16x32_bf16 v[124:127], v[112:115], a[124:127], v[124:127]\n"
        "v_pk_mul_f16 v150, v150, v166\n"
        "v_pk_mul_f16 v151, v151, v167\n"
        "v_mfma_f32_16x16x32_bf16 v[124:127], v[106:109], a[120:123], v[124:127]\n"
        "s_nop 0\n"
        "v_exp_f16_sdwa v150, v150 dst_sel:WORD_0 dst_unused:UNUSED_PRESERVE src0_sel:WORD_0\n"
        "v_mfma_f32_16x16x32_bf16 v[140:143], v[106:109], a[132:135], 0\n"
        "s_nop 0\n"
        "v_cvt_pk_bf16_f32 v128, v136, v137\n"
        "v_cvt_pk_bf16_f32 v106, v138, v139\n"
        "v_mfma_f32_16x16x32_bf16 v[140:143], v[112:115], a[136:139], v[140:143]\n"
        "s_nop 0\n"
        "v_cvt_pk_bf16_f32 v36, v124, v125\n"
        "v_cvt_pk_bf16_f32 v37, v126, v127\n"
        "v_mfma_f32_16x16x32_bf16 v[140:143], v[118:121], a[140:143], v[140:143]\n"
        "s_nop 0\n"
        "v_cvt_pk_bf16_f32 v124, v154, v155\n"
        "v_cvt_pk_bf16_f32 v129, v156, v157\n"
        "v_mfma_f32_16x16x32_bf16 v[136:139], v[42:45], v[110:113], 0\n"
        "s_nop 0\n"
        "v_exp_f16_sdwa v151, v151 dst_sel:WORD_0 dst_unused:UNUSED_PRESERVE src0_sel:WORD_0\n"
        "v_mfma_f32_16x16x32_bf16 v[152:155], v[44:47], v[114:117], 0\n"
        "s_nop 0\n"
        "v_cvt_pk_bf16_f32 v40, v140, v141\n"
        "v_cvt_pk_bf16_f32 v41, v142, v143\n"
        "v_mfma_f32_16x16x32_bf16 v[110:113], a[152:155], v[176:179], 0\n"
        "s_nop 0\n"
        "v_cvt_pk_bf16_f32 v107, v136, v137\n"
        "v_cvt_pk_bf16_f32 v125, v138, v139\n"
        "v_mfma_f32_16x16x32_bf16 v[110:113], a[148:151], v[168:171], v[110:113]\n"
        "v_cvt_pk_bf16_f32 v130, v152, v153\n"
        "v_cvt_pk_bf16_f32 v108, v154, v155\n"
        "v_mfma_f32_16x16x32_bf16 v[110:113], a[144:147], v[200:203], v[110:113]\n"
        "v_exp_f16_sdwa v150, v150 dst_sel:WORD_1 dst_unused:UNUSED_PRESERVE src0_sel:WORD_1\n"
        "v_mfma_f32_16x16x32_bf16 v[136:139], v[36:39], v[116:119], 0\n"
        "s_nop 0\n"
        "v_exp_f16_sdwa v151, v151 dst_sel:WORD_1 dst_unused:UNUSED_PRESERVE src0_sel:WORD_1\n"
        "v_mfma_f32_16x16x32_bf16 v[34:37], v[38:41], v[120:123], 0\n"
        "s_nop 0\n"
        "v_pk_add_f16 v104, v104, v150\n"
        "v_cndmask_b32_e64 v249, v144, v145, s[56:57]\n"
        "v_mfma_f32_16x16x32_bf16 v[114:117], a[156:159], v[200:203], 0\n"
        "s_nop 0\n"
        "v_cvt_pk_bf16_f32 v131, v138, v139\n"
        "v_cvt_pk_bf16_f32 v126, v136, v137\n"
        "v_mfma_f32_16x16x32_bf16 v[114:117], a[160:163], v[168:171], v[114:117]\n"
        "v_cvt_pk_bf16_f32 v109, v34, v35\n"
        "v_cvt_pk_bf16_f32 v127, v36, v37\n"
        "v_mfma_f32_16x16x32_bf16 v[114:117], a[164:167], v[176:179], v[114:117]\n"
        "ds_write_b128 v12, v[128:131] offset:6144\n"
        "v_mfma_f32_16x16x32_bf16 v[40:43], a[176:179], v[176:179], 0\n"
        "v_cndmask_b32_e64 v249, v249, v146, s[58:59]\n"
        "v_cndmask_b32_e64 v248, v132, v133, s[56:57]\n"
        "v_mfma_f32_16x16x32_bf16 v[40:43], a[172:175], v[168:171], v[40:43]\n"
        "v_cndmask_b32_e64 v248, v248, v134, s[58:59]\n"
        "v_cndmask_b32_e64 v248, v248, v135, s[60:61]\n"
        "v_mfma_f32_16x16x32_bf16 v[40:43], a[168:171], v[200:203], v[40:43]\n"
        "ds_write_b128 v12, v[106:109] offset:7168\n"
        "v_mfma_f32_16x16x32_bf16 v[120:123], v[128:131], v[128:131], 0\n"
        "v_cvt_pk_bf16_f32 v34, v110, v111\n"
        "v_cvt_pk_bf16_f32 v35, v112, v113\n"
        "v_mfma_f32_16x16x32_bf16 v[120:123], v[106:109], v[106:109], v[120:123]\n"
        "v_cvt_pk_bf16_f32 v36, v114, v115\n"
        "v_cvt_pk_bf16_f32 v37, v116, v117\n"
        "v_mfma_f32_16x16x32_bf16 v[120:123], v[124:127], v[124:127], v[120:123]\n"
        "s_nop 0\n"
        "ds_write_b128 v12, v[124:127] offset:8192\n"
        "v_mfma_f32_16x16x32_bf16 v[116:119], a[188:191], v[176:179], 0\n"
        "s_nop 0\n"
        "v_cvt_pk_bf16_f32 v106, v40, v41\n"
        "v_cvt_pk_bf16_f32 v107, v42, v43\n"
        "v_mfma_f32_16x16x32_bf16 v[116:119], a[184:187], v[168:171], v[116:119]\n"
        "s_nop 0\n"
        "v_cndmask_b32_e64 v247, v120, v121, s[56:57]\n"
        "v_cndmask_b32_e64 v247, v247, v122, s[58:59]\n"
        "v_mfma_f32_16x16x32_bf16 v[116:119], a[180:183], v[200:203], v[116:119]\n"
        "v_cndmask_b32_e64 v247, v247, v123, s[60:61]\n"
        "v_cndmask_b32_e64 v249, v249, v147, s[60:61]\n"
        "v_mfma_f32_16x16x32_bf16 v[112:115], a[192:195], v[200:203], 0\n"
        "s_nop 0\n"
        "v_cndmask_b32_dpp v248, v247, v248, vcc row_ror:4 row_mask:0xf bank_mask:0xf\n"
        "v_pk_add_f16 v104, v104, v151\n"
        "v_mfma_f32_16x16x32_bf16 v[112:115], a[196:199], v[168:171], v[112:115]\n"
        "s_nop 0\n"
        "v_cvt_pk_bf16_f32 v108, v116, v117\n"
        "v_cvt_pk_bf16_f32 v109, v118, v119\n"
        "v_mfma_f32_16x16x32_bf16 v[112:115], a[200:203], v[176:179], v[112:115]\n"
        "v_cndmask_b32_dpp v249, v248, v249, vcc row_ror:4 row_mask:0xf bank_mask:0xf\n"
        "v_fma_f32 v250, -v250, v251, v249\n"
        "v_mfma_f32_16x16x32_bf16 v[40:43], a[212:215], v[176:179], 0\n"
        "s_nop 0\n"
        "v_mov_b32 v251, s29\n"
        "v_max_f32 v250, 0, v250\n"
        "v_mfma_f32_16x16x32_bf16 v[40:43], a[208:211], v[168:171], v[40:43]\n"
        "s_nop 0\n"
        "v_cvt_pk_bf16_f32 v110, v112, v113\n"
        "v_cvt_pk_bf16_f32 v111, v114, v115\n"
        "v_mfma_f32_16x16x32_bf16 v[40:43], a[204:207], v[200:203], v[40:43]\n"
        "v_sub_f32 v249, 0x47c00000, v249\n"
        "v_fmaak_f32 v250, v28, v250, 0x3727c5ac\n"
        "v_mfma_f32_16x16x32_bf16 v[118:121], a[144:147], v[196:199], 0\n"
        "s_nop 0\n"
        "v_bfi_b32 v250, s84, v249, v250\n"
        "v_cvt_pk_f16_f32 v156, v204, v205\n"
        "v_mfma_f32_16x16x32_bf16 v[118:121], a[148:151], v[188:191], v[118:121]\n"
        "s_nop 0\n"
        "v_cvt_pk_bf16_f32 v112, v40, v41\n"
        "v_cvt_pk_bf16_f32 v113, v42, v43\n"
        "v_mfma_f32_16x16x32_bf16 v[118:121], a[152:155], v[172:175], v[118:121]\n"
        "v_rsq_f32 v249, v250\n"
        "v_mfma_f32_16x16x32_bf16 v[122:125], v[180:183], v[34:37], 0\n"
        "v_cvt_f16_f32 v249, v249\n"
        "v_cvt_pk_f16_f32 v157, v206, v207\n"
        "v_mfma_f32_16x16x32_bf16 v[122:125], v[192:195], v[106:109], v[122:125]\n"
        "ds_write_b16 v18, v249\n"
        "v_mfma_f32_16x16x32_bf16 v[122:125], v[184:187], v[110:113], v[122:125]\n"
        "ds_write_b16 v19, v249\n"
        "v_mfma_f32_16x16x32_bf16 v[40:43], v[176:179], v[110:113], 0\n"
        "ds_write_b16 v22, v251 offset:3072\n"
        "v_mfma_f32_16x16x32_bf16 v[40:43], v[168:171], v[106:109], v[40:43]\n"
        "v_pk_mul_f16 v156, v156, v216 op_sel_hi:[1,0]\n"
        "v_pk_mul_f16 v157, v157, v216 op_sel_hi:[1,0]\n"
        "v_mfma_f32_16x16x32_bf16 v[40:43], v[200:203], v[34:37], v[40:43]\n"
        "v_pk_mul_f16 v156, v156, v252\n"
        "v_pk_mul_f16 v157, v157, v253\n"
        "v_mfma_f32_16x16x32_bf16 v[114:117], v[196:199], v[34:37], 0\n"
        "v_exp_f16_sdwa v156, v156 dst_sel:WORD_0 dst_unused:UNUSED_PRESERVE src0_sel:WORD_0\n"
        "v_mfma_f32_16x16x32_bf16 v[114:117], v[188:191], v[106:109], v[114:117]\n"
        "v_exp_f16_sdwa v157, v157 dst_sel:WORD_0 dst_unused:UNUSED_PRESERVE src0_sel:WORD_0\n"
        "v_mfma_f32_16x16x32_bf16 v[114:117], v[172:175], v[110:113], v[114:117]\n"
        "v_exp_f16_sdwa v156, v156 dst_sel:WORD_1 dst_unused:UNUSED_PRESERVE src0_sel:WORD_1\n"
        "v_mfma_f32_16x16x32_bf16 v[106:109], a[164:167], v[172:175], 0\n"
        "v_exp_f16_sdwa v157, v157 dst_sel:WORD_1 dst_unused:UNUSED_PRESERVE src0_sel:WORD_1\n"
        "v_mfma_f32_16x16x32_bf16 v[106:109], a[160:163], v[188:191], v[106:109]\n"
        "v_pk_add_f16 v104, v104, v156\n"
        "v_pk_add_f16 v104, v104, v157\n"
        "v_mfma_f32_16x16x32_bf16 v[106:109], a[156:159], v[196:199], v[106:109]\n"
        "v_cvt_pk_bf16_f32 v34, v118, v119\n"
        "v_cvt_pk_f16_f32 v152, v122, v123\n"
        "v_mfma_f32_16x16x32_bf16 v[110:113], a[168:171], v[196:199], 0\n"
        "v_cvt_pk_f16_f32 v153, v124, v125\n"
        "v_pk_mul_f16 v152, v152, v216 op_sel:[0,1]\n"
        "v_mfma_f32_16x16x32_bf16 v[110:113], a[172:175], v[188:191], v[110:113]\n"
        "v_pk_mul_f16 v153, v153, v216 op_sel:[0,1]\n"
        "v_pk_mul_f16 v152, v152, v164\n"
        "v_mfma_f32_16x16x32_bf16 v[110:113], a[176:179], v[172:175], v[110:113]\n"
        "v_pk_mul_f16 v153, v153, v165\n"
        "v_cvt_pk_bf16_f32 v35, v120, v121\n"
        "v_mfma_f32_16x16x32_bf16 v[124:127], a[188:191], v[172:175], 0\n"
        "v_exp_f16_sdwa v152, v152 dst_sel:WORD_0 dst_unused:UNUSED_PRESERVE src0_sel:WORD_0\n"
        "v_mfma_f32_16x16x32_bf16 v[124:127], a[184:187], v[188:191], v[124:127]\n"
        "v_exp_f16_sdwa v153, v153 dst_sel:WORD_0 dst_unused:UNUSED_PRESERVE src0_sel:WORD_0\n"
        "v_mfma_f32_16x16x32_bf16 v[124:127], a[180:183], v[196:199], v[124:127]\n"
        "v_exp_f16_sdwa v152, v152 dst_sel:WORD_1 dst_unused:UNUSED_PRESERVE src0_sel:WORD_1\n"
        "v_mfma_f32_16x16x32_bf16 v[120:123], a[192:195], v[196:199], 0\n"
        "v_exp_f16_sdwa v153, v153 dst_sel:WORD_1 dst_unused:UNUSED_PRESERVE src0_sel:WORD_1\n"
        "v_mfma_f32_16x16x32_bf16 v[120:123], a[196:199], v[188:191], v[120:123]\n"
        "v_pk_add_f16 v105, v152, v153\n"
        "v_cvt_pk_f16_f32 v154, v40, v41\n"
        "v_mfma_f32_16x16x32_bf16 v[120:123], a[200:203], v[172:175], v[120:123]\n"
        "v_cvt_pk_f16_f32 v155, v42, v43\n"
        "v_pk_mul_f16 v154, v154, v216 op_sel:[0,1]\n"
        "v_mfma_f32_16x16x32_bf16 v[128:131], a[212:215], v[172:175], 0\n"
        "v_pk_mul_f16 v155, v155, v216 op_sel:[0,1]\n"
        "v_cvt_pk_bf16_f32 v36, v106, v107\n"
        "v_mfma_f32_16x16x32_bf16 v[128:131], a[208:211], v[188:191], v[128:131]\n"
        "v_cvt_pk_bf16_f32 v37, v108, v109\n"
        "v_cvt_pk_bf16_f32 v108, v110, v111\n"
        "v_mfma_f32_16x16x32_bf16 v[128:131], a[204:207], v[196:199], v[128:131]\n"
        "v_cvt_pk_bf16_f32 v109, v112, v113\n"
        "v_cvt_pk_bf16_f32 v110, v124, v125\n"
        "v_mfma_f32_16x16x32_bf16 v[40:43], v[180:183], a[216:219], 0\n"
        "v_cvt_pk_bf16_f32 v111, v126, v127\n"
        "v_cvt_pk_bf16_f32 v118, v120, v121\n"
        "v_mfma_f32_16x16x32_bf16 v[40:43], v[192:195], a[220:223], v[40:43]\n"
        "s_nop 0\n"
        "v_cvt_pk_bf16_f32 v119, v122, v123\n"
        "v_cvt_pk_bf16_f32 v120, v128, v129\n"
        "v_mfma_f32_16x16x32_bf16 v[40:43], v[184:187], a[224:227], v[40:43]\n"
        "v_cvt_pk_bf16_f32 v121, v130, v131\n"
        "v_pk_mul_f16 v154, v154, v166\n"
        "v_mfma_f32_16x16x32_bf16 v[122:125], v[176:179], a[224:227], 0\n"
        "v_pk_mul_f16 v155, v155, v167\n"
        "v_cvt_pk_f16_f32 v160, v114, v115\n"
        "v_mfma_f32_16x16x32_bf16 v[122:125], v[168:171], a[220:223], v[122:125]\n"
        "v_exp_f16_sdwa v154, v154 dst_sel:WORD_0 dst_unused:UNUSED_PRESERVE src0_sel:WORD_0\n"
        "v_mfma_f32_16x16x32_bf16 v[122:125], v[200:203], a[216:219], v[122:125]\n"
        "v_exp_f16_sdwa v155, v155 dst_sel:WORD_0 dst_unused:UNUSED_PRESERVE src0_sel:WORD_0\n"
        "v_mfma_f32_16x16x32_bf16 v[128:131], v[196:199], a[216:219], 0\n"
        "v_exp_f16_sdwa v154, v154 dst_sel:WORD_1 dst_unused:UNUSED_PRESERVE src0_sel:WORD_1\n"
        "v_mfma_f32_16x16x32_bf16 v[128:131], v[188:191], a[220:223], v[128:131]\n"
        "v_exp_f16_sdwa v155, v155 dst_sel:WORD_1 dst_unused:UNUSED_PRESERVE src0_sel:WORD_1\n"
        "v_mfma_f32_16x16x32_bf16 v[128:131], v[172:175], a[224:227], v[128:131]\n"
        "v_pk_add_f16 v105, v105, v154\n"
        "v_pk_add_f16 v105, v105, v155\n"
        "v_mfma_f32_16x16x32_bf16 v[132:135], v[180:183], v[34:37], 0\n"
        "v_cvt_pk_f16_f32 v161, v116, v117\n"
        "v_pk_mul_f16 v160, v160, v216 op_sel:[0,1]\n"
        "v_mfma_f32_16x16x32_bf16 v[132:135], v[192:195], v[108:111], v[132:135]\n"
        "v_pk_mul_f16 v161, v161, v216 op_sel:[0,1]\n"
        "v_pk_mul_f16 v160, v160, v252\n"
        "v_mfma_f32_16x16x32_bf16 v[132:135], v[184:187], v[118:121], v[132:135]\n"
        "v_pk_mul_f16 v161, v161, v253\n"
        "v_cvt_pk_f16_f32 v224, v40, v41\n"
        "v_mfma_f32_16x16x32_bf16 v[112:115], v[176:179], v[118:121], 0\n"
        "v_cndmask_b32_e64 v158, 0, v254, s[62:63]\n"
        "v_cndmask_b32_e64 v162, 0, v254, s[64:65]\n"
        "v_mfma_f32_16x16x32_bf16 v[112:115], v[168:171], v[108:111], v[112:115]\n"
        "v_pk_add_f16 v104, v104, v158\n"
        "v_cvt_pk_f16_f32 v225, v42, v43\n"
        "v_mfma_f32_16x16x32_bf16 v[112:115], v[200:203], v[34:37], v[112:115]\n"
        "v_exp_f16_sdwa v160, v160 dst_sel:WORD_0 dst_unused:UNUSED_PRESERVE src0_sel:WORD_0\n"
        "v_mfma_f32_16x16x32_bf16 v[40:43], v[196:199], v[34:37], 0\n"
        "v_mov_b64 v[106:107], 0\n"
        "v_cvt_pk_f16_f32 v226, v122, v123\n"
        "v_mfma_f32_16x16x32_bf16 v[40:43], v[188:191], v[108:111], v[40:43]\n"
        "v_exp_f16_sdwa v161, v161 dst_sel:WORD_0 dst_unused:UNUSED_PRESERVE src0_sel:WORD_0\n"
        "v_mfma_f32_16x16x32_bf16 v[40:43], v[172:175], v[118:121], v[40:43]\n"
        "v_exp_f16_sdwa v160, v160 dst_sel:WORD_1 dst_unused:UNUSED_PRESERVE src0_sel:WORD_1\n"
        "v_mfma_f32_16x16x32_bf16 v[108:111], v[172:175], a[236:239], 0\n"
        "v_exp_f16_sdwa v161, v161 dst_sel:WORD_1 dst_unused:UNUSED_PRESERVE src0_sel:WORD_1\n"
        "v_mfma_f32_16x16x32_bf16 v[108:111], v[188:191], a[232:235], v[108:111]\n"
        "v_pk_add_f16 v105, v105, v160\n"
        "v_pk_add_f16 v105, v105, v161\n"
        "v_mfma_f32_16x16x32_bf16 v[108:111], v[196:199], a[228:231], v[108:111]\n"
        "v_pk_add_f16 v105, v105, v162\n"
        "v_cvt_pk_f16_f32 v227, v124, v125\n"
        "v_mfma_f32_16x16x32_bf16 v[124:127], v[200:203], a[228:231], 0\n"
        "v_cvt_pk_f16_f32 v232, v128, v129\n"
        "v_cvt_pk_f16_f32 v233, v130, v131\n"
        "v_mfma_f32_16x16x32_bf16 v[124:127], v[168:171], a[232:235], v[124:127]\n"
        "v_cvt_pk_f16_f32 v34, v132, v133\n"
        "v_cvt_pk_f16_f32 v35, v134, v135\n"
        "v_mfma_f32_16x16x32_bf16 v[124:127], v[176:179], a[236:239], v[124:127]\n"
        "v_pk_mul_f16 v34, v34, v217 op_sel_hi:[1,0]\n"
        "v_pk_mul_f16 v35, v35, v217 op_sel_hi:[1,0]\n"
        "v_mfma_f32_16x16x32_bf16 v[128:131], v[184:187], a[236:239], 0\n"
        "v_pk_mul_f16 v34, v34, v164\n"
        "v_pk_mul_f16 v35, v35, v165\n"
        "v_mfma_f32_16x16x32_bf16 v[128:131], v[192:195], a[232:235], v[128:131]\n"
        "v_exp_f16_sdwa v34, v34 dst_sel:WORD_0 dst_unused:UNUSED_PRESERVE src0_sel:WORD_0\n"
        "v_mfma_f32_16x16x32_bf16 v[128:131], v[180:183], a[228:231], v[128:131]\n"
        "v_exp_f16_sdwa v35, v35 dst_sel:WORD_0 dst_unused:UNUSED_PRESERVE src0_sel:WORD_0\n"
        "v_mfma_f32_16x16x32_bf16 v[140:143], v[180:183], a[240:243], 0\n"
        "v_exp_f16_sdwa v34, v34 dst_sel:WORD_1 dst_unused:UNUSED_PRESERVE src0_sel:WORD_1\n"
        "v_mfma_f32_16x16x32_bf16 v[140:143], v[192:195], a[244:247], v[140:143]\n"
        "v_exp_f16_sdwa v35, v35 dst_sel:WORD_1 dst_unused:UNUSED_PRESERVE src0_sel:WORD_1\n"
        "v_mfma_f32_16x16x32_bf16 v[140:143], v[184:187], a[248:251], v[140:143]\n"
        "v_cvt_pk_f16_f32 v36, v112, v113\n"
        "v_cvt_pk_f16_f32 v37, v114, v115\n"
        "v_mfma_f32_16x16x32_bf16 v[132:135], v[176:179], a[248:251], 0\n"
        "v_pk_mul_f16 v36, v36, v217 op_sel_hi:[1,0]\n"
        "v_pk_mul_f16 v37, v37, v217 op_sel_hi:[1,0]\n"
        "v_mfma_f32_16x16x32_bf16 v[132:135], v[168:171], a[244:247], v[132:135]\n"
        "v_pk_mul_f16 v36, v36, v166\n"
        "v_pk_mul_f16 v37, v37, v167\n"
        "v_mfma_f32_16x16x32_bf16 v[132:135], v[200:203], a[240:243], v[132:135]\n"
        "v_exp_f16_sdwa v36, v36 dst_sel:WORD_0 dst_unused:UNUSED_PRESERVE src0_sel:WORD_0\n"
        "v_mfma_f32_16x16x32_bf16 v[136:139], v[196:199], a[240:243], 0\n"
        "v_exp_f16_sdwa v37, v37 dst_sel:WORD_0 dst_unused:UNUSED_PRESERVE src0_sel:WORD_0\n"
        "v_mfma_f32_16x16x32_bf16 v[136:139], v[188:191], a[244:247], v[136:139]\n"
        "v_exp_f16_sdwa v36, v36 dst_sel:WORD_1 dst_unused:UNUSED_PRESERVE src0_sel:WORD_1\n"
        "v_mfma_f32_16x16x32_bf16 v[136:139], v[172:175], a[248:251], v[136:139]\n"
        "v_exp_f16_sdwa v37, v37 dst_sel:WORD_1 dst_unused:UNUSED_PRESERVE src0_sel:WORD_1\n"
        "v_mfma_f32_16x16x32_f16 v[112:115], v[68:71], v[104:107], 0\n"
        "v_cvt_pk_f16_f32 v248, v40, v41\n"
        "v_cvt_pk_f16_f32 v249, v42, v43\n"
        "v_mfma_f32_16x16x32_bf16 v[40:43], v[172:175], v[48:51], 0\n"
        "v_pk_mul_f16 v248, v248, v217 op_sel_hi:[1,0]\n"
        "v_pk_mul_f16 v249, v249, v217 op_sel_hi:[1,0]\n"
        "v_mfma_f32_16x16x32_bf16 v[40:43], v[188:191], v[100:103], v[40:43]\n"
        "v_pk_mul_f16 v248, v248, v252\n"
        "v_pk_mul_f16 v249, v249, v253\n"
        "v_mfma_f32_16x16x32_bf16 v[40:43], v[196:199], a[252:255], v[40:43]\n"
        "v_exp_f16_sdwa v248, v248 dst_sel:WORD_0 dst_unused:UNUSED_PRESERVE src0_sel:WORD_0\n"
        "v_mfma_f32_16x16x32_bf16 v[236:239], v[200:203], a[252:255], 0\n"
        "v_cndmask_b32_e64 v250, 0, v254, s[66:67]\n"
        "v_cvt_pk_f16_f32 v208, v108, v109\n"
        "v_mfma_f32_16x16x32_bf16 v[236:239], v[168:171], v[100:103], v[236:239]\n"
        "ds_read2_b32 v[234:235], v24 offset0:0 offset1:0\n"
        "ds_read2_b32 v[210:211], v24 offset0:1 offset1:1\n"
        "v_mfma_f32_16x16x32_bf16 v[236:239], v[176:179], v[48:51], v[236:239]\n"
        "ds_read2_b32 v[242:243], v24 offset0:2 offset1:2\n"
        "ds_read2_b32 v[218:219], v24 offset0:3 offset1:3\n"
        "v_mfma_f32_16x16x32_bf16 v[116:119], v[184:187], v[48:51], 0\n"
        "ds_read2_b32 v[206:207], v24 offset0:4 offset1:4\n"
        "v_mov_b32 v159, 0\n"
        "v_mfma_f32_16x16x32_bf16 v[116:119], v[192:195], v[100:103], v[116:119]\n"
        "ds_read2_b32 v[222:223], v24 offset0:5 offset1:5\n"
        "v_cvt_pk_f16_f32 v209, v110, v111\n"
        "v_mfma_f32_16x16x32_bf16 v[116:119], v[180:183], a[252:255], v[116:119]\n"
        "v_exp_f16_sdwa v249, v249 dst_sel:WORD_0 dst_unused:UNUSED_PRESERVE src0_sel:WORD_0\n"
        "v_mfma_f32_16x16x32_bf16 v[120:123], v[180:183], v[84:87], 0\n"
        "v_exp_f16_sdwa v248, v248 dst_sel:WORD_1 dst_unused:UNUSED_PRESERVE src0_sel:WORD_1\n"
        "v_mfma_f32_16x16x32_bf16 v[120:123], v[192:195], v[96:99], v[120:123]\n"
        "v_exp_f16_sdwa v249, v249 dst_sel:WORD_1 dst_unused:UNUSED_PRESERVE src0_sel:WORD_1\n"
        "v_mfma_f32_16x16x32_bf16 v[120:123], v[184:187], v[76:79], v[120:123]\n"
        "v_cvt_pk_f16_f32 v246, v124, v125\n"
        "v_cvt_pk_f16_f32 v247, v126, v127\n"
        "v_mfma_f32_16x16x32_bf16 v[108:111], v[176:179], v[76:79], 0\n"
        "v_cvt_pk_f16_f32 v244, v128, v129\n"
        "v_cvt_pk_f16_f32 v245, v130, v131\n"
        "v_mfma_f32_16x16x32_bf16 v[108:111], v[168:171], v[96:99], v[108:111]\n"
        "v_cvt_pk_f16_f32 v212, v140, v141\n"
        "v_cvt_pk_f16_f32 v213, v142, v143\n"
        "v_mfma_f32_16x16x32_bf16 v[108:111], v[200:203], v[84:87], v[108:111]\n"
        "v_cvt_pk_f16_f32 v214, v132, v133\n"
        "v_cvt_pk_f16_f32 v215, v134, v135\n"
        "v_mfma_f32_16x16x32_bf16 v[128:131], v[196:199], v[84:87], 0\n"
        "s_waitcnt lgkmcnt(0)\n"
        "v_pk_add_f16 v106, v34, v35\n"
        "v_pk_add_f16 v106, v106, v36\n"
        "v_mfma_f32_16x16x32_bf16 v[128:131], v[188:191], v[96:99], v[128:131]\n"
        "v_pk_add_f16 v106, v106, v37\n"
        "v_pk_add_f16 v106, v106, v248\n"
        "v_mfma_f32_16x16x32_bf16 v[128:131], v[172:175], v[76:79], v[128:131]\n"
        "v_cvt_pk_f16_f32 v240, v136, v137\n"
        "v_cvt_pk_f16_f32 v241, v138, v139\n"
        "v_mfma_f32_16x16x32_bf16 v[132:135], v[172:175], v[80:83], 0\n"
        "v_pk_add_f16 v106, v106, v249\n"
        "v_pk_add_f16 v106, v106, v250\n"
        "v_mfma_f32_16x16x32_bf16 v[132:135], v[188:191], v[92:95], v[132:135]\n"
        "v_rcp_f32 v231, v112\n"
        "v_mfma_f32_16x16x32_bf16 v[132:135], v[196:199], v[64:67], v[132:135]\n"
        "v_cvt_f16_f32 v254, v231\n"
        "v_pk_mul_f16 v148, v148, v254 op_sel_hi:[1,0]\n"
        "v_mfma_f32_16x16x32_bf16 v[136:139], v[200:203], v[64:67], 0\n"
        "v_pk_mul_f16 v149, v149, v254 op_sel_hi:[1,0]\n"
        "v_pk_mul_f16 v150, v150, v254 op_sel_hi:[1,0]\n"
        "v_mfma_f32_16x16x32_bf16 v[136:139], v[168:171], v[92:95], v[136:139]\n"
        "v_pk_mul_f16 v151, v151, v254 op_sel_hi:[1,0]\n"
        "v_pk_mul_f16 v156, v156, v254 op_sel_hi:[1,0]\n"
        "v_mfma_f32_16x16x32_bf16 v[136:139], v[176:179], v[80:83], v[136:139]\n"
        "v_pk_mul_f16 v157, v157, v254 op_sel_hi:[1,0]\n"
        "v_pk_mul_f16 v158, v158, v254 op_sel_hi:[1,0]\n"
        "v_mfma_f32_16x16x32_bf16 v[144:147], v[184:187], v[80:83], 0\n"
        "v_mov_b32 v163, 0\n"
        "v_pk_mul_f16 v148, v148, v164\n"
        "v_mfma_f32_16x16x32_bf16 v[144:147], v[192:195], v[92:95], v[144:147]\n"
        "v_pk_mul_f16 v149, v149, v165\n"
        "v_pk_mul_f16 v150, v150, v166\n"
        "v_mfma_f32_16x16x32_bf16 v[144:147], v[180:183], v[64:67], v[144:147]\n"
        "v_pk_mul_f16 v151, v151, v167\n"
        "v_pk_mul_f16 v156, v156, v252\n"
        "v_mfma_f32_16x16x32_f16 v[140:143], v[68:71], v[104:107], 0\n"
        "v_pk_mul_f16 v157, v157, v253\n"
        "v_max3_f32 v251, s95, v112, v113\n"
        "v_mfma_f32_16x16x32_f16 v[104:107], v[148:151], v[224:227], 0\n"
        "v_rcp_f32 v230, v113\n"
        "v_mfma_f32_16x16x32_f16 v[104:107], v[156:159], v[232:235], v[104:107]\n"
        "v_add_f32 v254, v255, v231\n"
        "v_cvt_f16_f32 v255, v230\n"
        "v_mfma_f32_16x16x32_bf16 v[104:107], v[180:183], v[52:55], v[104:107]\n"
        "v_add_f32 v254, v254, v230\n"
        "v_pk_mul_f16 v152, v152, v255 op_sel_hi:[1,0]\n"
        "v_mfma_f32_16x16x32_bf16 v[124:127], v[180:183], v[56:59], 0\n"
        "v_pk_mul_f16 v153, v153, v255 op_sel_hi:[1,0]\n"
        "v_pk_mul_f16 v154, v154, v255 op_sel_hi:[1,0]\n"
        "v_mfma_f32_16x16x32_f16 v[124:127], v[156:159], v[208:211], v[124:127]\n"
        "v_pk_mul_f16 v155, v155, v255 op_sel_hi:[1,0]\n"
        "v_pk_mul_f16 v160, v160, v255 op_sel_hi:[1,0]\n"
        "v_mfma_f32_16x16x32_f16 v[124:127], v[148:151], v[244:247], v[124:127]\n"
        "v_pk_mul_f16 v161, v161, v255 op_sel_hi:[1,0]\n"
        "v_pk_mul_f16 v162, v162, v255 op_sel_hi:[1,0]\n"
        "v_mfma_f32_16x16x32_f16 v[112:115], v[148:151], v[212:215], 0\n"
        "v_cvt_pk_f16_f32 v216, v40, v41\n"
        "v_cvt_pk_f16_f32 v217, v42, v43\n"
        "v_mfma_f32_16x16x32_f16 v[112:115], v[156:159], v[240:243], v[112:115]\n"
        "v_cvt_pk_f16_f32 v230, v236, v237\n"
        "v_cvt_pk_f16_f32 v231, v238, v239\n"
        "v_mfma_f32_16x16x32_bf16 v[112:115], v[192:195], v[52:55], v[112:115]\n"
        "v_cvt_pk_f16_f32 v228, v116, v117\n"
        "v_cvt_pk_f16_f32 v229, v118, v119\n"
        "v_mfma_f32_16x16x32_bf16 v[116:119], v[192:195], v[56:59], 0\n"
        "v_cvt_pk_f16_f32 v40, v120, v121\n"
        "v_cvt_pk_f16_f32 v41, v122, v123\n"
        "v_mfma_f32_16x16x32_f16 v[116:119], v[156:159], v[216:219], v[116:119]\n"
        "v_cvt_pk_f16_f32 v42, v108, v109\n"
        "v_cvt_pk_f16_f32 v43, v110, v111\n"
        "v_mfma_f32_16x16x32_f16 v[116:119], v[148:151], v[228:231], v[116:119]\n"
        "v_cvt_pk_f16_f32 v204, v128, v129\n"
        "v_cvt_pk_f16_f32 v205, v130, v131\n"
        "v_mfma_f32_16x16x32_f16 v[128:131], v[148:151], v[40:43], 0\n"
        "v_pk_mul_f16 v152, v152, v164\n"
        "v_pk_mul_f16 v153, v153, v165\n"
        "v_mfma_f32_16x16x32_f16 v[128:131], v[156:159], v[204:207], v[128:131]\n"
        "v_cvt_pk_f16_f32 v220, v132, v133\n"
        "v_cvt_pk_f16_f32 v221, v134, v135\n"
        "v_mfma_f32_16x16x32_bf16 v[128:131], v[184:187], v[52:55], v[128:131]\n"
        "v_cvt_pk_f16_f32 v238, v136, v137\n"
        "v_cvt_pk_f16_f32 v239, v138, v139\n"
        "v_mfma_f32_16x16x32_bf16 v[136:139], v[184:187], v[56:59], 0\n"
        "v_cvt_pk_f16_f32 v236, v144, v145\n"
        "v_cvt_pk_f16_f32 v237, v146, v147\n"
        "v_mfma_f32_16x16x32_f16 v[136:139], v[156:159], v[220:223], v[136:139]\n"
        "v_pk_mul_f16 v154, v154, v166\n"
        "v_pk_mul_f16 v155, v155, v167\n"
        "v_mfma_f32_16x16x32_f16 v[136:139], v[148:151], v[236:239], v[136:139]\n"
        "s_add_u32 s90, s89, 0\n"
        "v_pk_mul_f16 v160, v160, v252\n"
        "v_pk_mul_f16 v161, v161, v253\n"
        "v_mfma_f32_16x16x32_f16 v[132:135], v[152:155], v[224:227], 0\n"
        "v_rcp_f32 v255, v142\n"
        "v_mfma_f32_16x16x32_f16 v[132:135], v[160:163], v[232:235], v[132:135]\n"
        "v_max_f32 v251, v251, v142\n"
        "v_add_f32 v254, v254, v255\n"
        "v_mfma_f32_16x16x32_bf16 v[132:135], v[200:203], v[52:55], v[132:135]\n"
        "v_fma_f32 v251, v254, s82, v251\n"
        "v_cmp_nlt_f32_e64 s[74:75], v251, s83\n"
        "v_mfma_f32_16x16x32_bf16 v[108:111], v[200:203], v[56:59], 0\n"
        "v_cvt_pk_bf16_f32 v140, v104, v124\n"
        "v_cvt_pk_bf16_f32 v141, v112, v116\n"
        "v_mfma_f32_16x16x32_f16 v[108:111], v[160:163], v[208:211], v[108:111]\n"
        "s_cmp_lg_u64 s[74:75], 0\n"
        "v_cvt_pk_bf16_f32 v142, v128, v136\n"
        "v_mov_b32 v251, 0\n"
        "v_mfma_f32_16x16x32_f16 v[108:111], v[152:155], v[244:247], v[108:111]\n"
        "s_cbranch_scc1 SLOW_%=\n"
        "v_cvt_pk_bf16_f32 v144, v105, v125\n"
        "v_cvt_pk_bf16_f32 v145, v113, v117\n"
        "v_mfma_f32_16x16x32_f16 v[120:123], v[152:155], v[212:215], 0\n"
        "s_add_u32 s91, s89, 98112\n"
        "v_cvt_pk_bf16_f32 v146, v129, v137\n"
        "v_cvt_f16_f32 v254, v255\n"
        "v_mfma_f32_16x16x32_f16 v[120:123], v[160:163], v[240:243], v[120:123]\n"
        "v_cvt_pk_bf16_f32 v112, v106, v126\n"
        "v_cvt_pk_bf16_f32 v113, v114, v118\n"
        "v_mfma_f32_16x16x32_bf16 v[120:123], v[168:171], v[52:55], v[120:123]\n"
        "buffer_store_dwordx3 v[140:142], v14, s[40:43], s90 offen sc1 nt\n"
        "v_cvt_pk_bf16_f32 v114, v130, v138\n"
        "v_pk_mul_f16 v34, v34, v254 op_sel_hi:[1,0]\n"
        "v_cvt_pk_bf16_f32 v124, v107, v127\n"
        "v_cvt_pk_bf16_f32 v125, v115, v119\n"
        "v_mfma_f32_16x16x32_bf16 v[104:107], v[168:171], v[56:59], 0\n"
        "v_cvt_pk_bf16_f32 v126, v131, v139\n"
        "v_pk_mul_f16 v35, v35, v254 op_sel_hi:[1,0]\n"
        "v_mfma_f32_16x16x32_f16 v[104:107], v[160:163], v[216:219], v[104:107]\n"
        "v_pk_mul_f16 v36, v36, v254 op_sel_hi:[1,0]\n"
        "v_pk_mul_f16 v37, v37, v254 op_sel_hi:[1,0]\n"
        "v_mfma_f32_16x16x32_f16 v[104:107], v[152:155], v[228:231], v[104:107]\n"
        "buffer_store_dwordx3 v[144:146], v15, s[40:43], s90 offen offset:768 sc1 nt\n"
        "v_pk_mul_f16 v248, v248, v254 op_sel_hi:[1,0]\n"
        "v_pk_mul_f16 v249, v249, v254 op_sel_hi:[1,0]\n"
        "v_pk_mul_f16 v250, v250, v254 op_sel_hi:[1,0]\n"
        "v_pk_mul_f16 v34, v34, v164\n"
        "v_mfma_f32_16x16x32_f16 v[116:119], v[152:155], v[40:43], 0\n"
        "v_pk_mul_f16 v35, v35, v165\n"
        "v_pk_mul_f16 v36, v36, v166\n"
        "v_mfma_f32_16x16x32_f16 v[116:119], v[160:163], v[204:207], v[116:119]\n"
        "v_pk_mul_f16 v37, v37, v167\n"
        "v_pk_mul_f16 v248, v248, v252\n"
        "v_mfma_f32_16x16x32_bf16 v[116:119], v[176:179], v[52:55], v[116:119]\n"
        "buffer_store_dwordx3 v[112:114], v14, s[40:43], s91 offen offset:192 sc1 nt\n"
        "v_pk_mul_f16 v249, v249, v253\n"
        "v_cvt_pk_bf16_f32 v142, v132, v108\n"
        "v_cvt_pk_bf16_f32 v180, v133, v109\n"
        "v_cvt_pk_bf16_f32 v184, v134, v110\n"
        "v_mfma_f32_16x16x32_bf16 v[112:115], v[176:179], v[56:59], 0\n"
        "v_cvt_pk_bf16_f32 v143, v120, v104\n"
        "v_cvt_pk_bf16_f32 v181, v121, v105\n"
        "v_mfma_f32_16x16x32_f16 v[112:115], v[160:163], v[220:223], v[112:115]\n"
        "v_cvt_pk_bf16_f32 v185, v122, v106\n"
        "v_cvt_pk_bf16_f32 v166, v135, v111\n"
        "v_mfma_f32_16x16x32_f16 v[112:115], v[152:155], v[236:239], v[112:115]\n"
        "buffer_store_dwordx3 v[124:126], v16, s[40:43], s91 offen offset:960 sc1 nt\n"
        "v_cvt_pk_bf16_f32 v167, v123, v107\n"
        "v_mfma_f32_16x16x32_f16 v[104:107], v[34:37], v[224:227], 0\n"
        "s_nop 2\n"
        "v_mfma_f32_16x16x32_f16 v[104:107], v[248:251], v[232:235], v[104:107]\n"
        "s_waitcnt vmcnt(4)\n"
        "v_cvt_pk_bf16_f32 v144, v116, v112\n"
        "v_cvt_pk_bf16_f32 v182, v117, v113\n"
        "v_mfma_f32_16x16x32_bf16 v[104:107], v[196:199], v[52:55], v[104:107]\n"
        "s_add_u32 s90, s89, 196224\n"
        "ds_read_b128 v[146:149], v9\n"
        "v_cvt_pk_bf16_f32 v186, v118, v114\n"
        "v_mfma_f32_16x16x32_bf16 v[108:111], v[196:199], v[56:59], 0\n"
        "s_cmp_le_i32 s85, 1\n"
        "ds_read_b128 v[152:155], v10\n"
        "v_cvt_pk_bf16_f32 v168, v119, v115\n"
        "v_mfma_f32_16x16x32_f16 v[108:111], v[248:251], v[208:211], v[108:111]\n"
        "s_cbranch_scc1 DY3_%=\n"
        "DY3B_%=:\n"
        "ds_read_b128 v[158:161], v11\n"
        "v_mfma_f32_16x16x32_f16 v[108:111], v[34:37], v[244:247], v[108:111]\n"
        "s_add_u32 s91, s89, 294336\n"
        "ds_read_b128 v[126:129], v9 offset:3072\n"
        "v_mfma_f32_16x16x32_f16 v[122:125], v[34:37], v[212:215], 0\n"
        "s_nop 0\n"
        "ds_read_b128 v[132:135], v10 offset:3072\n"
        "v_mfma_f32_16x16x32_f16 v[122:125], v[248:251], v[240:243], v[122:125]\n"
        "s_nop 0\n"
        "ds_read_b128 v[138:141], v11 offset:3072\n"
        "v_cvt_pk_bf16_f32 v192, v104, v108\n"
        "v_mfma_f32_16x16x32_bf16 v[122:125], v[188:191], v[52:55], v[122:125]\n"
        "buffer_store_dwordx3 v[142:144], v14, s[40:43], s90 offen offset:384 sc1 nt\n"
        "v_cvt_pk_bf16_f32 v196, v105, v109\n"
        "v_cvt_pk_bf16_f32 v200, v106, v110\n"
        "v_cvt_pk_bf16_f32 v176, v107, v111\n"
        "v_mfma_f32_16x16x32_bf16 v[142:145], v[188:191], v[56:59], 0\n"
        "ds_read_b128 v[106:109], v9 offset:6144\n"
        "ds_read_b128 v[112:115], v10 offset:6144\n"
        "v_mfma_f32_16x16x32_f16 v[142:145], v[248:251], v[216:219], v[142:145]\n"
        "ds_read_b128 v[118:121], v11 offset:6144\n"
        "v_mfma_f32_16x16x32_f16 v[142:145], v[34:37], v[228:231], v[142:145]\n"
        "buffer_store_dwordx3 v[180:182], v17, s[40:43], s90 offen offset:1152 sc1 nt\n"
        "s_add_u32 s90, s89, 392448\n"
        "v_mfma_f32_16x16x32_f16 v[162:165], v[34:37], v[40:43], 0\n"
        "s_waitcnt lgkmcnt(6)\n"
        "s_nop 1\n"
        "v_mfma_f32_16x16x32_f16 v[162:165], v[248:251], v[204:207], v[162:165]\n"
        "s_nop 0\n"
        "v_cvt_pk_bf16_f32 v193, v122, v142\n"
        "v_cvt_pk_bf16_f32 v197, v123, v143\n"
        "v_mfma_f32_16x16x32_bf16 v[162:165], v[172:175], v[52:55], v[162:165]\n"
        "buffer_store_dwordx3 v[184:186], v14, s[40:43], s91 offen offset:576 sc1 nt\n"
        "v_cvt_pk_bf16_f32 v201, v124, v144\n"
        "v_cvt_pk_bf16_f32 v177, v125, v145\n"
        "v_mfma_f32_16x16x32_bf16 v[178:181], v[172:175], v[56:59], 0\n"
        "s_cmp_ge_i32 s85, 0\n"
        "v_mfma_f32_16x16x32_f16 v[178:181], v[248:251], v[220:223], v[178:181]\n"
        "v_mfma_f32_16x16x32_f16 v[178:181], v[34:37], v[236:239], v[178:181]\n"
        "buffer_store_dwordx3 v[166:168], v14, s[40:43], s90 offen sc1 nt\n"
        "s_cbranch_scc1 TOP_%=\n"
        "LAST_%=:\n"
        "s_waitcnt lgkmcnt(0)\n"
        "s_nop 7\n"
        "s_mov_b32 s26, s89\n"
        "s_add_u32 s27, s26, 392448\n"
        "v_cvt_pk_bf16_f32 v194, v162, v178\n"
        "buffer_store_dwordx3 v[192:194], v15, s[40:43], s27 offen offset:768 sc1 nt\n"
        "v_cvt_pk_bf16_f32 v198, v163, v179\n"
        "s_add_u32 s28, s26, 490560\n"
        "buffer_store_dwordx3 v[196:198], v14, s[40:43], s28 offen offset:192 sc1 nt\n"
        "v_cvt_pk_bf16_f32 v202, v164, v180\n"
        "buffer_store_dwordx3 v[200:202], v16, s[40:43], s28 offen offset:960 sc1 nt\n"
        "v_cvt_pk_bf16_f32 v178, v165, v181\n"
        "s_add_u32 s27, s26, 588672\n"
        "buffer_store_dwordx3 v[176:178], v14, s[40:43], s27 offen offset:384 sc1 nt\n"
        "ds_read_u16 v255, v19 offset:2048\n"
        "s_waitcnt lgkmcnt(0)\n"
        "s_mov_b32 s30, s29\n"
        "v_cmp_ne_u32_e64 s[24:25], s30, v255\n"
        "s_nop 3\n"
        "s_cmp_lg_u64 s[24:25], 0\n"
        "s_cbranch_scc1 SP20_%=\n"
        "BK20_%=:\n"
        "ds_read_b128 v[180:183], v13\n"
        "ds_read_b128 v[192:195], v13 offset:1024\n"
        "ds_read_b128 v[184:187], v13 offset:2048\n"
        "ds_read_b128 v[200:203], v13 offset:3072\n"
        "ds_read_b128 v[168:171], v13 offset:4096\n"
        "ds_read_b128 v[176:179], v13 offset:5120\n"
        "ds_read_b128 v[196:199], v13 offset:6144\n"
        "ds_read_b128 v[188:191], v13 offset:7168\n"
        "ds_read_b128 v[172:175], v13 offset:8192\n"
        "ds_read_b128 v[132:135], v20\n"
        "ds_read_b64 v[30:31], v20 offset:16\n"
        "ds_read_u16 v253, v22\n"
        "ds_read_b64 v[34:35], v21\n"
        "v_mov_b32 v26, 0\n"
        "s_mov_b64 exec, 1\n"
        "buffer_atomic_add v26, off, s[16:19], s15 offset:4 sc0\n"
        "s_mov_b64 exec, -1\n"
        "s_waitcnt lgkmcnt(10)\n"
        "v_mfma_f32_16x16x32_bf16 v[112:115], a[152:155], v[184:187], 0\n"
        "s_add_u32 s89, s86, s51\n"
        "ds_read_b128 v[124:127], v20 offset:512\n"
        "v_add_u32 v23, s22, v23\n"
        "v_mfma_f32_16x16x32_bf16 v[112:115], a[148:151], v[192:195], v[112:115]\n"
        "s_add_u32 s29, s29, 1\n"
        "ds_read_b128 v[128:131], v20 offset:528\n"
        "v_add_u32 v24, s22, v24\n"
        "v_mfma_f32_16x16x32_bf16 v[112:115], a[144:147], v[180:183], v[112:115]\n"
        "s_mov_b32 s22, s23\n"
        "ds_read_b128 v[120:123], v23\n"
        "v_lshlrev_b32 v104, 16, v234\n"
        "v_mfma_f32_16x16x32_bf16 v[108:111], a[156:159], v[180:183], 0\n"
        "s_movk_i32 s23, 1584\n"
        "ds_read_b128 v[140:143], v23 offset:64\n"
        "v_lshlrev_b32 v105, 16, v210\n"
        "v_mfma_f32_16x16x32_bf16 v[108:111], a[160:163], v[192:195], v[108:111]\n"
        "s_mov_b32 s86, s87\n"
        "ds_read_b128 v[152:155], v23 offset:128\n"
        "v_lshlrev_b32 v106, 16, v242\n"
        "v_mfma_f32_16x16x32_bf16 v[108:111], a[164:167], v[184:187], v[108:111]\n"
        "s_add_u32 s88, s88, 32\n"
        "ds_read_b64 v[36:37], v24 offset:360\n"
        "v_lshlrev_b32 v107, 16, v218\n"
        "v_mfma_f32_16x16x32_bf16 v[136:139], a[176:179], v[184:187], 0\n"
        "s_add_u32 s87, s87, 43008\n"
        "v_lshlrev_b32 v118, 16, v206\n"
        "v_lshlrev_b32 v119, 16, v222\n"
        "v_mfma_f32_16x16x32_bf16 v[136:139], a[172:175], v[192:195], v[136:139]\n"
        "s_cmp_ge_u32 s88, 73\n"
        "v_cvt_pk_bf16_f32 v144, v112, v113\n"
        "v_cvt_pk_bf16_f32 v145, v114, v115\n"
        "v_mfma_f32_16x16x32_bf16 v[136:139], a[168:171], v[180:183], v[136:139]\n"
        "s_cselect_b32 s92, 588672, 0\n"
        "v_cvt_pk_bf16_f32 v146, v108, v109\n"
        "v_cvt_pk_bf16_f32 v147, v110, v111\n"
        "v_mfma_f32_16x16x32_bf16 v[156:159], a[180:183], v[180:183], 0\n"
        "s_waitcnt lgkmcnt(4)\n"
        "v_pk_mul_f16 v110, v124, v224\n"
        "v_pk_mul_f16 v111, v124, v244\n"
        "v_mfma_f32_16x16x32_bf16 v[156:159], a[184:187], v[192:195], v[156:159]\n"
        "s_cselect_b32 s94, 73, 0\n"
        "v_pk_mul_f16 v112, v124, v212\n"
        "v_pk_mul_f16 v113, v124, v228\n"
        "v_mfma_f32_16x16x32_bf16 v[156:159], a[188:191], v[184:187], v[156:159]\n"
        "s_sub_u32 s88, s88, s94\n"
        "v_pk_mul_f16 v114, v124, v40\n"
        "v_pk_mul_f16 v115, v124, v236\n"
        "v_mfma_f32_16x16x32_bf16 v[148:151], a[200:203], v[184:187], 0\n"
        "s_add_u32 s87, s87, s92\n"
        "v_pk_fma_f16 v110, v125, v225, v110\n"
        "v_pk_fma_f16 v111, v125, v245, v111\n"
        "v_mfma_f32_16x16x32_bf16 v[148:151], a[196:199], v[192:195], v[148:151]\n"
        "s_waitcnt lgkmcnt(0)\n"
        "v_pk_fma_f16 v112, v125, v213, v112\n"
        "v_pk_fma_f16 v113, v125, v229, v113\n"
        "v_mfma_f32_16x16x32_bf16 v[148:151], a[192:195], v[180:183], v[148:151]\n"
        "s_cmp_gt_i32 s85, 0\n"
        "v_pk_fma_f16 v114, v125, v41, v114\n"
        "v_pk_fma_f16 v115, v125, v237, v115\n"
        "v_mfma_f32_16x16x32_bf16 v[160:163], a[204:207], v[180:183], 0\n"
        "s_cselect_b32 s94, s87, s84\n"
        "v_readlane_b32 s93, v37, 1\n"
        "v_cndmask_b32_e64 v36, 0, v36, s[72:73]\n"
        "v_mfma_f32_16x16x32_bf16 v[160:163], a[208:211], v[192:195], v[160:163]\n"
        "s_sub_u32 s85, s85, 1\n"
        "v_pk_fma_f16 v110, v126, v226, v110\n"
        "v_pk_fma_f16 v111, v126, v246, v111\n"
        "v_mfma_f32_16x16x32_bf16 v[160:163], a[212:215], v[184:187], v[160:163]\n"
        "s_waitcnt lgkmcnt(0)\n"
        "v_pk_fma_f16 v112, v126, v214, v112\n"
        "v_pk_fma_f16 v113, v126, v230, v113\n"
        "v_mfma_f32_16x16x32_bf16 v[248:251], v[120:123], v[180:183], 0\n"
        "s_waitcnt lgkmcnt(0)\n"
        "v_pk_fma_f16 v114, v126, v42, v114\n"
        "v_pk_fma_f16 v115, v126, v238, v115\n"
        "v_mfma_f32_16x16x32_bf16 v[248:251], v[140:143], v[192:195], v[248:251]\n"
        "v_pk_fma_f16 v110, v127, v227, v110\n"
        "v_pk_fma_f16 v111, v127, v247, v111\n"
        "v_mfma_f32_16x16x32_bf16 v[248:251], v[152:155], v[184:187], v[248:251]\n"
        "v_pk_fma_f16 v112, v127, v215, v112\n"
        "v_pk_fma_f16 v113, v127, v231, v113\n"
        "v_mfma_f32_16x16x32_bf16 v[164:167], v[152:155], v[176:179], 0\n"
        "v_pk_fma_f16 v114, v127, v43, v114\n"
        "v_pk_fma_f16 v115, v127, v239, v115\n"
        "v_mfma_f32_16x16x32_bf16 v[164:167], v[140:143], v[168:171], v[164:167]\n"
        "v_pk_fma_f16 v110, v128, v232, v110\n"
        "v_pk_fma_f16 v111, v128, v208, v111\n"
        "v_mfma_f32_16x16x32_bf16 v[164:167], v[120:123], v[200:203], v[164:167]\n"
        "v_pk_fma_f16 v112, v128, v240, v112\n"
        "v_pk_fma_f16 v113, v128, v216, v113\n"
        "v_mfma_f32_16x16x32_bf16 v[40:43], v[120:123], v[196:199], 0\n"
        "v_pk_fma_f16 v114, v128, v204, v114\n"
        "v_pk_fma_f16 v115, v128, v220, v115\n"
        "v_mfma_f32_16x16x32_bf16 v[40:43], v[140:143], v[188:191], v[40:43]\n"
        "v_pk_fma_f16 v110, v129, v233, v110\n"
        "v_pk_fma_f16 v111, v129, v209, v111\n"
        "v_mfma_f32_16x16x32_bf16 v[40:43], v[152:155], v[172:175], v[40:43]\n"
        "v_pk_fma_f16 v112, v129, v241, v112\n"
        "v_pk_fma_f16 v113, v129, v217, v113\n"
        "v_mfma_f32_16x16x32_bf16 v[140:143], v[180:183], v[144:147], 0\n"
        "v_pk_fma_f16 v114, v129, v205, v114\n"
        "v_pk_fma_f16 v115, v129, v221, v115\n"
        "v_cvt_pk_bf16_f32 v152, v136, v137\n"
        "v_cvt_pk_bf16_f32 v153, v138, v139\n"
        "v_cvt_pk_bf16_f32 v154, v156, v157\n"
        "v_cvt_pk_bf16_f32 v155, v158, v159\n"
        "s_nop 1\n"
        "v_mfma_f32_16x16x32_bf16 v[140:143], v[192:195], v[152:155], v[140:143]\n"
        "v_pk_fma_f16 v110, v130, v234, v110\n"
        "v_pk_fma_f16 v111, v130, v210, v111\n"
        "v_cvt_pk_bf16_f32 v136, v148, v149\n"
        "v_cvt_pk_bf16_f32 v137, v150, v151\n"
        "v_cvt_pk_bf16_f32 v138, v160, v161\n"
        "v_cvt_pk_bf16_f32 v139, v162, v163\n"
        "s_nop 1\n"
        "v_mfma_f32_16x16x32_bf16 v[140:143], v[184:187], v[136:139], v[140:143]\n"
        "v_pk_fma_f16 v112, v130, v242, v112\n"
        "v_pk_fma_f16 v113, v130, v218, v113\n"
        "v_mfma_f32_16x16x32_bf16 v[124:127], v[176:179], v[136:139], 0\n"
        "v_pk_fma_f16 v114, v130, v206, v114\n"
        "v_pk_fma_f16 v115, v130, v222, v115\n"
        "v_mfma_f32_16x16x32_bf16 v[124:127], v[168:171], v[152:155], v[124:127]\n"
        "v_cndmask_b32_e64 v254, v248, v164, s[64:65]\n"
        "v_cndmask_b32_e64 v255, v249, v165, s[64:65]\n"
        "v_mfma_f32_16x16x32_bf16 v[124:127], v[200:203], v[144:147], v[124:127]\n"
        "v_cndmask_b32_e64 v254, v254, v40, s[70:71]\n"
        "v_cndmask_b32_e64 v255, v255, v41, s[70:71]\n"
        "v_mfma_f32_16x16x32_bf16 v[120:123], v[196:199], v[144:147], 0\n"
        "v_cvt_pk_f16_f32 v254, v254, v255\n"
        "v_pk_mul_f16 v254, v254, v253 op_sel_hi:[1,0]\n"
        "v_mfma_f32_16x16x32_bf16 v[120:123], v[188:191], v[152:155], v[120:123]\n"
        "s_add_u32 s46, s26, 590016\n"
        "v_pk_mul_f16 v254, v254, s93 op_sel_hi:[1,0]\n"
        "v_cvt_pk_f16_f32 v108, v140, v141\n"
        "v_mfma_f32_16x16x32_bf16 v[120:123], v[172:175], v[136:139], v[120:123]\n"
        "v_exp_f16_sdwa v254, v254 dst_sel:WORD_0 dst_unused:UNUSED_PRESERVE src0_sel:WORD_0\n"
        "v_mfma_f32_16x16x32_f16 v[104:107], v[68:71], v[110:113], v[104:107]\n"
        "s_min_u32 s46, s46, s42\n"
        "v_exp_f16_sdwa v254, v254 dst_sel:WORD_1 dst_unused:UNUSED_PRESERVE src0_sel:WORD_1\n"
        "v_mfma_f32_16x16x32_f16 v[116:119], v[68:71], v[112:115], v[116:119]\n"
        "v_and_b32 v255, 0xffff0000, v254\n"
        "v_cvt_f32_f16 v252, v254\n"
        "v_mfma_f32_16x16x32_bf16 v[40:43], a[152:155], v[176:179], 0\n"
        "v_cndmask_b32_e64 v252, v252, v36, s[68:69]\n"
        "v_cvt_pk_f16_f32 v109, v142, v143\n"
        "v_mfma_f32_16x16x32_bf16 v[40:43], a[148:151], v[168:171], v[40:43]\n"
        "v_add_f32_dpp v254, v252, v252 row_ror:8 row_mask:0xf bank_mask:0xf\n"
        "v_pk_mul_f16 v108, v108, v34 op_sel_hi:[1,0]\n"
        "v_mfma_f32_16x16x32_bf16 v[40:43], a[144:147], v[200:203], v[40:43]\n"
        "v_pk_mul_f16 v109, v109, v34 op_sel_hi:[1,0]\n"
        "v_pk_mul_f16 v108, v108, v132\n"
        "v_mfma_f32_16x16x32_bf16 v[142:145], a[156:159], v[200:203], 0\n"
        "v_pk_mul_f16 v109, v109, v133\n"
        "v_add_f32_dpp v254, v254, v254 row_ror:4 row_mask:0xf bank_mask:0xf\n"
        "v_mfma_f32_16x16x32_bf16 v[142:145], a[160:163], v[168:171], v[142:145]\n"
        "v_exp_f16_sdwa v108, v108 dst_sel:WORD_0 dst_unused:UNUSED_PRESERVE src0_sel:WORD_0\n"
        "v_mfma_f32_16x16x32_bf16 v[142:145], a[164:167], v[176:179], v[142:145]\n"
        "v_exp_f16_sdwa v109, v109 dst_sel:WORD_0 dst_unused:UNUSED_PRESERVE src0_sel:WORD_0\n"
        "v_mfma_f32_16x16x32_bf16 v[136:139], a[176:179], v[176:179], 0\n"
        "v_exp_f16_sdwa v108, v108 dst_sel:WORD_1 dst_unused:UNUSED_PRESERVE src0_sel:WORD_1\n"
        "v_mfma_f32_16x16x32_bf16 v[136:139], a[172:175], v[168:171], v[136:139]\n"
        "v_exp_f16_sdwa v109, v109 dst_sel:WORD_1 dst_unused:UNUSED_PRESERVE src0_sel:WORD_1\n"
        "v_mfma_f32_16x16x32_bf16 v[136:139], a[168:171], v[200:203], v[136:139]\n"
        "v_add_f32_dpp v254, v254, v254 row_ror:2 row_mask:0xf bank_mask:0xf\n"
        "v_cvt_pk_bf16_f32 v128, v104, v105\n"
        "v_mfma_f32_16x16x32_bf16 v[112:115], a[188:191], v[176:179], 0\n"
        "v_cvt_pk_bf16_f32 v129, v106, v107\n"
        "v_cvt_pk_bf16_f32 v130, v118, v119\n"
        "v_mfma_f32_16x16x32_bf16 v[112:115], a[184:187], v[168:171], v[112:115]\n"
        "v_pk_add_f16 v140, v108, v109\n"
        "v_add_f32_dpp v254, v254, v254 row_ror:1 row_mask:0xf bank_mask:0xf\n"
        "v_mfma_f32_16x16x32_bf16 v[112:115], a[180:183], v[200:203], v[112:115]\n"
        "buffer_store_dwordx3 v[128:130], v17, s[44:47], s27 offen offset:1152 sc1 nt\n"
        "v_add_f32_dpp v254, v254, v254 row_bcast:15 row_mask:0xa bank_mask:0xf\n"
        "v_cvt_pk_f16_f32 v110, v124, v125\n"
        "v_cvt_pk_f16_f32 v111, v126, v127\n"
        "v_pk_mul_f16 v110, v110, v34 op_sel_hi:[1,0]\n"
        "v_mfma_f32_16x16x32_bf16 v[124:127], a[192:195], v[200:203], 0\n"
        "v_pk_mul_f16 v111, v111, v34 op_sel_hi:[1,0]\n"
        "v_pk_mul_f16 v110, v110, v134\n"
        "v_mfma_f32_16x16x32_bf16 v[124:127], a[196:199], v[168:171], v[124:127]\n"
        "v_pk_mul_f16 v111, v111, v135\n"
        "v_add_f32_dpp v254, v254, v254 row_bcast:31 row_mask:0xc bank_mask:0xf\n"
        "v_mfma_f32_16x16x32_bf16 v[124:127], a[200:203], v[176:179], v[124:127]\n"
        "v_exp_f16_sdwa v110, v110 dst_sel:WORD_0 dst_unused:UNUSED_PRESERVE src0_sel:WORD_0\n"
        "v_mfma_f32_16x16x32_bf16 v[104:107], a[212:215], v[176:179], 0\n"
        "v_exp_f16_sdwa v111, v111 dst_sel:WORD_0 dst_unused:UNUSED_PRESERVE src0_sel:WORD_0\n"
        "v_mfma_f32_16x16x32_bf16 v[104:107], a[208:211], v[168:171], v[104:107]\n"
        "v_readlane_b32 s95, v254, 63\n"
        "v_cvt_pk_f16_f32 v128, v120, v121\n"
        "v_mfma_f32_16x16x32_bf16 v[104:107], a[204:207], v[200:203], v[104:107]\n"
        "v_exp_f16_sdwa v110, v110 dst_sel:WORD_1 dst_unused:UNUSED_PRESERVE src0_sel:WORD_1\n"
        "v_mfma_f32_16x16x32_bf16 v[146:149], v[180:183], a[216:219], 0\n"
        "v_exp_f16_sdwa v111, v111 dst_sel:WORD_1 dst_unused:UNUSED_PRESERVE src0_sel:WORD_1\n"
        "v_mfma_f32_16x16x32_bf16 v[146:149], v[192:195], a[220:223], v[146:149]\n"
        "v_pk_add_f16 v140, v140, v110\n"
        "v_pk_add_f16 v140, v140, v111\n"
        "v_mfma_f32_16x16x32_bf16 v[146:149], v[184:187], a[224:227], v[146:149]\n"
        "v_rcp_f32 v254, s95\n"
        "v_mfma_f32_16x16x32_bf16 v[116:119], v[176:179], a[224:227], 0\n"
        "v_mul_f32 v252, v252, v254\n"
        "v_cvt_f16_f32 v252, v252\n"
        "v_mfma_f32_16x16x32_bf16 v[116:119], v[168:171], a[220:223], v[116:119]\n"
        "v_mul_f16 v252, v252, v253\n"
        "v_cvt_pk_f16_f32 v129, v122, v123\n"
        "v_mfma_f32_16x16x32_bf16 v[116:119], v[200:203], a[216:219], v[116:119]\n"
        "ds_write_b16 v22, v252 offset:512\n"
        "v_mfma_f32_16x16x32_bf16 v[120:123], v[196:199], a[216:219], 0\n"
        "v_pk_mul_f16 v128, v128, v34 op_sel_hi:[1,0]\n"
        "v_pk_mul_f16 v129, v129, v34 op_sel_hi:[1,0]\n"
        "v_mfma_f32_16x16x32_bf16 v[120:123], v[188:191], a[220:223], v[120:123]\n"
        "v_pk_mul_f16 v128, v128, v30\n"
        "v_pk_mul_f16 v129, v129, v31\n"
        "v_mfma_f32_16x16x32_bf16 v[120:123], v[172:175], a[224:227], v[120:123]\n"
        "v_exp_f16_sdwa v128, v128 dst_sel:WORD_0 dst_unused:UNUSED_PRESERVE src0_sel:WORD_0\n"
        "v_cvt_pk_bf16_f32 v150, v40, v41\n"
        "v_cvt_pk_bf16_f32 v151, v42, v43\n"
        "v_cvt_pk_bf16_f32 v152, v142, v143\n"
        "v_cvt_pk_bf16_f32 v153, v144, v145\n"
        "s_nop 1\n"
        "v_mfma_f32_16x16x32_bf16 v[142:145], v[180:183], v[150:153], 0\n"
        "v_cvt_pk_bf16_f32 v40, v136, v137\n"
        "v_cvt_pk_bf16_f32 v41, v138, v139\n"
        "v_cvt_pk_bf16_f32 v42, v112, v113\n"
        "v_cvt_pk_bf16_f32 v43, v114, v115\n"
        "s_nop 1\n"
        "v_mfma_f32_16x16x32_bf16 v[142:145], v[192:195], v[40:43], v[142:145]\n"
        "v_cvt_pk_bf16_f32 v136, v124, v125\n"
        "v_cvt_pk_bf16_f32 v137, v126, v127\n"
        "v_cvt_pk_bf16_f32 v138, v104, v105\n"
        "v_cvt_pk_bf16_f32 v139, v106, v107\n"
        "s_nop 1\n"
        "v_mfma_f32_16x16x32_bf16 v[142:145], v[184:187], v[136:139], v[142:145]\n"
        "v_exp_f16_sdwa v129, v129 dst_sel:WORD_0 dst_unused:UNUSED_PRESERVE src0_sel:WORD_0\n"
        "v_mfma_f32_16x16x32_bf16 v[104:107], v[176:179], v[136:139], 0\n"
        "v_exp_f16_sdwa v128, v128 dst_sel:WORD_1 dst_unused:UNUSED_PRESERVE src0_sel:WORD_1\n"
        "v_mfma_f32_16x16x32_bf16 v[104:107], v[168:171], v[40:43], v[104:107]\n"
        "v_exp_f16_sdwa v129, v129 dst_sel:WORD_1 dst_unused:UNUSED_PRESERVE src0_sel:WORD_1\n"
        "v_mfma_f32_16x16x32_bf16 v[104:107], v[200:203], v[150:153], v[104:107]\n"
        "v_pk_add_f16 v140, v140, v128\n"
        "v_pk_add_f16 v140, v140, v129\n"
        "v_mfma_f32_16x16x32_bf16 v[124:127], v[196:199], v[150:153], 0\n"
        "v_cvt_pk_f16_f32 v112, v146, v147\n"
        "v_cvt_pk_f16_f32 v113, v148, v149\n"
        "v_mfma_f32_16x16x32_bf16 v[124:127], v[188:191], v[40:43], v[124:127]\n"
        "v_cvt_pk_f16_f32 v114, v116, v117\n"
        "v_cvt_pk_f16_f32 v115, v118, v119\n"
        "v_mfma_f32_16x16x32_bf16 v[124:127], v[172:175], v[136:139], v[124:127]\n"
        "v_cvt_pk_f16_f32 v116, v120, v121\n"
        "v_cvt_pk_f16_f32 v117, v122, v123\n"
        "v_mfma_f32_16x16x32_bf16 v[40:43], a[144:147], v[196:199], 0\n"
        "v_cvt_pk_f16_f32 v120, v142, v143\n"
        "v_cvt_pk_f16_f32 v121, v144, v145\n"
        "v_mfma_f32_16x16x32_bf16 v[40:43], a[148:151], v[188:191], v[40:43]\n"
        "v_pk_mul_f16 v120, v120, v34 op_sel:[0,1]\n"
        "v_pk_mul_f16 v121, v121, v34 op_sel:[0,1]\n"
        "v_mfma_f32_16x16x32_bf16 v[40:43], a[152:155], v[172:175], v[40:43]\n"
        "v_pk_mul_f16 v120, v120, v132\n"
        "v_pk_mul_f16 v121, v121, v133\n"
        "v_mfma_f32_16x16x32_bf16 v[136:139], a[164:167], v[172:175], 0\n"
        "v_exp_f16_sdwa v120, v120 dst_sel:WORD_0 dst_unused:UNUSED_PRESERVE src0_sel:WORD_0\n"
        "v_mfma_f32_16x16x32_bf16 v[136:139], a[160:163], v[188:191], v[136:139]\n"
        "v_exp_f16_sdwa v121, v121 dst_sel:WORD_0 dst_unused:UNUSED_PRESERVE src0_sel:WORD_0\n"
        "v_mfma_f32_16x16x32_bf16 v[136:139], a[156:159], v[196:199], v[136:139]\n"
        "v_exp_f16_sdwa v120, v120 dst_sel:WORD_1 dst_unused:UNUSED_PRESERVE src0_sel:WORD_1\n"
        "v_mfma_f32_16x16x32_bf16 v[142:145], a[168:171], v[196:199], 0\n"
        "v_exp_f16_sdwa v121, v121 dst_sel:WORD_1 dst_unused:UNUSED_PRESERVE src0_sel:WORD_1\n"
        "v_mfma_f32_16x16x32_bf16 v[142:145], a[172:175], v[188:191], v[142:145]\n"
        "v_pk_add_f16 v141, v120, v121\n"
        "v_cvt_pk_f16_f32 v122, v104, v105\n"
        "v_mfma_f32_16x16x32_bf16 v[142:145], a[176:179], v[172:175], v[142:145]\n"
        "v_cvt_pk_f16_f32 v123, v106, v107\n"
        "v_pk_mul_f16 v122, v122, v34 op_sel:[0,1]\n"
        "v_mfma_f32_16x16x32_bf16 v[146:149], a[188:191], v[172:175], 0\n"
        "v_pk_mul_f16 v123, v123, v34 op_sel:[0,1]\n"
        "v_pk_mul_f16 v122, v122, v134\n"
        "v_mfma_f32_16x16x32_bf16 v[146:149], a[184:187], v[188:191], v[146:149]\n"
        "v_pk_mul_f16 v123, v123, v135\n"
        "v_cvt_pk_f16_f32 v208, v124, v125\n"
        "v_mfma_f32_16x16x32_bf16 v[146:149], a[180:183], v[196:199], v[146:149]\n"
        "v_exp_f16_sdwa v122, v122 dst_sel:WORD_0 dst_unused:UNUSED_PRESERVE src0_sel:WORD_0\n"
        "v_mfma_f32_16x16x32_bf16 v[150:153], a[192:195], v[196:199], 0\n"
        "v_exp_f16_sdwa v123, v123 dst_sel:WORD_0 dst_unused:UNUSED_PRESERVE src0_sel:WORD_0\n"
        "v_mfma_f32_16x16x32_bf16 v[150:153], a[196:199], v[188:191], v[150:153]\n"
        "v_exp_f16_sdwa v122, v122 dst_sel:WORD_1 dst_unused:UNUSED_PRESERVE src0_sel:WORD_1\n"
        "v_mfma_f32_16x16x32_bf16 v[150:153], a[200:203], v[172:175], v[150:153]\n"
        "v_exp_f16_sdwa v123, v123 dst_sel:WORD_1 dst_unused:UNUSED_PRESERVE src0_sel:WORD_1\n"
        "v_mfma_f32_16x16x32_bf16 v[158:161], a[212:215], v[172:175], 0\n"
        "v_pk_add_f16 v141, v141, v122\n"
        "v_pk_add_f16 v141, v141, v123\n"
        "v_mfma_f32_16x16x32_bf16 v[158:161], a[208:211], v[188:191], v[158:161]\n"
        "v_cvt_pk_f16_f32 v209, v126, v127\n"
        "v_pk_mul_f16 v208, v208, v34 op_sel:[0,1]\n"
        "v_mfma_f32_16x16x32_bf16 v[158:161], a[204:207], v[196:199], v[158:161]\n"
        "v_pk_mul_f16 v209, v209, v34 op_sel:[0,1]\n"
        "v_pk_mul_f16 v208, v208, v30\n"
        "v_mfma_f32_16x16x32_bf16 v[104:107], v[172:175], a[236:239], 0\n"
        "v_pk_mul_f16 v209, v209, v31\n"
        "v_cvt_pk_bf16_f32 v124, v40, v41\n"
        "v_mfma_f32_16x16x32_bf16 v[104:107], v[188:191], a[232:235], v[104:107]\n"
        "v_exp_f16_sdwa v208, v208 dst_sel:WORD_0 dst_unused:UNUSED_PRESERVE src0_sel:WORD_0\n"
        "v_mfma_f32_16x16x32_bf16 v[104:107], v[196:199], a[228:231], v[104:107]\n"
        "v_cvt_pk_bf16_f32 v125, v42, v43\n"
        "v_cvt_pk_bf16_f32 v126, v136, v137\n"
        "v_mfma_f32_16x16x32_bf16 v[40:43], v[200:203], a[228:231], 0\n"
        "v_cndmask_b32_e64 v130, 0, v255, s[62:63]\n"
        "v_cndmask_b32_e64 v210, 0, v255, s[64:65]\n"
        "v_mfma_f32_16x16x32_bf16 v[40:43], v[168:171], a[232:235], v[40:43]\n"
        "v_pk_add_f16 v140, v140, v130\n"
        "v_cvt_pk_bf16_f32 v127, v138, v139\n"
        "v_mfma_f32_16x16x32_bf16 v[40:43], v[176:179], a[236:239], v[40:43]\n"
        "v_cvt_pk_bf16_f32 v212, v142, v143\n"
        "v_cvt_pk_bf16_f32 v213, v144, v145\n"
        "v_mfma_f32_16x16x32_bf16 v[154:157], v[184:187], a[236:239], 0\n"
        "v_mov_b64 v[142:143], 0\n"
        "v_cvt_pk_bf16_f32 v214, v146, v147\n"
        "v_mfma_f32_16x16x32_bf16 v[154:157], v[192:195], a[232:235], v[154:157]\n"
        "v_cvt_pk_bf16_f32 v215, v148, v149\n"
        "v_cvt_pk_bf16_f32 v148, v150, v151\n"
        "v_mfma_f32_16x16x32_bf16 v[154:157], v[180:183], a[228:231], v[154:157]\n"
        "v_cvt_pk_bf16_f32 v149, v152, v153\n"
        "v_cvt_pk_bf16_f32 v150, v158, v159\n"
        "v_mfma_f32_16x16x32_bf16 v[136:139], v[180:183], v[124:127], 0\n"
        "v_cvt_pk_bf16_f32 v151, v160, v161\n"
        "v_cvt_pk_f16_f32 v164, v104, v105\n"
        "v_mfma_f32_16x16x32_bf16 v[136:139], v[192:195], v[212:215], v[136:139]\n"
        "v_exp_f16_sdwa v209, v209 dst_sel:WORD_0 dst_unused:UNUSED_PRESERVE src0_sel:WORD_0\n"
        "v_mfma_f32_16x16x32_bf16 v[136:139], v[184:187], v[148:151], v[136:139]\n"
        "v_exp_f16_sdwa v208, v208 dst_sel:WORD_1 dst_unused:UNUSED_PRESERVE src0_sel:WORD_1\n"
        "v_mfma_f32_16x16x32_bf16 v[144:147], v[176:179], v[148:151], 0\n"
        "v_exp_f16_sdwa v209, v209 dst_sel:WORD_1 dst_unused:UNUSED_PRESERVE src0_sel:WORD_1\n"
        "v_mfma_f32_16x16x32_bf16 v[144:147], v[168:171], v[212:215], v[144:147]\n"
        "v_pk_add_f16 v141, v141, v208\n"
        "v_pk_add_f16 v141, v141, v209\n"
        "v_mfma_f32_16x16x32_bf16 v[144:147], v[200:203], v[124:127], v[144:147]\n"
        "v_pk_add_f16 v141, v141, v210\n"
        "v_cvt_pk_f16_f32 v165, v106, v107\n"
        "v_mfma_f32_16x16x32_bf16 v[104:107], v[196:199], v[124:127], 0\n"
        "v_cvt_pk_f16_f32 v206, v40, v41\n"
        "v_cvt_pk_f16_f32 v207, v42, v43\n"
        "v_mfma_f32_16x16x32_bf16 v[104:107], v[188:191], v[212:215], v[104:107]\n"
        "v_cvt_pk_f16_f32 v204, v154, v155\n"
        "v_cvt_pk_f16_f32 v205, v156, v157\n"
        "v_mfma_f32_16x16x32_bf16 v[104:107], v[172:175], v[148:151], v[104:107]\n"
        "v_cvt_pk_f16_f32 v124, v136, v137\n"
        "v_cvt_pk_f16_f32 v125, v138, v139\n"
        "v_mfma_f32_16x16x32_bf16 v[150:153], v[180:183], a[240:243], 0\n"
        "v_pk_mul_f16 v124, v124, v35 op_sel_hi:[1,0]\n"
        "v_pk_mul_f16 v125, v125, v35 op_sel_hi:[1,0]\n"
        "v_mfma_f32_16x16x32_bf16 v[150:153], v[192:195], a[244:247], v[150:153]\n"
        "v_pk_mul_f16 v124, v124, v132\n"
        "v_pk_mul_f16 v125, v125, v133\n"
        "v_mfma_f32_16x16x32_bf16 v[150:153], v[184:187], a[248:251], v[150:153]\n"
        "v_exp_f16_sdwa v124, v124 dst_sel:WORD_0 dst_unused:UNUSED_PRESERVE src0_sel:WORD_0\n"
        "v_mfma_f32_16x16x32_bf16 v[216:219], v[176:179], a[248:251], 0\n"
        "v_exp_f16_sdwa v125, v125 dst_sel:WORD_0 dst_unused:UNUSED_PRESERVE src0_sel:WORD_0\n"
        "v_mfma_f32_16x16x32_bf16 v[216:219], v[168:171], a[244:247], v[216:219]\n"
        "v_exp_f16_sdwa v124, v124 dst_sel:WORD_1 dst_unused:UNUSED_PRESERVE src0_sel:WORD_1\n"
        "v_mfma_f32_16x16x32_bf16 v[216:219], v[200:203], a[240:243], v[216:219]\n"
        "v_exp_f16_sdwa v125, v125 dst_sel:WORD_1 dst_unused:UNUSED_PRESERVE src0_sel:WORD_1\n"
        "v_mfma_f32_16x16x32_bf16 v[220:223], v[196:199], a[240:243], 0\n"
        "v_cvt_pk_f16_f32 v126, v144, v145\n"
        "v_cvt_pk_f16_f32 v127, v146, v147\n"
        "v_mfma_f32_16x16x32_bf16 v[220:223], v[188:191], a[244:247], v[220:223]\n"
        "v_pk_mul_f16 v126, v126, v35 op_sel_hi:[1,0]\n"
        "v_pk_mul_f16 v127, v127, v35 op_sel_hi:[1,0]\n"
        "v_mfma_f32_16x16x32_bf16 v[220:223], v[172:175], a[248:251], v[220:223]\n"
        "v_pk_mul_f16 v126, v126, v134\n"
        "v_pk_mul_f16 v127, v127, v135\n"
        "v_mfma_f32_16x16x32_f16 v[160:163], v[68:71], v[140:143], 0\n"
        "v_exp_f16_sdwa v126, v126 dst_sel:WORD_0 dst_unused:UNUSED_PRESERVE src0_sel:WORD_0\n"
        "v_mfma_f32_16x16x32_bf16 v[232:235], v[172:175], v[48:51], 0\n"
        "v_exp_f16_sdwa v127, v127 dst_sel:WORD_0 dst_unused:UNUSED_PRESERVE src0_sel:WORD_0\n"
        "v_mfma_f32_16x16x32_bf16 v[232:235], v[188:191], v[100:103], v[232:235]\n"
        "v_exp_f16_sdwa v126, v126 dst_sel:WORD_1 dst_unused:UNUSED_PRESERVE src0_sel:WORD_1\n"
        "v_mfma_f32_16x16x32_bf16 v[232:235], v[196:199], a[252:255], v[232:235]\n"
        "v_exp_f16_sdwa v127, v127 dst_sel:WORD_1 dst_unused:UNUSED_PRESERVE src0_sel:WORD_1\n"
        "v_mfma_f32_16x16x32_bf16 v[240:243], v[200:203], a[252:255], 0\n"
        "v_cndmask_b32_e64 v138, 0, v255, s[66:67]\n"
        "v_cvt_pk_f16_f32 v136, v104, v105\n"
        "v_mfma_f32_16x16x32_bf16 v[240:243], v[168:171], v[100:103], v[240:243]\n"
        "ds_read2_b32 v[118:119], v24 offset0:0 offset1:0\n"
        "ds_read2_b32 v[166:167], v24 offset0:1 offset1:1\n"
        "v_mfma_f32_16x16x32_bf16 v[240:243], v[176:179], v[48:51], v[240:243]\n"
        "ds_read2_b32 v[158:159], v24 offset0:2 offset1:2\n"
        "ds_read2_b32 v[144:145], v24 offset0:3 offset1:3\n"
        "v_mfma_f32_16x16x32_bf16 v[40:43], v[184:187], v[48:51], 0\n"
        "ds_read2_b32 v[214:215], v24 offset0:4 offset1:4\n"
        "v_mov_b32 v131, 0\n"
        "v_mfma_f32_16x16x32_bf16 v[40:43], v[192:195], v[100:103], v[40:43]\n"
        "ds_read2_b32 v[162:163], v24 offset0:5 offset1:5\n"
        "v_cvt_pk_f16_f32 v137, v106, v107\n"
        "v_mfma_f32_16x16x32_bf16 v[40:43], v[180:183], a[252:255], v[40:43]\n"
        "v_pk_mul_f16 v136, v136, v35 op_sel_hi:[1,0]\n"
        "v_pk_mul_f16 v137, v137, v35 op_sel_hi:[1,0]\n"
        "v_mfma_f32_16x16x32_bf16 v[104:107], v[180:183], v[84:87], 0\n"
        "v_pk_mul_f16 v136, v136, v30\n"
        "v_pk_mul_f16 v137, v137, v31\n"
        "v_mfma_f32_16x16x32_bf16 v[104:107], v[192:195], v[96:99], v[104:107]\n"
        "v_exp_f16_sdwa v136, v136 dst_sel:WORD_0 dst_unused:UNUSED_PRESERVE src0_sel:WORD_0\n"
        "v_mfma_f32_16x16x32_bf16 v[104:107], v[184:187], v[76:79], v[104:107]\n"
        "v_exp_f16_sdwa v137, v137 dst_sel:WORD_0 dst_unused:UNUSED_PRESERVE src0_sel:WORD_0\n"
        "v_mfma_f32_16x16x32_bf16 v[236:239], v[176:179], v[76:79], 0\n"
        "v_exp_f16_sdwa v136, v136 dst_sel:WORD_1 dst_unused:UNUSED_PRESERVE src0_sel:WORD_1\n"
        "v_mfma_f32_16x16x32_bf16 v[236:239], v[168:171], v[96:99], v[236:239]\n"
        "v_exp_f16_sdwa v137, v137 dst_sel:WORD_1 dst_unused:UNUSED_PRESERVE src0_sel:WORD_1\n"
        "v_mfma_f32_16x16x32_bf16 v[236:239], v[200:203], v[84:87], v[236:239]\n"
        "v_cvt_pk_f16_f32 v148, v150, v151\n"
        "v_cvt_pk_f16_f32 v149, v152, v153\n"
        "v_mfma_f32_16x16x32_bf16 v[248:251], v[196:199], v[84:87], 0\n"
        "s_waitcnt lgkmcnt(0)\n"
        "v_lshlrev_b32 v152, 16, v118\n"
        "v_lshlrev_b32 v153, 16, v166\n"
        "v_mfma_f32_16x16x32_bf16 v[248:251], v[188:191], v[96:99], v[248:251]\n"
        "v_lshlrev_b32 v154, 16, v158\n"
        "v_lshlrev_b32 v155, 16, v144\n"
        "v_mfma_f32_16x16x32_bf16 v[248:251], v[172:175], v[76:79], v[248:251]\n"
        "v_lshlrev_b32 v146, 16, v214\n"
        "v_lshlrev_b32 v147, 16, v162\n"
        "v_mfma_f32_16x16x32_bf16 v[34:37], v[172:175], v[80:83], 0\n"
        "v_cvt_pk_f16_f32 v150, v216, v217\n"
        "v_cvt_pk_f16_f32 v151, v218, v219\n"
        "v_mfma_f32_16x16x32_bf16 v[34:37], v[188:191], v[92:95], v[34:37]\n"
        "v_pk_add_f16 v142, v124, v125\n"
        "v_pk_add_f16 v142, v142, v126\n"
        "v_mfma_f32_16x16x32_bf16 v[34:37], v[196:199], v[64:67], v[34:37]\n"
        "s_waitcnt vmcnt(1)\n"
        "v_pk_add_f16 v142, v142, v127\n"
        "v_pk_add_f16 v142, v142, v136\n"
        "v_mfma_f32_16x16x32_bf16 v[216:219], v[200:203], v[64:67], 0\n"
        "v_readfirstlane_b32 s50, v26\n"
        "s_lshl_b32 s92, s15, 3\n"
        "s_add_u32 s92, s92, s48\n"
        "v_mov_b32 v27, s49\n"
        "s_nop 0\n"
        "s_cmp_eq_u32 s50, s21\n"
        "s_cselect_b32 s92, s92, 0x10000\n"
        "v_mov_b32 v26, s92\n"
        "ds_write_b32 v27, v26 offset:8\n"
        "v_mfma_f32_16x16x32_bf16 v[216:219], v[168:171], v[92:95], v[216:219]\n"
        "v_cvt_pk_f16_f32 v156, v220, v221\n"
        "v_cvt_pk_f16_f32 v157, v222, v223\n"
        "v_mfma_f32_16x16x32_bf16 v[216:219], v[176:179], v[80:83], v[216:219]\n"
        "v_pk_add_f16 v142, v142, v137\n"
        "v_pk_add_f16 v142, v142, v138\n"
        "v_mfma_f32_16x16x32_bf16 v[224:227], v[184:187], v[80:83], 0\n"
        "v_mov_b32 v211, 0\n"
        "v_max3_f32 v47, s95, v160, v161\n"
        "v_mfma_f32_16x16x32_bf16 v[224:227], v[192:195], v[92:95], v[224:227]\n"
        "v_rcp_f32 v255, v160\n"
        "v_mfma_f32_16x16x32_bf16 v[224:227], v[180:183], v[64:67], v[224:227]\n"
        "v_cvt_f16_f32 v253, v255\n"
        "v_pk_mul_f16 v108, v108, v253 op_sel_hi:[1,0]\n"
        "v_mfma_f32_16x16x32_f16 v[244:247], v[68:71], v[140:143], 0\n"
        "v_pk_mul_f16 v109, v109, v253 op_sel_hi:[1,0]\n"
        "v_pk_mul_f16 v110, v110, v253 op_sel_hi:[1,0]\n"
        "v_pk_mul_f16 v108, v108, v132\n"
        "v_pk_mul_f16 v109, v109, v133\n"
        "v_pk_mul_f16 v110, v110, v134\n"
        "v_pk_mul_f16 v111, v111, v253 op_sel_hi:[1,0]\n"
        "v_pk_mul_f16 v111, v111, v135\n"
        "s_nop 1\n"
        "v_mfma_f32_16x16x32_f16 v[228:231], v[108:111], v[112:115], 0\n"
        "v_pk_mul_f16 v128, v128, v253 op_sel_hi:[1,0]\n"
        "v_pk_mul_f16 v129, v129, v253 op_sel_hi:[1,0]\n"
        "v_pk_mul_f16 v130, v130, v253 op_sel_hi:[1,0]\n"
        "v_pk_mul_f16 v128, v128, v30\n"
        "v_pk_mul_f16 v129, v129, v31\n"
        "s_nop 1\n"
        "v_mfma_f32_16x16x32_f16 v[228:231], v[128:131], v[116:119], v[228:231]\n"
        "v_rcp_f32 v253, v161\n"
        "v_mfma_f32_16x16x32_bf16 v[228:231], v[180:183], v[52:55], v[228:231]\n"
        "v_add_f32 v247, v254, v255\n"
        "v_cvt_f16_f32 v245, v253\n"
        "v_mfma_f32_16x16x32_bf16 v[220:223], v[180:183], v[56:59], 0\n"
        "v_add_f32 v247, v247, v253\n"
        "v_pk_mul_f16 v120, v120, v245 op_sel_hi:[1,0]\n"
        "v_mfma_f32_16x16x32_f16 v[220:223], v[128:131], v[164:167], v[220:223]\n"
        "v_pk_mul_f16 v121, v121, v245 op_sel_hi:[1,0]\n"
        "v_pk_mul_f16 v122, v122, v245 op_sel_hi:[1,0]\n"
        "v_mfma_f32_16x16x32_f16 v[220:223], v[108:111], v[204:207], v[220:223]\n"
        "v_pk_mul_f16 v123, v123, v245 op_sel_hi:[1,0]\n"
        "v_pk_mul_f16 v208, v208, v245 op_sel_hi:[1,0]\n"
        "v_mfma_f32_16x16x32_f16 v[252:255], v[108:111], v[148:151], 0\n"
        "v_pk_mul_f16 v209, v209, v245 op_sel_hi:[1,0]\n"
        "v_pk_mul_f16 v210, v210, v245 op_sel_hi:[1,0]\n"
        "v_mfma_f32_16x16x32_f16 v[252:255], v[128:131], v[156:159], v[252:255]\n"
        "v_cvt_pk_f16_f32 v142, v232, v233\n"
        "v_cvt_pk_f16_f32 v143, v234, v235\n"
        "v_mfma_f32_16x16x32_bf16 v[252:255], v[192:195], v[52:55], v[252:255]\n"
        "v_cvt_pk_f16_f32 v234, v240, v241\n"
        "v_cvt_pk_f16_f32 v235, v242, v243\n"
        "v_mfma_f32_16x16x32_bf16 v[240:243], v[192:195], v[56:59], 0\n"
        "v_cvt_pk_f16_f32 v232, v40, v41\n"
        "v_cvt_pk_f16_f32 v233, v42, v43\n"
        "v_mfma_f32_16x16x32_f16 v[240:243], v[128:131], v[142:145], v[240:243]\n"
        "v_cvt_pk_f16_f32 v40, v104, v105\n"
        "v_cvt_pk_f16_f32 v41, v106, v107\n"
        "v_mfma_f32_16x16x32_f16 v[240:243], v[108:111], v[232:235], v[240:243]\n"
        "v_cvt_pk_f16_f32 v42, v236, v237\n"
        "v_cvt_pk_f16_f32 v43, v238, v239\n"
        "s_nop 1\n"
        "v_mfma_f32_16x16x32_f16 v[104:107], v[108:111], v[40:43], 0\n"
        "v_cvt_pk_f16_f32 v212, v248, v249\n"
        "v_cvt_pk_f16_f32 v213, v250, v251\n"
        "s_nop 1\n"
        "v_mfma_f32_16x16x32_f16 v[104:107], v[128:131], v[212:215], v[104:107]\n"
        "v_pk_mul_f16 v120, v120, v132\n"
        "v_pk_mul_f16 v121, v121, v133\n"
        "v_mfma_f32_16x16x32_bf16 v[104:107], v[184:187], v[52:55], v[104:107]\n"
        "v_mov_b32 v139, 0\n"
        "v_cvt_pk_f16_f32 v160, v34, v35\n"
        "v_mfma_f32_16x16x32_bf16 v[248:251], v[184:187], v[56:59], 0\n"
        "v_cvt_pk_f16_f32 v161, v36, v37\n"
        "v_cvt_pk_f16_f32 v238, v216, v217\n"
        "s_nop 0\n"
        "v_mfma_f32_16x16x32_f16 v[248:251], v[128:131], v[160:163], v[248:251]\n"
        "v_cvt_pk_f16_f32 v239, v218, v219\n"
        "v_cvt_pk_f16_f32 v236, v224, v225\n"
        "v_cvt_pk_f16_f32 v237, v226, v227\n"
        "s_nop 1\n"
        "v_mfma_f32_16x16x32_f16 v[248:251], v[108:111], v[236:239], v[248:251]\n"
        "s_add_u32 s90, s89, 0\n"
        "v_pk_mul_f16 v122, v122, v134\n"
        "v_pk_mul_f16 v123, v123, v135\n"
        "s_nop 1\n"
        "v_mfma_f32_16x16x32_f16 v[216:219], v[120:123], v[112:115], 0\n"
        "v_pk_mul_f16 v208, v208, v30\n"
        "v_pk_mul_f16 v209, v209, v31\n"
        "s_nop 1\n"
        "v_mfma_f32_16x16x32_f16 v[216:219], v[208:211], v[116:119], v[216:219]\n"
        "v_rcp_f32 v245, v246\n"
        "v_mfma_f32_16x16x32_bf16 v[216:219], v[200:203], v[52:55], v[216:219]\n"
        "v_max_f32 v47, v47, v246\n"
        "v_add_f32 v247, v247, v245\n"
        "v_mfma_f32_16x16x32_bf16 v[34:37], v[200:203], v[56:59], 0\n"
        "v_fma_f32 v47, v247, s82, v47\n"
        "v_cmp_nlt_f32_e64 s[74:75], v47, s83\n"
        "v_mfma_f32_16x16x32_f16 v[34:37], v[208:211], v[164:167], v[34:37]\n"
        "v_cvt_pk_bf16_f32 v128, v228, v220\n"
        "v_cvt_pk_bf16_f32 v129, v252, v240\n"
        "v_mfma_f32_16x16x32_f16 v[34:37], v[120:123], v[204:207], v[34:37]\n"
        "s_cmp_lg_u64 s[74:75], 0\n"
        "s_cbranch_scc1 SLOW_%=\n"
        "v_cvt_pk_bf16_f32 v130, v104, v248\n"
        "v_mfma_f32_16x16x32_f16 v[184:187], v[120:123], v[148:151], 0\n"
        "s_add_u32 s91, s89, 98112\n"
        "v_cvt_pk_bf16_f32 v108, v229, v221\n"
        "v_cvt_pk_bf16_f32 v109, v253, v241\n"
        "v_mfma_f32_16x16x32_f16 v[184:187], v[208:211], v[156:159], v[184:187]\n"
        "v_cvt_pk_bf16_f32 v110, v105, v249\n"
        "v_cvt_f16_f32 v253, v245\n"
        "v_mfma_f32_16x16x32_bf16 v[184:187], v[168:171], v[52:55], v[184:187]\n"
        "buffer_store_dwordx3 v[128:130], v14, s[40:43], s90 offen sc1 nt\n"
        "v_cvt_pk_bf16_f32 v192, v230, v222\n"
        "v_cvt_pk_bf16_f32 v193, v254, v242\n"
        "v_cvt_pk_bf16_f32 v194, v106, v250\n"
        "v_pk_mul_f16 v124, v124, v253 op_sel_hi:[1,0]\n"
        "v_mfma_f32_16x16x32_bf16 v[180:183], v[168:171], v[56:59], 0\n"
        "ds_read_b128 v[128:131], v20 offset:512\n"
        "ds_read_b128 v[168:171], v20 offset:528\n"
        "v_mfma_f32_16x16x32_f16 v[180:183], v[208:211], v[142:145], v[180:183]\n"
        "v_cvt_pk_bf16_f32 v200, v231, v223\n"
        "v_cvt_pk_bf16_f32 v201, v255, v243\n"
        "v_mfma_f32_16x16x32_f16 v[180:183], v[120:123], v[232:235], v[180:183]\n"
        "buffer_store_dwordx3 v[108:110], v15, s[40:43], s90 offen offset:768 sc1 nt\n"
        "v_cvt_pk_bf16_f32 v202, v107, v251\n"
        "v_pk_mul_f16 v125, v125, v253 op_sel_hi:[1,0]\n"
        "v_pk_mul_f16 v126, v126, v253 op_sel_hi:[1,0]\n"
        "v_pk_mul_f16 v127, v127, v253 op_sel_hi:[1,0]\n"
        "v_mfma_f32_16x16x32_f16 v[108:111], v[120:123], v[40:43], 0\n"
        "v_pk_mul_f16 v136, v136, v253 op_sel_hi:[1,0]\n"
        "v_pk_mul_f16 v137, v137, v253 op_sel_hi:[1,0]\n"
        "v_mfma_f32_16x16x32_f16 v[108:111], v[208:211], v[212:215], v[108:111]\n"
        "v_pk_mul_f16 v138, v138, v253 op_sel_hi:[1,0]\n"
        "v_pk_mul_f16 v124, v124, v132\n"
        "v_mfma_f32_16x16x32_bf16 v[108:111], v[176:179], v[52:55], v[108:111]\n"
        "buffer_store_dwordx3 v[192:194], v14, s[40:43], s91 offen offset:192 sc1 nt\n"
        "v_pk_mul_f16 v125, v125, v133\n"
        "v_pk_mul_f16 v126, v126, v134\n"
        "v_pk_mul_f16 v127, v127, v135\n"
        "v_pk_mul_f16 v136, v136, v30\n"
        "v_mfma_f32_16x16x32_bf16 v[132:135], v[176:179], v[56:59], 0\n"
        "v_pk_mul_f16 v137, v137, v31\n"
        "v_cvt_pk_bf16_f32 v192, v216, v34\n"
        "v_mfma_f32_16x16x32_f16 v[132:135], v[208:211], v[160:163], v[132:135]\n"
        "s_waitcnt lgkmcnt(0)\n"
        "v_pk_mul_f16 v104, v128, v112\n"
        "v_pk_fma_f16 v104, v129, v113, v104\n"
        "v_mfma_f32_16x16x32_f16 v[132:135], v[120:123], v[236:239], v[132:135]\n"
        "buffer_store_dwordx3 v[200:202], v16, s[40:43], s91 offen offset:960 sc1 nt\n"
        "v_pk_fma_f16 v104, v130, v114, v104\n"
        "v_pk_fma_f16 v104, v131, v115, v104\n"
        "v_pk_fma_f16 v104, v168, v116, v104\n"
        "v_pk_fma_f16 v104, v169, v117, v104\n"
        "v_mfma_f32_16x16x32_f16 v[120:123], v[124:127], v[112:115], 0\n"
        "v_pk_fma_f16 v104, v170, v118, v104\n"
        "v_pk_mul_f16 v105, v128, v204\n"
        "v_mfma_f32_16x16x32_f16 v[120:123], v[136:139], v[116:119], v[120:123]\n"
        "s_add_u32 s90, s89, 196224\n"
        "v_pk_fma_f16 v105, v129, v205, v105\n"
        "v_pk_fma_f16 v105, v130, v206, v105\n"
        "v_mfma_f32_16x16x32_bf16 v[120:123], v[196:199], v[52:55], v[120:123]\n"
        "v_pk_fma_f16 v105, v131, v207, v105\n"
        "v_pk_fma_f16 v105, v168, v164, v105\n"
        "v_mfma_f32_16x16x32_bf16 v[176:179], v[196:199], v[56:59], 0\n"
        "v_pk_fma_f16 v105, v169, v165, v105\n"
        "v_pk_fma_f16 v105, v170, v166, v105\n"
        "v_mfma_f32_16x16x32_f16 v[176:179], v[136:139], v[164:167], v[176:179]\n"
        "v_pk_mul_f16 v106, v128, v148\n"
        "v_pk_fma_f16 v106, v129, v149, v106\n"
        "v_mfma_f32_16x16x32_f16 v[176:179], v[124:127], v[204:207], v[176:179]\n"
        "s_add_u32 s91, s89, 294336\n"
        "v_pk_fma_f16 v106, v130, v150, v106\n"
        "v_pk_fma_f16 v106, v131, v151, v106\n"
        "v_mfma_f32_16x16x32_f16 v[116:119], v[124:127], v[148:151], 0\n"
        "v_pk_fma_f16 v106, v168, v156, v106\n"
        "v_pk_fma_f16 v106, v169, v157, v106\n"
        "v_mfma_f32_16x16x32_f16 v[116:119], v[136:139], v[156:159], v[116:119]\n"
        "v_pk_fma_f16 v106, v170, v158, v106\n"
        "v_pk_mul_f16 v107, v128, v232\n"
        "v_mfma_f32_16x16x32_bf16 v[116:119], v[188:191], v[52:55], v[116:119]\n"
        "v_pk_fma_f16 v107, v129, v233, v107\n"
        "v_pk_fma_f16 v107, v130, v234, v107\n"
        "v_mfma_f32_16x16x32_bf16 v[112:115], v[188:191], v[56:59], 0\n"
        "v_pk_fma_f16 v107, v131, v235, v107\n"
        "v_pk_fma_f16 v107, v168, v142, v107\n"
        "v_mfma_f32_16x16x32_f16 v[112:115], v[136:139], v[142:145], v[112:115]\n"
        "v_pk_fma_f16 v107, v169, v143, v107\n"
        "v_cvt_pk_bf16_f32 v193, v184, v180\n"
        "v_mfma_f32_16x16x32_f16 v[112:115], v[124:127], v[232:235], v[112:115]\n"
        "v_cvt_pk_bf16_f32 v194, v108, v132\n"
        "v_pk_fma_f16 v107, v170, v144, v107\n"
        "v_mfma_f32_16x16x32_f16 v[140:143], v[124:127], v[40:43], 0\n"
        "v_cvt_pk_bf16_f32 v156, v217, v35\n"
        "v_cvt_pk_bf16_f32 v157, v185, v181\n"
        "v_mfma_f32_16x16x32_f16 v[140:143], v[136:139], v[212:215], v[140:143]\n"
        "v_cvt_pk_bf16_f32 v158, v109, v133\n"
        "v_pk_mul_f16 v108, v128, v40\n"
        "v_mfma_f32_16x16x32_bf16 v[140:143], v[172:175], v[52:55], v[140:143]\n"
        "buffer_store_dwordx3 v[192:194], v14, s[40:43], s90 offen offset:384 sc1 nt\n"
        "v_cvt_pk_bf16_f32 v34, v218, v36\n"
        "v_cvt_pk_bf16_f32 v35, v186, v182\n"
        "v_cvt_pk_bf16_f32 v36, v110, v134\n"
        "v_pk_fma_f16 v108, v129, v41, v108\n"
        "v_mfma_f32_16x16x32_bf16 v[148:151], v[172:175], v[56:59], 0\n"
        "v_cvt_pk_bf16_f32 v132, v219, v37\n"
        "v_cvt_pk_bf16_f32 v133, v187, v183\n"
        "v_mfma_f32_16x16x32_f16 v[148:151], v[136:139], v[160:163], v[148:151]\n"
        "v_cvt_pk_bf16_f32 v134, v111, v135\n"
        "v_pk_fma_f16 v108, v130, v42, v108\n"
        "v_mfma_f32_16x16x32_f16 v[148:151], v[124:127], v[236:239], v[148:151]\n"
        "buffer_store_dwordx3 v[156:158], v17, s[40:43], s90 offen offset:1152 sc1 nt\n"
        "s_add_u32 s90, s89, 392448\n"
        "v_pk_fma_f16 v108, v131, v43, v108\n"
        "v_pk_fma_f16 v108, v168, v212, v108\n"
        "v_pk_fma_f16 v108, v169, v213, v108\n"
        "v_mfma_f32_16x16x32_f16 v[152:155], v[68:71], v[104:107], v[152:155]\n"
        "buffer_store_dwordx3 v[34:36], v14, s[40:43], s91 offen offset:576 sc1 nt\n"
        "v_pk_fma_f16 v108, v170, v214, v108\n"
        "v_pk_mul_f16 v109, v128, v236\n"
        "v_pk_fma_f16 v109, v129, v237, v109\n"
        "v_pk_fma_f16 v109, v130, v238, v109\n"
        "v_pk_fma_f16 v109, v131, v239, v109\n"
        "v_pk_fma_f16 v109, v168, v160, v109\n"
        "v_pk_fma_f16 v109, v169, v161, v109\n"
        "v_pk_fma_f16 v109, v170, v162, v109\n"
        "s_nop 1\n"
        "v_mfma_f32_16x16x32_f16 v[144:147], v[68:71], v[106:109], v[144:147]\n"
        "buffer_store_dwordx3 v[132:134], v14, s[40:43], s90 offen sc1 nt\n"
        "v_cvt_pk_bf16_f32 v106, v120, v176\n"
        "v_cvt_pk_bf16_f32 v40, v121, v177\n"
        "v_cvt_pk_bf16_f32 v104, v122, v178\n"
        "v_cvt_pk_bf16_f32 v34, v123, v179\n"
        "v_cvt_pk_bf16_f32 v107, v116, v112\n"
        "v_cvt_pk_bf16_f32 v41, v117, v113\n"
        "v_cvt_pk_bf16_f32 v108, v140, v148\n"
        "buffer_store_dwordx3 v[106:108], v15, s[40:43], s90 offen offset:768 sc1 nt\n"
        "s_add_u32 s46, s89, 590016\n"
        "v_cvt_pk_bf16_f32 v105, v118, v114\n"
        "v_cvt_pk_bf16_f32 v35, v119, v115\n"
        "v_cvt_pk_bf16_f32 v42, v141, v149\n"
        "s_add_u32 s91, s89, 490560\n"
        "v_cvt_pk_bf16_f32 v106, v142, v150\n"
        "buffer_store_dwordx3 v[40:42], v14, s[40:43], s91 offen offset:192 sc1 nt\n"
        "s_min_u32 s46, s46, s42\n"
        "v_cvt_pk_bf16_f32 v36, v143, v151\n"
        "v_cvt_pk_bf16_f32 v40, v152, v153\n"
        "v_cvt_pk_bf16_f32 v41, v154, v155\n"
        "v_cvt_pk_bf16_f32 v42, v146, v147\n"
        "buffer_store_dwordx3 v[104:106], v16, s[40:43], s91 offen offset:960 sc1 nt\n"
        "s_add_u32 s90, s89, 588672\n"
        "buffer_store_dwordx3 v[34:36], v14, s[40:43], s90 offen offset:384 sc1 nt\n"
        "buffer_store_dwordx3 v[40:42], v17, s[44:47], s90 offen offset:1152 sc1 nt\n"
        "FIN_%=:\n"
        "s_waitcnt lgkmcnt(0)\n"
        "s_barrier\n"
        "v_mov_b32 v27, 163432\n"
        "ds_read_b32 v27, v27\n"
        "s_waitcnt lgkmcnt(0)\n"
        "v_readfirstlane_b32 s50, v27\n"
        "s_nop 3\n"
        "s_cmp_lt_u32 s50, 5329\n"
        "s_cbranch_scc1 POST_%=\n"
        ".Lswin_fin:\n"
        "s_endpgm\n"
        "POST_%=:\n"
        "s_barrier\n"
        "s_mul_i32 s87, s50, 1344\n"
        "s_mul_i32 s92, s50, 57457\n"
        "s_lshr_b32 s92, s92, 22\n"
        "s_mul_i32 s92, s92, 588672\n"
        "s_add_u32 s87, s87, s92\n"
        "s_lshr_b32 s23, s15, 2\n"
        "s_add_u32 s23, s23, 20\n"
        "s_sub_u32 s23, s23, s53\n"
        "s_mul_i32 s23, s23, 1584\n"
        "s_mov_b32 s22, 0\n"
        "s_mov_b32 s48, 0x10000\n"
        "s_mov_b32 s18, 0\n"
        "s_add_u32 m0, s52, 0\n"
        "s_nop 0\n"
        "buffer_load_dwordx4 v0, s[36:39], s87 offen lds\n"
        "buffer_load_dwordx4 v1, s[36:39], s87 offen offset:1024 lds\n"
        "buffer_load_dwordx4 v2, s[36:39], s87 offen offset:2048 lds\n"
        "buffer_load_dwordx4 v3, s[36:39], s87 offen offset:3072 lds\n"
        "s_add_u32 m0, s52, 4096\n"
        "s_nop 0\n"
        "buffer_load_dwordx4 v4, s[36:39], s87 offen lds\n"
        "buffer_load_dwordx4 v5, s[36:39], s87 offen offset:1024 lds\n"
        "buffer_load_dwordx4 v6, s[36:39], s87 offen offset:2048 lds\n"
        "buffer_load_dwordx4 v7, s[36:39], s87 offen offset:3072 lds\n"
        "s_add_u32 m0, s52, 8192\n"
        "s_nop 0\n"
        "buffer_load_dwordx4 v8, s[36:39], s87 offen lds\n"
        "s_waitcnt vmcnt(0)\n"
        "ds_read_b128 v[146:149], v9\n"
        "ds_read_b128 v[152:155], v10\n"
        "ds_read_b128 v[158:161], v11\n"
        "ds_read_b128 v[126:129], v9 offset:3072\n"
        "ds_read_b128 v[132:135], v10 offset:3072\n"
        "ds_read_b128 v[138:141], v11 offset:3072\n"
        "ds_read_b128 v[106:109], v9 offset:6144\n"
        "ds_read_b128 v[112:115], v10 offset:6144\n"
        "ds_read_b128 v[118:121], v11 offset:6144\n"
        "s_mov_b32 s86, 0x80000000\n"
        "s_mov_b32 s89, 0x80000000\n"
        "s_mov_b32 s85, 0\n"
        "s_sub_u32 s29, s29, 1\n"
        "s_waitcnt lgkmcnt(0)\n"
        "s_nop 7\n"
        "s_branch TOP_%=\n"
        "SLOW_%=:\n"
        "s_waitcnt lgkmcnt(0)\n"
        "s_nop 15\n"
        "v_mbcnt_lo_u32_b32 v255, -1, 0\n"
        "v_mbcnt_hi_u32_b32 v255, -1, v255\n"
        "v_and_b32 v254, 15, v255\n"
        "v_mul_u32_u24 v252, 24, v254\n"
        "v_sub_u32 v253, v24, v252\n"
        "v_lshrrev_b32 v250, 4, v255\n"
        "v_mul_u32_u24 v252, 96, v250\n"
        "v_add_u32 v253, v253, v252\n"
        "ds_read_b64 v[132:133], v253\n"
        "ds_read_b64 v[30:31], v253 offset:24\n"
        "ds_read_b64 v[34:35], v253 offset:48\n"
        "ds_read_b64 v[46:47], v253 offset:72\n"
        "ds_read_b64 v[40:41], v253 offset:8\n"
        "ds_read_b64 v[136:137], v253 offset:32\n"
        "ds_read_b64 v[104:105], v253 offset:56\n"
        "ds_read_b64 v[144:145], v253 offset:80\n"
        "ds_read_b64 v[124:125], v253 offset:16\n"
        "ds_read_b64 v[116:117], v253 offset:40\n"
        "ds_read_b64 v[108:109], v253 offset:64\n"
        "ds_read_b64 v[140:141], v253 offset:88\n"
        "v_mfma_f32_16x16x32_bf16 v[120:123], v[180:183], v[180:183], 0\n"
        "v_mov_b32 v253, 0x05040100\n"
        "v_subrev_u32 v248, 159328, v20\n"
        "v_mfma_f32_16x16x32_bf16 v[120:123], v[192:195], v[192:195], v[120:123]\n"
        "v_mov_b32 v249, 161888\n"
        "v_mov_b32 v204, 0x3f803f80\n"
        "v_mfma_f32_16x16x32_bf16 v[120:123], v[184:187], v[184:187], v[120:123]\n"
        "v_mov_b32 v205, 0x3f803f80\n"
        "v_mov_b32 v206, 0x3f803f80\n"
        "v_mov_b32 v207, 0x3f803f80\n"
        "s_nop 1\n"
        "v_mfma_f32_16x16x32_bf16 v[112:115], v[204:207], v[184:187], 0\n"
        "v_mov_b32 v255, 0xff800000\n"
        "v_mov_b32 v139, 0\n"
        "v_mfma_f32_16x16x32_bf16 v[112:115], v[204:207], v[192:195], v[112:115]\n"
        "s_waitcnt lgkmcnt(11)\n"
        "v_mov_b32 v43, 0\n"
        "v_mov_b32 v143, 0\n"
        "v_mfma_f32_16x16x32_bf16 v[112:115], v[204:207], v[180:183], v[112:115]\n"
        "s_waitcnt lgkmcnt(10)\n"
        "v_mov_b32 v119, 0\n"
        "v_mov_b32 v107, 0\n"
        "v_mfma_f32_16x16x32_bf16 v[128:131], v[200:203], v[200:203], 0\n"
        "s_waitcnt lgkmcnt(9)\n"
        "v_mov_b32 v37, 0\n"
        "v_mov_b32 v111, 0\n"
        "v_mfma_f32_16x16x32_bf16 v[128:131], v[168:171], v[168:171], v[128:131]\n"
        "s_waitcnt lgkmcnt(8)\n"
        "v_mov_b32 v147, 0\n"
        "v_mov_b32 v135, 0\n"
        "v_mfma_f32_16x16x32_bf16 v[128:131], v[176:179], v[176:179], v[128:131]\n"
        "s_waitcnt lgkmcnt(7)\n"
        "v_mov_b32 v127, 0\n"
        "v_and_b32 v248, 384, v248\n"
        "v_mfma_f32_16x16x32_bf16 v[162:165], v[204:207], v[176:179], 0\n"
        "s_waitcnt lgkmcnt(6)\n"
        "v_lshlrev_b32 v248, 1, v248\n"
        "v_add_u32 v248, 160864, v248\n"
        "v_mfma_f32_16x16x32_bf16 v[162:165], v[204:207], v[168:171], v[162:165]\n"
        "s_waitcnt lgkmcnt(5)\n"
        "v_lshlrev_b32 v251, 2, v254\n"
        "v_lshlrev_b32 v252, 4, v250\n"
        "v_mfma_f32_16x16x32_bf16 v[162:165], v[204:207], v[200:203], v[162:165]\n"
        "s_waitcnt lgkmcnt(4)\n"
        "v_add_u32 v251, v251, v248\n"
        "v_add_u32 v252, v252, v248\n"
        "v_mfma_f32_16x16x32_bf16 v[152:155], v[196:199], v[196:199], 0\n"
        "s_waitcnt lgkmcnt(3)\n"
        "v_cndmask_b32 v249, v249, v251, vcc\n"
        "v_cndmask_b32_e64 v254, v120, v121, s[56:57]\n"
        "v_mfma_f32_16x16x32_bf16 v[152:155], v[188:191], v[188:191], v[152:155]\n"
        "s_waitcnt lgkmcnt(2)\n"
        "v_cndmask_b32_e64 v254, v254, v122, s[58:59]\n"
        "v_cndmask_b32_e64 v254, v254, v123, s[60:61]\n"
        "v_mfma_f32_16x16x32_bf16 v[152:155], v[172:175], v[172:175], v[152:155]\n"
        "s_waitcnt lgkmcnt(1)\n"
        "v_mul_f32 v254, s80, v254\n"
        "v_mul_f32 v250, s80, v112\n"
        "v_mfma_f32_16x16x32_bf16 v[156:159], v[204:207], v[172:175], 0\n"
        "s_waitcnt lgkmcnt(0)\n"
        "v_fma_f32 v250, -v250, v250, v254\n"
        "v_max_f32 v250, 0, v250\n"
        "v_mfma_f32_16x16x32_bf16 v[156:159], v[204:207], v[188:191], v[156:159]\n"
        "v_add_f32 v250, s81, v250\n"
        "v_perm_b32 v120, v133, v132, v253\n"
        "v_mfma_f32_16x16x32_bf16 v[156:159], v[204:207], v[196:199], v[156:159]\n"
        "v_rsq_f32 v250, v250\n"
        "v_perm_b32 v121, v31, v30, v253\n"
        "v_perm_b32 v122, v35, v34, v253\n"
        "v_perm_b32 v123, v47, v46, v253\n"
        "s_nop 1\n"
        "v_mfma_f32_16x16x32_bf16 v[164:167], v[120:123], v[120:123], 0\n"
        "ds_write_b32 v249, v250\n"
        "v_perm_b32 v112, v41, v40, v253\n"
        "v_perm_b32 v113, v137, v136, v253\n"
        "v_perm_b32 v114, v105, v104, v253\n"
        "v_perm_b32 v115, v145, v144, v253\n"
        "s_nop 1\n"
        "v_mfma_f32_16x16x32_bf16 v[164:167], v[112:115], v[112:115], v[164:167]\n"
        "v_perm_b32 v148, v125, v124, v253\n"
        "v_perm_b32 v149, v117, v116, v253\n"
        "v_perm_b32 v150, v109, v108, v253\n"
        "v_perm_b32 v151, v141, v140, v253\n"
        "s_nop 1\n"
        "v_mfma_f32_16x16x32_bf16 v[164:167], v[148:151], v[148:151], v[164:167]\n"
        "v_cndmask_b32_e64 v254, v128, v129, s[56:57]\n"
        "v_cndmask_b32_e64 v254, v254, v130, s[58:59]\n"
        "v_mfma_f32_16x16x32_bf16 v[158:161], v[204:207], v[148:151], 0\n"
        "v_cndmask_b32_e64 v254, v254, v131, s[60:61]\n"
        "v_mul_f32 v254, s80, v254\n"
        "v_mfma_f32_16x16x32_bf16 v[158:161], v[204:207], v[112:115], v[158:161]\n"
        "v_mul_f32 v253, s80, v162\n"
        "v_fma_f32 v253, -v253, v253, v254\n"
        "v_mfma_f32_16x16x32_bf16 v[158:161], v[204:207], v[120:123], v[158:161]\n"
        "v_max_f32 v253, 0, v253\n"
        "v_add_f32 v253, s81, v253\n"
        "v_mfma_f32_16x16x32_bf16 v[130:133], v[180:183], a[216:219], 0\n"
        "v_rsq_f32 v253, v253\n"
        "v_mfma_f32_16x16x32_bf16 v[130:133], v[192:195], a[220:223], v[130:133]\n"
        "ds_write_b32 v249, v253 offset:64\n"
        "v_mfma_f32_16x16x32_bf16 v[130:133], v[184:187], a[224:227], v[130:133]\n"
        "v_cndmask_b32_e64 v254, v152, v153, s[56:57]\n"
        "v_cndmask_b32_e64 v254, v254, v154, s[58:59]\n"
        "v_mfma_f32_16x16x32_bf16 v[204:207], v[176:179], a[224:227], 0\n"
        "v_cndmask_b32_e64 v254, v254, v155, s[60:61]\n"
        "v_mul_f32 v254, s80, v254\n"
        "v_mfma_f32_16x16x32_bf16 v[204:207], v[168:171], a[220:223], v[204:207]\n"
        "v_mul_f32 v253, s80, v156\n"
        "v_fma_f32 v253, -v253, v253, v254\n"
        "v_mfma_f32_16x16x32_bf16 v[204:207], v[200:203], a[216:219], v[204:207]\n"
        "v_max_f32 v253, 0, v253\n"
        "v_add_f32 v253, s81, v253\n"
        "v_mfma_f32_16x16x32_bf16 v[160:163], v[196:199], a[216:219], 0\n"
        "v_rsq_f32 v253, v253\n"
        "v_mfma_f32_16x16x32_bf16 v[160:163], v[188:191], a[220:223], v[160:163]\n"
        "ds_write_b32 v249, v253 offset:128\n"
        "v_mfma_f32_16x16x32_bf16 v[160:163], v[172:175], a[224:227], v[160:163]\n"
        "v_cndmask_b32_e64 v253, v164, v165, s[56:57]\n"
        "v_cndmask_b32_e64 v253, v253, v166, s[58:59]\n"
        "v_mfma_f32_16x16x32_bf16 v[152:155], v[148:151], a[224:227], 0\n"
        "v_cndmask_b32_e64 v253, v253, v167, s[60:61]\n"
        "v_mul_f32 v253, s80, v253\n"
        "v_mfma_f32_16x16x32_bf16 v[152:155], v[112:115], a[220:223], v[152:155]\n"
        "v_mul_f32 v254, s80, v158\n"
        "v_fma_f32 v254, -v254, v254, v253\n"
        "v_mfma_f32_16x16x32_bf16 v[152:155], v[120:123], a[216:219], v[152:155]\n"
        "v_max_f32 v254, 0, v254\n"
        "v_add_f32 v254, s81, v254\n"
        "v_mfma_f32_16x16x32_bf16 v[156:159], v[196:199], a[228:231], 0\n"
        "v_rsq_f32 v254, v254\n"
        "v_mfma_f32_16x16x32_bf16 v[156:159], v[188:191], a[232:235], v[156:159]\n"
        "ds_write_b32 v249, v254 offset:192\n"
        "v_mfma_f32_16x16x32_bf16 v[156:159], v[172:175], a[236:239], v[156:159]\n"
        "v_cvt_pk_bf16_f32 v128, v130, v131\n"
        "v_cvt_pk_bf16_f32 v129, v132, v133\n"
        "v_mfma_f32_16x16x32_bf16 v[164:167], v[176:179], a[236:239], 0\n"
        "v_cvt_pk_bf16_f32 v130, v204, v205\n"
        "v_cvt_pk_bf16_f32 v131, v206, v207\n"
        "v_mfma_f32_16x16x32_bf16 v[164:167], v[168:171], a[232:235], v[164:167]\n"
        "v_cvt_pk_bf16_f32 v136, v160, v161\n"
        "v_cvt_pk_bf16_f32 v137, v162, v163\n"
        "v_mfma_f32_16x16x32_bf16 v[164:167], v[200:203], a[228:231], v[164:167]\n"
        "s_nop 0\n"
        "v_cvt_pk_bf16_f32 v138, v152, 0\n"
        "v_cvt_pk_bf16_f32 v40, v156, v157\n"
        "v_mfma_f32_16x16x32_bf16 v[160:163], v[180:183], a[228:231], 0\n"
        "s_nop 0\n"
        "v_cvt_pk_bf16_f32 v41, v158, v159\n"
        "v_mfma_f32_16x16x32_bf16 v[160:163], v[192:195], a[232:235], v[160:163]\n"
        "s_nop 0\n"
        "v_cvt_pk_bf16_f32 v154, v164, v165\n"
        "v_cvt_pk_bf16_f32 v155, v166, v167\n"
        "v_mfma_f32_16x16x32_bf16 v[160:163], v[184:187], a[236:239], v[160:163]\n"
        "s_nop 1\n"
        "v_mfma_f32_16x16x32_bf16 v[156:159], v[148:151], a[236:239], 0\n"
        "s_nop 2\n"
        "v_mfma_f32_16x16x32_bf16 v[156:159], v[112:115], a[232:235], v[156:159]\n"
        "s_nop 0\n"
        "v_cvt_pk_bf16_f32 v152, v160, v161\n"
        "v_cvt_pk_bf16_f32 v153, v162, v163\n"
        "v_mfma_f32_16x16x32_bf16 v[156:159], v[120:123], a[228:231], v[156:159]\n"
        "s_nop 1\n"
        "v_mfma_f32_16x16x32_bf16 v[162:165], v[180:183], a[240:243], 0\n"
        "s_nop 2\n"
        "v_mfma_f32_16x16x32_bf16 v[162:165], v[192:195], a[244:247], v[162:165]\n"
        "s_nop 0\n"
        "v_cvt_pk_bf16_f32 v42, v156, 0\n"
        "v_mfma_f32_16x16x32_bf16 v[162:165], v[184:187], a[248:251], v[162:165]\n"
        "s_nop 1\n"
        "v_mfma_f32_16x16x32_bf16 v[204:207], v[176:179], a[248:251], 0\n"
        "s_nop 2\n"
        "v_mfma_f32_16x16x32_bf16 v[204:207], v[168:171], a[244:247], v[204:207]\n"
        "s_nop 0\n"
        "v_cvt_pk_bf16_f32 v160, v162, v163\n"
        "v_cvt_pk_bf16_f32 v161, v164, v165\n"
        "v_mfma_f32_16x16x32_bf16 v[204:207], v[200:203], a[240:243], v[204:207]\n"
        "s_nop 1\n"
        "v_mfma_f32_16x16x32_bf16 v[156:159], v[196:199], a[240:243], 0\n"
        "s_nop 2\n"
        "v_mfma_f32_16x16x32_bf16 v[156:159], v[188:191], a[244:247], v[156:159]\n"
        "s_nop 0\n"
        "v_cvt_pk_bf16_f32 v162, v204, v205\n"
        "v_cvt_pk_bf16_f32 v163, v206, v207\n"
        "v_mfma_f32_16x16x32_bf16 v[156:159], v[172:175], a[248:251], v[156:159]\n"
        "s_nop 1\n"
        "v_mfma_f32_16x16x32_bf16 v[164:167], v[148:151], a[248:251], 0\n"
        "s_nop 2\n"
        "v_mfma_f32_16x16x32_bf16 v[164:167], v[112:115], a[244:247], v[164:167]\n"
        "s_nop 0\n"
        "v_cvt_pk_bf16_f32 v140, v156, v157\n"
        "v_cvt_pk_bf16_f32 v141, v158, v159\n"
        "v_mfma_f32_16x16x32_bf16 v[164:167], v[120:123], a[240:243], v[164:167]\n"
        "s_nop 1\n"
        "v_mfma_f32_16x16x32_bf16 v[156:159], v[196:199], a[252:255], 0\n"
        "s_nop 2\n"
        "v_mfma_f32_16x16x32_bf16 v[156:159], v[188:191], v[100:103], v[156:159]\n"
        "s_nop 0\n"
        "v_cvt_pk_bf16_f32 v142, v164, 0\n"
        "v_mfma_f32_16x16x32_bf16 v[156:159], v[172:175], v[48:51], v[156:159]\n"
        "s_nop 1\n"
        "v_mfma_f32_16x16x32_bf16 v[164:167], v[176:179], v[48:51], 0\n"
        "s_nop 2\n"
        "v_mfma_f32_16x16x32_bf16 v[164:167], v[168:171], v[100:103], v[164:167]\n"
        "s_nop 0\n"
        "v_cvt_pk_bf16_f32 v116, v156, v157\n"
        "v_cvt_pk_bf16_f32 v117, v158, v159\n"
        "v_mfma_f32_16x16x32_bf16 v[164:167], v[200:203], a[252:255], v[164:167]\n"
        "s_nop 1\n"
        "v_mfma_f32_16x16x32_bf16 v[204:207], v[180:183], a[252:255], 0\n"
        "s_nop 2\n"
        "v_mfma_f32_16x16x32_bf16 v[204:207], v[192:195], v[100:103], v[204:207]\n"
        "s_nop 0\n"
        "v_cvt_pk_bf16_f32 v158, v164, v165\n"
        "v_cvt_pk_bf16_f32 v159, v166, v167\n"
        "v_mfma_f32_16x16x32_bf16 v[204:207], v[184:187], v[48:51], v[204:207]\n"
        "s_nop 1\n"
        "v_mfma_f32_16x16x32_bf16 v[164:167], v[148:151], v[48:51], 0\n"
        "s_nop 2\n"
        "v_mfma_f32_16x16x32_bf16 v[164:167], v[112:115], v[100:103], v[164:167]\n"
        "s_nop 0\n"
        "v_cvt_pk_bf16_f32 v156, v204, v205\n"
        "v_cvt_pk_bf16_f32 v157, v206, v207\n"
        "v_mfma_f32_16x16x32_bf16 v[164:167], v[120:123], a[252:255], v[164:167]\n"
        "s_nop 1\n"
        "v_mfma_f32_16x16x32_bf16 v[204:207], v[180:183], v[84:87], 0\n"
        "s_nop 2\n"
        "v_mfma_f32_16x16x32_bf16 v[204:207], v[192:195], v[96:99], v[204:207]\n"
        "s_nop 0\n"
        "v_cvt_pk_bf16_f32 v118, v164, 0\n"
        "v_mfma_f32_16x16x32_bf16 v[204:207], v[184:187], v[76:79], v[204:207]\n"
        "s_nop 1\n"
        "v_mfma_f32_16x16x32_bf16 v[212:215], v[176:179], v[76:79], 0\n"
        "s_nop 2\n"
        "v_mfma_f32_16x16x32_bf16 v[212:215], v[168:171], v[96:99], v[212:215]\n"
        "s_nop 0\n"
        "v_cvt_pk_bf16_f32 v164, v204, v205\n"
        "v_cvt_pk_bf16_f32 v165, v206, v207\n"
        "v_mfma_f32_16x16x32_bf16 v[212:215], v[200:203], v[84:87], v[212:215]\n"
        "s_nop 1\n"
        "v_mfma_f32_16x16x32_bf16 v[208:211], v[196:199], v[84:87], 0\n"
        "s_nop 2\n"
        "v_mfma_f32_16x16x32_bf16 v[208:211], v[188:191], v[96:99], v[208:211]\n"
        "s_nop 0\n"
        "v_cvt_pk_bf16_f32 v166, v212, v213\n"
        "v_cvt_pk_bf16_f32 v167, v214, v215\n"
        "v_mfma_f32_16x16x32_bf16 v[208:211], v[172:175], v[76:79], v[208:211]\n"
        "s_nop 1\n"
        "v_mfma_f32_16x16x32_bf16 v[204:207], v[148:151], v[76:79], 0\n"
        "s_nop 2\n"
        "v_mfma_f32_16x16x32_bf16 v[204:207], v[112:115], v[96:99], v[204:207]\n"
        "s_nop 0\n"
        "v_cvt_pk_bf16_f32 v104, v208, v209\n"
        "v_cvt_pk_bf16_f32 v105, v210, v211\n"
        "v_mfma_f32_16x16x32_bf16 v[204:207], v[120:123], v[84:87], v[204:207]\n"
        "s_nop 1\n"
        "v_mfma_f32_16x16x32_bf16 v[208:211], v[196:199], v[64:67], 0\n"
        "s_nop 2\n"
        "v_mfma_f32_16x16x32_bf16 v[208:211], v[188:191], v[92:95], v[208:211]\n"
        "s_nop 0\n"
        "v_cvt_pk_bf16_f32 v106, v204, 0\n"
        "v_mfma_f32_16x16x32_bf16 v[208:211], v[172:175], v[80:83], v[208:211]\n"
        "v_mfma_f32_16x16x32_bf16 v[228:231], v[176:179], v[80:83], 0\n"
        "v_mfma_f32_16x16x32_bf16 v[228:231], v[168:171], v[92:95], v[228:231]\n"
        "ds_read_b128 v[204:207], v252\n"
        "ds_read_b128 v[212:215], v252 offset:64\n"
        "v_mfma_f32_16x16x32_bf16 v[228:231], v[200:203], v[64:67], v[228:231]\n"
        "ds_read_b128 v[216:219], v252 offset:128\n"
        "ds_read_b32 v250, v251\n"
        "v_mfma_f32_16x16x32_bf16 v[224:227], v[180:183], v[64:67], 0\n"
        "ds_read_b32 v252, v251 offset:64\n"
        "ds_read_b32 v254, v251 offset:128\n"
        "v_mfma_f32_16x16x32_bf16 v[224:227], v[192:195], v[92:95], v[224:227]\n"
        "ds_read_b32 v253, v251 offset:192\n"
        "v_cvt_pk_bf16_f32 v34, v208, v209\n"
        "v_mfma_f32_16x16x32_bf16 v[224:227], v[184:187], v[80:83], v[224:227]\n"
        "s_nop 0\n"
        "v_cvt_pk_bf16_f32 v35, v210, v211\n"
        "v_cvt_pk_bf16_f32 v210, v228, v229\n"
        "v_mfma_f32_16x16x32_bf16 v[220:223], v[148:151], v[80:83], 0\n"
        "s_nop 0\n"
        "v_cvt_pk_bf16_f32 v211, v230, v231\n"
        "v_mfma_f32_16x16x32_bf16 v[220:223], v[112:115], v[92:95], v[220:223]\n"
        "s_nop 0\n"
        "v_cvt_pk_bf16_f32 v208, v224, v225\n"
        "v_cvt_pk_bf16_f32 v209, v226, v227\n"
        "v_mfma_f32_16x16x32_bf16 v[220:223], v[120:123], v[64:67], v[220:223]\n"
        "s_nop 1\n"
        "v_mfma_f32_16x16x32_bf16 v[224:227], a[144:147], v[180:183], 0\n"
        "s_nop 2\n"
        "v_mfma_f32_16x16x32_bf16 v[224:227], a[148:151], v[192:195], v[224:227]\n"
        "s_nop 0\n"
        "v_cvt_pk_bf16_f32 v36, v220, 0\n"
        "v_mfma_f32_16x16x32_bf16 v[224:227], a[152:155], v[184:187], v[224:227]\n"
        "s_nop 1\n"
        "v_mfma_f32_16x16x32_bf16 v[220:223], a[164:167], v[184:187], 0\n"
        "s_waitcnt lgkmcnt(0)\n"
        "s_nop 1\n"
        "v_mfma_f32_16x16x32_bf16 v[220:223], a[160:163], v[192:195], v[220:223]\n"
        "s_nop 0\n"
        "v_cvt_pk_bf16_f32 v236, v224, v225\n"
        "v_cvt_pk_bf16_f32 v237, v226, v227\n"
        "v_mfma_f32_16x16x32_bf16 v[220:223], a[156:159], v[180:183], v[220:223]\n"
        "s_nop 1\n"
        "v_mfma_f32_16x16x32_bf16 v[224:227], a[168:171], v[180:183], 0\n"
        "s_nop 2\n"
        "v_mfma_f32_16x16x32_bf16 v[224:227], a[172:175], v[192:195], v[224:227]\n"
        "s_nop 0\n"
        "v_cvt_pk_bf16_f32 v238, v220, v221\n"
        "v_cvt_pk_bf16_f32 v239, v222, v223\n"
        "v_mfma_f32_16x16x32_bf16 v[224:227], a[176:179], v[184:187], v[224:227]\n"
        "s_nop 1\n"
        "v_mfma_f32_16x16x32_bf16 v[220:223], a[188:191], v[184:187], 0\n"
        "s_nop 2\n"
        "v_mfma_f32_16x16x32_bf16 v[220:223], a[184:187], v[192:195], v[220:223]\n"
        "s_nop 0\n"
        "v_cvt_pk_bf16_f32 v232, v224, v225\n"
        "v_cvt_pk_bf16_f32 v233, v226, v227\n"
        "v_mfma_f32_16x16x32_bf16 v[220:223], a[180:183], v[180:183], v[220:223]\n"
        "s_nop 1\n"
        "v_mfma_f32_16x16x32_bf16 v[224:227], a[192:195], v[180:183], 0\n"
        "s_nop 2\n"
        "v_mfma_f32_16x16x32_bf16 v[224:227], a[196:199], v[192:195], v[224:227]\n"
        "s_nop 0\n"
        "v_cvt_pk_bf16_f32 v234, v220, v221\n"
        "v_cvt_pk_bf16_f32 v235, v222, v223\n"
        "v_mfma_f32_16x16x32_bf16 v[224:227], a[200:203], v[184:187], v[224:227]\n"
        "s_nop 1\n"
        "v_mfma_f32_16x16x32_bf16 v[228:231], a[212:215], v[184:187], 0\n"
        "s_nop 2\n"
        "v_mfma_f32_16x16x32_bf16 v[228:231], a[208:211], v[192:195], v[228:231]\n"
        "s_nop 0\n"
        "v_cvt_pk_bf16_f32 v220, v224, v225\n"
        "v_cvt_pk_bf16_f32 v221, v226, v227\n"
        "v_mfma_f32_16x16x32_bf16 v[228:231], a[204:207], v[180:183], v[228:231]\n"
        "s_nop 1\n"
        "v_mfma_f32_16x16x32_bf16 v[224:227], v[180:183], v[236:239], 0\n"
        "s_nop 2\n"
        "v_mfma_f32_16x16x32_bf16 v[224:227], v[192:195], v[232:235], v[224:227]\n"
        "s_nop 0\n"
        "v_cvt_pk_bf16_f32 v222, v228, v229\n"
        "v_cvt_pk_bf16_f32 v223, v230, v231\n"
        "s_nop 1\n"
        "v_mfma_f32_16x16x32_bf16 v[224:227], v[184:187], v[220:223], v[224:227]\n"
        "s_nop 1\n"
        "v_mfma_f32_16x16x32_bf16 v[228:231], v[176:179], v[220:223], 0\n"
        "s_nop 2\n"
        "v_mfma_f32_16x16x32_bf16 v[228:231], v[168:171], v[232:235], v[228:231]\n"
        "s_nop 0\n"
        "v_mul_f32 v224, v224, v204\n"
        "v_mul_f32 v225, v225, v205\n"
        "v_mfma_f32_16x16x32_bf16 v[228:231], v[200:203], v[236:239], v[228:231]\n"
        "s_nop 0\n"
        "v_mul_f32 v226, v226, v206\n"
        "v_mul_f32 v227, v227, v207\n"
        "v_mfma_f32_16x16x32_bf16 v[240:243], v[196:199], v[236:239], 0\n"
        "s_nop 0\n"
        "v_max3_f32 v251, v224, v225, v226\n"
        "v_mfma_f32_16x16x32_bf16 v[240:243], v[188:191], v[232:235], v[240:243]\n"
        "s_nop 0\n"
        "v_mul_f32 v228, v228, v212\n"
        "v_mul_f32 v229, v229, v213\n"
        "v_mfma_f32_16x16x32_bf16 v[240:243], v[172:175], v[220:223], v[240:243]\n"
        "v_mul_f32 v230, v230, v214\n"
        "v_mul_f32 v231, v231, v215\n"
        "v_mfma_f32_16x16x32_bf16 v[244:247], v[148:151], v[220:223], 0\n"
        "s_nop 0\n"
        "v_max3_f32 v251, v251, v227, v228\n"
        "v_max3_f32 v251, v251, v229, v230\n"
        "v_mfma_f32_16x16x32_bf16 v[244:247], v[112:115], v[232:235], v[244:247]\n"
        "s_nop 0\n"
        "v_mul_f32 v240, v240, v216\n"
        "v_mul_f32 v241, v241, v217\n"
        "v_mfma_f32_16x16x32_bf16 v[244:247], v[120:123], v[236:239], v[244:247]\n"
        "s_nop 0\n"
        "v_mul_f32 v242, v242, v218\n"
        "v_mul_f32 v243, v243, v219\n"
        "v_max3_f32 v251, v251, v231, v240\n"
        "v_max3_f32 v251, v251, v241, v242\n"
        "s_nop 2\n"
        "v_mul_f32 v244, v244, v253\n"
        "v_cndmask_b32_e64 v244, v255, v244, s[62:63]\n"
        "v_max3_f32 v251, v251, v243, v244\n"
        "v_mov_b32 v249, v251\n"
        "s_nop 1\n"
        "v_permlane32_swap_b32 v251, v249\n"
        "v_max_f32 v251, v251, v249\n"
        "v_mov_b32 v249, v251\n"
        "s_nop 1\n"
        "v_permlane16_swap_b32 v251, v249\n"
        "v_max_f32 v251, v251, v249\n"
        "v_mul_f32 v251, v251, v250\n"
        "v_fma_f32 v224, v224, v250, -v251\n"
        "v_exp_f32 v224, v224\n"
        "v_fma_f32 v225, v225, v250, -v251\n"
        "v_exp_f32 v225, v225\n"
        "s_nop 0\n"
        "v_add_f32 v249, v224, v225\n"
        "v_mul_f32 v224, v224, v204\n"
        "v_fma_f32 v244, v244, v250, -v251\n"
        "v_exp_f32 v244, v244\n"
        "v_fma_f32 v243, v243, v250, -v251\n"
        "v_exp_f32 v243, v243\n"
        "v_fma_f32 v242, v242, v250, -v251\n"
        "v_exp_f32 v242, v242\n"
        "v_fma_f32 v241, v241, v250, -v251\n"
        "v_exp_f32 v241, v241\n"
        "v_fma_f32 v240, v240, v250, -v251\n"
        "v_exp_f32 v240, v240\n"
        "v_fma_f32 v231, v231, v250, -v251\n"
        "v_exp_f32 v231, v231\n"
        "v_fma_f32 v230, v230, v250, -v251\n"
        "v_exp_f32 v230, v230\n"
        "v_fma_f32 v229, v229, v250, -v251\n"
        "v_exp_f32 v229, v229\n"
        "v_fma_f32 v228, v228, v250, -v251\n"
        "v_exp_f32 v228, v228\n"
        "v_fma_f32 v227, v227, v250, -v251\n"
        "v_exp_f32 v227, v227\n"
        "v_fma_f32 v226, v226, v250, -v251\n"
        "v_exp_f32 v226, v226\n"
        "s_nop 0\n"
        "v_add_f32 v249, v249, v226\n"
        "v_add_f32 v249, v249, v227\n"
        "v_add_f32 v249, v249, v228\n"
        "v_add_f32 v249, v249, v229\n"
        "v_add_f32 v249, v249, v230\n"
        "v_add_f32 v249, v249, v231\n"
        "v_add_f32 v249, v249, v240\n"
        "v_add_f32 v249, v249, v241\n"
        "v_add_f32 v249, v249, v242\n"
        "v_add_f32 v249, v249, v243\n"
        "v_add_f32 v249, v249, v244\n"
        "v_mov_b32 v251, v249\n"
        "s_nop 1\n"
        "v_permlane32_swap_b32 v249, v251\n"
        "v_add_f32 v249, v249, v251\n"
        "v_mov_b32 v251, v249\n"
        "s_nop 1\n"
        "v_permlane16_swap_b32 v249, v251\n"
        "v_add_f32 v249, v249, v251\n"
        "v_rcp_f32 v249, v249\n"
        "s_nop 0\n"
        "v_mul_f32 v224, v224, v249\n"
        "v_mul_f32 v225, v225, v205\n"
        "v_mul_f32 v225, v225, v249\n"
        "v_cvt_pk_bf16_f32 v232, v224, v225\n"
        "v_mul_f32 v226, v226, v206\n"
        "v_mul_f32 v226, v226, v249\n"
        "v_mul_f32 v227, v227, v207\n"
        "v_mul_f32 v227, v227, v249\n"
        "v_cvt_pk_bf16_f32 v233, v226, v227\n"
        "v_mul_f32 v228, v228, v212\n"
        "v_mul_f32 v228, v228, v249\n"
        "v_mul_f32 v229, v229, v213\n"
        "v_mul_f32 v229, v229, v249\n"
        "v_cvt_pk_bf16_f32 v234, v228, v229\n"
        "v_mul_f32 v230, v230, v214\n"
        "v_mul_f32 v230, v230, v249\n"
        "v_mul_f32 v231, v231, v215\n"
        "v_mul_f32 v231, v231, v249\n"
        "v_cvt_pk_bf16_f32 v235, v230, v231\n"
        "s_nop 1\n"
        "v_mfma_f32_16x16x32_bf16 v[220:223], v[232:235], v[128:131], 0\n"
        "v_mul_f32 v240, v240, v216\n"
        "v_mul_f32 v241, v241, v217\n"
        "v_mul_f32 v240, v240, v249\n"
        "v_mul_f32 v241, v241, v249\n"
        "v_cvt_pk_bf16_f32 v108, v240, v241\n"
        "v_mul_f32 v242, v242, v218\n"
        "v_mul_f32 v242, v242, v249\n"
        "v_mul_f32 v243, v243, v219\n"
        "v_mul_f32 v243, v243, v249\n"
        "v_cvt_pk_bf16_f32 v109, v242, v243\n"
        "v_mul_f32 v244, v244, v253\n"
        "v_mul_f32 v244, v244, v249\n"
        "v_cvt_pk_bf16_f32 v110, v244, 0\n"
        "s_nop 1\n"
        "v_mfma_f32_16x16x32_bf16 v[220:223], v[108:111], v[136:139], v[220:223]\n"
        "v_mfma_f32_16x16x32_bf16 v[220:223], v[180:183], v[52:55], v[220:223]\n"
        "v_mfma_f32_16x16x32_bf16 v[224:227], v[180:183], v[56:59], 0\n"
        "v_mfma_f32_16x16x32_bf16 v[224:227], v[108:111], v[40:43], v[224:227]\n"
        "v_mfma_f32_16x16x32_bf16 v[224:227], v[232:235], v[152:155], v[224:227]\n"
        "s_add_u32 s90, s89, 0\n"
        "s_nop 0\n"
        "v_mfma_f32_16x16x32_bf16 v[228:231], v[232:235], v[160:163], 0\n"
        "s_nop 2\n"
        "v_mfma_f32_16x16x32_bf16 v[228:231], v[108:111], v[140:143], v[228:231]\n"
        "s_nop 0\n"
        "v_cvt_pk_bf16_f32 v124, v220, v224\n"
        "v_cvt_pk_bf16_f32 v236, v221, v225\n"
        "v_mfma_f32_16x16x32_bf16 v[228:231], v[192:195], v[52:55], v[228:231]\n"
        "v_cvt_pk_bf16_f32 v132, v222, v226\n"
        "v_cvt_pk_bf16_f32 v144, v223, v227\n"
        "v_mfma_f32_16x16x32_bf16 v[220:223], v[192:195], v[56:59], 0\n"
        "s_add_u32 s91, s89, 98112\n"
        "v_mfma_f32_16x16x32_bf16 v[220:223], v[108:111], v[116:119], v[220:223]\n"
        "v_mfma_f32_16x16x32_bf16 v[220:223], v[232:235], v[156:159], v[220:223]\n"
        "s_nop 1\n"
        "v_mfma_f32_16x16x32_bf16 v[224:227], v[232:235], v[164:167], 0\n"
        "s_nop 2\n"
        "v_mfma_f32_16x16x32_bf16 v[224:227], v[108:111], v[104:107], v[224:227]\n"
        "s_nop 0\n"
        "v_cvt_pk_bf16_f32 v125, v228, v220\n"
        "v_cvt_pk_bf16_f32 v237, v229, v221\n"
        "v_mfma_f32_16x16x32_bf16 v[224:227], v[184:187], v[52:55], v[224:227]\n"
        "v_cvt_pk_bf16_f32 v133, v230, v222\n"
        "v_cvt_pk_bf16_f32 v145, v231, v223\n"
        "v_mfma_f32_16x16x32_bf16 v[220:223], v[184:187], v[56:59], 0\n"
        "v_mfma_f32_16x16x32_bf16 v[220:223], v[108:111], v[34:37], v[220:223]\n"
        "v_mfma_f32_16x16x32_bf16 v[220:223], v[232:235], v[208:211], v[220:223]\n"
        "s_nop 1\n"
        "v_mfma_f32_16x16x32_bf16 v[108:111], a[144:147], v[200:203], 0\n"
        "s_nop 2\n"
        "v_mfma_f32_16x16x32_bf16 v[108:111], a[148:151], v[168:171], v[108:111]\n"
        "s_nop 0\n"
        "v_cvt_pk_bf16_f32 v126, v224, v220\n"
        "v_cvt_pk_bf16_f32 v238, v225, v221\n"
        "v_mfma_f32_16x16x32_bf16 v[108:111], a[152:155], v[176:179], v[108:111]\n"
        "s_nop 0\n"
        "v_cvt_pk_bf16_f32 v134, v226, v222\n"
        "v_cvt_pk_bf16_f32 v146, v227, v223\n"
        "v_mfma_f32_16x16x32_bf16 v[224:227], a[164:167], v[176:179], 0\n"
        "s_nop 1\n"
        "v_mfma_f32_16x16x32_bf16 v[224:227], a[160:163], v[168:171], v[224:227]\n"
        "s_nop 0\n"
        "v_cvt_pk_bf16_f32 v220, v108, v109\n"
        "v_cvt_pk_bf16_f32 v221, v110, v111\n"
        "v_mfma_f32_16x16x32_bf16 v[224:227], a[156:159], v[200:203], v[224:227]\n"
        "s_nop 0\n"
        "v_mfma_f32_16x16x32_bf16 v[108:111], a[168:171], v[200:203], 0\n"
        "buffer_store_dwordx3 v[124:126], v14, s[40:43], s90 offen sc1 nt\n"
        "s_nop 2\n"
        "v_mfma_f32_16x16x32_bf16 v[108:111], a[172:175], v[168:171], v[108:111]\n"
        "s_nop 0\n"
        "v_cvt_pk_bf16_f32 v222, v224, v225\n"
        "v_cvt_pk_bf16_f32 v223, v226, v227\n"
        "v_mfma_f32_16x16x32_bf16 v[108:111], a[176:179], v[176:179], v[108:111]\n"
        "buffer_store_dwordx3 v[236:238], v15, s[40:43], s90 offen offset:768 sc1 nt\n"
        "s_nop 0\n"
        "v_mfma_f32_16x16x32_bf16 v[232:235], a[188:191], v[176:179], 0\n"
        "s_nop 2\n"
        "v_mfma_f32_16x16x32_bf16 v[232:235], a[184:187], v[168:171], v[232:235]\n"
        "buffer_store_dwordx3 v[132:134], v14, s[40:43], s91 offen offset:192 sc1 nt\n"
        "v_cvt_pk_bf16_f32 v224, v108, v109\n"
        "v_cvt_pk_bf16_f32 v225, v110, v111\n"
        "v_mfma_f32_16x16x32_bf16 v[232:235], a[180:183], v[200:203], v[232:235]\n"
        "s_nop 0\n"
        "v_mfma_f32_16x16x32_bf16 v[228:231], a[192:195], v[200:203], 0\n"
        "buffer_store_dwordx3 v[144:146], v16, s[40:43], s91 offen offset:960 sc1 nt\n"
        "s_nop 2\n"
        "v_mfma_f32_16x16x32_bf16 v[228:231], a[196:199], v[168:171], v[228:231]\n"
        "s_nop 0\n"
        "v_cvt_pk_bf16_f32 v226, v232, v233\n"
        "v_cvt_pk_bf16_f32 v227, v234, v235\n"
        "v_mfma_f32_16x16x32_bf16 v[228:231], a[200:203], v[176:179], v[228:231]\n"
        "s_nop 1\n"
        "v_mfma_f32_16x16x32_bf16 v[108:111], a[212:215], v[176:179], 0\n"
        "s_nop 2\n"
        "v_mfma_f32_16x16x32_bf16 v[108:111], a[208:211], v[168:171], v[108:111]\n"
        "s_nop 0\n"
        "v_cvt_pk_bf16_f32 v232, v228, v229\n"
        "v_cvt_pk_bf16_f32 v233, v230, v231\n"
        "v_mfma_f32_16x16x32_bf16 v[108:111], a[204:207], v[200:203], v[108:111]\n"
        "s_nop 1\n"
        "v_mfma_f32_16x16x32_bf16 v[236:239], v[180:183], v[220:223], 0\n"
        "s_nop 2\n"
        "v_mfma_f32_16x16x32_bf16 v[236:239], v[192:195], v[224:227], v[236:239]\n"
        "s_nop 0\n"
        "v_cvt_pk_bf16_f32 v234, v108, v109\n"
        "v_cvt_pk_bf16_f32 v235, v110, v111\n"
        "s_nop 1\n"
        "v_mfma_f32_16x16x32_bf16 v[236:239], v[184:187], v[232:235], v[236:239]\n"
        "s_nop 1\n"
        "v_mfma_f32_16x16x32_bf16 v[108:111], v[176:179], v[232:235], 0\n"
        "s_nop 2\n"
        "v_mfma_f32_16x16x32_bf16 v[108:111], v[168:171], v[224:227], v[108:111]\n"
        "s_nop 0\n"
        "v_mul_f32 v236, v236, v204\n"
        "v_mul_f32 v237, v237, v205\n"
        "v_mfma_f32_16x16x32_bf16 v[108:111], v[200:203], v[220:223], v[108:111]\n"
        "s_nop 0\n"
        "v_mul_f32 v238, v238, v206\n"
        "v_mul_f32 v239, v239, v207\n"
        "v_mfma_f32_16x16x32_bf16 v[228:231], v[196:199], v[220:223], 0\n"
        "s_nop 0\n"
        "v_max3_f32 v250, v236, v237, v238\n"
        "v_mfma_f32_16x16x32_bf16 v[228:231], v[188:191], v[224:227], v[228:231]\n"
        "s_nop 0\n"
        "v_mul_f32 v108, v108, v212\n"
        "v_mul_f32 v109, v109, v213\n"
        "v_mfma_f32_16x16x32_bf16 v[228:231], v[172:175], v[232:235], v[228:231]\n"
        "v_mul_f32 v110, v110, v214\n"
        "v_mul_f32 v111, v111, v215\n"
        "v_mfma_f32_16x16x32_bf16 v[240:243], v[148:151], v[232:235], 0\n"
        "s_nop 0\n"
        "v_max3_f32 v250, v250, v239, v108\n"
        "v_max3_f32 v250, v250, v109, v110\n"
        "v_mfma_f32_16x16x32_bf16 v[240:243], v[112:115], v[224:227], v[240:243]\n"
        "s_nop 0\n"
        "v_mul_f32 v228, v228, v216\n"
        "v_mul_f32 v229, v229, v217\n"
        "v_mfma_f32_16x16x32_bf16 v[240:243], v[120:123], v[220:223], v[240:243]\n"
        "s_nop 0\n"
        "v_mul_f32 v230, v230, v218\n"
        "v_mul_f32 v231, v231, v219\n"
        "v_max3_f32 v250, v250, v111, v228\n"
        "v_max3_f32 v250, v250, v229, v230\n"
        "s_nop 2\n"
        "v_mul_f32 v240, v240, v253\n"
        "v_cndmask_b32_e64 v240, v255, v240, s[62:63]\n"
        "v_max3_f32 v250, v250, v231, v240\n"
        "v_mov_b32 v251, v250\n"
        "s_nop 1\n"
        "v_permlane32_swap_b32 v250, v251\n"
        "v_max_f32 v250, v250, v251\n"
        "v_mov_b32 v251, v250\n"
        "s_nop 1\n"
        "v_permlane16_swap_b32 v250, v251\n"
        "v_max_f32 v250, v250, v251\n"
        "v_mul_f32 v250, v250, v252\n"
        "v_fma_f32 v236, v236, v252, -v250\n"
        "v_exp_f32 v236, v236\n"
        "v_fma_f32 v237, v237, v252, -v250\n"
        "v_exp_f32 v237, v237\n"
        "s_nop 0\n"
        "v_add_f32 v251, v236, v237\n"
        "v_mul_f32 v236, v236, v204\n"
        "v_fma_f32 v240, v240, v252, -v250\n"
        "v_exp_f32 v240, v240\n"
        "v_fma_f32 v231, v231, v252, -v250\n"
        "v_exp_f32 v231, v231\n"
        "v_fma_f32 v230, v230, v252, -v250\n"
        "v_exp_f32 v230, v230\n"
        "v_fma_f32 v229, v229, v252, -v250\n"
        "v_exp_f32 v229, v229\n"
        "v_fma_f32 v228, v228, v252, -v250\n"
        "v_exp_f32 v228, v228\n"
        "v_fma_f32 v111, v111, v252, -v250\n"
        "v_exp_f32 v111, v111\n"
        "v_fma_f32 v110, v110, v252, -v250\n"
        "v_exp_f32 v110, v110\n"
        "v_fma_f32 v109, v109, v252, -v250\n"
        "v_exp_f32 v109, v109\n"
        "v_fma_f32 v108, v108, v252, -v250\n"
        "v_exp_f32 v108, v108\n"
        "v_fma_f32 v239, v239, v252, -v250\n"
        "v_exp_f32 v239, v239\n"
        "v_fma_f32 v238, v238, v252, -v250\n"
        "v_exp_f32 v238, v238\n"
        "s_nop 0\n"
        "v_add_f32 v251, v251, v238\n"
        "v_add_f32 v251, v251, v239\n"
        "v_add_f32 v251, v251, v108\n"
        "v_add_f32 v251, v251, v109\n"
        "v_add_f32 v251, v251, v110\n"
        "v_add_f32 v251, v251, v111\n"
        "v_add_f32 v251, v251, v228\n"
        "v_add_f32 v251, v251, v229\n"
        "v_add_f32 v251, v251, v230\n"
        "v_add_f32 v251, v251, v231\n"
        "v_add_f32 v251, v251, v240\n"
        "v_mov_b32 v252, v251\n"
        "s_nop 1\n"
        "v_permlane32_swap_b32 v251, v252\n"
        "v_add_f32 v251, v251, v252\n"
        "v_mov_b32 v252, v251\n"
        "s_nop 1\n"
        "v_permlane16_swap_b32 v251, v252\n"
        "v_add_f32 v251, v251, v252\n"
        "v_rcp_f32 v251, v251\n"
        "s_nop 0\n"
        "v_mul_f32 v236, v236, v251\n"
        "v_mul_f32 v237, v237, v205\n"
        "v_mul_f32 v237, v237, v251\n"
        "v_cvt_pk_bf16_f32 v224, v236, v237\n"
        "v_mul_f32 v238, v238, v206\n"
        "v_mul_f32 v238, v238, v251\n"
        "v_mul_f32 v239, v239, v207\n"
        "v_mul_f32 v239, v239, v251\n"
        "v_cvt_pk_bf16_f32 v225, v238, v239\n"
        "v_mul_f32 v108, v108, v212\n"
        "v_mul_f32 v108, v108, v251\n"
        "v_mul_f32 v109, v109, v213\n"
        "v_mul_f32 v109, v109, v251\n"
        "v_cvt_pk_bf16_f32 v226, v108, v109\n"
        "v_mul_f32 v110, v110, v214\n"
        "v_mul_f32 v110, v110, v251\n"
        "v_mul_f32 v111, v111, v215\n"
        "v_mul_f32 v111, v111, v251\n"
        "v_cvt_pk_bf16_f32 v227, v110, v111\n"
        "s_nop 1\n"
        "v_mfma_f32_16x16x32_bf16 v[108:111], v[224:227], v[128:131], 0\n"
        "v_mul_f32 v228, v228, v216\n"
        "v_mul_f32 v229, v229, v217\n"
        "v_mul_f32 v228, v228, v251\n"
        "v_mul_f32 v229, v229, v251\n"
        "v_cvt_pk_bf16_f32 v144, v228, v229\n"
        "v_mul_f32 v230, v230, v218\n"
        "v_mul_f32 v230, v230, v251\n"
        "v_mul_f32 v231, v231, v219\n"
        "v_mul_f32 v231, v231, v251\n"
        "v_cvt_pk_bf16_f32 v145, v230, v231\n"
        "v_mul_f32 v240, v240, v253\n"
        "v_mul_f32 v240, v240, v251\n"
        "v_cvt_pk_bf16_f32 v146, v240, 0\n"
        "s_nop 1\n"
        "v_mfma_f32_16x16x32_bf16 v[108:111], v[144:147], v[136:139], v[108:111]\n"
        "v_mfma_f32_16x16x32_bf16 v[108:111], v[200:203], v[52:55], v[108:111]\n"
        "v_mfma_f32_16x16x32_bf16 v[228:231], v[200:203], v[56:59], 0\n"
        "v_mfma_f32_16x16x32_bf16 v[228:231], v[144:147], v[40:43], v[228:231]\n"
        "v_mfma_f32_16x16x32_bf16 v[228:231], v[224:227], v[152:155], v[228:231]\n"
        "s_add_u32 s90, s89, 196224\n"
        "s_nop 0\n"
        "v_mfma_f32_16x16x32_bf16 v[220:223], v[224:227], v[160:163], 0\n"
        "s_nop 2\n"
        "v_mfma_f32_16x16x32_bf16 v[220:223], v[144:147], v[140:143], v[220:223]\n"
        "s_nop 0\n"
        "v_cvt_pk_bf16_f32 v236, v108, v228\n"
        "v_cvt_pk_bf16_f32 v232, v109, v229\n"
        "v_mfma_f32_16x16x32_bf16 v[220:223], v[168:171], v[52:55], v[220:223]\n"
        "v_cvt_pk_bf16_f32 v132, v110, v230\n"
        "v_cvt_pk_bf16_f32 v124, v111, v231\n"
        "v_mfma_f32_16x16x32_bf16 v[228:231], v[168:171], v[56:59], 0\n"
        "s_add_u32 s91, s89, 294336\n"
        "v_mfma_f32_16x16x32_bf16 v[228:231], v[144:147], v[116:119], v[228:231]\n"
        "v_mfma_f32_16x16x32_bf16 v[228:231], v[224:227], v[156:159], v[228:231]\n"
        "s_nop 1\n"
        "v_mfma_f32_16x16x32_bf16 v[108:111], v[224:227], v[164:167], 0\n"
        "s_nop 2\n"
        "v_mfma_f32_16x16x32_bf16 v[108:111], v[144:147], v[104:107], v[108:111]\n"
        "s_nop 0\n"
        "v_cvt_pk_bf16_f32 v237, v220, v228\n"
        "v_cvt_pk_bf16_f32 v233, v221, v229\n"
        "v_mfma_f32_16x16x32_bf16 v[108:111], v[176:179], v[52:55], v[108:111]\n"
        "v_cvt_pk_bf16_f32 v133, v222, v230\n"
        "v_cvt_pk_bf16_f32 v125, v223, v231\n"
        "v_mfma_f32_16x16x32_bf16 v[220:223], v[176:179], v[56:59], 0\n"
        "v_mfma_f32_16x16x32_bf16 v[220:223], v[144:147], v[34:37], v[220:223]\n"
        "v_mfma_f32_16x16x32_bf16 v[220:223], v[224:227], v[208:211], v[220:223]\n"
        "s_nop 1\n"
        "v_mfma_f32_16x16x32_bf16 v[144:147], a[144:147], v[196:199], 0\n"
        "s_nop 2\n"
        "v_mfma_f32_16x16x32_bf16 v[144:147], a[148:151], v[188:191], v[144:147]\n"
        "s_nop 0\n"
        "v_cvt_pk_bf16_f32 v238, v108, v220\n"
        "v_cvt_pk_bf16_f32 v234, v109, v221\n"
        "v_mfma_f32_16x16x32_bf16 v[144:147], a[152:155], v[172:175], v[144:147]\n"
        "s_nop 0\n"
        "v_cvt_pk_bf16_f32 v134, v110, v222\n"
        "v_cvt_pk_bf16_f32 v126, v111, v223\n"
        "v_mfma_f32_16x16x32_bf16 v[220:223], a[164:167], v[172:175], 0\n"
        "s_nop 1\n"
        "v_mfma_f32_16x16x32_bf16 v[220:223], a[160:163], v[188:191], v[220:223]\n"
        "s_nop 0\n"
        "v_cvt_pk_bf16_f32 v224, v144, v145\n"
        "v_cvt_pk_bf16_f32 v225, v146, v147\n"
        "v_mfma_f32_16x16x32_bf16 v[220:223], a[156:159], v[196:199], v[220:223]\n"
        "s_nop 0\n"
        "v_mfma_f32_16x16x32_bf16 v[108:111], a[168:171], v[196:199], 0\n"
        "buffer_store_dwordx3 v[236:238], v14, s[40:43], s90 offen offset:384 sc1 nt\n"
        "s_nop 2\n"
        "v_mfma_f32_16x16x32_bf16 v[108:111], a[172:175], v[188:191], v[108:111]\n"
        "s_nop 0\n"
        "v_cvt_pk_bf16_f32 v226, v220, v221\n"
        "v_cvt_pk_bf16_f32 v227, v222, v223\n"
        "v_mfma_f32_16x16x32_bf16 v[108:111], a[176:179], v[172:175], v[108:111]\n"
        "buffer_store_dwordx3 v[232:234], v17, s[40:43], s90 offen offset:1152 sc1 nt\n"
        "s_add_u32 s90, s89, 392448\n"
        "v_mfma_f32_16x16x32_bf16 v[220:223], a[188:191], v[172:175], 0\n"
        "s_nop 2\n"
        "v_mfma_f32_16x16x32_bf16 v[220:223], a[184:187], v[188:191], v[220:223]\n"
        "buffer_store_dwordx3 v[132:134], v14, s[40:43], s91 offen offset:576 sc1 nt\n"
        "v_cvt_pk_bf16_f32 v144, v108, v109\n"
        "v_cvt_pk_bf16_f32 v145, v110, v111\n"
        "v_mfma_f32_16x16x32_bf16 v[220:223], a[180:183], v[196:199], v[220:223]\n"
        "s_nop 0\n"
        "v_mfma_f32_16x16x32_bf16 v[108:111], a[192:195], v[196:199], 0\n"
        "buffer_store_dwordx3 v[124:126], v14, s[40:43], s90 offen sc1 nt\n"
        "s_nop 2\n"
        "v_mfma_f32_16x16x32_bf16 v[108:111], a[196:199], v[188:191], v[108:111]\n"
        "s_nop 0\n"
        "v_cvt_pk_bf16_f32 v146, v220, v221\n"
        "v_cvt_pk_bf16_f32 v147, v222, v223\n"
        "v_mfma_f32_16x16x32_bf16 v[108:111], a[200:203], v[172:175], v[108:111]\n"
        "s_nop 1\n"
        "v_mfma_f32_16x16x32_bf16 v[220:223], a[212:215], v[172:175], 0\n"
        "s_nop 2\n"
        "v_mfma_f32_16x16x32_bf16 v[220:223], a[208:211], v[188:191], v[220:223]\n"
        "s_nop 0\n"
        "v_cvt_pk_bf16_f32 v228, v108, v109\n"
        "v_cvt_pk_bf16_f32 v229, v110, v111\n"
        "v_mfma_f32_16x16x32_bf16 v[220:223], a[204:207], v[196:199], v[220:223]\n"
        "s_nop 1\n"
        "v_mfma_f32_16x16x32_bf16 v[108:111], v[180:183], v[224:227], 0\n"
        "s_nop 2\n"
        "v_mfma_f32_16x16x32_bf16 v[108:111], v[192:195], v[144:147], v[108:111]\n"
        "s_nop 0\n"
        "v_cvt_pk_bf16_f32 v230, v220, v221\n"
        "v_cvt_pk_bf16_f32 v231, v222, v223\n"
        "s_nop 1\n"
        "v_mfma_f32_16x16x32_bf16 v[108:111], v[184:187], v[228:231], v[108:111]\n"
        "s_nop 1\n"
        "v_mfma_f32_16x16x32_bf16 v[232:235], v[176:179], v[228:231], 0\n"
        "s_nop 2\n"
        "v_mfma_f32_16x16x32_bf16 v[232:235], v[168:171], v[144:147], v[232:235]\n"
        "s_nop 0\n"
        "v_mul_f32 v108, v108, v204\n"
        "v_mul_f32 v109, v109, v205\n"
        "v_mfma_f32_16x16x32_bf16 v[232:235], v[200:203], v[224:227], v[232:235]\n"
        "s_nop 0\n"
        "v_mul_f32 v110, v110, v206\n"
        "v_mul_f32 v111, v111, v207\n"
        "v_mfma_f32_16x16x32_bf16 v[236:239], v[196:199], v[224:227], 0\n"
        "s_nop 0\n"
        "v_max3_f32 v251, v108, v109, v110\n"
        "v_mfma_f32_16x16x32_bf16 v[236:239], v[188:191], v[144:147], v[236:239]\n"
        "s_nop 0\n"
        "v_mul_f32 v232, v232, v212\n"
        "v_mul_f32 v233, v233, v213\n"
        "v_mfma_f32_16x16x32_bf16 v[236:239], v[172:175], v[228:231], v[236:239]\n"
        "v_mul_f32 v234, v234, v214\n"
        "v_mul_f32 v235, v235, v215\n"
        "v_mfma_f32_16x16x32_bf16 v[220:223], v[148:151], v[228:231], 0\n"
        "s_nop 0\n"
        "v_max3_f32 v251, v251, v111, v232\n"
        "v_max3_f32 v251, v251, v233, v234\n"
        "v_mfma_f32_16x16x32_bf16 v[220:223], v[112:115], v[144:147], v[220:223]\n"
        "s_nop 0\n"
        "v_mul_f32 v236, v236, v216\n"
        "v_mul_f32 v237, v237, v217\n"
        "v_mfma_f32_16x16x32_bf16 v[220:223], v[120:123], v[224:227], v[220:223]\n"
        "s_nop 0\n"
        "v_mul_f32 v238, v238, v218\n"
        "v_mul_f32 v239, v239, v219\n"
        "v_max3_f32 v251, v251, v235, v236\n"
        "v_max3_f32 v251, v251, v237, v238\n"
        "s_nop 2\n"
        "v_mul_f32 v220, v220, v253\n"
        "v_cndmask_b32_e64 v220, v255, v220, s[62:63]\n"
        "v_max3_f32 v251, v251, v239, v220\n"
        "v_mov_b32 v252, v251\n"
        "s_nop 1\n"
        "v_permlane32_swap_b32 v251, v252\n"
        "v_max_f32 v251, v251, v252\n"
        "v_mov_b32 v252, v251\n"
        "s_nop 1\n"
        "v_permlane16_swap_b32 v251, v252\n"
        "v_max_f32 v251, v251, v252\n"
        "v_mul_f32 v251, v251, v254\n"
        "v_fma_f32 v108, v108, v254, -v251\n"
        "v_exp_f32 v108, v108\n"
        "v_fma_f32 v109, v109, v254, -v251\n"
        "v_exp_f32 v109, v109\n"
        "s_nop 0\n"
        "v_add_f32 v252, v108, v109\n"
        "v_mul_f32 v108, v108, v204\n"
        "v_fma_f32 v220, v220, v254, -v251\n"
        "v_exp_f32 v220, v220\n"
        "v_fma_f32 v239, v239, v254, -v251\n"
        "v_exp_f32 v239, v239\n"
        "v_fma_f32 v238, v238, v254, -v251\n"
        "v_exp_f32 v238, v238\n"
        "v_fma_f32 v237, v237, v254, -v251\n"
        "v_exp_f32 v237, v237\n"
        "v_fma_f32 v236, v236, v254, -v251\n"
        "v_exp_f32 v236, v236\n"
        "v_fma_f32 v235, v235, v254, -v251\n"
        "v_exp_f32 v235, v235\n"
        "v_fma_f32 v234, v234, v254, -v251\n"
        "v_exp_f32 v234, v234\n"
        "v_fma_f32 v233, v233, v254, -v251\n"
        "v_exp_f32 v233, v233\n"
        "v_fma_f32 v232, v232, v254, -v251\n"
        "v_exp_f32 v232, v232\n"
        "v_fma_f32 v111, v111, v254, -v251\n"
        "v_exp_f32 v111, v111\n"
        "v_fma_f32 v110, v110, v254, -v251\n"
        "v_exp_f32 v110, v110\n"
        "s_nop 0\n"
        "v_add_f32 v252, v252, v110\n"
        "v_add_f32 v252, v252, v111\n"
        "v_add_f32 v252, v252, v232\n"
        "v_add_f32 v252, v252, v233\n"
        "v_add_f32 v252, v252, v234\n"
        "v_add_f32 v252, v252, v235\n"
        "v_add_f32 v252, v252, v236\n"
        "v_add_f32 v252, v252, v237\n"
        "v_add_f32 v252, v252, v238\n"
        "v_add_f32 v252, v252, v239\n"
        "v_add_f32 v252, v252, v220\n"
        "v_mov_b32 v254, v252\n"
        "s_nop 1\n"
        "v_permlane32_swap_b32 v252, v254\n"
        "v_add_f32 v252, v252, v254\n"
        "v_mov_b32 v254, v252\n"
        "s_nop 1\n"
        "v_permlane16_swap_b32 v252, v254\n"
        "v_add_f32 v252, v252, v254\n"
        "v_rcp_f32 v252, v252\n"
        "s_nop 0\n"
        "v_mul_f32 v108, v108, v252\n"
        "v_mul_f32 v109, v109, v205\n"
        "v_mul_f32 v109, v109, v252\n"
        "v_cvt_pk_bf16_f32 v224, v108, v109\n"
        "v_mul_f32 v110, v110, v206\n"
        "v_mul_f32 v110, v110, v252\n"
        "v_mul_f32 v111, v111, v207\n"
        "v_mul_f32 v111, v111, v252\n"
        "v_cvt_pk_bf16_f32 v225, v110, v111\n"
        "v_mul_f32 v232, v232, v212\n"
        "v_mul_f32 v232, v232, v252\n"
        "v_mul_f32 v233, v233, v213\n"
        "v_mul_f32 v233, v233, v252\n"
        "v_cvt_pk_bf16_f32 v226, v232, v233\n"
        "v_mul_f32 v234, v234, v214\n"
        "v_mul_f32 v234, v234, v252\n"
        "v_mul_f32 v235, v235, v215\n"
        "v_mul_f32 v235, v235, v252\n"
        "v_cvt_pk_bf16_f32 v227, v234, v235\n"
        "s_nop 1\n"
        "v_mfma_f32_16x16x32_bf16 v[108:111], v[224:227], v[128:131], 0\n"
        "v_mul_f32 v236, v236, v216\n"
        "v_mul_f32 v237, v237, v217\n"
        "v_mul_f32 v236, v236, v252\n"
        "v_mul_f32 v237, v237, v252\n"
        "v_cvt_pk_bf16_f32 v132, v236, v237\n"
        "v_mul_f32 v238, v238, v218\n"
        "v_mul_f32 v238, v238, v252\n"
        "v_mul_f32 v239, v239, v219\n"
        "v_mul_f32 v239, v239, v252\n"
        "v_cvt_pk_bf16_f32 v133, v238, v239\n"
        "v_mul_f32 v220, v220, v253\n"
        "v_mul_f32 v220, v220, v252\n"
        "v_cvt_pk_bf16_f32 v134, v220, 0\n"
        "s_nop 1\n"
        "v_mfma_f32_16x16x32_bf16 v[108:111], v[132:135], v[136:139], v[108:111]\n"
        "v_mfma_f32_16x16x32_bf16 v[108:111], v[196:199], v[52:55], v[108:111]\n"
        "v_mfma_f32_16x16x32_bf16 v[220:223], v[196:199], v[56:59], 0\n"
        "v_mfma_f32_16x16x32_bf16 v[220:223], v[132:135], v[40:43], v[220:223]\n"
        "v_mfma_f32_16x16x32_bf16 v[220:223], v[224:227], v[152:155], v[220:223]\n"
        "s_nop 1\n"
        "v_mfma_f32_16x16x32_bf16 v[144:147], v[224:227], v[160:163], 0\n"
        "s_nop 2\n"
        "v_mfma_f32_16x16x32_bf16 v[144:147], v[132:135], v[140:143], v[144:147]\n"
        "s_add_u32 s91, s89, 490560\n"
        "v_cvt_pk_bf16_f32 v236, v108, v220\n"
        "v_cvt_pk_bf16_f32 v124, v109, v221\n"
        "v_mfma_f32_16x16x32_bf16 v[144:147], v[188:191], v[52:55], v[144:147]\n"
        "v_cvt_pk_bf16_f32 v228, v110, v222\n"
        "v_cvt_pk_bf16_f32 v232, v111, v223\n"
        "v_mfma_f32_16x16x32_bf16 v[108:111], v[188:191], v[56:59], 0\n"
        "v_mfma_f32_16x16x32_bf16 v[108:111], v[132:135], v[116:119], v[108:111]\n"
        "v_mfma_f32_16x16x32_bf16 v[108:111], v[224:227], v[156:159], v[108:111]\n"
        "s_nop 1\n"
        "v_mfma_f32_16x16x32_bf16 v[220:223], v[224:227], v[164:167], 0\n"
        "s_nop 2\n"
        "v_mfma_f32_16x16x32_bf16 v[220:223], v[132:135], v[104:107], v[220:223]\n"
        "s_nop 0\n"
        "v_cvt_pk_bf16_f32 v237, v144, v108\n"
        "v_cvt_pk_bf16_f32 v125, v145, v109\n"
        "v_mfma_f32_16x16x32_bf16 v[220:223], v[172:175], v[52:55], v[220:223]\n"
        "v_cvt_pk_bf16_f32 v229, v146, v110\n"
        "v_cvt_pk_bf16_f32 v233, v147, v111\n"
        "v_mfma_f32_16x16x32_bf16 v[108:111], v[172:175], v[56:59], 0\n"
        "v_mfma_f32_16x16x32_bf16 v[108:111], v[132:135], v[34:37], v[108:111]\n"
        "v_mfma_f32_16x16x32_bf16 v[108:111], v[224:227], v[208:211], v[108:111]\n"
        "s_nop 1\n"
        "v_mfma_f32_16x16x32_bf16 v[144:147], a[144:147], v[120:123], 0\n"
        "s_nop 2\n"
        "v_mfma_f32_16x16x32_bf16 v[144:147], a[148:151], v[112:115], v[144:147]\n"
        "s_nop 0\n"
        "v_cvt_pk_bf16_f32 v238, v220, v108\n"
        "v_cvt_pk_bf16_f32 v126, v221, v109\n"
        "v_mfma_f32_16x16x32_bf16 v[144:147], a[152:155], v[148:151], v[144:147]\n"
        "s_nop 0\n"
        "v_cvt_pk_bf16_f32 v230, v222, v110\n"
        "v_cvt_pk_bf16_f32 v234, v223, v111\n"
        "v_mfma_f32_16x16x32_bf16 v[132:135], a[164:167], v[148:151], 0\n"
        "s_nop 1\n"
        "v_mfma_f32_16x16x32_bf16 v[132:135], a[160:163], v[112:115], v[132:135]\n"
        "s_nop 0\n"
        "v_cvt_pk_bf16_f32 v108, v144, v145\n"
        "v_cvt_pk_bf16_f32 v109, v146, v147\n"
        "v_mfma_f32_16x16x32_bf16 v[132:135], a[156:159], v[120:123], v[132:135]\n"
        "s_nop 0\n"
        "v_mfma_f32_16x16x32_bf16 v[220:223], a[168:171], v[120:123], 0\n"
        "buffer_store_dwordx3 v[236:238], v15, s[40:43], s90 offen offset:768 sc1 nt\n"
        "s_add_u32 s90, s89, 588672\n"
        "s_nop 1\n"
        "v_mfma_f32_16x16x32_bf16 v[220:223], a[172:175], v[112:115], v[220:223]\n"
        "s_nop 0\n"
        "v_cvt_pk_bf16_f32 v110, v132, v133\n"
        "v_cvt_pk_bf16_f32 v111, v134, v135\n"
        "v_mfma_f32_16x16x32_bf16 v[220:223], a[176:179], v[148:151], v[220:223]\n"
        "buffer_store_dwordx3 v[124:126], v14, s[40:43], s91 offen offset:192 sc1 nt\n"
        "s_nop 0\n"
        "v_mfma_f32_16x16x32_bf16 v[132:135], a[188:191], v[148:151], 0\n"
        "s_nop 2\n"
        "v_mfma_f32_16x16x32_bf16 v[132:135], a[184:187], v[112:115], v[132:135]\n"
        "buffer_store_dwordx3 v[228:230], v16, s[40:43], s91 offen offset:960 sc1 nt\n"
        "v_cvt_pk_bf16_f32 v144, v220, v221\n"
        "v_cvt_pk_bf16_f32 v145, v222, v223\n"
        "v_mfma_f32_16x16x32_bf16 v[132:135], a[180:183], v[120:123], v[132:135]\n"
        "s_nop 0\n"
        "v_mfma_f32_16x16x32_bf16 v[220:223], a[192:195], v[120:123], 0\n"
        "buffer_store_dwordx3 v[232:234], v14, s[40:43], s90 offen offset:384 sc1 nt\n"
        "s_nop 2\n"
        "v_mfma_f32_16x16x32_bf16 v[220:223], a[196:199], v[112:115], v[220:223]\n"
        "s_nop 0\n"
        "v_cvt_pk_bf16_f32 v146, v132, v133\n"
        "v_cvt_pk_bf16_f32 v147, v134, v135\n"
        "v_mfma_f32_16x16x32_bf16 v[220:223], a[200:203], v[148:151], v[220:223]\n"
        "s_nop 1\n"
        "v_mfma_f32_16x16x32_bf16 v[132:135], a[212:215], v[148:151], 0\n"
        "s_nop 2\n"
        "v_mfma_f32_16x16x32_bf16 v[132:135], a[208:211], v[112:115], v[132:135]\n"
        "s_nop 0\n"
        "v_cvt_pk_bf16_f32 v224, v220, v221\n"
        "v_cvt_pk_bf16_f32 v225, v222, v223\n"
        "v_mfma_f32_16x16x32_bf16 v[132:135], a[204:207], v[120:123], v[132:135]\n"
        "s_nop 1\n"
        "v_mfma_f32_16x16x32_bf16 v[220:223], v[180:183], v[108:111], 0\n"
        "s_nop 2\n"
        "v_mfma_f32_16x16x32_bf16 v[220:223], v[192:195], v[144:147], v[220:223]\n"
        "s_nop 0\n"
        "v_cvt_pk_bf16_f32 v226, v132, v133\n"
        "v_cvt_pk_bf16_f32 v227, v134, v135\n"
        "s_nop 1\n"
        "v_mfma_f32_16x16x32_bf16 v[220:223], v[184:187], v[224:227], v[220:223]\n"
        "s_nop 1\n"
        "v_mfma_f32_16x16x32_bf16 v[180:183], v[176:179], v[224:227], 0\n"
        "s_nop 2\n"
        "v_mfma_f32_16x16x32_bf16 v[180:183], v[168:171], v[144:147], v[180:183]\n"
        "s_nop 0\n"
        "v_mul_f32 v220, v220, v204\n"
        "v_mul_f32 v221, v221, v205\n"
        "v_mfma_f32_16x16x32_bf16 v[180:183], v[200:203], v[108:111], v[180:183]\n"
        "s_nop 0\n"
        "v_mul_f32 v222, v222, v206\n"
        "v_mul_f32 v223, v223, v207\n"
        "v_mfma_f32_16x16x32_bf16 v[168:171], v[196:199], v[108:111], 0\n"
        "s_nop 0\n"
        "v_max3_f32 v254, v220, v221, v222\n"
        "v_mfma_f32_16x16x32_bf16 v[168:171], v[188:191], v[144:147], v[168:171]\n"
        "s_nop 0\n"
        "v_mul_f32 v180, v180, v212\n"
        "v_mul_f32 v181, v181, v213\n"
        "v_mfma_f32_16x16x32_bf16 v[168:171], v[172:175], v[224:227], v[168:171]\n"
        "v_mul_f32 v182, v182, v214\n"
        "v_mul_f32 v183, v183, v215\n"
        "v_mfma_f32_16x16x32_bf16 v[132:135], v[148:151], v[224:227], 0\n"
        "s_nop 0\n"
        "v_max3_f32 v254, v254, v223, v180\n"
        "v_max3_f32 v254, v254, v181, v182\n"
        "v_mfma_f32_16x16x32_bf16 v[132:135], v[112:115], v[144:147], v[132:135]\n"
        "s_nop 0\n"
        "v_mul_f32 v168, v168, v216\n"
        "v_mul_f32 v169, v169, v217\n"
        "v_mfma_f32_16x16x32_bf16 v[132:135], v[120:123], v[108:111], v[132:135]\n"
        "s_nop 0\n"
        "v_mul_f32 v170, v170, v218\n"
        "v_mul_f32 v171, v171, v219\n"
        "v_max3_f32 v254, v254, v183, v168\n"
        "v_max3_f32 v254, v254, v169, v170\n"
        "s_nop 2\n"
        "v_mul_f32 v132, v132, v253\n"
        "v_cndmask_b32_e64 v132, v255, v132, s[62:63]\n"
        "v_max3_f32 v254, v254, v171, v132\n"
        "v_mov_b32 v255, v254\n"
        "s_nop 1\n"
        "v_permlane32_swap_b32 v254, v255\n"
        "v_max_f32 v254, v254, v255\n"
        "v_mov_b32 v255, v254\n"
        "s_nop 1\n"
        "v_permlane16_swap_b32 v254, v255\n"
        "v_max_f32 v254, v254, v255\n"
        "v_mul_f32 v254, v254, v253\n"
        "v_fma_f32 v220, v220, v253, -v254\n"
        "v_exp_f32 v220, v220\n"
        "v_fma_f32 v221, v221, v253, -v254\n"
        "v_exp_f32 v221, v221\n"
        "s_nop 0\n"
        "v_add_f32 v255, v220, v221\n"
        "v_mul_f32 v220, v220, v204\n"
        "v_fma_f32 v132, v132, v253, -v254\n"
        "v_exp_f32 v132, v132\n"
        "v_fma_f32 v171, v171, v253, -v254\n"
        "v_exp_f32 v171, v171\n"
        "v_fma_f32 v170, v170, v253, -v254\n"
        "v_exp_f32 v170, v170\n"
        "v_fma_f32 v169, v169, v253, -v254\n"
        "v_exp_f32 v169, v169\n"
        "v_fma_f32 v168, v168, v253, -v254\n"
        "v_exp_f32 v168, v168\n"
        "v_fma_f32 v183, v183, v253, -v254\n"
        "v_exp_f32 v183, v183\n"
        "v_fma_f32 v182, v182, v253, -v254\n"
        "v_exp_f32 v182, v182\n"
        "v_fma_f32 v181, v181, v253, -v254\n"
        "v_exp_f32 v181, v181\n"
        "v_fma_f32 v180, v180, v253, -v254\n"
        "v_exp_f32 v180, v180\n"
        "v_fma_f32 v223, v223, v253, -v254\n"
        "v_exp_f32 v223, v223\n"
        "v_fma_f32 v222, v222, v253, -v254\n"
        "v_exp_f32 v222, v222\n"
        "s_nop 0\n"
        "v_add_f32 v255, v255, v222\n"
        "v_add_f32 v255, v255, v223\n"
        "v_add_f32 v255, v255, v180\n"
        "v_add_f32 v255, v255, v181\n"
        "v_add_f32 v255, v255, v182\n"
        "v_add_f32 v255, v255, v183\n"
        "v_add_f32 v255, v255, v168\n"
        "v_add_f32 v255, v255, v169\n"
        "v_add_f32 v255, v255, v170\n"
        "v_add_f32 v255, v255, v171\n"
        "v_add_f32 v255, v255, v132\n"
        "v_mov_b32 v254, v255\n"
        "s_nop 1\n"
        "v_permlane32_swap_b32 v255, v254\n"
        "v_add_f32 v255, v255, v254\n"
        "v_mov_b32 v254, v255\n"
        "s_nop 1\n"
        "v_permlane16_swap_b32 v255, v254\n"
        "v_add_f32 v255, v255, v254\n"
        "v_rcp_f32 v255, v255\n"
        "s_nop 0\n"
        "v_mul_f32 v220, v220, v255\n"
        "v_mul_f32 v221, v221, v205\n"
        "v_mul_f32 v221, v221, v255\n"
        "v_cvt_pk_bf16_f32 v144, v220, v221\n"
        "v_mul_f32 v222, v222, v206\n"
        "v_mul_f32 v222, v222, v255\n"
        "v_mul_f32 v223, v223, v207\n"
        "v_mul_f32 v223, v223, v255\n"
        "v_cvt_pk_bf16_f32 v145, v222, v223\n"
        "v_mul_f32 v180, v180, v212\n"
        "v_mul_f32 v180, v180, v255\n"
        "v_mul_f32 v181, v181, v213\n"
        "v_mul_f32 v181, v181, v255\n"
        "v_cvt_pk_bf16_f32 v146, v180, v181\n"
        "v_mul_f32 v182, v182, v214\n"
        "v_mul_f32 v182, v182, v255\n"
        "v_mul_f32 v183, v183, v215\n"
        "v_mul_f32 v183, v183, v255\n"
        "v_cvt_pk_bf16_f32 v147, v182, v183\n"
        "s_nop 1\n"
        "v_mfma_f32_16x16x32_bf16 v[108:111], v[144:147], v[128:131], 0\n"
        "v_mul_f32 v168, v168, v216\n"
        "v_mul_f32 v169, v169, v217\n"
        "v_mul_f32 v168, v168, v255\n"
        "v_mul_f32 v169, v169, v255\n"
        "v_cvt_pk_bf16_f32 v124, v168, v169\n"
        "v_mul_f32 v170, v170, v218\n"
        "v_mul_f32 v170, v170, v255\n"
        "v_mul_f32 v171, v171, v219\n"
        "v_mul_f32 v171, v171, v255\n"
        "v_cvt_pk_bf16_f32 v125, v170, v171\n"
        "v_mul_f32 v132, v132, v253\n"
        "v_mul_f32 v132, v132, v255\n"
        "v_cvt_pk_bf16_f32 v126, v132, 0\n"
        "s_nop 1\n"
        "v_mfma_f32_16x16x32_bf16 v[108:111], v[124:127], v[136:139], v[108:111]\n"
        "v_mfma_f32_16x16x32_bf16 v[108:111], v[120:123], v[52:55], v[108:111]\n"
        "v_mfma_f32_16x16x32_bf16 v[128:131], v[120:123], v[56:59], 0\n"
        "v_mfma_f32_16x16x32_bf16 v[128:131], v[124:127], v[40:43], v[128:131]\n"
        "v_mfma_f32_16x16x32_bf16 v[128:131], v[144:147], v[152:155], v[128:131]\n"
        "s_nop 1\n"
        "v_mfma_f32_16x16x32_bf16 v[120:123], v[144:147], v[160:163], 0\n"
        "s_nop 2\n"
        "v_mfma_f32_16x16x32_bf16 v[120:123], v[124:127], v[140:143], v[120:123]\n"
        "s_nop 0\n"
        "v_cvt_pk_bf16_f32 v130, v108, v128\n"
        "v_mfma_f32_16x16x32_bf16 v[120:123], v[112:115], v[52:55], v[120:123]\n"
        "v_mfma_f32_16x16x32_bf16 v[108:111], v[112:115], v[56:59], 0\n"
        "v_mfma_f32_16x16x32_bf16 v[108:111], v[124:127], v[116:119], v[108:111]\n"
        "v_mfma_f32_16x16x32_bf16 v[108:111], v[144:147], v[156:159], v[108:111]\n"
        "s_nop 1\n"
        "v_mfma_f32_16x16x32_bf16 v[40:43], v[144:147], v[164:167], 0\n"
        "s_nop 2\n"
        "v_mfma_f32_16x16x32_bf16 v[40:43], v[124:127], v[104:107], v[40:43]\n"
        "s_nop 0\n"
        "v_cvt_pk_bf16_f32 v131, v120, v108\n"
        "v_mfma_f32_16x16x32_bf16 v[40:43], v[148:151], v[52:55], v[40:43]\n"
        "v_mfma_f32_16x16x32_bf16 v[104:107], v[148:151], v[56:59], 0\n"
        "v_mfma_f32_16x16x32_bf16 v[104:107], v[124:127], v[34:37], v[104:107]\n"
        "s_add_u32 s46, s89, 590016\n"
        "v_mfma_f32_16x16x32_bf16 v[104:107], v[144:147], v[208:211], v[104:107]\n"
        "s_nop 2\n"
        "s_min_u32 s46, s46, s42\n"
        "s_nop 3\n"
        "v_cvt_pk_bf16_f32 v132, v40, v104\n"
        "buffer_store_dwordx3 v[130:132], v17, s[44:47], s90 offen offset:1152 sc1 nt\n"
        "s_nop 15\n"
        "s_cmp_lt_i32 s85, -1\n"
        "s_cbranch_scc1 FIN_%=\n"
        "s_mov_b32 s89, 0x80000000\n"
        "s_waitcnt vmcnt(0)\n"
        "ds_read_b128 v[146:149], v9\n"
        "ds_read_b128 v[152:155], v10\n"
        "ds_read_b128 v[158:161], v11\n"
        "ds_read_b128 v[126:129], v9 offset:3072\n"
        "ds_read_b128 v[132:135], v10 offset:3072\n"
        "ds_read_b128 v[138:141], v11 offset:3072\n"
        "ds_read_b128 v[106:109], v9 offset:6144\n"
        "ds_read_b128 v[112:115], v10 offset:6144\n"
        "ds_read_b128 v[118:121], v11 offset:6144\n"
        "s_cmp_gt_i32 s85, 1\n"
        "s_cbranch_scc1 SLX_%=\n"
        "v_readfirstlane_b32 s50, v26\n"
        "s_nop 3\n"
        "s_sub_u32 s50, s50, s20\n"
        "s_min_u32 s50, s50, 31\n"
        "s_and_b32 s92, s29, 1\n"
        "s_lshl_b32 s92, s92, 2\n"
        "s_add_u32 s92, s92, s49\n"
        "v_mov_b32 v27, s92\n"
        "v_mov_b32 v26, s50\n"
        "ds_write_b32 v27, v26\n"
        "s_lshl_b32 s50, s50, 2\n"
        "s_waitcnt lgkmcnt(0)\n"
        "v_mov_b32 v27, s50\n"
        "v_mov_b32 v26, 1\n"
        "s_mov_b64 exec, 1\n"
        "buffer_atomic_add v26, v27, s[16:19], 0 offen offset:4\n"
        "s_mov_b64 exec, -1\n"
        "s_nop 1\n"
        "SLX_%=:\n"
        "s_waitcnt lgkmcnt(0)\n"
        "s_nop 7\n"
        "s_cmp_ge_i32 s85, 0\n"
        "s_cbranch_scc1 TOP_%=\n"
        "s_branch LAST_%=\n"
        "DY1_%=:\n"
        "s_and_b32 s50, s29, 1\n"
        "s_lshl_b32 s50, s50, 2\n"
        "s_add_u32 s50, s50, 163424\n"
        "v_mov_b32 v27, s50\n"
        "ds_read_b32 v27, v27\n"
        "s_waitcnt lgkmcnt(0)\n"
        "v_readfirstlane_b32 s50, v27\n"
        "s_nop 3\n"
        "s_lshl_b32 s92, s50, 5\n"
        "s_add_u32 s92, s92, s48\n"
        "s_cmp_ge_u32 s92, 5329\n"
        "s_cbranch_scc1 DY1B_%=\n"
        "s_mul_i32 s87, s92, 1344\n"
        "s_mul_i32 s92, s92, 57457\n"
        "s_lshr_b32 s92, s92, 22\n"
        "s_mul_i32 s92, s92, 588672\n"
        "s_add_u32 s87, s87, s92\n"
        "s_add_u32 s85, s85, 1\n"
        "s_add_u32 s50, s50, 20\n"
        "s_sub_u32 s23, s50, s53\n"
        "s_mul_i32 s23, s23, 1584\n"
        "s_mov_b32 s53, s50\n"
        "s_branch DY1B_%=\n"
        "DY2_%=:\n"
        "v_mov_b32 v26, 1\n"
        "s_mov_b64 exec, 1\n"
        "buffer_atomic_add v26, off, s[16:19], 0 sc0\n"
        "s_mov_b64 exec, -1\n"
        "s_branch DY2B_%=\n"
        "SP6_%=:\n"
        "s_sleep 1\n"
        "ds_read_u16 v253, v19 offset:2048\n"
        "s_waitcnt lgkmcnt(0)\n"
        "v_cmp_ne_u32_e64 s[24:25], s30, v253\n"
        "s_nop 4\n"
        "s_cmp_eq_u64 s[24:25], 0\n"
        "s_cbranch_scc1 BK6_%=\n"
        "s_sub_u32 s31, s31, 1\n"
        "s_cbranch_scc0 SP6_%=\n"
        "s_endpgm\n"
        "SP10_%=:\n"
        "s_sleep 1\n"
        "ds_read_u16 v137, v19 offset:2560\n"
        "s_waitcnt lgkmcnt(0)\n"
        "v_cmp_ne_u32_e64 s[24:25], s29, v137\n"
        "s_nop 4\n"
        "s_cmp_eq_u64 s[24:25], 0\n"
        "s_cbranch_scc1 BK10_%=\n"
        "s_sub_u32 s31, s31, 1\n"
        "s_cbranch_scc0 SP10_%=\n"
        "s_endpgm\n"
        "DY3_%=:\n"
        "v_readfirstlane_b32 s50, v26\n"
        "s_nop 3\n"
        "s_sub_u32 s50, s50, s20\n"
        "s_min_u32 s50, s50, 31\n"
        "s_and_b32 s92, s29, 1\n"
        "s_lshl_b32 s92, s92, 2\n"
        "s_add_u32 s92, s92, s49\n"
        "v_mov_b32 v27, s92\n"
        "v_mov_b32 v26, s50\n"
        "ds_write_b32 v27, v26\n"
        "s_lshl_b32 s50, s50, 2\n"
        "s_waitcnt lgkmcnt(0)\n"
        "v_mov_b32 v27, s50\n"
        "v_mov_b32 v26, 1\n"
        "s_mov_b64 exec, 1\n"
        "buffer_atomic_add v26, v27, s[16:19], 0 offen offset:4\n"
        "s_mov_b64 exec, -1\n"
        "s_nop 1\n"
        "s_branch DY3B_%=\n"
        "SP20_%=:\n"
        "s_sleep 1\n"
        "ds_read_u16 v255, v19 offset:2048\n"
        "s_waitcnt lgkmcnt(0)\n"
        "v_cmp_ne_u32_e64 s[24:25], s30, v255\n"
        "s_nop 4\n"
        "s_cmp_eq_u64 s[24:25], 0\n"
        "s_cbranch_scc1 BK20_%=\n"
        "s_sub_u32 s31, s31, 1\n"
        "s_cbranch_scc0 SP20_%=\n"
        "s_endpgm\n"
        :: "{v[0:15]}"(c0), "{v[16:23]}"(c1), "{v[24:27]}"(c2), "{v[206:213]}"(sp), "{v[30:45]}"(f0), "{v[46:49]}"(f1), "{s15}"(own4), "{s[16:19]}"(srdl), "{s[20:23]}"(sol), "{s[36:39]}"(srdx), "{s[40:43]}"(srdo), "{s[48:55]}"(si)
        : "v28", "v29", "v50", "v51", "v52", "v53", "v54", "v55", "v56", "v57", "v58", "v59", "v60", "v61", "v62", "v63", "v64", "v65", "v66", "v67", "v68", "v69", "v70", "v71", "v72", "v73", "v74", "v75", "v76", "v77", "v78", "v79", "v80", "v81", "v82", "v83", "v84", "v85", "v86", "v87", "v88", "v89", "v90", "v91", "v92", "v93", "v94", "v95", "v96", "v97", "v98", "v99", "v100", "v101", "v102", "v103", "v104", "v105", "v106", "v107", "v108", "v109", "v110", "v111", "v112", "v113", "v114", "v115", "v116", "v117", "v118", "v119", "v120", "v121", "v122", "v123", "v124", "v125", "v126", "v127", "v128", "v129", "v130", "v131", "v132", "v133", "v134", "v135", "v136", "v137", "v138", "v139", "v140", "v141", "v142", "v143", "v144", "v145", "v146", "v147", "v148", "v149", "v150", "v151", "v152", "v153", "v154", "v155", "v156", "v157", "v158", "v159", "v160", "v161", "v162", "v163", "v164", "v165", "v166", "v167", "v168", "v169", "v170", "v171", "v172", "v173", "v174", "v175", "v176", "v177", "v178", "v179", "v180", "v181", "v182", "v183", "v184", "v185", "v186", "v187", "v188", "v189", "v190", "v191", "v192", "v193", "v194", "v195", "v196", "v197", "v198", "v199", "v200", "v201", "v202", "v203", "v204", "v205", "v214", "v215", "v216", "v217", "v218", "v219", "v220", "v221", "v222", "v223", "v224", "v225", "v226", "v227", "v228", "v229", "v230", "v231", "v232", "v233", "v234", "v235", "v236", "v237", "v238", "v239", "v240", "v241", "v242", "v243", "v244", "v245", "v246", "v247", "v248", "v249", "v250", "v251", "v252", "v253", "v254", "v255", "a0", "a1", "a2", "a3", "a4", "a5", "a6", "a7", "a8", "a9", "a10", "a11", "a12", "a13", "a14", "a15", "a16", "a17", "a18", "a19", "a20", "a21", "a22", "a23", "a24", "a25", "a26", "a27", "a28", "a29", "a30", "a31", "a32", "a33", "a34", "a35", "a36", "a37", "a38", "a39", "a40", "a41", "a42", "a43", "a44", "a45", "a46", "a47", "a48", "a49", "a50", "a51", "a52", "a53", "a54", "a55", "a56", "a57", "a58", "a59", "a60", "a61", "a62", "a63", "a64", "a65", "a66", "a67", "a68", "a69", "a70", "a71", "a72", "a73", "a74", "a75", "a76", "a77", "a78", "a79", "a80", "a81", "a82", "a83", "a84", "a85", "a86", "a87", "a88", "a89", "a90", "a91", "a92", "a93", "a94", "a95", "a96", "a97", "a98", "a99", "a100", "a101", "a102", "a103", "a104", "a105", "a106", "a107", "a108", "a109", "a110", "a111", "a112", "a113", "a114", "a115", "a116", "a117", "a118", "a119", "a120", "a121", "a122", "a123", "a124", "a125", "a126", "a127", "a128", "a129", "a130", "a131", "a132", "a133", "a134", "a135", "a136", "a137", "a138", "a139", "a140", "a141", "a142", "a143", "a144", "a145", "a146", "a147", "a148", "a149", "a150", "a151", "a152", "a153", "a154", "a155", "a156", "a157", "a158", "a159", "a160", "a161", "a162", "a163", "a164", "a165", "a166", "a167", "a168", "a169", "a170", "a171", "a172", "a173", "a174", "a175", "a176", "a177", "a178", "a179", "a180", "a181", "a182", "a183", "a184", "a185", "a186", "a187", "a188", "a189", "a190", "a191", "a192", "a193", "a194", "a195", "a196", "a197", "a198", "a199", "a200", "a201", "a202", "a203", "a204", "a205", "a206", "a207", "a208", "a209", "a210", "a211", "a212", "a213", "a214", "a215", "a216", "a217", "a218", "a219", "a220", "a221", "a222", "a223", "a224", "a225", "a226", "a227", "a228", "a229", "a230", "a231", "a232", "a233", "a234", "a235", "a236", "a237", "a238", "a239", "a240", "a241", "a242", "a243", "a244", "a245", "a246", "a247", "a248", "a249", "a250", "a251", "a252", "a253", "a254", "a255", "s24", "s25", "s26", "s27", "s28", "s29", "s30", "s31", "s44", "s45", "s46", "s47", "s56", "s57", "s58", "s59", "s60", "s61", "s62", "s63", "s64", "s65", "s66", "s67", "s68", "s69", "s70", "s71", "s72", "s73", "s74", "s75", "s76", "s77", "s78", "s79", "s80", "s81", "s82", "s83", "s84", "s85", "s86", "s87", "s88", "s89", "s90", "s91", "s92", "s93", "s94", "s95", "vcc", "scc", "memory");
}

extern "C" void kernel_launch(void* const* d_in, const int* in_sizes, int n_in, void* d_out, int out_size, void* d_ws, size_t ws_size, hipStream_t stream) {
    (void)in_sizes; (void)n_in; (void)out_size; (void)ws_size;
    (void)hipFuncSetAttribute((const void*)swin_kernel, hipFuncAttributeMaxDynamicSharedMemorySize, LDS_BYTES);
    swin_kernel<<<GRID, 256, LDS_BYTES, stream>>>((const char*)d_in[0], (const char*)d_in[1], (const char*)d_in[2], (const char*)d_in[3], (const char*)d_in[4], (char*)d_out, (const char*)d_ws);
}
```

```cpp
#define NLC 5
#define WSTEP 32
#define CPL 8
#define SMASK 31914
#define ODDS 1
#define DYN 1
#define EARLY 1
#define TLBW 0
#include <hip/hip_runtime.h>
#include <hip/hip_bf16.h>

#define DI __device__ __forceinline__

using u32x2 = __attribute__((ext_vector_type(2))) unsigned;
using u32x4 = __attribute__((ext_vector_type(4))) unsigned;
using u32x8 = __attribute__((ext_vector_type(8))) unsigned;
using u32x16 = __attribute__((ext_vector_type(16))) unsigned;

constexpr int HH = 511, WW = 511, NWX = 73, NWIN = NWX * NWX, GRID = 256;
constexpr int TOKB = 192, ROWB = WW * TOKB;
constexpr unsigned FRAMEB = (unsigned)HH * ROWB, TOTALB = FRAMEB * 4;
constexpr unsigned FS = 9232, L_BUF = 0, L_STG = 4 * FS, REC = 1584, NREC = DYN ? 27 : 21, L_EXT = L_STG + 4 * 9216, L_MS = L_EXT + 2 * NREC * REC - (DYN ? 18432 : 0), L_STA = L_MS + 18432, L_PSC = L_STA + 512,
                   L_STB = L_PSC + 512, L_AUX = L_STB + 512, L_DUM = L_AUX + 1024, L_FA = L_DUM + 512, LDS_BYTES = 163840;
constexpr unsigned RAWM = 18432, L_GT = L_AUX + 768;
static_assert(L_EXT + 3 * RAWM <= L_MS && ((L_STG >> 10) & 3) == 0 && (L_STG & 1023) < 512 && L_FA + 1024 + 16 <= LDS_BYTES && (NWIN + GRID - 1) / GRID <= NREC, "LDS");

#define LP(T, a) ((__attribute__((address_space(3))) T*)(size_t)(unsigned)(a))
#define SB() __builtin_amdgcn_sched_barrier(0)
#define PIN(v) asm volatile("" : "+v"(v))
DI unsigned raw_lane(unsigned lane, unsigned m, unsigned A, unsigned B, unsigned C) {
    const unsigned n = 64 * m + lane, row = (n * 171) >> 11, pc = n - 12 * row;
    return (row >> 3) * (A & 0xffff) + ((row >> 1) & 3) * (B & 0xffff) + (row & 1) * (C & 0xffff) + (pc ^ ((row >> 2) & 3)) * 16 - 1024 * m;
}
DI u32x4 make_srd(unsigned lo, unsigned hi, unsigned bytes) { return u32x4{lo, hi & 0xffffu, bytes, 0x00020000u}; }
DI void raw_dma(unsigned v0, unsigned v1, unsigned v2, const u32x4& srd, unsigned so, unsigned dst) {
    asm volatile("s_mov_b32 m0, %5\n\ts_nop 0\n\tbuffer_load_dwordx4 %0, %3, %4 offen lds\n\tbuffer_load_dwordx4 %1, %3, %4 offen offset:1024 lds\n\tbuffer_load_dwordx4 %2, %3, %4 offen offset:2048 lds"
                 :: "v"(v0), "v"(v1), "v"(v2), "s"(srd), "s"(so), "s"(dst) : "memory");
}

__global__ __launch_bounds__(256, 1) void swin_kernel(const char* __restrict__ x_, const char* __restrict__ wqkv_t, const char* __restrict__ wo_t, const char* __restrict__ wqkv_s,
                                                           const char* __restrict__ wo_s, char* __restrict__ out_, const char* wf) {
    const unsigned k = __builtin_amdgcn_readfirstlane(threadIdx.x >> 6) & 3, lane = threadIdx.x & 63, k1 = k & 1, k2 = k >> 1;
    u32x2 kx, kl, kr; u32x4 ko;
    const unsigned long long kap = (unsigned long long)__builtin_amdgcn_kernarg_segment_ptr();
    asm volatile("s_load_dwordx2 %0, %4, %5\n\ts_load_dwordx2 %1, %4, %6\n\ts_load_dwordx2 %2, %4, 0x0\n\ts_load_dwordx4 %3, %4, 0x28"
                 : "=&s"(kr), "=&s"(kl), "=&s"(kx), "=&s"(ko) : "s"(kap), "s"(8 + 8 * k), "s"(8 + 16 * k2));
    SB();
    {
        unsigned long long pc; unsigned co = ((((blockIdx.x >> 3) * 4 + k) * CPL + lane / (64 / CPL)) & 511) * 128;
        asm volatile("s_getpc_b64 %0\n.Lswin_pc:\n\ts_mov_b32 m0, %2\n\tv_min_u32 %1, .Lswin_fin-.Lswin_pc, %1\n\tglobal_load_lds_dword %1, %0" : "=&s"(pc), "+v"(co) : "s"(L_DUM + 256) : "memory");
    }
    SB();
#define TS(i)
    const unsigned Bl = 1152 + 2304 * k2, Ql = 4608 - 3456 * k2;
    const unsigned Ar = 13824 - 12288 * k1, Br = k1 ? 384 : Bl, Cr = 576 - 384 * k1, Pr = 27648 - 24576 * k1, Qr = k1 ? 6144 : Ql, cr = 192 - 192 * k1, szr = 55296 - 36864 * k1;
    const unsigned ldr = L_STG + k * RAWM + k2 * RAWM, ldl = k1 * RAWM + k2 * (L_MS - k1 * (L_MS - L_EXT + RAWM));
    static_assert(L_STG + 2 * RAWM == L_EXT, "");
    unsigned vW = (((blockIdx.x >> 3) & 31) * 14 + lane) * 128 + (lane >= 14) * 0x40000000u; PIN(vW);
    unsigned vr[3];
#pragma unroll
    for (int m = 0; m < EARLY; ++m) { vr[m] = raw_lane(lane, m, Ar, Br, Cr); PIN(vr[m]); }
    SB();
    asm volatile("s_waitcnt lgkmcnt(0)" : "+s"(kr), "+s"(kl), "+s"(kx), "+s"(ko));
    const u32x4 srdr = make_srd(kr[0], kr[1], szr);
    asm volatile("s_mov_b32 m0, %2\n\ts_nop 0\n\tbuffer_load_dword %0, %1, 0 offen lds" :: "v"(vW), "s"(srdr), "s"(L_DUM) : "memory");
    SB();
#pragma unroll
    for (int m = EARLY; m < 3; ++m) { vr[m] = raw_lane(lane, m, Ar, Br, Cr); PIN(vr[m]); }
#define RDMA(n) do { SB(); raw_dma(vr[0], vr[1], vr[2], srdr, cr + (n & 1) * Pr + (n >> 1) * Qr, ldr + n * 3072); SB(); } while (0)
    RDMA(0); RDMA(1);
    TS(1);
    const u32x4 srdl = make_srd(kl[0], kl[1], 55296), srdx = make_srd(kx[0], kx[1], TOTALB), srdo = make_srd(ko[0], ko[1], TOTALB);
    const unsigned vb = (blockIdx.x & 7) * (GRID / 8) + (blockIdx.x >> 3);
    const unsigned w0 = (blockIdx.x & 7) * 640 + (blockIdx.x >> 3), nwin = 5120 + (blockIdx.x >> 3), wy0 = w0 / NWX, wx0 = w0 - wy0 * NWX, own4 = (blockIdx.x & 7) * 4;
    static_assert(WSTEP == 32, "");
    const unsigned base0 = wy0 * 7 * ROWB + wx0 * 7 * TOKB;
#define SLOT(n) do { constexpr int a_ = __builtin_popcount(SMASK & ((1 << (n)) - 1)); if ((SMASK >> (n)) & 1) { if (a_ < 4) RDMA((a_ + 2)); else LDMA((a_ - 4)); } } while (0)
    static_assert(__builtin_popcount(SMASK) == 4 + NLC, "");
    const unsigned i = lane & 15, g = lane >> 4, p = i >> 2, t = i & 3, cl = 384 * k1;
    u32x16 c0, f0; u32x8 c1, sp; u32x4 c2;
    const unsigned stg = L_STG + k * 9216, ext = L_EXT + k * 384, exv = L_EXT + NREC * REC + k * 392;
    unsigned vl[3];
#pragma unroll
    for (int m = 0; m < 3; ++m) { vl[m] = raw_lane(lane, m, 13824, Bl, 576); PIN(vl[m]); }
#define LDMA(n) do { SB(); raw_dma(vl[0], vl[1], vl[2], srdl, cl + (n & 1) * 27648 + (n >> 1) * Ql, ldl + n * 3072); SB(); } while (0)
    SLOT(0);
#pragma unroll
    for (int a = 0; a < 2; ++a) {
        const unsigned ra = (8 * p + 4 * a + t) * 192 + ((g ^ ((2 * p + a) & 3)) * 16);
        f0[a] = ldl + ra; f0[2 + a] = ldr + ra; f0[4 + a] = ldr + (8 * g + 4 * a + p) * 192 + ((t ^ ((2 * g + a) & 3)) * 16);
        PIN(f0[a]); PIN(f0[2 + a]); PIN(f0[4 + a]);
    }
    SLOT(1);
#pragma unroll
    for (int a = 0; a < 2; ++a) {
        const unsigned n = 16 * a + 4 * k + p;
        const unsigned w = (DYN && n >= 20 ? nwin - 20 * WSTEP : w0) + n * WSTEP;
        f0[6 + a] = (DYN ? w < NWIN : n < nwin) ? t * FRAMEB + w * (7 * TOKB) + ((w * 57457) >> 22) * (7 * ROWB - NWX * 7 * TOKB) + 6 * ROWB + 6 * TOKB + g * 48 : 0x80000000u; PIN(f0[6 + a]);
        if (a == 0) SLOT(2);
    }
    f0[8] = i == 0 ? L_AUX + 192 + g * 16 : L_DUM; f0[15] = 0;
    SLOT(3);
    {
        const unsigned sh = 2 - 2 * k2, ts = 3072 + 16 * k2, ml = 64 - 48 * k2, xm = 64 * (k == 1), a0 = ldl + g * 256 + (k == 1 ? t * 16 : k1 ? 0 : (i ^ ((g & k & 2) << 2)) * 16);
#pragma unroll
        for (int n = 0; n < 6; ++n) {
            const unsigned z = (32 * (n >> 1) + 4 * (n & 1) + 8 * p + t) >> sh;
            const unsigned z6 = (z * 43) >> 8, tl = z - 6 * z6;
            f0[9 + n] = a0 + (tl * ts + ((z6 * ml) ^ ((tl << 5) & xm))) * k1 + n * 3072 * (1 - k1); PIN(f0[9 + n]);
            if (n == 0) SLOT(4);
            if (n == 2) SLOT(5);
            if (n == 4) SLOT(6);
        }
    }
    SLOT(7);
#pragma unroll
    for (int m = 0; m < 3; ++m) {
        const unsigned n = 64 * m + lane, tf = (n * 171) >> 13, r = n - 48 * tf, q = (r * 43) >> 9;
        const unsigned c = tf * FRAMEB + ((r - 12 * q) ^ tf) * 16;
#pragma unroll
        for (int u = 0; u < 3; ++u) {
            const unsigned px = 16 * u + 4 * k + q;
            c0[3 * u + m] = c + px * TOKB + ((px * 37) >> 8) * (ROWB - 7 * TOKB) - 1024 * ((3 * u + m) % 4); PIN(c0[3 * u + m]);
        }
        if (m == 0) SLOT(8);
        if (m == 1) SLOT(9);
        if (m == 2) SLOT(10);
    }
#pragma unroll
    for (int S = 0; S < 3; ++S) c0[9 + S] = stg + t * 768 + p * TOKB + ((3 * g + S) ^ t) * 16;
    c0[12] = L_BUF + t * FS + g * 256 + p * 64 + k * 16;
    c0[13] = L_BUF + k * FS + lane * 16;
    const unsigned vs = g * TOKB + 12 * i, brk = ROWB - 7 * TOKB, u = (p - g) & 3;
    c0[14] = vs; c0[15] = vs + (g >= 3 ? brk : 0);
    PIN(c0);
    SLOT(11);
    c1[0] = vs + (g >= 2 ? brk : 0); c1[1] = vs + (g >= 1 ? brk : 0);
    c1[2] = u < 3 ? L_STA + t * 128 + g * 32 + u * 8 + k * 2 : L_DUM;
    c1[3] = L_STB + t * 128 + (4 * g + k) * 8 + u * 2;
    c1[4] = L_STA + k * 128 + g * 32;
    c1[5] = L_STB + k * 128 + i * 8;
    c1[6] = L_STA + k * 128 + p * 32 + g * 8 + t * 2;
    c1[7] = ext - REC + (i & 1) * 192 + g * 16;
    c2[0] = exv - REC + 24 * i;
    c2[1] = p == g ? ~0u : 0u;
    c2[2] = L_AUX + (t < 2 ? t : 2) * 192 + g * 16;
    c2[3] = p == g ? L_AUX + 576 + t * 16 : L_AUX + 384;
    PIN(c1); PIN(c2);
    SLOT(12);
    sp[0] = lane * 16; sp[1] = L_MS + (16 * t + ((4 * g + p) ^ ((t & 2) << 2))) * 16;
#pragma unroll
    for (int T = 0; T < 2; ++T) {
        const unsigned n = 16 * T + 4 * t + p;
        sp[2 + T] = n < NREC ? ext + n * REC + g * 16 : L_DUM + (i & 7) * 16; sp[4 + T] = n < NREC && p == g ? exv + n * REC + 384 : L_DUM;
    }
    sp[6] = exv + g * REC + 24 * i; sp[7] = g < (DYN ? 3 : 1) ? exv + ((DYN ? 24 : 20) + g) * REC + 24 * i : L_DUM + 24 * i;
    PIN(sp);
    SLOT(13);
    {
        const unsigned e = 64 * k + lane, y = e - 144, one = (y < 16 ? (y >> 2) & 1 : y >> 5) ? 0x3f800000u : 0x3f80u;
        *LP(unsigned, L_AUX + e * 4) = e < 48 ? 0x3f803f80u : e == 192 ? 0x46800000u : y < 48 && t == (y < 16 ? y >> 3 : (y >> 2) & 3) ? one : 0u;
        *LP(unsigned, L_STA + k * 128 + (lane & 6) * 16 + 24 + (lane & 1) * 4) = 0x3c003c00u;
        *LP(unsigned, L_FA + e * 4) = 0u;
    }
    SLOT(14);
    const u32x4 f1 = {vl[0], vl[1], vl[2], 0u}, sol = {cl + Ql, cl + Ql + 27648, cl + 2 * Ql, cl + 2 * Ql + 27648};
    const unsigned long long gl = (((unsigned long long)ko[3] << 32) | ko[2]) + (blockIdx.x >> 3) * 128;
    const u32x8 si = {(unsigned)nwin, base0, (unsigned)wx0, k * FRAMEB, stg, ldl, (unsigned)gl, (unsigned)(gl >> 32) & 0xffffu};
    asm volatile(
        "s_bitcmp1_b32 s52, 10\n"
        "s_cbranch_scc1 FV_%=\n"
        "s_mov_b32 s24, 0x3e16c740\n"
        "v_mov_b32 v50, 0x3f803f80\n"
        "v_mov_b32 v51, 0x3f803f80\n"
        "v_mov_b32 v52, 0x3f803f80\n"
        "v_mov_b32 v53, 0x3f803f80\n"
        "s_waitcnt vmcnt(15)\n"
        "ds_read_b64_tr_b16 a[72:73], v34 offset:0\n"
        "ds_read_b64_tr_b16 a[74:75], v35 offset:0\n"
        "ds_read_b64_tr_b16 a[76:77], v34 offset:6144\n"
        "ds_read_b64_tr_b16 a[78:79], v35 offset:6144\n"
        "ds_read_b64_tr_b16 a[80:81], v34 offset:12288\n"
        "ds_read_b64_tr_b16 a[82:83], v35 offset:12288\n"
        "s_add_u32 m0, s53, 15360\n"
        "s_nop 0\n"
        "buffer_load_dwordx4 v46, s[16:19], s23 offen lds\n"
        "ds_read_b64_tr_b16 a[84:85], v34 offset:8\n"
        "ds_read_b64_tr_b16 a[86:87], v35 offset:8\n"
        "ds_read_b64_tr_b16 a[88:89], v34 offset:6152\n"
        "ds_read_b64_tr_b16 a[90:91], v35 offset:6152\n"
        "ds_read_b64_tr_b16 a[92:93], v34 offset:12296\n"
        "ds_read_b64_tr_b16 a[94:95], v35 offset:12296\n"
        "buffer_load_dwordx4 v47, s[16:19], s23 offen offset:1024 lds\n"
        "ds_read_b64_tr_b16 a[96:97], v34 offset:64\n"
        "ds_read_b64_tr_b16 a[98:99], v35 offset:64\n"
        "ds_read_b64_tr_b16 a[100:101], v34 offset:6208\n"
        "ds_read_b64_tr_b16 a[102:103], v35 offset:6208\n"
        "ds_read_b64_tr_b16 a[104:105], v34 offset:12352\n"
        "ds_read_b64_tr_b16 a[106:107], v35 offset:12352\n"
        "buffer_load_dwordx4 v48, s[16:19], s23 offen offset:2048 lds\n"
        "ds_read_b64_tr_b16 a[108:109], v34 offset:72\n"
        "ds_read_b64_tr_b16 a[110:111], v35 offset:72\n"
        "ds_read_b64_tr_b16 a[112:113], v34 offset:6216\n"
        "ds_read_b64_tr_b16 a[114:115], v35 offset:6216\n"
        "ds_read_b64_tr_b16 a[116:117], v34 offset:12360\n"
        "ds_read_b64_tr_b16 a[118:119], v35 offset:12360\n"
        "ds_read_b64_tr_b16 a[120:121], v34 offset:128\n"
        "ds_read_b64_tr_b16 a[122:123], v35 offset:128\n"
        "ds_read_b64_tr_b16 a[124:125], v34 offset:6272\n"
        "ds_read_b64_tr_b16 a[126:127], v35 offset:6272\n"
        "ds_read_b64_tr_b16 a[128:129], v34 offset:12416\n"
        "ds_read_b64_tr_b16 a[130:131], v35 offset:12416\n"
        "ds_read_b64_tr_b16 a[132:133], v34 offset:136\n"
        "ds_read_b64_tr_b16 a[134:135], v35 offset:136\n"
        "ds_read_b64_tr_b16 a[136:137], v34 offset:6280\n"
        "ds_read_b64_tr_b16 a[138:139], v35 offset:6280\n"
        "ds_read_b64_tr_b16 a[140:141], v34 offset:12424\n"
        "ds_read_b64_tr_b16 a[142:143], v35 offset:12424\n"
        "s_waitcnt lgkmcnt(15)\n"
        "v_mfma_f32_16x16x32_bf16 v[46:49], a[72:75], v[50:53], 0\n"
        "s_waitcnt vmcnt(12)\n"
        "s_waitcnt vmcnt(6)\n"
        "v_mfma_f32_16x16x32_bf16 v[46:49], a[76:79], v[50:53], v[46:49]\n"
        "v_mfma_f32_16x16x32_bf16 v[46:49], a[80:83], v[50:53], v[46:49]\n"
        "s_waitcnt lgkmcnt(15)\n"
        "s_nop 0\n"
        "v_mfma_f32_16x16x32_bf16 v[54:57], a[92:95], v[50:53], 0\n"
        "s_nop 2\n"
        "v_mfma_f32_16x16x32_bf16 v[54:57], a[88:91], v[50:53], v[54:57]\n"
        "s_waitcnt lgkmcnt(15)\n"
        "v_mul_f32 v46, 0xbc2aaaab, v46\n"
        "v_mul_f32 v47, 0xbc2aaaab, v47\n"
        "v_mul_f32 v48, 0xbc2aaaab, v48\n"
        "v_mfma_f32_16x16x32_bf16 v[54:57], a[84:87], v[50:53], v[54:57]\n"
        "s_waitcnt vmcnt(0)\n"
        "v_mul_f32 v49, 0xbc2aaaab, v49\n"
        "v_cvt_pk_bf16_f32 v86, v46, v47\n"
        "v_cvt_pk_bf16_f32 v87, v48, v49\n"
        "v_mfma_f32_16x16x32_bf16 v[46:49], a[96:99], v[50:53], 0\n"
        "s_waitcnt lgkmcnt(12)\n"
        "ds_read_b128 a[0:3], v32\n"
        "v_mfma_f32_16x16x32_bf16 v[46:49], a[100:103], v[50:53], v[46:49]\n"
        "s_waitcnt lgkmcnt(7)\n"
        "ds_read_b128 a[4:7], v32 offset:64\n"
        "v_mul_f32 v54, 0xbc2aaaab, v54\n"
        "v_mul_f32 v55, 0xbc2aaaab, v55\n"
        "v_mfma_f32_16x16x32_bf16 v[46:49], a[104:107], v[50:53], v[46:49]\n"
        "s_waitcnt lgkmcnt(2)\n"
        "ds_read_b128 a[8:11], v32 offset:128\n"
        "v_mul_f32 v56, 0xbc2aaaab, v56\n"
        "v_mul_f32 v57, 0xbc2aaaab, v57\n"
        "v_mfma_f32_16x16x32_bf16 v[58:61], a[116:119], v[50:53], 0\n"
        "ds_read_b128 a[144:147], v30\n"
        "v_cvt_pk_bf16_f32 v88, v54, v55\n"
        "v_cvt_pk_bf16_f32 v89, v56, v57\n"
        "v_mfma_f32_16x16x32_bf16 v[58:61], a[112:115], v[50:53], v[58:61]\n"
        "ds_read_b128 a[148:151], v30 offset:64\n"
        "v_mul_f32 v46, 0xbc2aaaab, v46\n"
        "v_mul_f32 v47, 0xbc2aaaab, v47\n"
        "v_mfma_f32_16x16x32_bf16 v[58:61], a[108:111], v[50:53], v[58:61]\n"
        "ds_read_b128 a[152:155], v30 offset:128\n"
        "v_mul_f32 v48, 0xbc2aaaab, v48\n"
        "v_mul_f32 v49, 0xbc2aaaab, v49\n"
        "v_mfma_f32_16x16x32_bf16 v[54:57], a[120:123], v[50:53], 0\n"
        "ds_read_b128 a[156:159], v31\n"
        "v_cvt_pk_bf16_f32 v90, v46, v47\n"
        "v_cvt_pk_bf16_f32 v91, v48, v49\n"
        "v_mfma_f32_16x16x32_bf16 v[54:57], a[124:127], v[50:53], v[54:57]\n"
        "ds_read_b128 a[160:163], v31 offset:64\n"
        "v_mul_f32 v58, 0xbc2aaaab, v58\n"
        "v_mul_f32 v59, 0xbc2aaaab, v59\n"
        "v_mfma_f32_16x16x32_bf16 v[54:57], a[128:131], v[50:53], v[54:57]\n"
        "ds_read_b128 a[164:167], v31 offset:128\n"
        "v_mul_f32 v60, 0xbc2aaaab, v60\n"
        "v_mul_f32 v61, 0xbc2aaaab, v61\n"
        "v_mfma_f32_16x16x32_bf16 v[62:65], a[140:143], v[50:53], 0\n"
        "ds_read_b128 a[168:171], v30 offset:6144\n"
        "v_cvt_pk_bf16_f32 v92, v58, v59\n"
        "v_cvt_pk_bf16_f32 v93, v60, v61\n"
        "v_mfma_f32_16x16x32_bf16 v[62:65], a[136:139], v[50:53], v[62:65]\n"
        "ds_read_b128 a[172:175], v30 offset:6208\n"
        "v_mul_f32 v54, 0xbc2aaaab, v54\n"
        "v_mul_f32 v55, 0xbc2aaaab, v55\n"
        "v_mfma_f32_16x16x32_bf16 v[62:65], a[132:135], v[50:53], v[62:65]\n"
        "s_waitcnt lgkmcnt(8)\n"
        "ds_read_b128 a[176:179], v30 offset:6272\n"
        "v_mul_f32 v56, 0xbc2aaaab, v56\n"
        "v_mul_f32 v57, 0xbc2aaaab, v57\n"
        "s_waitcnt lgkmcnt(6)\n"
        "v_mfma_f32_16x16x32_bf16 v[46:49], a[144:147], v[86:89], 0\n"
        "ds_read_b128 a[180:183], v31 offset:6144\n"
        "v_cvt_pk_bf16_f32 v94, v54, v55\n"
        "v_cvt_pk_bf16_f32 v95, v56, v57\n"
        "v_mfma_f32_16x16x32_bf16 v[46:49], a[148:151], v[90:93], v[46:49]\n"
        "ds_read_b128 a[184:187], v31 offset:6208\n"
        "v_mul_f32 v62, 0xbc2aaaab, v62\n"
        "v_mul_f32 v63, 0xbc2aaaab, v63\n"
        "v_cvt_pk_bf16_f32 v96, v62, v63\n"
        "v_mul_f32 v64, 0xbc2aaaab, v64\n"
        "v_mul_f32 v65, 0xbc2aaaab, v65\n"
        "v_cvt_pk_bf16_f32 v97, v64, v65\n"
        "s_nop 1\n"
        "v_mfma_f32_16x16x32_bf16 v[46:49], a[152:155], v[94:97], v[46:49]\n"
        "s_waitcnt lgkmcnt(5)\n"
        "ds_read_b128 a[188:191], v31 offset:6272\n"
        "v_mfma_f32_16x16x32_bf16 v[54:57], a[152:155], a[8:11], v[46:49]\n"
        "buffer_load_dwordx4 v[62:65], v36, s[36:39], 0 offen\n"
        "v_mfma_f32_16x16x32_bf16 v[54:57], a[148:151], a[4:7], v[54:57]\n"
        "ds_read_b128 a[192:195], v30 offset:12288\n"
        "ds_read_b128 a[196:199], v30 offset:12352\n"
        "v_mfma_f32_16x16x32_bf16 v[54:57], a[144:147], a[0:3], v[54:57]\n"
        "s_waitcnt lgkmcnt(5)\n"
        "ds_read_b128 a[200:203], v30 offset:12416\n"
        "v_mfma_f32_16x16x32_bf16 v[50:53], a[156:159], v[86:89], 0\n"
        "s_nop 0\n"
        "ds_read_b128 a[204:207], v31 offset:12288\n"
        "v_mfma_f32_16x16x32_bf16 v[50:53], a[160:163], v[90:93], v[50:53]\n"
        "s_nop 0\n"
        "ds_read_b128 a[208:211], v31 offset:12352\n"
        "v_add_f32 v255, v54, v55\n"
        "v_add_f32 v255, v255, v56\n"
        "v_mfma_f32_16x16x32_bf16 v[50:53], a[164:167], v[94:97], v[50:53]\n"
        "ds_read_b128 a[212:215], v31 offset:12416\n"
        "v_add_f32 v255, v255, v57\n"
        "v_mfma_f32_16x16x32_bf16 v[98:101], a[164:167], a[8:11], v[50:53]\n"
        "ds_read_b128 a[12:15], v33\n"
        "v_mfma_f32_16x16x32_bf16 v[98:101], a[160:163], a[4:7], v[98:101]\n"
        "ds_read_b128 a[16:19], v33 offset:64\n"
        "v_mfma_f32_16x16x32_bf16 v[98:101], a[156:159], a[0:3], v[98:101]\n"
        "ds_read_b128 a[20:23], v33 offset:128\n"
        "v_mfma_f32_16x16x32_bf16 v[58:61], a[168:171], v[86:89], 0\n"
        "s_waitcnt lgkmcnt(9)\n"
        "buffer_load_dwordx4 v[68:71], v36, s[36:39], 0 offen offset:16\n"
        "v_mfma_f32_16x16x32_bf16 v[58:61], a[172:175], v[90:93], v[58:61]\n"
        "s_mov_b32 s78, 0xf0000f\n"
        "ds_read_b128 a[24:27], v32 offset:6144\n"
        "ds_read_b128 a[28:31], v32 offset:6208\n"
        "v_add_f32 v255, v255, v98\n"
        "v_mfma_f32_16x16x32_bf16 v[58:61], a[176:179], v[94:97], v[58:61]\n"
        "s_mov_b32 s79, 0xf0000f00\n"
        "ds_read_b128 a[32:35], v32 offset:6272\n"
        "v_add_f32 v255, v255, v99\n"
        "v_add_f32 v255, v255, v100\n"
        "v_mfma_f32_16x16x32_bf16 v[102:105], a[176:179], a[8:11], v[58:61]\n"
        "s_waitcnt lgkmcnt(9)\n"
        "ds_read_b128 a[36:39], v33 offset:6144\n"
        "v_add_f32 v255, v255, v101\n"
        "v_mfma_f32_16x16x32_bf16 v[102:105], a[172:175], a[4:7], v[102:105]\n"
        "s_mov_b32 s76, 0x11111111\n"
        "ds_read_b128 a[40:43], v33 offset:6208\n"
        "v_mfma_f32_16x16x32_bf16 v[102:105], a[168:171], a[0:3], v[102:105]\n"
        "s_mov_b32 s77, 0x11111111\n"
        "ds_read_b128 a[44:47], v33 offset:6272\n"
        "v_mfma_f32_16x16x32_bf16 v[82:85], a[180:183], v[86:89], 0\n"
        "s_waitcnt lgkmcnt(9)\n"
        "ds_read_b128 a[48:51], v32 offset:12288\n"
        "v_mfma_f32_16x16x32_bf16 v[82:85], a[184:187], v[90:93], v[82:85]\n"
        "s_mov_b32 s56, 0x22222222\n"
        "ds_read_b128 a[52:55], v32 offset:12352\n"
        "v_add_f32 v255, v255, v102\n"
        "v_add_f32 v255, v255, v103\n"
        "v_mfma_f32_16x16x32_bf16 v[82:85], a[188:191], v[94:97], v[82:85]\n"
        "s_mov_b32 s57, 0x22222222\n"
        "ds_read_b128 a[56:59], v32 offset:12416\n"
        "v_add_f32 v255, v255, v104\n"
        "v_add_f32 v255, v255, v105\n"
        "v_mfma_f32_16x16x32_bf16 v[106:109], a[188:191], a[8:11], v[82:85]\n"
        "s_mov_b32 s58, 0x44444444\n"
        "buffer_load_dwordx4 v[74:77], v36, s[36:39], 0 offen offset:32\n"
        "v_mfma_f32_16x16x32_bf16 v[106:109], a[184:187], a[4:7], v[106:109]\n"
        "s_waitcnt lgkmcnt(9)\n"
        "ds_read_b128 a[60:63], v33 offset:12288\n"
        "ds_read_b128 a[64:67], v33 offset:12352\n"
        "v_mfma_f32_16x16x32_bf16 v[106:109], a[180:183], a[0:3], v[106:109]\n"
        "s_mov_b32 s59, 0x44444444\n"
        "ds_read_b128 a[68:71], v33 offset:12416\n"
        "v_mfma_f32_16x16x32_bf16 v[78:81], a[192:195], v[86:89], 0\n"
        "s_mov_b32 s60, 0x88888888\n"
        "s_nop 1\n"
        "v_mfma_f32_16x16x32_bf16 v[78:81], a[196:199], v[90:93], v[78:81]\n"
        "s_mov_b32 s61, 0x88888888\n"
        "v_add_f32 v255, v255, v106\n"
        "v_add_f32 v255, v255, v107\n"
        "v_add_f32 v255, v255, v108\n"
        "v_mfma_f32_16x16x32_bf16 v[78:81], a[200:203], v[94:97], v[78:81]\n"
        "s_mov_b32 s62, 0xffff\n"
        "v_add_f32 v255, v255, v109\n"
        "v_mfma_f32_16x16x32_bf16 v[110:113], a[200:203], a[8:11], v[78:81]\n"
        "s_mov_b32 s63, 0x0\n"
        "v_mfma_f32_16x16x32_bf16 v[110:113], a[196:199], a[4:7], v[110:113]\n"
        "s_waitcnt lgkmcnt(9)\n"
        "v_mfma_f32_16x16x32_bf16 v[110:113], a[192:195], a[0:3], v[110:113]\n"
        "s_mov_b32 s64, 0xffff0000\n"
        "buffer_load_dwordx4 v[176:179], v37, s[36:39], 0 offen\n"
        "v_mfma_f32_16x16x32_bf16 v[30:33], a[204:207], v[86:89], 0\n"
        "s_mov_b32 s65, 0x0\n"
        "s_nop 1\n"
        "v_mfma_f32_16x16x32_bf16 v[30:33], a[208:211], v[90:93], v[30:33]\n"
        "s_mov_b32 s66, 0x0\n"
        "v_add_f32 v255, v255, v110\n"
        "v_add_f32 v255, v255, v111\n"
        "v_add_f32 v255, v255, v112\n"
        "v_mfma_f32_16x16x32_bf16 v[30:33], a[212:215], v[94:97], v[30:33]\n"
        "s_mov_b32 s67, 0xffff\n"
        "v_add_f32 v255, v255, v113\n"
        "v_mfma_f32_16x16x32_bf16 v[86:89], a[212:215], a[8:11], v[30:33]\n"
        "s_waitcnt lgkmcnt(0)\n"
        "v_mfma_f32_16x16x32_bf16 v[86:89], a[208:211], a[4:7], v[86:89]\n"
        "s_mov_b32 s68, 0x0\n"
        "v_mfma_f32_16x16x32_bf16 v[86:89], a[204:207], a[0:3], v[86:89]\n"
        "s_mov_b32 s69, 0xffff0000\n"
        "s_nop 0\n"
        "v_mfma_f32_16x16x32_bf16 v[114:117], a[144:147], a[12:15], v[46:49]\n"
        "s_mov_b32 s70, 0x0\n"
        "s_nop 1\n"
        "v_mfma_f32_16x16x32_bf16 v[114:117], a[148:151], a[16:19], v[114:117]\n"
        "s_mov_b32 s71, 0xffffffff\n"
        "v_add_f32 v255, v255, v86\n"
        "v_add_f32 v255, v255, v87\n"
        "v_add_f32 v255, v255, v88\n"
        "v_mfma_f32_16x16x32_bf16 v[114:117], a[152:155], a[20:23], v[114:117]\n"
        "s_barrier\n"
        "buffer_load_dwordx4 v[182:185], v37, s[36:39], 0 offen offset:16\n"
        "v_add_f32 v255, v255, v89\n"
        "v_mov_b32 v253, v255\n"
        "v_mfma_f32_16x16x32_bf16 v[122:125], a[164:167], a[20:23], v[50:53]\n"
        "s_mov_b32 s72, 0x0\n"
        "v_permlane32_swap_b32 v255, v253\n"
        "v_add_f32 v255, v255, v253\n"
        "v_mfma_f32_16x16x32_bf16 v[122:125], a[160:163], a[16:19], v[122:125]\n"
        "s_mov_b32 s73, 0x20000\n"
        "v_mov_b32 v253, v255\n"
        "v_add_f32 v254, v114, v115\n"
        "v_add_f32 v254, v254, v116\n"
        "v_mfma_f32_16x16x32_bf16 v[122:125], a[156:159], a[12:15], v[122:125]\n"
        "s_mov_b64 vcc, s[78:79]\n"
        "v_permlane16_swap_b32 v255, v253\n"
        "v_add_f32 v255, v255, v253\n"
        "v_mfma_f32_16x16x32_bf16 v[126:129], a[168:171], a[12:15], v[58:61]\n"
        "s_mov_b32 s80, 0x3c2aaaab\n"
        "v_mul_f32 v255, 0xbac909aa, v255\n"
        "v_add_f32 v254, v254, v117\n"
        "v_fma_f32 v54, v54, s24, v255\n"
        "v_mfma_f32_16x16x32_bf16 v[126:129], a[172:175], a[16:19], v[126:129]\n"
        "s_mov_b32 s81, 0x3727c5ac\n"
        "v_fma_f32 v55, v55, s24, v255\n"
        "v_fma_f32 v56, v56, s24, v255\n"
        "v_fma_f32 v57, v57, s24, v255\n"
        "v_mfma_f32_16x16x32_bf16 v[126:129], a[176:179], a[20:23], v[126:129]\n"
        "s_mov_b32 s82, 0x43800000\n"
        "v_fma_f32 v98, v98, s24, v255\n"
        "v_fma_f32 v99, v99, s24, v255\n"
        "v_fma_f32 v100, v100, s24, v255\n"
        "v_mfma_f32_16x16x32_bf16 v[118:121], a[188:191], a[20:23], v[82:85]\n"
        "s_mov_b32 s84, 0x80000000\n"
        "v_fma_f32 v101, v101, s24, v255\n"
        "v_fma_f32 v102, v102, s24, v255\n"
        "v_fma_f32 v103, v103, s24, v255\n"
        "v_mfma_f32_16x16x32_bf16 v[118:121], a[184:187], a[16:19], v[118:121]\n"
        "s_mov_b32 s85, 19\n"
        "v_fma_f32 v104, v104, s24, v255\n"
        "v_fma_f32 v105, v105, s24, v255\n"
        "v_fma_f32 v106, v106, s24, v255\n"
        "v_mfma_f32_16x16x32_bf16 v[118:121], a[180:183], a[12:15], v[118:121]\n"
        "s_mov_b32 s86, 0x80000000\n"
        "buffer_load_dwordx4 v[188:191], v37, s[36:39], 0 offen offset:32\n"
        "v_fma_f32 v107, v107, s24, v255\n"
        "v_fma_f32 v108, v108, s24, v255\n"
        "v_mfma_f32_16x16x32_bf16 v[134:137], a[192:195], a[12:15], v[78:81]\n"
        "s_mov_b32 s89, 0x80000000\n"
        "v_fma_f32 v109, v109, s24, v255\n"
        "v_fma_f32 v110, v110, s24, v255\n"
        "v_fma_f32 v111, v111, s24, v255\n"
        "v_mfma_f32_16x16x32_bf16 v[134:137], a[196:199], a[16:19], v[134:137]\n"
        "s_mov_b32 s29, 1\n"
        "v_fma_f32 v112, v112, s24, v255\n"
        "v_fma_f32 v113, v113, s24, v255\n"
        "v_fma_f32 v86, v86, s24, v255\n"
        "v_mfma_f32_16x16x32_bf16 v[134:137], a[200:203], a[20:23], v[134:137]\n"
        "s_mov_b32 s31, 0x400000\n"
        "v_fma_f32 v87, v87, s24, v255\n"
        "v_fma_f32 v88, v88, s24, v255\n"
        "v_fma_f32 v89, v89, s24, v255\n"
        "v_mfma_f32_16x16x32_bf16 v[138:141], a[212:215], a[20:23], v[30:33]\n"
        "s_mov_b32 s87, s49\n"
        "v_cvt_pk_bf16_f32 v34, v54, v55\n"
        "v_cvt_pk_bf16_f32 v35, v56, v57\n"
        "v_cvt_pk_bf16_f32 v36, v98, v99\n"
        "v_mfma_f32_16x16x32_bf16 v[138:141], a[208:211], a[16:19], v[138:141]\n"
        "s_mov_b32 s88, s50\n"
        "v_cvt_pk_bf16_f32 v37, v100, v101\n"
        "v_cvt_pk_bf16_f32 v54, v102, v103\n"
        "v_cvt_pk_bf16_f32 v55, v104, v105\n"
        "v_mfma_f32_16x16x32_bf16 v[138:141], a[204:207], a[12:15], v[138:141]\n"
        "s_mov_b64 s[44:45], s[40:41]\n"
        "ds_write_b128 v39, v[34:37]\n"
        "v_cvt_pk_bf16_f32 v56, v106, v107\n"
        "v_mfma_f32_16x16x32_bf16 v[34:37], a[144:147], a[24:27], v[46:49]\n"
        "s_mov_b32 s46, 0\n"
        "v_cvt_pk_bf16_f32 v57, v108, v109\n"
        "v_cvt_pk_bf16_f32 v90, v110, v111\n"
        "v_cvt_pk_bf16_f32 v91, v112, v113\n"
        "v_mfma_f32_16x16x32_bf16 v[34:37], a[148:151], a[28:31], v[34:37]\n"
        "s_add_u32 m0, s52, 0\n"
        "ds_write_b128 v39, v[54:57] offset:1024\n"
        "v_cvt_pk_bf16_f32 v92, v86, v87\n"
        "v_mfma_f32_16x16x32_bf16 v[34:37], a[152:155], a[32:35], v[34:37]\n"
        "s_mov_b32 s47, s43\n"
        "buffer_load_dwordx4 v0, s[36:39], s49 offen lds\n"
        "v_cvt_pk_bf16_f32 v93, v88, v89\n"
        "v_add_f32 v254, v254, v122\n"
        "v_mfma_f32_16x16x32_bf16 v[54:57], a[164:167], a[32:35], v[50:53]\n"
        "s_waitcnt lgkmcnt(2)\n"
        "ds_write_b128 v39, v[90:93] offset:2048\n"
        "v_add_f32 v254, v254, v123\n"
        "v_mfma_f32_16x16x32_bf16 v[54:57], a[160:163], a[28:31], v[54:57]\n"
        "s_waitcnt lgkmcnt(3)\n"
        "v_add_f32 v254, v254, v124\n"
        "v_add_f32 v254, v254, v125\n"
        "v_add_f32 v254, v254, v126\n"
        "v_mfma_f32_16x16x32_bf16 v[54:57], a[156:159], a[24:27], v[54:57]\n"
        "s_waitcnt lgkmcnt(3)\n"
        "v_add_f32 v254, v254, v127\n"
        "v_add_f32 v254, v254, v128\n"
        "v_add_f32 v254, v254, v129\n"
        "v_mfma_f32_16x16x32_bf16 v[86:89], a[168:171], a[24:27], v[58:61]\n"
        "v_add_f32 v254, v254, v118\n"
        "v_add_f32 v254, v254, v119\n"
        "v_add_f32 v254, v254, v120\n"
        "v_mfma_f32_16x16x32_bf16 v[86:89], a[172:175], a[28:31], v[86:89]\n"
        "v_add_f32 v254, v254, v121\n"
        "v_add_f32 v254, v254, v134\n"
        "v_add_f32 v254, v254, v135\n"
        "v_mfma_f32_16x16x32_bf16 v[86:89], a[176:179], a[32:35], v[86:89]\n"
        "buffer_load_dwordx4 v1, s[36:39], s49 offen offset:1024 lds\n"
        "v_add_f32 v254, v254, v136\n"
        "v_add_f32 v254, v254, v137\n"
        "v_mfma_f32_16x16x32_bf16 v[130:133], a[188:191], a[32:35], v[82:85]\n"
        "v_add_f32 v254, v254, v138\n"
        "v_add_f32 v254, v254, v139\n"
        "v_add_f32 v254, v254, v140\n"
        "v_mfma_f32_16x16x32_bf16 v[130:133], a[184:187], a[28:31], v[130:133]\n"
        "v_add_f32 v254, v254, v141\n"
        "v_mov_b32 v253, v254\n"
        "v_add_f32 v255, v34, v35\n"
        "v_mfma_f32_16x16x32_bf16 v[130:133], a[180:183], a[24:27], v[130:133]\n"
        "v_permlane32_swap_b32 v254, v253\n"
        "v_add_f32 v254, v254, v253\n"
        "v_mfma_f32_16x16x32_bf16 v[142:145], a[192:195], a[24:27], v[78:81]\n"
        "v_mov_b32 v253, v254\n"
        "v_add_f32 v255, v255, v36\n"
        "v_add_f32 v255, v255, v37\n"
        "v_mfma_f32_16x16x32_bf16 v[142:145], a[196:199], a[28:31], v[142:145]\n"
        "v_permlane16_swap_b32 v254, v253\n"
        "v_add_f32 v254, v254, v253\n"
        "v_mfma_f32_16x16x32_bf16 v[142:145], a[200:203], a[32:35], v[142:145]\n"
        "buffer_load_dwordx4 v2, s[36:39], s49 offen offset:2048 lds\n"
        "v_mul_f32 v254, 0xbac909aa, v254\n"
        "v_fma_f32 v114, v114, s24, v254\n"
        "v_mfma_f32_16x16x32_bf16 v[146:149], a[212:215], a[32:35], v[30:33]\n"
        "v_fma_f32 v115, v115, s24, v254\n"
        "v_fma_f32 v116, v116, s24, v254\n"
        "v_fma_f32 v117, v117, s24, v254\n"
        "v_mfma_f32_16x16x32_bf16 v[146:149], a[208:211], a[28:31], v[146:149]\n"
        "v_fma_f32 v122, v122, s24, v254\n"
        "v_fma_f32 v123, v123, s24, v254\n"
        "v_fma_f32 v124, v124, s24, v254\n"
        "v_mfma_f32_16x16x32_bf16 v[146:149], a[204:207], a[24:27], v[146:149]\n"
        "v_fma_f32 v125, v125, s24, v254\n"
        "v_fma_f32 v126, v126, s24, v254\n"
        "v_fma_f32 v127, v127, s24, v254\n"
        "v_mfma_f32_16x16x32_bf16 v[90:93], a[144:147], a[36:39], v[46:49]\n"
        "v_fma_f32 v128, v128, s24, v254\n"
        "v_fma_f32 v129, v129, s24, v254\n"
        "v_fma_f32 v118, v118, s24, v254\n"
        "v_mfma_f32_16x16x32_bf16 v[90:93], a[148:151], a[40:43], v[90:93]\n"
        "v_fma_f32 v119, v119, s24, v254\n"
        "v_fma_f32 v120, v120, s24, v254\n"
        "v_fma_f32 v121, v121, s24, v254\n"
        "v_mfma_f32_16x16x32_bf16 v[90:93], a[152:155], a[44:47], v[90:93]\n"
        "buffer_load_dwordx4 v3, s[36:39], s49 offen offset:3072 lds\n"
        "v_fma_f32 v134, v134, s24, v254\n"
        "v_fma_f32 v135, v135, s24, v254\n"
        "v_mfma_f32_16x16x32_bf16 v[94:97], a[164:167], a[44:47], v[50:53]\n"
        "v_fma_f32 v136, v136, s24, v254\n"
        "v_fma_f32 v137, v137, s24, v254\n"
        "v_fma_f32 v138, v138, s24, v254\n"
        "v_mfma_f32_16x16x32_bf16 v[94:97], a[160:163], a[40:43], v[94:97]\n"
        "v_fma_f32 v139, v139, s24, v254\n"
        "v_fma_f32 v140, v140, s24, v254\n"
        "v_fma_f32 v141, v141, s24, v254\n"
        "v_mfma_f32_16x16x32_bf16 v[94:97], a[156:159], a[36:39], v[94:97]\n"
        "v_cvt_pk_bf16_f32 v100, v114, v115\n"
        "v_cvt_pk_bf16_f32 v101, v116, v117\n"
        "v_cvt_pk_bf16_f32 v102, v122, v123\n"
        "v_mfma_f32_16x16x32_bf16 v[106:109], a[168:171], a[36:39], v[58:61]\n"
        "v_cvt_pk_bf16_f32 v103, v124, v125\n"
        "v_cvt_pk_bf16_f32 v98, v126, v127\n"
        "v_cvt_pk_bf16_f32 v99, v128, v129\n"
        "v_mfma_f32_16x16x32_bf16 v[106:109], a[172:175], a[40:43], v[106:109]\n"
        "s_add_u32 m0, s52, 4096\n"
        "ds_write_b128 v40, v[100:103]\n"
        "v_cvt_pk_bf16_f32 v100, v118, v119\n"
        "v_mfma_f32_16x16x32_bf16 v[106:109], a[176:179], a[44:47], v[106:109]\n"
        "buffer_load_dwordx4 v4, s[36:39], s49 offen lds\n"
        "v_cvt_pk_bf16_f32 v101, v120, v121\n"
        "v_cvt_pk_bf16_f32 v110, v134, v135\n"
        "v_mfma_f32_16x16x32_bf16 v[102:105], a[188:191], a[44:47], v[82:85]\n"
        "ds_write_b128 v40, v[98:101] offset:1024\n"
        "v_cvt_pk_bf16_f32 v111, v136, v137\n"
        "v_mfma_f32_16x16x32_bf16 v[102:105], a[184:187], a[40:43], v[102:105]\n"
        "v_cvt_pk_bf16_f32 v112, v138, v139\n"
        "v_cvt_pk_bf16_f32 v113, v140, v141\n"
        "v_add_f32 v255, v255, v54\n"
        "v_mfma_f32_16x16x32_bf16 v[102:105], a[180:183], a[36:39], v[102:105]\n"
        "ds_write_b128 v40, v[110:113] offset:2048\n"
        "v_add_f32 v255, v255, v55\n"
        "v_mfma_f32_16x16x32_bf16 v[110:113], a[192:195], a[36:39], v[78:81]\n"
        "v_add_f32 v255, v255, v56\n"
        "v_add_f32 v255, v255, v57\n"
        "v_add_f32 v255, v255, v86\n"
        "v_mfma_f32_16x16x32_bf16 v[110:113], a[196:199], a[40:43], v[110:113]\n"
        "v_add_f32 v255, v255, v87\n"
        "v_add_f32 v255, v255, v88\n"
        "v_add_f32 v255, v255, v89\n"
        "v_mfma_f32_16x16x32_bf16 v[110:113], a[200:203], a[44:47], v[110:113]\n"
        "buffer_load_dwordx4 v5, s[36:39], s49 offen offset:1024 lds\n"
        "v_add_f32 v255, v255, v130\n"
        "v_add_f32 v255, v255, v131\n"
        "v_mfma_f32_16x16x32_bf16 v[98:101], a[212:215], a[44:47], v[30:33]\n"
        "v_add_f32 v255, v255, v132\n"
        "v_add_f32 v255, v255, v133\n"
        "v_add_f32 v255, v255, v142\n"
        "v_mfma_f32_16x16x32_bf16 v[98:101], a[208:211], a[40:43], v[98:101]\n"
        "v_add_f32 v255, v255, v143\n"
        "v_add_f32 v255, v255, v144\n"
        "v_add_f32 v255, v255, v145\n"
        "v_mfma_f32_16x16x32_bf16 v[98:101], a[204:207], a[36:39], v[98:101]\n"
        "v_add_f32 v255, v255, v146\n"
        "v_add_f32 v255, v255, v147\n"
        "v_add_f32 v255, v255, v148\n"
        "v_mfma_f32_16x16x32_bf16 v[118:121], a[144:147], a[48:51], v[46:49]\n"
        "v_add_f32 v255, v255, v149\n"
        "v_mov_b32 v254, v255\n"
        "v_add_f32 v66, v90, v91\n"
        "v_mfma_f32_16x16x32_bf16 v[118:121], a[148:151], a[52:55], v[118:121]\n"
        "v_permlane32_swap_b32 v255, v254\n"
        "v_add_f32 v255, v255, v254\n"
        "v_mfma_f32_16x16x32_bf16 v[118:121], a[152:155], a[56:59], v[118:121]\n"
        "buffer_load_dwordx4 v6, s[36:39], s49 offen offset:2048 lds\n"
        "v_mov_b32 v254, v255\n"
        "v_add_f32 v66, v66, v92\n"
        "v_mfma_f32_16x16x32_bf16 v[122:125], a[164:167], a[56:59], v[50:53]\n"
        "v_permlane16_swap_b32 v255, v254\n"
        "v_add_f32 v255, v255, v254\n"
        "v_mfma_f32_16x16x32_bf16 v[122:125], a[160:163], a[52:55], v[122:125]\n"
        "v_mul_f32 v255, 0xbac909aa, v255\n"
        "v_add_f32 v66, v66, v93\n"
        "v_fma_f32 v34, v34, s24, v255\n"
        "v_mfma_f32_16x16x32_bf16 v[122:125], a[156:159], a[48:51], v[122:125]\n"
        "v_fma_f32 v35, v35, s24, v255\n"
        "v_fma_f32 v36, v36, s24, v255\n"
        "v_fma_f32 v37, v37, s24, v255\n"
        "v_mfma_f32_16x16x32_bf16 v[114:117], a[168:171], a[48:51], v[58:61]\n"
        "v_fma_f32 v54, v54, s24, v255\n"
        "v_fma_f32 v55, v55, s24, v255\n"
        "v_fma_f32 v56, v56, s24, v255\n"
        "v_mfma_f32_16x16x32_bf16 v[114:117], a[172:175], a[52:55], v[114:117]\n"
        "v_fma_f32 v57, v57, s24, v255\n"
        "v_fma_f32 v86, v86, s24, v255\n"
        "v_fma_f32 v87, v87, s24, v255\n"
        "v_mfma_f32_16x16x32_bf16 v[114:117], a[176:179], a[56:59], v[114:117]\n"
        "buffer_load_dwordx4 v7, s[36:39], s49 offen offset:3072 lds\n"
        "v_fma_f32 v88, v88, s24, v255\n"
        "v_fma_f32 v89, v89, s24, v255\n"
        "v_mfma_f32_16x16x32_bf16 v[126:129], a[188:191], a[56:59], v[82:85]\n"
        "v_fma_f32 v130, v130, s24, v255\n"
        "v_fma_f32 v131, v131, s24, v255\n"
        "v_fma_f32 v132, v132, s24, v255\n"
        "v_mfma_f32_16x16x32_bf16 v[126:129], a[184:187], a[52:55], v[126:129]\n"
        "v_fma_f32 v133, v133, s24, v255\n"
        "v_fma_f32 v142, v142, s24, v255\n"
        "v_fma_f32 v143, v143, s24, v255\n"
        "v_mfma_f32_16x16x32_bf16 v[126:129], a[180:183], a[48:51], v[126:129]\n"
        "v_fma_f32 v144, v144, s24, v255\n"
        "v_fma_f32 v145, v145, s24, v255\n"
        "v_fma_f32 v146, v146, s24, v255\n"
        "v_mfma_f32_16x16x32_bf16 v[134:137], a[192:195], a[48:51], v[78:81]\n"
        "v_fma_f32 v147, v147, s24, v255\n"
        "v_fma_f32 v148, v148, s24, v255\n"
        "v_fma_f32 v149, v149, s24, v255\n"
        "v_mfma_f32_16x16x32_bf16 v[134:137], a[196:199], a[52:55], v[134:137]\n"
        "s_add_u32 m0, s52, 8192\n"
        "v_cvt_pk_bf16_f32 v138, v34, v35\n"
        "v_cvt_pk_bf16_f32 v139, v36, v37\n"
        "v_cvt_pk_bf16_f32 v140, v54, v55\n"
        "v_mfma_f32_16x16x32_bf16 v[134:137], a[200:203], a[56:59], v[134:137]\n"
        "buffer_load_dwordx4 v8, s[36:39], s49 offen lds\n"
        "v_cvt_pk_bf16_f32 v141, v56, v57\n"
        "v_cvt_pk_bf16_f32 v34, v86, v87\n"
        "v_mfma_f32_16x16x32_bf16 v[54:57], a[212:215], a[56:59], v[30:33]\n"
        "ds_write_b128 v41, v[138:141]\n"
        "v_cvt_pk_bf16_f32 v35, v88, v89\n"
        "v_mfma_f32_16x16x32_bf16 v[54:57], a[208:211], a[52:55], v[54:57]\n"
        "v_cvt_pk_bf16_f32 v36, v130, v131\n"
        "v_cvt_pk_bf16_f32 v37, v132, v133\n"
        "v_cvt_pk_bf16_f32 v86, v142, v143\n"
        "v_mfma_f32_16x16x32_bf16 v[54:57], a[204:207], a[48:51], v[54:57]\n"
        "ds_write_b128 v41, v[34:37] offset:1024\n"
        "v_cvt_pk_bf16_f32 v87, v144, v145\n"
        "v_mfma_f32_16x16x32_bf16 v[34:37], a[144:147], a[60:63], v[46:49]\n"
        "v_cvt_pk_bf16_f32 v88, v146, v147\n"
        "v_cvt_pk_bf16_f32 v89, v148, v149\n"
        "v_add_f32 v66, v66, v94\n"
        "v_mfma_f32_16x16x32_bf16 v[34:37], a[148:151], a[64:67], v[34:37]\n"
        "ds_write_b128 v41, v[86:89] offset:2048\n"
        "v_add_f32 v66, v66, v95\n"
        "v_mfma_f32_16x16x32_bf16 v[34:37], a[152:155], a[68:71], v[34:37]\n"
        "v_add_f32 v66, v66, v96\n"
        "v_add_f32 v66, v66, v97\n"
        "v_add_f32 v66, v66, v106\n"
        "v_mfma_f32_16x16x32_bf16 v[46:49], a[164:167], a[68:71], v[50:53]\n"
        "v_add_f32 v66, v66, v107\n"
        "v_add_f32 v66, v66, v108\n"
        "v_add_f32 v66, v66, v109\n"
        "v_mfma_f32_16x16x32_bf16 v[46:49], a[160:163], a[64:67], v[46:49]\n"
        "v_add_f32 v66, v66, v102\n"
        "v_add_f32 v66, v66, v103\n"
        "v_add_f32 v66, v66, v104\n"
        "v_mfma_f32_16x16x32_bf16 v[46:49], a[156:159], a[60:63], v[46:49]\n"
        "v_add_f32 v66, v66, v105\n"
        "v_add_f32 v66, v66, v110\n"
        "v_add_f32 v66, v66, v111\n"
        "v_mfma_f32_16x16x32_bf16 v[50:53], a[168:171], a[60:63], v[58:61]\n"
        "v_add_f32 v66, v66, v112\n"
        "v_add_f32 v66, v66, v113\n"
        "v_add_f32 v66, v66, v98\n"
        "v_mfma_f32_16x16x32_bf16 v[50:53], a[172:175], a[64:67], v[50:53]\n"
        "v_add_f32 v66, v66, v99\n"
        "v_add_f32 v66, v66, v100\n"
        "v_add_f32 v66, v66, v101\n"
        "v_mfma_f32_16x16x32_bf16 v[50:53], a[176:179], a[68:71], v[50:53]\n"
        "v_mov_b32 v67, v66\n"
        "v_add_f32 v72, v118, v119\n"
        "v_add_f32 v72, v72, v120\n"
        "v_mfma_f32_16x16x32_bf16 v[58:61], a[188:191], a[68:71], v[82:85]\n"
        "v_permlane32_swap_b32 v66, v67\n"
        "v_add_f32 v66, v66, v67\n"
        "v_mfma_f32_16x16x32_bf16 v[58:61], a[184:187], a[64:67], v[58:61]\n"
        "v_mov_b32 v67, v66\n"
        "v_add_f32 v72, v72, v121\n"
        "v_add_f32 v72, v72, v122\n"
        "v_mfma_f32_16x16x32_bf16 v[58:61], a[180:183], a[60:63], v[58:61]\n"
        "v_permlane16_swap_b32 v66, v67\n"
        "v_add_f32 v66, v66, v67\n"
        "v_mfma_f32_16x16x32_bf16 v[38:41], a[192:195], a[60:63], v[78:81]\n"
        "v_mul_f32 v66, 0xbac909aa, v66\n"
        "v_add_f32 v72, v72, v123\n"
        "v_add_f32 v72, v72, v124\n"
        "v_mfma_f32_16x16x32_bf16 v[38:41], a[196:199], a[64:67], v[38:41]\n"
        "v_add_f32 v72, v72, v125\n"
        "v_add_f32 v72, v72, v114\n"
        "v_add_f32 v72, v72, v115\n"
        "v_mfma_f32_16x16x32_bf16 v[38:41], a[200:203], a[68:71], v[38:41]\n"
        "v_add_f32 v72, v72, v116\n"
        "v_add_f32 v72, v72, v117\n"
        "v_add_f32 v72, v72, v126\n"
        "v_mfma_f32_16x16x32_bf16 v[78:81], a[212:215], a[68:71], v[30:33]\n"
        "v_add_f32 v72, v72, v127\n"
        "v_add_f32 v72, v72, v128\n"
        "v_add_f32 v72, v72, v129\n"
        "v_mfma_f32_16x16x32_bf16 v[78:81], a[208:211], a[64:67], v[78:81]\n"
        "v_add_f32 v72, v72, v134\n"
        "v_add_f32 v72, v72, v135\n"
        "v_add_f32 v72, v72, v136\n"
        "v_mfma_f32_16x16x32_bf16 v[78:81], a[204:207], a[60:63], v[78:81]\n"
        "v_add_f32 v72, v72, v137\n"
        "v_add_f32 v72, v72, v54\n"
        "v_add_f32 v72, v72, v55\n"
        "v_add_f32 v72, v72, v56\n"
        "v_add_f32 v72, v72, v57\n"
        "v_mov_b32 v73, v72\n"
        "v_add_f32 v130, v34, v35\n"
        "v_add_f32 v130, v130, v36\n"
        "v_permlane32_swap_b32 v72, v73\n"
        "v_add_f32 v72, v72, v73\n"
        "v_mov_b32 v73, v72\n"
        "v_add_f32 v130, v130, v37\n"
        "v_add_f32 v130, v130, v46\n"
        "v_permlane16_swap_b32 v72, v73\n"
        "v_add_f32 v72, v72, v73\n"
        "v_mul_f32 v72, 0xbac909aa, v72\n"
        "v_add_f32 v130, v130, v47\n"
        "v_add_f32 v130, v130, v48\n"
        "v_add_f32 v130, v130, v49\n"
        "v_add_f32 v130, v130, v50\n"
        "v_add_f32 v130, v130, v51\n"
        "v_add_f32 v130, v130, v52\n"
        "v_add_f32 v130, v130, v53\n"
        "v_add_f32 v130, v130, v58\n"
        "v_add_f32 v130, v130, v59\n"
        "v_add_f32 v130, v130, v60\n"
        "v_add_f32 v130, v130, v61\n"
        "v_add_f32 v130, v130, v38\n"
        "v_add_f32 v130, v130, v39\n"
        "v_add_f32 v130, v130, v40\n"
        "v_add_f32 v130, v130, v41\n"
        "v_pk_fma_f32 v[90:91], v[90:91], s[24:25], v[66:67] op_sel_hi:[1,0,0]\n"
        "v_pk_fma_f32 v[92:93], v[92:93], s[24:25], v[66:67] op_sel_hi:[1,0,0]\n"
        "v_pk_fma_f32 v[94:95], v[94:95], s[24:25], v[66:67] op_sel_hi:[1,0,0]\n"
        "v_pk_fma_f32 v[96:97], v[96:97], s[24:25], v[66:67] op_sel_hi:[1,0,0]\n"
        "v_pk_fma_f32 v[106:107], v[106:107], s[24:25], v[66:67] op_sel_hi:[1,0,0]\n"
        "v_pk_fma_f32 v[108:109], v[108:109], s[24:25], v[66:67] op_sel_hi:[1,0,0]\n"
        "v_pk_fma_f32 v[102:103], v[102:103], s[24:25], v[66:67] op_sel_hi:[1,0,0]\n"
        "v_pk_fma_f32 v[104:105], v[104:105], s[24:25], v[66:67] op_sel_hi:[1,0,0]\n"
        "v_pk_fma_f32 v[110:111], v[110:111], s[24:25], v[66:67] op_sel_hi:[1,0,0]\n"
        "v_pk_fma_f32 v[112:113], v[112:113], s[24:25], v[66:67] op_sel_hi:[1,0,0]\n"
        "v_pk_fma_f32 v[98:99], v[98:99], s[24:25], v[66:67] op_sel_hi:[1,0,0]\n"
        "v_pk_fma_f32 v[100:101], v[100:101], s[24:25], v[66:67] op_sel_hi:[1,0,0]\n"
        "v_pk_fma_f32 v[118:119], v[118:119], s[24:25], v[72:73] op_sel_hi:[1,0,0]\n"
        "v_pk_fma_f32 v[120:121], v[120:121], s[24:25], v[72:73] op_sel_hi:[1,0,0]\n"
        "v_pk_fma_f32 v[122:123], v[122:123], s[24:25], v[72:73] op_sel_hi:[1,0,0]\n"
        "v_pk_fma_f32 v[124:125], v[124:125], s[24:25], v[72:73] op_sel_hi:[1,0,0]\n"
        "v_pk_fma_f32 v[114:115], v[114:115], s[24:25], v[72:73] op_sel_hi:[1,0,0]\n"
        "v_pk_fma_f32 v[116:117], v[116:117], s[24:25], v[72:73] op_sel_hi:[1,0,0]\n"
        "v_pk_fma_f32 v[126:127], v[126:127], s[24:25], v[72:73] op_sel_hi:[1,0,0]\n"
        "v_pk_fma_f32 v[128:129], v[128:129], s[24:25], v[72:73] op_sel_hi:[1,0,0]\n"
        "v_pk_fma_f32 v[134:135], v[134:135], s[24:25], v[72:73] op_sel_hi:[1,0,0]\n"
        "v_pk_fma_f32 v[136:137], v[136:137], s[24:25], v[72:73] op_sel_hi:[1,0,0]\n"
        "v_pk_fma_f32 v[54:55], v[54:55], s[24:25], v[72:73] op_sel_hi:[1,0,0]\n"
        "v_pk_fma_f32 v[56:57], v[56:57], s[24:25], v[72:73] op_sel_hi:[1,0,0]\n"
        "v_cvt_pk_bf16_f32 v30, v90, v91\n"
        "v_cvt_pk_bf16_f32 v31, v92, v93\n"
        "v_cvt_pk_bf16_f32 v32, v94, v95\n"
        "v_cvt_pk_bf16_f32 v33, v96, v97\n"
        "v_cvt_pk_bf16_f32 v82, v106, v107\n"
        "v_cvt_pk_bf16_f32 v83, v108, v109\n"
        "v_cvt_pk_bf16_f32 v84, v102, v103\n"
        "v_cvt_pk_bf16_f32 v85, v104, v105\n"
        "v_cvt_pk_bf16_f32 v86, v110, v111\n"
        "v_cvt_pk_bf16_f32 v87, v112, v113\n"
        "v_cvt_pk_bf16_f32 v88, v98, v99\n"
        "v_cvt_pk_bf16_f32 v89, v100, v101\n"
        "v_cvt_pk_bf16_f32 v94, v118, v119\n"
        "v_cvt_pk_bf16_f32 v95, v120, v121\n"
        "v_cvt_pk_bf16_f32 v96, v122, v123\n"
        "v_cvt_pk_bf16_f32 v97, v124, v125\n"
        "v_cvt_pk_bf16_f32 v90, v114, v115\n"
        "v_cvt_pk_bf16_f32 v91, v116, v117\n"
        "v_cvt_pk_bf16_f32 v92, v126, v127\n"
        "v_cvt_pk_bf16_f32 v93, v128, v129\n"
        "v_cvt_pk_bf16_f32 v98, v134, v135\n"
        "v_cvt_pk_bf16_f32 v99, v136, v137\n"
        "v_cvt_pk_bf16_f32 v100, v54, v55\n"
        "v_cvt_pk_bf16_f32 v101, v56, v57\n"
        "v_add_f32 v130, v130, v78\n"
        "ds_write_b128 v42, v[30:33]\n"
        "ds_write_b128 v42, v[82:85] offset:1024\n"
        "ds_write_b128 v42, v[86:89] offset:2048\n"
        "ds_write_b128 v43, v[94:97]\n"
        "ds_write_b128 v43, v[90:93] offset:1024\n"
        "ds_write_b128 v43, v[98:101] offset:2048\n"
        "v_add_f32 v130, v130, v79\n"
        "v_add_f32 v130, v130, v80\n"
        "v_add_f32 v130, v130, v81\n"
        "v_mov_b32 v131, v130\n"
        "s_waitcnt vmcnt(9)\n"
        "s_nop 0\n"
        "v_permlane32_swap_b32 v130, v131\n"
        "v_add_f32 v130, v130, v131\n"
        "v_mov_b32 v131, v130\n"
        "s_nop 1\n"
        "v_permlane16_swap_b32 v130, v131\n"
        "v_add_f32 v130, v130, v131\n"
        "v_mul_f32 v130, 0xbac909aa, v130\n"
        "v_pk_fma_f32 v[34:35], v[34:35], s[24:25], v[130:131] op_sel_hi:[1,0,0]\n"
        "v_pk_fma_f32 v[36:37], v[36:37], s[24:25], v[130:131] op_sel_hi:[1,0,0]\n"
        "v_pk_fma_f32 v[46:47], v[46:47], s[24:25], v[130:131] op_sel_hi:[1,0,0]\n"
        "v_pk_fma_f32 v[48:49], v[48:49], s[24:25], v[130:131] op_sel_hi:[1,0,0]\n"
        "v_pk_fma_f32 v[50:51], v[50:51], s[24:25], v[130:131] op_sel_hi:[1,0,0]\n"
        "v_pk_fma_f32 v[52:53], v[52:53], s[24:25], v[130:131] op_sel_hi:[1,0,0]\n"
        "v_pk_fma_f32 v[58:59], v[58:59], s[24:25], v[130:131] op_sel_hi:[1,0,0]\n"
        "v_pk_fma_f32 v[60:61], v[60:61], s[24:25], v[130:131] op_sel_hi:[1,0,0]\n"
        "v_pk_fma_f32 v[38:39], v[38:39], s[24:25], v[130:131] op_sel_hi:[1,0,0]\n"
        "v_pk_fma_f32 v[40:41], v[40:41], s[24:25], v[130:131] op_sel_hi:[1,0,0]\n"
        "v_pk_fma_f32 v[78:79], v[78:79], s[24:25], v[130:131] op_sel_hi:[1,0,0]\n"
        "v_pk_fma_f32 v[80:81], v[80:81], s[24:25], v[130:131] op_sel_hi:[1,0,0]\n"
        "v_cvt_pk_bf16_f32 v54, v34, v35\n"
        "v_cvt_pk_bf16_f32 v55, v36, v37\n"
        "v_cvt_pk_bf16_f32 v56, v46, v47\n"
        "v_cvt_pk_bf16_f32 v57, v48, v49\n"
        "v_cvt_pk_bf16_f32 v34, v50, v51\n"
        "v_cvt_pk_bf16_f32 v35, v52, v53\n"
        "v_cvt_pk_bf16_f32 v36, v58, v59\n"
        "v_cvt_pk_bf16_f32 v37, v60, v61\n"
        "v_cvt_pk_bf16_f32 v30, v38, v39\n"
        "v_cvt_pk_bf16_f32 v31, v40, v41\n"
        "v_cvt_pk_bf16_f32 v32, v78, v79\n"
        "v_cvt_pk_bf16_f32 v33, v80, v81\n"
        "ds_write_b128 v44, v[54:57]\n"
        "ds_write_b128 v44, v[34:37] offset:1024\n"
        "ds_write_b128 v44, v[30:33] offset:2048\n"
        "s_waitcnt lgkmcnt(0)\n"
        "s_barrier\n"
        "s_branch FE_%=\n"
        "FV_%=:\n"
        "s_bitcmp1_b32 s52, 11\n"
        "s_cbranch_scc1 FG_%=\n"
        "v_mov_b32 v50, 0x3f803f80\n"
        "v_mov_b32 v51, 0x3f803f80\n"
        "v_mov_b32 v52, 0x3f803f80\n"
        "v_mov_b32 v53, 0x3f803f80\n"
        "s_waitcnt vmcnt(15)\n"
        "ds_read_b128 a[0:3], v32\n"
        "ds_read_b128 a[4:7], v32 offset:64\n"
        "ds_read_b128 a[8:11], v32 offset:128\n"
        "s_add_u32 m0, s53, 15360\n"
        "s_nop 0\n"
        "buffer_load_dwordx4 v46, s[16:19], s23 offen lds\n"
        "ds_read_b128 a[12:15], v33\n"
        "ds_read_b128 a[16:19], v33 offset:64\n"
        "ds_read_b128 a[20:23], v33 offset:128\n"
        "buffer_load_dwordx4 v47, s[16:19], s23 offen offset:1024 lds\n"
        "ds_read_b128 a[24:27], v32 offset:6144\n"
        "ds_read_b128 a[28:31], v32 offset:6208\n"
        "ds_read_b128 a[32:35], v32 offset:6272\n"
        "buffer_load_dwordx4 v48, s[16:19], s23 offen offset:2048 lds\n"
        "ds_read_b128 a[36:39], v33 offset:6144\n"
        "ds_read_b128 a[40:43], v33 offset:6208\n"
        "ds_read_b128 a[44:47], v33 offset:6272\n"
        "ds_read_b128 a[48:51], v32 offset:12288\n"
        "ds_read_b128 a[52:55], v32 offset:12352\n"
        "ds_read_b128 a[56:59], v32 offset:12416\n"
        "ds_read_b128 a[60:63], v33 offset:12288\n"
        "ds_read_b128 a[64:67], v33 offset:12352\n"
        "ds_read_b128 a[68:71], v33 offset:12416\n"
        "ds_read_b64_tr_b16 a[72:73], v34 offset:0\n"
        "ds_read_b64_tr_b16 a[74:75], v35 offset:0\n"
        "ds_read_b64_tr_b16 a[76:77], v34 offset:6144\n"
        "ds_read_b64_tr_b16 a[78:79], v35 offset:6144\n"
        "ds_read_b64_tr_b16 a[80:81], v34 offset:12288\n"
        "ds_read_b64_tr_b16 a[82:83], v35 offset:12288\n"
        "s_waitcnt lgkmcnt(15)\n"
        "v_mfma_f32_16x16x32_bf16 v[54:57], a[0:3], v[50:53], 0\n"
        "s_waitcnt vmcnt(12)\n"
        "s_waitcnt vmcnt(6)\n"
        "v_mfma_f32_16x16x32_bf16 v[54:57], a[4:7], v[50:53], v[54:57]\n"
        "s_waitcnt lgkmcnt(15)\n"
        "v_mfma_f32_16x16x32_bf16 v[54:57], a[8:11], v[50:53], v[54:57]\n"
        "s_waitcnt vmcnt(0)\n"
        "s_nop 0\n"
        "v_mfma_f32_16x16x32_bf16 v[58:61], a[20:23], v[50:53], 0\n"
        "s_waitcnt lgkmcnt(15)\n"
        "s_nop 1\n"
        "v_mfma_f32_16x16x32_bf16 v[58:61], a[16:19], v[50:53], v[58:61]\n"
        "s_waitcnt lgkmcnt(12)\n"
        "v_cvt_pk_bf16_f32 v46, v54, v55\n"
        "v_cvt_pk_bf16_f32 v47, v56, v57\n"
        "v_mfma_f32_16x16x32_bf16 v[58:61], a[12:15], v[50:53], v[58:61]\n"
        "s_waitcnt lgkmcnt(9)\n"
        "v_mfma_f32_16x16x32_bf16 v[54:57], a[24:27], v[50:53], 0\n"
        "s_waitcnt lgkmcnt(6)\n"
        "ds_read_b128 a[144:147], v30\n"
        "s_nop 0\n"
        "v_mfma_f32_16x16x32_bf16 v[54:57], a[28:31], v[50:53], v[54:57]\n"
        "s_waitcnt lgkmcnt(1)\n"
        "ds_read_b128 a[148:151], v30 offset:64\n"
        "v_cvt_pk_bf16_f32 v48, v58, v59\n"
        "v_cvt_pk_bf16_f32 v49, v60, v61\n"
        "v_mfma_f32_16x16x32_bf16 v[54:57], a[32:35], v[50:53], v[54:57]\n"
        "s_nop 0\n"
        "ds_read_b128 a[152:155], v30 offset:128\n"
        "v_mfma_f32_16x16x32_bf16 v[58:61], a[44:47], v[50:53], 0\n"
        "s_nop 0\n"
        "ds_read_b128 a[156:159], v31\n"
        "v_mfma_f32_16x16x32_bf16 v[58:61], a[40:43], v[50:53], v[58:61]\n"
        "s_nop 0\n"
        "ds_read_b128 a[160:163], v31 offset:64\n"
        "v_cvt_pk_bf16_f32 v82, v54, v55\n"
        "v_cvt_pk_bf16_f32 v83, v56, v57\n"
        "v_mfma_f32_16x16x32_bf16 v[58:61], a[36:39], v[50:53], v[58:61]\n"
        "s_nop 0\n"
        "ds_read_b128 a[164:167], v31 offset:128\n"
        "v_mfma_f32_16x16x32_bf16 v[54:57], a[48:51], v[50:53], 0\n"
        "s_nop 0\n"
        "ds_read_b128 a[168:171], v30 offset:6144\n"
        "v_mfma_f32_16x16x32_bf16 v[54:57], a[52:55], v[50:53], v[54:57]\n"
        "s_nop 0\n"
        "ds_read_b128 a[172:175], v30 offset:6208\n"
        "v_cvt_pk_bf16_f32 v84, v58, v59\n"
        "v_cvt_pk_bf16_f32 v85, v60, v61\n"
        "v_mfma_f32_16x16x32_bf16 v[54:57], a[56:59], v[50:53], v[54:57]\n"
        "s_nop 0\n"
        "ds_read_b128 a[176:179], v30 offset:6272\n"
        "v_mfma_f32_16x16x32_bf16 v[58:61], a[68:71], v[50:53], 0\n"
        "s_nop 0\n"
        "ds_read_b128 a[180:183], v31 offset:6144\n"
        "v_mfma_f32_16x16x32_bf16 v[58:61], a[64:67], v[50:53], v[58:61]\n"
        "s_nop 0\n"
        "ds_read_b128 a[184:187], v31 offset:6208\n"
        "v_cvt_pk_bf16_f32 v86, v54, v55\n"
        "v_cvt_pk_bf16_f32 v87, v56, v57\n"
        "v_mfma_f32_16x16x32_bf16 v[58:61], a[60:63], v[50:53], v[58:61]\n"
        "ds_read_b128 a[188:191], v31 offset:6272\n"
        "s_waitcnt lgkmcnt(9)\n"
        "v_mfma_f32_16x16x32_bf16 v[50:53], a[144:147], a[72:75], 0\n"
        "s_nop 0\n"
        "ds_read_b128 a[192:195], v30 offset:12288\n"
        "v_mfma_f32_16x16x32_bf16 v[50:53], a[148:151], a[76:79], v[50:53]\n"
        "s_nop 0\n"
        "ds_read_b128 a[196:199], v30 offset:12352\n"
        "v_cvt_pk_bf16_f32 v88, v58, v59\n"
        "v_cvt_pk_bf16_f32 v89, v60, v61\n"
        "v_mfma_f32_16x16x32_bf16 v[50:53], a[152:155], a[80:83], v[50:53]\n"
        "ds_read_b128 a[200:203], v30 offset:12416\n"
        "v_mfma_f32_16x16x32_bf16 v[54:57], a[152:155], v[86:89], 0\n"
        "s_waitcnt lgkmcnt(9)\n"
        "buffer_load_dwordx4 v[62:65], v36, s[36:39], 0 offen\n"
        "v_mfma_f32_16x16x32_bf16 v[54:57], a[148:151], v[82:85], v[54:57]\n"
        "s_nop 0\n"
        "ds_read_b128 a[204:207], v31 offset:12288\n"
        "ds_read_b128 a[208:211], v31 offset:12352\n"
        "v_add_f32 v254, v50, v51\n"
        "v_mfma_f32_16x16x32_bf16 v[54:57], a[144:147], v[46:49], v[54:57]\n"
        "ds_read_b128 a[212:215], v31 offset:12416\n"
        "v_add_f32 v254, v254, v52\n"
        "v_add_f32 v254, v254, v53\n"
        "v_mfma_f32_16x16x32_bf16 v[98:101], a[156:159], a[72:75], 0\n"
        "s_waitcnt lgkmcnt(9)\n"
        "ds_read_b64_tr_b16 a[84:85], v34 offset:8\n"
        "v_mfma_f32_16x16x32_bf16 v[98:101], a[160:163], a[76:79], v[98:101]\n"
        "ds_read_b64_tr_b16 a[86:87], v35 offset:8\n"
        "v_add_f32 v255, v54, v55\n"
        "v_add_f32 v255, v255, v56\n"
        "v_mfma_f32_16x16x32_bf16 v[98:101], a[164:167], a[80:83], v[98:101]\n"
        "ds_read_b64_tr_b16 a[88:89], v34 offset:6152\n"
        "v_add_f32 v255, v255, v57\n"
        "v_mfma_f32_16x16x32_bf16 v[30:33], a[164:167], v[86:89], 0\n"
        "s_waitcnt lgkmcnt(9)\n"
        "ds_read_b64_tr_b16 a[90:91], v35 offset:6152\n"
        "v_mfma_f32_16x16x32_bf16 v[30:33], a[160:163], v[82:85], v[30:33]\n"
        "s_nop 0\n"
        "ds_read_b64_tr_b16 a[92:93], v34 offset:12296\n"
        "v_add_f32 v254, v254, v98\n"
        "v_add_f32 v254, v254, v99\n"
        "v_mfma_f32_16x16x32_bf16 v[30:33], a[156:159], v[46:49], v[30:33]\n"
        "ds_read_b64_tr_b16 a[94:95], v35 offset:12296\n"
        "v_add_f32 v254, v254, v100\n"
        "v_add_f32 v254, v254, v101\n"
        "v_mfma_f32_16x16x32_bf16 v[104:107], a[168:171], a[72:75], 0\n"
        "s_waitcnt lgkmcnt(9)\n"
        "buffer_load_dwordx4 v[68:71], v36, s[36:39], 0 offen offset:16\n"
        "v_mfma_f32_16x16x32_bf16 v[104:107], a[172:175], a[76:79], v[104:107]\n"
        "s_mov_b32 s78, 0xf0000f\n"
        "ds_read_b64_tr_b16 a[96:97], v34 offset:64\n"
        "ds_read_b64_tr_b16 a[98:99], v35 offset:64\n"
        "v_add_f32 v255, v255, v30\n"
        "v_mfma_f32_16x16x32_bf16 v[104:107], a[176:179], a[80:83], v[104:107]\n"
        "s_mov_b32 s79, 0xf0000f00\n"
        "ds_read_b64_tr_b16 a[100:101], v34 offset:6208\n"
        "v_add_f32 v255, v255, v31\n"
        "v_add_f32 v255, v255, v32\n"
        "v_mfma_f32_16x16x32_bf16 v[58:61], a[176:179], v[86:89], 0\n"
        "s_mov_b32 s76, 0x11111111\n"
        "ds_read_b64_tr_b16 a[102:103], v35 offset:6208\n"
        "v_add_f32 v255, v255, v33\n"
        "v_add_f32 v254, v254, v104\n"
        "v_mfma_f32_16x16x32_bf16 v[58:61], a[172:175], v[82:85], v[58:61]\n"
        "s_waitcnt lgkmcnt(10)\n"
        "ds_read_b64_tr_b16 a[104:105], v34 offset:12352\n"
        "v_add_f32 v254, v254, v105\n"
        "v_add_f32 v254, v254, v106\n"
        "v_mfma_f32_16x16x32_bf16 v[58:61], a[168:171], v[46:49], v[58:61]\n"
        "s_mov_b32 s77, 0x11111111\n"
        "ds_read_b64_tr_b16 a[106:107], v35 offset:12352\n"
        "v_add_f32 v254, v254, v107\n"
        "v_mfma_f32_16x16x32_bf16 v[108:111], a[180:183], a[72:75], 0\n"
        "s_mov_b32 s56, 0x22222222\n"
        "ds_read_b64_tr_b16 a[108:109], v34 offset:72\n"
        "v_mfma_f32_16x16x32_bf16 v[108:111], a[184:187], a[76:79], v[108:111]\n"
        "s_mov_b32 s57, 0x22222222\n"
        "ds_read_b64_tr_b16 a[110:111], v35 offset:72\n"
        "v_add_f32 v255, v255, v58\n"
        "v_add_f32 v255, v255, v59\n"
        "v_mfma_f32_16x16x32_bf16 v[108:111], a[188:191], a[80:83], v[108:111]\n"
        "s_mov_b32 s58, 0x44444444\n"
        "ds_read_b64_tr_b16 a[112:113], v34 offset:6216\n"
        "v_add_f32 v255, v255, v60\n"
        "v_add_f32 v255, v255, v61\n"
        "v_mfma_f32_16x16x32_bf16 v[78:81], a[188:191], v[86:89], 0\n"
        "s_waitcnt lgkmcnt(9)\n"
        "buffer_load_dwordx4 v[74:77], v36, s[36:39], 0 offen offset:32\n"
        "v_mfma_f32_16x16x32_bf16 v[78:81], a[184:187], v[82:85], v[78:81]\n"
        "s_mov_b32 s59, 0x44444444\n"
        "ds_read_b64_tr_b16 a[114:115], v35 offset:6216\n"
        "ds_read_b64_tr_b16 a[116:117], v34 offset:12360\n"
        "v_add_f32 v254, v254, v108\n"
        "v_mfma_f32_16x16x32_bf16 v[78:81], a[180:183], v[46:49], v[78:81]\n"
        "s_mov_b32 s60, 0x88888888\n"
        "ds_read_b64_tr_b16 a[118:119], v35 offset:12360\n"
        "v_add_f32 v254, v254, v109\n"
        "v_add_f32 v254, v254, v110\n"
        "v_mfma_f32_16x16x32_bf16 v[112:115], a[192:195], a[72:75], 0\n"
        "s_mov_b32 s61, 0x88888888\n"
        "ds_read_b64_tr_b16 a[120:121], v34 offset:128\n"
        "v_add_f32 v254, v254, v111\n"
        "v_add_f32 v255, v255, v78\n"
        "v_mfma_f32_16x16x32_bf16 v[112:115], a[196:199], a[76:79], v[112:115]\n"
        "s_mov_b32 s62, 0xffff\n"
        "ds_read_b64_tr_b16 a[122:123], v35 offset:128\n"
        "v_add_f32 v255, v255, v79\n"
        "v_add_f32 v255, v255, v80\n"
        "v_mfma_f32_16x16x32_bf16 v[112:115], a[200:203], a[80:83], v[112:115]\n"
        "s_mov_b32 s63, 0x0\n"
        "ds_read_b64_tr_b16 a[124:125], v34 offset:6272\n"
        "v_add_f32 v255, v255, v81\n"
        "v_mfma_f32_16x16x32_bf16 v[126:129], a[200:203], v[86:89], 0\n"
        "s_mov_b32 s64, 0xffff0000\n"
        "ds_read_b64_tr_b16 a[126:127], v35 offset:6272\n"
        "v_mfma_f32_16x16x32_bf16 v[126:129], a[196:199], v[82:85], v[126:129]\n"
        "s_mov_b32 s65, 0x0\n"
        "ds_read_b64_tr_b16 a[128:129], v34 offset:12416\n"
        "v_add_f32 v254, v254, v112\n"
        "v_add_f32 v254, v254, v113\n"
        "v_mfma_f32_16x16x32_bf16 v[126:129], a[192:195], v[46:49], v[126:129]\n"
        "s_mov_b32 s66, 0x0\n"
        "ds_read_b64_tr_b16 a[130:131], v35 offset:12416\n"
        "v_add_f32 v254, v254, v114\n"
        "v_add_f32 v254, v254, v115\n"
        "v_mfma_f32_16x16x32_bf16 v[122:125], a[204:207], a[72:75], 0\n"
        "s_waitcnt lgkmcnt(12)\n"
        "buffer_load_dwordx4 v[176:179], v37, s[36:39], 0 offen\n"
        "v_mfma_f32_16x16x32_bf16 v[122:125], a[208:211], a[76:79], v[122:125]\n"
        "s_mov_b32 s67, 0xffff\n"
        "ds_read_b64_tr_b16 a[132:133], v34 offset:136\n"
        "ds_read_b64_tr_b16 a[134:135], v35 offset:136\n"
        "v_add_f32 v255, v255, v126\n"
        "v_mfma_f32_16x16x32_bf16 v[122:125], a[212:215], a[80:83], v[122:125]\n"
        "s_mov_b32 s68, 0x0\n"
        "ds_read_b64_tr_b16 a[136:137], v34 offset:6280\n"
        "v_add_f32 v255, v255, v127\n"
        "v_add_f32 v255, v255, v128\n"
        "v_mfma_f32_16x16x32_bf16 v[118:121], a[212:215], v[86:89], 0\n"
        "s_mov_b32 s69, 0xffff0000\n"
        "ds_read_b64_tr_b16 a[138:139], v35 offset:6280\n"
        "v_add_f32 v255, v255, v129\n"
        "v_add_f32 v254, v254, v122\n"
        "v_mfma_f32_16x16x32_bf16 v[118:121], a[208:211], v[82:85], v[118:121]\n"
        "s_mov_b32 s70, 0x0\n"
        "ds_read_b64_tr_b16 a[140:141], v34 offset:12424\n"
        "v_add_f32 v254, v254, v123\n"
        "v_add_f32 v254, v254, v124\n"
        "v_mfma_f32_16x16x32_bf16 v[118:121], a[204:207], v[46:49], v[118:121]\n"
        "s_mov_b32 s71, 0xffffffff\n"
        "ds_read_b64_tr_b16 a[142:143], v35 offset:12424\n"
        "v_add_f32 v254, v254, v125\n"
        "v_mov_b32 v253, v254\n"
        "v_mfma_f32_16x16x32_bf16 v[46:49], a[144:147], a[84:87], 0\n"
        "s_mov_b32 s72, 0x0\n"
        "v_permlane32_swap_b32 v254, v253\n"
        "v_add_f32 v254, v254, v253\n"
        "v_mfma_f32_16x16x32_bf16 v[46:49], a[148:151], a[88:91], v[46:49]\n"
        "s_mov_b32 s73, 0x20000\n"
        "v_mov_b32 v253, v254\n"
        "v_add_f32 v255, v255, v118\n"
        "v_add_f32 v255, v255, v119\n"
        "v_mfma_f32_16x16x32_bf16 v[46:49], a[152:155], a[92:95], v[46:49]\n"
        "s_mov_b64 vcc, s[78:79]\n"
        "buffer_load_dwordx4 v[182:185], v37, s[36:39], 0 offen offset:16\n"
        "v_add_f32 v255, v255, v120\n"
        "v_add_f32 v255, v255, v121\n"
        "v_mfma_f32_16x16x32_bf16 v[86:89], a[164:167], a[92:95], 0\n"
        "s_mov_b32 s80, 0x3c2aaaab\n"
        "v_permlane16_swap_b32 v254, v253\n"
        "v_add_f32 v254, v254, v253\n"
        "v_mfma_f32_16x16x32_bf16 v[86:89], a[160:163], a[88:91], v[86:89]\n"
        "s_mov_b32 s81, 0x3727c5ac\n"
        "v_mul_f32 v254, 0xbc2aaaab, v254\n"
        "v_add_f32 v50, v50, v254\n"
        "v_add_f32 v51, v51, v254\n"
        "v_mfma_f32_16x16x32_bf16 v[86:89], a[156:159], a[84:87], v[86:89]\n"
        "s_mov_b32 s82, 0x43800000\n"
        "v_add_f32 v52, v52, v254\n"
        "v_add_f32 v53, v53, v254\n"
        "v_add_f32 v98, v98, v254\n"
        "v_mfma_f32_16x16x32_bf16 v[90:93], a[168:171], a[84:87], 0\n"
        "s_mov_b32 s84, 0x80000000\n"
        "v_add_f32 v99, v99, v254\n"
        "v_add_f32 v100, v100, v254\n"
        "v_add_f32 v101, v101, v254\n"
        "v_mfma_f32_16x16x32_bf16 v[90:93], a[172:175], a[88:91], v[90:93]\n"
        "s_mov_b32 s85, 19\n"
        "v_add_f32 v104, v104, v254\n"
        "v_add_f32 v105, v105, v254\n"
        "v_add_f32 v106, v106, v254\n"
        "v_mfma_f32_16x16x32_bf16 v[90:93], a[176:179], a[92:95], v[90:93]\n"
        "s_waitcnt lgkmcnt(0)\n"
        "v_add_f32 v107, v107, v254\n"
        "v_add_f32 v108, v108, v254\n"
        "v_add_f32 v109, v109, v254\n"
        "v_mfma_f32_16x16x32_bf16 v[82:85], a[188:191], a[92:95], 0\n"
        "s_barrier\n"
        "v_add_f32 v110, v110, v254\n"
        "v_add_f32 v111, v111, v254\n"
        "v_add_f32 v112, v112, v254\n"
        "v_mfma_f32_16x16x32_bf16 v[82:85], a[184:187], a[88:91], v[82:85]\n"
        "s_mov_b32 s86, 0x80000000\n"
        "v_add_f32 v113, v113, v254\n"
        "v_add_f32 v114, v114, v254\n"
        "v_add_f32 v115, v115, v254\n"
        "v_mfma_f32_16x16x32_bf16 v[82:85], a[180:183], a[84:87], v[82:85]\n"
        "s_mov_b32 s89, 0x80000000\n"
        "buffer_load_dwordx4 v[188:191], v37, s[36:39], 0 offen offset:32\n"
        "v_add_f32 v122, v122, v254\n"
        "v_add_f32 v123, v123, v254\n"
        "v_mfma_f32_16x16x32_bf16 v[94:97], a[192:195], a[84:87], 0\n"
        "s_mov_b32 s29, 1\n"
        "v_add_f32 v124, v124, v254\n"
        "v_add_f32 v125, v125, v254\n"
        "v_cvt_pk_bf16_f32 v34, v50, v51\n"
        "v_mfma_f32_16x16x32_bf16 v[94:97], a[196:199], a[88:91], v[94:97]\n"
        "s_mov_b32 s31, 0x400000\n"
        "v_cvt_pk_bf16_f32 v35, v52, v53\n"
        "v_cvt_pk_bf16_f32 v36, v98, v99\n"
        "v_cvt_pk_bf16_f32 v37, v100, v101\n"
        "v_mfma_f32_16x16x32_bf16 v[94:97], a[200:203], a[92:95], v[94:97]\n"
        "s_mov_b32 s87, s49\n"
        "ds_write_b128 v39, v[34:37]\n"
        "v_cvt_pk_bf16_f32 v34, v104, v105\n"
        "v_mfma_f32_16x16x32_bf16 v[102:105], a[212:215], a[92:95], 0\n"
        "s_mov_b32 s88, s50\n"
        "v_cvt_pk_bf16_f32 v35, v106, v107\n"
        "v_cvt_pk_bf16_f32 v36, v108, v109\n"
        "v_cvt_pk_bf16_f32 v37, v110, v111\n"
        "v_mfma_f32_16x16x32_bf16 v[102:105], a[208:211], a[88:91], v[102:105]\n"
        "s_mov_b64 s[44:45], s[40:41]\n"
        "ds_write_b128 v39, v[34:37] offset:1024\n"
        "v_cvt_pk_bf16_f32 v34, v112, v113\n"
        "v_mfma_f32_16x16x32_bf16 v[102:105], a[204:207], a[84:87], v[102:105]\n"
        "s_mov_b32 s46, 0\n"
        "v_cvt_pk_bf16_f32 v35, v114, v115\n"
        "v_cvt_pk_bf16_f32 v36, v122, v123\n"
        "v_cvt_pk_bf16_f32 v37, v124, v125\n"
        "v_mfma_f32_16x16x32_bf16 v[50:53], a[144:147], a[96:99], 0\n"
        "s_mov_b32 s47, s43\n"
        "ds_write_b128 v39, v[34:37] offset:2048\n"
        "v_mov_b32 v254, v255\n"
        "v_mfma_f32_16x16x32_bf16 v[50:53], a[148:151], a[100:103], v[50:53]\n"
        "s_add_u32 m0, s52, 0\n"
        "v_permlane32_swap_b32 v255, v254\n"
        "v_add_f32 v255, v255, v254\n"
        "v_mfma_f32_16x16x32_bf16 v[50:53], a[152:155], a[104:107], v[50:53]\n"
        "s_waitcnt lgkmcnt(3)\n"
        "buffer_load_dwordx4 v0, s[36:39], s49 offen lds\n"
        "v_mov_b32 v254, v255\n"
        "v_add_f32 v253, v46, v47\n"
        "v_mfma_f32_16x16x32_bf16 v[98:101], a[164:167], a[104:107], 0\n"
        "s_waitcnt lgkmcnt(3)\n"
        "v_permlane16_swap_b32 v255, v254\n"
        "v_add_f32 v255, v255, v254\n"
        "v_mfma_f32_16x16x32_bf16 v[98:101], a[160:163], a[100:103], v[98:101]\n"
        "s_waitcnt lgkmcnt(3)\n"
        "v_add_f32 v253, v253, v48\n"
        "v_mul_f32 v255, 0xbc2aaaab, v255\n"
        "v_add_f32 v253, v253, v49\n"
        "v_mfma_f32_16x16x32_bf16 v[98:101], a[156:159], a[96:99], v[98:101]\n"
        "v_add_f32 v54, v54, v255\n"
        "v_add_f32 v55, v55, v255\n"
        "v_add_f32 v56, v56, v255\n"
        "v_mfma_f32_16x16x32_bf16 v[110:113], a[168:171], a[96:99], 0\n"
        "v_add_f32 v57, v57, v255\n"
        "v_add_f32 v30, v30, v255\n"
        "v_add_f32 v31, v31, v255\n"
        "v_mfma_f32_16x16x32_bf16 v[110:113], a[172:175], a[100:103], v[110:113]\n"
        "v_add_f32 v32, v32, v255\n"
        "v_add_f32 v33, v33, v255\n"
        "v_add_f32 v58, v58, v255\n"
        "v_mfma_f32_16x16x32_bf16 v[110:113], a[176:179], a[104:107], v[110:113]\n"
        "buffer_load_dwordx4 v1, s[36:39], s49 offen offset:1024 lds\n"
        "v_add_f32 v59, v59, v255\n"
        "v_add_f32 v60, v60, v255\n"
        "v_mfma_f32_16x16x32_bf16 v[34:37], a[188:191], a[104:107], 0\n"
        "v_add_f32 v61, v61, v255\n"
        "v_add_f32 v78, v78, v255\n"
        "v_add_f32 v79, v79, v255\n"
        "v_mfma_f32_16x16x32_bf16 v[34:37], a[184:187], a[100:103], v[34:37]\n"
        "v_add_f32 v80, v80, v255\n"
        "v_add_f32 v81, v81, v255\n"
        "v_add_f32 v126, v126, v255\n"
        "v_mfma_f32_16x16x32_bf16 v[34:37], a[180:183], a[96:99], v[34:37]\n"
        "v_add_f32 v127, v127, v255\n"
        "v_add_f32 v128, v128, v255\n"
        "v_add_f32 v129, v129, v255\n"
        "v_mfma_f32_16x16x32_bf16 v[106:109], a[192:195], a[96:99], 0\n"
        "v_add_f32 v118, v118, v255\n"
        "v_add_f32 v119, v119, v255\n"
        "v_add_f32 v120, v120, v255\n"
        "v_mfma_f32_16x16x32_bf16 v[106:109], a[196:199], a[100:103], v[106:109]\n"
        "v_add_f32 v121, v121, v255\n"
        "v_cvt_pk_bf16_f32 v122, v54, v55\n"
        "v_cvt_pk_bf16_f32 v123, v56, v57\n"
        "v_mfma_f32_16x16x32_bf16 v[106:109], a[200:203], a[104:107], v[106:109]\n"
        "buffer_load_dwordx4 v2, s[36:39], s49 offen offset:2048 lds\n"
        "v_cvt_pk_bf16_f32 v124, v30, v31\n"
        "v_cvt_pk_bf16_f32 v125, v32, v33\n"
        "v_mfma_f32_16x16x32_bf16 v[114:117], a[212:215], a[104:107], 0\n"
        "ds_write_b128 v38, v[122:125]\n"
        "v_cvt_pk_bf16_f32 v30, v58, v59\n"
        "v_mfma_f32_16x16x32_bf16 v[114:117], a[208:211], a[100:103], v[114:117]\n"
        "v_cvt_pk_bf16_f32 v31, v60, v61\n"
        "v_cvt_pk_bf16_f32 v32, v78, v79\n"
        "v_cvt_pk_bf16_f32 v33, v80, v81\n"
        "v_mfma_f32_16x16x32_bf16 v[114:117], a[204:207], a[96:99], v[114:117]\n"
        "ds_write_b128 v38, v[30:33] offset:64\n"
        "v_cvt_pk_bf16_f32 v54, v126, v127\n"
        "v_mfma_f32_16x16x32_bf16 v[30:33], a[144:147], a[108:111], 0\n"
        "v_cvt_pk_bf16_f32 v55, v128, v129\n"
        "v_cvt_pk_bf16_f32 v56, v118, v119\n"
        "v_cvt_pk_bf16_f32 v57, v120, v121\n"
        "v_mfma_f32_16x16x32_bf16 v[30:33], a[148:151], a[112:115], v[30:33]\n"
        "ds_write_b128 v38, v[54:57] offset:128\n"
        "v_add_f32 v253, v253, v86\n"
        "v_mfma_f32_16x16x32_bf16 v[30:33], a[152:155], a[116:119], v[30:33]\n"
        "buffer_load_dwordx4 v3, s[36:39], s49 offen offset:3072 lds\n"
        "v_add_f32 v253, v253, v87\n"
        "v_add_f32 v253, v253, v88\n"
        "v_mfma_f32_16x16x32_bf16 v[54:57], a[164:167], a[116:119], 0\n"
        "v_add_f32 v253, v253, v89\n"
        "v_add_f32 v253, v253, v90\n"
        "v_add_f32 v253, v253, v91\n"
        "v_mfma_f32_16x16x32_bf16 v[54:57], a[160:163], a[112:115], v[54:57]\n"
        "v_add_f32 v253, v253, v92\n"
        "v_add_f32 v253, v253, v93\n"
        "v_add_f32 v253, v253, v82\n"
        "v_mfma_f32_16x16x32_bf16 v[54:57], a[156:159], a[108:111], v[54:57]\n"
        "v_add_f32 v253, v253, v83\n"
        "v_add_f32 v253, v253, v84\n"
        "v_add_f32 v253, v253, v85\n"
        "v_mfma_f32_16x16x32_bf16 v[118:121], a[168:171], a[108:111], 0\n"
        "v_add_f32 v253, v253, v94\n"
        "v_add_f32 v253, v253, v95\n"
        "v_add_f32 v253, v253, v96\n"
        "v_mfma_f32_16x16x32_bf16 v[118:121], a[172:175], a[112:115], v[118:121]\n"
        "s_add_u32 m0, s52, 4096\n"
        "v_add_f32 v253, v253, v97\n"
        "v_add_f32 v253, v253, v102\n"
        "v_add_f32 v253, v253, v103\n"
        "v_mfma_f32_16x16x32_bf16 v[118:121], a[176:179], a[116:119], v[118:121]\n"
        "buffer_load_dwordx4 v4, s[36:39], s49 offen lds\n"
        "v_add_f32 v253, v253, v104\n"
        "v_add_f32 v253, v253, v105\n"
        "v_mfma_f32_16x16x32_bf16 v[78:81], a[188:191], a[116:119], 0\n"
        "v_mov_b32 v254, v253\n"
        "v_add_f32 v255, v50, v51\n"
        "v_add_f32 v255, v255, v52\n"
        "v_mfma_f32_16x16x32_bf16 v[78:81], a[184:187], a[112:115], v[78:81]\n"
        "v_permlane32_swap_b32 v253, v254\n"
        "v_add_f32 v253, v253, v254\n"
        "v_mfma_f32_16x16x32_bf16 v[78:81], a[180:183], a[108:111], v[78:81]\n"
        "v_mov_b32 v254, v253\n"
        "v_add_f32 v255, v255, v53\n"
        "v_add_f32 v255, v255, v98\n"
        "v_mfma_f32_16x16x32_bf16 v[122:125], a[192:195], a[108:111], 0\n"
        "v_permlane16_swap_b32 v253, v254\n"
        "v_add_f32 v253, v253, v254\n"
        "v_mfma_f32_16x16x32_bf16 v[122:125], a[196:199], a[112:115], v[122:125]\n"
        "v_mul_f32 v253, 0xbc2aaaab, v253\n"
        "v_add_f32 v46, v46, v253\n"
        "v_add_f32 v47, v47, v253\n"
        "v_mfma_f32_16x16x32_bf16 v[122:125], a[200:203], a[116:119], v[122:125]\n"
        "buffer_load_dwordx4 v5, s[36:39], s49 offen offset:1024 lds\n"
        "v_add_f32 v48, v48, v253\n"
        "v_add_f32 v49, v49, v253\n"
        "v_mfma_f32_16x16x32_bf16 v[58:61], a[212:215], a[116:119], 0\n"
        "v_add_f32 v86, v86, v253\n"
        "v_add_f32 v87, v87, v253\n"
        "v_add_f32 v88, v88, v253\n"
        "v_mfma_f32_16x16x32_bf16 v[58:61], a[208:211], a[112:115], v[58:61]\n"
        "v_add_f32 v89, v89, v253\n"
        "v_add_f32 v90, v90, v253\n"
        "v_add_f32 v91, v91, v253\n"
        "v_mfma_f32_16x16x32_bf16 v[58:61], a[204:207], a[108:111], v[58:61]\n"
        "v_add_f32 v92, v92, v253\n"
        "v_add_f32 v93, v93, v253\n"
        "v_add_f32 v82, v82, v253\n"
        "v_mfma_f32_16x16x32_bf16 v[126:129], a[144:147], a[120:123], 0\n"
        "v_add_f32 v83, v83, v253\n"
        "v_add_f32 v84, v84, v253\n"
        "v_add_f32 v85, v85, v253\n"
        "v_mfma_f32_16x16x32_bf16 v[126:129], a[148:151], a[124:127], v[126:129]\n"
        "v_add_f32 v94, v94, v253\n"
        "v_add_f32 v95, v95, v253\n"
        "v_add_f32 v96, v96, v253\n"
        "v_mfma_f32_16x16x32_bf16 v[126:129], a[152:155], a[128:131], v[126:129]\n"
        "buffer_load_dwordx4 v6, s[36:39], s49 offen offset:2048 lds\n"
        "v_add_f32 v97, v97, v253\n"
        "v_add_f32 v102, v102, v253\n"
        "v_mfma_f32_16x16x32_bf16 v[130:133], a[164:167], a[128:131], 0\n"
        "v_add_f32 v103, v103, v253\n"
        "v_add_f32 v104, v104, v253\n"
        "v_add_f32 v105, v105, v253\n"
        "v_mfma_f32_16x16x32_bf16 v[130:133], a[160:163], a[124:127], v[130:133]\n"
        "v_cvt_pk_bf16_f32 v134, v46, v47\n"
        "v_cvt_pk_bf16_f32 v135, v48, v49\n"
        "v_cvt_pk_bf16_f32 v136, v86, v87\n"
        "v_mfma_f32_16x16x32_bf16 v[130:133], a[156:159], a[120:123], v[130:133]\n"
        "v_cvt_pk_bf16_f32 v137, v88, v89\n"
        "v_cvt_pk_bf16_f32 v86, v90, v91\n"
        "v_cvt_pk_bf16_f32 v87, v92, v93\n"
        "v_mfma_f32_16x16x32_bf16 v[46:49], a[168:171], a[120:123], 0\n"
        "ds_write_b128 v40, v[134:137]\n"
        "v_cvt_pk_bf16_f32 v88, v82, v83\n"
        "v_mfma_f32_16x16x32_bf16 v[46:49], a[172:175], a[124:127], v[46:49]\n"
        "v_cvt_pk_bf16_f32 v89, v84, v85\n"
        "v_cvt_pk_bf16_f32 v90, v94, v95\n"
        "v_cvt_pk_bf16_f32 v91, v96, v97\n"
        "v_mfma_f32_16x16x32_bf16 v[46:49], a[176:179], a[128:131], v[46:49]\n"
        "ds_write_b128 v40, v[86:89] offset:1024\n"
        "v_cvt_pk_bf16_f32 v92, v102, v103\n"
        "v_mfma_f32_16x16x32_bf16 v[82:85], a[188:191], a[128:131], 0\n"
        "buffer_load_dwordx4 v7, s[36:39], s49 offen offset:3072 lds\n"
        "v_cvt_pk_bf16_f32 v93, v104, v105\n"
        "v_add_f32 v255, v255, v99\n"
        "v_mfma_f32_16x16x32_bf16 v[82:85], a[184:187], a[124:127], v[82:85]\n"
        "ds_write_b128 v40, v[90:93] offset:2048\n"
        "v_add_f32 v255, v255, v100\n"
        "v_mfma_f32_16x16x32_bf16 v[82:85], a[180:183], a[120:123], v[82:85]\n"
        "v_add_f32 v255, v255, v101\n"
        "v_add_f32 v255, v255, v110\n"
        "v_add_f32 v255, v255, v111\n"
        "v_mfma_f32_16x16x32_bf16 v[86:89], a[192:195], a[120:123], 0\n"
        "v_add_f32 v255, v255, v112\n"
        "v_add_f32 v255, v255, v113\n"
        "v_add_f32 v255, v255, v34\n"
        "v_mfma_f32_16x16x32_bf16 v[86:89], a[196:199], a[124:127], v[86:89]\n"
        "s_add_u32 m0, s52, 8192\n"
        "v_add_f32 v255, v255, v35\n"
        "v_add_f32 v255, v255, v36\n"
        "v_add_f32 v255, v255, v37\n"
        "v_mfma_f32_16x16x32_bf16 v[86:89], a[200:203], a[128:131], v[86:89]\n"
        "buffer_load_dwordx4 v8, s[36:39], s49 offen lds\n"
        "v_add_f32 v255, v255, v106\n"
        "v_add_f32 v255, v255, v107\n"
        "v_mfma_f32_16x16x32_bf16 v[94:97], a[212:215], a[128:131], 0\n"
        "v_add_f32 v255, v255, v108\n"
        "v_add_f32 v255, v255, v109\n"
        "v_add_f32 v255, v255, v114\n"
        "v_mfma_f32_16x16x32_bf16 v[94:97], a[208:211], a[124:127], v[94:97]\n"
        "v_add_f32 v255, v255, v115\n"
        "v_add_f32 v255, v255, v116\n"
        "v_add_f32 v255, v255, v117\n"
        "v_mfma_f32_16x16x32_bf16 v[94:97], a[204:207], a[120:123], v[94:97]\n"
        "v_mov_b32 v254, v255\n"
        "v_add_f32 v38, v30, v31\n"
        "v_add_f32 v38, v38, v32\n"
        "v_mfma_f32_16x16x32_bf16 v[90:93], a[144:147], a[132:135], 0\n"
        "v_permlane32_swap_b32 v255, v254\n"
        "v_add_f32 v255, v255, v254\n"
        "v_mfma_f32_16x16x32_bf16 v[90:93], a[148:151], a[136:139], v[90:93]\n"
        "v_mov_b32 v254, v255\n"
        "v_add_f32 v38, v38, v33\n"
        "v_add_f32 v38, v38, v54\n"
        "v_mfma_f32_16x16x32_bf16 v[90:93], a[152:155], a[140:143], v[90:93]\n"
        "v_permlane16_swap_b32 v255, v254\n"
        "v_add_f32 v255, v255, v254\n"
        "v_mfma_f32_16x16x32_bf16 v[102:105], a[164:167], a[140:143], 0\n"
        "v_mul_f32 v255, 0xbc2aaaab, v255\n"
        "v_add_f32 v50, v50, v255\n"
        "v_add_f32 v51, v51, v255\n"
        "v_mfma_f32_16x16x32_bf16 v[102:105], a[160:163], a[136:139], v[102:105]\n"
        "v_add_f32 v52, v52, v255\n"
        "v_add_f32 v53, v53, v255\n"
        "v_add_f32 v98, v98, v255\n"
        "v_mfma_f32_16x16x32_bf16 v[102:105], a[156:159], a[132:135], v[102:105]\n"
        "v_add_f32 v99, v99, v255\n"
        "v_add_f32 v100, v100, v255\n"
        "v_add_f32 v101, v101, v255\n"
        "v_mfma_f32_16x16x32_bf16 v[134:137], a[168:171], a[132:135], 0\n"
        "v_add_f32 v110, v110, v255\n"
        "v_add_f32 v111, v111, v255\n"
        "v_add_f32 v112, v112, v255\n"
        "v_mfma_f32_16x16x32_bf16 v[134:137], a[172:175], a[136:139], v[134:137]\n"
        "v_add_f32 v113, v113, v255\n"
        "v_add_f32 v34, v34, v255\n"
        "v_add_f32 v35, v35, v255\n"
        "v_mfma_f32_16x16x32_bf16 v[134:137], a[176:179], a[140:143], v[134:137]\n"
        "v_add_f32 v36, v36, v255\n"
        "v_add_f32 v37, v37, v255\n"
        "v_add_f32 v106, v106, v255\n"
        "v_mfma_f32_16x16x32_bf16 v[142:145], a[188:191], a[140:143], 0\n"
        "v_add_f32 v107, v107, v255\n"
        "v_add_f32 v108, v108, v255\n"
        "v_add_f32 v109, v109, v255\n"
        "v_mfma_f32_16x16x32_bf16 v[142:145], a[184:187], a[136:139], v[142:145]\n"
        "v_add_f32 v114, v114, v255\n"
        "v_add_f32 v115, v115, v255\n"
        "v_add_f32 v116, v116, v255\n"
        "v_mfma_f32_16x16x32_bf16 v[142:145], a[180:183], a[132:135], v[142:145]\n"
        "v_add_f32 v117, v117, v255\n"
        "v_cvt_pk_bf16_f32 v138, v50, v51\n"
        "v_cvt_pk_bf16_f32 v139, v52, v53\n"
        "v_mfma_f32_16x16x32_bf16 v[50:53], a[192:195], a[132:135], 0\n"
        "v_cvt_pk_bf16_f32 v140, v98, v99\n"
        "v_cvt_pk_bf16_f32 v141, v100, v101\n"
        "v_cvt_pk_bf16_f32 v98, v110, v111\n"
        "v_mfma_f32_16x16x32_bf16 v[50:53], a[196:199], a[136:139], v[50:53]\n"
        "ds_write_b128 v41, v[138:141]\n"
        "v_cvt_pk_bf16_f32 v99, v112, v113\n"
        "v_mfma_f32_16x16x32_bf16 v[50:53], a[200:203], a[140:143], v[50:53]\n"
        "v_cvt_pk_bf16_f32 v100, v34, v35\n"
        "v_cvt_pk_bf16_f32 v101, v36, v37\n"
        "v_cvt_pk_bf16_f32 v34, v106, v107\n"
        "v_mfma_f32_16x16x32_bf16 v[110:113], a[212:215], a[140:143], 0\n"
        "ds_write_b128 v41, v[98:101] offset:1024\n"
        "v_cvt_pk_bf16_f32 v35, v108, v109\n"
        "v_mfma_f32_16x16x32_bf16 v[110:113], a[208:211], a[136:139], v[110:113]\n"
        "v_cvt_pk_bf16_f32 v36, v114, v115\n"
        "v_cvt_pk_bf16_f32 v37, v116, v117\n"
        "v_add_f32 v38, v38, v55\n"
        "v_mfma_f32_16x16x32_bf16 v[110:113], a[204:207], a[132:135], v[110:113]\n"
        "ds_write_b128 v41, v[34:37] offset:2048\n"
        "v_add_f32 v38, v38, v56\n"
        "v_add_f32 v38, v38, v57\n"
        "v_add_f32 v38, v38, v118\n"
        "v_add_f32 v38, v38, v119\n"
        "v_add_f32 v38, v38, v120\n"
        "v_add_f32 v38, v38, v121\n"
        "v_add_f32 v38, v38, v78\n"
        "v_add_f32 v38, v38, v79\n"
        "v_add_f32 v38, v38, v80\n"
        "v_add_f32 v38, v38, v81\n"
        "v_add_f32 v38, v38, v122\n"
        "v_add_f32 v38, v38, v123\n"
        "v_add_f32 v38, v38, v124\n"
        "v_add_f32 v38, v38, v125\n"
        "v_add_f32 v38, v38, v58\n"
        "v_add_f32 v38, v38, v59\n"
        "v_add_f32 v38, v38, v60\n"
        "v_add_f32 v38, v38, v61\n"
        "v_mov_b32 v39, v38\n"
        "v_add_f32 v34, v126, v127\n"
        "v_add_f32 v34, v34, v128\n"
        "v_permlane32_swap_b32 v38, v39\n"
        "v_add_f32 v38, v38, v39\n"
        "v_mov_b32 v39, v38\n"
        "v_add_f32 v34, v34, v129\n"
        "v_add_f32 v34, v34, v130\n"
        "v_permlane16_swap_b32 v38, v39\n"
        "v_add_f32 v38, v38, v39\n"
        "v_mul_f32 v38, 0xbc2aaaab, v38\n"
        "v_add_f32 v34, v34, v131\n"
        "v_add_f32 v34, v34, v132\n"
        "v_add_f32 v34, v34, v133\n"
        "v_add_f32 v34, v34, v46\n"
        "v_add_f32 v34, v34, v47\n"
        "v_add_f32 v34, v34, v48\n"
        "v_add_f32 v34, v34, v49\n"
        "v_add_f32 v34, v34, v82\n"
        "v_add_f32 v34, v34, v83\n"
        "v_add_f32 v34, v34, v84\n"
        "v_add_f32 v34, v34, v85\n"
        "v_add_f32 v34, v34, v86\n"
        "v_add_f32 v34, v34, v87\n"
        "v_add_f32 v34, v34, v88\n"
        "v_add_f32 v34, v34, v89\n"
        "v_add_f32 v34, v34, v94\n"
        "v_add_f32 v34, v34, v95\n"
        "v_add_f32 v34, v34, v96\n"
        "v_add_f32 v34, v34, v97\n"
        "v_mov_b32 v35, v34\n"
        "v_add_f32 v66, v90, v91\n"
        "v_add_f32 v66, v66, v92\n"
        "v_permlane32_swap_b32 v34, v35\n"
        "v_add_f32 v34, v34, v35\n"
        "v_mov_b32 v35, v34\n"
        "v_add_f32 v66, v66, v93\n"
        "v_add_f32 v66, v66, v102\n"
        "v_permlane16_swap_b32 v34, v35\n"
        "v_add_f32 v34, v34, v35\n"
        "v_mul_f32 v34, 0xbc2aaaab, v34\n"
        "v_add_f32 v66, v66, v103\n"
        "v_add_f32 v66, v66, v104\n"
        "v_add_f32 v66, v66, v105\n"
        "v_add_f32 v66, v66, v134\n"
        "v_add_f32 v66, v66, v135\n"
        "v_add_f32 v66, v66, v136\n"
        "v_add_f32 v66, v66, v137\n"
        "v_add_f32 v66, v66, v142\n"
        "v_add_f32 v66, v66, v143\n"
        "v_add_f32 v66, v66, v144\n"
        "v_add_f32 v66, v66, v145\n"
        "v_add_f32 v66, v66, v50\n"
        "v_add_f32 v66, v66, v51\n"
        "v_add_f32 v66, v66, v52\n"
        "v_add_f32 v66, v66, v53\n"
        "v_pk_add_f32 v[30:31], v[30:31], v[38:39] op_sel_hi:[1,0]\n"
        "v_pk_add_f32 v[32:33], v[32:33], v[38:39] op_sel_hi:[1,0]\n"
        "v_pk_add_f32 v[54:55], v[54:55], v[38:39] op_sel_hi:[1,0]\n"
        "v_pk_add_f32 v[56:57], v[56:57], v[38:39] op_sel_hi:[1,0]\n"
        "v_pk_add_f32 v[118:119], v[118:119], v[38:39] op_sel_hi:[1,0]\n"
        "v_pk_add_f32 v[120:121], v[120:121], v[38:39] op_sel_hi:[1,0]\n"
        "v_pk_add_f32 v[78:79], v[78:79], v[38:39] op_sel_hi:[1,0]\n"
        "v_pk_add_f32 v[80:81], v[80:81], v[38:39] op_sel_hi:[1,0]\n"
        "v_pk_add_f32 v[122:123], v[122:123], v[38:39] op_sel_hi:[1,0]\n"
        "v_pk_add_f32 v[124:125], v[124:125], v[38:39] op_sel_hi:[1,0]\n"
        "v_pk_add_f32 v[58:59], v[58:59], v[38:39] op_sel_hi:[1,0]\n"
        "v_pk_add_f32 v[60:61], v[60:61], v[38:39] op_sel_hi:[1,0]\n"
        "v_pk_add_f32 v[126:127], v[126:127], v[34:35] op_sel_hi:[1,0]\n"
        "v_pk_add_f32 v[128:129], v[128:129], v[34:35] op_sel_hi:[1,0]\n"
        "v_pk_add_f32 v[130:131], v[130:131], v[34:35] op_sel_hi:[1,0]\n"
        "v_pk_add_f32 v[132:133], v[132:133], v[34:35] op_sel_hi:[1,0]\n"
        "v_pk_add_f32 v[46:47], v[46:47], v[34:35] op_sel_hi:[1,0]\n"
        "v_pk_add_f32 v[48:49], v[48:49], v[34:35] op_sel_hi:[1,0]\n"
        "v_pk_add_f32 v[82:83], v[82:83], v[34:35] op_sel_hi:[1,0]\n"
        "v_pk_add_f32 v[84:85], v[84:85], v[34:35] op_sel_hi:[1,0]\n"
        "v_pk_add_f32 v[86:87], v[86:87], v[34:35] op_sel_hi:[1,0]\n"
        "v_pk_add_f32 v[88:89], v[88:89], v[34:35] op_sel_hi:[1,0]\n"
        "v_pk_add_f32 v[94:95], v[94:95], v[34:35] op_sel_hi:[1,0]\n"
        "v_pk_add_f32 v[96:97], v[96:97], v[34:35] op_sel_hi:[1,0]\n"
        "v_cvt_pk_bf16_f32 v34, v30, v31\n"
        "v_cvt_pk_bf16_f32 v35, v32, v33\n"
        "v_cvt_pk_bf16_f32 v36, v54, v55\n"
        "v_cvt_pk_bf16_f32 v37, v56, v57\n"
        "v_cvt_pk_bf16_f32 v54, v118, v119\n"
        "v_cvt_pk_bf16_f32 v55, v120, v121\n"
        "v_cvt_pk_bf16_f32 v56, v78, v79\n"
        "v_cvt_pk_bf16_f32 v57, v80, v81\n"
        "v_cvt_pk_bf16_f32 v38, v122, v123\n"
        "v_cvt_pk_bf16_f32 v39, v124, v125\n"
        "v_cvt_pk_bf16_f32 v40, v58, v59\n"
        "v_cvt_pk_bf16_f32 v41, v60, v61\n"
        "v_cvt_pk_bf16_f32 v58, v126, v127\n"
        "v_cvt_pk_bf16_f32 v59, v128, v129\n"
        "v_cvt_pk_bf16_f32 v60, v130, v131\n"
        "v_cvt_pk_bf16_f32 v61, v132, v133\n"
        "v_cvt_pk_bf16_f32 v78, v46, v47\n"
        "v_cvt_pk_bf16_f32 v79, v48, v49\n"
        "v_cvt_pk_bf16_f32 v80, v82, v83\n"
        "v_cvt_pk_bf16_f32 v81, v84, v85\n"
        "v_cvt_pk_bf16_f32 v30, v86, v87\n"
        "v_cvt_pk_bf16_f32 v31, v88, v89\n"
        "v_cvt_pk_bf16_f32 v32, v94, v95\n"
        "v_cvt_pk_bf16_f32 v33, v96, v97\n"
        "v_add_f32 v66, v66, v110\n"
        "ds_write_b128 v42, v[34:37]\n"
        "ds_write_b128 v42, v[54:57] offset:1024\n"
        "ds_write_b128 v42, v[38:41] offset:2048\n"
        "ds_write_b128 v43, v[58:61]\n"
        "ds_write_b128 v43, v[78:81] offset:1024\n"
        "ds_write_b128 v43, v[30:33] offset:2048\n"
        "v_add_f32 v66, v66, v111\n"
        "v_add_f32 v66, v66, v112\n"
        "v_add_f32 v66, v66, v113\n"
        "v_mov_b32 v67, v66\n"
        "s_waitcnt vmcnt(9)\n"
        "s_nop 0\n"
        "v_permlane32_swap_b32 v66, v67\n"
        "v_add_f32 v66, v66, v67\n"
        "v_mov_b32 v67, v66\n"
        "s_nop 1\n"
        "v_permlane16_swap_b32 v66, v67\n"
        "v_add_f32 v66, v66, v67\n"
        "v_mul_f32 v66, 0xbc2aaaab, v66\n"
        "v_pk_add_f32 v[90:91], v[90:91], v[66:67] op_sel_hi:[1,0]\n"
        "v_pk_add_f32 v[92:93], v[92:93], v[66:67] op_sel_hi:[1,0]\n"
        "v_pk_add_f32 v[102:103], v[102:103], v[66:67] op_sel_hi:[1,0]\n"
        "v_pk_add_f32 v[104:105], v[104:105], v[66:67] op_sel_hi:[1,0]\n"
        "v_pk_add_f32 v[134:135], v[134:135], v[66:67] op_sel_hi:[1,0]\n"
        "v_pk_add_f32 v[136:137], v[136:137], v[66:67] op_sel_hi:[1,0]\n"
        "v_pk_add_f32 v[142:143], v[142:143], v[66:67] op_sel_hi:[1,0]\n"
        "v_pk_add_f32 v[144:145], v[144:145], v[66:67] op_sel_hi:[1,0]\n"
        "v_pk_add_f32 v[50:51], v[50:51], v[66:67] op_sel_hi:[1,0]\n"
        "v_pk_add_f32 v[52:53], v[52:53], v[66:67] op_sel_hi:[1,0]\n"
        "v_pk_add_f32 v[110:111], v[110:111], v[66:67] op_sel_hi:[1,0]\n"
        "v_pk_add_f32 v[112:113], v[112:113], v[66:67] op_sel_hi:[1,0]\n"
        "v_cvt_pk_bf16_f32 v34, v90, v91\n"
        "v_cvt_pk_bf16_f32 v35, v92, v93\n"
        "v_cvt_pk_bf16_f32 v36, v102, v103\n"
        "v_cvt_pk_bf16_f32 v37, v104, v105\n"
        "v_cvt_pk_bf16_f32 v38, v134, v135\n"
        "v_cvt_pk_bf16_f32 v39, v136, v137\n"
        "v_cvt_pk_bf16_f32 v40, v142, v143\n"
        "v_cvt_pk_bf16_f32 v41, v144, v145\n"
        "v_cvt_pk_bf16_f32 v30, v50, v51\n"
        "v_cvt_pk_bf16_f32 v31, v52, v53\n"
        "v_cvt_pk_bf16_f32 v32, v110, v111\n"
        "v_cvt_pk_bf16_f32 v33, v112, v113\n"
        "ds_write_b128 v44, v[34:37]\n"
        "ds_write_b128 v44, v[38:41] offset:1024\n"
        "ds_write_b128 v44, v[30:33] offset:2048\n"
        "s_waitcnt lgkmcnt(0)\n"
        "s_barrier\n"
        "s_branch FE_%=\n"
        "FG_%=:\n"
        "s_mov_b32 s24, 0x471c4000\n"
        "v_mov_b32 v255, 0x3f803f80\n"
        "v_mov_b32 v255, 0x3f803f80\n"
        "v_mov_b32 v255, 0x3f803f80\n"
        "v_mov_b32 v255, 0x3f803f80\n"
        "s_waitcnt vmcnt(15)\n"
        "ds_read_b64_tr_b16 a[72:73], v34 offset:0\n"
        "ds_read_b64_tr_b16 a[74:75], v35 offset:0\n"
        "ds_read_b64_tr_b16 a[76:77], v34 offset:6144\n"
        "ds_read_b64_tr_b16 a[78:79], v35 offset:6144\n"
        "ds_read_b64_tr_b16 a[80:81], v34 offset:12288\n"
        "ds_read_b64_tr_b16 a[82:83], v35 offset:12288\n"
        "s_add_u32 m0, s53, 15360\n"
        "s_nop 0\n"
        "buffer_load_dwordx4 v46, s[16:19], s23 offen lds\n"
        "ds_read_b64_tr_b16 a[84:85], v34 offset:8\n"
        "ds_read_b64_tr_b16 a[86:87], v35 offset:8\n"
        "ds_read_b64_tr_b16 a[88:89], v34 offset:6152\n"
        "ds_read_b64_tr_b16 a[90:91], v35 offset:6152\n"
        "ds_read_b64_tr_b16 a[92:93], v34 offset:12296\n"
        "ds_read_b64_tr_b16 a[94:95], v35 offset:12296\n"
        "buffer_load_dwordx4 v47, s[16:19], s23 offen offset:1024 lds\n"
        "ds_read_b64_tr_b16 a[96:97], v34 offset:64\n"
        "ds_read_b64_tr_b16 a[98:99], v35 offset:64\n"
        "ds_read_b64_tr_b16 a[100:101], v34 offset:6208\n"
        "ds_read_b64_tr_b16 a[102:103], v35 offset:6208\n"
        "ds_read_b64_tr_b16 a[104:105], v34 offset:12352\n"
        "ds_read_b64_tr_b16 a[106:107], v35 offset:12352\n"
        "buffer_load_dwordx4 v48, s[16:19], s23 offen offset:2048 lds\n"
        "ds_read_b64_tr_b16 a[108:109], v34 offset:72\n"
        "ds_read_b64_tr_b16 a[110:111], v35 offset:72\n"
        "ds_read_b64_tr_b16 a[112:113], v34 offset:6216\n"
        "ds_read_b64_tr_b16 a[114:115], v35 offset:6216\n"
        "ds_read_b64_tr_b16 a[116:117], v34 offset:12360\n"
        "ds_read_b64_tr_b16 a[118:119], v35 offset:12360\n"
        "ds_read_b64_tr_b16 a[120:121], v34 offset:128\n"
        "ds_read_b64_tr_b16 a[122:123], v35 offset:128\n"
        "ds_read_b64_tr_b16 a[124:125], v34 offset:6272\n"
        "ds_read_b64_tr_b16 a[126:127], v35 offset:6272\n"
        "ds_read_b64_tr_b16 a[128:129], v34 offset:12416\n"
        "ds_read_b64_tr_b16 a[130:131], v35 offset:12416\n"
        "ds_read_b64_tr_b16 a[132:133], v34 offset:136\n"
        "ds_read_b64_tr_b16 a[134:135], v35 offset:136\n"
        "ds_read_b64_tr_b16 a[136:137], v34 offset:6280\n"
        "ds_read_b64_tr_b16 a[138:139], v35 offset:6280\n"
        "ds_read_b64_tr_b16 a[140:141], v34 offset:12424\n"
        "ds_read_b64_tr_b16 a[142:143], v35 offset:12424\n"
        "s_waitcnt vmcnt(12)\n"
        "ds_read_b128 a[144:147], v30\n"
        "ds_read_b128 a[148:151], v30 offset:64\n"
        "ds_read_b128 a[152:155], v30 offset:128\n"
        "ds_read_b128 a[156:159], v31\n"
        "ds_read_b128 a[160:163], v31 offset:64\n"
        "ds_read_b128 a[164:167], v31 offset:128\n"
        "s_waitcnt lgkmcnt(15)\n"
        "s_waitcnt lgkmcnt(3)\n"
        "v_mfma_f32_16x16x32_bf16 v[50:53], a[144:147], a[72:75], 0\n"
        "s_waitcnt vmcnt(6)\n"
        "ds_read_b128 a[168:171], v30 offset:6144\n"
        "v_mov_b32 v255, 161888\n"
        "v_mfma_f32_16x16x32_bf16 v[50:53], a[148:151], a[76:79], v[50:53]\n"
        "s_waitcnt vmcnt(0)\n"
        "ds_read_b128 a[172:175], v30 offset:6208\n"
        "ds_read_b128 a[176:179], v30 offset:6272\n"
        "v_mfma_f32_16x16x32_bf16 v[50:53], a[152:155], a[80:83], v[50:53]\n"
        "s_waitcnt lgkmcnt(6)\n"
        "ds_read_b128 a[180:183], v31 offset:6144\n"
        "ds_read_b128 a[184:187], v31 offset:6208\n"
        "s_waitcnt lgkmcnt(5)\n"
        "v_mfma_f32_16x16x32_bf16 v[58:61], a[164:167], a[80:83], 0\n"
        "s_waitcnt lgkmcnt(5)\n"
        "ds_read_b128 a[188:191], v31 offset:6272\n"
        "ds_read_b128 a[192:195], v30 offset:12288\n"
        "v_add_f32 v254, v50, v51\n"
        "v_mfma_f32_16x16x32_bf16 v[58:61], a[160:163], a[76:79], v[58:61]\n"
        "s_waitcnt lgkmcnt(7)\n"
        "ds_read_b128 a[196:199], v30 offset:12352\n"
        "ds_read_b128 a[200:203], v30 offset:12416\n"
        "v_add_f32 v254, v254, v52\n"
        "v_mfma_f32_16x16x32_bf16 v[58:61], a[156:159], a[72:75], v[58:61]\n"
        "s_waitcnt lgkmcnt(9)\n"
        "ds_read_b128 a[204:207], v31 offset:12288\n"
        "v_add_f32 v254, v254, v53\n"
        "s_waitcnt lgkmcnt(7)\n"
        "v_mfma_f32_16x16x32_bf16 v[80:83], a[168:171], a[72:75], 0\n"
        "s_waitcnt lgkmcnt(7)\n"
        "ds_read_b128 a[208:211], v31 offset:12352\n"
        "ds_read_b128 a[212:215], v31 offset:12416\n"
        "v_add_f32 v254, v254, v58\n"
        "v_mfma_f32_16x16x32_bf16 v[80:83], a[172:175], a[76:79], v[80:83]\n"
        "s_waitcnt lgkmcnt(9)\n"
        "v_add_f32 v254, v254, v59\n"
        "v_add_f32 v254, v254, v60\n"
        "v_add_f32 v254, v254, v61\n"
        "v_mfma_f32_16x16x32_bf16 v[80:83], a[176:179], a[80:83], v[80:83]\n"
        "s_waitcnt lgkmcnt(6)\n"
        "s_nop 0\n"
        "v_mfma_f32_16x16x32_bf16 v[72:75], a[188:191], a[80:83], 0\n"
        "s_waitcnt lgkmcnt(3)\n"
        "s_nop 1\n"
        "v_mfma_f32_16x16x32_bf16 v[72:75], a[184:187], a[76:79], v[72:75]\n"
        "s_nop 0\n"
        "v_add_f32 v254, v254, v80\n"
        "v_add_f32 v254, v254, v81\n"
        "v_add_f32 v254, v254, v82\n"
        "v_mfma_f32_16x16x32_bf16 v[72:75], a[180:183], a[72:75], v[72:75]\n"
        "s_nop 0\n"
        "v_add_f32 v254, v254, v83\n"
        "v_mfma_f32_16x16x32_bf16 v[88:91], a[192:195], a[72:75], 0\n"
        "s_nop 2\n"
        "v_mfma_f32_16x16x32_bf16 v[88:91], a[196:199], a[76:79], v[88:91]\n"
        "s_nop 0\n"
        "v_add_f32 v254, v254, v72\n"
        "v_add_f32 v254, v254, v73\n"
        "v_add_f32 v254, v254, v74\n"
        "v_mfma_f32_16x16x32_bf16 v[88:91], a[200:203], a[80:83], v[88:91]\n"
        "s_nop 0\n"
        "v_add_f32 v254, v254, v75\n"
        "s_waitcnt lgkmcnt(0)\n"
        "v_mfma_f32_16x16x32_bf16 v[84:87], a[212:215], a[80:83], 0\n"
        "s_nop 1\n"
        "v_mfma_f32_16x16x32_bf16 v[84:87], a[208:211], a[76:79], v[84:87]\n"
        "s_nop 0\n"
        "v_add_f32 v254, v254, v88\n"
        "v_add_f32 v254, v254, v89\n"
        "v_add_f32 v254, v254, v90\n"
        "v_mfma_f32_16x16x32_bf16 v[84:87], a[204:207], a[72:75], v[84:87]\n"
        "s_nop 0\n"
        "v_add_f32 v254, v254, v91\n"
        "v_mfma_f32_16x16x32_bf16 v[30:33], a[144:147], a[84:87], 0\n"
        "s_nop 2\n"
        "v_mfma_f32_16x16x32_bf16 v[30:33], a[148:151], a[88:91], v[30:33]\n"
        "s_nop 0\n"
        "v_add_f32 v254, v254, v84\n"
        "v_add_f32 v254, v254, v85\n"
        "v_add_f32 v254, v254, v86\n"
        "v_mfma_f32_16x16x32_bf16 v[30:33], a[152:155], a[92:95], v[30:33]\n"
        "buffer_load_dwordx4 v[62:65], v36, s[36:39], 0 offen\n"
        "v_add_f32 v254, v254, v87\n"
        "v_mov_b32 v253, v254\n"
        "v_mfma_f32_16x16x32_bf16 v[46:49], a[164:167], a[92:95], 0\n"
        "s_nop 0\n"
        "v_permlane32_swap_b32 v254, v253\n"
        "v_add_f32 v254, v254, v253\n"
        "v_mfma_f32_16x16x32_bf16 v[46:49], a[160:163], a[88:91], v[46:49]\n"
        "v_mov_b32 v253, v254\n"
        "v_add_f32 v252, v30, v31\n"
        "v_add_f32 v252, v252, v32\n"
        "v_mfma_f32_16x16x32_bf16 v[46:49], a[156:159], a[84:87], v[46:49]\n"
        "v_permlane16_swap_b32 v254, v253\n"
        "v_add_f32 v254, v254, v253\n"
        "v_mfma_f32_16x16x32_bf16 v[108:111], a[168:171], a[84:87], 0\n"
        "v_mul_f32 v254, 0xbc2aaaab, v254\n"
        "v_add_f32 v252, v252, v33\n"
        "v_add_f32 v50, v50, v254\n"
        "v_mfma_f32_16x16x32_bf16 v[108:111], a[172:175], a[88:91], v[108:111]\n"
        "v_add_f32 v51, v51, v254\n"
        "v_add_f32 v52, v52, v254\n"
        "v_add_f32 v53, v53, v254\n"
        "v_mfma_f32_16x16x32_bf16 v[108:111], a[176:179], a[92:95], v[108:111]\n"
        "v_add_f32 v58, v58, v254\n"
        "v_add_f32 v59, v59, v254\n"
        "v_add_f32 v60, v60, v254\n"
        "v_mfma_f32_16x16x32_bf16 v[118:121], a[188:191], a[92:95], 0\n"
        "v_add_f32 v61, v61, v254\n"
        "v_add_f32 v80, v80, v254\n"
        "v_add_f32 v81, v81, v254\n"
        "v_mfma_f32_16x16x32_bf16 v[118:121], a[184:187], a[88:91], v[118:121]\n"
        "v_add_f32 v82, v82, v254\n"
        "v_add_f32 v83, v83, v254\n"
        "v_add_f32 v72, v72, v254\n"
        "v_mfma_f32_16x16x32_bf16 v[118:121], a[180:183], a[84:87], v[118:121]\n"
        "buffer_load_dwordx4 v[68:71], v36, s[36:39], 0 offen offset:16\n"
        "v_add_f32 v73, v73, v254\n"
        "v_add_f32 v74, v74, v254\n"
        "v_mfma_f32_16x16x32_bf16 v[122:125], a[192:195], a[84:87], 0\n"
        "s_mov_b32 s78, 0xf0000f\n"
        "v_add_f32 v75, v75, v254\n"
        "v_add_f32 v88, v88, v254\n"
        "v_add_f32 v89, v89, v254\n"
        "v_mfma_f32_16x16x32_bf16 v[122:125], a[196:199], a[88:91], v[122:125]\n"
        "s_mov_b32 s79, 0xf0000f00\n"
        "v_add_f32 v90, v90, v254\n"
        "v_add_f32 v91, v91, v254\n"
        "v_add_f32 v84, v84, v254\n"
        "v_mfma_f32_16x16x32_bf16 v[122:125], a[200:203], a[92:95], v[122:125]\n"
        "s_mov_b32 s76, 0x11111111\n"
        "v_add_f32 v85, v85, v254\n"
        "v_add_f32 v86, v86, v254\n"
        "v_add_f32 v87, v87, v254\n"
        "v_mfma_f32_16x16x32_bf16 v[126:129], a[212:215], a[92:95], 0\n"
        "s_mov_b32 s77, 0x11111111\n"
        "v_cvt_pk_bf16_f32 v54, v50, v51\n"
        "v_cvt_pk_bf16_f32 v55, v52, v53\n"
        "v_cvt_pk_bf16_f32 v56, v58, v59\n"
        "v_mfma_f32_16x16x32_bf16 v[126:129], a[208:211], a[88:91], v[126:129]\n"
        "s_mov_b32 s56, 0x22222222\n"
        "v_cvt_pk_bf16_f32 v57, v60, v61\n"
        "v_cvt_pk_bf16_f32 v78, v80, v81\n"
        "v_cvt_pk_bf16_f32 v79, v82, v83\n"
        "v_mfma_f32_16x16x32_bf16 v[126:129], a[204:207], a[84:87], v[126:129]\n"
        "s_mov_b32 s57, 0x22222222\n"
        "v_cvt_pk_bf16_f32 v80, v72, v73\n"
        "v_cvt_pk_bf16_f32 v81, v74, v75\n"
        "v_cvt_pk_bf16_f32 v50, v88, v89\n"
        "v_mfma_f32_16x16x32_bf16 v[58:61], a[144:147], a[96:99], 0\n"
        "s_mov_b32 s58, 0x44444444\n"
        "v_cvt_pk_bf16_f32 v51, v90, v91\n"
        "v_cvt_pk_bf16_f32 v52, v84, v85\n"
        "v_cvt_pk_bf16_f32 v53, v86, v87\n"
        "v_mfma_f32_16x16x32_bf16 v[58:61], a[148:151], a[100:103], v[58:61]\n"
        "s_mov_b32 s59, 0x44444444\n"
        "v_add_f32 v252, v252, v46\n"
        "v_add_f32 v252, v252, v47\n"
        "v_add_f32 v252, v252, v48\n"
        "v_mfma_f32_16x16x32_bf16 v[58:61], a[152:155], a[104:107], v[58:61]\n"
        "s_mov_b32 s60, 0x88888888\n"
        "buffer_load_dwordx4 v[74:77], v36, s[36:39], 0 offen offset:32\n"
        "v_add_f32 v252, v252, v49\n"
        "v_add_f32 v252, v252, v108\n"
        "v_mfma_f32_16x16x32_bf16 v[82:85], a[164:167], a[104:107], 0\n"
        "s_mov_b32 s61, 0x88888888\n"
        "v_add_f32 v252, v252, v109\n"
        "v_add_f32 v252, v252, v110\n"
        "v_add_f32 v252, v252, v111\n"
        "v_mfma_f32_16x16x32_bf16 v[82:85], a[160:163], a[100:103], v[82:85]\n"
        "s_mov_b32 s62, 0xffff\n"
        "v_add_f32 v252, v252, v118\n"
        "v_add_f32 v252, v252, v119\n"
        "v_add_f32 v252, v252, v120\n"
        "v_mfma_f32_16x16x32_bf16 v[82:85], a[156:159], a[96:99], v[82:85]\n"
        "s_mov_b32 s63, 0x0\n"
        "v_add_f32 v252, v252, v121\n"
        "v_add_f32 v252, v252, v122\n"
        "v_add_f32 v252, v252, v123\n"
        "v_mfma_f32_16x16x32_bf16 v[86:89], a[168:171], a[96:99], 0\n"
        "s_mov_b32 s64, 0xffff0000\n"
        "v_add_f32 v252, v252, v124\n"
        "v_add_f32 v252, v252, v125\n"
        "v_add_f32 v252, v252, v126\n"
        "v_mfma_f32_16x16x32_bf16 v[86:89], a[172:175], a[100:103], v[86:89]\n"
        "s_mov_b32 s65, 0x0\n"
        "v_add_f32 v252, v252, v127\n"
        "v_add_f32 v252, v252, v128\n"
        "v_add_f32 v252, v252, v129\n"
        "v_mfma_f32_16x16x32_bf16 v[86:89], a[176:179], a[104:107], v[86:89]\n"
        "s_barrier\n"
        "ds_write_b128 v39, v[54:57]\n"
        "v_mov_b32 v254, v252\n"
        "v_mfma_f32_16x16x32_bf16 v[94:97], a[188:191], a[104:107], 0\n"
        "s_mov_b32 s66, 0x0\n"
        "ds_write_b128 v39, v[78:81] offset:1024\n"
        "v_add_f32 v253, v58, v59\n"
        "v_mfma_f32_16x16x32_bf16 v[94:97], a[184:187], a[100:103], v[94:97]\n"
        "s_mov_b32 s67, 0xffff\n"
        "ds_write_b128 v39, v[50:53] offset:2048\n"
        "v_add_f32 v253, v253, v60\n"
        "v_mfma_f32_16x16x32_bf16 v[94:97], a[180:183], a[96:99], v[94:97]\n"
        "s_mov_b32 s68, 0x0\n"
        "buffer_load_dwordx4 v[176:179], v37, s[36:39], 0 offen\n"
        "v_add_f32 v253, v253, v61\n"
        "v_add_f32 v253, v253, v82\n"
        "v_mfma_f32_16x16x32_bf16 v[134:137], a[192:195], a[96:99], 0\n"
        "s_mov_b32 s69, 0xffff0000\n"
        "v_permlane32_swap_b32 v252, v254\n"
        "v_add_f32 v252, v252, v254\n"
        "v_mfma_f32_16x16x32_bf16 v[134:137], a[196:199], a[100:103], v[134:137]\n"
        "s_mov_b32 s70, 0x0\n"
        "v_mov_b32 v254, v252\n"
        "v_add_f32 v253, v253, v83\n"
        "v_add_f32 v253, v253, v84\n"
        "v_mfma_f32_16x16x32_bf16 v[134:137], a[200:203], a[104:107], v[134:137]\n"
        "s_mov_b32 s71, 0xffffffff\n"
        "v_permlane16_swap_b32 v252, v254\n"
        "v_add_f32 v252, v252, v254\n"
        "v_mfma_f32_16x16x32_bf16 v[130:133], a[212:215], a[104:107], 0\n"
        "s_mov_b32 s72, 0x0\n"
        "v_mul_f32 v252, 0xbc2aaaab, v252\n"
        "v_add_f32 v30, v30, v252\n"
        "v_add_f32 v31, v31, v252\n"
        "v_mfma_f32_16x16x32_bf16 v[130:133], a[208:211], a[100:103], v[130:133]\n"
        "s_mov_b32 s73, 0x20000\n"
        "v_add_f32 v32, v32, v252\n"
        "v_add_f32 v33, v33, v252\n"
        "v_add_f32 v46, v46, v252\n"
        "v_mfma_f32_16x16x32_bf16 v[130:133], a[204:207], a[96:99], v[130:133]\n"
        "s_mov_b64 vcc, s[78:79]\n"
        "v_add_f32 v47, v47, v252\n"
        "v_add_f32 v48, v48, v252\n"
        "v_add_f32 v49, v49, v252\n"
        "v_mfma_f32_16x16x32_bf16 v[90:93], a[144:147], a[108:111], 0\n"
        "s_mov_b32 s80, 0x3c2aaaab\n"
        "v_add_f32 v108, v108, v252\n"
        "v_add_f32 v109, v109, v252\n"
        "v_add_f32 v110, v110, v252\n"
        "v_mfma_f32_16x16x32_bf16 v[90:93], a[148:151], a[112:115], v[90:93]\n"
        "s_mov_b32 s81, 0x3727c5ac\n"
        "v_add_f32 v111, v111, v252\n"
        "v_add_f32 v118, v118, v252\n"
        "v_add_f32 v119, v119, v252\n"
        "v_mfma_f32_16x16x32_bf16 v[90:93], a[152:155], a[116:119], v[90:93]\n"
        "s_mov_b32 s82, 0x43800000\n"
        "buffer_load_dwordx4 v[182:185], v37, s[36:39], 0 offen offset:16\n"
        "v_add_f32 v120, v120, v252\n"
        "v_add_f32 v121, v121, v252\n"
        "v_mfma_f32_16x16x32_bf16 v[98:101], a[164:167], a[116:119], 0\n"
        "s_mov_b32 s84, 0x80000000\n"
        "v_add_f32 v122, v122, v252\n"
        "v_add_f32 v123, v123, v252\n"
        "v_add_f32 v124, v124, v252\n"
        "v_mfma_f32_16x16x32_bf16 v[98:101], a[160:163], a[112:115], v[98:101]\n"
        "s_mov_b32 s85, 19\n"
        "v_add_f32 v125, v125, v252\n"
        "v_add_f32 v126, v126, v252\n"
        "v_add_f32 v127, v127, v252\n"
        "v_mfma_f32_16x16x32_bf16 v[98:101], a[156:159], a[108:111], v[98:101]\n"
        "s_mov_b32 s86, 0x80000000\n"
        "v_add_f32 v128, v128, v252\n"
        "v_add_f32 v129, v129, v252\n"
        "v_cvt_pk_bf16_f32 v102, v30, v31\n"
        "v_mfma_f32_16x16x32_bf16 v[114:117], a[168:171], a[108:111], 0\n"
        "s_mov_b32 s89, 0x80000000\n"
        "v_cvt_pk_bf16_f32 v103, v32, v33\n"
        "v_cvt_pk_bf16_f32 v104, v46, v47\n"
        "v_cvt_pk_bf16_f32 v105, v48, v49\n"
        "v_mfma_f32_16x16x32_bf16 v[114:117], a[172:175], a[112:115], v[114:117]\n"
        "s_mov_b32 s29, 1\n"
        "ds_write_b128 v40, v[102:105]\n"
        "v_cvt_pk_bf16_f32 v106, v108, v109\n"
        "v_mfma_f32_16x16x32_bf16 v[114:117], a[176:179], a[116:119], v[114:117]\n"
        "s_mov_b32 s31, 0x400000\n"
        "v_cvt_pk_bf16_f32 v107, v110, v111\n"
        "v_cvt_pk_bf16_f32 v108, v118, v119\n"
        "v_cvt_pk_bf16_f32 v109, v120, v121\n"
        "v_mfma_f32_16x16x32_bf16 v[110:113], a[188:191], a[116:119], 0\n"
        "s_mov_b32 s87, s49\n"
        "v_cvt_pk_bf16_f32 v46, v122, v123\n"
        "v_cvt_pk_bf16_f32 v47, v124, v125\n"
        "v_cvt_pk_bf16_f32 v48, v126, v127\n"
        "v_mfma_f32_16x16x32_bf16 v[110:113], a[184:187], a[112:115], v[110:113]\n"
        "s_mov_b32 s88, s50\n"
        "v_cvt_pk_bf16_f32 v49, v128, v129\n"
        "v_add_f32 v253, v253, v85\n"
        "v_add_f32 v253, v253, v86\n"
        "v_mfma_f32_16x16x32_bf16 v[110:113], a[180:183], a[108:111], v[110:113]\n"
        "s_mov_b64 s[44:45], s[40:41]\n"
        "buffer_load_dwordx4 v[188:191], v37, s[36:39], 0 offen offset:32\n"
        "v_add_f32 v253, v253, v87\n"
        "v_add_f32 v253, v253, v88\n"
        "v_mfma_f32_16x16x32_bf16 v[118:121], a[192:195], a[108:111], 0\n"
        "s_mov_b32 s46, 0\n"
        "v_add_f32 v253, v253, v89\n"
        "v_add_f32 v253, v253, v94\n"
        "v_add_f32 v253, v253, v95\n"
        "v_mfma_f32_16x16x32_bf16 v[118:121], a[196:199], a[112:115], v[118:121]\n"
        "s_mov_b32 s47, s43\n"
        "v_add_f32 v253, v253, v96\n"
        "v_add_f32 v253, v253, v97\n"
        "v_add_f32 v253, v253, v134\n"
        "v_mfma_f32_16x16x32_bf16 v[118:121], a[200:203], a[116:119], v[118:121]\n"
        "v_add_f32 v253, v253, v135\n"
        "v_add_f32 v253, v253, v136\n"
        "v_add_f32 v253, v253, v137\n"
        "v_mfma_f32_16x16x32_bf16 v[30:33], a[212:215], a[116:119], 0\n"
        "v_add_f32 v253, v253, v130\n"
        "v_add_f32 v253, v253, v131\n"
        "v_add_f32 v253, v253, v132\n"
        "v_mfma_f32_16x16x32_bf16 v[30:33], a[208:211], a[112:115], v[30:33]\n"
        "v_add_f32 v253, v253, v133\n"
        "v_mov_b32 v254, v253\n"
        "v_add_f32 v38, v90, v91\n"
        "v_mfma_f32_16x16x32_bf16 v[30:33], a[204:207], a[108:111], v[30:33]\n"
        "v_permlane32_swap_b32 v253, v254\n"
        "v_add_f32 v253, v253, v254\n"
        "v_mfma_f32_16x16x32_bf16 v[122:125], a[144:147], a[120:123], 0\n"
        "v_mov_b32 v254, v253\n"
        "v_add_f32 v38, v38, v92\n"
        "v_add_f32 v38, v38, v93\n"
        "v_mfma_f32_16x16x32_bf16 v[122:125], a[148:151], a[124:127], v[122:125]\n"
        "s_add_u32 m0, s52, 0\n"
        "v_permlane16_swap_b32 v253, v254\n"
        "v_add_f32 v253, v253, v254\n"
        "v_mfma_f32_16x16x32_bf16 v[122:125], a[152:155], a[128:131], v[122:125]\n"
        "buffer_load_dwordx4 v0, s[36:39], s49 offen lds\n"
        "v_mul_f32 v253, 0xbc2aaaab, v253\n"
        "v_add_f32 v58, v58, v253\n"
        "v_mfma_f32_16x16x32_bf16 v[34:37], a[164:167], a[128:131], 0\n"
        "v_add_f32 v59, v59, v253\n"
        "v_add_f32 v60, v60, v253\n"
        "v_add_f32 v61, v61, v253\n"
        "v_mfma_f32_16x16x32_bf16 v[34:37], a[160:163], a[124:127], v[34:37]\n"
        "v_add_f32 v82, v82, v253\n"
        "v_add_f32 v83, v83, v253\n"
        "v_add_f32 v84, v84, v253\n"
        "v_mfma_f32_16x16x32_bf16 v[34:37], a[156:159], a[120:123], v[34:37]\n"
        "v_add_f32 v85, v85, v253\n"
        "v_add_f32 v86, v86, v253\n"
        "v_add_f32 v87, v87, v253\n"
        "v_mfma_f32_16x16x32_bf16 v[146:149], a[168:171], a[120:123], 0\n"
        "v_add_f32 v88, v88, v253\n"
        "v_add_f32 v89, v89, v253\n"
        "v_add_f32 v94, v94, v253\n"
        "v_mfma_f32_16x16x32_bf16 v[146:149], a[172:175], a[124:127], v[146:149]\n"
        "v_add_f32 v95, v95, v253\n"
        "v_add_f32 v96, v96, v253\n"
        "v_add_f32 v97, v97, v253\n"
        "v_mfma_f32_16x16x32_bf16 v[146:149], a[176:179], a[128:131], v[146:149]\n"
        "buffer_load_dwordx4 v1, s[36:39], s49 offen offset:1024 lds\n"
        "v_add_f32 v134, v134, v253\n"
        "v_add_f32 v135, v135, v253\n"
        "v_mfma_f32_16x16x32_bf16 v[138:141], a[188:191], a[128:131], 0\n"
        "v_add_f32 v136, v136, v253\n"
        "v_add_f32 v137, v137, v253\n"
        "v_add_f32 v130, v130, v253\n"
        "v_mfma_f32_16x16x32_bf16 v[138:141], a[184:187], a[124:127], v[138:141]\n"
        "v_add_f32 v131, v131, v253\n"
        "v_add_f32 v132, v132, v253\n"
        "v_add_f32 v133, v133, v253\n"
        "v_mfma_f32_16x16x32_bf16 v[138:141], a[180:183], a[120:123], v[138:141]\n"
        "v_cvt_pk_bf16_f32 v126, v58, v59\n"
        "v_cvt_pk_bf16_f32 v127, v60, v61\n"
        "v_cvt_pk_bf16_f32 v128, v82, v83\n"
        "v_mfma_f32_16x16x32_bf16 v[142:145], a[192:195], a[120:123], 0\n"
        "v_cvt_pk_bf16_f32 v129, v84, v85\n"
        "v_cvt_pk_bf16_f32 v58, v86, v87\n"
        "v_cvt_pk_bf16_f32 v59, v88, v89\n"
        "v_mfma_f32_16x16x32_bf16 v[142:145], a[196:199], a[124:127], v[142:145]\n"
        "v_cvt_pk_bf16_f32 v60, v94, v95\n"
        "v_cvt_pk_bf16_f32 v61, v96, v97\n"
        "v_cvt_pk_bf16_f32 v82, v134, v135\n"
        "v_mfma_f32_16x16x32_bf16 v[142:145], a[200:203], a[128:131], v[142:145]\n"
        "buffer_load_dwordx4 v2, s[36:39], s49 offen offset:2048 lds\n"
        "v_cvt_pk_bf16_f32 v83, v136, v137\n"
        "v_cvt_pk_bf16_f32 v84, v130, v131\n"
        "v_mfma_f32_16x16x32_bf16 v[134:137], a[212:215], a[128:131], 0\n"
        "v_cvt_pk_bf16_f32 v85, v132, v133\n"
        "v_add_f32 v38, v38, v98\n"
        "v_add_f32 v38, v38, v99\n"
        "v_mfma_f32_16x16x32_bf16 v[134:137], a[208:211], a[124:127], v[134:137]\n"
        "v_add_f32 v38, v38, v100\n"
        "v_add_f32 v38, v38, v101\n"
        "v_add_f32 v38, v38, v114\n"
        "v_mfma_f32_16x16x32_bf16 v[134:137], a[204:207], a[120:123], v[134:137]\n"
        "v_add_f32 v38, v38, v115\n"
        "v_add_f32 v38, v38, v116\n"
        "v_add_f32 v38, v38, v117\n"
        "v_mfma_f32_16x16x32_bf16 v[86:89], a[144:147], a[132:135], 0\n"
        "v_add_f32 v38, v38, v110\n"
        "v_add_f32 v38, v38, v111\n"
        "v_add_f32 v38, v38, v112\n"
        "v_mfma_f32_16x16x32_bf16 v[86:89], a[148:151], a[136:139], v[86:89]\n"
        "v_add_f32 v38, v38, v113\n"
        "v_add_f32 v38, v38, v118\n"
        "v_add_f32 v38, v38, v119\n"
        "v_mfma_f32_16x16x32_bf16 v[86:89], a[152:155], a[140:143], v[86:89]\n"
        "buffer_load_dwordx4 v3, s[36:39], s49 offen offset:3072 lds\n"
        "v_add_f32 v38, v38, v120\n"
        "v_add_f32 v38, v38, v121\n"
        "v_mfma_f32_16x16x32_bf16 v[130:133], a[164:167], a[140:143], 0\n"
        "v_add_f32 v38, v38, v30\n"
        "v_add_f32 v38, v38, v31\n"
        "v_add_f32 v38, v38, v32\n"
        "v_mfma_f32_16x16x32_bf16 v[130:133], a[160:163], a[136:139], v[130:133]\n"
        "v_add_f32 v38, v38, v33\n"
        "v_mov_b32 v39, v38\n"
        "v_add_f32 v66, v122, v123\n"
        "v_mfma_f32_16x16x32_bf16 v[130:133], a[156:159], a[132:135], v[130:133]\n"
        "v_permlane32_swap_b32 v38, v39\n"
        "v_add_f32 v38, v38, v39\n"
        "v_mfma_f32_16x16x32_bf16 v[154:157], a[168:171], a[132:135], 0\n"
        "v_mov_b32 v39, v38\n"
        "v_add_f32 v66, v66, v124\n"
        "v_add_f32 v66, v66, v125\n"
        "v_mfma_f32_16x16x32_bf16 v[154:157], a[172:175], a[136:139], v[154:157]\n"
        "s_add_u32 m0, s52, 4096\n"
        "v_permlane16_swap_b32 v38, v39\n"
        "v_add_f32 v38, v38, v39\n"
        "v_mfma_f32_16x16x32_bf16 v[154:157], a[176:179], a[140:143], v[154:157]\n"
        "buffer_load_dwordx4 v4, s[36:39], s49 offen lds\n"
        "v_mul_f32 v38, 0xbc2aaaab, v38\n"
        "v_add_f32 v66, v66, v34\n"
        "v_mfma_f32_16x16x32_bf16 v[94:97], a[188:191], a[140:143], 0\n"
        "v_add_f32 v66, v66, v35\n"
        "v_add_f32 v66, v66, v36\n"
        "v_add_f32 v66, v66, v37\n"
        "v_mfma_f32_16x16x32_bf16 v[94:97], a[184:187], a[136:139], v[94:97]\n"
        "v_add_f32 v66, v66, v146\n"
        "v_add_f32 v66, v66, v147\n"
        "v_add_f32 v66, v66, v148\n"
        "v_mfma_f32_16x16x32_bf16 v[94:97], a[180:183], a[132:135], v[94:97]\n"
        "v_add_f32 v66, v66, v149\n"
        "v_add_f32 v66, v66, v138\n"
        "v_add_f32 v66, v66, v139\n"
        "v_mfma_f32_16x16x32_bf16 v[158:161], a[192:195], a[132:135], 0\n"
        "v_add_f32 v66, v66, v140\n"
        "v_add_f32 v66, v66, v141\n"
        "v_add_f32 v66, v66, v142\n"
        "v_mfma_f32_16x16x32_bf16 v[158:161], a[196:199], a[136:139], v[158:161]\n"
        "v_add_f32 v66, v66, v143\n"
        "v_add_f32 v66, v66, v144\n"
        "v_add_f32 v66, v66, v145\n"
        "v_mfma_f32_16x16x32_bf16 v[158:161], a[200:203], a[140:143], v[158:161]\n"
        "buffer_load_dwordx4 v5, s[36:39], s49 offen offset:1024 lds\n"
        "v_add_f32 v66, v66, v134\n"
        "v_add_f32 v66, v66, v135\n"
        "v_mfma_f32_16x16x32_bf16 v[150:153], a[212:215], a[140:143], 0\n"
        "v_add_f32 v66, v66, v136\n"
        "v_add_f32 v66, v66, v137\n"
        "v_mov_b32 v67, v66\n"
        "v_mfma_f32_16x16x32_bf16 v[150:153], a[208:211], a[136:139], v[150:153]\n"
        "ds_write_b128 v40, v[106:109] offset:1024\n"
        "v_add_f32 v72, v86, v87\n"
        "v_mfma_f32_16x16x32_bf16 v[150:153], a[204:207], a[132:135], v[150:153]\n"
        "ds_write_b128 v40, v[46:49] offset:2048\n"
        "v_add_f32 v72, v72, v88\n"
        "v_pk_add_f32 v[90:91], v[90:91], v[38:39] op_sel_hi:[1,0]\n"
        "v_cvt_pk_bf16_f32 v162, v90, v91\n"
        "v_pk_add_f32 v[92:93], v[92:93], v[38:39] op_sel_hi:[1,0]\n"
        "v_cvt_pk_bf16_f32 v163, v92, v93\n"
        "v_pk_add_f32 v[98:99], v[98:99], v[38:39] op_sel_hi:[1,0]\n"
        "v_cvt_pk_bf16_f32 v164, v98, v99\n"
        "v_pk_add_f32 v[100:101], v[100:101], v[38:39] op_sel_hi:[1,0]\n"
        "v_cvt_pk_bf16_f32 v165, v100, v101\n"
        "v_pk_add_f32 v[114:115], v[114:115], v[38:39] op_sel_hi:[1,0]\n"
        "v_cvt_pk_bf16_f32 v98, v114, v115\n"
        "v_pk_add_f32 v[116:117], v[116:117], v[38:39] op_sel_hi:[1,0]\n"
        "v_cvt_pk_bf16_f32 v99, v116, v117\n"
        "v_pk_add_f32 v[110:111], v[110:111], v[38:39] op_sel_hi:[1,0]\n"
        "v_cvt_pk_bf16_f32 v100, v110, v111\n"
        "v_pk_add_f32 v[112:113], v[112:113], v[38:39] op_sel_hi:[1,0]\n"
        "v_cvt_pk_bf16_f32 v101, v112, v113\n"
        "v_pk_add_f32 v[118:119], v[118:119], v[38:39] op_sel_hi:[1,0]\n"
        "v_cvt_pk_bf16_f32 v90, v118, v119\n"
        "v_pk_add_f32 v[120:121], v[120:121], v[38:39] op_sel_hi:[1,0]\n"
        "v_cvt_pk_bf16_f32 v91, v120, v121\n"
        "v_pk_add_f32 v[30:31], v[30:31], v[38:39] op_sel_hi:[1,0]\n"
        "v_cvt_pk_bf16_f32 v92, v30, v31\n"
        "v_pk_add_f32 v[32:33], v[32:33], v[38:39] op_sel_hi:[1,0]\n"
        "v_cvt_pk_bf16_f32 v93, v32, v33\n"
        "v_permlane32_swap_b32 v66, v67\n"
        "v_add_f32 v66, v66, v67\n"
        "v_mov_b32 v67, v66\n"
        "s_nop 1\n"
        "v_permlane16_swap_b32 v66, v67\n"
        "v_add_f32 v66, v66, v67\n"
        "v_mul_f32 v66, 0xbc2aaaab, v66\n"
        "v_pk_add_f32 v[122:123], v[122:123], v[66:67] op_sel_hi:[1,0]\n"
        "v_cvt_pk_bf16_f32 v110, v122, v123\n"
        "v_pk_add_f32 v[124:125], v[124:125], v[66:67] op_sel_hi:[1,0]\n"
        "v_cvt_pk_bf16_f32 v111, v124, v125\n"
        "v_pk_add_f32 v[34:35], v[34:35], v[66:67] op_sel_hi:[1,0]\n"
        "v_cvt_pk_bf16_f32 v112, v34, v35\n"
        "v_pk_add_f32 v[36:37], v[36:37], v[66:67] op_sel_hi:[1,0]\n"
        "v_cvt_pk_bf16_f32 v113, v36, v37\n"
        "v_pk_add_f32 v[146:147], v[146:147], v[66:67] op_sel_hi:[1,0]\n"
        "v_cvt_pk_bf16_f32 v30, v146, v147\n"
        "v_pk_add_f32 v[148:149], v[148:149], v[66:67] op_sel_hi:[1,0]\n"
        "v_cvt_pk_bf16_f32 v31, v148, v149\n"
        "v_pk_add_f32 v[138:139], v[138:139], v[66:67] op_sel_hi:[1,0]\n"
        "v_cvt_pk_bf16_f32 v32, v138, v139\n"
        "v_pk_add_f32 v[140:141], v[140:141], v[66:67] op_sel_hi:[1,0]\n"
        "v_cvt_pk_bf16_f32 v33, v140, v141\n"
        "v_pk_add_f32 v[142:143], v[142:143], v[66:67] op_sel_hi:[1,0]\n"
        "v_cvt_pk_bf16_f32 v34, v142, v143\n"
        "v_pk_add_f32 v[144:145], v[144:145], v[66:67] op_sel_hi:[1,0]\n"
        "v_cvt_pk_bf16_f32 v35, v144, v145\n"
        "v_pk_add_f32 v[134:135], v[134:135], v[66:67] op_sel_hi:[1,0]\n"
        "v_cvt_pk_bf16_f32 v36, v134, v135\n"
        "v_pk_add_f32 v[136:137], v[136:137], v[66:67] op_sel_hi:[1,0]\n"
        "v_cvt_pk_bf16_f32 v37, v136, v137\n"
        "v_add_f32 v72, v72, v89\n"
        "v_add_f32 v72, v72, v130\n"
        "v_add_f32 v72, v72, v131\n"
        "v_add_f32 v72, v72, v132\n"
        "v_add_f32 v72, v72, v133\n"
        "v_add_f32 v72, v72, v154\n"
        "v_add_f32 v72, v72, v155\n"
        "v_add_f32 v72, v72, v156\n"
        "v_add_f32 v72, v72, v157\n"
        "v_add_f32 v72, v72, v94\n"
        "v_add_f32 v72, v72, v95\n"
        "v_add_f32 v72, v72, v96\n"
        "v_add_f32 v72, v72, v97\n"
        "v_add_f32 v72, v72, v158\n"
        "v_add_f32 v72, v72, v159\n"
        "v_add_f32 v72, v72, v160\n"
        "v_add_f32 v72, v72, v161\n"
        "v_add_f32 v72, v72, v150\n"
        "v_add_f32 v72, v72, v151\n"
        "v_add_f32 v72, v72, v152\n"
        "v_add_f32 v72, v72, v153\n"
        "v_mov_b32 v73, v72\n"
        "s_nop 1\n"
        "v_permlane32_swap_b32 v72, v73\n"
        "v_add_f32 v72, v72, v73\n"
        "v_mov_b32 v73, v72\n"
        "s_nop 1\n"
        "v_permlane16_swap_b32 v72, v73\n"
        "v_add_f32 v72, v72, v73\n"
        "v_mul_f32 v72, 0xbc2aaaab, v72\n"
        "v_pk_add_f32 v[86:87], v[86:87], v[72:73] op_sel_hi:[1,0]\n"
        "v_cvt_pk_bf16_f32 v114, v86, v87\n"
        "v_pk_add_f32 v[88:89], v[88:89], v[72:73] op_sel_hi:[1,0]\n"
        "v_cvt_pk_bf16_f32 v115, v88, v89\n"
        "v_pk_add_f32 v[130:131], v[130:131], v[72:73] op_sel_hi:[1,0]\n"
        "v_cvt_pk_bf16_f32 v116, v130, v131\n"
        "v_pk_add_f32 v[132:133], v[132:133], v[72:73] op_sel_hi:[1,0]\n"
        "v_cvt_pk_bf16_f32 v117, v132, v133\n"
        "v_pk_add_f32 v[154:155], v[154:155], v[72:73] op_sel_hi:[1,0]\n"
        "v_cvt_pk_bf16_f32 v86, v154, v155\n"
        "v_pk_add_f32 v[156:157], v[156:157], v[72:73] op_sel_hi:[1,0]\n"
        "v_cvt_pk_bf16_f32 v87, v156, v157\n"
        "v_pk_add_f32 v[94:95], v[94:95], v[72:73] op_sel_hi:[1,0]\n"
        "v_cvt_pk_bf16_f32 v88, v94, v95\n"
        "v_pk_add_f32 v[96:97], v[96:97], v[72:73] op_sel_hi:[1,0]\n"
        "v_cvt_pk_bf16_f32 v89, v96, v97\n"
        "v_pk_add_f32 v[158:159], v[158:159], v[72:73] op_sel_hi:[1,0]\n"
        "v_cvt_pk_bf16_f32 v94, v158, v159\n"
        "v_pk_add_f32 v[160:161], v[160:161], v[72:73] op_sel_hi:[1,0]\n"
        "v_cvt_pk_bf16_f32 v95, v160, v161\n"
        "v_pk_add_f32 v[150:151], v[150:151], v[72:73] op_sel_hi:[1,0]\n"
        "v_cvt_pk_bf16_f32 v96, v150, v151\n"
        "v_pk_add_f32 v[152:153], v[152:153], v[72:73] op_sel_hi:[1,0]\n"
        "v_cvt_pk_bf16_f32 v97, v152, v153\n"
        "s_nop 1\n"
        "v_mfma_f32_16x16x32_bf16 v[118:121], v[54:57], v[54:57], 0\n"
        "ds_write_b128 v41, v[126:129]\n"
        "v_mfma_f32_16x16x32_bf16 v[118:121], v[78:81], v[78:81], v[118:121]\n"
        "ds_write_b128 v41, v[58:61] offset:1024\n"
        "v_mfma_f32_16x16x32_bf16 v[118:121], v[50:53], v[50:53], v[118:121]\n"
        "buffer_load_dwordx4 v6, s[36:39], s49 offen offset:2048 lds\n"
        "v_mfma_f32_16x16x32_bf16 v[118:121], v[102:105], v[102:105], v[118:121]\n"
        "ds_write_b128 v41, v[82:85] offset:2048\n"
        "v_mfma_f32_16x16x32_bf16 v[118:121], v[106:109], v[106:109], v[118:121]\n"
        "ds_write_b128 v42, v[162:165]\n"
        "v_mfma_f32_16x16x32_bf16 v[118:121], v[46:49], v[46:49], v[118:121]\n"
        "ds_write_b128 v42, v[98:101] offset:1024\n"
        "v_mfma_f32_16x16x32_bf16 v[118:121], v[126:129], v[126:129], v[118:121]\n"
        "ds_write_b128 v42, v[90:93] offset:2048\n"
        "v_mfma_f32_16x16x32_bf16 v[118:121], v[58:61], v[58:61], v[118:121]\n"
        "ds_write_b128 v43, v[110:113]\n"
        "v_mfma_f32_16x16x32_bf16 v[118:121], v[82:85], v[82:85], v[118:121]\n"
        "buffer_load_dwordx4 v7, s[36:39], s49 offen offset:3072 lds\n"
        "v_mfma_f32_16x16x32_bf16 v[118:121], v[162:165], v[162:165], v[118:121]\n"
        "ds_write_b128 v43, v[30:33] offset:1024\n"
        "v_mfma_f32_16x16x32_bf16 v[118:121], v[98:101], v[98:101], v[118:121]\n"
        "ds_write_b128 v43, v[34:37] offset:2048\n"
        "v_mfma_f32_16x16x32_bf16 v[118:121], v[90:93], v[90:93], v[118:121]\n"
        "ds_write_b128 v44, v[114:117]\n"
        "v_mfma_f32_16x16x32_bf16 v[118:121], v[110:113], v[110:113], v[118:121]\n"
        "ds_write_b128 v44, v[86:89] offset:1024\n"
        "v_mfma_f32_16x16x32_bf16 v[118:121], v[30:33], v[30:33], v[118:121]\n"
        "s_add_u32 m0, s52, 8192\n"
        "ds_write_b128 v44, v[94:97] offset:2048\n"
        "v_mfma_f32_16x16x32_bf16 v[118:121], v[34:37], v[34:37], v[118:121]\n"
        "buffer_load_dwordx4 v8, s[36:39], s49 offen lds\n"
        "v_mfma_f32_16x16x32_bf16 v[118:121], v[114:117], v[114:117], v[118:121]\n"
        "v_mfma_f32_16x16x32_bf16 v[118:121], v[86:89], v[86:89], v[118:121]\n"
        "v_mfma_f32_16x16x32_bf16 v[118:121], v[94:97], v[94:97], v[118:121]\n"
        "s_nop 2\n"
        "s_waitcnt vmcnt(9)\n"
        "s_nop 3\n"
        "v_max3_f32 v254, v118, v119, v120\n"
        "v_max_f32 v254, v254, v121\n"
        "v_cmp_nge_f32_e64 s[74:75], s24, v254\n"
        "v_mov_b32 v254, 161632\n"
        "s_nop 0\n"
        "v_cndmask_b32_e64 v254, v255, v254, s[74:75]\n"
        "v_mov_b32 v255, 0\n"
        "ds_write_b32 v254, v255\n"
        "s_waitcnt lgkmcnt(0)\n"
        "s_barrier\n"
        "FE_%=:\n"
        "s_mov_b64 s[16:17], s[54:55]\n"
        "s_cmp_eq_u32 s51, 0\n"
        "s_cselect_b32 s18, 128, 0\n"
        "s_mov_b32 s49, 161888\n"
        "s_cselect_b32 s49, 163424, s49\n"
        "s_movk_i32 s22, 1584\n"
        "s_movk_i32 s23, 1584\n"
        "s_mov_b32 s53, 19\n"
        "s_mov_b32 s54, 0x05040302\n"
        "s_mov_b32 s55, 0x07060302\n"
        "v_mov_b32 v28, s80\n"
        "ds_read_b128 v[84:87], v26\n"
        "ds_read_b128 v[48:51], v26 offset:64\n"
        "ds_read_b128 v[56:59], v26 offset:128\n"
        "v_xor_b32 v251, 64, v166\n"
        "ds_read_b128 a[0:3], v166\n"
        "ds_read_b128 a[4:7], v166 offset:1024\n"
        "ds_read_b128 a[8:11], v166 offset:2048\n"
        "ds_read_b128 a[12:15], v166 offset:3072\n"
        "ds_read_b128 a[16:19], v166 offset:4096\n"
        "ds_read_b128 a[20:23], v166 offset:5120\n"
        "ds_read_b128 a[24:27], v166 offset:6144\n"
        "ds_read_b128 a[28:31], v166 offset:7168\n"
        "ds_read_b128 a[32:35], v166 offset:8192\n"
        "v_mfma_f32_16x16x32_bf16 v[30:33], v[62:65], v[62:65], 0\n"
        "ds_read_b128 a[36:39], v166 offset:9216\n"
        "v_mov_b32 v253, 161632\n"
        "v_mfma_f32_16x16x32_bf16 v[30:33], v[68:71], v[68:71], v[30:33]\n"
        "ds_read_b128 a[40:43], v166 offset:10240\n"
        "v_lshrrev_b32 v254, 2, v166\n"
        "v_mfma_f32_16x16x32_bf16 v[30:33], v[74:77], v[74:77], v[30:33]\n"
        "ds_read_b128 a[44:47], v166 offset:11264\n"
        "v_and_b32 v254, 128, v254\n"
        "v_mfma_f32_16x16x32_bf16 v[36:39], v[176:179], v[176:179], 0\n"
        "ds_read_b128 a[48:51], v166 offset:12288\n"
        "v_xor_b32 v254, v254, v166\n"
        "v_mfma_f32_16x16x32_bf16 v[36:39], v[182:185], v[182:185], v[36:39]\n"
        "s_nop 0\n"
        "ds_read_b128 a[52:55], v166 offset:13312\n"
        "v_cndmask_b32_e64 v252, v30, v31, s[56:57]\n"
        "v_mfma_f32_16x16x32_bf16 v[36:39], v[188:191], v[188:191], v[36:39]\n"
        "s_waitcnt lgkmcnt(14)\n"
        "ds_read_b128 a[56:59], v166 offset:14336\n"
        "v_cndmask_b32_e64 v252, v252, v32, s[58:59]\n"
        "v_mfma_f32_16x16x32_bf16 v[40:43], v[56:59], v[188:191], 0\n"
        "ds_read_b128 a[60:63], v166 offset:15360\n"
        "v_cndmask_b32_e64 v252, v252, v33, s[60:61]\n"
        "v_mfma_f32_16x16x32_bf16 v[40:43], v[48:51], v[182:185], v[40:43]\n"
        "ds_read_b128 a[64:67], v166 offset:16384\n"
        "v_cndmask_b32_e64 v255, v36, v37, s[56:57]\n"
        "v_mfma_f32_16x16x32_bf16 v[40:43], v[84:87], v[176:179], v[40:43]\n"
        "s_waitcnt lgkmcnt(14)\n"
        "ds_read_b128 a[68:71], v166 offset:17408\n"
        "v_cndmask_b32_e64 v255, v255, v38, s[58:59]\n"
        "v_mfma_f32_16x16x32_bf16 v[34:37], v[84:87], v[62:65], 0\n"
        "s_waitcnt lgkmcnt(12)\n"
        "ds_read_b128 a[72:75], v166 offset:18432\n"
        "v_cndmask_b32_e64 v255, v255, v39, s[60:61]\n"
        "v_mfma_f32_16x16x32_bf16 v[34:37], v[48:51], v[68:71], v[34:37]\n"
        "s_waitcnt lgkmcnt(10)\n"
        "ds_read_b128 a[76:79], v166 offset:19456\n"
        "v_cndmask_b32_dpp v252, v255, v252, vcc row_ror:4 row_mask:0xf bank_mask:0xf\n"
        "v_mfma_f32_16x16x32_bf16 v[34:37], v[56:59], v[74:77], v[34:37]\n"
        "ds_read_b128 a[80:83], v166 offset:20480\n"
        "v_add_u32 v255, 73792, v166\n"
        "v_mfma_f32_16x16x32_bf16 v[30:33], a[8:11], v[74:77], 0\n"
        "ds_read_b128 a[84:87], v166 offset:21504\n"
        "v_add_u32 v254, 140896, v254\n"
        "v_mfma_f32_16x16x32_bf16 v[30:33], a[4:7], v[68:71], v[30:33]\n"
        "s_waitcnt lgkmcnt(10)\n"
        "ds_read_b128 a[88:91], v166 offset:22528\n"
        "v_cndmask_b32_dpp v250, v40, v34, vcc row_ror:4 row_mask:0xf bank_mask:0xf\n"
        "v_mfma_f32_16x16x32_bf16 v[30:33], a[0:3], v[62:65], v[30:33]\n"
        "ds_read_b128 a[92:95], v166 offset:23552\n"
        "v_mul_f32 v249, s80, v250\n"
        "v_mfma_f32_16x16x32_bf16 v[42:45], a[12:15], v[62:65], 0\n"
        "ds_read_b128 a[96:99], v251 offset:24576\n"
        "v_fma_f32 v249, -v249, v250, v252\n"
        "v_mfma_f32_16x16x32_bf16 v[42:45], a[16:19], v[68:71], v[42:45]\n"
        "s_waitcnt lgkmcnt(10)\n"
        "ds_read_b128 a[100:103], v251 offset:25600\n"
        "v_cvt_pk_bf16_f32 v36, v30, v31\n"
        "v_mfma_f32_16x16x32_bf16 v[42:45], a[20:23], v[74:77], v[42:45]\n"
        "ds_read_b128 a[104:107], v251 offset:26624\n"
        "v_cvt_pk_bf16_f32 v37, v32, v33\n"
        "v_mfma_f32_16x16x32_bf16 v[52:55], a[32:35], v[74:77], 0\n"
        "ds_read_b128 a[108:111], v251 offset:27648\n"
        "v_max_f32 v249, 0, v249\n"
        "v_mfma_f32_16x16x32_bf16 v[52:55], a[28:31], v[68:71], v[52:55]\n"
        "s_waitcnt lgkmcnt(10)\n"
        "ds_read_b128 a[112:115], v251 offset:28672\n"
        "v_cvt_pk_bf16_f32 v38, v42, v43\n"
        "v_mfma_f32_16x16x32_bf16 v[52:55], a[24:27], v[62:65], v[52:55]\n"
        "ds_read_b128 a[116:119], v251 offset:29696\n"
        "v_cvt_pk_bf16_f32 v39, v44, v45\n"
        "v_mfma_f32_16x16x32_bf16 v[30:33], a[36:39], v[62:65], 0\n"
        "ds_read_b128 a[120:123], v166 offset:30720\n"
        "v_fmaak_f32 v249, v28, v249, 0x3727c5ac\n"
        "v_mfma_f32_16x16x32_bf16 v[30:33], a[40:43], v[68:71], v[30:33]\n"
        "s_nop 0\n"
        "ds_read_b128 a[124:127], v166 offset:31744\n"
        "v_cvt_pk_bf16_f32 v78, v52, v53\n"
        "v_mfma_f32_16x16x32_bf16 v[30:33], a[44:47], v[74:77], v[30:33]\n"
        "ds_read_b128 a[128:131], v166 offset:32768\n"
        "v_cvt_pk_bf16_f32 v79, v54, v55\n"
        "v_mfma_f32_16x16x32_bf16 v[42:45], a[56:59], v[74:77], 0\n"
        "ds_read_b128 a[132:135], v166 offset:33792\n"
        "v_cndmask_b32_dpp v251, v41, v35, vcc row_ror:4 row_mask:0xf bank_mask:0xf\n"
        "v_mfma_f32_16x16x32_bf16 v[42:45], a[52:55], v[68:71], v[42:45]\n"
        "s_nop 0\n"
        "ds_read_b128 a[136:139], v166 offset:34816\n"
        "v_cvt_pk_bf16_f32 v80, v30, v31\n"
        "v_mfma_f32_16x16x32_bf16 v[42:45], a[48:51], v[62:65], v[42:45]\n"
        "s_nop 0\n"
        "ds_read_b128 a[140:143], v166 offset:35840\n"
        "v_cvt_pk_bf16_f32 v81, v32, v33\n"
        "v_mfma_f32_16x16x32_bf16 v[52:55], a[60:63], v[62:65], 0\n"
        "s_nop 0\n"
        "v_rsq_f32 v252, v249\n"
        "v_mfma_f32_16x16x32_bf16 v[52:55], a[64:67], v[68:71], v[52:55]\n"
        "s_nop 0\n"
        "v_cvt_pk_bf16_f32 v30, v42, v43\n"
        "v_cvt_pk_bf16_f32 v31, v44, v45\n"
        "v_mfma_f32_16x16x32_bf16 v[52:55], a[68:71], v[74:77], v[52:55]\n"
        "s_nop 1\n"
        "v_mfma_f32_16x16x32_bf16 v[40:43], a[8:11], v[188:191], 0\n"
        "s_nop 2\n"
        "v_mfma_f32_16x16x32_bf16 v[40:43], a[4:7], v[182:185], v[40:43]\n"
        "s_nop 0\n"
        "v_cvt_pk_bf16_f32 v32, v52, v53\n"
        "v_cvt_pk_bf16_f32 v33, v54, v55\n"
        "v_mfma_f32_16x16x32_bf16 v[40:43], a[0:3], v[176:179], v[40:43]\n"
        "s_nop 1\n"
        "v_mfma_f32_16x16x32_bf16 v[52:55], v[62:65], v[36:39], 0\n"
        "s_nop 2\n"
        "v_mfma_f32_16x16x32_bf16 v[52:55], v[68:71], v[78:81], v[52:55]\n"
        "s_nop 0\n"
        "v_cvt_pk_bf16_f32 v44, v40, v41\n"
        "v_cvt_pk_bf16_f32 v45, v42, v43\n"
        "v_mfma_f32_16x16x32_bf16 v[52:55], v[74:77], v[30:33], v[52:55]\n"
        "v_mfma_f32_16x16x32_bf16 v[30:33], a[20:23], v[188:191], 0\n"
        "s_waitcnt lgkmcnt(0)\n"
        "v_mfma_f32_16x16x32_bf16 v[30:33], a[16:19], v[182:185], v[30:33]\n"
        "s_barrier\n"
        "ds_read_b128 a[144:147], v254\n"
        "v_mfma_f32_16x16x32_bf16 v[30:33], a[12:15], v[176:179], v[30:33]\n"
        "s_nop 1\n"
        "v_mfma_f32_16x16x32_bf16 v[34:37], a[24:27], v[176:179], 0\n"
        "s_nop 0\n"
        "ds_read_b128 a[148:151], v254 offset:1024\n"
        "s_nop 0\n"
        "v_mfma_f32_16x16x32_bf16 v[34:37], a[28:31], v[182:185], v[34:37]\n"
        "s_nop 0\n"
        "v_cvt_pk_bf16_f32 v46, v30, v31\n"
        "v_cvt_pk_bf16_f32 v47, v32, v33\n"
        "v_mfma_f32_16x16x32_bf16 v[34:37], a[32:35], v[188:191], v[34:37]\n"
        "s_nop 1\n"
        "v_mfma_f32_16x16x32_bf16 v[78:81], a[44:47], v[188:191], 0\n"
        "s_nop 2\n"
        "v_mfma_f32_16x16x32_bf16 v[78:81], a[40:43], v[182:185], v[78:81]\n"
        "s_nop 0\n"
        "v_cvt_pk_bf16_f32 v30, v34, v35\n"
        "v_cvt_pk_bf16_f32 v31, v36, v37\n"
        "v_mfma_f32_16x16x32_bf16 v[78:81], a[36:39], v[176:179], v[78:81]\n"
        "s_nop 0\n"
        "ds_read_b128 a[152:155], v254 offset:2048\n"
        "v_mfma_f32_16x16x32_bf16 v[40:43], a[48:51], v[176:179], 0\n"
        "s_nop 2\n"
        "v_mfma_f32_16x16x32_bf16 v[40:43], a[52:55], v[182:185], v[40:43]\n"
        "s_nop 0\n"
        "v_cvt_pk_bf16_f32 v32, v78, v79\n"
        "v_cvt_pk_bf16_f32 v33, v80, v81\n"
        "v_mfma_f32_16x16x32_bf16 v[40:43], a[56:59], v[188:191], v[40:43]\n"
        "s_nop 0\n"
        "ds_read_b128 a[156:159], v254 offset:3072\n"
        "v_mfma_f32_16x16x32_bf16 v[34:37], a[68:71], v[188:191], 0\n"
        "s_nop 2\n"
        "v_mfma_f32_16x16x32_bf16 v[34:37], a[64:67], v[182:185], v[34:37]\n"
        "s_nop 0\n"
        "v_cvt_pk_bf16_f32 v38, v40, v41\n"
        "v_cvt_pk_bf16_f32 v39, v42, v43\n"
        "v_mfma_f32_16x16x32_bf16 v[34:37], a[60:63], v[176:179], v[34:37]\n"
        "s_nop 0\n"
        "ds_read_b128 a[160:163], v254 offset:4096\n"
        "v_mfma_f32_16x16x32_bf16 v[88:91], v[62:65], a[72:75], 0\n"
        "s_nop 2\n"
        "v_mfma_f32_16x16x32_bf16 v[88:91], v[68:71], a[76:79], v[88:91]\n"
        "s_nop 0\n"
        "v_cvt_pk_bf16_f32 v40, v34, v35\n"
        "v_cvt_pk_bf16_f32 v41, v36, v37\n"
        "v_mfma_f32_16x16x32_bf16 v[88:91], v[74:77], a[80:83], v[88:91]\n"
        "s_nop 1\n"
        "v_mfma_f32_16x16x32_bf16 v[78:81], v[74:77], a[92:95], 0\n"
        "s_nop 0\n"
        "ds_read_b128 a[164:167], v254 offset:5120\n"
        "s_nop 0\n"
        "v_mfma_f32_16x16x32_bf16 v[78:81], v[68:71], a[88:91], v[78:81]\n"
        "s_nop 0\n"
        "v_cvt_pk_bf16_f32 v42, v88, v89\n"
        "v_cvt_pk_bf16_f32 v43, v90, v91\n"
        "v_mfma_f32_16x16x32_bf16 v[78:81], v[62:65], a[84:87], v[78:81]\n"
        "s_nop 0\n"
        "v_mfma_f32_16x16x32_bf16 v[34:37], v[176:179], v[44:47], 0\n"
        "s_nop 2\n"
        "v_mfma_f32_16x16x32_bf16 v[34:37], v[182:185], v[30:33], v[34:37]\n"
        "s_nop 0\n"
        "ds_read_b128 a[168:171], v254 offset:6144\n"
        "v_cvt_pk_bf16_f32 v46, v78, v79\n"
        "v_mfma_f32_16x16x32_bf16 v[34:37], v[188:191], v[38:41], v[34:37]\n"
        "s_waitcnt vmcnt(0)\n"
        "v_cvt_pk_bf16_f32 v47, v80, v81\n"
        "v_mfma_f32_16x16x32_bf16 v[38:41], v[74:77], a[104:107], 0\n"
        "s_nop 0\n"
        "ds_read_b128 v[126:129], v9\n"
        "v_mfma_f32_16x16x32_bf16 v[38:41], v[68:71], a[100:103], v[38:41]\n"
        "s_nop 0\n"
        "ds_read_b128 v[132:135], v10\n"
        "v_cndmask_b32_dpp v249, v34, v52, vcc row_ror:4 row_mask:0xf bank_mask:0xf\n"
        "v_mfma_f32_16x16x32_bf16 v[38:41], v[62:65], a[96:99], v[38:41]\n"
        "ds_read_b128 v[138:141], v11\n"
        "v_cndmask_b32_dpp v246, v35, v53, vcc row_ror:4 row_mask:0xf bank_mask:0xf\n"
        "v_mfma_f32_16x16x32_bf16 v[142:145], v[62:65], a[108:111], 0\n"
        "ds_read_b128 v[106:109], v9 offset:3072\n"
        "v_cndmask_b32_dpp v248, v36, v54, vcc row_ror:4 row_mask:0xf bank_mask:0xf\n"
        "v_mfma_f32_16x16x32_bf16 v[142:145], v[68:71], a[112:115], v[142:145]\n"
        "ds_read_b128 v[112:115], v10 offset:3072\n"
        "v_cndmask_b32_dpp v247, v37, v55, vcc row_ror:4 row_mask:0xf bank_mask:0xf\n"
        "v_mfma_f32_16x16x32_bf16 v[142:145], v[74:77], a[116:119], v[142:145]\n"
        "ds_read_b128 v[118:121], v11 offset:3072\n"
        "v_mul_f32_dpp v249, v252, v249 quad_perm:[0,0,0,0] row_mask:0xf bank_mask:0xf\n"
        "v_mfma_f32_16x16x32_bf16 v[122:125], v[74:77], a[128:131], 0\n"
        "ds_read_b128 v[146:149], v9 offset:6144\n"
        "v_mul_f32_dpp v246, v252, v246 quad_perm:[1,1,1,1] row_mask:0xf bank_mask:0xf\n"
        "v_mfma_f32_16x16x32_bf16 v[122:125], v[68:71], a[124:127], v[122:125]\n"
        "ds_read_b128 v[152:155], v10 offset:6144\n"
        "v_mul_f32_dpp v248, v252, v248 quad_perm:[2,2,2,2] row_mask:0xf bank_mask:0xf\n"
        "v_mfma_f32_16x16x32_bf16 v[122:125], v[62:65], a[120:123], v[122:125]\n"
        "ds_read_b128 v[158:161], v11 offset:6144\n"
        "v_mul_f32_dpp v247, v252, v247 quad_perm:[3,3,3,3] row_mask:0xf bank_mask:0xf\n"
        "v_mfma_f32_16x16x32_bf16 v[162:165], v[62:65], a[132:135], 0\n"
        "ds_read_b128 a[172:175], v254 offset:7168\n"
        "v_max3_f32 v245, v249, v246, v248\n"
        "v_mfma_f32_16x16x32_bf16 v[162:165], v[68:71], a[136:139], v[162:165]\n"
        "s_waitcnt lgkmcnt(7)\n"
        "ds_read_b128 a[176:179], v254 offset:8192\n"
        "v_max_f32 v245, v245, v247\n"
        "v_mfma_f32_16x16x32_bf16 v[162:165], v[74:77], a[140:143], v[162:165]\n"
        "v_mul_f32 v245, v245, v252\n"
        "v_fma_f32 v249, v249, v252, -v245\n"
        "v_mfma_f32_16x16x32_bf16 v[100:103], v[126:129], v[126:129], 0\n"
        "s_mov_b32 s26, s89\n"
        "v_fma_f32 v246, v246, v252, -v245\n"
        "v_fma_f32 v248, v248, v252, -v245\n"
        "v_mfma_f32_16x16x32_bf16 v[100:103], v[132:135], v[132:135], v[100:103]\n"
        "s_add_u32 s89, s86, s51\n"
        "v_fma_f32 v247, v247, v252, -v245\n"
        "v_cvt_pk_bf16_f32 v30, v38, v39\n"
        "v_mfma_f32_16x16x32_bf16 v[100:103], v[138:141], v[138:141], v[100:103]\n"
        "s_mov_b32 s30, s29\n"
        "v_exp_f32 v249, v249\n"
        "v_mfma_f32_16x16x32_bf16 v[52:55], v[56:59], v[138:141], 0\n"
        "s_add_u32 s29, s29, 1\n"
        "v_exp_f32 v246, v246\n"
        "v_mfma_f32_16x16x32_bf16 v[52:55], v[48:51], v[132:135], v[52:55]\n"
        "s_mov_b32 s86, s87\n"
        "v_exp_f32 v248, v248\n"
        "v_mfma_f32_16x16x32_bf16 v[52:55], v[84:87], v[126:129], v[52:55]\n"
        "s_add_u32 s88, s88, 32\n"
        "v_exp_f32 v247, v247\n"
        "v_mfma_f32_16x16x32_bf16 v[80:83], a[0:3], v[126:129], 0\n"
        "s_add_u32 s87, s87, 43008\n"
        "v_add_f32 v245, v249, v246\n"
        "v_add_f32 v244, v248, v247\n"
        "v_mfma_f32_16x16x32_bf16 v[80:83], a[4:7], v[132:135], v[80:83]\n"
        "s_cmp_ge_u32 s88, 73\n"
        "v_add_f32 v245, v245, v244\n"
        "v_cvt_pk_bf16_f32 v31, v40, v41\n"
        "v_mfma_f32_16x16x32_bf16 v[80:83], a[8:11], v[138:141], v[80:83]\n"
        "s_cselect_b32 s92, 588672, 0\n"
        "v_rcp_f32 v245, v245\n"
        "v_mfma_f32_16x16x32_bf16 v[88:91], a[20:23], v[138:141], 0\n"
        "s_cselect_b32 s94, 73, 0\n"
        "ds_read_b64 v[44:45], v27\n"
        "ds_read_b64 v[32:33], v27\n"
        "v_mfma_f32_16x16x32_bf16 v[88:91], a[16:19], v[132:135], v[88:91]\n"
        "s_sub_u32 s88, s88, s94\n"
        "ds_read_b64 v[38:39], v27\n"
        "v_mul_f32 v249, v249, v245\n"
        "v_mfma_f32_16x16x32_bf16 v[88:91], a[12:15], v[126:129], v[88:91]\n"
        "s_add_u32 s87, s87, s92\n"
        "ds_read_b32 v253, v253\n"
        "v_mul_f32 v246, v246, v245\n"
        "v_mfma_f32_16x16x32_bf16 v[92:95], a[24:27], v[126:129], 0\n"
        "s_cmp_gt_i32 s85, 0\n"
        "v_mul_f32 v248, v248, v245\n"
        "v_mul_f32 v247, v247, v245\n"
        "v_mfma_f32_16x16x32_bf16 v[92:95], a[28:31], v[132:135], v[92:95]\n"
        "s_cselect_b32 s94, s87, s84\n"
        "v_mul_f32_dpp v249, v252, v249 quad_perm:[0,0,0,0] row_mask:0xf bank_mask:0xf\n"
        "v_mul_f32_dpp v246, v252, v246 quad_perm:[1,1,1,1] row_mask:0xf bank_mask:0xf\n"
        "v_mfma_f32_16x16x32_bf16 v[92:95], a[32:35], v[138:141], v[92:95]\n"
        "s_sub_u32 s85, s85, 1\n"
        "v_mul_f32_dpp v248, v252, v248 quad_perm:[2,2,2,2] row_mask:0xf bank_mask:0xf\n"
        "v_mul_f32_dpp v247, v252, v247 quad_perm:[3,3,3,3] row_mask:0xf bank_mask:0xf\n"
        "v_mfma_f32_16x16x32_bf16 v[96:99], a[44:47], v[138:141], 0\n"
        "s_waitcnt lgkmcnt(9)\n"
        "v_cvt_pk_bf16_f32 v245, v249, v246\n"
        "v_cvt_pk_bf16_f32 v252, v248, v247\n"
        "v_mfma_f32_16x16x32_bf16 v[96:99], a[40:43], v[132:135], v[96:99]\n"
        "s_waitcnt lgkmcnt(6)\n"
        "v_and_b32 v60, v25, v245\n"
        "v_and_b32 v61, v25, v252\n"
        "v_mfma_f32_16x16x32_bf16 v[96:99], a[36:39], v[126:129], v[96:99]\n"
        "v_mov_b64 v[66:67], v[60:61]\n"
        "v_mov_b64 v[72:73], v[60:61]\n"
        "v_mfma_f32_16x16x32_bf16 v[198:201], a[48:51], v[126:129], 0\n"
        "s_waitcnt lgkmcnt(1)\n"
        "v_cvt_pk_bf16_f32 v34, v142, v143\n"
        "v_cvt_pk_bf16_f32 v35, v144, v145\n"
        "v_mfma_f32_16x16x32_bf16 v[198:201], a[52:55], v[132:135], v[198:201]\n"
        "s_waitcnt lgkmcnt(0)\n"
        "v_readfirstlane_b32 s83, v253\n"
        "v_cvt_pk_bf16_f32 v36, v122, v123\n"
        "v_mfma_f32_16x16x32_bf16 v[198:201], a[56:59], v[138:141], v[198:201]\n"
        "v_cvt_pk_bf16_f32 v37, v124, v125\n"
        "v_mov_b64 v[78:79], v[60:61]\n"
        "v_mfma_f32_16x16x32_bf16 v[194:197], a[68:71], v[138:141], 0\n"
        "v_cvt_pk_bf16_f32 v40, v162, v163\n"
        "v_cvt_pk_bf16_f32 v41, v164, v165\n"
        "v_mfma_f32_16x16x32_bf16 v[194:197], a[64:67], v[132:135], v[194:197]\n"
        "ds_read_b128 a[180:183], v254 offset:9216\n"
        "v_and_b32_dpp v174, v245, v25 row_ror:12 row_mask:0xf bank_mask:0xf\n"
        "v_mfma_f32_16x16x32_bf16 v[194:197], a[60:63], v[126:129], v[194:197]\n"
        "ds_read_b128 a[184:187], v254 offset:10240\n"
        "v_and_b32_dpp v175, v252, v25 row_ror:12 row_mask:0xf bank_mask:0xf\n"
        "v_mfma_f32_16x16x32_bf16 v[142:145], v[42:45], v[60:63], 0\n"
        "ds_read_b128 a[188:191], v254 offset:11264\n"
        "v_mov_b64 v[180:181], v[174:175]\n"
        "v_mfma_f32_16x16x32_bf16 v[122:125], v[44:47], v[64:67], 0\n"
        "s_nop 0\n"
        "ds_read_b128 a[192:195], v254 offset:12288\n"
        "v_mov_b64 v[186:187], v[174:175]\n"
        "v_mfma_f32_16x16x32_bf16 v[62:65], v[176:179], a[72:75], 0\n"
        "s_nop 0\n"
        "v_cvt_pk_bf16_f32 v162, v142, v143\n"
        "v_cvt_pk_bf16_f32 v142, v144, v145\n"
        "v_mfma_f32_16x16x32_bf16 v[62:65], v[182:185], a[76:79], v[62:65]\n"
        "v_cvt_pk_bf16_f32 v163, v124, v125\n"
        "v_cvt_pk_bf16_f32 v60, v122, v123\n"
        "v_mfma_f32_16x16x32_bf16 v[62:65], v[188:191], a[80:83], v[62:65]\n"
        "s_add_u32 m0, s52, 0\n"
        "ds_read_b128 a[196:199], v254 offset:13312\n"
        "v_mov_b64 v[192:193], v[174:175]\n"
        "v_mfma_f32_16x16x32_bf16 v[122:125], v[188:191], a[92:95], 0\n"
        "ds_read_b128 a[200:203], v254 offset:14336\n"
        "v_cvt_pk_bf16_f32 v202, v80, v81\n"
        "v_mfma_f32_16x16x32_bf16 v[122:125], v[182:185], a[88:91], v[122:125]\n"
        "buffer_load_dwordx4 v0, s[36:39], s94 offen lds\n"
        "v_cvt_pk_bf16_f32 v42, v62, v63\n"
        "v_mfma_f32_16x16x32_bf16 v[122:125], v[176:179], a[84:87], v[122:125]\n"
        "ds_read_b128 a[204:207], v254 offset:15360\n"
        "v_cvt_pk_bf16_f32 v43, v64, v65\n"
        "v_mfma_f32_16x16x32_bf16 v[62:65], v[30:33], v[66:69], 0\n"
        "ds_read_b128 a[208:211], v254 offset:16384\n"
        "v_cvt_pk_bf16_f32 v203, v82, v83\n"
        "v_mfma_f32_16x16x32_bf16 v[80:83], v[32:35], v[70:73], 0\n"
        "s_nop 0\n"
        "ds_read_b128 a[212:215], v254 offset:17408\n"
        "v_cvt_pk_bf16_f32 v46, v122, v123\n"
        "v_mfma_f32_16x16x32_bf16 v[66:69], v[176:179], a[96:99], 0\n"
        "s_nop 0\n"
        "v_cvt_pk_bf16_f32 v143, v62, v63\n"
        "v_cvt_pk_bf16_f32 v61, v64, v65\n"
        "v_mfma_f32_16x16x32_bf16 v[66:69], v[182:185], a[100:103], v[66:69]\n"
        "v_cvt_pk_bf16_f32 v164, v80, v81\n"
        "v_cvt_pk_bf16_f32 v144, v82, v83\n"
        "v_mfma_f32_16x16x32_bf16 v[66:69], v[188:191], a[104:107], v[66:69]\n"
        "v_cvt_pk_bf16_f32 v47, v124, v125\n"
        "v_cvt_pk_bf16_f32 v204, v88, v89\n"
        "v_mfma_f32_16x16x32_bf16 v[122:125], v[188:191], a[116:119], 0\n"
        "ds_read_b128 a[216:219], v255\n"
        "v_cvt_pk_bf16_f32 v205, v90, v91\n"
        "v_mfma_f32_16x16x32_bf16 v[122:125], v[182:185], a[112:115], v[122:125]\n"
        "s_nop 0\n"
        "buffer_load_dwordx4 v1, s[36:39], s94 offen offset:1024 lds\n"
        "v_cvt_pk_bf16_f32 v30, v66, v67\n"
        "v_mfma_f32_16x16x32_bf16 v[122:125], v[176:179], a[108:111], v[122:125]\n"
        "v_cvt_pk_bf16_f32 v31, v68, v69\n"
        "v_cvt_pk_bf16_f32 v80, v92, v93\n"
        "v_mfma_f32_16x16x32_bf16 v[88:91], v[36:39], v[72:75], 0\n"
        "s_nop 0\n"
        "ds_read_b128 a[220:223], v255 offset:1024\n"
        "v_cvt_pk_bf16_f32 v81, v94, v95\n"
        "v_mfma_f32_16x16x32_bf16 v[64:67], v[38:41], v[76:79], 0\n"
        "s_nop 0\n"
        "v_cvt_pk_bf16_f32 v34, v122, v123\n"
        "v_cvt_pk_bf16_f32 v35, v124, v125\n"
        "v_mfma_f32_16x16x32_bf16 v[68:71], v[176:179], a[120:123], 0\n"
        "v_cvt_pk_bf16_f32 v62, v88, v89\n"
        "v_cvt_pk_bf16_f32 v165, v90, v91\n"
        "v_mfma_f32_16x16x32_bf16 v[68:71], v[182:185], a[124:127], v[68:71]\n"
        "ds_write_b128 v12, v[162:165]\n"
        "v_mfma_f32_16x16x32_bf16 v[68:71], v[188:191], a[128:131], v[68:71]\n"
        "v_cvt_pk_bf16_f32 v145, v64, v65\n"
        "v_cvt_pk_bf16_f32 v63, v66, v67\n"
        "v_mfma_f32_16x16x32_bf16 v[76:79], v[188:191], a[140:143], 0\n"
        "ds_write_b128 v12, v[142:145] offset:1024\n"
        "v_mfma_f32_16x16x32_bf16 v[76:79], v[182:185], a[136:139], v[76:79]\n"
        "ds_write_b128 v12, v[60:63] offset:2048\n"
        "v_mfma_f32_16x16x32_bf16 v[76:79], v[176:179], a[132:135], v[76:79]\n"
        "buffer_load_dwordx4 v2, s[36:39], s94 offen offset:2048 lds\n"
        "v_cvt_pk_bf16_f32 v36, v68, v69\n"
        "v_mfma_f32_16x16x32_bf16 v[64:67], v[162:165], v[162:165], 0\n"
        "v_cvt_pk_bf16_f32 v82, v96, v97\n"
        "v_cvt_pk_bf16_f32 v83, v98, v99\n"
        "v_mfma_f32_16x16x32_bf16 v[64:67], v[142:145], v[142:145], v[64:67]\n"
        "v_cvt_pk_bf16_f32 v37, v70, v71\n"
        "v_cvt_pk_bf16_f32 v88, v198, v199\n"
        "v_mfma_f32_16x16x32_bf16 v[64:67], v[60:63], v[60:63], v[64:67]\n"
        "v_cvt_pk_bf16_f32 v89, v200, v201\n"
        "v_cvt_pk_bf16_f32 v90, v194, v195\n"
        "v_mfma_f32_16x16x32_bf16 v[72:75], v[106:109], v[106:109], 0\n"
        "v_cvt_pk_bf16_f32 v91, v196, v197\n"
        "v_cvt_pk_bf16_f32 v40, v76, v77\n"
        "v_mfma_f32_16x16x32_bf16 v[72:75], v[112:115], v[112:115], v[72:75]\n"
        "v_cvt_pk_bf16_f32 v41, v78, v79\n"
        "v_mul_f32_dpp v252, v251, v249 quad_perm:[0,0,0,0] row_mask:0xf bank_mask:0xf\n"
        "v_mfma_f32_16x16x32_bf16 v[72:75], v[118:121], v[118:121], v[72:75]\n"
        "ds_read_b128 a[224:227], v255 offset:2048\n"
        "v_fmac_f32_dpp v252, v251, v246 quad_perm:[1,1,1,1] row_mask:0xf bank_mask:0xf\n"
        "v_mfma_f32_16x16x32_bf16 v[68:71], v[56:59], v[118:121], 0\n"
        "buffer_load_dwordx4 v3, s[36:39], s94 offen offset:3072 lds\n"
        "v_fmac_f32_dpp v252, v251, v248 quad_perm:[2,2,2,2] row_mask:0xf bank_mask:0xf\n"
        "v_mfma_f32_16x16x32_bf16 v[68:71], v[48:51], v[112:115], v[68:71]\n"
        "ds_read_b128 a[228:231], v255 offset:3088\n"
        "v_cndmask_b32_e64 v249, v64, v65, s[56:57]\n"
        "v_mfma_f32_16x16x32_bf16 v[68:71], v[84:87], v[106:109], v[68:71]\n"
        "ds_read_b128 a[232:235], v255 offset:4112\n"
        "v_cndmask_b32_e64 v249, v249, v66, s[58:59]\n"
        "v_mfma_f32_16x16x32_bf16 v[76:79], v[42:45], v[174:177], 0\n"
        "ds_read_b128 a[236:239], v255 offset:5136\n"
        "v_cndmask_b32_e64 v249, v249, v67, s[60:61]\n"
        "v_mfma_f32_16x16x32_bf16 v[60:63], v[44:47], v[178:181], 0\n"
        "s_nop 0\n"
        "ds_read_b128 a[240:243], v255 offset:6176\n"
        "v_fmac_f32_dpp v252, v251, v247 quad_perm:[3,3,3,3] row_mask:0xf bank_mask:0xf\n"
        "v_mfma_f32_16x16x32_bf16 v[64:67], v[30:33], v[180:183], 0\n"
        "s_nop 0\n"
        "v_cvt_pk_bf16_f32 v142, v76, v77\n"
        "v_cvt_pk_bf16_f32 v92, v78, v79\n"
        "v_mfma_f32_16x16x32_bf16 v[122:125], v[32:35], v[184:187], 0\n"
        "s_add_u32 m0, s52, 4096\n"
        "v_cvt_pk_bf16_f32 v76, v60, v61\n"
        "v_cvt_pk_bf16_f32 v143, v62, v63\n"
        "v_mfma_f32_16x16x32_bf16 v[96:99], v[36:39], v[186:189], 0\n"
        "v_cvt_pk_bf16_f32 v93, v64, v65\n"
        "v_cvt_pk_bf16_f32 v77, v66, v67\n"
        "v_mfma_f32_16x16x32_bf16 v[60:63], v[38:41], v[190:193], 0\n"
        "buffer_load_dwordx4 v4, s[36:39], s94 offen lds\n"
        "v_cvt_pk_bf16_f32 v144, v122, v123\n"
        "v_mfma_f32_16x16x32_bf16 v[34:37], v[126:129], v[202:205], 0\n"
        "s_nop 0\n"
        "v_cvt_pk_bf16_f32 v94, v124, v125\n"
        "v_cvt_pk_bf16_f32 v78, v96, v97\n"
        "v_mfma_f32_16x16x32_bf16 v[34:37], v[132:135], v[80:83], v[34:37]\n"
        "v_cvt_pk_bf16_f32 v145, v98, v99\n"
        "v_cvt_pk_bf16_f32 v95, v60, v61\n"
        "v_mfma_f32_16x16x32_bf16 v[34:37], v[138:141], v[88:91], v[34:37]\n"
        "ds_write_b128 v12, v[142:145] offset:3072\n"
        "v_mfma_f32_16x16x32_bf16 v[40:43], a[8:11], v[118:121], 0\n"
        "ds_write_b128 v12, v[92:95] offset:4096\n"
        "v_mfma_f32_16x16x32_bf16 v[40:43], a[4:7], v[112:115], v[40:43]\n"
        "v_cvt_pk_bf16_f32 v79, v62, v63\n"
        "v_add_f32 v252, v252, v250\n"
        "v_mfma_f32_16x16x32_bf16 v[40:43], a[0:3], v[106:109], v[40:43]\n"
        "ds_write_b128 v12, v[76:79] offset:5120\n"
        "v_mfma_f32_16x16x32_bf16 v[60:63], v[142:145], v[142:145], 0\n"
        "s_nop 0\n"
        "v_mul_f32 v253, s80, v252\n"
        "v_cndmask_b32_e64 v251, v72, v73, s[56:57]\n"
        "v_mfma_f32_16x16x32_bf16 v[60:63], v[92:95], v[92:95], v[60:63]\n"
        "s_nop 0\n"
        "buffer_load_dwordx4 v5, s[36:39], s94 offen offset:1024 lds\n"
        "v_cvt_pk_bf16_f32 v88, v40, v41\n"
        "v_mfma_f32_16x16x32_bf16 v[60:63], v[76:79], v[76:79], v[60:63]\n"
        "v_cvt_pk_bf16_f32 v89, v42, v43\n"
        "v_cndmask_b32_e64 v251, v251, v74, s[58:59]\n"
        "v_mfma_f32_16x16x32_bf16 v[64:67], a[20:23], v[118:121], 0\n"
        "s_nop 0\n"
        "ds_read_b128 a[244:247], v255 offset:7200\n"
        "ds_read_b128 a[248:251], v255 offset:8224\n"
        "v_mfma_f32_16x16x32_bf16 v[64:67], a[16:19], v[112:115], v[64:67]\n"
        "s_nop 0\n"
        "v_cndmask_b32_e64 v254, v60, v61, s[56:57]\n"
        "v_cndmask_b32_e64 v254, v254, v62, s[58:59]\n"
        "v_mfma_f32_16x16x32_bf16 v[64:67], a[12:15], v[106:109], v[64:67]\n"
        "v_cndmask_b32_e64 v254, v254, v63, s[60:61]\n"
        "v_cndmask_b32_e64 v251, v251, v75, s[60:61]\n"
        "v_mfma_f32_16x16x32_bf16 v[60:63], a[24:27], v[106:109], 0\n"
        "v_cndmask_b32_dpp v249, v254, v249, vcc row_ror:4 row_mask:0xf bank_mask:0xf\n"
        "v_fma_f32 v253, -v253, v252, v249\n"
        "v_mfma_f32_16x16x32_bf16 v[60:63], a[28:31], v[112:115], v[60:63]\n"
        "v_max_f32 v253, 0, v253\n"
        "v_fmaak_f32 v253, v28, v253, 0x3727c5ac\n"
        "v_mfma_f32_16x16x32_bf16 v[60:63], a[32:35], v[118:121], v[60:63]\n"
        "v_sub_f32 v249, 0x47c00000, v249\n"
        "v_bfi_b32 v253, s84, v249, v253\n"
        "v_mfma_f32_16x16x32_bf16 v[40:43], a[44:47], v[118:121], 0\n"
        "buffer_load_dwordx4 v6, s[36:39], s94 offen offset:2048 lds\n"
        "v_cvt_pk_bf16_f32 v90, v64, v65\n"
        "v_mfma_f32_16x16x32_bf16 v[40:43], a[40:43], v[112:115], v[40:43]\n"
        "v_rsq_f32 v254, v253\n"
        "v_mfma_f32_16x16x32_bf16 v[40:43], a[36:39], v[106:109], v[40:43]\n"
        "v_cvt_f16_f32 v254, v254\n"
        "v_cvt_pk_bf16_f32 v91, v66, v67\n"
        "v_mfma_f32_16x16x32_bf16 v[70:73], a[48:51], v[106:109], 0\n"
        "ds_write_b16 v18, v254\n"
        "v_mfma_f32_16x16x32_bf16 v[70:73], a[52:55], v[112:115], v[70:73]\n"
        "ds_write_b16 v19, v254\n"
        "v_mfma_f32_16x16x32_bf16 v[70:73], a[56:59], v[118:121], v[70:73]\n"
        "ds_read_b128 a[252:255], v255 offset:9264\n"
        "v_cvt_pk_bf16_f32 v142, v60, v61\n"
        "v_mfma_f32_16x16x32_bf16 v[80:83], a[68:71], v[118:121], 0\n"
        "ds_read_b128 v[76:79], v255 offset:10288\n"
        "v_cvt_pk_bf16_f32 v143, v62, v63\n"
        "v_mfma_f32_16x16x32_bf16 v[80:83], a[64:67], v[112:115], v[80:83]\n"
        "ds_read_b128 v[96:99], v255 offset:11312\n"
        "v_cvt_pk_bf16_f32 v144, v40, v41\n"
        "v_mfma_f32_16x16x32_bf16 v[80:83], a[60:63], v[106:109], v[80:83]\n"
        "buffer_load_dwordx4 v7, s[36:39], s94 offen offset:3072 lds\n"
        "v_cvt_pk_bf16_f32 v145, v42, v43\n"
        "v_mfma_f32_16x16x32_bf16 v[40:43], v[146:149], v[146:149], 0\n"
        "ds_read_b128 v[64:67], v255 offset:12352\n"
        "v_cvt_pk_bf16_f32 v122, v70, v71\n"
        "v_mfma_f32_16x16x32_bf16 v[40:43], v[152:155], v[152:155], v[40:43]\n"
        "ds_read_b128 v[60:63], v255 offset:13376\n"
        "v_cvt_pk_bf16_f32 v123, v72, v73\n"
        "v_mfma_f32_16x16x32_bf16 v[40:43], v[158:161], v[158:161], v[40:43]\n"
        "ds_read_b128 v[72:75], v255 offset:14400\n"
        "v_cvt_pk_bf16_f32 v124, v80, v81\n"
        "v_mfma_f32_16x16x32_bf16 v[162:165], v[56:59], v[158:161], 0\n"
        "s_waitcnt lgkmcnt(6)\n"
        "ds_read_b128 v[92:95], v255 offset:15440\n"
        "v_cvt_pk_bf16_f32 v125, v82, v83\n"
        "v_mfma_f32_16x16x32_bf16 v[162:165], v[48:51], v[152:155], v[162:165]\n"
        "s_barrier\n"
        "ds_read_b128 v[80:83], v255 offset:16464\n"
        "v_cndmask_b32_e64 v253, v40, v41, s[56:57]\n"
        "v_mfma_f32_16x16x32_bf16 v[162:165], v[84:87], v[146:149], v[162:165]\n"
        "s_add_u32 m0, s52, 8192\n"
        "v_cndmask_b32_e64 v253, v253, v42, s[58:59]\n"
        "v_cndmask_b32_e64 v253, v253, v43, s[60:61]\n"
        "v_mfma_f32_16x16x32_bf16 v[198:201], v[106:109], v[88:91], 0\n"
        "ds_read_b128 v[88:91], v255 offset:17488\n"
        "v_cndmask_b32_e64 v255, v100, v101, s[56:57]\n"
        "v_mfma_f32_16x16x32_bf16 v[198:201], v[112:115], v[142:145], v[198:201]\n"
        "buffer_load_dwordx4 v8, s[36:39], s94 offen lds\n"
        "v_cndmask_b32_e64 v255, v255, v102, s[58:59]\n"
        "v_mfma_f32_16x16x32_bf16 v[198:201], v[118:121], v[122:125], v[198:201]\n"
        "v_cndmask_b32_dpp v254, v162, v68, vcc row_ror:4 row_mask:0xf bank_mask:0xf\n"
        "v_cndmask_b32_e64 v255, v255, v103, s[60:61]\n"
        "v_mfma_f32_16x16x32_bf16 v[40:43], a[8:11], v[158:161], 0\n"
        "v_cndmask_b32_dpp v251, v253, v251, vcc row_ror:4 row_mask:0xf bank_mask:0xf\n"
        "v_cndmask_b32_dpp v254, v254, v52, vcc row_ror:4 row_mask:0xf bank_mask:0xf\n"
        "v_mfma_f32_16x16x32_bf16 v[40:43], a[4:7], v[152:155], v[40:43]\n"
        "v_cndmask_b32_dpp v255, v251, v255, vcc row_ror:4 row_mask:0xf bank_mask:0xf\n"
        "v_mul_f32 v253, s80, v254\n"
        "v_mfma_f32_16x16x32_bf16 v[40:43], a[0:3], v[146:149], v[40:43]\n"
        "v_fma_f32 v253, -v253, v254, v255\n"
        "v_max_f32 v253, 0, v253\n"
        "v_mfma_f32_16x16x32_bf16 v[100:103], a[12:15], v[146:149], 0\n"
        "s_nop 0\n"
        "v_fmaak_f32 v253, v28, v253, 0x3727c5ac\n"
        "v_cndmask_b32_dpp v255, v163, v69, vcc row_ror:4 row_mask:0xf bank_mask:0xf\n"
        "v_mfma_f32_16x16x32_bf16 v[100:103], a[16:19], v[152:155], v[100:103]\n"
        "s_nop 0\n"
        "v_cvt_pk_bf16_f32 v68, v40, v41\n"
        "v_cvt_pk_bf16_f32 v69, v42, v43\n"
        "v_mfma_f32_16x16x32_bf16 v[100:103], a[20:23], v[158:161], v[100:103]\n"
        "s_waitcnt lgkmcnt(0)\n"
        "v_rsq_f32 v250, v253\n"
        "v_mfma_f32_16x16x32_bf16 v[40:43], a[32:35], v[158:161], 0\n"
        "s_nop 0\n"
        "v_cndmask_b32_dpp v255, v255, v53, vcc row_ror:4 row_mask:0xf bank_mask:0xf\n"
        "s_nop 0\n"
        "v_mfma_f32_16x16x32_bf16 v[40:43], a[28:31], v[152:155], v[40:43]\n"
        "s_nop 0\n"
        "v_cvt_pk_bf16_f32 v70, v100, v101\n"
        "v_cvt_pk_bf16_f32 v71, v102, v103\n"
        "v_mfma_f32_16x16x32_bf16 v[40:43], a[24:27], v[146:149], v[40:43]\n"
        "s_nop 1\n"
        "v_mfma_f32_16x16x32_bf16 v[52:55], a[36:39], v[146:149], 0\n"
        "s_nop 2\n"
        "v_mfma_f32_16x16x32_bf16 v[52:55], a[40:43], v[152:155], v[52:55]\n"
        "s_nop 0\n"
        "v_cvt_pk_bf16_f32 v214, v40, v41\n"
        "v_cvt_pk_bf16_f32 v215, v42, v43\n"
        "v_mfma_f32_16x16x32_bf16 v[52:55], a[44:47], v[158:161], v[52:55]\n"
        "s_nop 0\n"
        "v_mfma_f32_16x16x32_bf16 v[40:43], a[56:59], v[158:161], 0\n"
        "s_nop 0\n"
        "ds_read_b128 v[174:177], v13\n"
        "ds_read_b128 v[178:181], v13 offset:1024\n"
        "v_mfma_f32_16x16x32_bf16 v[40:43], a[52:55], v[152:155], v[40:43]\n"
        "s_nop 0\n"
        "ds_read_b128 v[194:197], v13 offset:2048\n"
        "v_cvt_pk_bf16_f32 v216, v52, v53\n"
        "v_mfma_f32_16x16x32_bf16 v[40:43], a[48:51], v[146:149], v[40:43]\n"
        "v_mov_b64 v[210:211], 0\n"
        "s_mov_b64 exec, 1\n"
        "buffer_atomic_add v210, off, s[16:19], 0 sc0\n"
        "buffer_atomic_add v211, off, s[16:19], s15 offset:4 sc0\n"
        "s_mov_b64 exec, -1\n"
        "v_mfma_f32_16x16x32_bf16 v[218:221], a[60:63], v[146:149], 0\n"
        "v_cvt_pk_bf16_f32 v217, v54, v55\n"
        "v_mfma_f32_16x16x32_bf16 v[218:221], a[64:67], v[152:155], v[218:221]\n"
        "v_cvt_pk_bf16_f32 v162, v40, v41\n"
        "v_cvt_pk_bf16_f32 v163, v42, v43\n"
        "v_mfma_f32_16x16x32_bf16 v[218:221], a[68:71], v[158:161], v[218:221]\n"
        "v_mfma_f32_16x16x32_bf16 v[52:55], v[138:141], a[80:83], 0\n"
        "v_mfma_f32_16x16x32_bf16 v[52:55], v[132:135], a[76:79], v[52:55]\n"
        "ds_read_b64_tr_b16 v[100:101], v167\n"
        "ds_read_b64_tr_b16 v[102:103], v167 offset:3072\n"
        "v_mfma_f32_16x16x32_bf16 v[52:55], v[126:129], a[72:75], v[52:55]\n"
        "s_waitcnt lgkmcnt(2)\n"
        "ds_read_b64_tr_b16 v[122:123], v167 offset:6144\n"
        "ds_read_b64_tr_b16 v[124:125], v167 offset:9216\n"
        "v_mfma_f32_16x16x32_bf16 v[142:145], v[126:129], a[84:87], 0\n"
        "ds_read_b64_tr_b16 v[206:207], v167 offset:12288\n"
        "ds_read_b128 v[182:185], v13 offset:3072\n"
        "v_mfma_f32_16x16x32_bf16 v[142:145], v[132:135], a[88:91], v[142:145]\n"
        "ds_read_b128 v[190:193], v13 offset:4096\n"
        "ds_read_b128 v[186:189], v13 offset:5120\n"
        "v_mfma_f32_16x16x32_bf16 v[142:145], v[138:141], a[92:95], v[142:145]\n"
        "ds_read_b128 v[202:205], v20\n"
        "ds_read_b64 v[212:213], v21\n"
        "v_mfma_f32_16x16x32_bf16 v[222:225], v[146:149], v[68:71], 0\n"
        "ds_read_b64_tr_b16 v[208:209], v167 offset:15360\n"
        "v_cvt_pk_bf16_f32 v164, v218, v219\n"
        "v_mfma_f32_16x16x32_bf16 v[222:225], v[152:155], v[214:217], v[222:225]\n"
        "v_cvt_pk_bf16_f32 v165, v220, v221\n"
        "v_cvt_pk_bf16_f32 v42, v52, v53\n"
        "s_nop 0\n"
        "v_mfma_f32_16x16x32_bf16 v[222:225], v[158:161], v[162:165], v[222:225]\n"
        "s_nop 0\n"
        "v_cvt_pk_bf16_f32 v43, v54, v55\n"
        "v_cvt_pk_bf16_f32 v46, v142, v143\n"
        "v_mfma_f32_16x16x32_bf16 v[162:165], a[152:155], v[194:197], 0\n"
        "s_nop 0\n"
        "v_cvt_pk_bf16_f32 v47, v144, v145\n"
        "v_mfma_f32_16x16x32_bf16 v[162:165], a[148:151], v[178:181], v[162:165]\n"
        "s_nop 0\n"
        "v_cndmask_b32_dpp v251, v222, v198, vcc row_ror:4 row_mask:0xf bank_mask:0xf\n"
        "v_cndmask_b32_dpp v252, v223, v199, vcc row_ror:4 row_mask:0xf bank_mask:0xf\n"
        "v_mfma_f32_16x16x32_bf16 v[162:165], a[144:147], v[174:177], v[162:165]\n"
        "v_cndmask_b32_dpp v253, v224, v200, vcc row_ror:4 row_mask:0xf bank_mask:0xf\n"
        "v_cndmask_b32_dpp v166, v225, v201, vcc row_ror:4 row_mask:0xf bank_mask:0xf\n"
        "v_mfma_f32_16x16x32_bf16 v[142:145], a[156:159], v[174:177], 0\n"
        "v_cndmask_b32_dpp v251, v251, v34, vcc row_ror:4 row_mask:0xf bank_mask:0xf\n"
        "v_cndmask_b32_dpp v252, v252, v35, vcc row_ror:4 row_mask:0xf bank_mask:0xf\n"
        "v_mfma_f32_16x16x32_bf16 v[142:145], a[160:163], v[178:181], v[142:145]\n"
        "s_waitcnt lgkmcnt(1)\n"
        "ds_read_b64_tr_b16 v[214:215], v167 offset:8\n"
        "ds_read_b64_tr_b16 v[216:217], v167 offset:3080\n"
        "v_mfma_f32_16x16x32_bf16 v[142:145], a[164:167], v[194:197], v[142:145]\n"
        "s_waitcnt lgkmcnt(2)\n"
        "ds_read_b64_tr_b16 v[68:69], v167 offset:6152\n"
        "ds_read_b64_tr_b16 v[70:71], v167 offset:9224\n"
        "v_mfma_f32_16x16x32_bf16 v[52:55], v[206:209], v[194:197], 0\n"
        "s_barrier\n"
        "ds_read_b64_tr_b16 v[220:221], v167 offset:12296\n"
        "ds_read_b64_tr_b16 v[222:223], v167 offset:15368\n"
        "v_mfma_f32_16x16x32_bf16 v[52:55], v[122:125], v[178:181], v[52:55]\n"
        "v_cvt_pk_bf16_f32 v198, v162, v163\n"
        "v_cvt_pk_bf16_f32 v199, v164, v165\n"
        "v_mfma_f32_16x16x32_bf16 v[52:55], v[100:103], v[174:177], v[52:55]\n"
        "s_nop 0\n"
        "v_cvt_pk_bf16_f32 v200, v142, v143\n"
        "v_cvt_pk_bf16_f32 v201, v144, v145\n"
        "v_mfma_f32_16x16x32_bf16 v[142:145], v[100:103], v[182:185], 0\n"
        "s_nop 0\n"
        "ds_write_b128 v168, v[198:201]\n"
        "v_mfma_f32_16x16x32_bf16 v[142:145], v[122:125], v[190:193], v[142:145]\n"
        "s_nop 0\n"
        "v_cvt_pk_bf16_f32 v100, v52, v53\n"
        "v_cvt_pk_bf16_f32 v101, v54, v55\n"
        "v_mfma_f32_16x16x32_bf16 v[142:145], v[206:209], v[186:189], v[142:145]\n"
        "v_cndmask_b32_dpp v253, v253, v36, vcc row_ror:4 row_mask:0xf bank_mask:0xf\n"
        "v_cndmask_b32_dpp v166, v166, v37, vcc row_ror:4 row_mask:0xf bank_mask:0xf\n"
        "v_mfma_f32_16x16x32_bf16 v[102:105], a[176:179], v[194:197], 0\n"
        "s_nop 0\n"
        "v_mul_f32_dpp v251, v250, v251 quad_perm:[0,0,0,0] row_mask:0xf bank_mask:0xf\n"
        "v_mul_f32_dpp v252, v250, v252 quad_perm:[1,1,1,1] row_mask:0xf bank_mask:0xf\n"
        "v_mfma_f32_16x16x32_bf16 v[102:105], a[172:175], v[178:181], v[102:105]\n"
        "s_waitcnt lgkmcnt(1)\n"
        "v_cvt_pk_bf16_f32 v218, v142, v143\n"
        "v_cvt_pk_bf16_f32 v219, v144, v145\n"
        "v_mfma_f32_16x16x32_bf16 v[102:105], a[168:171], v[174:177], v[102:105]\n"
        "v_mul_f32_dpp v253, v250, v253 quad_perm:[2,2,2,2] row_mask:0xf bank_mask:0xf\n"
        "v_mul_f32_dpp v166, v250, v166 quad_perm:[3,3,3,3] row_mask:0xf bank_mask:0xf\n"
        "v_mfma_f32_16x16x32_bf16 v[122:125], a[180:183], v[174:177], 0\n"
        "v_max3_f32 v249, v251, v252, v253\n"
        "v_max_f32 v249, v249, v166\n"
        "v_mfma_f32_16x16x32_bf16 v[122:125], a[184:187], v[178:181], v[122:125]\n"
        "ds_read_b64_tr_b16 v[34:35], v167 offset:1024\n"
        "v_mul_f32 v249, v249, v250\n"
        "v_mfma_f32_16x16x32_bf16 v[122:125], a[188:191], v[194:197], v[122:125]\n"
        "ds_read_b64_tr_b16 v[36:37], v167 offset:4096\n"
        "v_fma_f32 v251, v251, v250, -v249\n"
        "v_mfma_f32_16x16x32_bf16 v[226:229], v[220:223], v[194:197], 0\n"
        "v_fma_f32 v252, v252, v250, -v249\n"
        "v_fma_f32 v253, v253, v250, -v249\n"
        "v_mfma_f32_16x16x32_bf16 v[226:229], v[68:71], v[178:181], v[226:229]\n"
        "ds_read_b64_tr_b16 v[234:235], v167 offset:7168\n"
        "v_fma_f32 v166, v166, v250, -v249\n"
        "v_mfma_f32_16x16x32_bf16 v[226:229], v[214:217], v[174:177], v[226:229]\n"
        "ds_read_b64_tr_b16 v[236:237], v167 offset:10240\n"
        "v_cvt_pk_bf16_f32 v206, v102, v103\n"
        "v_mfma_f32_16x16x32_bf16 v[52:55], v[214:217], v[182:185], 0\n"
        "ds_read_b64_tr_b16 v[102:103], v167 offset:13312\n"
        "v_cvt_pk_bf16_f32 v207, v104, v105\n"
        "v_mfma_f32_16x16x32_bf16 v[52:55], v[68:71], v[190:193], v[52:55]\n"
        "ds_read_b64_tr_b16 v[104:105], v167 offset:16384\n"
        "v_cvt_pk_bf16_f32 v208, v122, v123\n"
        "v_mfma_f32_16x16x32_bf16 v[52:55], v[220:223], v[186:189], v[52:55]\n"
        "v_exp_f32 v251, v251\n"
        "v_mfma_f32_16x16x32_bf16 v[230:233], a[200:203], v[194:197], 0\n"
        "v_exp_f32 v252, v252\n"
        "v_mfma_f32_16x16x32_bf16 v[230:233], a[196:199], v[178:181], v[230:233]\n"
        "v_exp_f32 v253, v253\n"
        "v_mfma_f32_16x16x32_bf16 v[230:233], a[192:195], v[174:177], v[230:233]\n"
        "v_exp_f32 v166, v166\n"
        "v_mfma_f32_16x16x32_bf16 v[162:165], a[204:207], v[174:177], 0\n"
        "v_add_f32 v245, v251, v252\n"
        "v_add_f32 v249, v253, v166\n"
        "v_mfma_f32_16x16x32_bf16 v[162:165], a[208:211], v[178:181], v[162:165]\n"
        "v_add_f32 v245, v245, v249\n"
        "v_cvt_pk_bf16_f32 v209, v124, v125\n"
        "v_mfma_f32_16x16x32_bf16 v[162:165], a[212:215], v[194:197], v[162:165]\n"
        "v_rcp_f32 v245, v245\n"
        "s_waitcnt lgkmcnt(0)\n"
        "v_mfma_f32_16x16x32_bf16 v[68:71], v[102:105], v[194:197], 0\n"
        "v_mul_f32 v251, v251, v245\n"
        "v_mul_f32 v252, v252, v245\n"
        "v_mfma_f32_16x16x32_bf16 v[68:71], v[234:237], v[178:181], v[68:71]\n"
        "ds_read_b64_tr_b16 v[246:247], v167 offset:1032\n"
        "v_mul_f32 v253, v253, v245\n"
        "v_mfma_f32_16x16x32_bf16 v[68:71], v[34:37], v[174:177], v[68:71]\n"
        "ds_read_b64_tr_b16 v[248:249], v167 offset:4104\n"
        "v_mul_f32 v166, v166, v245\n"
        "v_mfma_f32_16x16x32_bf16 v[214:217], v[34:37], v[182:185], 0\n"
        "ds_read_b64_tr_b16 v[142:143], v167 offset:7176\n"
        "v_mul_f32_dpp v251, v250, v251 quad_perm:[0,0,0,0] row_mask:0xf bank_mask:0xf\n"
        "v_mfma_f32_16x16x32_bf16 v[214:217], v[234:237], v[190:193], v[214:217]\n"
        "ds_read_b64_tr_b16 v[144:145], v167 offset:10248\n"
        "v_mul_f32_dpp v252, v250, v252 quad_perm:[1,1,1,1] row_mask:0xf bank_mask:0xf\n"
        "v_mfma_f32_16x16x32_bf16 v[214:217], v[102:105], v[186:189], v[214:217]\n"
        "ds_read_b64_tr_b16 v[222:223], v167 offset:13320\n"
        "v_mul_f32_dpp v253, v250, v253 quad_perm:[2,2,2,2] row_mask:0xf bank_mask:0xf\n"
        "v_mfma_f32_16x16x32_bf16 v[34:37], v[138:141], a[104:107], 0\n"
        "ds_read_b64_tr_b16 v[224:225], v167 offset:16392\n"
        "v_mul_f32_dpp v166, v250, v166 quad_perm:[3,3,3,3] row_mask:0xf bank_mask:0xf\n"
        "v_mfma_f32_16x16x32_bf16 v[34:37], v[132:135], a[100:103], v[34:37]\n"
        "ds_write_b128 v168, v[206:209] offset:64\n"
        "v_mfma_f32_16x16x32_bf16 v[34:37], v[126:129], a[96:99], v[34:37]\n"
        "v_cvt_pk_bf16_f32 v213, v251, v252\n"
        "v_cvt_pk_bf16_f32 v250, v253, v166\n"
        "v_mfma_f32_16x16x32_bf16 v[242:245], v[126:129], a[108:111], 0\n"
        "v_and_b32 v124, v25, v213\n"
        "v_and_b32 v125, v25, v250\n"
        "v_mfma_f32_16x16x32_bf16 v[242:245], v[132:135], a[112:115], v[242:245]\n"
        "v_mov_b64 v[130:131], v[124:125]\n"
        "v_cvt_pk_bf16_f32 v102, v226, v227\n"
        "v_mfma_f32_16x16x32_bf16 v[242:245], v[138:141], a[116:119], v[242:245]\n"
        "v_cvt_pk_bf16_f32 v103, v228, v229\n"
        "v_cvt_pk_bf16_f32 v220, v52, v53\n"
        "v_mfma_f32_16x16x32_bf16 v[238:241], v[42:45], v[124:127], 0\n"
        "ds_write_b128 v168, v[100:103] offset:192\n"
        "v_mfma_f32_16x16x32_bf16 v[234:237], v[44:47], v[128:131], 0\n"
        "s_waitcnt lgkmcnt(2)\n"
        "v_cvt_pk_bf16_f32 v221, v54, v55\n"
        "v_cvt_pk_bf16_f32 v52, v230, v231\n"
        "v_mfma_f32_16x16x32_bf16 v[102:105], v[222:225], v[194:197], 0\n"
        "ds_write_b128 v169, v[218:221] offset:192\n"
        "v_mfma_f32_16x16x32_bf16 v[102:105], v[142:145], v[178:181], v[102:105]\n"
        "ds_read_b64_tr_b16 v[230:231], v167 offset:2048\n"
        "v_cvt_pk_bf16_f32 v53, v232, v233\n"
        "v_mfma_f32_16x16x32_bf16 v[102:105], v[246:249], v[174:177], v[102:105]\n"
        "ds_read_b64_tr_b16 v[232:233], v167 offset:5120\n"
        "v_cvt_pk_bf16_f32 v54, v162, v163\n"
        "v_mfma_f32_16x16x32_bf16 v[226:229], v[246:249], v[182:185], 0\n"
        "ds_read_b64_tr_b16 v[218:219], v167 offset:8192\n"
        "v_cvt_pk_bf16_f32 v55, v164, v165\n"
        "v_mfma_f32_16x16x32_bf16 v[226:229], v[142:145], v[190:193], v[226:229]\n"
        "ds_write_b128 v168, v[52:55] offset:128\n"
        "v_mfma_f32_16x16x32_bf16 v[226:229], v[222:225], v[186:189], v[226:229]\n"
        "ds_read_b64_tr_b16 v[220:221], v167 offset:11264\n"
        "v_mov_b64 v[136:137], v[124:125]\n"
        "v_mfma_f32_16x16x32_bf16 v[40:43], v[138:141], a[128:131], 0\n"
        "ds_read_b64_tr_b16 v[222:223], v167 offset:14336\n"
        "v_cvt_pk_bf16_f32 v142, v68, v69\n"
        "v_mfma_f32_16x16x32_bf16 v[40:43], v[132:135], a[124:127], v[40:43]\n"
        "ds_read_b64_tr_b16 v[224:225], v167 offset:17408\n"
        "v_cvt_pk_bf16_f32 v143, v70, v71\n"
        "v_mfma_f32_16x16x32_bf16 v[40:43], v[126:129], a[120:123], v[40:43]\n"
        "ds_read_b64_tr_b16 v[68:69], v167 offset:2056\n"
        "v_cvt_pk_bf16_f32 v100, v214, v215\n"
        "v_mfma_f32_16x16x32_bf16 v[162:165], v[126:129], a[132:135], 0\n"
        "ds_read_b64_tr_b16 v[70:71], v167 offset:5128\n"
        "v_cvt_pk_bf16_f32 v101, v216, v217\n"
        "v_mfma_f32_16x16x32_bf16 v[162:165], v[132:135], a[136:139], v[162:165]\n"
        "ds_read_b64_tr_b16 v[214:215], v167 offset:8200\n"
        "v_cvt_pk_bf16_f32 v30, v34, v35\n"
        "v_mfma_f32_16x16x32_bf16 v[162:165], v[138:141], a[140:143], v[162:165]\n"
        "ds_read_b64_tr_b16 v[216:217], v167 offset:11272\n"
        "v_cvt_pk_bf16_f32 v31, v36, v37\n"
        "s_nop 1\n"
        "v_mfma_f32_16x16x32_bf16 v[126:129], v[30:33], v[130:133], 0\n"
        "ds_read_b64_tr_b16 v[246:247], v167 offset:14344\n"
        "v_cvt_pk_bf16_f32 v34, v242, v243\n"
        "v_cvt_pk_bf16_f32 v35, v244, v245\n"
        "s_nop 1\n"
        "v_mfma_f32_16x16x32_bf16 v[130:133], v[32:35], v[134:137], 0\n"
        "ds_read_b64_tr_b16 v[248:249], v167 offset:17416\n"
        "v_cvt_pk_bf16_f32 v144, v102, v103\n"
        "s_waitcnt lgkmcnt(6)\n"
        "v_mfma_f32_16x16x32_bf16 v[242:245], v[222:225], v[194:197], 0\n"
        "v_cvt_pk_bf16_f32 v145, v104, v105\n"
        "v_cvt_pk_bf16_f32 v102, v226, v227\n"
        "v_mfma_f32_16x16x32_bf16 v[242:245], v[218:221], v[178:181], v[242:245]\n"
        "ds_write_b128 v168, v[142:145] offset:256\n"
        "v_mfma_f32_16x16x32_bf16 v[242:245], v[230:233], v[174:177], v[242:245]\n"
        "v_cvt_pk_bf16_f32 v103, v228, v229\n"
        "v_cvt_pk_bf16_f32 v36, v40, v41\n"
        "v_mfma_f32_16x16x32_bf16 v[226:229], v[230:233], v[182:185], 0\n"
        "ds_write_b128 v169, v[100:103] offset:256\n"
        "v_mfma_f32_16x16x32_bf16 v[226:229], v[218:221], v[190:193], v[226:229]\n"
        "v_cvt_pk_bf16_f32 v37, v42, v43\n"
        "v_mov_b64 v[142:143], v[124:125]\n"
        "v_mfma_f32_16x16x32_bf16 v[226:229], v[222:225], v[186:189], v[226:229]\n"
        "s_waitcnt lgkmcnt(2)\n"
        "v_cvt_pk_bf16_f32 v40, v162, v163\n"
        "v_cvt_pk_bf16_f32 v41, v164, v165\n"
        "v_mfma_f32_16x16x32_bf16 v[100:103], v[246:249], v[194:197], 0\n"
        "s_barrier\n"
        "v_cvt_pk_bf16_f32 v224, v242, v243\n"
        "v_cvt_pk_bf16_f32 v225, v244, v245\n"
        "v_mfma_f32_16x16x32_bf16 v[100:103], v[214:217], v[178:181], v[100:103]\n"
        "v_cvt_pk_bf16_f32 v222, v226, v227\n"
        "v_cvt_pk_bf16_f32 v223, v228, v229\n"
        "v_mfma_f32_16x16x32_bf16 v[100:103], v[68:71], v[174:177], v[100:103]\n"
        "v_cvt_pk_bf16_f32 v162, v238, v239\n"
        "v_cvt_pk_bf16_f32 v163, v236, v237\n"
        "v_mfma_f32_16x16x32_bf16 v[218:221], v[68:71], v[182:185], 0\n"
        "s_nop 0\n"
        "v_cvt_pk_bf16_f32 v164, v130, v131\n"
        "v_cvt_pk_bf16_f32 v122, v240, v241\n"
        "v_mfma_f32_16x16x32_bf16 v[218:221], v[214:217], v[190:193], v[218:221]\n"
        "s_nop 0\n"
        "v_cvt_pk_bf16_f32 v226, v100, v101\n"
        "v_cvt_pk_bf16_f32 v227, v102, v103\n"
        "v_mfma_f32_16x16x32_bf16 v[218:221], v[246:249], v[186:189], v[218:221]\n"
        "s_nop 0\n"
        "ds_write_b128 v168, v[224:227] offset:320\n"
        "v_mfma_f32_16x16x32_bf16 v[214:217], v[118:121], a[80:83], 0\n"
        "s_nop 0\n"
        "v_cvt_pk_bf16_f32 v123, v126, v127\n"
        "v_cvt_pk_bf16_f32 v124, v132, v133\n"
        "v_mfma_f32_16x16x32_bf16 v[214:217], v[112:115], a[76:79], v[214:217]\n"
        "s_nop 0\n"
        "v_cvt_pk_bf16_f32 v224, v218, v219\n"
        "v_cvt_pk_bf16_f32 v225, v220, v221\n"
        "v_mfma_f32_16x16x32_bf16 v[214:217], v[106:109], a[72:75], v[214:217]\n"
        "s_nop 0\n"
        "ds_read_b128 v[68:71], v27 offset:64\n"
        "ds_read_b128 v[100:103], v27 offset:128\n"
        "v_mfma_f32_16x16x32_bf16 v[130:133], v[106:109], a[84:87], 0\n"
        "s_nop 0\n"
        "ds_write_b128 v169, v[222:225] offset:320\n"
        "v_mfma_f32_16x16x32_bf16 v[130:133], v[112:115], a[88:91], v[130:133]\n"
        "s_nop 0\n"
        "v_cvt_pk_bf16_f32 v42, v214, v215\n"
        "v_cvt_pk_bf16_f32 v43, v216, v217\n"
        "v_mfma_f32_16x16x32_bf16 v[130:133], v[118:121], a[92:95], v[130:133]\n"
        "v_cvt_pk_bf16_f32 v134, v234, v235\n"
        "v_cvt_pk_bf16_f32 v135, v128, v129\n"
        "v_mfma_f32_16x16x32_bf16 v[214:217], v[36:39], v[136:139], 0\n"
        "s_nop 0\n"
        "v_cvt_f32_f16_sdwa v249, v212 dst_sel:DWORD dst_unused:UNUSED_PAD src0_sel:WORD_0\n"
        "v_lshrrev_b32 v31, 0, v212\n"
        "v_mfma_f32_16x16x32_bf16 v[34:37], v[38:41], v[140:143], 0\n"
        "s_nop 0\n"
        "v_cvt_pk_bf16_f32 v46, v130, v131\n"
        "v_cvt_pk_bf16_f32 v47, v132, v133\n"
        "v_mfma_f32_16x16x32_bf16 v[126:129], v[174:177], v[198:201], 0\n"
        "v_cvt_pk_bf16_f32 v165, v216, v217\n"
        "v_cvt_pk_bf16_f32 v136, v214, v215\n"
        "v_mfma_f32_16x16x32_bf16 v[126:129], v[178:181], v[206:209], v[126:129]\n"
        "s_waitcnt lgkmcnt(1)\n"
        "ds_write_b128 v12, v[162:165]\n"
        "v_mfma_f32_16x16x32_bf16 v[126:129], v[194:197], v[52:55], v[126:129]\n"
        "v_cvt_pk_bf16_f32 v125, v34, v35\n"
        "v_cvt_pk_bf16_f32 v137, v36, v37\n"
        "v_mfma_f32_16x16x32_bf16 v[130:133], v[194:197], a[224:227], 0\n"
        "ds_write_b128 v12, v[122:125] offset:1024\n"
        "v_mfma_f32_16x16x32_bf16 v[130:133], v[178:181], a[220:223], v[130:133]\n"
        "ds_write_b128 v12, v[134:137] offset:2048\n"
        "v_mfma_f32_16x16x32_bf16 v[130:133], v[174:177], a[216:219], v[130:133]\n"
        "s_nop 0\n"
        "v_cndmask_b32_e64 v30, v126, v127, s[56:57]\n"
        "v_cndmask_b32_e64 v30, v30, v128, s[58:59]\n"
        "v_mfma_f32_16x16x32_bf16 v[198:201], v[174:177], a[228:231], 0\n"
        "v_cndmask_b32_e64 v30, v30, v129, s[60:61]\n"
        "v_mul_f32 v30, v30, v249\n"
        "v_mfma_f32_16x16x32_bf16 v[198:201], v[178:181], a[232:235], v[198:201]\n"
        "v_mul_f32 v30, v30, v249\n"
        "v_and_b32_dpp v104, v213, v25 row_ror:12 row_mask:0xf bank_mask:0xf\n"
        "v_mfma_f32_16x16x32_bf16 v[198:201], v[194:197], a[236:239], v[198:201]\n"
        "v_and_b32_dpp v105, v250, v25 row_ror:12 row_mask:0xf bank_mask:0xf\n"
        "v_mov_b64 v[110:111], v[104:105]\n"
        "v_mfma_f32_16x16x32_bf16 v[34:37], v[174:177], v[68:71], 0\n"
        "v_exp_f32 v30, v30\n"
        "v_mfma_f32_16x16x32_bf16 v[138:141], v[174:177], v[100:103], 0\n"
        "ds_write_b64 v170, v[30:31]\n"
        "v_mfma_f32_16x16x32_bf16 v[206:209], v[118:121], a[104:107], 0\n"
        "v_cvt_f32_f16_sdwa v249, v202 dst_sel:DWORD dst_unused:UNUSED_PAD src0_sel:WORD_0\n"
        "v_mul_f32 v130, v130, v249\n"
        "v_mfma_f32_16x16x32_bf16 v[206:209], v[112:115], a[100:103], v[206:209]\n"
        "v_mul_f32 v198, v198, v249\n"
        "v_cvt_pk_f16_f32 v130, v130, v198\n"
        "v_mfma_f32_16x16x32_bf16 v[206:209], v[106:109], a[96:99], v[206:209]\n"
        "v_cvt_f32_f16_sdwa v247, v202 dst_sel:DWORD dst_unused:UNUSED_PAD src0_sel:WORD_1\n"
        "v_mul_f32 v131, v131, v247\n"
        "v_mfma_f32_16x16x32_bf16 v[214:217], v[106:109], a[108:111], 0\n"
        "v_mul_f32 v199, v199, v247\n"
        "v_cvt_pk_f16_f32 v131, v131, v199\n"
        "v_mfma_f32_16x16x32_bf16 v[214:217], v[112:115], a[112:115], v[214:217]\n"
        "v_perm_b32 v30, v130, v34, s54\n"
        "v_perm_b32 v34, v131, v35, s54\n"
        "v_mfma_f32_16x16x32_bf16 v[214:217], v[118:121], a[116:119], v[214:217]\n"
        "v_cvt_f32_f16_sdwa v248, v203 dst_sel:DWORD dst_unused:UNUSED_PAD src0_sel:WORD_0\n"
        "v_mul_f32 v132, v132, v248\n"
        "v_mfma_f32_16x16x32_bf16 v[126:129], v[42:45], v[104:107], 0\n"
        "v_mul_f32 v200, v200, v248\n"
        "v_cvt_pk_f16_f32 v132, v132, v200\n"
        "v_mfma_f32_16x16x32_bf16 v[52:55], v[44:47], v[108:111], 0\n"
        "v_perm_b32 v31, v130, v138, s55\n"
        "v_perm_b32 v46, v132, v36, s54\n"
        "v_mfma_f32_16x16x32_bf16 v[40:43], v[194:197], a[248:251], 0\n"
        "ds_write_b64 v172, v[30:31]\n"
        "v_mfma_f32_16x16x32_bf16 v[40:43], v[178:181], a[244:247], v[40:43]\n"
        "v_perm_b32 v35, v131, v139, s55\n"
        "v_perm_b32 v47, v132, v140, s55\n"
        "v_mfma_f32_16x16x32_bf16 v[40:43], v[174:177], a[240:243], v[40:43]\n"
        "ds_write_b64 v172, v[34:35] offset:6336\n"
        "v_mfma_f32_16x16x32_bf16 v[142:145], v[174:177], a[252:255], 0\n"
        "ds_write_b64 v172, v[46:47] offset:12672\n"
        "v_mfma_f32_16x16x32_bf16 v[142:145], v[178:181], v[76:79], v[142:145]\n"
        "v_cvt_f32_f16_sdwa v246, v203 dst_sel:DWORD dst_unused:UNUSED_PAD src0_sel:WORD_1\n"
        "v_mul_f32 v133, v133, v246\n"
        "v_mfma_f32_16x16x32_bf16 v[142:145], v[194:197], v[96:99], v[142:145]\n"
        "v_mul_f32 v201, v201, v246\n"
        "v_cvt_pk_f16_f32 v133, v133, v201\n"
        "v_mfma_f32_16x16x32_bf16 v[218:221], v[178:181], v[68:71], 0\n"
        "v_perm_b32 v30, v133, v37, s54\n"
        "v_perm_b32 v31, v133, v141, s55\n"
        "v_mfma_f32_16x16x32_bf16 v[130:133], v[178:181], v[100:103], 0\n"
        "ds_write_b64 v172, v[30:31] offset:19008\n"
        "v_mfma_f32_16x16x32_bf16 v[198:201], v[162:165], v[162:165], 0\n"
        "v_cvt_pk_bf16_f32 v30, v206, v207\n"
        "v_cvt_pk_bf16_f32 v31, v208, v209\n"
        "v_mfma_f32_16x16x32_bf16 v[198:201], v[122:125], v[122:125], v[198:201]\n"
        "v_cvt_pk_bf16_f32 v34, v214, v215\n"
        "v_cvt_pk_bf16_f32 v35, v216, v217\n"
        "v_mfma_f32_16x16x32_bf16 v[198:201], v[134:137], v[134:137], v[198:201]\n"
        "v_mul_f32 v40, v40, v249\n"
        "v_mul_f32 v142, v142, v249\n"
        "v_mfma_f32_16x16x32_bf16 v[138:141], a[152:155], v[186:189], 0\n"
        "v_mul_f32 v41, v41, v247\n"
        "v_mul_f32 v143, v143, v247\n"
        "v_mfma_f32_16x16x32_bf16 v[138:141], a[148:151], v[190:193], v[138:141]\n"
        "v_cvt_pk_f16_f32 v40, v40, v142\n"
        "v_cvt_pk_f16_f32 v41, v41, v143\n"
        "v_mfma_f32_16x16x32_bf16 v[138:141], a[144:147], v[182:185], v[138:141]\n"
        "v_mul_f32 v42, v42, v248\n"
        "v_mul_f32 v144, v144, v248\n"
        "v_mfma_f32_16x16x32_bf16 v[122:125], a[156:159], v[182:185], 0\n"
        "v_mov_b64 v[116:117], v[104:105]\n"
        "v_cvt_pk_f16_f32 v42, v42, v144\n"
        "v_mfma_f32_16x16x32_bf16 v[122:125], a[160:163], v[190:193], v[122:125]\n"
        "v_perm_b32 v46, v40, v218, s54\n"
        "v_perm_b32 v150, v41, v219, s54\n"
        "v_mfma_f32_16x16x32_bf16 v[122:125], a[164:167], v[186:189], v[122:125]\n"
        "v_perm_b32 v36, v42, v220, s54\n"
        "v_mul_f32 v43, v43, v246\n"
        "v_mfma_f32_16x16x32_bf16 v[134:137], v[118:121], a[128:131], 0\n"
        "v_mul_f32 v145, v145, v246\n"
        "v_cvt_pk_f16_f32 v43, v43, v145\n"
        "v_mfma_f32_16x16x32_bf16 v[134:137], v[112:115], a[124:127], v[134:137]\n"
        "v_perm_b32 v47, v40, v130, s55\n"
        "v_perm_b32 v40, v43, v221, s54\n"
        "v_mfma_f32_16x16x32_bf16 v[134:137], v[106:109], a[120:123], v[134:137]\n"
        "ds_write_b64 v172, v[46:47] offset:8\n"
        "v_mfma_f32_16x16x32_bf16 v[142:145], v[106:109], a[132:135], 0\n"
        "v_perm_b32 v151, v41, v131, s55\n"
        "v_perm_b32 v37, v42, v132, s55\n"
        "v_mfma_f32_16x16x32_bf16 v[142:145], v[112:115], a[136:139], v[142:145]\n"
        "ds_write_b64 v172, v[150:151] offset:6344\n"
        "v_mfma_f32_16x16x32_bf16 v[142:145], v[118:121], a[140:143], v[142:145]\n"
        "ds_write_b64 v172, v[36:37] offset:12680\n"
        "v_mfma_f32_16x16x32_bf16 v[106:109], v[30:33], v[110:113], 0\n"
        "v_perm_b32 v41, v43, v133, s55\n"
        "v_cvt_pk_bf16_f32 v112, v138, v139\n"
        "v_mfma_f32_16x16x32_bf16 v[206:209], v[32:35], v[114:117], 0\n"
        "ds_write_b64 v172, v[40:41] offset:19016\n"
        "v_mfma_f32_16x16x32_bf16 v[130:133], v[194:197], v[72:75], 0\n"
        "v_cvt_pk_bf16_f32 v113, v140, v141\n"
        "v_cvt_pk_bf16_f32 v114, v122, v123\n"
        "v_mfma_f32_16x16x32_bf16 v[130:133], v[178:181], v[60:63], v[130:133]\n"
        "v_cvt_pk_bf16_f32 v115, v124, v125\n"
        "v_cvt_pk_bf16_f32 v36, v134, v135\n"
        "v_mfma_f32_16x16x32_bf16 v[130:133], v[174:177], v[64:67], v[130:133]\n"
        "v_cvt_pk_bf16_f32 v37, v136, v137\n"
        "v_cvt_pk_bf16_f32 v40, v142, v143\n"
        "v_mfma_f32_16x16x32_bf16 v[214:217], v[174:177], v[92:95], 0\n"
        "s_nop 0\n"
        "v_cvt_pk_bf16_f32 v41, v144, v145\n"
        "v_mov_b64 v[122:123], v[104:105]\n"
        "v_mfma_f32_16x16x32_bf16 v[214:217], v[178:181], v[80:83], v[214:217]\n"
        "s_nop 0\n"
        "v_mul_f32 v130, v130, v249\n"
        "v_mul_f32 v131, v131, v247\n"
        "v_mfma_f32_16x16x32_bf16 v[214:217], v[194:197], v[88:91], v[214:217]\n"
        "v_cvt_pk_bf16_f32 v174, v126, v127\n"
        "v_cvt_pk_bf16_f32 v175, v54, v55\n"
        "v_mfma_f32_16x16x32_bf16 v[124:127], v[194:197], v[68:71], 0\n"
        "s_nop 0\n"
        "v_cvt_pk_bf16_f32 v176, v206, v207\n"
        "v_mul_f32 v132, v132, v248\n"
        "v_mfma_f32_16x16x32_bf16 v[136:139], v[194:197], v[100:103], 0\n"
        "s_nop 0\n"
        "v_mul_f32 v214, v214, v249\n"
        "v_cvt_pk_f16_f32 v130, v130, v214\n"
        "v_mfma_f32_16x16x32_bf16 v[162:165], v[158:161], a[80:83], 0\n"
        "v_perm_b32 v30, v130, v124, s54\n"
        "v_mul_f32 v215, v215, v247\n"
        "v_mfma_f32_16x16x32_bf16 v[162:165], v[152:155], a[76:79], v[162:165]\n"
        "v_cvt_pk_f16_f32 v131, v131, v215\n"
        "v_perm_b32 v34, v131, v125, s54\n"
        "v_mfma_f32_16x16x32_bf16 v[162:165], v[146:149], a[72:75], v[162:165]\n"
        "v_perm_b32 v31, v130, v136, s55\n"
        "v_perm_b32 v35, v131, v137, s55\n"
        "v_mfma_f32_16x16x32_bf16 v[134:137], v[146:149], a[84:87], 0\n"
        "ds_write_b64 v172, v[30:31] offset:16\n"
        "v_mfma_f32_16x16x32_bf16 v[134:137], v[152:155], a[88:91], v[134:137]\n"
        "ds_write_b64 v172, v[34:35] offset:6352\n"
        "v_mfma_f32_16x16x32_bf16 v[134:137], v[158:161], a[92:95], v[134:137]\n"
        "v_mul_f32 v216, v216, v248\n"
        "v_cvt_pk_f16_f32 v132, v132, v216\n"
        "v_mfma_f32_16x16x32_bf16 v[140:143], v[36:39], v[116:119], 0\n"
        "v_perm_b32 v30, v132, v126, s54\n"
        "v_perm_b32 v31, v132, v138, s55\n"
        "v_mfma_f32_16x16x32_bf16 v[178:181], v[38:41], v[120:123], 0\n"
        "ds_write_b64 v172, v[30:31] offset:12688\n"
        "v_mfma_f32_16x16x32_bf16 v[120:123], a[176:179], v[186:189], 0\n"
        "v_cvt_pk_bf16_f32 v124, v128, v129\n"
        "v_cvt_pk_bf16_f32 v125, v106, v107\n"
        "v_mfma_f32_16x16x32_bf16 v[120:123], a[172:175], v[190:193], v[120:123]\n"
        "v_cvt_pk_bf16_f32 v126, v208, v209\n"
        "v_mul_f32 v133, v133, v246\n"
        "v_mfma_f32_16x16x32_bf16 v[120:123], a[168:171], v[182:185], v[120:123]\n"
        "v_mul_f32 v217, v217, v246\n"
        "v_cvt_pk_f16_f32 v133, v133, v217\n"
        "v_mfma_f32_16x16x32_bf16 v[194:197], a[180:183], v[182:185], 0\n"
        "v_perm_b32 v30, v133, v127, s54\n"
        "v_perm_b32 v31, v133, v139, s55\n"
        "v_mfma_f32_16x16x32_bf16 v[194:197], a[184:187], v[190:193], v[194:197]\n"
        "ds_write_b64 v172, v[30:31] offset:19024\n"
        "v_mfma_f32_16x16x32_bf16 v[194:197], a[188:191], v[186:189], v[194:197]\n"
        "v_and_b32_dpp v144, v213, v25 row_ror:8 row_mask:0xf bank_mask:0xf\n"
        "v_and_b32_dpp v145, v250, v25 row_ror:8 row_mask:0xf bank_mask:0xf\n"
        "v_mfma_f32_16x16x32_bf16 v[34:37], v[158:161], a[104:107], 0\n"
        "v_cvt_pk_bf16_f32 v116, v52, v53\n"
        "v_cvt_pk_bf16_f32 v117, v108, v109\n"
        "v_mfma_f32_16x16x32_bf16 v[34:37], v[152:155], a[100:103], v[34:37]\n"
        "v_mov_b64 v[150:151], v[144:145]\n"
        "v_cvt_pk_bf16_f32 v42, v162, v163\n"
        "v_mfma_f32_16x16x32_bf16 v[34:37], v[146:149], a[96:99], v[34:37]\n"
        "v_cvt_pk_bf16_f32 v43, v164, v165\n"
        "v_cvt_pk_bf16_f32 v46, v134, v135\n"
        "v_mfma_f32_16x16x32_bf16 v[104:107], v[146:149], a[108:111], 0\n"
        "v_cvt_pk_bf16_f32 v47, v136, v137\n"
        "v_cvt_pk_bf16_f32 v118, v140, v141\n"
        "v_mfma_f32_16x16x32_bf16 v[104:107], v[152:155], a[112:115], v[104:107]\n"
        "v_cvt_pk_bf16_f32 v177, v142, v143\n"
        "v_cvt_pk_bf16_f32 v127, v178, v179\n"
        "v_mfma_f32_16x16x32_bf16 v[104:107], v[158:161], a[116:119], v[104:107]\n"
        "ds_write_b128 v12, v[174:177] offset:3072\n"
        "v_mfma_f32_16x16x32_bf16 v[132:135], v[42:45], v[144:147], 0\n"
        "ds_write_b128 v12, v[124:127] offset:4096\n"
        "v_mfma_f32_16x16x32_bf16 v[128:131], v[44:47], v[148:151], 0\n"
        "v_cvt_pk_bf16_f32 v119, v180, v181\n"
        "v_cvt_pk_bf16_f32 v52, v120, v121\n"
        "v_mfma_f32_16x16x32_bf16 v[136:139], v[186:189], a[224:227], 0\n"
        "ds_write_b128 v12, v[116:119] offset:5120\n"
        "v_mfma_f32_16x16x32_bf16 v[136:139], v[190:193], a[220:223], v[136:139]\n"
        "v_cvt_pk_bf16_f32 v53, v122, v123\n"
        "v_cvt_pk_bf16_f32 v54, v194, v195\n"
        "v_mfma_f32_16x16x32_bf16 v[136:139], v[182:185], a[216:219], v[136:139]\n"
        "v_cvt_pk_bf16_f32 v55, v196, v197\n"
        "v_cvt_pk_bf16_f32 v30, v34, v35\n"
        "v_mfma_f32_16x16x32_bf16 v[120:123], v[182:185], a[228:231], 0\n"
        "v_cvt_pk_bf16_f32 v31, v36, v37\n"
        "v_cvt_pk_bf16_f32 v34, v104, v105\n"
        "v_mfma_f32_16x16x32_bf16 v[120:123], v[190:193], a[232:235], v[120:123]\n"
        "v_cvt_pk_bf16_f32 v35, v106, v107\n"
        "v_mov_b64 v[156:157], v[144:145]\n"
        "v_mfma_f32_16x16x32_bf16 v[120:123], v[186:189], a[236:239], v[120:123]\n"
        "v_cvt_f32_f16_sdwa v250, v204 dst_sel:DWORD dst_unused:UNUSED_PAD src0_sel:WORD_0\n"
        "v_mul_f32 v136, v136, v250\n"
        "v_mfma_f32_16x16x32_bf16 v[104:107], v[182:185], v[68:71], 0\n"
        "s_nop 0\n"
        "v_cvt_f32_f16_sdwa v248, v204 dst_sel:DWORD dst_unused:UNUSED_PAD src0_sel:WORD_1\n"
        "v_mul_f32 v137, v137, v248\n"
        "v_mfma_f32_16x16x32_bf16 v[40:43], v[182:185], v[100:103], 0\n"
        "s_nop 0\n"
        "v_mul_f32 v120, v120, v250\n"
        "v_cvt_pk_f16_f32 v136, v136, v120\n"
        "v_mfma_f32_16x16x32_bf16 v[108:111], v[174:177], v[174:177], 0\n"
        "v_perm_b32 v36, v136, v104, s54\n"
        "v_mul_f32 v121, v121, v248\n"
        "v_mfma_f32_16x16x32_bf16 v[108:111], v[124:127], v[124:127], v[108:111]\n"
        "s_nop 0\n"
        "v_perm_b32 v37, v136, v40, s55\n"
        "v_cvt_pk_f16_f32 v137, v137, v121\n"
        "v_mfma_f32_16x16x32_bf16 v[108:111], v[116:119], v[116:119], v[108:111]\n"
        "ds_write_b64 v172, v[36:37] offset:25344\n"
        "v_mfma_f32_16x16x32_bf16 v[124:127], v[158:161], a[128:131], 0\n"
        "ds_write_b128 v169, v[112:115]\n"
        "v_mfma_f32_16x16x32_bf16 v[124:127], v[152:155], a[124:127], v[124:127]\n"
        "v_perm_b32 v36, v137, v105, s54\n"
        "v_perm_b32 v37, v137, v41, s55\n"
        "v_mfma_f32_16x16x32_bf16 v[124:127], v[146:149], a[120:123], v[124:127]\n"
        "ds_write_b64 v172, v[36:37] offset:31680\n"
        "v_mfma_f32_16x16x32_bf16 v[140:143], v[146:149], a[132:135], 0\n"
        "ds_write_b128 v169, v[52:55] offset:64\n"
        "v_mfma_f32_16x16x32_bf16 v[140:143], v[152:155], a[136:139], v[140:143]\n"
        "v_cvt_f32_f16_sdwa v249, v205 dst_sel:DWORD dst_unused:UNUSED_PAD src0_sel:WORD_0\n"
        "v_mul_f32 v138, v138, v249\n"
        "v_mfma_f32_16x16x32_bf16 v[140:143], v[158:161], a[140:143], v[140:143]\n"
        "v_mul_f32 v122, v122, v249\n"
        "v_cvt_pk_f16_f32 v138, v138, v122\n"
        "v_mfma_f32_16x16x32_bf16 v[118:121], v[30:33], v[150:153], 0\n"
        "v_perm_b32 v30, v138, v106, s54\n"
        "v_perm_b32 v31, v138, v42, s55\n"
        "v_mfma_f32_16x16x32_bf16 v[136:139], v[32:35], v[154:157], 0\n"
        "ds_write_b64 v173, v[30:31]\n"
        "v_mfma_f32_16x16x32_bf16 v[104:107], a[200:203], v[186:189], 0\n"
        "v_cvt_pk_bf16_f32 v36, v124, v125\n"
        "v_cvt_pk_bf16_f32 v37, v126, v127\n"
        "v_mfma_f32_16x16x32_bf16 v[104:107], a[196:199], v[190:193], v[104:107]\n"
        "v_cvt_pk_bf16_f32 v40, v140, v141\n"
        "v_cvt_pk_bf16_f32 v41, v142, v143\n"
        "v_mfma_f32_16x16x32_bf16 v[104:107], a[192:195], v[182:185], v[104:107]\n"
        "v_mov_b64 v[162:163], v[144:145]\n"
        "v_cvt_pk_bf16_f32 v122, v132, v133\n"
        "v_mfma_f32_16x16x32_bf16 v[144:147], a[204:207], v[182:185], 0\n"
        "s_nop 0\n"
        "v_cvt_pk_bf16_f32 v123, v130, v131\n"
        "v_cvt_pk_bf16_f32 v124, v136, v137\n"
        "v_mfma_f32_16x16x32_bf16 v[144:147], a[208:211], v[190:193], v[144:147]\n"
        "s_nop 0\n"
        "v_cvt_pk_bf16_f32 v178, v104, v105\n"
        "v_cvt_pk_bf16_f32 v179, v106, v107\n"
        "v_mfma_f32_16x16x32_bf16 v[144:147], a[212:215], v[186:189], v[144:147]\n"
        "v_cvt_pk_bf16_f32 v104, v134, v135\n"
        "v_cvt_pk_bf16_f32 v105, v118, v119\n"
        "v_mfma_f32_16x16x32_bf16 v[150:153], v[186:189], a[248:251], 0\n"
        "s_nop 0\n"
        "v_cvt_pk_bf16_f32 v106, v138, v139\n"
        "v_cvt_pk_bf16_f32 v142, v128, v129\n"
        "v_mfma_f32_16x16x32_bf16 v[150:153], v[190:193], a[244:247], v[150:153]\n"
        "s_nop 0\n"
        "v_cvt_pk_bf16_f32 v180, v144, v145\n"
        "v_cvt_pk_bf16_f32 v181, v146, v147\n"
        "v_mfma_f32_16x16x32_bf16 v[150:153], v[182:185], a[240:243], v[150:153]\n"
        "s_nop 0\n"
        "ds_write_b128 v169, v[178:181] offset:128\n"
        "v_mfma_f32_16x16x32_bf16 v[116:119], v[182:185], a[252:255], 0\n"
        "s_nop 0\n"
        "v_cvt_pk_bf16_f32 v143, v120, v121\n"
        "v_mul_f32_dpp v247, v255, v251 quad_perm:[0,0,0,0] row_mask:0xf bank_mask:0xf\n"
        "v_mfma_f32_16x16x32_bf16 v[116:119], v[190:193], v[76:79], v[116:119]\n"
        "s_nop 0\n"
        "v_mul_f32 v150, v150, v250\n"
        "v_mul_f32 v151, v151, v248\n"
        "v_mfma_f32_16x16x32_bf16 v[116:119], v[186:189], v[96:99], v[116:119]\n"
        "v_mul_f32 v152, v152, v249\n"
        "v_fmac_f32_dpp v247, v255, v252 quad_perm:[1,1,1,1] row_mask:0xf bank_mask:0xf\n"
        "v_mfma_f32_16x16x32_bf16 v[134:137], v[190:193], v[68:71], 0\n"
        "s_nop 0\n"
        "v_cndmask_b32_e64 v252, v108, v109, s[56:57]\n"
        "v_cndmask_b32_e64 v252, v252, v110, s[58:59]\n"
        "v_mfma_f32_16x16x32_bf16 v[174:177], v[190:193], v[100:103], 0\n"
        "s_nop 0\n"
        "v_mul_f32 v116, v116, v250\n"
        "v_cvt_pk_f16_f32 v150, v150, v116\n"
        "v_mfma_f32_16x16x32_bf16 v[126:129], v[36:39], v[156:159], 0\n"
        "v_perm_b32 v30, v150, v134, s54\n"
        "v_mul_f32 v117, v117, v248\n"
        "v_mfma_f32_16x16x32_bf16 v[34:37], v[38:41], v[160:163], 0\n"
        "s_nop 0\n"
        "v_perm_b32 v31, v150, v174, s55\n"
        "v_cvt_pk_f16_f32 v151, v151, v117\n"
        "v_mfma_f32_16x16x32_bf16 v[154:157], v[186:189], v[72:75], 0\n"
        "ds_write_b64 v172, v[30:31] offset:25352\n"
        "v_mfma_f32_16x16x32_bf16 v[154:157], v[190:193], v[60:63], v[154:157]\n"
        "v_cvt_pk_bf16_f32 v125, v128, v129\n"
        "v_cvt_pk_bf16_f32 v144, v126, v127\n"
        "v_mfma_f32_16x16x32_bf16 v[154:157], v[182:185], v[64:67], v[154:157]\n"
        "ds_write_b128 v12, v[122:125] offset:6144\n"
        "v_mfma_f32_16x16x32_bf16 v[40:43], v[182:185], v[92:95], 0\n"
        "s_waitcnt vmcnt(0)\n"
        "v_cvt_pk_bf16_f32 v107, v34, v35\n"
        "v_cvt_pk_bf16_f32 v145, v36, v37\n"
        "v_mfma_f32_16x16x32_bf16 v[40:43], v[190:193], v[80:83], v[40:43]\n"
        "ds_write_b128 v12, v[104:107] offset:7168\n"
        "v_mfma_f32_16x16x32_bf16 v[40:43], v[186:189], v[88:91], v[40:43]\n"
        "ds_write_b128 v12, v[142:145] offset:8192\n"
        "v_mfma_f32_16x16x32_bf16 v[34:37], v[186:189], v[68:71], 0\n"
        "ds_read_b128 v[126:129], v9\n"
        "v_perm_b32 v30, v151, v135, s54\n"
        "v_mfma_f32_16x16x32_bf16 v[146:149], v[186:189], v[100:103], 0\n"
        "ds_read_b128 v[132:135], v10\n"
        "v_perm_b32 v31, v151, v175, s55\n"
        "v_mfma_f32_16x16x32_bf16 v[158:161], v[122:125], v[122:125], 0\n"
        "ds_write_b64 v172, v[30:31] offset:31688\n"
        "v_mfma_f32_16x16x32_bf16 v[158:161], v[104:107], v[104:107], v[158:161]\n"
        "ds_read_b128 v[138:141], v11\n"
        "v_mul_f32 v118, v118, v249\n"
        "v_mfma_f32_16x16x32_bf16 v[158:161], v[142:145], v[142:145], v[158:161]\n"
        "ds_read_b128 v[106:109], v9 offset:3072\n"
        "v_cvt_pk_f16_f32 v152, v152, v118\n"
        "v_mfma_f32_16x16x32_bf16 v[122:125], v[182:185], v[112:115], 0\n"
        "ds_read_b128 v[112:115], v10 offset:3072\n"
        "v_perm_b32 v30, v152, v136, s54\n"
        "v_mfma_f32_16x16x32_bf16 v[122:125], v[190:193], v[52:55], v[122:125]\n"
        "ds_read_b128 v[118:121], v11 offset:3072\n"
        "v_perm_b32 v31, v152, v176, s55\n"
        "v_mfma_f32_16x16x32_bf16 v[122:125], v[186:189], v[178:181], v[122:125]\n"
        "ds_write_b64 v173, v[30:31] offset:8\n"
        "v_cndmask_b32_e64 v252, v252, v111, s[60:61]\n"
        "v_fmac_f32_dpp v247, v255, v253 quad_perm:[2,2,2,2] row_mask:0xf bank_mask:0xf\n"
        "v_cndmask_b32_e64 v253, v198, v199, s[56:57]\n"
        "v_cndmask_b32_e64 v253, v253, v200, s[58:59]\n"
        "v_cndmask_b32_e64 v253, v253, v201, s[60:61]\n"
        "v_fmac_f32_dpp v247, v255, v166 quad_perm:[3,3,3,3] row_mask:0xf bank_mask:0xf\n"
        "v_mul_f32 v154, v154, v250\n"
        "v_mul_f32 v40, v40, v250\n"
        "v_add_f32 v247, v247, v254\n"
        "v_cvt_pk_f16_f32 v154, v154, v40\n"
        "v_mul_f32 v250, s80, v247\n"
        "v_cvt_f32_f16_sdwa v251, v212 dst_sel:DWORD dst_unused:UNUSED_PAD src0_sel:WORD_1\n"
        "v_perm_b32 v46, v154, v34, s54\n"
        "v_mul_f32 v155, v155, v248\n"
        "v_mul_f32 v41, v41, v248\n"
        "v_mul_f32 v156, v156, v249\n"
        "v_mul_f32 v42, v42, v249\n"
        "v_cvt_pk_f16_f32 v155, v155, v41\n"
        "v_cvt_pk_f16_f32 v156, v156, v42\n"
        "v_mov_b32 v255, 0x3c003c00\n"
        "v_perm_b32 v34, v155, v35, s54\n"
        "v_lshrrev_b32 v31, 16, v212\n"
        "v_mov_b32 v254, s29\n"
        "v_perm_b32 v40, v156, v36, s54\n"
        "v_cndmask_b32_e64 v52, 0, v255, s[76:77]\n"
        "v_cndmask_b32_e64 v53, 0, v255, s[56:57]\n"
        "v_cndmask_b32_e64 v54, 0, v255, s[58:59]\n"
        "v_cndmask_b32_e64 v55, 0, v255, s[60:61]\n"
        "v_readfirstlane_b32 s20, v210\n"
        "v_readfirstlane_b32 s21, v211\n"
        "v_perm_b32 v47, v154, v146, s55\n"
        "v_perm_b32 v35, v155, v147, s55\n"
        "v_perm_b32 v41, v156, v148, s55\n"
        "ds_write_b64 v172, v[46:47] offset:25360\n"
        "ds_write_b64 v172, v[34:35] offset:31696\n"
        "ds_write_b64 v173, v[40:41] offset:16\n"
        "ds_read_b128 v[146:149], v9 offset:6144\n"
        "v_cndmask_b32_e64 v255, v158, v159, s[56:57]\n"
        "v_cndmask_b32_e64 v255, v255, v160, s[58:59]\n"
        "v_cndmask_b32_e64 v255, v255, v161, s[60:61]\n"
        "ds_read_b128 v[152:155], v10 offset:6144\n"
        "ds_read_b128 v[158:161], v11 offset:6144\n"
        "v_cndmask_b32_dpp v252, v255, v252, vcc row_ror:4 row_mask:0xf bank_mask:0xf\n"
        "v_cndmask_b32_e64 v30, v122, v123, s[56:57]\n"
        "v_cndmask_b32_e64 v30, v30, v124, s[58:59]\n"
        "v_cndmask_b32_dpp v253, v252, v253, vcc row_ror:4 row_mask:0xf bank_mask:0xf\n"
        "v_fma_f32 v250, -v250, v247, v253\n"
        "v_max_f32 v250, 0, v250\n"
        "v_sub_f32 v253, 0x47c00000, v253\n"
        "v_fmaak_f32 v250, v28, v250, 0x3727c5ac\n"
        "v_bfi_b32 v250, s84, v253, v250\n"
        "v_rsq_f32 v255, v250\n"
        "v_cndmask_b32_e64 v30, v30, v125, s[60:61]\n"
        "v_cvt_f16_f32 v255, v255\n"
        "v_mul_f32 v30, v30, v251\n"
        "ds_write_b16 v18, v255\n"
        "ds_write_b16 v19, v255\n"
        "v_mul_f32 v30, v30, v251\n"
        "v_exp_f32 v30, v30\n"
        "ds_write_b16 v22, v254 offset:3072\n"
        "ds_write_b64 v171, v[30:31]\n"
        "s_waitcnt lgkmcnt(0)\n"
        "s_nop 7\n"
        ".p2align 6\n"
        "TOP_%=:\n"
        "v_mfma_f32_16x16x32_bf16 v[198:201], v[126:129], v[126:129], 0\n"
        "s_mov_b32 s26, s89\n"
        "ds_read_b128 v[224:227], v20 offset:512\n"
        "s_mov_b32 s30, s29\n"
        "v_mfma_f32_16x16x32_bf16 v[198:201], v[132:135], v[132:135], v[198:201]\n"
        "s_add_u32 s88, s88, 32\n"
        "v_add_u32 v23, s22, v23\n"
        "v_add_u32 v24, s22, v24\n"
        "v_mfma_f32_16x16x32_bf16 v[198:201], v[138:141], v[138:141], v[198:201]\n"
        "s_mov_b32 s22, s23\n"
        "ds_read_b128 v[210:213], v20 offset:528\n"
        "s_add_u32 s89, s86, s51\n"
        "v_mfma_f32_16x16x32_bf16 v[122:125], v[56:59], v[138:141], 0\n"
        "s_add_u32 s29, s29, 1\n"
        "s_add_u32 s27, s26, 392448\n"
        "v_cvt_pk_bf16_f32 v230, v206, v194\n"
        "v_mfma_f32_16x16x32_bf16 v[122:125], v[48:51], v[132:135], v[122:125]\n"
        "s_cmp_ge_u32 s88, 73\n"
        "v_cvt_pk_bf16_f32 v254, v207, v195\n"
        "v_lshlrev_b32 v218, 16, v180\n"
        "v_mfma_f32_16x16x32_bf16 v[122:125], v[84:87], v[126:129], v[122:125]\n"
        "buffer_store_dwordx3 v[228:230], v15, s[40:43], s27 offen offset:768 sc1 nt\n"
        "v_lshlrev_b32 v219, 16, v184\n"
        "v_lshlrev_b32 v220, 16, v164\n"
        "v_cvt_pk_bf16_f32 v234, v208, v196\n"
        "v_lshlrev_b32 v221, 16, v188\n"
        "v_mfma_f32_16x16x32_bf16 v[214:217], a[0:3], v[126:129], 0\n"
        "s_movk_i32 s23, 1584\n"
        "v_lshlrev_b32 v202, 16, v176\n"
        "v_lshlrev_b32 v203, 16, v172\n"
        "v_mfma_f32_16x16x32_bf16 v[214:217], a[4:7], v[132:135], v[214:217]\n"
        "s_mov_b32 s86, s87\n"
        "v_cvt_pk_bf16_f32 v42, v209, v197\n"
        "v_cndmask_b32_e64 v255, v198, v199, s[56:57]\n"
        "v_mfma_f32_16x16x32_bf16 v[214:217], a[8:11], v[138:141], v[214:217]\n"
        "s_cselect_b32 s92, 588672, 0\n"
        "v_cndmask_b32_e64 v255, v255, v200, s[58:59]\n"
        "v_cndmask_b32_e64 v255, v255, v201, s[60:61]\n"
        "v_mfma_f32_16x16x32_bf16 v[228:231], a[20:23], v[138:141], 0\n"
        "s_cselect_b32 s94, 73, 0\n"
        "s_add_u32 s28, s26, 490560\n"
        "s_add_u32 s87, s87, 43008\n"
        "v_mfma_f32_16x16x32_bf16 v[228:231], a[16:19], v[132:135], v[228:231]\n"
        "s_waitcnt lgkmcnt(0)\n"
        "v_pk_mul_f16 v194, v224, v166\n"
        "v_pk_mul_f16 v195, v224, v142\n"
        "v_mfma_f32_16x16x32_bf16 v[228:231], a[12:15], v[126:129], v[228:231]\n"
        "buffer_store_dwordx3 v[252:254], v14, s[40:43], s28 offen offset:192 sc1 nt\n"
        "v_pk_mul_f16 v196, v224, v190\n"
        "v_pk_mul_f16 v197, v224, v34\n"
        "v_pk_mul_f16 v198, v224, v244\n"
        "v_pk_mul_f16 v199, v224, v240\n"
        "v_mfma_f32_16x16x32_bf16 v[204:207], a[24:27], v[126:129], 0\n"
        "s_add_u32 s27, s26, 588672\n"
        "v_pk_fma_f16 v194, v225, v167, v194\n"
        "v_pk_fma_f16 v195, v225, v143, v195\n"
        "v_mfma_f32_16x16x32_bf16 v[204:207], a[28:31], v[132:135], v[204:207]\n"
        "s_sub_u32 s88, s88, s94\n"
        "v_pk_fma_f16 v196, v225, v191, v196\n"
        "v_pk_fma_f16 v197, v225, v35, v197\n"
        "v_mfma_f32_16x16x32_bf16 v[204:207], a[32:35], v[138:141], v[204:207]\n"
        "buffer_store_dwordx3 v[232:234], v16, s[40:43], s28 offen offset:960 sc1 nt\n"
        "s_add_u32 s87, s87, s92\n"
        "v_pk_fma_f16 v198, v225, v245, v198\n"
        "v_pk_fma_f16 v199, v225, v241, v199\n"
        "v_pk_fma_f16 v194, v226, v168, v194\n"
        "v_mfma_f32_16x16x32_bf16 v[222:225], a[44:47], v[138:141], 0\n"
        "s_waitcnt lgkmcnt(0)\n"
        "v_pk_fma_f16 v195, v226, v144, v195\n"
        "v_pk_fma_f16 v196, v226, v192, v196\n"
        "v_mfma_f32_16x16x32_bf16 v[222:225], a[40:43], v[132:135], v[222:225]\n"
        "s_waitcnt lgkmcnt(0)\n"
        "v_pk_fma_f16 v197, v226, v36, v197\n"
        "v_pk_fma_f16 v198, v226, v246, v198\n"
        "v_mfma_f32_16x16x32_bf16 v[222:225], a[36:39], v[126:129], v[222:225]\n"
        "buffer_store_dwordx3 v[40:42], v14, s[40:43], s27 offen offset:384 sc1 nt\n"
        "v_pk_fma_f16 v199, v226, v242, v199\n"
        "v_pk_fma_f16 v194, v227, v169, v194\n"
        "v_pk_fma_f16 v195, v227, v145, v195\n"
        "v_pk_fma_f16 v196, v227, v193, v196\n"
        "v_mfma_f32_16x16x32_bf16 v[166:169], a[48:51], v[126:129], 0\n"
        "v_pk_fma_f16 v197, v227, v37, v197\n"
        "v_pk_fma_f16 v198, v227, v247, v198\n"
        "v_mfma_f32_16x16x32_bf16 v[166:169], a[52:55], v[132:135], v[166:169]\n"
        "v_pk_fma_f16 v199, v227, v243, v199\n"
        "v_pk_fma_f16 v194, v210, v178, v194\n"
        "v_mfma_f32_16x16x32_bf16 v[166:169], a[56:59], v[138:141], v[166:169]\n"
        "v_pk_fma_f16 v195, v210, v182, v195\n"
        "v_cvt_pk_bf16_f32 v34, v214, v215\n"
        "v_mfma_f32_16x16x32_bf16 v[40:43], a[68:71], v[138:141], 0\n"
        "v_cvt_pk_bf16_f32 v35, v216, v217\n"
        "v_cvt_pk_bf16_f32 v36, v228, v229\n"
        "v_mfma_f32_16x16x32_bf16 v[40:43], a[64:67], v[132:135], v[40:43]\n"
        "v_cvt_pk_bf16_f32 v37, v230, v231\n"
        "v_cvt_pk_bf16_f32 v190, v204, v205\n"
        "v_mfma_f32_16x16x32_bf16 v[40:43], a[60:63], v[126:129], v[40:43]\n"
        "v_cvt_pk_bf16_f32 v191, v206, v207\n"
        "v_cvt_pk_bf16_f32 v192, v222, v223\n"
        "v_mfma_f32_16x16x32_bf16 v[142:145], v[106:109], v[106:109], 0\n"
        "v_cvt_pk_bf16_f32 v193, v224, v225\n"
        "v_cvt_pk_bf16_f32 v204, v166, v167\n"
        "v_mfma_f32_16x16x32_bf16 v[142:145], v[112:115], v[112:115], v[142:145]\n"
        "s_nop 0\n"
        "v_cvt_pk_bf16_f32 v205, v168, v169\n"
        "v_cvt_pk_bf16_f32 v206, v40, v41\n"
        "v_mfma_f32_16x16x32_bf16 v[142:145], v[118:121], v[118:121], v[142:145]\n"
        "v_cvt_pk_bf16_f32 v207, v42, v43\n"
        "v_pk_fma_f16 v196, v210, v162, v196\n"
        "v_mfma_f32_16x16x32_bf16 v[166:169], v[56:59], v[118:121], 0\n"
        "v_pk_fma_f16 v197, v210, v186, v197\n"
        "v_pk_fma_f16 v198, v210, v174, v198\n"
        "v_mfma_f32_16x16x32_bf16 v[166:169], v[48:51], v[112:115], v[166:169]\n"
        "v_pk_fma_f16 v199, v210, v170, v199\n"
        "v_pk_fma_f16 v194, v211, v179, v194\n"
        "v_mfma_f32_16x16x32_bf16 v[166:169], v[84:87], v[106:109], v[166:169]\n"
        "v_pk_fma_f16 v195, v211, v183, v195\n"
        "v_pk_fma_f16 v196, v211, v163, v196\n"
        "v_mfma_f32_16x16x32_bf16 v[40:43], v[126:129], v[34:37], 0\n"
        "v_pk_fma_f16 v197, v211, v187, v197\n"
        "v_pk_fma_f16 v198, v211, v175, v198\n"
        "v_mfma_f32_16x16x32_bf16 v[40:43], v[132:135], v[190:193], v[40:43]\n"
        "v_pk_fma_f16 v199, v211, v171, v199\n"
        "v_pk_fma_f16 v194, v212, v180, v194\n"
        "v_mfma_f32_16x16x32_bf16 v[40:43], v[138:141], v[204:207], v[40:43]\n"
        "v_pk_fma_f16 v195, v212, v184, v195\n"
        "v_pk_fma_f16 v196, v212, v164, v196\n"
        "v_mfma_f32_16x16x32_bf16 v[34:37], a[8:11], v[118:121], 0\n"
        "v_pk_fma_f16 v197, v212, v188, v197\n"
        "v_pk_fma_f16 v198, v212, v176, v198\n"
        "v_mfma_f32_16x16x32_bf16 v[34:37], a[4:7], v[112:115], v[34:37]\n"
        "v_pk_fma_f16 v199, v212, v172, v199\n"
        "v_cndmask_b32_e64 v254, v142, v143, s[56:57]\n"
        "v_mfma_f32_16x16x32_bf16 v[34:37], a[0:3], v[106:109], v[34:37]\n"
        "s_nop 0\n"
        "v_cndmask_b32_e64 v254, v254, v144, s[58:59]\n"
        "v_cndmask_b32_e64 v254, v254, v145, s[60:61]\n"
        "v_mfma_f32_16x16x32_bf16 v[162:165], a[12:15], v[106:109], 0\n"
        "s_nop 1\n"
        "v_mfma_f32_16x16x32_bf16 v[162:165], a[16:19], v[112:115], v[162:165]\n"
        "s_add_u32 s46, s26, 590016\n"
        "v_cvt_pk_bf16_f32 v142, v34, v35\n"
        "v_cvt_pk_bf16_f32 v143, v36, v37\n"
        "v_mfma_f32_16x16x32_bf16 v[162:165], a[20:23], v[118:121], v[162:165]\n"
        "s_nop 1\n"
        "v_mfma_f32_16x16x32_f16 v[218:221], v[52:55], v[194:197], v[218:221]\n"
        "s_min_u32 s46, s46, s42\n"
        "s_nop 1\n"
        "v_mfma_f32_16x16x32_f16 v[200:203], v[52:55], v[196:199], v[200:203]\n"
        "s_nop 0\n"
        "v_cvt_pk_bf16_f32 v144, v162, v163\n"
        "v_cvt_pk_bf16_f32 v145, v164, v165\n"
        "v_mfma_f32_16x16x32_bf16 v[162:165], a[32:35], v[118:121], 0\n"
        "v_cvt_pk_bf16_f32 v170, v218, v219\n"
        "v_cvt_pk_bf16_f32 v171, v220, v221\n"
        "v_mfma_f32_16x16x32_bf16 v[162:165], a[28:31], v[112:115], v[162:165]\n"
        "s_nop 0\n"
        "v_cvt_pk_bf16_f32 v172, v202, v203\n"
        "v_mfma_f32_16x16x32_bf16 v[162:165], a[24:27], v[106:109], v[162:165]\n"
        "s_nop 1\n"
        "v_mfma_f32_16x16x32_bf16 v[34:37], a[36:39], v[106:109], 0\n"
        "s_nop 0\n"
        "ds_read_u16 v253, v19 offset:2048\n"
        "s_nop 0\n"
        "v_mfma_f32_16x16x32_bf16 v[34:37], a[40:43], v[112:115], v[34:37]\n"
        "s_nop 0\n"
        "v_cvt_pk_bf16_f32 v168, v162, v163\n"
        "v_cvt_pk_bf16_f32 v169, v164, v165\n"
        "v_mfma_f32_16x16x32_bf16 v[34:37], a[44:47], v[118:121], v[34:37]\n"
        "buffer_store_dwordx3 v[170:172], v17, s[44:47], s27 offen offset:1152 sc1 nt\n"
        "s_nop 1\n"
        "v_mfma_f32_16x16x32_bf16 v[172:175], a[56:59], v[118:121], 0\n"
        "s_nop 1\n"
        "v_mfma_f32_16x16x32_bf16 v[172:175], a[52:55], v[112:115], v[172:175]\n"
        "s_nop 0\n"
        "v_cvt_pk_bf16_f32 v170, v34, v35\n"
        "v_cvt_pk_bf16_f32 v171, v36, v37\n"
        "v_mfma_f32_16x16x32_bf16 v[172:175], a[48:51], v[106:109], v[172:175]\n"
        "s_nop 1\n"
        "v_mfma_f32_16x16x32_bf16 v[162:165], a[60:63], v[106:109], 0\n"
        "s_nop 2\n"
        "v_mfma_f32_16x16x32_bf16 v[162:165], a[64:67], v[112:115], v[162:165]\n"
        "s_nop 0\n"
        "v_cvt_pk_bf16_f32 v34, v172, v173\n"
        "v_cvt_pk_bf16_f32 v35, v174, v175\n"
        "v_mfma_f32_16x16x32_bf16 v[162:165], a[68:71], v[118:121], v[162:165]\n"
        "s_waitcnt lgkmcnt(0)\n"
        "v_cmp_ne_u32_e64 s[24:25], s30, v253\n"
        "v_mfma_f32_16x16x32_bf16 v[176:179], v[146:149], v[146:149], 0\n"
        "s_nop 2\n"
        "v_mfma_f32_16x16x32_bf16 v[176:179], v[152:155], v[152:155], v[176:179]\n"
        "s_cmp_lg_u64 s[24:25], 0\n"
        "v_cvt_pk_bf16_f32 v36, v162, v163\n"
        "v_cvt_pk_bf16_f32 v37, v164, v165\n"
        "v_mfma_f32_16x16x32_bf16 v[176:179], v[158:161], v[158:161], v[176:179]\n"
        "s_cbranch_scc1 SP2_%=\n"
        "BK2_%=:\n"
        "s_cmp_le_i32 s85, 0\n"
        "v_mfma_f32_16x16x32_bf16 v[172:175], v[56:59], v[158:161], 0\n"
        "s_cbranch_scc1 DY1_%=\n"
        "DY1B_%=:\n"
        "s_nop 1\n"
        "v_mfma_f32_16x16x32_bf16 v[172:175], v[48:51], v[152:155], v[172:175]\n"
        "s_cmp_gt_i32 s85, 0\n"
        "v_cndmask_b32_e64 v253, v176, v177, s[56:57]\n"
        "v_cndmask_b32_e64 v253, v253, v178, s[58:59]\n"
        "v_mfma_f32_16x16x32_bf16 v[172:175], v[84:87], v[146:149], v[172:175]\n"
        "s_cselect_b32 s94, s87, s84\n"
        "v_cndmask_b32_e64 v253, v253, v179, s[60:61]\n"
        "v_mfma_f32_16x16x32_bf16 v[162:165], v[106:109], v[142:145], 0\n"
        "s_sub_u32 s85, s85, 1\n"
        "v_cndmask_b32_dpp v254, v253, v254, vcc row_ror:4 row_mask:0xf bank_mask:0xf\n"
        "v_mfma_f32_16x16x32_bf16 v[162:165], v[112:115], v[168:171], v[162:165]\n"
        "s_cmp_le_i32 s85, 0\n"
        "v_cndmask_b32_dpp v255, v254, v255, vcc row_ror:4 row_mask:0xf bank_mask:0xf\n"
        "v_cndmask_b32_dpp v254, v172, v166, vcc row_ror:4 row_mask:0xf bank_mask:0xf\n"
        "v_mfma_f32_16x16x32_bf16 v[162:165], v[118:121], v[34:37], v[162:165]\n"
        "s_cbranch_scc1 DY2_%=\n"
        "DY2B_%=:\n"
        "v_cndmask_b32_dpp v254, v254, v122, vcc row_ror:4 row_mask:0xf bank_mask:0xf\n"
        "v_mul_f32 v253, s80, v254\n"
        "v_mfma_f32_16x16x32_bf16 v[34:37], a[8:11], v[158:161], 0\n"
        "s_add_u32 m0, s52, 0\n"
        "v_fma_f32 v253, -v253, v254, v255\n"
        "v_max_f32 v253, 0, v253\n"
        "v_mfma_f32_16x16x32_bf16 v[34:37], a[4:7], v[152:155], v[34:37]\n"
        "v_fmaak_f32 v253, v28, v253, 0x3727c5ac\n"
        "v_cndmask_b32_dpp v255, v173, v167, vcc row_ror:4 row_mask:0xf bank_mask:0xf\n"
        "v_mfma_f32_16x16x32_bf16 v[34:37], a[0:3], v[146:149], v[34:37]\n"
        "buffer_load_dwordx4 v0, s[36:39], s94 offen lds\n"
        "v_cndmask_b32_dpp v255, v255, v123, vcc row_ror:4 row_mask:0xf bank_mask:0xf\n"
        "v_mfma_f32_16x16x32_bf16 v[172:175], a[12:15], v[146:149], 0\n"
        "ds_read_b128 v[142:145], v23\n"
        "ds_read_b128 v[122:125], v23 offset:64\n"
        "v_mfma_f32_16x16x32_bf16 v[172:175], a[16:19], v[152:155], v[172:175]\n"
        "ds_read_b128 v[176:179], v23 offset:128\n"
        "ds_read_b64 v[30:31], v24 offset:360\n"
        "v_mfma_f32_16x16x32_bf16 v[172:175], a[20:23], v[158:161], v[172:175]\n"
        "ds_read_b128 v[194:197], v13\n"
        "v_mov_b32 v252, s29\n"
        "v_mfma_f32_16x16x32_bf16 v[180:183], a[32:35], v[158:161], 0\n"
        "ds_read_b128 v[206:209], v13 offset:1024\n"
        "ds_read_b128 v[210:213], v13 offset:2048\n"
        "v_mfma_f32_16x16x32_bf16 v[180:183], a[28:31], v[152:155], v[180:183]\n"
        "ds_read_b128 v[222:225], v13 offset:3072\n"
        "v_cvt_pk_bf16_f32 v166, v34, v35\n"
        "v_mfma_f32_16x16x32_bf16 v[180:183], a[24:27], v[146:149], v[180:183]\n"
        "buffer_load_dwordx4 v1, s[36:39], s94 offen offset:1024 lds\n"
        "v_cvt_pk_bf16_f32 v167, v36, v37\n"
        "v_mfma_f32_16x16x32_bf16 v[34:37], a[36:39], v[146:149], 0\n"
        "ds_read_b128 v[226:229], v13 offset:4096\n"
        "ds_read_b128 v[218:221], v13 offset:5120\n"
        "v_mfma_f32_16x16x32_bf16 v[34:37], a[40:43], v[152:155], v[34:37]\n"
        "ds_read_b128 v[198:201], v13 offset:6144\n"
        "ds_read_b128 v[214:217], v13 offset:7168\n"
        "v_mfma_f32_16x16x32_bf16 v[34:37], a[44:47], v[158:161], v[34:37]\n"
        "ds_read_b128 v[202:205], v13 offset:8192\n"
        "ds_read_b128 v[230:233], v20\n"
        "v_mfma_f32_16x16x32_bf16 v[188:191], a[56:59], v[158:161], 0\n"
        "s_waitcnt lgkmcnt(10)\n"
        "ds_read_b64 v[238:239], v20 offset:16\n"
        "ds_read_u16 v247, v22\n"
        "v_mfma_f32_16x16x32_bf16 v[188:191], a[52:55], v[152:155], v[188:191]\n"
        "ds_read_b64 v[170:171], v21\n"
        "v_readlane_b32 s93, v31, 1\n"
        "v_mfma_f32_16x16x32_bf16 v[188:191], a[48:51], v[146:149], v[188:191]\n"
        "s_waitcnt lgkmcnt(10)\n"
        "buffer_load_dwordx4 v2, s[36:39], s94 offen offset:2048 lds\n"
        "v_cvt_pk_bf16_f32 v168, v172, v173\n"
        "v_mfma_f32_16x16x32_bf16 v[184:187], a[60:63], v[146:149], 0\n"
        "ds_write_b16 v22, v252 offset:3584\n"
        "v_mfma_f32_16x16x32_bf16 v[184:187], a[64:67], v[152:155], v[184:187]\n"
        "v_cvt_pk_bf16_f32 v169, v174, v175\n"
        "v_cvt_pk_bf16_f32 v172, v180, v181\n"
        "v_mfma_f32_16x16x32_bf16 v[184:187], a[68:71], v[158:161], v[184:187]\n"
        "s_waitcnt lgkmcnt(8)\n"
        "v_cvt_pk_bf16_f32 v173, v182, v183\n"
        "v_cvt_pk_bf16_f32 v174, v34, v35\n"
        "v_mfma_f32_16x16x32_bf16 v[180:183], v[142:145], v[194:197], 0\n"
        "v_cvt_pk_bf16_f32 v175, v36, v37\n"
        "v_cvt_pk_bf16_f32 v34, v188, v189\n"
        "v_mfma_f32_16x16x32_bf16 v[180:183], v[122:125], v[206:209], v[180:183]\n"
        "v_cvt_pk_bf16_f32 v35, v190, v191\n"
        "v_cvt_pk_bf16_f32 v36, v184, v185\n"
        "v_mfma_f32_16x16x32_bf16 v[180:183], v[176:179], v[210:213], v[180:183]\n"
        "buffer_load_dwordx4 v3, s[36:39], s94 offen offset:3072 lds\n"
        "v_cvt_pk_bf16_f32 v37, v186, v187\n"
        "v_mfma_f32_16x16x32_bf16 v[184:187], v[176:179], v[218:221], 0\n"
        "s_waitcnt lgkmcnt(1)\n"
        "v_cndmask_b32_e64 v30, 0, v30, s[72:73]\n"
        "v_mfma_f32_16x16x32_bf16 v[184:187], v[122:125], v[226:229], v[184:187]\n"
        "v_rsq_f32 v252, v253\n"
        "v_mfma_f32_16x16x32_bf16 v[184:187], v[142:145], v[222:225], v[184:187]\n"
        "s_nop 1\n"
        "v_mfma_f32_16x16x32_bf16 v[188:191], v[142:145], v[198:201], 0\n"
        "s_add_u32 m0, s52, 4096\n"
        "s_nop 1\n"
        "v_mfma_f32_16x16x32_bf16 v[188:191], v[122:125], v[214:217], v[188:191]\n"
        "s_nop 0\n"
        "v_cndmask_b32_e64 v246, v180, v184, s[64:65]\n"
        "v_cndmask_b32_e64 v253, v181, v185, s[64:65]\n"
        "v_mfma_f32_16x16x32_bf16 v[188:191], v[176:179], v[202:205], v[188:191]\n"
        "s_nop 0\n"
        "buffer_load_dwordx4 v4, s[36:39], s94 offen lds\n"
        "v_mfma_f32_16x16x32_bf16 v[122:125], v[146:149], v[166:169], 0\n"
        "s_nop 2\n"
        "v_mfma_f32_16x16x32_bf16 v[122:125], v[152:155], v[172:175], v[122:125]\n"
        "s_nop 0\n"
        "v_cndmask_b32_e64 v246, v246, v188, s[70:71]\n"
        "v_cndmask_b32_e64 v253, v253, v189, s[70:71]\n"
        "v_mfma_f32_16x16x32_bf16 v[122:125], v[158:161], v[34:37], v[122:125]\n"
        "v_cvt_pk_f16_f32 v246, v246, v253\n"
        "v_pk_mul_f16 v246, v246, v247 op_sel_hi:[1,0]\n"
        "v_mfma_f32_16x16x32_bf16 v[166:169], a[152:155], v[210:213], 0\n"
        "v_pk_mul_f16 v246, v246, s93 op_sel_hi:[1,0]\n"
        "v_mfma_f32_16x16x32_bf16 v[166:169], a[148:151], v[206:209], v[166:169]\n"
        "v_exp_f16_sdwa v246, v246 dst_sel:WORD_0 dst_unused:UNUSED_PRESERVE src0_sel:WORD_0\n"
        "v_mfma_f32_16x16x32_bf16 v[166:169], a[144:147], v[194:197], v[166:169]\n"
        "buffer_load_dwordx4 v5, s[36:39], s94 offen offset:1024 lds\n"
        "v_cndmask_b32_dpp v248, v122, v162, vcc row_ror:4 row_mask:0xf bank_mask:0xf\n"
        "v_mfma_f32_16x16x32_bf16 v[172:175], a[156:159], v[194:197], 0\n"
        "v_cndmask_b32_dpp v250, v123, v163, vcc row_ror:4 row_mask:0xf bank_mask:0xf\n"
        "v_cndmask_b32_dpp v251, v124, v164, vcc row_ror:4 row_mask:0xf bank_mask:0xf\n"
        "v_mfma_f32_16x16x32_bf16 v[172:175], a[160:163], v[206:209], v[172:175]\n"
        "v_cndmask_b32_dpp v249, v125, v165, vcc row_ror:4 row_mask:0xf bank_mask:0xf\n"
        "v_cndmask_b32_dpp v248, v248, v40, vcc row_ror:4 row_mask:0xf bank_mask:0xf\n"
        "v_mfma_f32_16x16x32_bf16 v[172:175], a[164:167], v[210:213], v[172:175]\n"
        "v_cndmask_b32_dpp v250, v250, v41, vcc row_ror:4 row_mask:0xf bank_mask:0xf\n"
        "v_cndmask_b32_dpp v251, v251, v42, vcc row_ror:4 row_mask:0xf bank_mask:0xf\n"
        "v_mfma_f32_16x16x32_bf16 v[34:37], a[176:179], v[210:213], 0\n"
        "v_cndmask_b32_dpp v249, v249, v43, vcc row_ror:4 row_mask:0xf bank_mask:0xf\n"
        "v_mul_f32_dpp v248, v252, v248 quad_perm:[0,0,0,0] row_mask:0xf bank_mask:0xf\n"
        "v_mfma_f32_16x16x32_bf16 v[34:37], a[172:175], v[206:209], v[34:37]\n"
        "v_mul_f32_dpp v250, v252, v250 quad_perm:[1,1,1,1] row_mask:0xf bank_mask:0xf\n"
        "v_mul_f32_dpp v251, v252, v251 quad_perm:[2,2,2,2] row_mask:0xf bank_mask:0xf\n"
        "v_mfma_f32_16x16x32_bf16 v[34:37], a[168:171], v[194:197], v[34:37]\n"
        "buffer_load_dwordx4 v6, s[36:39], s94 offen offset:2048 lds\n"
        "v_mul_f32_dpp v249, v252, v249 quad_perm:[3,3,3,3] row_mask:0xf bank_mask:0xf\n"
        "v_mfma_f32_16x16x32_bf16 v[122:125], a[180:183], v[194:197], 0\n"
        "v_max3_f32 v253, v248, v250, v251\n"
        "v_max_f32 v253, v253, v249\n"
        "v_mfma_f32_16x16x32_bf16 v[122:125], a[184:187], v[206:209], v[122:125]\n"
        "v_mul_f32 v253, v253, v252\n"
        "v_fma_f32 v248, v248, v252, -v253\n"
        "v_mfma_f32_16x16x32_bf16 v[122:125], a[188:191], v[210:213], v[122:125]\n"
        "v_fma_f32 v250, v250, v252, -v253\n"
        "v_fma_f32 v251, v251, v252, -v253\n"
        "v_mfma_f32_16x16x32_bf16 v[162:165], a[200:203], v[210:213], 0\n"
        "v_fma_f32 v249, v249, v252, -v253\n"
        "v_cvt_pk_bf16_f32 v142, v166, v167\n"
        "v_mfma_f32_16x16x32_bf16 v[162:165], a[196:199], v[206:209], v[162:165]\n"
        "v_exp_f32 v248, v248\n"
        "v_mfma_f32_16x16x32_bf16 v[162:165], a[192:195], v[194:197], v[162:165]\n"
        "buffer_load_dwordx4 v7, s[36:39], s94 offen offset:3072 lds\n"
        "v_cvt_pk_bf16_f32 v143, v168, v169\n"
        "v_mfma_f32_16x16x32_bf16 v[40:43], a[204:207], v[194:197], 0\n"
        "v_cvt_pk_bf16_f32 v144, v172, v173\n"
        "v_cvt_pk_bf16_f32 v145, v174, v175\n"
        "v_mfma_f32_16x16x32_bf16 v[40:43], a[208:211], v[206:209], v[40:43]\n"
        "v_cvt_pk_bf16_f32 v180, v34, v35\n"
        "v_cvt_pk_bf16_f32 v181, v36, v37\n"
        "v_mfma_f32_16x16x32_bf16 v[40:43], a[212:215], v[210:213], v[40:43]\n"
        "v_cvt_pk_bf16_f32 v182, v122, v123\n"
        "v_cvt_pk_bf16_f32 v183, v124, v125\n"
        "v_mfma_f32_16x16x32_bf16 v[34:37], v[138:141], a[80:83], 0\n"
        "s_add_u32 m0, s52, 8192\n"
        "v_cvt_pk_bf16_f32 v122, v162, v163\n"
        "v_cvt_pk_bf16_f32 v123, v164, v165\n"
        "v_mfma_f32_16x16x32_bf16 v[34:37], v[132:135], a[76:79], v[34:37]\n"
        "s_nop 0\n"
        "v_cvt_pk_bf16_f32 v124, v40, v41\n"
        "v_cvt_pk_bf16_f32 v125, v42, v43\n"
        "v_mfma_f32_16x16x32_bf16 v[34:37], v[126:129], a[72:75], v[34:37]\n"
        "buffer_load_dwordx4 v8, s[36:39], s94 offen lds\n"
        "v_mfma_f32_16x16x32_bf16 v[184:187], v[126:129], a[84:87], 0\n"
        "v_exp_f32 v250, v250\n"
        "v_mfma_f32_16x16x32_bf16 v[184:187], v[132:135], a[88:91], v[184:187]\n"
        "v_exp_f32 v251, v251\n"
        "v_mfma_f32_16x16x32_bf16 v[184:187], v[138:141], a[92:95], v[184:187]\n"
        "v_exp_f32 v249, v249\n"
        "v_mfma_f32_16x16x32_bf16 v[176:179], v[194:197], v[142:145], 0\n"
        "v_add_f32 v253, v248, v250\n"
        "v_add_f32 v245, v251, v249\n"
        "v_mfma_f32_16x16x32_bf16 v[176:179], v[206:209], v[180:183], v[176:179]\n"
        "v_add_f32 v253, v253, v245\n"
        "v_cvt_pk_bf16_f32 v42, v34, v35\n"
        "v_mfma_f32_16x16x32_bf16 v[176:179], v[210:213], v[122:125], v[176:179]\n"
        "v_rcp_f32 v253, v253\n"
        "v_mfma_f32_16x16x32_bf16 v[172:175], v[218:221], v[122:125], 0\n"
        "v_exp_f16_sdwa v246, v246 dst_sel:WORD_1 dst_unused:UNUSED_PRESERVE src0_sel:WORD_1\n"
        "v_mfma_f32_16x16x32_bf16 v[172:175], v[226:229], v[180:183], v[172:175]\n"
        "v_mul_f32 v248, v248, v253\n"
        "v_mul_f32 v250, v250, v253\n"
        "v_mfma_f32_16x16x32_bf16 v[172:175], v[222:225], v[142:145], v[172:175]\n"
        "v_mul_f32 v251, v251, v253\n"
        "v_mul_f32 v249, v249, v253\n"
        "v_mfma_f32_16x16x32_bf16 v[164:167], v[198:201], v[142:145], 0\n"
        "v_mul_f32_dpp v248, v252, v248 quad_perm:[0,0,0,0] row_mask:0xf bank_mask:0xf\n"
        "v_mul_f32_dpp v250, v252, v250 quad_perm:[1,1,1,1] row_mask:0xf bank_mask:0xf\n"
        "v_mfma_f32_16x16x32_bf16 v[164:167], v[214:217], v[180:183], v[164:167]\n"
        "v_mul_f32_dpp v251, v252, v251 quad_perm:[2,2,2,2] row_mask:0xf bank_mask:0xf\n"
        "v_mul_f32_dpp v249, v252, v249 quad_perm:[3,3,3,3] row_mask:0xf bank_mask:0xf\n"
        "v_mfma_f32_16x16x32_bf16 v[164:167], v[202:205], v[122:125], v[164:167]\n"
        "v_cvt_pk_bf16_f32 v244, v248, v250\n"
        "v_cvt_pk_bf16_f32 v243, v251, v249\n"
        "v_mfma_f32_16x16x32_bf16 v[142:145], v[138:141], a[104:107], 0\n"
        "v_and_b32 v124, v25, v244\n"
        "v_and_b32 v125, v25, v243\n"
        "v_mfma_f32_16x16x32_bf16 v[142:145], v[132:135], a[100:103], v[142:145]\n"
        "v_mov_b64 v[130:131], v[124:125]\n"
        "v_cvt_pk_bf16_f32 v43, v36, v37\n"
        "v_mfma_f32_16x16x32_bf16 v[142:145], v[126:129], a[96:99], v[142:145]\n"
        "v_cvt_pk_bf16_f32 v46, v184, v185\n"
        "v_cvt_pk_bf16_f32 v47, v186, v187\n"
        "v_mfma_f32_16x16x32_bf16 v[184:187], v[126:129], a[108:111], 0\n"
        "v_cvt_f32_f16 v242, v246\n"
        "v_cndmask_b32_e64 v242, v242, v30, s[68:69]\n"
        "v_mfma_f32_16x16x32_bf16 v[184:187], v[132:135], a[112:115], v[184:187]\n"
        "s_nop 0\n"
        "v_add_f32_dpp v253, v242, v242 row_ror:8 row_mask:0xf bank_mask:0xf\n"
        "v_mov_b64 v[136:137], v[124:125]\n"
        "v_mfma_f32_16x16x32_bf16 v[184:187], v[138:141], a[116:119], v[184:187]\n"
        "v_add_f32_dpp v253, v253, v253 row_ror:4 row_mask:0xf bank_mask:0xf\n"
        "v_cvt_pk_bf16_f32 v30, v142, v143\n"
        "v_mfma_f32_16x16x32_bf16 v[188:191], v[42:45], v[124:127], 0\n"
        "ds_read_u16 v245, v19 offset:2560\n"
        "v_add_f32_dpp v253, v253, v253 row_ror:2 row_mask:0xf bank_mask:0xf\n"
        "v_mfma_f32_16x16x32_bf16 v[180:183], v[44:47], v[128:131], 0\n"
        "s_nop 0\n"
        "v_add_f32_dpp v253, v253, v253 row_ror:1 row_mask:0xf bank_mask:0xf\n"
        "v_cvt_pk_bf16_f32 v31, v144, v145\n"
        "v_mfma_f32_16x16x32_bf16 v[40:43], v[138:141], a[128:131], 0\n"
        "v_add_f32_dpp v253, v253, v253 row_bcast:15 row_mask:0xa bank_mask:0xf\n"
        "v_cvt_pk_bf16_f32 v34, v184, v185\n"
        "v_mfma_f32_16x16x32_bf16 v[40:43], v[132:135], a[124:127], v[40:43]\n"
        "v_cvt_pk_bf16_f32 v35, v186, v187\n"
        "v_add_f32_dpp v253, v253, v253 row_bcast:31 row_mask:0xc bank_mask:0xf\n"
        "v_mfma_f32_16x16x32_bf16 v[40:43], v[126:129], a[120:123], v[40:43]\n"
        "v_readlane_b32 s95, v253, 63\n"
        "v_mov_b64 v[142:143], v[124:125]\n"
        "v_mfma_f32_16x16x32_bf16 v[234:237], v[126:129], a[132:135], 0\n"
        "v_rcp_f32 v252, s95\n"
        "v_mfma_f32_16x16x32_bf16 v[234:237], v[132:135], a[136:139], v[234:237]\n"
        "v_mul_f32 v242, v242, v252\n"
        "v_cvt_f16_f32 v242, v242\n"
        "v_mfma_f32_16x16x32_bf16 v[234:237], v[138:141], a[140:143], v[234:237]\n"
        "v_mul_f16 v242, v242, v247\n"
        "v_and_b32_dpp v104, v244, v25 row_ror:12 row_mask:0xf bank_mask:0xf\n"
        "v_mfma_f32_16x16x32_bf16 v[124:127], v[30:33], v[130:133], 0\n"
        "s_waitcnt lgkmcnt(0)\n"
        "ds_write_b16 v22, v242 offset:512\n"
        "v_mfma_f32_16x16x32_bf16 v[184:187], v[32:35], v[134:137], 0\n"
        "v_cmp_ne_u32_e64 s[24:25], s29, v245\n"
        "v_and_b32_dpp v105, v243, v25 row_ror:12 row_mask:0xf bank_mask:0xf\n"
        "v_mfma_f32_16x16x32_bf16 v[132:135], v[118:121], a[80:83], 0\n"
        "v_cvt_pk_bf16_f32 v36, v40, v41\n"
        "v_cvt_pk_bf16_f32 v37, v42, v43\n"
        "v_mfma_f32_16x16x32_bf16 v[132:135], v[112:115], a[76:79], v[132:135]\n"
        "s_cmp_lg_u64 s[24:25], 0\n"
        "v_cvt_pk_bf16_f32 v40, v234, v235\n"
        "v_cvt_pk_bf16_f32 v41, v236, v237\n"
        "v_mfma_f32_16x16x32_bf16 v[132:135], v[106:109], a[72:75], v[132:135]\n"
        "s_cbranch_scc1 SP10_%=\n"
        "BK10_%=:\n"
        "v_mov_b64 v[110:111], v[104:105]\n"
        "v_cvt_pk_bf16_f32 v234, v188, v189\n"
        "v_mfma_f32_16x16x32_bf16 v[128:131], v[106:109], a[84:87], 0\n"
        "v_cvt_pk_bf16_f32 v188, v190, v191\n"
        "v_cvt_pk_bf16_f32 v122, v180, v181\n"
        "v_mfma_f32_16x16x32_bf16 v[128:131], v[112:115], a[88:91], v[128:131]\n"
        "v_cvt_pk_bf16_f32 v235, v182, v183\n"
        "v_cvt_pk_bf16_f32 v189, v124, v125\n"
        "v_mfma_f32_16x16x32_bf16 v[128:131], v[118:121], a[92:95], v[128:131]\n"
        "v_cvt_pk_bf16_f32 v123, v126, v127\n"
        "v_cvt_pk_bf16_f32 v236, v184, v185\n"
        "v_mfma_f32_16x16x32_bf16 v[180:183], v[36:39], v[136:139], 0\n"
        "s_nop 0\n"
        "v_cvt_pk_bf16_f32 v42, v132, v133\n"
        "v_cvt_pk_bf16_f32 v43, v134, v135\n"
        "v_mfma_f32_16x16x32_bf16 v[132:135], v[38:41], v[140:143], 0\n"
        "s_nop 0\n"
        "v_cvt_pk_bf16_f32 v46, v128, v129\n"
        "v_cvt_pk_bf16_f32 v47, v130, v131\n"
        "v_mfma_f32_16x16x32_bf16 v[34:37], v[118:121], a[104:107], 0\n"
        "v_cvt_pk_bf16_f32 v237, v182, v183\n"
        "v_cvt_pk_bf16_f32 v190, v186, v187\n"
        "v_mfma_f32_16x16x32_bf16 v[34:37], v[112:115], a[100:103], v[34:37]\n"
        "v_cvt_pk_bf16_f32 v124, v180, v181\n"
        "v_cvt_pk_bf16_f32 v191, v132, v133\n"
        "v_mfma_f32_16x16x32_bf16 v[34:37], v[106:109], a[96:99], v[34:37]\n"
        "ds_write_b128 v12, v[234:237]\n"
        "v_mfma_f32_16x16x32_bf16 v[130:133], v[106:109], a[108:111], 0\n"
        "s_nop 0\n"
        "v_cvt_pk_bf16_f32 v125, v134, v135\n"
        "v_and_b32 v253, 0xffff0000, v246\n"
        "v_mfma_f32_16x16x32_bf16 v[130:133], v[112:115], a[112:115], v[130:133]\n"
        "s_nop 0\n"
        "v_mov_b64 v[116:117], v[104:105]\n"
        "v_cvt_pk_bf16_f32 v30, v34, v35\n"
        "v_mfma_f32_16x16x32_bf16 v[130:133], v[118:121], a[116:119], v[130:133]\n"
        "s_nop 0\n"
        "ds_write_b128 v12, v[188:191] offset:1024\n"
        "v_mfma_f32_16x16x32_bf16 v[126:129], v[42:45], v[104:107], 0\n"
        "s_nop 0\n"
        "ds_write_b128 v12, v[122:125] offset:2048\n"
        "v_mfma_f32_16x16x32_bf16 v[40:43], v[44:47], v[108:111], 0\n"
        "s_nop 0\n"
        "v_cvt_pk_bf16_f32 v31, v36, v37\n"
        "v_cvt_pk_bf16_f32 v34, v130, v131\n"
        "v_mfma_f32_16x16x32_bf16 v[134:137], v[234:237], v[234:237], 0\n"
        "v_cvt_pk_bf16_f32 v35, v132, v133\n"
        "v_mul_f32_dpp v247, v255, v248 quad_perm:[0,0,0,0] row_mask:0xf bank_mask:0xf\n"
        "v_mfma_f32_16x16x32_bf16 v[134:137], v[188:191], v[188:191], v[134:137]\n"
        "v_and_b32_dpp v144, v244, v25 row_ror:8 row_mask:0xf bank_mask:0xf\n"
        "v_and_b32_dpp v145, v243, v25 row_ror:8 row_mask:0xf bank_mask:0xf\n"
        "v_mfma_f32_16x16x32_bf16 v[134:137], v[122:125], v[122:125], v[134:137]\n"
        "v_mov_b64 v[122:123], v[104:105]\n"
        "v_mov_b64 v[150:151], v[144:145]\n"
        "v_mfma_f32_16x16x32_bf16 v[140:143], v[118:121], a[128:131], 0\n"
        "v_mov_b64 v[156:157], v[144:145]\n"
        "v_fmac_f32_dpp v247, v255, v250 quad_perm:[1,1,1,1] row_mask:0xf bank_mask:0xf\n"
        "v_mfma_f32_16x16x32_bf16 v[140:143], v[112:115], a[124:127], v[140:143]\n"
        "v_mov_b64 v[162:163], v[144:145]\n"
        "v_fmac_f32_dpp v247, v255, v251 quad_perm:[2,2,2,2] row_mask:0xf bank_mask:0xf\n"
        "v_mfma_f32_16x16x32_bf16 v[140:143], v[106:109], a[120:123], v[140:143]\n"
        "s_nop 0\n"
        "v_fmac_f32_dpp v247, v255, v249 quad_perm:[3,3,3,3] row_mask:0xf bank_mask:0xf\n"
        "v_add_f32 v247, v247, v254\n"
        "v_mfma_f32_16x16x32_bf16 v[130:133], v[106:109], a[132:135], 0\n"
        "v_cvt_pk_f16_f32 v138, v176, v177\n"
        "v_cvt_pk_f16_f32 v139, v178, v179\n"
        "v_mfma_f32_16x16x32_bf16 v[130:133], v[112:115], a[136:139], v[130:133]\n"
        "s_nop 0\n"
        "v_cvt_pk_bf16_f32 v36, v140, v141\n"
        "v_cvt_pk_bf16_f32 v37, v142, v143\n"
        "v_mfma_f32_16x16x32_bf16 v[130:133], v[118:121], a[140:143], v[130:133]\n"
        "v_cvt_pk_bf16_f32 v104, v126, v127\n"
        "v_cvt_pk_bf16_f32 v124, v128, v129\n"
        "v_mfma_f32_16x16x32_bf16 v[126:129], v[30:33], v[110:113], 0\n"
        "s_nop 0\n"
        "v_cvt_pk_bf16_f32 v112, v40, v41\n"
        "v_cvt_pk_bf16_f32 v105, v42, v43\n"
        "v_mfma_f32_16x16x32_bf16 v[108:111], v[32:35], v[114:117], 0\n"
        "s_nop 0\n"
        "v_cvt_pk_bf16_f32 v40, v130, v131\n"
        "v_cvt_pk_bf16_f32 v41, v132, v133\n"
        "v_mfma_f32_16x16x32_bf16 v[130:133], v[158:161], a[80:83], 0\n"
        "v_cvt_pk_bf16_f32 v125, v126, v127\n"
        "v_cvt_pk_bf16_f32 v113, v128, v129\n"
        "v_mfma_f32_16x16x32_bf16 v[130:133], v[152:155], a[76:79], v[130:133]\n"
        "s_nop 0\n"
        "v_cvt_pk_bf16_f32 v106, v108, v109\n"
        "v_cvt_pk_bf16_f32 v126, v110, v111\n"
        "v_mfma_f32_16x16x32_bf16 v[130:133], v[146:149], a[72:75], v[130:133]\n"
        "v_pk_mul_f16 v138, v138, v170 op_sel_hi:[1,0]\n"
        "v_pk_mul_f16 v139, v139, v170 op_sel_hi:[1,0]\n"
        "v_mfma_f32_16x16x32_bf16 v[108:111], v[146:149], a[84:87], 0\n"
        "s_nop 0\n"
        "v_pk_mul_f16 v138, v138, v230\n"
        "v_pk_mul_f16 v139, v139, v231\n"
        "v_mfma_f32_16x16x32_bf16 v[108:111], v[152:155], a[88:91], v[108:111]\n"
        "s_nop 0\n"
        "v_cvt_pk_bf16_f32 v42, v130, v131\n"
        "v_cvt_pk_bf16_f32 v43, v132, v133\n"
        "v_mfma_f32_16x16x32_bf16 v[108:111], v[158:161], a[92:95], v[108:111]\n"
        "v_exp_f16_sdwa v138, v138 dst_sel:WORD_0 dst_unused:UNUSED_PRESERVE src0_sel:WORD_0\n"
        "v_mfma_f32_16x16x32_bf16 v[128:131], v[36:39], v[116:119], 0\n"
        "v_exp_f16_sdwa v139, v139 dst_sel:WORD_0 dst_unused:UNUSED_PRESERVE src0_sel:WORD_0\n"
        "v_mfma_f32_16x16x32_bf16 v[116:119], v[38:41], v[120:123], 0\n"
        "s_nop 0\n"
        "v_exp_f16_sdwa v138, v138 dst_sel:WORD_1 dst_unused:UNUSED_PRESERVE src0_sel:WORD_1\n"
        "v_mfma_f32_16x16x32_bf16 v[34:37], v[158:161], a[104:107], 0\n"
        "s_nop 0\n"
        "v_cvt_pk_bf16_f32 v46, v108, v109\n"
        "v_cvt_pk_bf16_f32 v47, v110, v111\n"
        "v_mfma_f32_16x16x32_bf16 v[34:37], v[152:155], a[100:103], v[34:37]\n"
        "v_cvt_pk_bf16_f32 v107, v130, v131\n"
        "v_cvt_pk_bf16_f32 v114, v128, v129\n"
        "v_mfma_f32_16x16x32_bf16 v[34:37], v[146:149], a[96:99], v[34:37]\n"
        "s_nop 0\n"
        "ds_write_b128 v12, v[104:107] offset:3072\n"
        "v_mfma_f32_16x16x32_bf16 v[120:123], v[146:149], a[108:111], 0\n"
        "s_nop 0\n"
        "v_cvt_pk_bf16_f32 v127, v116, v117\n"
        "v_cvt_pk_bf16_f32 v115, v118, v119\n"
        "v_mfma_f32_16x16x32_bf16 v[120:123], v[152:155], a[112:115], v[120:123]\n"
        "s_nop 0\n"
        "v_cvt_pk_bf16_f32 v30, v34, v35\n"
        "v_cvt_pk_bf16_f32 v31, v36, v37\n"
        "v_mfma_f32_16x16x32_bf16 v[120:123], v[158:161], a[116:119], v[120:123]\n"
        "ds_write_b128 v12, v[124:127] offset:4096\n"
        "v_mfma_f32_16x16x32_bf16 v[128:131], v[42:45], v[144:147], 0\n"
        "ds_write_b128 v12, v[112:115] offset:5120\n"
        "v_mfma_f32_16x16x32_bf16 v[108:111], v[44:47], v[148:151], 0\n"
        "s_nop 0\n"
        "v_exp_f16_sdwa v139, v139 dst_sel:WORD_1 dst_unused:UNUSED_PRESERVE src0_sel:WORD_1\n"
        "v_mfma_f32_16x16x32_bf16 v[116:119], v[104:107], v[104:107], 0\n"
        "s_nop 0\n"
        "v_cvt_pk_bf16_f32 v34, v120, v121\n"
        "v_cvt_pk_bf16_f32 v35, v122, v123\n"
        "v_mfma_f32_16x16x32_bf16 v[116:119], v[124:127], v[124:127], v[116:119]\n"
        "v_mul_f32 v254, s80, v247\n"
        "v_pk_add_f16 v120, v138, v139\n"
        "v_mfma_f32_16x16x32_bf16 v[116:119], v[112:115], v[112:115], v[116:119]\n"
        "v_cvt_pk_f16_f32 v140, v172, v173\n"
        "v_cvt_pk_f16_f32 v141, v174, v175\n"
        "v_mfma_f32_16x16x32_bf16 v[40:43], v[158:161], a[128:131], 0\n"
        "v_pk_mul_f16 v140, v140, v170 op_sel_hi:[1,0]\n"
        "v_pk_mul_f16 v141, v141, v170 op_sel_hi:[1,0]\n"
        "v_mfma_f32_16x16x32_bf16 v[40:43], v[152:155], a[124:127], v[40:43]\n"
        "v_pk_mul_f16 v140, v140, v232\n"
        "v_pk_mul_f16 v141, v141, v233\n"
        "v_mfma_f32_16x16x32_bf16 v[40:43], v[146:149], a[120:123], v[40:43]\n"
        "s_nop 0\n"
        "v_exp_f16_sdwa v140, v140 dst_sel:WORD_0 dst_unused:UNUSED_PRESERVE src0_sel:WORD_0\n"
        "v_mfma_f32_16x16x32_bf16 v[142:145], v[146:149], a[132:135], 0\n"
        "s_nop 0\n"
        "v_cvt_pk_bf16_f32 v146, v128, v129\n"
        "v_cvt_pk_bf16_f32 v126, v130, v131\n"
        "v_mfma_f32_16x16x32_bf16 v[142:145], v[152:155], a[136:139], v[142:145]\n"
        "s_nop 0\n"
        "v_cvt_pk_bf16_f32 v36, v40, v41\n"
        "v_cvt_pk_bf16_f32 v37, v42, v43\n"
        "v_mfma_f32_16x16x32_bf16 v[142:145], v[158:161], a[140:143], v[142:145]\n"
        "s_nop 0\n"
        "v_cvt_pk_bf16_f32 v112, v108, v109\n"
        "v_cvt_pk_bf16_f32 v147, v110, v111\n"
        "v_mfma_f32_16x16x32_bf16 v[122:125], v[30:33], v[150:153], 0\n"
        "s_nop 0\n"
        "v_exp_f16_sdwa v141, v141 dst_sel:WORD_0 dst_unused:UNUSED_PRESERVE src0_sel:WORD_0\n"
        "v_mfma_f32_16x16x32_bf16 v[104:107], v[32:35], v[154:157], 0\n"
        "s_nop 0\n"
        "v_cvt_pk_bf16_f32 v40, v142, v143\n"
        "v_cvt_pk_bf16_f32 v41, v144, v145\n"
        "v_mfma_f32_16x16x32_bf16 v[108:111], a[152:155], v[218:221], 0\n"
        "s_nop 0\n"
        "v_cvt_pk_bf16_f32 v127, v122, v123\n"
        "v_cvt_pk_bf16_f32 v113, v124, v125\n"
        "v_mfma_f32_16x16x32_bf16 v[108:111], a[148:151], v[226:229], v[108:111]\n"
        "v_cvt_pk_bf16_f32 v148, v104, v105\n"
        "v_cvt_pk_bf16_f32 v128, v106, v107\n"
        "v_mfma_f32_16x16x32_bf16 v[108:111], a[144:147], v[222:225], v[108:111]\n"
        "v_exp_f16_sdwa v140, v140 dst_sel:WORD_1 dst_unused:UNUSED_PRESERVE src0_sel:WORD_1\n"
        "v_mfma_f32_16x16x32_bf16 v[104:107], v[36:39], v[156:159], 0\n"
        "s_nop 0\n"
        "v_exp_f16_sdwa v141, v141 dst_sel:WORD_1 dst_unused:UNUSED_PRESERVE src0_sel:WORD_1\n"
        "v_mfma_f32_16x16x32_bf16 v[34:37], v[38:41], v[160:163], 0\n"
        "s_nop 0\n"
        "v_pk_add_f16 v120, v120, v140\n"
        "v_cndmask_b32_e64 v251, v134, v135, s[56:57]\n"
        "v_mfma_f32_16x16x32_bf16 v[40:43], a[156:159], v[222:225], 0\n"
        "s_nop 0\n"
        "v_cvt_pk_bf16_f32 v149, v106, v107\n"
        "v_cvt_pk_bf16_f32 v114, v104, v105\n"
        "v_mfma_f32_16x16x32_bf16 v[40:43], a[160:163], v[226:229], v[40:43]\n"
        "v_cvt_pk_bf16_f32 v129, v34, v35\n"
        "v_cvt_pk_bf16_f32 v115, v36, v37\n"
        "v_mfma_f32_16x16x32_bf16 v[40:43], a[164:167], v[218:221], v[40:43]\n"
        "ds_write_b128 v12, v[146:149] offset:6144\n"
        "v_mfma_f32_16x16x32_bf16 v[34:37], a[176:179], v[218:221], 0\n"
        "v_cndmask_b32_e64 v251, v251, v136, s[58:59]\n"
        "v_cndmask_b32_e64 v255, v116, v117, s[56:57]\n"
        "v_mfma_f32_16x16x32_bf16 v[34:37], a[172:175], v[226:229], v[34:37]\n"
        "v_cndmask_b32_e64 v255, v255, v118, s[58:59]\n"
        "v_cndmask_b32_e64 v255, v255, v119, s[60:61]\n"
        "v_mfma_f32_16x16x32_bf16 v[34:37], a[168:171], v[222:225], v[34:37]\n"
        "ds_write_b128 v12, v[126:129] offset:7168\n"
        "v_mfma_f32_16x16x32_bf16 v[104:107], v[146:149], v[146:149], 0\n"
        "v_cvt_pk_bf16_f32 v116, v108, v109\n"
        "v_cvt_pk_bf16_f32 v117, v110, v111\n"
        "v_mfma_f32_16x16x32_bf16 v[104:107], v[126:129], v[126:129], v[104:107]\n"
        "v_cvt_pk_bf16_f32 v118, v40, v41\n"
        "v_cvt_pk_bf16_f32 v119, v42, v43\n"
        "v_mfma_f32_16x16x32_bf16 v[104:107], v[112:115], v[112:115], v[104:107]\n"
        "s_nop 0\n"
        "ds_write_b128 v12, v[112:115] offset:8192\n"
        "v_mfma_f32_16x16x32_bf16 v[40:43], a[188:191], v[218:221], 0\n"
        "s_cmp_le_i32 s85, 0\n"
        "v_cvt_pk_bf16_f32 v108, v34, v35\n"
        "v_cvt_pk_bf16_f32 v109, v36, v37\n"
        "v_mfma_f32_16x16x32_bf16 v[40:43], a[184:187], v[226:229], v[40:43]\n"
        "s_cbranch_scc1 DY3_%=\n"
        "DY3B_%=:\n"
        "v_cndmask_b32_e64 v250, v104, v105, s[56:57]\n"
        "v_cndmask_b32_e64 v250, v250, v106, s[58:59]\n"
        "v_mfma_f32_16x16x32_bf16 v[40:43], a[180:183], v[222:225], v[40:43]\n"
        "v_cndmask_b32_e64 v250, v250, v107, s[60:61]\n"
        "v_cndmask_b32_e64 v251, v251, v137, s[60:61]\n"
        "v_mfma_f32_16x16x32_bf16 v[112:115], a[192:195], v[222:225], 0\n"
        "s_nop 0\n"
        "v_cndmask_b32_dpp v255, v250, v255, vcc row_ror:4 row_mask:0xf bank_mask:0xf\n"
        "v_pk_add_f16 v120, v120, v141\n"
        "v_mfma_f32_16x16x32_bf16 v[112:115], a[196:199], v[226:229], v[112:115]\n"
        "s_nop 0\n"
        "v_cvt_pk_bf16_f32 v110, v40, v41\n"
        "v_cvt_pk_bf16_f32 v111, v42, v43\n"
        "v_mfma_f32_16x16x32_bf16 v[112:115], a[200:203], v[218:221], v[112:115]\n"
        "v_cndmask_b32_dpp v251, v255, v251, vcc row_ror:4 row_mask:0xf bank_mask:0xf\n"
        "v_fma_f32 v254, -v254, v247, v251\n"
        "v_mfma_f32_16x16x32_bf16 v[34:37], a[212:215], v[218:221], 0\n"
        "s_nop 0\n"
        "v_mov_b32 v255, s29\n"
        "v_max_f32 v254, 0, v254\n"
        "v_mfma_f32_16x16x32_bf16 v[34:37], a[208:211], v[226:229], v[34:37]\n"
        "s_nop 0\n"
        "v_cvt_pk_bf16_f32 v104, v112, v113\n"
        "v_cvt_pk_bf16_f32 v105, v114, v115\n"
        "v_mfma_f32_16x16x32_bf16 v[34:37], a[204:207], v[222:225], v[34:37]\n"
        "v_sub_f32 v251, 0x47c00000, v251\n"
        "v_fmaak_f32 v254, v28, v254, 0x3727c5ac\n"
        "v_mfma_f32_16x16x32_bf16 v[112:115], a[144:147], v[198:201], 0\n"
        "s_nop 0\n"
        "v_bfi_b32 v254, s84, v251, v254\n"
        "v_cvt_pk_f16_f32 v150, v164, v165\n"
        "v_mfma_f32_16x16x32_bf16 v[112:115], a[148:151], v[214:217], v[112:115]\n"
        "s_nop 0\n"
        "v_cvt_pk_bf16_f32 v106, v34, v35\n"
        "v_cvt_pk_bf16_f32 v107, v36, v37\n"
        "v_mfma_f32_16x16x32_bf16 v[112:115], a[152:155], v[202:205], v[112:115]\n"
        "v_rsq_f32 v251, v254\n"
        "v_mfma_f32_16x16x32_bf16 v[40:43], v[194:197], v[116:119], 0\n"
        "v_cvt_f16_f32 v251, v251\n"
        "v_cvt_pk_f16_f32 v151, v166, v167\n"
        "v_mfma_f32_16x16x32_bf16 v[40:43], v[206:209], v[108:111], v[40:43]\n"
        "ds_write_b16 v18, v251\n"
        "v_mfma_f32_16x16x32_bf16 v[40:43], v[210:213], v[104:107], v[40:43]\n"
        "ds_write_b16 v19, v251\n"
        "v_mfma_f32_16x16x32_bf16 v[34:37], v[218:221], v[104:107], 0\n"
        "ds_write_b16 v22, v255 offset:3072\n"
        "v_mfma_f32_16x16x32_bf16 v[34:37], v[226:229], v[108:111], v[34:37]\n"
        "v_pk_mul_f16 v150, v150, v170 op_sel_hi:[1,0]\n"
        "v_pk_mul_f16 v151, v151, v170 op_sel_hi:[1,0]\n"
        "v_mfma_f32_16x16x32_bf16 v[34:37], v[222:225], v[116:119], v[34:37]\n"
        "v_pk_mul_f16 v150, v150, v238\n"
        "v_pk_mul_f16 v151, v151, v239\n"
        "v_mfma_f32_16x16x32_bf16 v[122:125], v[198:201], v[116:119], 0\n"
        "v_exp_f16_sdwa v150, v150 dst_sel:WORD_0 dst_unused:UNUSED_PRESERVE src0_sel:WORD_0\n"
        "v_mfma_f32_16x16x32_bf16 v[122:125], v[214:217], v[108:111], v[122:125]\n"
        "v_exp_f16_sdwa v151, v151 dst_sel:WORD_0 dst_unused:UNUSED_PRESERVE src0_sel:WORD_0\n"
        "v_mfma_f32_16x16x32_bf16 v[122:125], v[202:205], v[104:107], v[122:125]\n"
        "v_exp_f16_sdwa v150, v150 dst_sel:WORD_1 dst_unused:UNUSED_PRESERVE src0_sel:WORD_1\n"
        "v_mfma_f32_16x16x32_bf16 v[116:119], a[164:167], v[202:205], 0\n"
        "v_exp_f16_sdwa v151, v151 dst_sel:WORD_1 dst_unused:UNUSED_PRESERVE src0_sel:WORD_1\n"
        "v_mfma_f32_16x16x32_bf16 v[116:119], a[160:163], v[214:217], v[116:119]\n"
        "v_pk_add_f16 v120, v120, v150\n"
        "v_pk_add_f16 v120, v120, v151\n"
        "v_mfma_f32_16x16x32_bf16 v[116:119], a[156:159], v[198:201], v[116:119]\n"
        "v_cvt_pk_bf16_f32 v108, v112, v113\n"
        "v_cvt_pk_f16_f32 v154, v40, v41\n"
        "v_mfma_f32_16x16x32_bf16 v[104:107], a[168:171], v[198:201], 0\n"
        "v_cvt_pk_f16_f32 v155, v42, v43\n"
        "v_pk_mul_f16 v154, v154, v170 op_sel:[0,1]\n"
        "v_mfma_f32_16x16x32_bf16 v[104:107], a[172:175], v[214:217], v[104:107]\n"
        "v_pk_mul_f16 v155, v155, v170 op_sel:[0,1]\n"
        "v_pk_mul_f16 v154, v154, v230\n"
        "v_mfma_f32_16x16x32_bf16 v[104:107], a[176:179], v[202:205], v[104:107]\n"
        "v_pk_mul_f16 v155, v155, v231\n"
        "v_cvt_pk_bf16_f32 v109, v114, v115\n"
        "v_mfma_f32_16x16x32_bf16 v[40:43], a[188:191], v[202:205], 0\n"
        "v_exp_f16_sdwa v154, v154 dst_sel:WORD_0 dst_unused:UNUSED_PRESERVE src0_sel:WORD_0\n"
        "v_mfma_f32_16x16x32_bf16 v[40:43], a[184:187], v[214:217], v[40:43]\n"
        "v_exp_f16_sdwa v155, v155 dst_sel:WORD_0 dst_unused:UNUSED_PRESERVE src0_sel:WORD_0\n"
        "v_mfma_f32_16x16x32_bf16 v[40:43], a[180:183], v[198:201], v[40:43]\n"
        "v_exp_f16_sdwa v154, v154 dst_sel:WORD_1 dst_unused:UNUSED_PRESERVE src0_sel:WORD_1\n"
        "v_mfma_f32_16x16x32_bf16 v[112:115], a[192:195], v[198:201], 0\n"
        "v_exp_f16_sdwa v155, v155 dst_sel:WORD_1 dst_unused:UNUSED_PRESERVE src0_sel:WORD_1\n"
        "v_mfma_f32_16x16x32_bf16 v[112:115], a[196:199], v[214:217], v[112:115]\n"
        "v_pk_add_f16 v121, v154, v155\n"
        "v_cvt_pk_f16_f32 v156, v34, v35\n"
        "v_mfma_f32_16x16x32_bf16 v[112:115], a[200:203], v[202:205], v[112:115]\n"
        "v_cvt_pk_f16_f32 v157, v36, v37\n"
        "v_pk_mul_f16 v156, v156, v170 op_sel:[0,1]\n"
        "v_mfma_f32_16x16x32_bf16 v[34:37], a[212:215], v[202:205], 0\n"
        "v_pk_mul_f16 v157, v157, v170 op_sel:[0,1]\n"
        "v_cvt_pk_bf16_f32 v110, v116, v117\n"
        "v_mfma_f32_16x16x32_bf16 v[34:37], a[208:211], v[214:217], v[34:37]\n"
        "v_cvt_pk_bf16_f32 v111, v118, v119\n"
        "v_cvt_pk_bf16_f32 v126, v104, v105\n"
        "v_mfma_f32_16x16x32_bf16 v[34:37], a[204:207], v[198:201], v[34:37]\n"
        "v_cvt_pk_bf16_f32 v127, v106, v107\n"
        "v_cvt_pk_bf16_f32 v128, v40, v41\n"
        "v_mfma_f32_16x16x32_bf16 v[104:107], v[194:197], a[216:219], 0\n"
        "v_cvt_pk_bf16_f32 v129, v42, v43\n"
        "v_cvt_pk_bf16_f32 v40, v112, v113\n"
        "v_mfma_f32_16x16x32_bf16 v[104:107], v[206:209], a[220:223], v[104:107]\n"
        "s_nop 0\n"
        "v_cvt_pk_bf16_f32 v41, v114, v115\n"
        "v_cvt_pk_bf16_f32 v42, v34, v35\n"
        "v_mfma_f32_16x16x32_bf16 v[104:107], v[210:213], a[224:227], v[104:107]\n"
        "v_cvt_pk_bf16_f32 v43, v36, v37\n"
        "v_pk_mul_f16 v156, v156, v232\n"
        "v_mfma_f32_16x16x32_bf16 v[116:119], v[218:221], a[224:227], 0\n"
        "v_pk_mul_f16 v157, v157, v233\n"
        "v_cvt_pk_f16_f32 v146, v122, v123\n"
        "v_mfma_f32_16x16x32_bf16 v[116:119], v[226:229], a[220:223], v[116:119]\n"
        "v_exp_f16_sdwa v156, v156 dst_sel:WORD_0 dst_unused:UNUSED_PRESERVE src0_sel:WORD_0\n"
        "v_mfma_f32_16x16x32_bf16 v[116:119], v[222:225], a[216:219], v[116:119]\n"
        "v_exp_f16_sdwa v157, v157 dst_sel:WORD_0 dst_unused:UNUSED_PRESERVE src0_sel:WORD_0\n"
        "v_mfma_f32_16x16x32_bf16 v[130:133], v[198:201], a[216:219], 0\n"
        "v_exp_f16_sdwa v156, v156 dst_sel:WORD_1 dst_unused:UNUSED_PRESERVE src0_sel:WORD_1\n"
        "v_mfma_f32_16x16x32_bf16 v[130:133], v[214:217], a[220:223], v[130:133]\n"
        "v_exp_f16_sdwa v157, v157 dst_sel:WORD_1 dst_unused:UNUSED_PRESERVE src0_sel:WORD_1\n"
        "v_mfma_f32_16x16x32_bf16 v[130:133], v[202:205], a[224:227], v[130:133]\n"
        "v_pk_add_f16 v121, v121, v156\n"
        "v_pk_add_f16 v121, v121, v157\n"
        "v_mfma_f32_16x16x32_bf16 v[34:37], v[194:197], v[108:111], 0\n"
        "v_cvt_pk_f16_f32 v147, v124, v125\n"
        "v_pk_mul_f16 v146, v146, v170 op_sel:[0,1]\n"
        "v_mfma_f32_16x16x32_bf16 v[34:37], v[206:209], v[126:129], v[34:37]\n"
        "v_pk_mul_f16 v147, v147, v170 op_sel:[0,1]\n"
        "v_pk_mul_f16 v146, v146, v238\n"
        "v_mfma_f32_16x16x32_bf16 v[34:37], v[210:213], v[40:43], v[34:37]\n"
        "v_pk_mul_f16 v147, v147, v239\n"
        "v_cvt_pk_f16_f32 v166, v104, v105\n"
        "v_mfma_f32_16x16x32_bf16 v[112:115], v[218:221], v[40:43], 0\n"
        "v_cndmask_b32_e64 v152, 0, v253, s[62:63]\n"
        "v_cndmask_b32_e64 v148, 0, v253, s[64:65]\n"
        "v_mfma_f32_16x16x32_bf16 v[112:115], v[226:229], v[126:129], v[112:115]\n"
        "v_pk_add_f16 v120, v120, v152\n"
        "v_cvt_pk_f16_f32 v167, v106, v107\n"
        "v_mfma_f32_16x16x32_bf16 v[112:115], v[222:225], v[108:111], v[112:115]\n"
        "v_exp_f16_sdwa v146, v146 dst_sel:WORD_0 dst_unused:UNUSED_PRESERVE src0_sel:WORD_0\n"
        "v_mfma_f32_16x16x32_bf16 v[104:107], v[198:201], v[108:111], 0\n"
        "v_mov_b64 v[122:123], 0\n"
        "v_cvt_pk_f16_f32 v168, v116, v117\n"
        "v_mfma_f32_16x16x32_bf16 v[104:107], v[214:217], v[126:129], v[104:107]\n"
        "v_exp_f16_sdwa v147, v147 dst_sel:WORD_0 dst_unused:UNUSED_PRESERVE src0_sel:WORD_0\n"
        "v_mfma_f32_16x16x32_bf16 v[104:107], v[202:205], v[40:43], v[104:107]\n"
        "v_exp_f16_sdwa v146, v146 dst_sel:WORD_1 dst_unused:UNUSED_PRESERVE src0_sel:WORD_1\n"
        "v_mfma_f32_16x16x32_bf16 v[40:43], v[202:205], a[236:239], 0\n"
        "v_exp_f16_sdwa v147, v147 dst_sel:WORD_1 dst_unused:UNUSED_PRESERVE src0_sel:WORD_1\n"
        "v_mfma_f32_16x16x32_bf16 v[40:43], v[214:217], a[232:235], v[40:43]\n"
        "v_pk_add_f16 v121, v121, v146\n"
        "v_pk_add_f16 v121, v121, v147\n"
        "v_mfma_f32_16x16x32_bf16 v[40:43], v[198:201], a[228:231], v[40:43]\n"
        "v_pk_add_f16 v121, v121, v148\n"
        "v_cvt_pk_f16_f32 v169, v118, v119\n"
        "v_mfma_f32_16x16x32_bf16 v[124:127], v[222:225], a[228:231], 0\n"
        "v_cvt_pk_f16_f32 v178, v130, v131\n"
        "v_cvt_pk_f16_f32 v179, v132, v133\n"
        "v_mfma_f32_16x16x32_bf16 v[124:127], v[226:229], a[232:235], v[124:127]\n"
        "v_cvt_pk_f16_f32 v234, v34, v35\n"
        "v_cvt_pk_f16_f32 v235, v36, v37\n"
        "v_mfma_f32_16x16x32_bf16 v[124:127], v[218:221], a[236:239], v[124:127]\n"
        "v_pk_mul_f16 v234, v234, v171 op_sel_hi:[1,0]\n"
        "v_pk_mul_f16 v235, v235, v171 op_sel_hi:[1,0]\n"
        "v_mfma_f32_16x16x32_bf16 v[116:119], v[210:213], a[236:239], 0\n"
        "v_pk_mul_f16 v234, v234, v230\n"
        "v_pk_mul_f16 v235, v235, v231\n"
        "v_mfma_f32_16x16x32_bf16 v[116:119], v[206:209], a[232:235], v[116:119]\n"
        "v_exp_f16_sdwa v234, v234 dst_sel:WORD_0 dst_unused:UNUSED_PRESERVE src0_sel:WORD_0\n"
        "v_mfma_f32_16x16x32_bf16 v[116:119], v[194:197], a[228:231], v[116:119]\n"
        "v_exp_f16_sdwa v235, v235 dst_sel:WORD_0 dst_unused:UNUSED_PRESERVE src0_sel:WORD_0\n"
        "v_mfma_f32_16x16x32_bf16 v[34:37], v[194:197], a[240:243], 0\n"
        "v_exp_f16_sdwa v234, v234 dst_sel:WORD_1 dst_unused:UNUSED_PRESERVE src0_sel:WORD_1\n"
        "v_mfma_f32_16x16x32_bf16 v[34:37], v[206:209], a[244:247], v[34:37]\n"
        "v_exp_f16_sdwa v235, v235 dst_sel:WORD_1 dst_unused:UNUSED_PRESERVE src0_sel:WORD_1\n"
        "v_mfma_f32_16x16x32_bf16 v[34:37], v[210:213], a[248:251], v[34:37]\n"
        "v_cvt_pk_f16_f32 v236, v112, v113\n"
        "v_cvt_pk_f16_f32 v237, v114, v115\n"
        "v_mfma_f32_16x16x32_bf16 v[132:135], v[218:221], a[248:251], 0\n"
        "v_pk_mul_f16 v236, v236, v171 op_sel_hi:[1,0]\n"
        "v_pk_mul_f16 v237, v237, v171 op_sel_hi:[1,0]\n"
        "v_mfma_f32_16x16x32_bf16 v[132:135], v[226:229], a[244:247], v[132:135]\n"
        "v_pk_mul_f16 v236, v236, v232\n"
        "v_pk_mul_f16 v237, v237, v233\n"
        "v_mfma_f32_16x16x32_bf16 v[132:135], v[222:225], a[240:243], v[132:135]\n"
        "v_exp_f16_sdwa v236, v236 dst_sel:WORD_0 dst_unused:UNUSED_PRESERVE src0_sel:WORD_0\n"
        "v_mfma_f32_16x16x32_bf16 v[112:115], v[198:201], a[240:243], 0\n"
        "v_exp_f16_sdwa v237, v237 dst_sel:WORD_0 dst_unused:UNUSED_PRESERVE src0_sel:WORD_0\n"
        "v_mfma_f32_16x16x32_bf16 v[112:115], v[214:217], a[244:247], v[112:115]\n"
        "v_exp_f16_sdwa v236, v236 dst_sel:WORD_1 dst_unused:UNUSED_PRESERVE src0_sel:WORD_1\n"
        "v_mfma_f32_16x16x32_bf16 v[112:115], v[202:205], a[248:251], v[112:115]\n"
        "v_exp_f16_sdwa v237, v237 dst_sel:WORD_1 dst_unused:UNUSED_PRESERVE src0_sel:WORD_1\n"
        "v_mfma_f32_16x16x32_f16 v[128:131], v[52:55], v[120:123], 0\n"
        "v_cvt_pk_f16_f32 v248, v104, v105\n"
        "v_cvt_pk_f16_f32 v249, v106, v107\n"
        "v_mfma_f32_16x16x32_bf16 v[158:161], v[202:205], v[96:99], 0\n"
        "v_pk_mul_f16 v248, v248, v171 op_sel_hi:[1,0]\n"
        "v_pk_mul_f16 v249, v249, v171 op_sel_hi:[1,0]\n"
        "v_mfma_f32_16x16x32_bf16 v[158:161], v[214:217], v[76:79], v[158:161]\n"
        "v_pk_mul_f16 v248, v248, v238\n"
        "v_pk_mul_f16 v249, v249, v239\n"
        "v_mfma_f32_16x16x32_bf16 v[158:161], v[198:201], a[252:255], v[158:161]\n"
        "v_exp_f16_sdwa v248, v248 dst_sel:WORD_0 dst_unused:UNUSED_PRESERVE src0_sel:WORD_0\n"
        "v_mfma_f32_16x16x32_bf16 v[240:243], v[222:225], a[252:255], 0\n"
        "v_cndmask_b32_e64 v250, 0, v253, s[66:67]\n"
        "v_cvt_pk_f16_f32 v182, v40, v41\n"
        "v_mfma_f32_16x16x32_bf16 v[240:243], v[226:229], v[76:79], v[240:243]\n"
        "ds_read2_b32 v[180:181], v24 offset0:0 offset1:0\n"
        "ds_read2_b32 v[184:185], v24 offset0:1 offset1:1\n"
        "v_mfma_f32_16x16x32_bf16 v[240:243], v[218:221], v[96:99], v[240:243]\n"
        "ds_read2_b32 v[164:165], v24 offset0:2 offset1:2\n"
        "ds_read2_b32 v[188:189], v24 offset0:3 offset1:3\n"
        "v_mfma_f32_16x16x32_bf16 v[244:247], v[210:213], v[96:99], 0\n"
        "ds_read2_b32 v[176:177], v24 offset0:4 offset1:4\n"
        "v_mov_b32 v153, 0\n"
        "v_mfma_f32_16x16x32_bf16 v[244:247], v[206:209], v[76:79], v[244:247]\n"
        "ds_read2_b32 v[172:173], v24 offset0:5 offset1:5\n"
        "v_cvt_pk_f16_f32 v183, v42, v43\n"
        "v_mfma_f32_16x16x32_bf16 v[244:247], v[194:197], a[252:255], v[244:247]\n"
        "v_exp_f16_sdwa v249, v249 dst_sel:WORD_0 dst_unused:UNUSED_PRESERVE src0_sel:WORD_0\n"
        "v_mfma_f32_16x16x32_bf16 v[108:111], v[194:197], v[64:67], 0\n"
        "v_exp_f16_sdwa v248, v248 dst_sel:WORD_1 dst_unused:UNUSED_PRESERVE src0_sel:WORD_1\n"
        "v_mfma_f32_16x16x32_bf16 v[108:111], v[206:209], v[60:63], v[108:111]\n"
        "v_exp_f16_sdwa v249, v249 dst_sel:WORD_1 dst_unused:UNUSED_PRESERVE src0_sel:WORD_1\n"
        "v_mfma_f32_16x16x32_bf16 v[108:111], v[210:213], v[72:75], v[108:111]\n"
        "v_cvt_pk_f16_f32 v144, v124, v125\n"
        "v_cvt_pk_f16_f32 v145, v126, v127\n"
        "v_mfma_f32_16x16x32_bf16 v[40:43], v[218:221], v[72:75], 0\n"
        "v_cvt_pk_f16_f32 v142, v116, v117\n"
        "v_cvt_pk_f16_f32 v143, v118, v119\n"
        "v_mfma_f32_16x16x32_bf16 v[40:43], v[226:229], v[60:63], v[40:43]\n"
        "v_cvt_pk_f16_f32 v190, v34, v35\n"
        "v_cvt_pk_f16_f32 v191, v36, v37\n"
        "v_mfma_f32_16x16x32_bf16 v[40:43], v[222:225], v[64:67], v[40:43]\n"
        "v_cvt_pk_f16_f32 v192, v132, v133\n"
        "v_cvt_pk_f16_f32 v193, v134, v135\n"
        "v_mfma_f32_16x16x32_bf16 v[104:107], v[198:201], v[64:67], 0\n"
        "s_waitcnt lgkmcnt(0)\n"
        "v_pk_add_f16 v122, v234, v235\n"
        "v_pk_add_f16 v122, v122, v236\n"
        "v_mfma_f32_16x16x32_bf16 v[104:107], v[214:217], v[60:63], v[104:107]\n"
        "v_pk_add_f16 v122, v122, v237\n"
        "v_pk_add_f16 v122, v122, v248\n"
        "v_mfma_f32_16x16x32_bf16 v[104:107], v[202:205], v[72:75], v[104:107]\n"
        "v_cvt_pk_f16_f32 v162, v112, v113\n"
        "v_cvt_pk_f16_f32 v163, v114, v115\n"
        "v_mfma_f32_16x16x32_bf16 v[116:119], v[202:205], v[88:91], 0\n"
        "v_pk_add_f16 v122, v122, v249\n"
        "v_pk_add_f16 v122, v122, v250\n"
        "v_mfma_f32_16x16x32_bf16 v[116:119], v[214:217], v[80:83], v[116:119]\n"
        "v_rcp_f32 v254, v128\n"
        "v_mfma_f32_16x16x32_bf16 v[116:119], v[198:201], v[92:95], v[116:119]\n"
        "v_cvt_f16_f32 v255, v254\n"
        "v_pk_mul_f16 v138, v138, v255 op_sel_hi:[1,0]\n"
        "v_mfma_f32_16x16x32_bf16 v[124:127], v[222:225], v[92:95], 0\n"
        "v_pk_mul_f16 v139, v139, v255 op_sel_hi:[1,0]\n"
        "v_pk_mul_f16 v140, v140, v255 op_sel_hi:[1,0]\n"
        "v_mfma_f32_16x16x32_bf16 v[124:127], v[226:229], v[80:83], v[124:127]\n"
        "v_pk_mul_f16 v141, v141, v255 op_sel_hi:[1,0]\n"
        "v_pk_mul_f16 v150, v150, v255 op_sel_hi:[1,0]\n"
        "v_mfma_f32_16x16x32_bf16 v[124:127], v[218:221], v[88:91], v[124:127]\n"
        "v_pk_mul_f16 v151, v151, v255 op_sel_hi:[1,0]\n"
        "v_pk_mul_f16 v152, v152, v255 op_sel_hi:[1,0]\n"
        "v_mfma_f32_16x16x32_bf16 v[112:115], v[210:213], v[88:91], 0\n"
        "v_mov_b32 v149, 0\n"
        "v_pk_mul_f16 v138, v138, v230\n"
        "v_mfma_f32_16x16x32_bf16 v[112:115], v[206:209], v[80:83], v[112:115]\n"
        "v_pk_mul_f16 v139, v139, v231\n"
        "v_pk_mul_f16 v140, v140, v232\n"
        "v_mfma_f32_16x16x32_bf16 v[112:115], v[194:197], v[92:95], v[112:115]\n"
        "v_pk_mul_f16 v141, v141, v233\n"
        "v_pk_mul_f16 v150, v150, v238\n"
        "v_mfma_f32_16x16x32_f16 v[134:137], v[52:55], v[120:123], 0\n"
        "v_pk_mul_f16 v151, v151, v239\n"
        "v_max3_f32 v255, s95, v128, v129\n"
        "v_mfma_f32_16x16x32_f16 v[120:123], v[138:141], v[166:169], 0\n"
        "v_rcp_f32 v251, v129\n"
        "v_mfma_f32_16x16x32_f16 v[120:123], v[150:153], v[178:181], v[120:123]\n"
        "v_add_f32 v253, v252, v254\n"
        "v_cvt_f16_f32 v254, v251\n"
        "v_mfma_f32_16x16x32_bf16 v[120:123], v[194:197], v[68:71], v[120:123]\n"
        "v_add_f32 v253, v253, v251\n"
        "v_pk_mul_f16 v154, v154, v254 op_sel_hi:[1,0]\n"
        "v_mfma_f32_16x16x32_bf16 v[132:135], v[194:197], v[100:103], 0\n"
        "v_pk_mul_f16 v155, v155, v254 op_sel_hi:[1,0]\n"
        "v_pk_mul_f16 v156, v156, v254 op_sel_hi:[1,0]\n"
        "v_mfma_f32_16x16x32_f16 v[132:135], v[150:153], v[182:185], v[132:135]\n"
        "v_pk_mul_f16 v157, v157, v254 op_sel_hi:[1,0]\n"
        "v_pk_mul_f16 v146, v146, v254 op_sel_hi:[1,0]\n"
        "v_mfma_f32_16x16x32_f16 v[132:135], v[138:141], v[142:145], v[132:135]\n"
        "v_pk_mul_f16 v147, v147, v254 op_sel_hi:[1,0]\n"
        "v_pk_mul_f16 v148, v148, v254 op_sel_hi:[1,0]\n"
        "v_mfma_f32_16x16x32_f16 v[128:131], v[138:141], v[190:193], 0\n"
        "v_cvt_pk_f16_f32 v186, v158, v159\n"
        "v_cvt_pk_f16_f32 v187, v160, v161\n"
        "v_mfma_f32_16x16x32_f16 v[128:131], v[150:153], v[162:165], v[128:131]\n"
        "v_cvt_pk_f16_f32 v36, v240, v241\n"
        "v_cvt_pk_f16_f32 v37, v242, v243\n"
        "v_mfma_f32_16x16x32_bf16 v[128:131], v[206:209], v[68:71], v[128:131]\n"
        "v_cvt_pk_f16_f32 v34, v244, v245\n"
        "v_cvt_pk_f16_f32 v35, v246, v247\n"
        "v_mfma_f32_16x16x32_bf16 v[158:161], v[206:209], v[100:103], 0\n"
        "v_cvt_pk_f16_f32 v244, v108, v109\n"
        "v_cvt_pk_f16_f32 v245, v110, v111\n"
        "v_mfma_f32_16x16x32_f16 v[158:161], v[150:153], v[186:189], v[158:161]\n"
        "v_cvt_pk_f16_f32 v246, v40, v41\n"
        "v_cvt_pk_f16_f32 v247, v42, v43\n"
        "v_mfma_f32_16x16x32_f16 v[158:161], v[138:141], v[34:37], v[158:161]\n"
        "v_cvt_pk_f16_f32 v174, v104, v105\n"
        "v_cvt_pk_f16_f32 v175, v106, v107\n"
        "v_mfma_f32_16x16x32_f16 v[40:43], v[138:141], v[244:247], 0\n"
        "v_pk_mul_f16 v154, v154, v230\n"
        "v_pk_mul_f16 v155, v155, v231\n"
        "v_mfma_f32_16x16x32_f16 v[40:43], v[150:153], v[174:177], v[40:43]\n"
        "v_cvt_pk_f16_f32 v170, v116, v117\n"
        "v_cvt_pk_f16_f32 v171, v118, v119\n"
        "v_mfma_f32_16x16x32_bf16 v[40:43], v[210:213], v[68:71], v[40:43]\n"
        "v_cvt_pk_f16_f32 v242, v124, v125\n"
        "v_cvt_pk_f16_f32 v243, v126, v127\n"
        "v_mfma_f32_16x16x32_bf16 v[124:127], v[210:213], v[100:103], 0\n"
        "v_cvt_pk_f16_f32 v240, v112, v113\n"
        "v_cvt_pk_f16_f32 v241, v114, v115\n"
        "v_mfma_f32_16x16x32_f16 v[124:127], v[150:153], v[170:173], v[124:127]\n"
        "v_pk_mul_f16 v156, v156, v232\n"
        "v_pk_mul_f16 v157, v157, v233\n"
        "v_mfma_f32_16x16x32_f16 v[124:127], v[138:141], v[240:243], v[124:127]\n"
        "s_add_u32 s90, s89, 0\n"
        "v_pk_mul_f16 v146, v146, v238\n"
        "v_pk_mul_f16 v147, v147, v239\n"
        "v_mfma_f32_16x16x32_f16 v[116:119], v[154:157], v[166:169], 0\n"
        "v_rcp_f32 v254, v136\n"
        "v_mfma_f32_16x16x32_f16 v[116:119], v[146:149], v[178:181], v[116:119]\n"
        "v_max_f32 v255, v255, v136\n"
        "v_add_f32 v253, v253, v254\n"
        "v_mfma_f32_16x16x32_bf16 v[116:119], v[222:225], v[68:71], v[116:119]\n"
        "v_fma_f32 v255, v253, s82, v255\n"
        "v_cmp_nlt_f32_e64 s[74:75], v255, s83\n"
        "v_mfma_f32_16x16x32_bf16 v[108:111], v[222:225], v[100:103], 0\n"
        "v_cvt_pk_bf16_f32 v104, v120, v132\n"
        "v_cvt_pk_bf16_f32 v105, v128, v158\n"
        "v_mfma_f32_16x16x32_f16 v[108:111], v[146:149], v[182:185], v[108:111]\n"
        "s_cmp_lg_u64 s[74:75], 0\n"
        "v_cvt_pk_bf16_f32 v106, v40, v124\n"
        "v_mov_b32 v251, 0\n"
        "v_mfma_f32_16x16x32_f16 v[108:111], v[154:157], v[142:145], v[108:111]\n"
        "s_cbranch_scc1 SLOW_%=\n"
        "v_cvt_pk_bf16_f32 v136, v121, v133\n"
        "v_cvt_pk_bf16_f32 v137, v129, v159\n"
        "v_mfma_f32_16x16x32_f16 v[112:115], v[154:157], v[190:193], 0\n"
        "s_add_u32 s91, s89, 98112\n"
        "v_cvt_pk_bf16_f32 v138, v41, v125\n"
        "v_cvt_f16_f32 v255, v254\n"
        "v_mfma_f32_16x16x32_f16 v[112:115], v[146:149], v[162:165], v[112:115]\n"
        "v_cvt_pk_bf16_f32 v128, v122, v134\n"
        "v_cvt_pk_bf16_f32 v129, v130, v160\n"
        "v_mfma_f32_16x16x32_bf16 v[112:115], v[226:229], v[68:71], v[112:115]\n"
        "buffer_store_dwordx3 v[104:106], v14, s[40:43], s90 offen sc1 nt\n"
        "v_cvt_pk_bf16_f32 v130, v42, v126\n"
        "v_pk_mul_f16 v234, v234, v255 op_sel_hi:[1,0]\n"
        "v_cvt_pk_bf16_f32 v40, v123, v135\n"
        "v_cvt_pk_bf16_f32 v41, v131, v161\n"
        "v_mfma_f32_16x16x32_bf16 v[104:107], v[226:229], v[100:103], 0\n"
        "v_cvt_pk_bf16_f32 v42, v43, v127\n"
        "v_pk_mul_f16 v235, v235, v255 op_sel_hi:[1,0]\n"
        "v_mfma_f32_16x16x32_f16 v[104:107], v[146:149], v[186:189], v[104:107]\n"
        "v_pk_mul_f16 v236, v236, v255 op_sel_hi:[1,0]\n"
        "v_pk_mul_f16 v237, v237, v255 op_sel_hi:[1,0]\n"
        "v_mfma_f32_16x16x32_f16 v[104:107], v[154:157], v[34:37], v[104:107]\n"
        "buffer_store_dwordx3 v[136:138], v15, s[40:43], s90 offen offset:768 sc1 nt\n"
        "v_pk_mul_f16 v248, v248, v255 op_sel_hi:[1,0]\n"
        "v_pk_mul_f16 v249, v249, v255 op_sel_hi:[1,0]\n"
        "v_pk_mul_f16 v250, v250, v255 op_sel_hi:[1,0]\n"
        "v_pk_mul_f16 v234, v234, v230\n"
        "v_mfma_f32_16x16x32_f16 v[120:123], v[154:157], v[244:247], 0\n"
        "v_pk_mul_f16 v235, v235, v231\n"
        "v_pk_mul_f16 v236, v236, v232\n"
        "v_mfma_f32_16x16x32_f16 v[120:123], v[146:149], v[174:177], v[120:123]\n"
        "v_pk_mul_f16 v237, v237, v233\n"
        "v_pk_mul_f16 v248, v248, v238\n"
        "v_mfma_f32_16x16x32_bf16 v[120:123], v[218:221], v[68:71], v[120:123]\n"
        "buffer_store_dwordx3 v[128:130], v14, s[40:43], s91 offen offset:192 sc1 nt\n"
        "v_pk_mul_f16 v249, v249, v239\n"
        "v_cvt_pk_bf16_f32 v150, v116, v108\n"
        "v_cvt_pk_bf16_f32 v206, v117, v109\n"
        "v_cvt_pk_bf16_f32 v222, v118, v110\n"
        "v_mfma_f32_16x16x32_bf16 v[128:131], v[218:221], v[100:103], 0\n"
        "v_cvt_pk_bf16_f32 v151, v112, v104\n"
        "v_cvt_pk_bf16_f32 v207, v113, v105\n"
        "v_mfma_f32_16x16x32_f16 v[128:131], v[146:149], v[170:173], v[128:131]\n"
        "v_cvt_pk_bf16_f32 v223, v114, v106\n"
        "v_cvt_pk_bf16_f32 v210, v119, v111\n"
        "v_mfma_f32_16x16x32_f16 v[128:131], v[154:157], v[240:243], v[128:131]\n"
        "buffer_store_dwordx3 v[40:42], v16, s[40:43], s91 offen offset:960 sc1 nt\n"
        "v_cvt_pk_bf16_f32 v211, v115, v107\n"
        "s_nop 0\n"
        "v_mfma_f32_16x16x32_f16 v[40:43], v[234:237], v[166:169], 0\n"
        "s_nop 1\n"
        "v_mfma_f32_16x16x32_f16 v[40:43], v[248:251], v[178:181], v[40:43]\n"
        "s_waitcnt vmcnt(4)\n"
        "v_cvt_pk_bf16_f32 v152, v120, v128\n"
        "v_cvt_pk_bf16_f32 v208, v121, v129\n"
        "v_mfma_f32_16x16x32_bf16 v[40:43], v[198:201], v[68:71], v[40:43]\n"
        "s_add_u32 s90, s89, 196224\n"
        "ds_read_b128 v[126:129], v9\n"
        "v_cvt_pk_bf16_f32 v224, v122, v130\n"
        "v_mfma_f32_16x16x32_bf16 v[146:149], v[198:201], v[100:103], 0\n"
        "ds_read_b128 v[132:135], v10\n"
        "v_cvt_pk_bf16_f32 v212, v123, v131\n"
        "v_mfma_f32_16x16x32_f16 v[146:149], v[248:251], v[182:185], v[146:149]\n"
        "ds_read_b128 v[138:141], v11\n"
        "v_mfma_f32_16x16x32_f16 v[146:149], v[234:237], v[142:145], v[146:149]\n"
        "s_add_u32 s91, s89, 294336\n"
        "ds_read_b128 v[106:109], v9 offset:3072\n"
        "v_mfma_f32_16x16x32_f16 v[122:125], v[234:237], v[190:193], 0\n"
        "s_nop 0\n"
        "ds_read_b128 v[112:115], v10 offset:3072\n"
        "v_mfma_f32_16x16x32_f16 v[122:125], v[248:251], v[162:165], v[122:125]\n"
        "s_nop 0\n"
        "ds_read_b128 v[118:121], v11 offset:3072\n"
        "v_cvt_pk_bf16_f32 v228, v40, v146\n"
        "v_mfma_f32_16x16x32_bf16 v[122:125], v[214:217], v[68:71], v[122:125]\n"
        "buffer_store_dwordx3 v[150:152], v14, s[40:43], s90 offen offset:384 sc1 nt\n"
        "v_cvt_pk_bf16_f32 v252, v41, v147\n"
        "v_cvt_pk_bf16_f32 v232, v42, v148\n"
        "v_cvt_pk_bf16_f32 v40, v43, v149\n"
        "v_mfma_f32_16x16x32_bf16 v[194:197], v[214:217], v[100:103], 0\n"
        "ds_read_b128 v[146:149], v9 offset:6144\n"
        "ds_read_b128 v[152:155], v10 offset:6144\n"
        "v_mfma_f32_16x16x32_f16 v[194:197], v[248:251], v[186:189], v[194:197]\n"
        "ds_read_b128 v[158:161], v11 offset:6144\n"
        "v_mfma_f32_16x16x32_f16 v[194:197], v[234:237], v[34:37], v[194:197]\n"
        "buffer_store_dwordx3 v[206:208], v17, s[40:43], s90 offen offset:1152 sc1 nt\n"
        "s_add_u32 s90, s89, 392448\n"
        "s_nop 0\n"
        "v_mfma_f32_16x16x32_f16 v[206:209], v[234:237], v[244:247], 0\n"
        "s_waitcnt lgkmcnt(6)\n"
        "s_nop 0\n"
        "v_mfma_f32_16x16x32_f16 v[206:209], v[248:251], v[174:177], v[206:209]\n"
        "s_nop 0\n"
        "v_cvt_pk_bf16_f32 v229, v122, v194\n"
        "v_cvt_pk_bf16_f32 v253, v123, v195\n"
        "v_mfma_f32_16x16x32_bf16 v[206:209], v[202:205], v[68:71], v[206:209]\n"
        "buffer_store_dwordx3 v[222:224], v14, s[40:43], s91 offen offset:576 sc1 nt\n"
        "v_cvt_pk_bf16_f32 v233, v124, v196\n"
        "v_cvt_pk_bf16_f32 v41, v125, v197\n"
        "v_mfma_f32_16x16x32_bf16 v[194:197], v[202:205], v[100:103], 0\n"
        "s_cmp_ge_i32 s85, 0\n"
        "v_mfma_f32_16x16x32_f16 v[194:197], v[248:251], v[170:173], v[194:197]\n"
        "v_mfma_f32_16x16x32_f16 v[194:197], v[234:237], v[240:243], v[194:197]\n"
        "buffer_store_dwordx3 v[210:212], v14, s[40:43], s90 offen sc1 nt\n"
        "s_cbranch_scc1 TOP_%=\n"
        "LAST_%=:\n"
        "s_waitcnt lgkmcnt(0)\n"
        "s_nop 7\n"
        "s_mov_b32 s26, s89\n"
        "s_add_u32 s27, s26, 392448\n"
        "v_cvt_pk_bf16_f32 v230, v206, v194\n"
        "buffer_store_dwordx3 v[228:230], v15, s[40:43], s27 offen offset:768 sc1 nt\n"
        "v_cvt_pk_bf16_f32 v254, v207, v195\n"
        "s_add_u32 s28, s26, 490560\n"
        "buffer_store_dwordx3 v[252:254], v14, s[40:43], s28 offen offset:192 sc1 nt\n"
        "v_cvt_pk_bf16_f32 v234, v208, v196\n"
        "buffer_store_dwordx3 v[232:234], v16, s[40:43], s28 offen offset:960 sc1 nt\n"
        "v_cvt_pk_bf16_f32 v42, v209, v197\n"
        "s_add_u32 s27, s26, 588672\n"
        "buffer_store_dwordx3 v[40:42], v14, s[40:43], s27 offen offset:384 sc1 nt\n"
        "ds_read_u16 v202, v19 offset:2048\n"
        "s_waitcnt lgkmcnt(0)\n"
        "s_mov_b32 s30, s29\n"
        "v_cmp_ne_u32_e64 s[24:25], s30, v202\n"
        "s_nop 3\n"
        "s_cmp_lg_u64 s[24:25], 0\n"
        "s_cbranch_scc1 SP20_%=\n"
        "BK20_%=:\n"
        "ds_read_b128 v[194:197], v13\n"
        "ds_read_b128 v[206:209], v13 offset:1024\n"
        "ds_read_b128 v[210:213], v13 offset:2048\n"
        "ds_read_b128 v[222:225], v13 offset:3072\n"
        "ds_read_b128 v[226:229], v13 offset:4096\n"
        "ds_read_b128 v[218:221], v13 offset:5120\n"
        "ds_read_b128 v[198:201], v13 offset:6144\n"
        "ds_read_b128 v[214:217], v13 offset:7168\n"
        "ds_read_b128 v[202:205], v13 offset:8192\n"
        "ds_read_b128 v[40:43], v20\n"
        "ds_read_b64 v[30:31], v20 offset:16\n"
        "ds_read_u16 v108, v22\n"
        "ds_read_b64 v[146:147], v21\n"
        "v_mov_b32 v26, 0\n"
        "s_mov_b64 exec, 1\n"
        "buffer_atomic_add v26, off, s[16:19], s15 offset:4 sc0\n"
        "s_mov_b64 exec, -1\n"
        "s_waitcnt lgkmcnt(10)\n"
        "v_mfma_f32_16x16x32_bf16 v[230:233], a[152:155], v[210:213], 0\n"
        "s_add_u32 s89, s86, s51\n"
        "ds_read_b128 v[126:129], v20 offset:512\n"
        "v_add_u32 v23, s22, v23\n"
        "v_mfma_f32_16x16x32_bf16 v[230:233], a[148:151], v[206:209], v[230:233]\n"
        "s_add_u32 s29, s29, 1\n"
        "ds_read_b128 v[248:251], v20 offset:528\n"
        "v_add_u32 v24, s22, v24\n"
        "v_mfma_f32_16x16x32_bf16 v[230:233], a[144:147], v[194:197], v[230:233]\n"
        "s_mov_b32 s22, s23\n"
        "ds_read_b128 v[112:115], v23\n"
        "v_lshlrev_b32 v252, 16, v180\n"
        "v_mfma_f32_16x16x32_bf16 v[130:133], a[156:159], v[194:197], 0\n"
        "s_movk_i32 s23, 1584\n"
        "ds_read_b128 v[148:151], v23 offset:64\n"
        "v_lshlrev_b32 v253, 16, v184\n"
        "v_mfma_f32_16x16x32_bf16 v[130:133], a[160:163], v[206:209], v[130:133]\n"
        "s_mov_b32 s86, s87\n"
        "ds_read_b128 v[158:161], v23 offset:128\n"
        "v_lshlrev_b32 v254, 16, v164\n"
        "v_mfma_f32_16x16x32_bf16 v[130:133], a[164:167], v[210:213], v[130:133]\n"
        "s_add_u32 s88, s88, 32\n"
        "ds_read_b64 v[236:237], v24 offset:360\n"
        "v_lshlrev_b32 v255, 16, v188\n"
        "v_mfma_f32_16x16x32_bf16 v[152:155], a[176:179], v[210:213], 0\n"
        "s_add_u32 s87, s87, 43008\n"
        "v_lshlrev_b32 v106, 16, v176\n"
        "v_lshlrev_b32 v107, 16, v172\n"
        "v_mfma_f32_16x16x32_bf16 v[152:155], a[172:175], v[206:209], v[152:155]\n"
        "s_cmp_ge_u32 s88, 73\n"
        "v_cvt_pk_bf16_f32 v120, v230, v231\n"
        "v_cvt_pk_bf16_f32 v121, v232, v233\n"
        "v_mfma_f32_16x16x32_bf16 v[152:155], a[168:171], v[194:197], v[152:155]\n"
        "s_cselect_b32 s92, 588672, 0\n"
        "v_cvt_pk_bf16_f32 v122, v130, v131\n"
        "v_cvt_pk_bf16_f32 v123, v132, v133\n"
        "v_mfma_f32_16x16x32_bf16 v[130:133], a[180:183], v[194:197], 0\n"
        "s_waitcnt lgkmcnt(4)\n"
        "v_pk_mul_f16 v230, v126, v166\n"
        "v_pk_mul_f16 v231, v126, v142\n"
        "v_mfma_f32_16x16x32_bf16 v[130:133], a[184:187], v[206:209], v[130:133]\n"
        "s_cselect_b32 s94, 73, 0\n"
        "v_pk_mul_f16 v232, v126, v190\n"
        "v_pk_mul_f16 v233, v126, v34\n"
        "v_mfma_f32_16x16x32_bf16 v[130:133], a[188:191], v[210:213], v[130:133]\n"
        "s_sub_u32 s88, s88, s94\n"
        "v_pk_mul_f16 v234, v126, v244\n"
        "v_pk_mul_f16 v235, v126, v240\n"
        "v_mfma_f32_16x16x32_bf16 v[116:119], a[200:203], v[210:213], 0\n"
        "s_add_u32 s87, s87, s92\n"
        "v_pk_fma_f16 v230, v127, v167, v230\n"
        "v_pk_fma_f16 v231, v127, v143, v231\n"
        "v_mfma_f32_16x16x32_bf16 v[116:119], a[196:199], v[206:209], v[116:119]\n"
        "s_waitcnt lgkmcnt(0)\n"
        "v_pk_fma_f16 v232, v127, v191, v232\n"
        "v_pk_fma_f16 v233, v127, v35, v233\n"
        "v_mfma_f32_16x16x32_bf16 v[116:119], a[192:195], v[194:197], v[116:119]\n"
        "s_cmp_gt_i32 s85, 0\n"
        "v_pk_fma_f16 v234, v127, v245, v234\n"
        "v_pk_fma_f16 v235, v127, v241, v235\n"
        "v_mfma_f32_16x16x32_bf16 v[124:127], a[204:207], v[194:197], 0\n"
        "s_cselect_b32 s94, s87, s84\n"
        "v_readlane_b32 s93, v237, 1\n"
        "v_cndmask_b32_e64 v236, 0, v236, s[72:73]\n"
        "v_mfma_f32_16x16x32_bf16 v[124:127], a[208:211], v[206:209], v[124:127]\n"
        "s_sub_u32 s85, s85, 1\n"
        "v_pk_fma_f16 v230, v128, v168, v230\n"
        "v_pk_fma_f16 v231, v128, v144, v231\n"
        "v_mfma_f32_16x16x32_bf16 v[124:127], a[212:215], v[210:213], v[124:127]\n"
        "s_waitcnt lgkmcnt(0)\n"
        "v_pk_fma_f16 v232, v128, v192, v232\n"
        "v_pk_fma_f16 v233, v128, v36, v233\n"
        "v_mfma_f32_16x16x32_bf16 v[136:139], v[112:115], v[194:197], 0\n"
        "s_waitcnt lgkmcnt(0)\n"
        "v_pk_fma_f16 v234, v128, v246, v234\n"
        "v_pk_fma_f16 v235, v128, v242, v235\n"
        "v_mfma_f32_16x16x32_bf16 v[136:139], v[148:151], v[206:209], v[136:139]\n"
        "v_pk_fma_f16 v230, v129, v169, v230\n"
        "v_pk_fma_f16 v231, v129, v145, v231\n"
        "v_mfma_f32_16x16x32_bf16 v[136:139], v[158:161], v[210:213], v[136:139]\n"
        "v_pk_fma_f16 v232, v129, v193, v232\n"
        "v_pk_fma_f16 v233, v129, v37, v233\n"
        "v_mfma_f32_16x16x32_bf16 v[190:193], v[158:161], v[218:221], 0\n"
        "v_pk_fma_f16 v234, v129, v247, v234\n"
        "v_pk_fma_f16 v235, v129, v243, v235\n"
        "v_mfma_f32_16x16x32_bf16 v[190:193], v[148:151], v[226:229], v[190:193]\n"
        "v_pk_fma_f16 v230, v248, v178, v230\n"
        "v_pk_fma_f16 v231, v248, v182, v231\n"
        "v_mfma_f32_16x16x32_bf16 v[190:193], v[112:115], v[222:225], v[190:193]\n"
        "v_pk_fma_f16 v232, v248, v162, v232\n"
        "v_pk_fma_f16 v233, v248, v186, v233\n"
        "v_mfma_f32_16x16x32_bf16 v[244:247], v[112:115], v[198:201], 0\n"
        "v_pk_fma_f16 v234, v248, v174, v234\n"
        "v_pk_fma_f16 v235, v248, v170, v235\n"
        "v_mfma_f32_16x16x32_bf16 v[244:247], v[148:151], v[214:217], v[244:247]\n"
        "v_pk_fma_f16 v230, v249, v179, v230\n"
        "v_pk_fma_f16 v231, v249, v183, v231\n"
        "v_mfma_f32_16x16x32_bf16 v[244:247], v[158:161], v[202:205], v[244:247]\n"
        "v_pk_fma_f16 v232, v249, v163, v232\n"
        "v_pk_fma_f16 v233, v249, v187, v233\n"
        "v_mfma_f32_16x16x32_bf16 v[166:169], v[194:197], v[120:123], 0\n"
        "v_pk_fma_f16 v234, v249, v175, v234\n"
        "v_pk_fma_f16 v235, v249, v171, v235\n"
        "v_cvt_pk_bf16_f32 v112, v152, v153\n"
        "v_cvt_pk_bf16_f32 v113, v154, v155\n"
        "v_cvt_pk_bf16_f32 v114, v130, v131\n"
        "v_cvt_pk_bf16_f32 v115, v132, v133\n"
        "s_nop 1\n"
        "v_mfma_f32_16x16x32_bf16 v[166:169], v[206:209], v[112:115], v[166:169]\n"
        "v_pk_fma_f16 v230, v250, v180, v230\n"
        "v_pk_fma_f16 v231, v250, v184, v231\n"
        "v_cvt_pk_bf16_f32 v34, v116, v117\n"
        "v_cvt_pk_bf16_f32 v35, v118, v119\n"
        "v_cvt_pk_bf16_f32 v36, v124, v125\n"
        "v_cvt_pk_bf16_f32 v37, v126, v127\n"
        "s_nop 1\n"
        "v_mfma_f32_16x16x32_bf16 v[166:169], v[210:213], v[34:37], v[166:169]\n"
        "v_pk_fma_f16 v232, v250, v164, v232\n"
        "v_pk_fma_f16 v233, v250, v188, v233\n"
        "v_mfma_f32_16x16x32_bf16 v[126:129], v[218:221], v[34:37], 0\n"
        "v_pk_fma_f16 v234, v250, v176, v234\n"
        "v_pk_fma_f16 v235, v250, v172, v235\n"
        "v_mfma_f32_16x16x32_bf16 v[126:129], v[226:229], v[112:115], v[126:129]\n"
        "v_cndmask_b32_e64 v247, v136, v190, s[64:65]\n"
        "v_cndmask_b32_e64 v180, v137, v191, s[64:65]\n"
        "v_mfma_f32_16x16x32_bf16 v[126:129], v[222:225], v[120:123], v[126:129]\n"
        "v_cndmask_b32_e64 v247, v247, v244, s[70:71]\n"
        "v_cndmask_b32_e64 v180, v180, v245, s[70:71]\n"
        "v_mfma_f32_16x16x32_bf16 v[186:189], v[198:201], v[120:123], 0\n"
        "v_cvt_pk_f16_f32 v247, v247, v180\n"
        "v_pk_mul_f16 v247, v247, v108 op_sel_hi:[1,0]\n"
        "v_mfma_f32_16x16x32_bf16 v[186:189], v[214:217], v[112:115], v[186:189]\n"
        "s_add_u32 s46, s26, 590016\n"
        "v_pk_mul_f16 v247, v247, s93 op_sel_hi:[1,0]\n"
        "v_cvt_pk_f16_f32 v246, v166, v167\n"
        "v_mfma_f32_16x16x32_bf16 v[186:189], v[202:205], v[34:37], v[186:189]\n"
        "v_exp_f16_sdwa v247, v247 dst_sel:WORD_0 dst_unused:UNUSED_PRESERVE src0_sel:WORD_0\n"
        "v_mfma_f32_16x16x32_f16 v[252:255], v[52:55], v[230:233], v[252:255]\n"
        "s_min_u32 s46, s46, s42\n"
        "v_exp_f16_sdwa v247, v247 dst_sel:WORD_1 dst_unused:UNUSED_PRESERVE src0_sel:WORD_1\n"
        "v_mfma_f32_16x16x32_f16 v[104:107], v[52:55], v[232:235], v[104:107]\n"
        "v_and_b32 v180, 0xffff0000, v247\n"
        "v_cvt_f32_f16 v179, v247\n"
        "v_mfma_f32_16x16x32_bf16 v[120:123], a[152:155], v[218:221], 0\n"
        "v_cndmask_b32_e64 v179, v179, v236, s[68:69]\n"
        "v_cvt_pk_f16_f32 v247, v168, v169\n"
        "v_mfma_f32_16x16x32_bf16 v[120:123], a[148:151], v[226:229], v[120:123]\n"
        "v_add_f32_dpp v184, v179, v179 row_ror:8 row_mask:0xf bank_mask:0xf\n"
        "v_pk_mul_f16 v246, v246, v146 op_sel_hi:[1,0]\n"
        "v_mfma_f32_16x16x32_bf16 v[120:123], a[144:147], v[222:225], v[120:123]\n"
        "v_pk_mul_f16 v247, v247, v146 op_sel_hi:[1,0]\n"
        "v_pk_mul_f16 v246, v246, v40\n"
        "v_mfma_f32_16x16x32_bf16 v[230:233], a[156:159], v[222:225], 0\n"
        "v_pk_mul_f16 v247, v247, v41\n"
        "v_add_f32_dpp v184, v184, v184 row_ror:4 row_mask:0xf bank_mask:0xf\n"
        "v_mfma_f32_16x16x32_bf16 v[230:233], a[160:163], v[226:229], v[230:233]\n"
        "v_exp_f16_sdwa v246, v246 dst_sel:WORD_0 dst_unused:UNUSED_PRESERVE src0_sel:WORD_0\n"
        "v_mfma_f32_16x16x32_bf16 v[230:233], a[164:167], v[218:221], v[230:233]\n"
        "v_exp_f16_sdwa v247, v247 dst_sel:WORD_0 dst_unused:UNUSED_PRESERVE src0_sel:WORD_0\n"
        "v_mfma_f32_16x16x32_bf16 v[164:167], a[176:179], v[218:221], 0\n"
        "v_exp_f16_sdwa v246, v246 dst_sel:WORD_1 dst_unused:UNUSED_PRESERVE src0_sel:WORD_1\n"
        "v_mfma_f32_16x16x32_bf16 v[164:167], a[172:175], v[226:229], v[164:167]\n"
        "v_exp_f16_sdwa v247, v247 dst_sel:WORD_1 dst_unused:UNUSED_PRESERVE src0_sel:WORD_1\n"
        "v_mfma_f32_16x16x32_bf16 v[164:167], a[168:171], v[222:225], v[164:167]\n"
        "v_add_f32_dpp v184, v184, v184 row_ror:2 row_mask:0xf bank_mask:0xf\n"
        "v_cvt_pk_bf16_f32 v34, v252, v253\n"
        "v_mfma_f32_16x16x32_bf16 v[250:253], a[188:191], v[218:221], 0\n"
        "v_cvt_pk_bf16_f32 v35, v254, v255\n"
        "v_cvt_pk_bf16_f32 v36, v106, v107\n"
        "v_mfma_f32_16x16x32_bf16 v[250:253], a[184:187], v[226:229], v[250:253]\n"
        "v_pk_add_f16 v168, v246, v247\n"
        "v_add_f32_dpp v184, v184, v184 row_ror:1 row_mask:0xf bank_mask:0xf\n"
        "v_mfma_f32_16x16x32_bf16 v[250:253], a[180:183], v[222:225], v[250:253]\n"
        "buffer_store_dwordx3 v[34:36], v17, s[44:47], s27 offen offset:1152 sc1 nt\n"
        "v_add_f32_dpp v184, v184, v184 row_bcast:15 row_mask:0xa bank_mask:0xf\n"
        "v_cvt_pk_f16_f32 v248, v126, v127\n"
        "v_cvt_pk_f16_f32 v249, v128, v129\n"
        "v_pk_mul_f16 v248, v248, v146 op_sel_hi:[1,0]\n"
        "v_mfma_f32_16x16x32_bf16 v[34:37], a[192:195], v[222:225], 0\n"
        "v_pk_mul_f16 v249, v249, v146 op_sel_hi:[1,0]\n"
        "v_pk_mul_f16 v248, v248, v42\n"
        "v_mfma_f32_16x16x32_bf16 v[34:37], a[196:199], v[226:229], v[34:37]\n"
        "v_pk_mul_f16 v249, v249, v43\n"
        "v_add_f32_dpp v184, v184, v184 row_bcast:31 row_mask:0xc bank_mask:0xf\n"
        "v_mfma_f32_16x16x32_bf16 v[34:37], a[200:203], v[218:221], v[34:37]\n"
        "v_exp_f16_sdwa v248, v248 dst_sel:WORD_0 dst_unused:UNUSED_PRESERVE src0_sel:WORD_0\n"
        "v_mfma_f32_16x16x32_bf16 v[104:107], a[212:215], v[218:221], 0\n"
        "v_exp_f16_sdwa v249, v249 dst_sel:WORD_0 dst_unused:UNUSED_PRESERVE src0_sel:WORD_0\n"
        "v_mfma_f32_16x16x32_bf16 v[104:107], a[208:211], v[226:229], v[104:107]\n"
        "v_readlane_b32 s95, v184, 63\n"
        "v_cvt_pk_f16_f32 v128, v186, v187\n"
        "v_mfma_f32_16x16x32_bf16 v[104:107], a[204:207], v[222:225], v[104:107]\n"
        "v_exp_f16_sdwa v248, v248 dst_sel:WORD_1 dst_unused:UNUSED_PRESERVE src0_sel:WORD_1\n"
        "v_mfma_f32_16x16x32_bf16 v[130:133], v[194:197], a[216:219], 0\n"
        "v_exp_f16_sdwa v249, v249 dst_sel:WORD_1 dst_unused:UNUSED_PRESERVE src0_sel:WORD_1\n"
        "v_mfma_f32_16x16x32_bf16 v[130:133], v[206:209], a[220:223], v[130:133]\n"
        "v_pk_add_f16 v168, v168, v248\n"
        "v_pk_add_f16 v168, v168, v249\n"
        "v_mfma_f32_16x16x32_bf16 v[130:133], v[210:213], a[224:227], v[130:133]\n"
        "v_rcp_f32 v184, s95\n"
        "v_mfma_f32_16x16x32_bf16 v[112:115], v[218:221], a[224:227], 0\n"
        "v_mul_f32 v179, v179, v184\n"
        "v_cvt_f16_f32 v179, v179\n"
        "v_mfma_f32_16x16x32_bf16 v[112:115], v[226:229], a[220:223], v[112:115]\n"
        "v_mul_f16 v179, v179, v108\n"
        "v_cvt_pk_f16_f32 v129, v188, v189\n"
        "v_mfma_f32_16x16x32_bf16 v[112:115], v[222:225], a[216:219], v[112:115]\n"
        "ds_write_b16 v22, v179 offset:512\n"
        "v_mfma_f32_16x16x32_bf16 v[186:189], v[198:201], a[216:219], 0\n"
        "v_pk_mul_f16 v128, v128, v146 op_sel_hi:[1,0]\n"
        "v_pk_mul_f16 v129, v129, v146 op_sel_hi:[1,0]\n"
        "v_mfma_f32_16x16x32_bf16 v[186:189], v[214:217], a[220:223], v[186:189]\n"
        "v_pk_mul_f16 v128, v128, v30\n"
        "v_pk_mul_f16 v129, v129, v31\n"
        "v_mfma_f32_16x16x32_bf16 v[186:189], v[202:205], a[224:227], v[186:189]\n"
        "v_exp_f16_sdwa v128, v128 dst_sel:WORD_0 dst_unused:UNUSED_PRESERVE src0_sel:WORD_0\n"
        "v_cvt_pk_bf16_f32 v108, v120, v121\n"
        "v_cvt_pk_bf16_f32 v109, v122, v123\n"
        "v_cvt_pk_bf16_f32 v110, v230, v231\n"
        "v_cvt_pk_bf16_f32 v111, v232, v233\n"
        "s_nop 1\n"
        "v_mfma_f32_16x16x32_bf16 v[230:233], v[194:197], v[108:111], 0\n"
        "v_cvt_pk_bf16_f32 v148, v164, v165\n"
        "v_cvt_pk_bf16_f32 v149, v166, v167\n"
        "v_cvt_pk_bf16_f32 v150, v250, v251\n"
        "v_cvt_pk_bf16_f32 v151, v252, v253\n"
        "s_nop 1\n"
        "v_mfma_f32_16x16x32_bf16 v[230:233], v[206:209], v[148:151], v[230:233]\n"
        "v_cvt_pk_bf16_f32 v252, v34, v35\n"
        "v_cvt_pk_bf16_f32 v253, v36, v37\n"
        "v_cvt_pk_bf16_f32 v254, v104, v105\n"
        "v_cvt_pk_bf16_f32 v255, v106, v107\n"
        "s_nop 1\n"
        "v_mfma_f32_16x16x32_bf16 v[230:233], v[210:213], v[252:255], v[230:233]\n"
        "v_exp_f16_sdwa v129, v129 dst_sel:WORD_0 dst_unused:UNUSED_PRESERVE src0_sel:WORD_0\n"
        "v_mfma_f32_16x16x32_bf16 v[34:37], v[218:221], v[252:255], 0\n"
        "v_exp_f16_sdwa v128, v128 dst_sel:WORD_1 dst_unused:UNUSED_PRESERVE src0_sel:WORD_1\n"
        "v_mfma_f32_16x16x32_bf16 v[34:37], v[226:229], v[148:151], v[34:37]\n"
        "v_exp_f16_sdwa v129, v129 dst_sel:WORD_1 dst_unused:UNUSED_PRESERVE src0_sel:WORD_1\n"
        "v_mfma_f32_16x16x32_bf16 v[34:37], v[222:225], v[108:111], v[34:37]\n"
        "v_pk_add_f16 v168, v168, v128\n"
        "v_pk_add_f16 v168, v168, v129\n"
        "v_mfma_f32_16x16x32_bf16 v[120:123], v[198:201], v[108:111], 0\n"
        "v_cvt_pk_f16_f32 v104, v130, v131\n"
        "v_cvt_pk_f16_f32 v105, v132, v133\n"
        "v_mfma_f32_16x16x32_bf16 v[120:123], v[214:217], v[148:151], v[120:123]\n"
        "v_cvt_pk_f16_f32 v106, v112, v113\n"
        "v_cvt_pk_f16_f32 v107, v114, v115\n"
        "v_mfma_f32_16x16x32_bf16 v[120:123], v[202:205], v[252:255], v[120:123]\n"
        "v_cvt_pk_f16_f32 v108, v186, v187\n"
        "v_cvt_pk_f16_f32 v109, v188, v189\n"
        "v_mfma_f32_16x16x32_bf16 v[164:167], a[144:147], v[198:201], 0\n"
        "v_cvt_pk_f16_f32 v252, v230, v231\n"
        "v_cvt_pk_f16_f32 v253, v232, v233\n"
        "v_mfma_f32_16x16x32_bf16 v[164:167], a[148:151], v[214:217], v[164:167]\n"
        "v_pk_mul_f16 v252, v252, v146 op_sel:[0,1]\n"
        "v_pk_mul_f16 v253, v253, v146 op_sel:[0,1]\n"
        "v_mfma_f32_16x16x32_bf16 v[164:167], a[152:155], v[202:205], v[164:167]\n"
        "v_pk_mul_f16 v252, v252, v40\n"
        "v_pk_mul_f16 v253, v253, v41\n"
        "v_mfma_f32_16x16x32_bf16 v[186:189], a[164:167], v[202:205], 0\n"
        "v_exp_f16_sdwa v252, v252 dst_sel:WORD_0 dst_unused:UNUSED_PRESERVE src0_sel:WORD_0\n"
        "v_mfma_f32_16x16x32_bf16 v[186:189], a[160:163], v[214:217], v[186:189]\n"
        "v_exp_f16_sdwa v253, v253 dst_sel:WORD_0 dst_unused:UNUSED_PRESERVE src0_sel:WORD_0\n"
        "v_mfma_f32_16x16x32_bf16 v[186:189], a[156:159], v[198:201], v[186:189]\n"
        "v_exp_f16_sdwa v252, v252 dst_sel:WORD_1 dst_unused:UNUSED_PRESERVE src0_sel:WORD_1\n"
        "v_mfma_f32_16x16x32_bf16 v[148:151], a[168:171], v[198:201], 0\n"
        "v_exp_f16_sdwa v253, v253 dst_sel:WORD_1 dst_unused:UNUSED_PRESERVE src0_sel:WORD_1\n"
        "v_mfma_f32_16x16x32_bf16 v[148:151], a[172:175], v[214:217], v[148:151]\n"
        "v_pk_add_f16 v169, v252, v253\n"
        "v_cvt_pk_f16_f32 v254, v34, v35\n"
        "v_mfma_f32_16x16x32_bf16 v[148:151], a[176:179], v[202:205], v[148:151]\n"
        "v_cvt_pk_f16_f32 v255, v36, v37\n"
        "v_pk_mul_f16 v254, v254, v146 op_sel:[0,1]\n"
        "v_mfma_f32_16x16x32_bf16 v[112:115], a[188:191], v[202:205], 0\n"
        "v_pk_mul_f16 v255, v255, v146 op_sel:[0,1]\n"
        "v_pk_mul_f16 v254, v254, v42\n"
        "v_mfma_f32_16x16x32_bf16 v[112:115], a[184:187], v[214:217], v[112:115]\n"
        "v_pk_mul_f16 v255, v255, v43\n"
        "v_cvt_pk_f16_f32 v230, v120, v121\n"
        "v_mfma_f32_16x16x32_bf16 v[112:115], a[180:183], v[198:201], v[112:115]\n"
        "v_exp_f16_sdwa v254, v254 dst_sel:WORD_0 dst_unused:UNUSED_PRESERVE src0_sel:WORD_0\n"
        "v_mfma_f32_16x16x32_bf16 v[34:37], a[192:195], v[198:201], 0\n"
        "v_exp_f16_sdwa v255, v255 dst_sel:WORD_0 dst_unused:UNUSED_PRESERVE src0_sel:WORD_0\n"
        "v_mfma_f32_16x16x32_bf16 v[34:37], a[196:199], v[214:217], v[34:37]\n"
        "v_exp_f16_sdwa v254, v254 dst_sel:WORD_1 dst_unused:UNUSED_PRESERVE src0_sel:WORD_1\n"
        "v_mfma_f32_16x16x32_bf16 v[34:37], a[200:203], v[202:205], v[34:37]\n"
        "v_exp_f16_sdwa v255, v255 dst_sel:WORD_1 dst_unused:UNUSED_PRESERVE src0_sel:WORD_1\n"
        "v_mfma_f32_16x16x32_bf16 v[234:237], a[212:215], v[202:205], 0\n"
        "v_pk_add_f16 v169, v169, v254\n"
        "v_pk_add_f16 v169, v169, v255\n"
        "v_mfma_f32_16x16x32_bf16 v[234:237], a[208:211], v[214:217], v[234:237]\n"
        "v_cvt_pk_f16_f32 v231, v122, v123\n"
        "v_pk_mul_f16 v230, v230, v146 op_sel:[0,1]\n"
        "v_mfma_f32_16x16x32_bf16 v[234:237], a[204:207], v[198:201], v[234:237]\n"
        "v_pk_mul_f16 v231, v231, v146 op_sel:[0,1]\n"
        "v_pk_mul_f16 v230, v230, v30\n"
        "v_mfma_f32_16x16x32_bf16 v[124:127], v[202:205], a[236:239], 0\n"
        "v_pk_mul_f16 v231, v231, v31\n"
        "v_cvt_pk_bf16_f32 v120, v164, v165\n"
        "v_mfma_f32_16x16x32_bf16 v[124:127], v[214:217], a[232:235], v[124:127]\n"
        "v_exp_f16_sdwa v230, v230 dst_sel:WORD_0 dst_unused:UNUSED_PRESERVE src0_sel:WORD_0\n"
        "v_mfma_f32_16x16x32_bf16 v[124:127], v[198:201], a[228:231], v[124:127]\n"
        "v_cvt_pk_bf16_f32 v121, v166, v167\n"
        "v_cvt_pk_bf16_f32 v122, v186, v187\n"
        "v_mfma_f32_16x16x32_bf16 v[176:179], v[222:225], a[228:231], 0\n"
        "v_cndmask_b32_e64 v130, 0, v180, s[62:63]\n"
        "v_cndmask_b32_e64 v232, 0, v180, s[64:65]\n"
        "v_mfma_f32_16x16x32_bf16 v[176:179], v[226:229], a[232:235], v[176:179]\n"
        "v_pk_add_f16 v168, v168, v130\n"
        "v_cvt_pk_bf16_f32 v123, v188, v189\n"
        "v_mfma_f32_16x16x32_bf16 v[176:179], v[218:221], a[236:239], v[176:179]\n"
        "v_cvt_pk_bf16_f32 v190, v148, v149\n"
        "v_cvt_pk_bf16_f32 v191, v150, v151\n"
        "v_mfma_f32_16x16x32_bf16 v[148:151], v[210:213], a[236:239], 0\n"
        "v_mov_b64 v[170:171], 0\n"
        "v_cvt_pk_bf16_f32 v192, v112, v113\n"
        "v_mfma_f32_16x16x32_bf16 v[148:151], v[206:209], a[232:235], v[148:151]\n"
        "v_cvt_pk_bf16_f32 v193, v114, v115\n"
        "v_cvt_pk_bf16_f32 v136, v34, v35\n"
        "v_mfma_f32_16x16x32_bf16 v[148:151], v[194:197], a[228:231], v[148:151]\n"
        "v_cvt_pk_bf16_f32 v137, v36, v37\n"
        "v_cvt_pk_bf16_f32 v138, v234, v235\n"
        "v_mfma_f32_16x16x32_bf16 v[34:37], v[194:197], v[120:123], 0\n"
        "v_cvt_pk_bf16_f32 v139, v236, v237\n"
        "v_cvt_pk_f16_f32 v234, v124, v125\n"
        "v_mfma_f32_16x16x32_bf16 v[34:37], v[206:209], v[190:193], v[34:37]\n"
        "v_exp_f16_sdwa v231, v231 dst_sel:WORD_0 dst_unused:UNUSED_PRESERVE src0_sel:WORD_0\n"
        "v_mfma_f32_16x16x32_bf16 v[34:37], v[210:213], v[136:139], v[34:37]\n"
        "v_exp_f16_sdwa v230, v230 dst_sel:WORD_1 dst_unused:UNUSED_PRESERVE src0_sel:WORD_1\n"
        "v_mfma_f32_16x16x32_bf16 v[164:167], v[218:221], v[136:139], 0\n"
        "v_exp_f16_sdwa v231, v231 dst_sel:WORD_1 dst_unused:UNUSED_PRESERVE src0_sel:WORD_1\n"
        "v_mfma_f32_16x16x32_bf16 v[164:167], v[226:229], v[190:193], v[164:167]\n"
        "v_pk_add_f16 v169, v169, v230\n"
        "v_pk_add_f16 v169, v169, v231\n"
        "v_mfma_f32_16x16x32_bf16 v[164:167], v[222:225], v[120:123], v[164:167]\n"
        "v_pk_add_f16 v169, v169, v232\n"
        "v_cvt_pk_f16_f32 v235, v126, v127\n"
        "v_mfma_f32_16x16x32_bf16 v[186:189], v[198:201], v[120:123], 0\n"
        "v_cvt_pk_f16_f32 v114, v176, v177\n"
        "v_cvt_pk_f16_f32 v115, v178, v179\n"
        "v_mfma_f32_16x16x32_bf16 v[186:189], v[214:217], v[190:193], v[186:189]\n"
        "v_cvt_pk_f16_f32 v112, v148, v149\n"
        "v_cvt_pk_f16_f32 v113, v150, v151\n"
        "v_mfma_f32_16x16x32_bf16 v[186:189], v[202:205], v[136:139], v[186:189]\n"
        "v_cvt_pk_f16_f32 v120, v34, v35\n"
        "v_cvt_pk_f16_f32 v121, v36, v37\n"
        "v_mfma_f32_16x16x32_bf16 v[176:179], v[194:197], a[240:243], 0\n"
        "v_pk_mul_f16 v120, v120, v147 op_sel_hi:[1,0]\n"
        "v_pk_mul_f16 v121, v121, v147 op_sel_hi:[1,0]\n"
        "v_mfma_f32_16x16x32_bf16 v[176:179], v[206:209], a[244:247], v[176:179]\n"
        "v_pk_mul_f16 v120, v120, v40\n"
        "v_pk_mul_f16 v121, v121, v41\n"
        "v_mfma_f32_16x16x32_bf16 v[176:179], v[210:213], a[248:251], v[176:179]\n"
        "v_exp_f16_sdwa v120, v120 dst_sel:WORD_0 dst_unused:UNUSED_PRESERVE src0_sel:WORD_0\n"
        "v_mfma_f32_16x16x32_bf16 v[240:243], v[218:221], a[248:251], 0\n"
        "v_exp_f16_sdwa v121, v121 dst_sel:WORD_0 dst_unused:UNUSED_PRESERVE src0_sel:WORD_0\n"
        "v_mfma_f32_16x16x32_bf16 v[240:243], v[226:229], a[244:247], v[240:243]\n"
        "v_exp_f16_sdwa v120, v120 dst_sel:WORD_1 dst_unused:UNUSED_PRESERVE src0_sel:WORD_1\n"
        "v_mfma_f32_16x16x32_bf16 v[240:243], v[222:225], a[240:243], v[240:243]\n"
        "v_exp_f16_sdwa v121, v121 dst_sel:WORD_1 dst_unused:UNUSED_PRESERVE src0_sel:WORD_1\n"
        "v_mfma_f32_16x16x32_bf16 v[34:37], v[198:201], a[240:243], 0\n"
        "v_cvt_pk_f16_f32 v122, v164, v165\n"
        "v_cvt_pk_f16_f32 v123, v166, v167\n"
        "v_mfma_f32_16x16x32_bf16 v[34:37], v[214:217], a[244:247], v[34:37]\n"
        "v_pk_mul_f16 v122, v122, v147 op_sel_hi:[1,0]\n"
        "v_pk_mul_f16 v123, v123, v147 op_sel_hi:[1,0]\n"
        "v_mfma_f32_16x16x32_bf16 v[34:37], v[202:205], a[248:251], v[34:37]\n"
        "v_pk_mul_f16 v122, v122, v42\n"
        "v_pk_mul_f16 v123, v123, v43\n"
        "v_mfma_f32_16x16x32_f16 v[150:153], v[52:55], v[168:171], 0\n"
        "v_exp_f16_sdwa v122, v122 dst_sel:WORD_0 dst_unused:UNUSED_PRESERVE src0_sel:WORD_0\n"
        "v_mfma_f32_16x16x32_bf16 v[136:139], v[202:205], v[96:99], 0\n"
        "v_exp_f16_sdwa v123, v123 dst_sel:WORD_0 dst_unused:UNUSED_PRESERVE src0_sel:WORD_0\n"
        "v_mfma_f32_16x16x32_bf16 v[136:139], v[214:217], v[76:79], v[136:139]\n"
        "v_exp_f16_sdwa v122, v122 dst_sel:WORD_1 dst_unused:UNUSED_PRESERVE src0_sel:WORD_1\n"
        "v_mfma_f32_16x16x32_bf16 v[136:139], v[198:201], a[252:255], v[136:139]\n"
        "v_exp_f16_sdwa v123, v123 dst_sel:WORD_1 dst_unused:UNUSED_PRESERVE src0_sel:WORD_1\n"
        "v_mfma_f32_16x16x32_bf16 v[190:193], v[222:225], a[252:255], 0\n"
        "v_cndmask_b32_e64 v134, 0, v180, s[66:67]\n"
        "v_cvt_pk_f16_f32 v132, v186, v187\n"
        "v_mfma_f32_16x16x32_bf16 v[190:193], v[226:229], v[76:79], v[190:193]\n"
        "ds_read2_b32 v[110:111], v24 offset0:0 offset1:0\n"
        "ds_read2_b32 v[236:237], v24 offset0:1 offset1:1\n"
        "v_mfma_f32_16x16x32_bf16 v[190:193], v[218:221], v[96:99], v[190:193]\n"
        "ds_read2_b32 v[126:127], v24 offset0:2 offset1:2\n"
        "ds_read2_b32 v[152:153], v24 offset0:3 offset1:3\n"
        "v_mfma_f32_16x16x32_bf16 v[158:161], v[210:213], v[96:99], 0\n"
        "ds_read2_b32 v[166:167], v24 offset0:4 offset1:4\n"
        "v_mov_b32 v131, 0\n"
        "v_mfma_f32_16x16x32_bf16 v[158:161], v[206:209], v[76:79], v[158:161]\n"
        "ds_read2_b32 v[180:181], v24 offset0:5 offset1:5\n"
        "v_cvt_pk_f16_f32 v133, v188, v189\n"
        "v_mfma_f32_16x16x32_bf16 v[158:161], v[194:197], a[252:255], v[158:161]\n"
        "v_pk_mul_f16 v132, v132, v147 op_sel_hi:[1,0]\n"
        "v_pk_mul_f16 v133, v133, v147 op_sel_hi:[1,0]\n"
        "v_mfma_f32_16x16x32_bf16 v[172:175], v[194:197], v[64:67], 0\n"
        "v_pk_mul_f16 v132, v132, v30\n"
        "v_pk_mul_f16 v133, v133, v31\n"
        "v_mfma_f32_16x16x32_bf16 v[172:175], v[206:209], v[60:63], v[172:175]\n"
        "v_exp_f16_sdwa v132, v132 dst_sel:WORD_0 dst_unused:UNUSED_PRESERVE src0_sel:WORD_0\n"
        "v_mfma_f32_16x16x32_bf16 v[172:175], v[210:213], v[72:75], v[172:175]\n"
        "v_exp_f16_sdwa v133, v133 dst_sel:WORD_0 dst_unused:UNUSED_PRESERVE src0_sel:WORD_0\n"
        "v_mfma_f32_16x16x32_bf16 v[162:165], v[218:221], v[72:75], 0\n"
        "v_exp_f16_sdwa v132, v132 dst_sel:WORD_1 dst_unused:UNUSED_PRESERVE src0_sel:WORD_1\n"
        "v_mfma_f32_16x16x32_bf16 v[162:165], v[226:229], v[60:63], v[162:165]\n"
        "v_exp_f16_sdwa v133, v133 dst_sel:WORD_1 dst_unused:UNUSED_PRESERVE src0_sel:WORD_1\n"
        "v_mfma_f32_16x16x32_bf16 v[162:165], v[222:225], v[64:67], v[162:165]\n"
        "v_cvt_pk_f16_f32 v116, v176, v177\n"
        "v_cvt_pk_f16_f32 v117, v178, v179\n"
        "v_mfma_f32_16x16x32_bf16 v[176:179], v[198:201], v[64:67], 0\n"
        "s_waitcnt lgkmcnt(0)\n"
        "v_lshlrev_b32 v146, 16, v110\n"
        "v_lshlrev_b32 v147, 16, v236\n"
        "v_mfma_f32_16x16x32_bf16 v[176:179], v[214:217], v[60:63], v[176:179]\n"
        "v_lshlrev_b32 v148, 16, v126\n"
        "v_lshlrev_b32 v149, 16, v152\n"
        "v_mfma_f32_16x16x32_bf16 v[176:179], v[202:205], v[72:75], v[176:179]\n"
        "v_lshlrev_b32 v250, 16, v166\n"
        "v_lshlrev_b32 v251, 16, v180\n"
        "v_mfma_f32_16x16x32_bf16 v[186:189], v[202:205], v[88:91], 0\n"
        "v_cvt_pk_f16_f32 v118, v240, v241\n"
        "v_cvt_pk_f16_f32 v119, v242, v243\n"
        "v_mfma_f32_16x16x32_bf16 v[186:189], v[214:217], v[80:83], v[186:189]\n"
        "v_pk_add_f16 v170, v120, v121\n"
        "v_pk_add_f16 v170, v170, v122\n"
        "v_mfma_f32_16x16x32_bf16 v[186:189], v[198:201], v[92:95], v[186:189]\n"
        "s_waitcnt vmcnt(1)\n"
        "v_pk_add_f16 v170, v170, v123\n"
        "v_pk_add_f16 v170, v170, v132\n"
        "v_mfma_f32_16x16x32_bf16 v[142:145], v[222:225], v[92:95], 0\n"
        "v_readfirstlane_b32 s50, v26\n"
        "s_lshl_b32 s92, s15, 3\n"
        "s_add_u32 s92, s92, s48\n"
        "v_mov_b32 v27, s49\n"
        "s_nop 0\n"
        "s_cmp_eq_u32 s50, s21\n"
        "s_cselect_b32 s92, s92, 0x10000\n"
        "v_mov_b32 v26, s92\n"
        "ds_write_b32 v27, v26 offset:8\n"
        "v_mfma_f32_16x16x32_bf16 v[142:145], v[226:229], v[80:83], v[142:145]\n"
        "v_cvt_pk_f16_f32 v124, v34, v35\n"
        "v_cvt_pk_f16_f32 v125, v36, v37\n"
        "v_mfma_f32_16x16x32_bf16 v[142:145], v[218:221], v[88:91], v[142:145]\n"
        "v_pk_add_f16 v170, v170, v133\n"
        "v_pk_add_f16 v170, v170, v134\n"
        "v_mfma_f32_16x16x32_bf16 v[34:37], v[210:213], v[88:91], 0\n"
        "v_mov_b32 v233, 0\n"
        "v_max3_f32 v239, s95, v150, v151\n"
        "v_mfma_f32_16x16x32_bf16 v[34:37], v[206:209], v[80:83], v[34:37]\n"
        "v_rcp_f32 v183, v150\n"
        "v_mfma_f32_16x16x32_bf16 v[34:37], v[194:197], v[92:95], v[34:37]\n"
        "v_cvt_f16_f32 v150, v183\n"
        "v_pk_mul_f16 v246, v246, v150 op_sel_hi:[1,0]\n"
        "v_mfma_f32_16x16x32_f16 v[242:245], v[52:55], v[168:171], 0\n"
        "v_pk_mul_f16 v247, v247, v150 op_sel_hi:[1,0]\n"
        "v_pk_mul_f16 v248, v248, v150 op_sel_hi:[1,0]\n"
        "v_pk_mul_f16 v246, v246, v40\n"
        "v_pk_mul_f16 v247, v247, v41\n"
        "v_pk_mul_f16 v248, v248, v42\n"
        "v_pk_mul_f16 v249, v249, v150 op_sel_hi:[1,0]\n"
        "v_pk_mul_f16 v249, v249, v43\n"
        "s_nop 1\n"
        "v_mfma_f32_16x16x32_f16 v[240:243], v[246:249], v[104:107], 0\n"
        "v_pk_mul_f16 v128, v128, v150 op_sel_hi:[1,0]\n"
        "v_pk_mul_f16 v129, v129, v150 op_sel_hi:[1,0]\n"
        "v_pk_mul_f16 v130, v130, v150 op_sel_hi:[1,0]\n"
        "v_pk_mul_f16 v128, v128, v30\n"
        "v_pk_mul_f16 v129, v129, v31\n"
        "s_nop 1\n"
        "v_mfma_f32_16x16x32_f16 v[240:243], v[128:131], v[108:111], v[240:243]\n"
        "v_rcp_f32 v169, v151\n"
        "v_mfma_f32_16x16x32_bf16 v[240:243], v[194:197], v[68:71], v[240:243]\n"
        "v_add_f32 v245, v184, v183\n"
        "v_cvt_f16_f32 v168, v169\n"
        "v_mfma_f32_16x16x32_bf16 v[154:157], v[194:197], v[100:103], 0\n"
        "v_add_f32 v245, v245, v169\n"
        "v_pk_mul_f16 v252, v252, v168 op_sel_hi:[1,0]\n"
        "v_mfma_f32_16x16x32_f16 v[154:157], v[128:131], v[234:237], v[154:157]\n"
        "v_pk_mul_f16 v253, v253, v168 op_sel_hi:[1,0]\n"
        "v_pk_mul_f16 v254, v254, v168 op_sel_hi:[1,0]\n"
        "v_mfma_f32_16x16x32_f16 v[154:157], v[246:249], v[112:115], v[154:157]\n"
        "v_pk_mul_f16 v255, v255, v168 op_sel_hi:[1,0]\n"
        "v_pk_mul_f16 v230, v230, v168 op_sel_hi:[1,0]\n"
        "v_mfma_f32_16x16x32_f16 v[182:185], v[246:249], v[116:119], 0\n"
        "v_pk_mul_f16 v231, v231, v168 op_sel_hi:[1,0]\n"
        "v_pk_mul_f16 v232, v232, v168 op_sel_hi:[1,0]\n"
        "v_mfma_f32_16x16x32_f16 v[182:185], v[128:131], v[124:127], v[182:185]\n"
        "v_cvt_pk_f16_f32 v150, v136, v137\n"
        "v_cvt_pk_f16_f32 v151, v138, v139\n"
        "v_mfma_f32_16x16x32_bf16 v[182:185], v[206:209], v[68:71], v[182:185]\n"
        "v_cvt_pk_f16_f32 v170, v190, v191\n"
        "v_cvt_pk_f16_f32 v171, v192, v193\n"
        "v_mfma_f32_16x16x32_bf16 v[190:193], v[206:209], v[100:103], 0\n"
        "v_cvt_pk_f16_f32 v168, v158, v159\n"
        "v_cvt_pk_f16_f32 v169, v160, v161\n"
        "v_mfma_f32_16x16x32_f16 v[190:193], v[128:131], v[150:153], v[190:193]\n"
        "v_cvt_pk_f16_f32 v136, v172, v173\n"
        "v_cvt_pk_f16_f32 v137, v174, v175\n"
        "v_mfma_f32_16x16x32_f16 v[190:193], v[246:249], v[168:171], v[190:193]\n"
        "v_cvt_pk_f16_f32 v138, v162, v163\n"
        "v_cvt_pk_f16_f32 v139, v164, v165\n"
        "s_nop 1\n"
        "v_mfma_f32_16x16x32_f16 v[172:175], v[246:249], v[136:139], 0\n"
        "v_cvt_pk_f16_f32 v164, v176, v177\n"
        "v_cvt_pk_f16_f32 v165, v178, v179\n"
        "s_nop 1\n"
        "v_mfma_f32_16x16x32_f16 v[172:175], v[128:131], v[164:167], v[172:175]\n"
        "v_pk_mul_f16 v252, v252, v40\n"
        "v_pk_mul_f16 v253, v253, v41\n"
        "v_mfma_f32_16x16x32_bf16 v[172:175], v[210:213], v[68:71], v[172:175]\n"
        "v_mov_b32 v135, 0\n"
        "v_cvt_pk_f16_f32 v178, v186, v187\n"
        "v_mfma_f32_16x16x32_bf16 v[160:163], v[210:213], v[100:103], 0\n"
        "v_cvt_pk_f16_f32 v179, v188, v189\n"
        "v_cvt_pk_f16_f32 v188, v142, v143\n"
        "s_nop 0\n"
        "v_mfma_f32_16x16x32_f16 v[160:163], v[128:131], v[178:181], v[160:163]\n"
        "v_cvt_pk_f16_f32 v189, v144, v145\n"
        "v_cvt_pk_f16_f32 v186, v34, v35\n"
        "v_cvt_pk_f16_f32 v187, v36, v37\n"
        "s_nop 1\n"
        "v_mfma_f32_16x16x32_f16 v[160:163], v[246:249], v[186:189], v[160:163]\n"
        "s_add_u32 s90, s89, 0\n"
        "v_pk_mul_f16 v254, v254, v42\n"
        "v_pk_mul_f16 v255, v255, v43\n"
        "s_nop 1\n"
        "v_mfma_f32_16x16x32_f16 v[246:249], v[252:255], v[104:107], 0\n"
        "v_pk_mul_f16 v230, v230, v30\n"
        "v_pk_mul_f16 v231, v231, v31\n"
        "s_nop 1\n"
        "v_mfma_f32_16x16x32_f16 v[246:249], v[230:233], v[108:111], v[246:249]\n"
        "v_rcp_f32 v176, v244\n"
        "v_mfma_f32_16x16x32_bf16 v[246:249], v[222:225], v[68:71], v[246:249]\n"
        "v_max_f32 v239, v239, v244\n"
        "v_add_f32 v245, v245, v176\n"
        "v_mfma_f32_16x16x32_bf16 v[34:37], v[222:225], v[100:103], 0\n"
        "v_fma_f32 v239, v245, s82, v239\n"
        "v_cmp_nlt_f32_e64 s[74:75], v239, s83\n"
        "v_mfma_f32_16x16x32_f16 v[34:37], v[230:233], v[234:237], v[34:37]\n"
        "v_cvt_pk_bf16_f32 v142, v240, v154\n"
        "v_cvt_pk_bf16_f32 v143, v182, v190\n"
        "v_mfma_f32_16x16x32_f16 v[34:37], v[252:255], v[112:115], v[34:37]\n"
        "s_cmp_lg_u64 s[74:75], 0\n"
        "s_cbranch_scc1 SLOW_%=\n"
        "v_cvt_pk_bf16_f32 v144, v172, v160\n"
        "v_mfma_f32_16x16x32_f16 v[128:131], v[252:255], v[116:119], 0\n"
        "s_add_u32 s91, s89, 98112\n"
        "v_cvt_pk_bf16_f32 v194, v241, v155\n"
        "v_cvt_pk_bf16_f32 v195, v183, v191\n"
        "v_mfma_f32_16x16x32_f16 v[128:131], v[230:233], v[124:127], v[128:131]\n"
        "v_cvt_pk_bf16_f32 v196, v173, v161\n"
        "v_cvt_f16_f32 v197, v176\n"
        "v_mfma_f32_16x16x32_bf16 v[128:131], v[226:229], v[68:71], v[128:131]\n"
        "buffer_store_dwordx3 v[142:144], v14, s[40:43], s90 offen sc1 nt\n"
        "v_cvt_pk_bf16_f32 v190, v242, v156\n"
        "v_cvt_pk_bf16_f32 v191, v184, v192\n"
        "v_cvt_pk_bf16_f32 v192, v174, v162\n"
        "v_pk_mul_f16 v120, v120, v197 op_sel_hi:[1,0]\n"
        "v_mfma_f32_16x16x32_bf16 v[210:213], v[226:229], v[100:103], 0\n"
        "ds_read_b128 v[226:229], v20 offset:512\n"
        "ds_read_b128 v[222:225], v20 offset:528\n"
        "v_mfma_f32_16x16x32_f16 v[210:213], v[230:233], v[150:153], v[210:213]\n"
        "v_cvt_pk_bf16_f32 v182, v243, v157\n"
        "v_cvt_pk_bf16_f32 v183, v185, v193\n"
        "v_mfma_f32_16x16x32_f16 v[210:213], v[252:255], v[168:171], v[210:213]\n"
        "buffer_store_dwordx3 v[194:196], v15, s[40:43], s90 offen offset:768 sc1 nt\n"
        "v_cvt_pk_bf16_f32 v184, v175, v163\n"
        "v_pk_mul_f16 v121, v121, v197 op_sel_hi:[1,0]\n"
        "v_pk_mul_f16 v122, v122, v197 op_sel_hi:[1,0]\n"
        "v_pk_mul_f16 v123, v123, v197 op_sel_hi:[1,0]\n"
        "v_mfma_f32_16x16x32_f16 v[206:209], v[252:255], v[136:139], 0\n"
        "v_pk_mul_f16 v132, v132, v197 op_sel_hi:[1,0]\n"
        "v_pk_mul_f16 v133, v133, v197 op_sel_hi:[1,0]\n"
        "v_mfma_f32_16x16x32_f16 v[206:209], v[230:233], v[164:167], v[206:209]\n"
        "v_pk_mul_f16 v134, v134, v197 op_sel_hi:[1,0]\n"
        "v_pk_mul_f16 v120, v120, v40\n"
        "v_mfma_f32_16x16x32_bf16 v[206:209], v[218:221], v[68:71], v[206:209]\n"
        "buffer_store_dwordx3 v[190:192], v14, s[40:43], s91 offen offset:192 sc1 nt\n"
        "v_pk_mul_f16 v121, v121, v41\n"
        "v_pk_mul_f16 v122, v122, v42\n"
        "v_pk_mul_f16 v123, v123, v43\n"
        "v_pk_mul_f16 v132, v132, v30\n"
        "v_mfma_f32_16x16x32_bf16 v[192:195], v[218:221], v[100:103], 0\n"
        "v_pk_mul_f16 v133, v133, v31\n"
        "v_cvt_pk_bf16_f32 v244, v246, v34\n"
        "v_mfma_f32_16x16x32_f16 v[192:195], v[230:233], v[178:181], v[192:195]\n"
        "s_waitcnt lgkmcnt(0)\n"
        "v_pk_mul_f16 v196, v226, v104\n"
        "v_pk_fma_f16 v196, v227, v105, v196\n"
        "v_mfma_f32_16x16x32_f16 v[192:195], v[252:255], v[186:189], v[192:195]\n"
        "buffer_store_dwordx3 v[182:184], v16, s[40:43], s91 offen offset:960 sc1 nt\n"
        "v_pk_fma_f16 v196, v228, v106, v196\n"
        "v_pk_fma_f16 v196, v229, v107, v196\n"
        "v_pk_fma_f16 v196, v222, v108, v196\n"
        "v_pk_fma_f16 v196, v223, v109, v196\n"
        "v_mfma_f32_16x16x32_f16 v[218:221], v[120:123], v[104:107], 0\n"
        "v_pk_fma_f16 v196, v224, v110, v196\n"
        "v_pk_mul_f16 v197, v226, v112\n"
        "v_mfma_f32_16x16x32_f16 v[218:221], v[132:135], v[108:111], v[218:221]\n"
        "s_add_u32 s90, s89, 196224\n"
        "v_pk_fma_f16 v197, v227, v113, v197\n"
        "v_pk_fma_f16 v197, v228, v114, v197\n"
        "v_mfma_f32_16x16x32_bf16 v[218:221], v[198:201], v[68:71], v[218:221]\n"
        "v_pk_fma_f16 v197, v229, v115, v197\n"
        "v_pk_fma_f16 v197, v222, v234, v197\n"
        "v_mfma_f32_16x16x32_bf16 v[40:43], v[198:201], v[100:103], 0\n"
        "v_pk_fma_f16 v197, v223, v235, v197\n"
        "v_pk_fma_f16 v197, v224, v236, v197\n"
        "v_mfma_f32_16x16x32_f16 v[40:43], v[132:135], v[234:237], v[40:43]\n"
        "v_pk_mul_f16 v198, v226, v116\n"
        "v_pk_fma_f16 v198, v227, v117, v198\n"
        "v_mfma_f32_16x16x32_f16 v[40:43], v[120:123], v[112:115], v[40:43]\n"
        "s_add_u32 s91, s89, 294336\n"
        "v_pk_fma_f16 v198, v228, v118, v198\n"
        "v_pk_fma_f16 v198, v229, v119, v198\n"
        "v_mfma_f32_16x16x32_f16 v[106:109], v[120:123], v[116:119], 0\n"
        "v_pk_fma_f16 v198, v222, v124, v198\n"
        "v_pk_fma_f16 v198, v223, v125, v198\n"
        "v_mfma_f32_16x16x32_f16 v[106:109], v[132:135], v[124:127], v[106:109]\n"
        "v_pk_fma_f16 v198, v224, v126, v198\n"
        "v_pk_mul_f16 v199, v226, v168\n"
        "v_mfma_f32_16x16x32_bf16 v[106:109], v[214:217], v[68:71], v[106:109]\n"
        "v_pk_fma_f16 v199, v227, v169, v199\n"
        "v_pk_fma_f16 v199, v228, v170, v199\n"
        "v_mfma_f32_16x16x32_bf16 v[252:255], v[214:217], v[100:103], 0\n"
        "v_pk_fma_f16 v199, v229, v171, v199\n"
        "v_pk_fma_f16 v199, v222, v150, v199\n"
        "v_mfma_f32_16x16x32_f16 v[252:255], v[132:135], v[150:153], v[252:255]\n"
        "v_pk_fma_f16 v199, v223, v151, v199\n"
        "v_cvt_pk_bf16_f32 v245, v128, v210\n"
        "v_mfma_f32_16x16x32_f16 v[252:255], v[120:123], v[168:171], v[252:255]\n"
        "v_cvt_pk_bf16_f32 v246, v206, v192\n"
        "v_pk_fma_f16 v199, v224, v152, v199\n"
        "v_mfma_f32_16x16x32_f16 v[230:233], v[120:123], v[136:139], 0\n"
        "v_cvt_pk_bf16_f32 v112, v247, v35\n"
        "v_cvt_pk_bf16_f32 v113, v129, v211\n"
        "v_mfma_f32_16x16x32_f16 v[230:233], v[132:135], v[164:167], v[230:233]\n"
        "v_cvt_pk_bf16_f32 v114, v207, v193\n"
        "v_pk_mul_f16 v200, v226, v136\n"
        "v_mfma_f32_16x16x32_bf16 v[230:233], v[202:205], v[68:71], v[230:233]\n"
        "buffer_store_dwordx3 v[244:246], v14, s[40:43], s90 offen offset:384 sc1 nt\n"
        "v_cvt_pk_bf16_f32 v210, v248, v36\n"
        "v_cvt_pk_bf16_f32 v211, v130, v212\n"
        "v_cvt_pk_bf16_f32 v212, v208, v194\n"
        "v_pk_fma_f16 v200, v227, v137, v200\n"
        "v_mfma_f32_16x16x32_bf16 v[214:217], v[202:205], v[100:103], 0\n"
        "v_cvt_pk_bf16_f32 v202, v249, v37\n"
        "v_cvt_pk_bf16_f32 v203, v131, v213\n"
        "v_mfma_f32_16x16x32_f16 v[214:217], v[132:135], v[178:181], v[214:217]\n"
        "v_cvt_pk_bf16_f32 v204, v209, v195\n"
        "v_pk_fma_f16 v200, v228, v138, v200\n"
        "v_mfma_f32_16x16x32_f16 v[214:217], v[120:123], v[186:189], v[214:217]\n"
        "buffer_store_dwordx3 v[112:114], v17, s[40:43], s90 offen offset:1152 sc1 nt\n"
        "s_add_u32 s90, s89, 392448\n"
        "v_pk_fma_f16 v200, v229, v139, v200\n"
        "v_pk_fma_f16 v200, v222, v164, v200\n"
        "v_pk_fma_f16 v200, v223, v165, v200\n"
        "v_mfma_f32_16x16x32_f16 v[146:149], v[52:55], v[196:199], v[146:149]\n"
        "buffer_store_dwordx3 v[210:212], v14, s[40:43], s91 offen offset:576 sc1 nt\n"
        "v_pk_fma_f16 v200, v224, v166, v200\n"
        "v_pk_mul_f16 v201, v226, v186\n"
        "v_pk_fma_f16 v201, v227, v187, v201\n"
        "v_pk_fma_f16 v201, v228, v188, v201\n"
        "v_pk_fma_f16 v201, v229, v189, v201\n"
        "v_pk_fma_f16 v201, v222, v178, v201\n"
        "v_pk_fma_f16 v201, v223, v179, v201\n"
        "v_pk_fma_f16 v201, v224, v180, v201\n"
        "s_nop 1\n"
        "v_mfma_f32_16x16x32_f16 v[248:251], v[52:55], v[198:201], v[248:251]\n"
        "buffer_store_dwordx3 v[202:204], v14, s[40:43], s90 offen sc1 nt\n"
        "v_cvt_pk_bf16_f32 v222, v218, v40\n"
        "v_cvt_pk_bf16_f32 v226, v219, v41\n"
        "v_cvt_pk_bf16_f32 v218, v220, v42\n"
        "v_cvt_pk_bf16_f32 v202, v221, v43\n"
        "v_cvt_pk_bf16_f32 v223, v106, v252\n"
        "v_cvt_pk_bf16_f32 v227, v107, v253\n"
        "v_cvt_pk_bf16_f32 v224, v230, v214\n"
        "buffer_store_dwordx3 v[222:224], v15, s[40:43], s90 offen offset:768 sc1 nt\n"
        "s_add_u32 s46, s89, 590016\n"
        "v_cvt_pk_bf16_f32 v219, v108, v254\n"
        "v_cvt_pk_bf16_f32 v203, v109, v255\n"
        "v_cvt_pk_bf16_f32 v228, v231, v215\n"
        "s_add_u32 s91, s89, 490560\n"
        "v_cvt_pk_bf16_f32 v220, v232, v216\n"
        "buffer_store_dwordx3 v[226:228], v14, s[40:43], s91 offen offset:192 sc1 nt\n"
        "s_min_u32 s46, s46, s42\n"
        "v_cvt_pk_bf16_f32 v204, v233, v217\n"
        "v_cvt_pk_bf16_f32 v226, v146, v147\n"
        "v_cvt_pk_bf16_f32 v227, v148, v149\n"
        "v_cvt_pk_bf16_f32 v228, v250, v251\n"
        "buffer_store_dwordx3 v[218:220], v16, s[40:43], s91 offen offset:960 sc1 nt\n"
        "s_add_u32 s90, s89, 588672\n"
        "buffer_store_dwordx3 v[202:204], v14, s[40:43], s90 offen offset:384 sc1 nt\n"
        "buffer_store_dwordx3 v[226:228], v17, s[44:47], s90 offen offset:1152 sc1 nt\n"
        "FIN_%=:\n"
        "s_waitcnt lgkmcnt(0)\n"
        "s_barrier\n"
        "v_mov_b32 v27, 163432\n"
        "ds_read_b32 v27, v27\n"
        "s_waitcnt lgkmcnt(0)\n"
        "v_readfirstlane_b32 s50, v27\n"
        "s_nop 3\n"
        "s_cmp_lt_u32 s50, 5329\n"
        "s_cbranch_scc1 POST_%=\n"
        ".Lswin_fin:\n"
        "s_endpgm\n"
        "POST_%=:\n"
        "s_barrier\n"
        "s_mul_i32 s87, s50, 1344\n"
        "s_mul_i32 s92, s50, 57457\n"
        "s_lshr_b32 s92, s92, 22\n"
        "s_mul_i32 s92, s92, 588672\n"
        "s_add_u32 s87, s87, s92\n"
        "s_lshr_b32 s23, s15, 2\n"
        "s_add_u32 s23, s23, 20\n"
        "s_sub_u32 s23, s23, s53\n"
        "s_mul_i32 s23, s23, 1584\n"
        "s_mov_b32 s22, 0\n"
        "s_mov_b32 s48, 0x10000\n"
        "s_mov_b32 s18, 0\n"
        "s_add_u32 m0, s52, 0\n"
        "s_nop 0\n"
        "buffer_load_dwordx4 v0, s[36:39], s87 offen lds\n"
        "buffer_load_dwordx4 v1, s[36:39], s87 offen offset:1024 lds\n"
        "buffer_load_dwordx4 v2, s[36:39], s87 offen offset:2048 lds\n"
        "buffer_load_dwordx4 v3, s[36:39], s87 offen offset:3072 lds\n"
        "s_add_u32 m0, s52, 4096\n"
        "s_nop 0\n"
        "buffer_load_dwordx4 v4, s[36:39], s87 offen lds\n"
        "buffer_load_dwordx4 v5, s[36:39], s87 offen offset:1024 lds\n"
        "buffer_load_dwordx4 v6, s[36:39], s87 offen offset:2048 lds\n"
        "buffer_load_dwordx4 v7, s[36:39], s87 offen offset:3072 lds\n"
        "s_add_u32 m0, s52, 8192\n"
        "s_nop 0\n"
        "buffer_load_dwordx4 v8, s[36:39], s87 offen lds\n"
        "s_waitcnt vmcnt(0)\n"
        "ds_read_b128 v[126:129], v9\n"
        "ds_read_b128 v[132:135], v10\n"
        "ds_read_b128 v[138:141], v11\n"
        "ds_read_b128 v[106:109], v9 offset:3072\n"
        "ds_read_b128 v[112:115], v10 offset:3072\n"
        "ds_read_b128 v[118:121], v11 offset:3072\n"
        "ds_read_b128 v[146:149], v9 offset:6144\n"
        "ds_read_b128 v[152:155], v10 offset:6144\n"
        "ds_read_b128 v[158:161], v11 offset:6144\n"
        "s_mov_b32 s86, 0x80000000\n"
        "s_mov_b32 s89, 0x80000000\n"
        "s_mov_b32 s85, 0\n"
        "s_sub_u32 s29, s29, 1\n"
        "s_waitcnt lgkmcnt(0)\n"
        "s_nop 7\n"
        "s_branch TOP_%=\n"
        "SLOW_%=:\n"
        "s_waitcnt lgkmcnt(0)\n"
        "s_nop 15\n"
        "v_mbcnt_lo_u32_b32 v255, -1, 0\n"
        "v_mbcnt_hi_u32_b32 v255, -1, v255\n"
        "v_and_b32 v254, 15, v255\n"
        "v_mul_u32_u24 v252, 24, v254\n"
        "v_sub_u32 v253, v24, v252\n"
        "v_lshrrev_b32 v250, 4, v255\n"
        "v_mul_u32_u24 v252, 96, v250\n"
        "v_add_u32 v253, v253, v252\n"
        "ds_read_b64 v[132:133], v253\n"
        "ds_read_b64 v[30:31], v253 offset:24\n"
        "ds_read_b64 v[34:35], v253 offset:48\n"
        "ds_read_b64 v[46:47], v253 offset:72\n"
        "ds_read_b64 v[40:41], v253 offset:8\n"
        "ds_read_b64 v[136:137], v253 offset:32\n"
        "ds_read_b64 v[104:105], v253 offset:56\n"
        "ds_read_b64 v[144:145], v253 offset:80\n"
        "ds_read_b64 v[124:125], v253 offset:16\n"
        "ds_read_b64 v[116:117], v253 offset:40\n"
        "ds_read_b64 v[108:109], v253 offset:64\n"
        "ds_read_b64 v[140:141], v253 offset:88\n"
        "v_mfma_f32_16x16x32_bf16 v[120:123], v[194:197], v[194:197], 0\n"
        "v_mov_b32 v253, 0x05040100\n"
        "v_subrev_u32 v248, 159328, v20\n"
        "v_mfma_f32_16x16x32_bf16 v[120:123], v[206:209], v[206:209], v[120:123]\n"
        "v_mov_b32 v249, 161888\n"
        "v_mov_b32 v166, 0x3f803f80\n"
        "v_mfma_f32_16x16x32_bf16 v[120:123], v[210:213], v[210:213], v[120:123]\n"
        "v_mov_b32 v167, 0x3f803f80\n"
        "v_mov_b32 v168, 0x3f803f80\n"
        "v_mov_b32 v169, 0x3f803f80\n"
        "s_nop 1\n"
        "v_mfma_f32_16x16x32_bf16 v[112:115], v[166:169], v[210:213], 0\n"
        "v_mov_b32 v255, 0xff800000\n"
        "v_mov_b32 v139, 0\n"
        "v_mfma_f32_16x16x32_bf16 v[112:115], v[166:169], v[206:209], v[112:115]\n"
        "s_waitcnt lgkmcnt(11)\n"
        "v_mov_b32 v43, 0\n"
        "v_mov_b32 v143, 0\n"
        "v_mfma_f32_16x16x32_bf16 v[112:115], v[166:169], v[194:197], v[112:115]\n"
        "s_waitcnt lgkmcnt(10)\n"
        "v_mov_b32 v119, 0\n"
        "v_mov_b32 v107, 0\n"
        "v_mfma_f32_16x16x32_bf16 v[128:131], v[222:225], v[222:225], 0\n"
        "s_waitcnt lgkmcnt(9)\n"
        "v_mov_b32 v37, 0\n"
        "v_mov_b32 v111, 0\n"
        "v_mfma_f32_16x16x32_bf16 v[128:131], v[226:229], v[226:229], v[128:131]\n"
        "s_waitcnt lgkmcnt(8)\n"
        "v_mov_b32 v147, 0\n"
        "v_mov_b32 v135, 0\n"
        "v_mfma_f32_16x16x32_bf16 v[128:131], v[218:221], v[218:221], v[128:131]\n"
        "s_waitcnt lgkmcnt(7)\n"
        "v_mov_b32 v127, 0\n"
        "v_and_b32 v248, 384, v248\n"
        "v_mfma_f32_16x16x32_bf16 v[162:165], v[166:169], v[218:221], 0\n"
        "s_waitcnt lgkmcnt(6)\n"
        "v_lshlrev_b32 v248, 1, v248\n"
        "v_add_u32 v248, 160864, v248\n"
        "v_mfma_f32_16x16x32_bf16 v[162:165], v[166:169], v[226:229], v[162:165]\n"
        "s_waitcnt lgkmcnt(5)\n"
        "v_lshlrev_b32 v251, 2, v254\n"
        "v_lshlrev_b32 v252, 4, v250\n"
        "v_mfma_f32_16x16x32_bf16 v[162:165], v[166:169], v[222:225], v[162:165]\n"
        "s_waitcnt lgkmcnt(4)\n"
        "v_add_u32 v251, v251, v248\n"
        "v_add_u32 v252, v252, v248\n"
        "v_mfma_f32_16x16x32_bf16 v[152:155], v[198:201], v[198:201], 0\n"
        "s_waitcnt lgkmcnt(3)\n"
        "v_cndmask_b32 v249, v249, v251, vcc\n"
        "v_cndmask_b32_e64 v254, v120, v121, s[56:57]\n"
        "v_mfma_f32_16x16x32_bf16 v[152:155], v[214:217], v[214:217], v[152:155]\n"
        "s_waitcnt lgkmcnt(2)\n"
        "v_cndmask_b32_e64 v254, v254, v122, s[58:59]\n"
        "v_cndmask_b32_e64 v254, v254, v123, s[60:61]\n"
        "v_mfma_f32_16x16x32_bf16 v[152:155], v[202:205], v[202:205], v[152:155]\n"
        "s_waitcnt lgkmcnt(1)\n"
        "v_mul_f32 v254, s80, v254\n"
        "v_mul_f32 v250, s80, v112\n"
        "v_mfma_f32_16x16x32_bf16 v[156:159], v[166:169], v[202:205], 0\n"
        "s_waitcnt lgkmcnt(0)\n"
        "v_fma_f32 v250, -v250, v250, v254\n"
        "v_max_f32 v250, 0, v250\n"
        "v_mfma_f32_16x16x32_bf16 v[156:159], v[166:169], v[214:217], v[156:159]\n"
        "v_add_f32 v250, s81, v250\n"
        "v_perm_b32 v120, v133, v132, v253\n"
        "v_mfma_f32_16x16x32_bf16 v[156:159], v[166:169], v[198:201], v[156:159]\n"
        "v_rsq_f32 v250, v250\n"
        "v_perm_b32 v121, v31, v30, v253\n"
        "v_perm_b32 v122, v35, v34, v253\n"
        "v_perm_b32 v123, v47, v46, v253\n"
        "s_nop 1\n"
        "v_mfma_f32_16x16x32_bf16 v[170:173], v[120:123], v[120:123], 0\n"
        "ds_write_b32 v249, v250\n"
        "v_perm_b32 v112, v41, v40, v253\n"
        "v_perm_b32 v113, v137, v136, v253\n"
        "v_perm_b32 v114, v105, v104, v253\n"
        "v_perm_b32 v115, v145, v144, v253\n"
        "s_nop 1\n"
        "v_mfma_f32_16x16x32_bf16 v[170:173], v[112:115], v[112:115], v[170:173]\n"
        "v_perm_b32 v148, v125, v124, v253\n"
        "v_perm_b32 v149, v117, v116, v253\n"
        "v_perm_b32 v150, v109, v108, v253\n"
        "v_perm_b32 v151, v141, v140, v253\n"
        "s_nop 1\n"
        "v_mfma_f32_16x16x32_bf16 v[170:173], v[148:151], v[148:151], v[170:173]\n"
        "v_cndmask_b32_e64 v254, v128, v129, s[56:57]\n"
        "v_cndmask_b32_e64 v254, v254, v130, s[58:59]\n"
        "v_mfma_f32_16x16x32_bf16 v[158:161], v[166:169], v[148:151], 0\n"
        "v_cndmask_b32_e64 v254, v254, v131, s[60:61]\n"
        "v_mul_f32 v254, s80, v254\n"
        "v_mfma_f32_16x16x32_bf16 v[158:161], v[166:169], v[112:115], v[158:161]\n"
        "v_mul_f32 v253, s80, v162\n"
        "v_fma_f32 v253, -v253, v253, v254\n"
        "v_mfma_f32_16x16x32_bf16 v[158:161], v[166:169], v[120:123], v[158:161]\n"
        "v_max_f32 v253, 0, v253\n"
        "v_add_f32 v253, s81, v253\n"
        "v_mfma_f32_16x16x32_bf16 v[130:133], v[194:197], a[216:219], 0\n"
        "v_rsq_f32 v253, v253\n"
        "v_mfma_f32_16x16x32_bf16 v[130:133], v[206:209], a[220:223], v[130:133]\n"
        "ds_write_b32 v249, v253 offset:64\n"
        "v_mfma_f32_16x16x32_bf16 v[130:133], v[210:213], a[224:227], v[130:133]\n"
        "v_cndmask_b32_e64 v254, v152, v153, s[56:57]\n"
        "v_cndmask_b32_e64 v254, v254, v154, s[58:59]\n"
        "v_mfma_f32_16x16x32_bf16 v[164:167], v[218:221], a[224:227], 0\n"
        "v_cndmask_b32_e64 v254, v254, v155, s[60:61]\n"
        "v_mul_f32 v254, s80, v254\n"
        "v_mfma_f32_16x16x32_bf16 v[164:167], v[226:229], a[220:223], v[164:167]\n"
        "v_mul_f32 v253, s80, v156\n"
        "v_fma_f32 v253, -v253, v253, v254\n"
        "v_mfma_f32_16x16x32_bf16 v[164:167], v[222:225], a[216:219], v[164:167]\n"
        "v_max_f32 v253, 0, v253\n"
        "v_add_f32 v253, s81, v253\n"
        "v_mfma_f32_16x16x32_bf16 v[160:163], v[198:201], a[216:219], 0\n"
        "v_rsq_f32 v253, v253\n"
        "v_mfma_f32_16x16x32_bf16 v[160:163], v[214:217], a[220:223], v[160:163]\n"
        "ds_write_b32 v249, v253 offset:128\n"
        "v_mfma_f32_16x16x32_bf16 v[160:163], v[202:205], a[224:227], v[160:163]\n"
        "v_cndmask_b32_e64 v253, v170, v171, s[56:57]\n"
        "v_cndmask_b32_e64 v253, v253, v172, s[58:59]\n"
        "v_mfma_f32_16x16x32_bf16 v[152:155], v[148:151], a[224:227], 0\n"
        "v_cndmask_b32_e64 v253, v253, v173, s[60:61]\n"
        "v_mul_f32 v253, s80, v253\n"
        "v_mfma_f32_16x16x32_bf16 v[152:155], v[112:115], a[220:223], v[152:155]\n"
        "v_mul_f32 v254, s80, v158\n"
        "v_fma_f32 v254, -v254, v254, v253\n"
        "v_mfma_f32_16x16x32_bf16 v[152:155], v[120:123], a[216:219], v[152:155]\n"
        "v_max_f32 v254, 0, v254\n"
        "v_add_f32 v254, s81, v254\n"
        "v_mfma_f32_16x16x32_bf16 v[156:159], v[198:201], a[228:231], 0\n"
        "v_rsq_f32 v254, v254\n"
        "v_mfma_f32_16x16x32_bf16 v[156:159], v[214:217], a[232:235], v[156:159]\n"
        "ds_write_b32 v249, v254 offset:192\n"
        "v_mfma_f32_16x16x32_bf16 v[156:159], v[202:205], a[236:239], v[156:159]\n"
        "v_cvt_pk_bf16_f32 v128, v130, v131\n"
        "v_cvt_pk_bf16_f32 v129, v132, v133\n"
        "v_mfma_f32_16x16x32_bf16 v[168:171], v[218:221], a[236:239], 0\n"
        "v_cvt_pk_bf16_f32 v130, v164, v165\n"
        "v_cvt_pk_bf16_f32 v131, v166, v167\n"
        "v_mfma_f32_16x16x32_bf16 v[168:171], v[226:229], a[232:235], v[168:171]\n"
        "v_cvt_pk_bf16_f32 v136, v160, v161\n"
        "v_cvt_pk_bf16_f32 v137, v162, v163\n"
        "v_mfma_f32_16x16x32_bf16 v[168:171], v[222:225], a[228:231], v[168:171]\n"
        "s_nop 0\n"
        "v_cvt_pk_bf16_f32 v138, v152, 0\n"
        "v_cvt_pk_bf16_f32 v40, v156, v157\n"
        "v_mfma_f32_16x16x32_bf16 v[160:163], v[194:197], a[228:231], 0\n"
        "s_nop 0\n"
        "v_cvt_pk_bf16_f32 v41, v158, v159\n"
        "v_mfma_f32_16x16x32_bf16 v[160:163], v[206:209], a[232:235], v[160:163]\n"
        "s_nop 0\n"
        "v_cvt_pk_bf16_f32 v154, v168, v169\n"
        "v_cvt_pk_bf16_f32 v155, v170, v171\n"
        "v_mfma_f32_16x16x32_bf16 v[160:163], v[210:213], a[236:239], v[160:163]\n"
        "s_nop 1\n"
        "v_mfma_f32_16x16x32_bf16 v[156:159], v[148:151], a[236:239], 0\n"
        "s_nop 2\n"
        "v_mfma_f32_16x16x32_bf16 v[156:159], v[112:115], a[232:235], v[156:159]\n"
        "s_nop 0\n"
        "v_cvt_pk_bf16_f32 v152, v160, v161\n"
        "v_cvt_pk_bf16_f32 v153, v162, v163\n"
        "v_mfma_f32_16x16x32_bf16 v[156:159], v[120:123], a[228:231], v[156:159]\n"
        "s_nop 1\n"
        "v_mfma_f32_16x16x32_bf16 v[162:165], v[194:197], a[240:243], 0\n"
        "s_nop 2\n"
        "v_mfma_f32_16x16x32_bf16 v[162:165], v[206:209], a[244:247], v[162:165]\n"
        "s_nop 0\n"
        "v_cvt_pk_bf16_f32 v42, v156, 0\n"
        "v_mfma_f32_16x16x32_bf16 v[162:165], v[210:213], a[248:251], v[162:165]\n"
        "s_nop 1\n"
        "v_mfma_f32_16x16x32_bf16 v[166:169], v[218:221], a[248:251], 0\n"
        "s_nop 2\n"
        "v_mfma_f32_16x16x32_bf16 v[166:169], v[226:229], a[244:247], v[166:169]\n"
        "s_nop 0\n"
        "v_cvt_pk_bf16_f32 v160, v162, v163\n"
        "v_cvt_pk_bf16_f32 v161, v164, v165\n"
        "v_mfma_f32_16x16x32_bf16 v[166:169], v[222:225], a[240:243], v[166:169]\n"
        "s_nop 1\n"
        "v_mfma_f32_16x16x32_bf16 v[156:159], v[198:201], a[240:243], 0\n"
        "s_nop 2\n"
        "v_mfma_f32_16x16x32_bf16 v[156:159], v[214:217], a[244:247], v[156:159]\n"
        "s_nop 0\n"
        "v_cvt_pk_bf16_f32 v162, v166, v167\n"
        "v_cvt_pk_bf16_f32 v163, v168, v169\n"
        "v_mfma_f32_16x16x32_bf16 v[156:159], v[202:205], a[248:251], v[156:159]\n"
        "s_nop 1\n"
        "v_mfma_f32_16x16x32_bf16 v[164:167], v[148:151], a[248:251], 0\n"
        "s_nop 2\n"
        "v_mfma_f32_16x16x32_bf16 v[164:167], v[112:115], a[244:247], v[164:167]\n"
        "s_nop 0\n"
        "v_cvt_pk_bf16_f32 v140, v156, v157\n"
        "v_cvt_pk_bf16_f32 v141, v158, v159\n"
        "v_mfma_f32_16x16x32_bf16 v[164:167], v[120:123], a[240:243], v[164:167]\n"
        "s_nop 1\n"
        "v_mfma_f32_16x16x32_bf16 v[156:159], v[198:201], a[252:255], 0\n"
        "s_nop 2\n"
        "v_mfma_f32_16x16x32_bf16 v[156:159], v[214:217], v[76:79], v[156:159]\n"
        "s_nop 0\n"
        "v_cvt_pk_bf16_f32 v142, v164, 0\n"
        "v_mfma_f32_16x16x32_bf16 v[156:159], v[202:205], v[96:99], v[156:159]\n"
        "s_nop 1\n"
        "v_mfma_f32_16x16x32_bf16 v[164:167], v[218:221], v[96:99], 0\n"
        "s_nop 2\n"
        "v_mfma_f32_16x16x32_bf16 v[164:167], v[226:229], v[76:79], v[164:167]\n"
        "s_nop 0\n"
        "v_cvt_pk_bf16_f32 v116, v156, v157\n"
        "v_cvt_pk_bf16_f32 v117, v158, v159\n"
        "v_mfma_f32_16x16x32_bf16 v[164:167], v[222:225], a[252:255], v[164:167]\n"
        "s_nop 1\n"
        "v_mfma_f32_16x16x32_bf16 v[168:171], v[194:197], a[252:255], 0\n"
        "s_nop 2\n"
        "v_mfma_f32_16x16x32_bf16 v[168:171], v[206:209], v[76:79], v[168:171]\n"
        "s_nop 0\n"
        "v_cvt_pk_bf16_f32 v158, v164, v165\n"
        "v_cvt_pk_bf16_f32 v159, v166, v167\n"
        "v_mfma_f32_16x16x32_bf16 v[168:171], v[210:213], v[96:99], v[168:171]\n"
        "s_nop 1\n"
        "v_mfma_f32_16x16x32_bf16 v[164:167], v[148:151], v[96:99], 0\n"
        "s_nop 2\n"
        "v_mfma_f32_16x16x32_bf16 v[164:167], v[112:115], v[76:79], v[164:167]\n"
        "s_nop 0\n"
        "v_cvt_pk_bf16_f32 v156, v168, v169\n"
        "v_cvt_pk_bf16_f32 v157, v170, v171\n"
        "v_mfma_f32_16x16x32_bf16 v[164:167], v[120:123], a[252:255], v[164:167]\n"
        "s_nop 1\n"
        "v_mfma_f32_16x16x32_bf16 v[168:171], v[194:197], v[64:67], 0\n"
        "s_nop 2\n"
        "v_mfma_f32_16x16x32_bf16 v[168:171], v[206:209], v[60:63], v[168:171]\n"
        "s_nop 0\n"
        "v_cvt_pk_bf16_f32 v118, v164, 0\n"
        "v_mfma_f32_16x16x32_bf16 v[168:171], v[210:213], v[72:75], v[168:171]\n"
        "s_nop 1\n"
        "v_mfma_f32_16x16x32_bf16 v[176:179], v[218:221], v[72:75], 0\n"
        "s_nop 2\n"
        "v_mfma_f32_16x16x32_bf16 v[176:179], v[226:229], v[60:63], v[176:179]\n"
        "s_nop 0\n"
        "v_cvt_pk_bf16_f32 v164, v168, v169\n"
        "v_cvt_pk_bf16_f32 v165, v170, v171\n"
        "v_mfma_f32_16x16x32_bf16 v[176:179], v[222:225], v[64:67], v[176:179]\n"
        "s_nop 1\n"
        "v_mfma_f32_16x16x32_bf16 v[172:175], v[198:201], v[64:67], 0\n"
        "s_nop 2\n"
        "v_mfma_f32_16x16x32_bf16 v[172:175], v[214:217], v[60:63], v[172:175]\n"
        "s_nop 0\n"
        "v_cvt_pk_bf16_f32 v166, v176, v177\n"
        "v_cvt_pk_bf16_f32 v167, v178, v179\n"
        "v_mfma_f32_16x16x32_bf16 v[172:175], v[202:205], v[72:75], v[172:175]\n"
        "s_nop 1\n"
        "v_mfma_f32_16x16x32_bf16 v[168:171], v[148:151], v[72:75], 0\n"
        "s_nop 2\n"
        "v_mfma_f32_16x16x32_bf16 v[168:171], v[112:115], v[60:63], v[168:171]\n"
        "s_nop 0\n"
        "v_cvt_pk_bf16_f32 v104, v172, v173\n"
        "v_cvt_pk_bf16_f32 v105, v174, v175\n"
        "v_mfma_f32_16x16x32_bf16 v[168:171], v[120:123], v[64:67], v[168:171]\n"
        "s_nop 1\n"
        "v_mfma_f32_16x16x32_bf16 v[172:175], v[198:201], v[92:95], 0\n"
        "s_nop 2\n"
        "v_mfma_f32_16x16x32_bf16 v[172:175], v[214:217], v[80:83], v[172:175]\n"
        "s_nop 0\n"
        "v_cvt_pk_bf16_f32 v106, v168, 0\n"
        "v_mfma_f32_16x16x32_bf16 v[172:175], v[202:205], v[88:91], v[172:175]\n"
        "v_mfma_f32_16x16x32_bf16 v[230:233], v[218:221], v[88:91], 0\n"
        "v_mfma_f32_16x16x32_bf16 v[230:233], v[226:229], v[80:83], v[230:233]\n"
        "ds_read_b128 v[168:171], v252\n"
        "ds_read_b128 v[176:179], v252 offset:64\n"
        "v_mfma_f32_16x16x32_bf16 v[230:233], v[222:225], v[92:95], v[230:233]\n"
        "ds_read_b128 v[180:183], v252 offset:128\n"
        "ds_read_b32 v250, v251\n"
        "v_mfma_f32_16x16x32_bf16 v[188:191], v[194:197], v[92:95], 0\n"
        "ds_read_b32 v252, v251 offset:64\n"
        "ds_read_b32 v254, v251 offset:128\n"
        "v_mfma_f32_16x16x32_bf16 v[188:191], v[206:209], v[80:83], v[188:191]\n"
        "ds_read_b32 v253, v251 offset:192\n"
        "v_cvt_pk_bf16_f32 v34, v172, v173\n"
        "v_mfma_f32_16x16x32_bf16 v[188:191], v[210:213], v[88:91], v[188:191]\n"
        "s_nop 0\n"
        "v_cvt_pk_bf16_f32 v35, v174, v175\n"
        "v_cvt_pk_bf16_f32 v174, v230, v231\n"
        "v_mfma_f32_16x16x32_bf16 v[184:187], v[148:151], v[88:91], 0\n"
        "s_nop 0\n"
        "v_cvt_pk_bf16_f32 v175, v232, v233\n"
        "v_mfma_f32_16x16x32_bf16 v[184:187], v[112:115], v[80:83], v[184:187]\n"
        "s_nop 0\n"
        "v_cvt_pk_bf16_f32 v172, v188, v189\n"
        "v_cvt_pk_bf16_f32 v173, v190, v191\n"
        "v_mfma_f32_16x16x32_bf16 v[184:187], v[120:123], v[92:95], v[184:187]\n"
        "s_nop 1\n"
        "v_mfma_f32_16x16x32_bf16 v[188:191], a[144:147], v[194:197], 0\n"
        "s_nop 2\n"
        "v_mfma_f32_16x16x32_bf16 v[188:191], a[148:151], v[206:209], v[188:191]\n"
        "s_nop 0\n"
        "v_cvt_pk_bf16_f32 v36, v184, 0\n"
        "v_mfma_f32_16x16x32_bf16 v[188:191], a[152:155], v[210:213], v[188:191]\n"
        "s_nop 1\n"
        "v_mfma_f32_16x16x32_bf16 v[184:187], a[164:167], v[210:213], 0\n"
        "s_waitcnt lgkmcnt(0)\n"
        "s_nop 1\n"
        "v_mfma_f32_16x16x32_bf16 v[184:187], a[160:163], v[206:209], v[184:187]\n"
        "s_nop 0\n"
        "v_cvt_pk_bf16_f32 v238, v188, v189\n"
        "v_cvt_pk_bf16_f32 v239, v190, v191\n"
        "v_mfma_f32_16x16x32_bf16 v[184:187], a[156:159], v[194:197], v[184:187]\n"
        "s_nop 1\n"
        "v_mfma_f32_16x16x32_bf16 v[188:191], a[168:171], v[194:197], 0\n"
        "s_nop 2\n"
        "v_mfma_f32_16x16x32_bf16 v[188:191], a[172:175], v[206:209], v[188:191]\n"
        "s_nop 0\n"
        "v_cvt_pk_bf16_f32 v240, v184, v185\n"
        "v_cvt_pk_bf16_f32 v241, v186, v187\n"
        "v_mfma_f32_16x16x32_bf16 v[188:191], a[176:179], v[210:213], v[188:191]\n"
        "s_nop 1\n"
        "v_mfma_f32_16x16x32_bf16 v[184:187], a[188:191], v[210:213], 0\n"
        "s_nop 2\n"
        "v_mfma_f32_16x16x32_bf16 v[184:187], a[184:187], v[206:209], v[184:187]\n"
        "s_nop 0\n"
        "v_cvt_pk_bf16_f32 v234, v188, v189\n"
        "v_cvt_pk_bf16_f32 v235, v190, v191\n"
        "v_mfma_f32_16x16x32_bf16 v[184:187], a[180:183], v[194:197], v[184:187]\n"
        "s_nop 1\n"
        "v_mfma_f32_16x16x32_bf16 v[188:191], a[192:195], v[194:197], 0\n"
        "s_nop 2\n"
        "v_mfma_f32_16x16x32_bf16 v[188:191], a[196:199], v[206:209], v[188:191]\n"
        "s_nop 0\n"
        "v_cvt_pk_bf16_f32 v236, v184, v185\n"
        "v_cvt_pk_bf16_f32 v237, v186, v187\n"
        "v_mfma_f32_16x16x32_bf16 v[188:191], a[200:203], v[210:213], v[188:191]\n"
        "s_nop 1\n"
        "v_mfma_f32_16x16x32_bf16 v[230:233], a[212:215], v[210:213], 0\n"
        "s_nop 2\n"
        "v_mfma_f32_16x16x32_bf16 v[230:233], a[208:211], v[206:209], v[230:233]\n"
        "s_nop 0\n"
        "v_cvt_pk_bf16_f32 v184, v188, v189\n"
        "v_cvt_pk_bf16_f32 v185, v190, v191\n"
        "v_mfma_f32_16x16x32_bf16 v[230:233], a[204:207], v[194:197], v[230:233]\n"
        "s_nop 1\n"
        "v_mfma_f32_16x16x32_bf16 v[188:191], v[194:197], v[238:241], 0\n"
        "s_nop 2\n"
        "v_mfma_f32_16x16x32_bf16 v[188:191], v[206:209], v[234:237], v[188:191]\n"
        "s_nop 0\n"
        "v_cvt_pk_bf16_f32 v186, v230, v231\n"
        "v_cvt_pk_bf16_f32 v187, v232, v233\n"
        "s_nop 1\n"
        "v_mfma_f32_16x16x32_bf16 v[188:191], v[210:213], v[184:187], v[188:191]\n"
        "s_nop 1\n"
        "v_mfma_f32_16x16x32_bf16 v[230:233], v[218:221], v[184:187], 0\n"
        "s_nop 2\n"
        "v_mfma_f32_16x16x32_bf16 v[230:233], v[226:229], v[234:237], v[230:233]\n"
        "s_nop 0\n"
        "v_mul_f32 v188, v188, v168\n"
        "v_mul_f32 v189, v189, v169\n"
        "v_mfma_f32_16x16x32_bf16 v[230:233], v[222:225], v[238:241], v[230:233]\n"
        "s_nop 0\n"
        "v_mul_f32 v190, v190, v170\n"
        "v_mul_f32 v191, v191, v171\n"
        "v_mfma_f32_16x16x32_bf16 v[242:245], v[198:201], v[238:241], 0\n"
        "s_nop 0\n"
        "v_max3_f32 v251, v188, v189, v190\n"
        "v_mfma_f32_16x16x32_bf16 v[242:245], v[214:217], v[234:237], v[242:245]\n"
        "s_nop 0\n"
        "v_mul_f32 v230, v230, v176\n"
        "v_mul_f32 v231, v231, v177\n"
        "v_mfma_f32_16x16x32_bf16 v[242:245], v[202:205], v[184:187], v[242:245]\n"
        "v_mul_f32 v232, v232, v178\n"
        "v_mul_f32 v233, v233, v179\n"
        "v_mfma_f32_16x16x32_bf16 v[246:249], v[148:151], v[184:187], 0\n"
        "s_nop 0\n"
        "v_max3_f32 v251, v251, v191, v230\n"
        "v_max3_f32 v251, v251, v231, v232\n"
        "v_mfma_f32_16x16x32_bf16 v[246:249], v[112:115], v[234:237], v[246:249]\n"
        "s_nop 0\n"
        "v_mul_f32 v242, v242, v180\n"
        "v_mul_f32 v243, v243, v181\n"
        "v_mfma_f32_16x16x32_bf16 v[246:249], v[120:123], v[238:241], v[246:249]\n"
        "s_nop 0\n"
        "v_mul_f32 v244, v244, v182\n"
        "v_mul_f32 v245, v245, v183\n"
        "v_max3_f32 v251, v251, v233, v242\n"
        "v_max3_f32 v251, v251, v243, v244\n"
        "s_nop 2\n"
        "v_mul_f32 v246, v246, v253\n"
        "v_cndmask_b32_e64 v246, v255, v246, s[62:63]\n"
        "v_max3_f32 v251, v251, v245, v246\n"
        "v_mov_b32 v249, v251\n"
        "s_nop 1\n"
        "v_permlane32_swap_b32 v251, v249\n"
        "v_max_f32 v251, v251, v249\n"
        "v_mov_b32 v249, v251\n"
        "s_nop 1\n"
        "v_permlane16_swap_b32 v251, v249\n"
        "v_max_f32 v251, v251, v249\n"
        "v_mul_f32 v251, v251, v250\n"
        "v_fma_f32 v188, v188, v250, -v251\n"
        "v_exp_f32 v188, v188\n"
        "v_fma_f32 v189, v189, v250, -v251\n"
        "v_exp_f32 v189, v189\n"
        "s_nop 0\n"
        "v_add_f32 v249, v188, v189\n"
        "v_mul_f32 v188, v188, v168\n"
        "v_fma_f32 v246, v246, v250, -v251\n"
        "v_exp_f32 v246, v246\n"
        "v_fma_f32 v245, v245, v250, -v251\n"
        "v_exp_f32 v245, v245\n"
        "v_fma_f32 v244, v244, v250, -v251\n"
        "v_exp_f32 v244, v244\n"
        "v_fma_f32 v243, v243, v250, -v251\n"
        "v_exp_f32 v243, v243\n"
        "v_fma_f32 v242, v242, v250, -v251\n"
        "v_exp_f32 v242, v242\n"
        "v_fma_f32 v233, v233, v250, -v251\n"
        "v_exp_f32 v233, v233\n"
        "v_fma_f32 v232, v232, v250, -v251\n"
        "v_exp_f32 v232, v232\n"
        "v_fma_f32 v231, v231, v250, -v251\n"
        "v_exp_f32 v231, v231\n"
        "v_fma_f32 v230, v230, v250, -v251\n"
        "v_exp_f32 v230, v230\n"
        "v_fma_f32 v191, v191, v250, -v251\n"
        "v_exp_f32 v191, v191\n"
        "v_fma_f32 v190, v190, v250, -v251\n"
        "v_exp_f32 v190, v190\n"
        "s_nop 0\n"
        "v_add_f32 v249, v249, v190\n"
        "v_add_f32 v249, v249, v191\n"
        "v_add_f32 v249, v249, v230\n"
        "v_add_f32 v249, v249, v231\n"
        "v_add_f32 v249, v249, v232\n"
        "v_add_f32 v249, v249, v233\n"
        "v_add_f32 v249, v249, v242\n"
        "v_add_f32 v249, v249, v243\n"
        "v_add_f32 v249, v249, v244\n"
        "v_add_f32 v249, v249, v245\n"
        "v_add_f32 v249, v249, v246\n"
        "v_mov_b32 v251, v249\n"
        "s_nop 1\n"
        "v_permlane32_swap_b32 v249, v251\n"
        "v_add_f32 v249, v249, v251\n"
        "v_mov_b32 v251, v249\n"
        "s_nop 1\n"
        "v_permlane16_swap_b32 v249, v251\n"
        "v_add_f32 v249, v249, v251\n"
        "v_rcp_f32 v249, v249\n"
        "s_nop 0\n"
        "v_mul_f32 v188, v188, v249\n"
        "v_mul_f32 v189, v189, v169\n"
        "v_mul_f32 v189, v189, v249\n"
        "v_cvt_pk_bf16_f32 v234, v188, v189\n"
        "v_mul_f32 v190, v190, v170\n"
        "v_mul_f32 v190, v190, v249\n"
        "v_mul_f32 v191, v191, v171\n"
        "v_mul_f32 v191, v191, v249\n"
        "v_cvt_pk_bf16_f32 v235, v190, v191\n"
        "v_mul_f32 v230, v230, v176\n"
        "v_mul_f32 v230, v230, v249\n"
        "v_mul_f32 v231, v231, v177\n"
        "v_mul_f32 v231, v231, v249\n"
        "v_cvt_pk_bf16_f32 v236, v230, v231\n"
        "v_mul_f32 v232, v232, v178\n"
        "v_mul_f32 v232, v232, v249\n"
        "v_mul_f32 v233, v233, v179\n"
        "v_mul_f32 v233, v233, v249\n"
        "v_cvt_pk_bf16_f32 v237, v232, v233\n"
        "s_nop 1\n"
        "v_mfma_f32_16x16x32_bf16 v[184:187], v[234:237], v[128:131], 0\n"
        "v_mul_f32 v242, v242, v180\n"
        "v_mul_f32 v243, v243, v181\n"
        "v_mul_f32 v242, v242, v249\n"
        "v_mul_f32 v243, v243, v249\n"
        "v_cvt_pk_bf16_f32 v108, v242, v243\n"
        "v_mul_f32 v244, v244, v182\n"
        "v_mul_f32 v244, v244, v249\n"
        "v_mul_f32 v245, v245, v183\n"
        "v_mul_f32 v245, v245, v249\n"
        "v_cvt_pk_bf16_f32 v109, v244, v245\n"
        "v_mul_f32 v246, v246, v253\n"
        "v_mul_f32 v246, v246, v249\n"
        "v_cvt_pk_bf16_f32 v110, v246, 0\n"
        "s_nop 1\n"
        "v_mfma_f32_16x16x32_bf16 v[184:187], v[108:111], v[136:139], v[184:187]\n"
        "v_mfma_f32_16x16x32_bf16 v[184:187], v[194:197], v[68:71], v[184:187]\n"
        "v_mfma_f32_16x16x32_bf16 v[188:191], v[194:197], v[100:103], 0\n"
        "v_mfma_f32_16x16x32_bf16 v[188:191], v[108:111], v[40:43], v[188:191]\n"
        "v_mfma_f32_16x16x32_bf16 v[188:191], v[234:237], v[152:155], v[188:191]\n"
        "s_add_u32 s90, s89, 0\n"
        "s_nop 0\n"
        "v_mfma_f32_16x16x32_bf16 v[230:233], v[234:237], v[160:163], 0\n"
        "s_nop 2\n"
        "v_mfma_f32_16x16x32_bf16 v[230:233], v[108:111], v[140:143], v[230:233]\n"
        "s_nop 0\n"
        "v_cvt_pk_bf16_f32 v124, v184, v188\n"
        "v_cvt_pk_bf16_f32 v238, v185, v189\n"
        "v_mfma_f32_16x16x32_bf16 v[230:233], v[206:209], v[68:71], v[230:233]\n"
        "v_cvt_pk_bf16_f32 v132, v186, v190\n"
        "v_cvt_pk_bf16_f32 v144, v187, v191\n"
        "v_mfma_f32_16x16x32_bf16 v[184:187], v[206:209], v[100:103], 0\n"
        "s_add_u32 s91, s89, 98112\n"
        "v_mfma_f32_16x16x32_bf16 v[184:187], v[108:111], v[116:119], v[184:187]\n"
        "v_mfma_f32_16x16x32_bf16 v[184:187], v[234:237], v[156:159], v[184:187]\n"
        "s_nop 1\n"
        "v_mfma_f32_16x16x32_bf16 v[188:191], v[234:237], v[164:167], 0\n"
        "s_nop 2\n"
        "v_mfma_f32_16x16x32_bf16 v[188:191], v[108:111], v[104:107], v[188:191]\n"
        "s_nop 0\n"
        "v_cvt_pk_bf16_f32 v125, v230, v184\n"
        "v_cvt_pk_bf16_f32 v239, v231, v185\n"
        "v_mfma_f32_16x16x32_bf16 v[188:191], v[210:213], v[68:71], v[188:191]\n"
        "v_cvt_pk_bf16_f32 v133, v232, v186\n"
        "v_cvt_pk_bf16_f32 v145, v233, v187\n"
        "v_mfma_f32_16x16x32_bf16 v[184:187], v[210:213], v[100:103], 0\n"
        "v_mfma_f32_16x16x32_bf16 v[184:187], v[108:111], v[34:37], v[184:187]\n"
        "v_mfma_f32_16x16x32_bf16 v[184:187], v[234:237], v[172:175], v[184:187]\n"
        "s_nop 1\n"
        "v_mfma_f32_16x16x32_bf16 v[108:111], a[144:147], v[222:225], 0\n"
        "s_nop 2\n"
        "v_mfma_f32_16x16x32_bf16 v[108:111], a[148:151], v[226:229], v[108:111]\n"
        "s_nop 0\n"
        "v_cvt_pk_bf16_f32 v126, v188, v184\n"
        "v_cvt_pk_bf16_f32 v240, v189, v185\n"
        "v_mfma_f32_16x16x32_bf16 v[108:111], a[152:155], v[218:221], v[108:111]\n"
        "s_nop 0\n"
        "v_cvt_pk_bf16_f32 v134, v190, v186\n"
        "v_cvt_pk_bf16_f32 v146, v191, v187\n"
        "v_mfma_f32_16x16x32_bf16 v[188:191], a[164:167], v[218:221], 0\n"
        "s_nop 1\n"
        "v_mfma_f32_16x16x32_bf16 v[188:191], a[160:163], v[226:229], v[188:191]\n"
        "s_nop 0\n"
        "v_cvt_pk_bf16_f32 v184, v108, v109\n"
        "v_cvt_pk_bf16_f32 v185, v110, v111\n"
        "v_mfma_f32_16x16x32_bf16 v[188:191], a[156:159], v[222:225], v[188:191]\n"
        "s_nop 0\n"
        "v_mfma_f32_16x16x32_bf16 v[108:111], a[168:171], v[222:225], 0\n"
        "buffer_store_dwordx3 v[124:126], v14, s[40:43], s90 offen sc1 nt\n"
        "s_nop 2\n"
        "v_mfma_f32_16x16x32_bf16 v[108:111], a[172:175], v[226:229], v[108:111]\n"
        "s_nop 0\n"
        "v_cvt_pk_bf16_f32 v186, v188, v189\n"
        "v_cvt_pk_bf16_f32 v187, v190, v191\n"
        "v_mfma_f32_16x16x32_bf16 v[108:111], a[176:179], v[218:221], v[108:111]\n"
        "buffer_store_dwordx3 v[238:240], v15, s[40:43], s90 offen offset:768 sc1 nt\n"
        "s_nop 0\n"
        "v_mfma_f32_16x16x32_bf16 v[234:237], a[188:191], v[218:221], 0\n"
        "s_nop 2\n"
        "v_mfma_f32_16x16x32_bf16 v[234:237], a[184:187], v[226:229], v[234:237]\n"
        "buffer_store_dwordx3 v[132:134], v14, s[40:43], s91 offen offset:192 sc1 nt\n"
        "v_cvt_pk_bf16_f32 v188, v108, v109\n"
        "v_cvt_pk_bf16_f32 v189, v110, v111\n"
        "v_mfma_f32_16x16x32_bf16 v[234:237], a[180:183], v[222:225], v[234:237]\n"
        "s_nop 0\n"
        "v_mfma_f32_16x16x32_bf16 v[230:233], a[192:195], v[222:225], 0\n"
        "buffer_store_dwordx3 v[144:146], v16, s[40:43], s91 offen offset:960 sc1 nt\n"
        "s_nop 2\n"
        "v_mfma_f32_16x16x32_bf16 v[230:233], a[196:199], v[226:229], v[230:233]\n"
        "s_nop 0\n"
        "v_cvt_pk_bf16_f32 v190, v234, v235\n"
        "v_cvt_pk_bf16_f32 v191, v236, v237\n"
        "v_mfma_f32_16x16x32_bf16 v[230:233], a[200:203], v[218:221], v[230:233]\n"
        "s_nop 1\n"
        "v_mfma_f32_16x16x32_bf16 v[108:111], a[212:215], v[218:221], 0\n"
        "s_nop 2\n"
        "v_mfma_f32_16x16x32_bf16 v[108:111], a[208:211], v[226:229], v[108:111]\n"
        "s_nop 0\n"
        "v_cvt_pk_bf16_f32 v234, v230, v231\n"
        "v_cvt_pk_bf16_f32 v235, v232, v233\n"
        "v_mfma_f32_16x16x32_bf16 v[108:111], a[204:207], v[222:225], v[108:111]\n"
        "s_nop 1\n"
        "v_mfma_f32_16x16x32_bf16 v[238:241], v[194:197], v[184:187], 0\n"
        "s_nop 2\n"
        "v_mfma_f32_16x16x32_bf16 v[238:241], v[206:209], v[188:191], v[238:241]\n"
        "s_nop 0\n"
        "v_cvt_pk_bf16_f32 v236, v108, v109\n"
        "v_cvt_pk_bf16_f32 v237, v110, v111\n"
        "s_nop 1\n"
        "v_mfma_f32_16x16x32_bf16 v[238:241], v[210:213], v[234:237], v[238:241]\n"
        "s_nop 1\n"
        "v_mfma_f32_16x16x32_bf16 v[108:111], v[218:221], v[234:237], 0\n"
        "s_nop 2\n"
        "v_mfma_f32_16x16x32_bf16 v[108:111], v[226:229], v[188:191], v[108:111]\n"
        "s_nop 0\n"
        "v_mul_f32 v238, v238, v168\n"
        "v_mul_f32 v239, v239, v169\n"
        "v_mfma_f32_16x16x32_bf16 v[108:111], v[222:225], v[184:187], v[108:111]\n"
        "s_nop 0\n"
        "v_mul_f32 v240, v240, v170\n"
        "v_mul_f32 v241, v241, v171\n"
        "v_mfma_f32_16x16x32_bf16 v[230:233], v[198:201], v[184:187], 0\n"
        "s_nop 0\n"
        "v_max3_f32 v250, v238, v239, v240\n"
        "v_mfma_f32_16x16x32_bf16 v[230:233], v[214:217], v[188:191], v[230:233]\n"
        "s_nop 0\n"
        "v_mul_f32 v108, v108, v176\n"
        "v_mul_f32 v109, v109, v177\n"
        "v_mfma_f32_16x16x32_bf16 v[230:233], v[202:205], v[234:237], v[230:233]\n"
        "v_mul_f32 v110, v110, v178\n"
        "v_mul_f32 v111, v111, v179\n"
        "v_mfma_f32_16x16x32_bf16 v[242:245], v[148:151], v[234:237], 0\n"
        "s_nop 0\n"
        "v_max3_f32 v250, v250, v241, v108\n"
        "v_max3_f32 v250, v250, v109, v110\n"
        "v_mfma_f32_16x16x32_bf16 v[242:245], v[112:115], v[188:191], v[242:245]\n"
        "s_nop 0\n"
        "v_mul_f32 v230, v230, v180\n"
        "v_mul_f32 v231, v231, v181\n"
        "v_mfma_f32_16x16x32_bf16 v[242:245], v[120:123], v[184:187], v[242:245]\n"
        "s_nop 0\n"
        "v_mul_f32 v232, v232, v182\n"
        "v_mul_f32 v233, v233, v183\n"
        "v_max3_f32 v250, v250, v111, v230\n"
        "v_max3_f32 v250, v250, v231, v232\n"
        "s_nop 2\n"
        "v_mul_f32 v242, v242, v253\n"
        "v_cndmask_b32_e64 v242, v255, v242, s[62:63]\n"
        "v_max3_f32 v250, v250, v233, v242\n"
        "v_mov_b32 v251, v250\n"
        "s_nop 1\n"
        "v_permlane32_swap_b32 v250, v251\n"
        "v_max_f32 v250, v250, v251\n"
        "v_mov_b32 v251, v250\n"
        "s_nop 1\n"
        "v_permlane16_swap_b32 v250, v251\n"
        "v_max_f32 v250, v250, v251\n"
        "v_mul_f32 v250, v250, v252\n"
        "v_fma_f32 v238, v238, v252, -v250\n"
        "v_exp_f32 v238, v238\n"
        "v_fma_f32 v239, v239, v252, -v250\n"
        "v_exp_f32 v239, v239\n"
        "s_nop 0\n"
        "v_add_f32 v251, v238, v239\n"
        "v_mul_f32 v238, v238, v168\n"
        "v_fma_f32 v242, v242, v252, -v250\n"
        "v_exp_f32 v242, v242\n"
        "v_fma_f32 v233, v233, v252, -v250\n"
        "v_exp_f32 v233, v233\n"
        "v_fma_f32 v232, v232, v252, -v250\n"
        "v_exp_f32 v232, v232\n"
        "v_fma_f32 v231, v231, v252, -v250\n"
        "v_exp_f32 v231, v231\n"
        "v_fma_f32 v230, v230, v252, -v250\n"
        "v_exp_f32 v230, v230\n"
        "v_fma_f32 v111, v111, v252, -v250\n"
        "v_exp_f32 v111, v111\n"
        "v_fma_f32 v110, v110, v252, -v250\n"
        "v_exp_f32 v110, v110\n"
        "v_fma_f32 v109, v109, v252, -v250\n"
        "v_exp_f32 v109, v109\n"
        "v_fma_f32 v108, v108, v252, -v250\n"
        "v_exp_f32 v108, v108\n"
        "v_fma_f32 v241, v241, v252, -v250\n"
        "v_exp_f32 v241, v241\n"
        "v_fma_f32 v240, v240, v252, -v250\n"
        "v_exp_f32 v240, v240\n"
        "s_nop 0\n"
        "v_add_f32 v251, v251, v240\n"
        "v_add_f32 v251, v251, v241\n"
        "v_add_f32 v251, v251, v108\n"
        "v_add_f32 v251, v251, v109\n"
        "v_add_f32 v251, v251, v110\n"
        "v_add_f32 v251, v251, v111\n"
        "v_add_f32 v251, v251, v230\n"
        "v_add_f32 v251, v251, v231\n"
        "v_add_f32 v251, v251, v232\n"
        "v_add_f32 v251, v251, v233\n"
        "v_add_f32 v251, v251, v242\n"
        "v_mov_b32 v252, v251\n"
        "s_nop 1\n"
        "v_permlane32_swap_b32 v251, v252\n"
        "v_add_f32 v251, v251, v252\n"
        "v_mov_b32 v252, v251\n"
        "s_nop 1\n"
        "v_permlane16_swap_b32 v251, v252\n"
        "v_add_f32 v251, v251, v252\n"
        "v_rcp_f32 v251, v251\n"
        "s_nop 0\n"
        "v_mul_f32 v238, v238, v251\n"
        "v_mul_f32 v239, v239, v169\n"
        "v_mul_f32 v239, v239, v251\n"
        "v_cvt_pk_bf16_f32 v188, v238, v239\n"
        "v_mul_f32 v240, v240, v170\n"
        "v_mul_f32 v240, v240, v251\n"
        "v_mul_f32 v241, v241, v171\n"
        "v_mul_f32 v241, v241, v251\n"
        "v_cvt_pk_bf16_f32 v189, v240, v241\n"
        "v_mul_f32 v108, v108, v176\n"
        "v_mul_f32 v108, v108, v251\n"
        "v_mul_f32 v109, v109, v177\n"
        "v_mul_f32 v109, v109, v251\n"
        "v_cvt_pk_bf16_f32 v190, v108, v109\n"
        "v_mul_f32 v110, v110, v178\n"
        "v_mul_f32 v110, v110, v251\n"
        "v_mul_f32 v111, v111, v179\n"
        "v_mul_f32 v111, v111, v251\n"
        "v_cvt_pk_bf16_f32 v191, v110, v111\n"
        "s_nop 1\n"
        "v_mfma_f32_16x16x32_bf16 v[108:111], v[188:191], v[128:131], 0\n"
        "v_mul_f32 v230, v230, v180\n"
        "v_mul_f32 v231, v231, v181\n"
        "v_mul_f32 v230, v230, v251\n"
        "v_mul_f32 v231, v231, v251\n"
        "v_cvt_pk_bf16_f32 v144, v230, v231\n"
        "v_mul_f32 v232, v232, v182\n"
        "v_mul_f32 v232, v232, v251\n"
        "v_mul_f32 v233, v233, v183\n"
        "v_mul_f32 v233, v233, v251\n"
        "v_cvt_pk_bf16_f32 v145, v232, v233\n"
        "v_mul_f32 v242, v242, v253\n"
        "v_mul_f32 v242, v242, v251\n"
        "v_cvt_pk_bf16_f32 v146, v242, 0\n"
        "s_nop 1\n"
        "v_mfma_f32_16x16x32_bf16 v[108:111], v[144:147], v[136:139], v[108:111]\n"
        "v_mfma_f32_16x16x32_bf16 v[108:111], v[222:225], v[68:71], v[108:111]\n"
        "v_mfma_f32_16x16x32_bf16 v[230:233], v[222:225], v[100:103], 0\n"
        "v_mfma_f32_16x16x32_bf16 v[230:233], v[144:147], v[40:43], v[230:233]\n"
        "v_mfma_f32_16x16x32_bf16 v[230:233], v[188:191], v[152:155], v[230:233]\n"
        "s_add_u32 s90, s89, 196224\n"
        "s_nop 0\n"
        "v_mfma_f32_16x16x32_bf16 v[184:187], v[188:191], v[160:163], 0\n"
        "s_nop 2\n"
        "v_mfma_f32_16x16x32_bf16 v[184:187], v[144:147], v[140:143], v[184:187]\n"
        "s_nop 0\n"
        "v_cvt_pk_bf16_f32 v238, v108, v230\n"
        "v_cvt_pk_bf16_f32 v234, v109, v231\n"
        "v_mfma_f32_16x16x32_bf16 v[184:187], v[226:229], v[68:71], v[184:187]\n"
        "v_cvt_pk_bf16_f32 v132, v110, v232\n"
        "v_cvt_pk_bf16_f32 v124, v111, v233\n"
        "v_mfma_f32_16x16x32_bf16 v[230:233], v[226:229], v[100:103], 0\n"
        "s_add_u32 s91, s89, 294336\n"
        "v_mfma_f32_16x16x32_bf16 v[230:233], v[144:147], v[116:119], v[230:233]\n"
        "v_mfma_f32_16x16x32_bf16 v[230:233], v[188:191], v[156:159], v[230:233]\n"
        "s_nop 1\n"
        "v_mfma_f32_16x16x32_bf16 v[108:111], v[188:191], v[164:167], 0\n"
        "s_nop 2\n"
        "v_mfma_f32_16x16x32_bf16 v[108:111], v[144:147], v[104:107], v[108:111]\n"
        "s_nop 0\n"
        "v_cvt_pk_bf16_f32 v239, v184, v230\n"
        "v_cvt_pk_bf16_f32 v235, v185, v231\n"
        "v_mfma_f32_16x16x32_bf16 v[108:111], v[218:221], v[68:71], v[108:111]\n"
        "v_cvt_pk_bf16_f32 v133, v186, v232\n"
        "v_cvt_pk_bf16_f32 v125, v187, v233\n"
        "v_mfma_f32_16x16x32_bf16 v[184:187], v[218:221], v[100:103], 0\n"
        "v_mfma_f32_16x16x32_bf16 v[184:187], v[144:147], v[34:37], v[184:187]\n"
        "v_mfma_f32_16x16x32_bf16 v[184:187], v[188:191], v[172:175], v[184:187]\n"
        "s_nop 1\n"
        "v_mfma_f32_16x16x32_bf16 v[144:147], a[144:147], v[198:201], 0\n"
        "s_nop 2\n"
        "v_mfma_f32_16x16x32_bf16 v[144:147], a[148:151], v[214:217], v[144:147]\n"
        "s_nop 0\n"
        "v_cvt_pk_bf16_f32 v240, v108, v184\n"
        "v_cvt_pk_bf16_f32 v236, v109, v185\n"
        "v_mfma_f32_16x16x32_bf16 v[144:147], a[152:155], v[202:205], v[144:147]\n"
        "s_nop 0\n"
        "v_cvt_pk_bf16_f32 v134, v110, v186\n"
        "v_cvt_pk_bf16_f32 v126, v111, v187\n"
        "v_mfma_f32_16x16x32_bf16 v[184:187], a[164:167], v[202:205], 0\n"
        "s_nop 1\n"
        "v_mfma_f32_16x16x32_bf16 v[184:187], a[160:163], v[214:217], v[184:187]\n"
        "s_nop 0\n"
        "v_cvt_pk_bf16_f32 v188, v144, v145\n"
        "v_cvt_pk_bf16_f32 v189, v146, v147\n"
        "v_mfma_f32_16x16x32_bf16 v[184:187], a[156:159], v[198:201], v[184:187]\n"
        "s_nop 0\n"
        "v_mfma_f32_16x16x32_bf16 v[108:111], a[168:171], v[198:201], 0\n"
        "buffer_store_dwordx3 v[238:240], v14, s[40:43], s90 offen offset:384 sc1 nt\n"
        "s_nop 2\n"
        "v_mfma_f32_16x16x32_bf16 v[108:111], a[172:175], v[214:217], v[108:111]\n"
        "s_nop 0\n"
        "v_cvt_pk_bf16_f32 v190, v184, v185\n"
        "v_cvt_pk_bf16_f32 v191, v186, v187\n"
        "v_mfma_f32_16x16x32_bf16 v[108:111], a[176:179], v[202:205], v[108:111]\n"
        "buffer_store_dwordx3 v[234:236], v17, s[40:43], s90 offen offset:1152 sc1 nt\n"
        "s_add_u32 s90, s89, 392448\n"
        "v_mfma_f32_16x16x32_bf16 v[184:187], a[188:191], v[202:205], 0\n"
        "s_nop 2\n"
        "v_mfma_f32_16x16x32_bf16 v[184:187], a[184:187], v[214:217], v[184:187]\n"
        "buffer_store_dwordx3 v[132:134], v14, s[40:43], s91 offen offset:576 sc1 nt\n"
        "v_cvt_pk_bf16_f32 v144, v108, v109\n"
        "v_cvt_pk_bf16_f32 v145, v110, v111\n"
        "v_mfma_f32_16x16x32_bf16 v[184:187], a[180:183], v[198:201], v[184:187]\n"
        "s_nop 0\n"
        "v_mfma_f32_16x16x32_bf16 v[108:111], a[192:195], v[198:201], 0\n"
        "buffer_store_dwordx3 v[124:126], v14, s[40:43], s90 offen sc1 nt\n"
        "s_nop 2\n"
        "v_mfma_f32_16x16x32_bf16 v[108:111], a[196:199], v[214:217], v[108:111]\n"
        "s_nop 0\n"
        "v_cvt_pk_bf16_f32 v146, v184, v185\n"
        "v_cvt_pk_bf16_f32 v147, v186, v187\n"
        "v_mfma_f32_16x16x32_bf16 v[108:111], a[200:203], v[202:205], v[108:111]\n"
        "s_nop 1\n"
        "v_mfma_f32_16x16x32_bf16 v[184:187], a[212:215], v[202:205], 0\n"
        "s_nop 2\n"
        "v_mfma_f32_16x16x32_bf16 v[184:187], a[208:211], v[214:217], v[184:187]\n"
        "s_nop 0\n"
        "v_cvt_pk_bf16_f32 v230, v108, v109\n"
        "v_cvt_pk_bf16_f32 v231, v110, v111\n"
        "v_mfma_f32_16x16x32_bf16 v[184:187], a[204:207], v[198:201], v[184:187]\n"
        "s_nop 1\n"
        "v_mfma_f32_16x16x32_bf16 v[108:111], v[194:197], v[188:191], 0\n"
        "s_nop 2\n"
        "v_mfma_f32_16x16x32_bf16 v[108:111], v[206:209], v[144:147], v[108:111]\n"
        "s_nop 0\n"
        "v_cvt_pk_bf16_f32 v232, v184, v185\n"
        "v_cvt_pk_bf16_f32 v233, v186, v187\n"
        "s_nop 1\n"
        "v_mfma_f32_16x16x32_bf16 v[108:111], v[210:213], v[230:233], v[108:111]\n"
        "s_nop 1\n"
        "v_mfma_f32_16x16x32_bf16 v[234:237], v[218:221], v[230:233], 0\n"
        "s_nop 2\n"
        "v_mfma_f32_16x16x32_bf16 v[234:237], v[226:229], v[144:147], v[234:237]\n"
        "s_nop 0\n"
        "v_mul_f32 v108, v108, v168\n"
        "v_mul_f32 v109, v109, v169\n"
        "v_mfma_f32_16x16x32_bf16 v[234:237], v[222:225], v[188:191], v[234:237]\n"
        "s_nop 0\n"
        "v_mul_f32 v110, v110, v170\n"
        "v_mul_f32 v111, v111, v171\n"
        "v_mfma_f32_16x16x32_bf16 v[238:241], v[198:201], v[188:191], 0\n"
        "s_nop 0\n"
        "v_max3_f32 v251, v108, v109, v110\n"
        "v_mfma_f32_16x16x32_bf16 v[238:241], v[214:217], v[144:147], v[238:241]\n"
        "s_nop 0\n"
        "v_mul_f32 v234, v234, v176\n"
        "v_mul_f32 v235, v235, v177\n"
        "v_mfma_f32_16x16x32_bf16 v[238:241], v[202:205], v[230:233], v[238:241]\n"
        "v_mul_f32 v236, v236, v178\n"
        "v_mul_f32 v237, v237, v179\n"
        "v_mfma_f32_16x16x32_bf16 v[184:187], v[148:151], v[230:233], 0\n"
        "s_nop 0\n"
        "v_max3_f32 v251, v251, v111, v234\n"
        "v_max3_f32 v251, v251, v235, v236\n"
        "v_mfma_f32_16x16x32_bf16 v[184:187], v[112:115], v[144:147], v[184:187]\n"
        "s_nop 0\n"
        "v_mul_f32 v238, v238, v180\n"
        "v_mul_f32 v239, v239, v181\n"
        "v_mfma_f32_16x16x32_bf16 v[184:187], v[120:123], v[188:191], v[184:187]\n"
        "s_nop 0\n"
        "v_mul_f32 v240, v240, v182\n"
        "v_mul_f32 v241, v241, v183\n"
        "v_max3_f32 v251, v251, v237, v238\n"
        "v_max3_f32 v251, v251, v239, v240\n"
        "s_nop 2\n"
        "v_mul_f32 v184, v184, v253\n"
        "v_cndmask_b32_e64 v184, v255, v184, s[62:63]\n"
        "v_max3_f32 v251, v251, v241, v184\n"
        "v_mov_b32 v252, v251\n"
        "s_nop 1\n"
        "v_permlane32_swap_b32 v251, v252\n"
        "v_max_f32 v251, v251, v252\n"
        "v_mov_b32 v252, v251\n"
        "s_nop 1\n"
        "v_permlane16_swap_b32 v251, v252\n"
        "v_max_f32 v251, v251, v252\n"
        "v_mul_f32 v251, v251, v254\n"
        "v_fma_f32 v108, v108, v254, -v251\n"
        "v_exp_f32 v108, v108\n"
        "v_fma_f32 v109, v109, v254, -v251\n"
        "v_exp_f32 v109, v109\n"
        "s_nop 0\n"
        "v_add_f32 v252, v108, v109\n"
        "v_mul_f32 v108, v108, v168\n"
        "v_fma_f32 v184, v184, v254, -v251\n"
        "v_exp_f32 v184, v184\n"
        "v_fma_f32 v241, v241, v254, -v251\n"
        "v_exp_f32 v241, v241\n"
        "v_fma_f32 v240, v240, v254, -v251\n"
        "v_exp_f32 v240, v240\n"
        "v_fma_f32 v239, v239, v254, -v251\n"
        "v_exp_f32 v239, v239\n"
        "v_fma_f32 v238, v238, v254, -v251\n"
        "v_exp_f32 v238, v238\n"
        "v_fma_f32 v237, v237, v254, -v251\n"
        "v_exp_f32 v237, v237\n"
        "v_fma_f32 v236, v236, v254, -v251\n"
        "v_exp_f32 v236, v236\n"
        "v_fma_f32 v235, v235, v254, -v251\n"
        "v_exp_f32 v235, v235\n"
        "v_fma_f32 v234, v234, v254, -v251\n"
        "v_exp_f32 v234, v234\n"
        "v_fma_f32 v111, v111, v254, -v251\n"
        "v_exp_f32 v111, v111\n"
        "v_fma_f32 v110, v110, v254, -v251\n"
        "v_exp_f32 v110, v110\n"
        "s_nop 0\n"
        "v_add_f32 v252, v252, v110\n"
        "v_add_f32 v252, v252, v111\n"
        "v_add_f32 v252, v252, v234\n"
        "v_add_f32 v252, v252, v235\n"
        "v_add_f32 v252, v252, v236\n"
        "v_add_f32 v252, v252, v237\n"
        "v_add_f32 v252, v252, v238\n"
        "v_add_f32 v252, v252, v239\n"
        "v_add_f32 v252, v252, v240\n"
        "v_add_f32 v252, v252, v241\n"
        "v_add_f32 v252, v252, v184\n"
        "v_mov_b32 v254, v252\n"
        "s_nop 1\n"
        "v_permlane32_swap_b32 v252, v254\n"
        "v_add_f32 v252, v252, v254\n"
        "v_mov_b32 v254, v252\n"
        "s_nop 1\n"
        "v_permlane16_swap_b32 v252, v254\n"
        "v_add_f32 v252, v252, v254\n"
        "v_rcp_f32 v252, v252\n"
        "s_nop 0\n"
        "v_mul_f32 v108, v108, v252\n"
        "v_mul_f32 v109, v109, v169\n"
        "v_mul_f32 v109, v109, v252\n"
        "v_cvt_pk_bf16_f32 v188, v108, v109\n"
        "v_mul_f32 v110, v110, v170\n"
        "v_mul_f32 v110, v110, v252\n"
        "v_mul_f32 v111, v111, v171\n"
        "v_mul_f32 v111, v111, v252\n"
        "v_cvt_pk_bf16_f32 v189, v110, v111\n"
        "v_mul_f32 v234, v234, v176\n"
        "v_mul_f32 v234, v234, v252\n"
        "v_mul_f32 v235, v235, v177\n"
        "v_mul_f32 v235, v235, v252\n"
        "v_cvt_pk_bf16_f32 v190, v234, v235\n"
        "v_mul_f32 v236, v236, v178\n"
        "v_mul_f32 v236, v236, v252\n"
        "v_mul_f32 v237, v237, v179\n"
        "v_mul_f32 v237, v237, v252\n"
        "v_cvt_pk_bf16_f32 v191, v236, v237\n"
        "s_nop 1\n"
        "v_mfma_f32_16x16x32_bf16 v[108:111], v[188:191], v[128:131], 0\n"
        "v_mul_f32 v238, v238, v180\n"
        "v_mul_f32 v239, v239, v181\n"
        "v_mul_f32 v238, v238, v252\n"
        "v_mul_f32 v239, v239, v252\n"
        "v_cvt_pk_bf16_f32 v132, v238, v239\n"
        "v_mul_f32 v240, v240, v182\n"
        "v_mul_f32 v240, v240, v252\n"
        "v_mul_f32 v241, v241, v183\n"
        "v_mul_f32 v241, v241, v252\n"
        "v_cvt_pk_bf16_f32 v133, v240, v241\n"
        "v_mul_f32 v184, v184, v253\n"
        "v_mul_f32 v184, v184, v252\n"
        "v_cvt_pk_bf16_f32 v134, v184, 0\n"
        "s_nop 1\n"
        "v_mfma_f32_16x16x32_bf16 v[108:111], v[132:135], v[136:139], v[108:111]\n"
        "v_mfma_f32_16x16x32_bf16 v[108:111], v[198:201], v[68:71], v[108:111]\n"
        "v_mfma_f32_16x16x32_bf16 v[184:187], v[198:201], v[100:103], 0\n"
        "v_mfma_f32_16x16x32_bf16 v[184:187], v[132:135], v[40:43], v[184:187]\n"
        "v_mfma_f32_16x16x32_bf16 v[184:187], v[188:191], v[152:155], v[184:187]\n"
        "s_nop 1\n"
        "v_mfma_f32_16x16x32_bf16 v[144:147], v[188:191], v[160:163], 0\n"
        "s_nop 2\n"
        "v_mfma_f32_16x16x32_bf16 v[144:147], v[132:135], v[140:143], v[144:147]\n"
        "s_add_u32 s91, s89, 490560\n"
        "v_cvt_pk_bf16_f32 v238, v108, v184\n"
        "v_cvt_pk_bf16_f32 v124, v109, v185\n"
        "v_mfma_f32_16x16x32_bf16 v[144:147], v[214:217], v[68:71], v[144:147]\n"
        "v_cvt_pk_bf16_f32 v230, v110, v186\n"
        "v_cvt_pk_bf16_f32 v234, v111, v187\n"
        "v_mfma_f32_16x16x32_bf16 v[108:111], v[214:217], v[100:103], 0\n"
        "v_mfma_f32_16x16x32_bf16 v[108:111], v[132:135], v[116:119], v[108:111]\n"
        "v_mfma_f32_16x16x32_bf16 v[108:111], v[188:191], v[156:159], v[108:111]\n"
        "s_nop 1\n"
        "v_mfma_f32_16x16x32_bf16 v[184:187], v[188:191], v[164:167], 0\n"
        "s_nop 2\n"
        "v_mfma_f32_16x16x32_bf16 v[184:187], v[132:135], v[104:107], v[184:187]\n"
        "s_nop 0\n"
        "v_cvt_pk_bf16_f32 v239, v144, v108\n"
        "v_cvt_pk_bf16_f32 v125, v145, v109\n"
        "v_mfma_f32_16x16x32_bf16 v[184:187], v[202:205], v[68:71], v[184:187]\n"
        "v_cvt_pk_bf16_f32 v231, v146, v110\n"
        "v_cvt_pk_bf16_f32 v235, v147, v111\n"
        "v_mfma_f32_16x16x32_bf16 v[108:111], v[202:205], v[100:103], 0\n"
        "v_mfma_f32_16x16x32_bf16 v[108:111], v[132:135], v[34:37], v[108:111]\n"
        "v_mfma_f32_16x16x32_bf16 v[108:111], v[188:191], v[172:175], v[108:111]\n"
        "s_nop 1\n"
        "v_mfma_f32_16x16x32_bf16 v[144:147], a[144:147], v[120:123], 0\n"
        "s_nop 2\n"
        "v_mfma_f32_16x16x32_bf16 v[144:147], a[148:151], v[112:115], v[144:147]\n"
        "s_nop 0\n"
        "v_cvt_pk_bf16_f32 v240, v184, v108\n"
        "v_cvt_pk_bf16_f32 v126, v185, v109\n"
        "v_mfma_f32_16x16x32_bf16 v[144:147], a[152:155], v[148:151], v[144:147]\n"
        "s_nop 0\n"
        "v_cvt_pk_bf16_f32 v232, v186, v110\n"
        "v_cvt_pk_bf16_f32 v236, v187, v111\n"
        "v_mfma_f32_16x16x32_bf16 v[132:135], a[164:167], v[148:151], 0\n"
        "s_nop 1\n"
        "v_mfma_f32_16x16x32_bf16 v[132:135], a[160:163], v[112:115], v[132:135]\n"
        "s_nop 0\n"
        "v_cvt_pk_bf16_f32 v108, v144, v145\n"
        "v_cvt_pk_bf16_f32 v109, v146, v147\n"
        "v_mfma_f32_16x16x32_bf16 v[132:135], a[156:159], v[120:123], v[132:135]\n"
        "s_nop 0\n"
        "v_mfma_f32_16x16x32_bf16 v[184:187], a[168:171], v[120:123], 0\n"
        "buffer_store_dwordx3 v[238:240], v15, s[40:43], s90 offen offset:768 sc1 nt\n"
        "s_add_u32 s90, s89, 588672\n"
        "s_nop 1\n"
        "v_mfma_f32_16x16x32_bf16 v[184:187], a[172:175], v[112:115], v[184:187]\n"
        "s_nop 0\n"
        "v_cvt_pk_bf16_f32 v110, v132, v133\n"
        "v_cvt_pk_bf16_f32 v111, v134, v135\n"
        "v_mfma_f32_16x16x32_bf16 v[184:187], a[176:179], v[148:151], v[184:187]\n"
        "buffer_store_dwordx3 v[124:126], v14, s[40:43], s91 offen offset:192 sc1 nt\n"
        "s_nop 0\n"
        "v_mfma_f32_16x16x32_bf16 v[132:135], a[188:191], v[148:151], 0\n"
        "s_nop 2\n"
        "v_mfma_f32_16x16x32_bf16 v[132:135], a[184:187], v[112:115], v[132:135]\n"
        "buffer_store_dwordx3 v[230:232], v16, s[40:43], s91 offen offset:960 sc1 nt\n"
        "v_cvt_pk_bf16_f32 v144, v184, v185\n"
        "v_cvt_pk_bf16_f32 v145, v186, v187\n"
        "v_mfma_f32_16x16x32_bf16 v[132:135], a[180:183], v[120:123], v[132:135]\n"
        "s_nop 0\n"
        "v_mfma_f32_16x16x32_bf16 v[184:187], a[192:195], v[120:123], 0\n"
        "buffer_store_dwordx3 v[234:236], v14, s[40:43], s90 offen offset:384 sc1 nt\n"
        "s_nop 2\n"
        "v_mfma_f32_16x16x32_bf16 v[184:187], a[196:199], v[112:115], v[184:187]\n"
        "s_nop 0\n"
        "v_cvt_pk_bf16_f32 v146, v132, v133\n"
        "v_cvt_pk_bf16_f32 v147, v134, v135\n"
        "v_mfma_f32_16x16x32_bf16 v[184:187], a[200:203], v[148:151], v[184:187]\n"
        "s_nop 1\n"
        "v_mfma_f32_16x16x32_bf16 v[132:135], a[212:215], v[148:151], 0\n"
        "s_nop 2\n"
        "v_mfma_f32_16x16x32_bf16 v[132:135], a[208:211], v[112:115], v[132:135]\n"
        "s_nop 0\n"
        "v_cvt_pk_bf16_f32 v230, v184, v185\n"
        "v_cvt_pk_bf16_f32 v231, v186, v187\n"
        "v_mfma_f32_16x16x32_bf16 v[132:135], a[204:207], v[120:123], v[132:135]\n"
        "s_nop 1\n"
        "v_mfma_f32_16x16x32_bf16 v[184:187], v[194:197], v[108:111], 0\n"
        "s_nop 2\n"
        "v_mfma_f32_16x16x32_bf16 v[184:187], v[206:209], v[144:147], v[184:187]\n"
        "s_nop 0\n"
        "v_cvt_pk_bf16_f32 v232, v132, v133\n"
        "v_cvt_pk_bf16_f32 v233, v134, v135\n"
        "s_nop 1\n"
        "v_mfma_f32_16x16x32_bf16 v[184:187], v[210:213], v[230:233], v[184:187]\n"
        "s_nop 1\n"
        "v_mfma_f32_16x16x32_bf16 v[192:195], v[218:221], v[230:233], 0\n"
        "s_nop 2\n"
        "v_mfma_f32_16x16x32_bf16 v[192:195], v[226:229], v[144:147], v[192:195]\n"
        "s_nop 0\n"
        "v_mul_f32 v184, v184, v168\n"
        "v_mul_f32 v185, v185, v169\n"
        "v_mfma_f32_16x16x32_bf16 v[192:195], v[222:225], v[108:111], v[192:195]\n"
        "s_nop 0\n"
        "v_mul_f32 v186, v186, v170\n"
        "v_mul_f32 v187, v187, v171\n"
        "v_mfma_f32_16x16x32_bf16 v[188:191], v[198:201], v[108:111], 0\n"
        "s_nop 0\n"
        "v_max3_f32 v254, v184, v185, v186\n"
        "v_mfma_f32_16x16x32_bf16 v[188:191], v[214:217], v[144:147], v[188:191]\n"
        "s_nop 0\n"
        "v_mul_f32 v192, v192, v176\n"
        "v_mul_f32 v193, v193, v177\n"
        "v_mfma_f32_16x16x32_bf16 v[188:191], v[202:205], v[230:233], v[188:191]\n"
        "v_mul_f32 v194, v194, v178\n"
        "v_mul_f32 v195, v195, v179\n"
        "v_mfma_f32_16x16x32_bf16 v[132:135], v[148:151], v[230:233], 0\n"
        "s_nop 0\n"
        "v_max3_f32 v254, v254, v187, v192\n"
        "v_max3_f32 v254, v254, v193, v194\n"
        "v_mfma_f32_16x16x32_bf16 v[132:135], v[112:115], v[144:147], v[132:135]\n"
        "s_nop 0\n"
        "v_mul_f32 v188, v188, v180\n"
        "v_mul_f32 v189, v189, v181\n"
        "v_mfma_f32_16x16x32_bf16 v[132:135], v[120:123], v[108:111], v[132:135]\n"
        "s_nop 0\n"
        "v_mul_f32 v190, v190, v182\n"
        "v_mul_f32 v191, v191, v183\n"
        "v_max3_f32 v254, v254, v195, v188\n"
        "v_max3_f32 v254, v254, v189, v190\n"
        "s_nop 2\n"
        "v_mul_f32 v132, v132, v253\n"
        "v_cndmask_b32_e64 v132, v255, v132, s[62:63]\n"
        "v_max3_f32 v254, v254, v191, v132\n"
        "v_mov_b32 v255, v254\n"
        "s_nop 1\n"
        "v_permlane32_swap_b32 v254, v255\n"
        "v_max_f32 v254, v254, v255\n"
        "v_mov_b32 v255, v254\n"
        "s_nop 1\n"
        "v_permlane16_swap_b32 v254, v255\n"
        "v_max_f32 v254, v254, v255\n"
        "v_mul_f32 v254, v254, v253\n"
        "v_fma_f32 v184, v184, v253, -v254\n"
        "v_exp_f32 v184, v184\n"
        "v_fma_f32 v185, v185, v253, -v254\n"
        "v_exp_f32 v185, v185\n"
        "s_nop 0\n"
        "v_add_f32 v255, v184, v185\n"
        "v_mul_f32 v184, v184, v168\n"
        "v_fma_f32 v132, v132, v253, -v254\n"
        "v_exp_f32 v132, v132\n"
        "v_fma_f32 v191, v191, v253, -v254\n"
        "v_exp_f32 v191, v191\n"
        "v_fma_f32 v190, v190, v253, -v254\n"
        "v_exp_f32 v190, v190\n"
        "v_fma_f32 v189, v189, v253, -v254\n"
        "v_exp_f32 v189, v189\n"
        "v_fma_f32 v188, v188, v253, -v254\n"
        "v_exp_f32 v188, v188\n"
        "v_fma_f32 v195, v195, v253, -v254\n"
        "v_exp_f32 v195, v195\n"
        "v_fma_f32 v194, v194, v253, -v254\n"
        "v_exp_f32 v194, v194\n"
        "v_fma_f32 v193, v193, v253, -v254\n"
        "v_exp_f32 v193, v193\n"
        "v_fma_f32 v192, v192, v253, -v254\n"
        "v_exp_f32 v192, v192\n"
        "v_fma_f32 v187, v187, v253, -v254\n"
        "v_exp_f32 v187, v187\n"
        "v_fma_f32 v186, v186, v253, -v254\n"
        "v_exp_f32 v186, v186\n"
        "s_nop 0\n"
        "v_add_f32 v255, v255, v186\n"
        "v_add_f32 v255, v255, v187\n"
        "v_add_f32 v255, v255, v192\n"
        "v_add_f32 v255, v255, v193\n"
        "v_add_f32 v255, v255, v194\n"
        "v_add_f32 v255, v255, v195\n"
        "v_add_f32 v255, v255, v188\n"
        "v_add_f32 v255, v255, v189\n"
        "v_add_f32 v255, v255, v190\n"
        "v_add_f32 v255, v255, v191\n"
        "v_add_f32 v255, v255, v132\n"
        "v_mov_b32 v254, v255\n"
        "s_nop 1\n"
        "v_permlane32_swap_b32 v255, v254\n"
        "v_add_f32 v255, v255, v254\n"
        "v_mov_b32 v254, v255\n"
        "s_nop 1\n"
        "v_permlane16_swap_b32 v255, v254\n"
        "v_add_f32 v255, v255, v254\n"
        "v_rcp_f32 v255, v255\n"
        "s_nop 0\n"
        "v_mul_f32 v184, v184, v255\n"
        "v_mul_f32 v185, v185, v169\n"
        "v_mul_f32 v185, v185, v255\n"
        "v_cvt_pk_bf16_f32 v144, v184, v185\n"
        "v_mul_f32 v186, v186, v170\n"
        "v_mul_f32 v186, v186, v255\n"
        "v_mul_f32 v187, v187, v171\n"
        "v_mul_f32 v187, v187, v255\n"
        "v_cvt_pk_bf16_f32 v145, v186, v187\n"
        "v_mul_f32 v192, v192, v176\n"
        "v_mul_f32 v192, v192, v255\n"
        "v_mul_f32 v193, v193, v177\n"
        "v_mul_f32 v193, v193, v255\n"
        "v_cvt_pk_bf16_f32 v146, v192, v193\n"
        "v_mul_f32 v194, v194, v178\n"
        "v_mul_f32 v194, v194, v255\n"
        "v_mul_f32 v195, v195, v179\n"
        "v_mul_f32 v195, v195, v255\n"
        "v_cvt_pk_bf16_f32 v147, v194, v195\n"
        "s_nop 1\n"
        "v_mfma_f32_16x16x32_bf16 v[108:111], v[144:147], v[128:131], 0\n"
        "v_mul_f32 v188, v188, v180\n"
        "v_mul_f32 v189, v189, v181\n"
        "v_mul_f32 v188, v188, v255\n"
        "v_mul_f32 v189, v189, v255\n"
        "v_cvt_pk_bf16_f32 v124, v188, v189\n"
        "v_mul_f32 v190, v190, v182\n"
        "v_mul_f32 v190, v190, v255\n"
        "v_mul_f32 v191, v191, v183\n"
        "v_mul_f32 v191, v191, v255\n"
        "v_cvt_pk_bf16_f32 v125, v190, v191\n"
        "v_mul_f32 v132, v132, v253\n"
        "v_mul_f32 v132, v132, v255\n"
        "v_cvt_pk_bf16_f32 v126, v132, 0\n"
        "s_nop 1\n"
        "v_mfma_f32_16x16x32_bf16 v[108:111], v[124:127], v[136:139], v[108:111]\n"
        "v_mfma_f32_16x16x32_bf16 v[108:111], v[120:123], v[68:71], v[108:111]\n"
        "v_mfma_f32_16x16x32_bf16 v[128:131], v[120:123], v[100:103], 0\n"
        "v_mfma_f32_16x16x32_bf16 v[128:131], v[124:127], v[40:43], v[128:131]\n"
        "v_mfma_f32_16x16x32_bf16 v[128:131], v[144:147], v[152:155], v[128:131]\n"
        "s_nop 1\n"
        "v_mfma_f32_16x16x32_bf16 v[120:123], v[144:147], v[160:163], 0\n"
        "s_nop 2\n"
        "v_mfma_f32_16x16x32_bf16 v[120:123], v[124:127], v[140:143], v[120:123]\n"
        "s_nop 0\n"
        "v_cvt_pk_bf16_f32 v130, v108, v128\n"
        "v_mfma_f32_16x16x32_bf16 v[120:123], v[112:115], v[68:71], v[120:123]\n"
        "v_mfma_f32_16x16x32_bf16 v[108:111], v[112:115], v[100:103], 0\n"
        "v_mfma_f32_16x16x32_bf16 v[108:111], v[124:127], v[116:119], v[108:111]\n"
        "v_mfma_f32_16x16x32_bf16 v[108:111], v[144:147], v[156:159], v[108:111]\n"
        "s_nop 1\n"
        "v_mfma_f32_16x16x32_bf16 v[40:43], v[144:147], v[164:167], 0\n"
        "s_nop 2\n"
        "v_mfma_f32_16x16x32_bf16 v[40:43], v[124:127], v[104:107], v[40:43]\n"
        "s_nop 0\n"
        "v_cvt_pk_bf16_f32 v131, v120, v108\n"
        "v_mfma_f32_16x16x32_bf16 v[40:43], v[148:151], v[68:71], v[40:43]\n"
        "v_mfma_f32_16x16x32_bf16 v[104:107], v[148:151], v[100:103], 0\n"
        "v_mfma_f32_16x16x32_bf16 v[104:107], v[124:127], v[34:37], v[104:107]\n"
        "s_add_u32 s46, s89, 590016\n"
        "v_mfma_f32_16x16x32_bf16 v[104:107], v[144:147], v[172:175], v[104:107]\n"
        "s_nop 2\n"
        "s_min_u32 s46, s46, s42\n"
        "s_nop 3\n"
        "v_cvt_pk_bf16_f32 v132, v40, v104\n"
        "buffer_store_dwordx3 v[130:132], v17, s[44:47], s90 offen offset:1152 sc1 nt\n"
        "s_nop 15\n"
        "s_cmp_lt_i32 s85, -1\n"
        "s_cbranch_scc1 FIN_%=\n"
        "s_mov_b32 s89, 0x80000000\n"
        "s_waitcnt vmcnt(0)\n"
        "ds_read_b128 v[126:129], v9\n"
        "ds_read_b128 v[132:135], v10\n"
        "ds_read_b128 v[138:141], v11\n"
        "ds_read_b128 v[106:109], v9 offset:3072\n"
        "ds_read_b128 v[112:115], v10 offset:3072\n"
        "ds_read_b128 v[118:121], v11 offset:3072\n"
        "ds_read_b128 v[146:149], v9 offset:6144\n"
        "ds_read_b128 v[152:155], v10 offset:6144\n"
        "ds_read_b128 v[158:161], v11 offset:6144\n"
        "s_waitcnt lgkmcnt(0)\n"
        "s_nop 7\n"
        "s_cmp_ge_i32 s85, 0\n"
        "s_cbranch_scc1 TOP_%=\n"
        "s_branch LAST_%=\n"
        "SP2_%=:\n"
        "s_sleep 1\n"
        "ds_read_u16 v253, v19 offset:2048\n"
        "s_waitcnt lgkmcnt(0)\n"
        "v_cmp_ne_u32_e64 s[24:25], s30, v253\n"
        "s_nop 4\n"
        "s_cmp_eq_u64 s[24:25], 0\n"
        "s_cbranch_scc1 BK2_%=\n"
        "s_sub_u32 s31, s31, 1\n"
        "s_cbranch_scc0 SP2_%=\n"
        "s_endpgm\n"
        "DY1_%=:\n"
        "v_mov_b32 v27, 163424\n"
        "ds_read_b32 v27, v27\n"
        "s_waitcnt lgkmcnt(0)\n"
        "v_readfirstlane_b32 s50, v27\n"
        "s_nop 3\n"
        "s_lshl_b32 s92, s50, 5\n"
        "s_add_u32 s92, s92, s48\n"
        "s_cmp_ge_u32 s92, 5329\n"
        "s_cbranch_scc1 DY1F_%=\n"
        "s_mul_i32 s87, s92, 1344\n"
        "s_mul_i32 s92, s92, 57457\n"
        "s_lshr_b32 s92, s92, 22\n"
        "s_mul_i32 s92, s92, 588672\n"
        "s_add_u32 s87, s87, s92\n"
        "s_add_u32 s85, s85, 1\n"
        "s_add_u32 s50, s50, 20\n"
        "s_sub_u32 s23, s50, s53\n"
        "s_mul_i32 s23, s23, 1584\n"
        "s_mov_b32 s53, s50\n"
        "s_branch DY1B_%=\n"
        "DY1F_%=:\n"
        "s_cmp_ge_u32 s92, 5521\n"
        "s_cbranch_scc1 DY1B_%=\n"
        "s_sleep 9\n"
        "s_add_u32 s92, s92, 32\n"
        "s_branch DY1F_%=\n"
        "s_branch DY1B_%=\n"
        "DY2_%=:\n"
        "v_mov_b32 v26, 1\n"
        "s_mov_b64 exec, 1\n"
        "buffer_atomic_add v26, off, s[16:19], 0 sc0\n"
        "s_mov_b64 exec, -1\n"
        "s_branch DY2B_%=\n"
        "SP10_%=:\n"
        "s_sleep 1\n"
        "ds_read_u16 v245, v19 offset:2560\n"
        "s_waitcnt lgkmcnt(0)\n"
        "v_cmp_ne_u32_e64 s[24:25], s29, v245\n"
        "s_nop 4\n"
        "s_cmp_eq_u64 s[24:25], 0\n"
        "s_cbranch_scc1 BK10_%=\n"
        "s_sub_u32 s31, s31, 1\n"
        "s_cbranch_scc0 SP10_%=\n"
        "s_endpgm\n"
        "DY3_%=:\n"
        "s_waitcnt vmcnt(9)\n"
        "v_readfirstlane_b32 s50, v26\n"
        "s_nop 3\n"
        "s_sub_u32 s50, s50, s20\n"
        "s_min_u32 s50, s50, 31\n"
        "v_mov_b32 v27, s49\n"
        "v_mov_b32 v26, s50\n"
        "ds_write_b32 v27, v26\n"
        "s_lshl_b32 s50, s50, 2\n"
        "s_waitcnt lgkmcnt(0)\n"
        "v_mov_b32 v27, s50\n"
        "v_mov_b32 v26, 1\n"
        "s_mov_b64 exec, 1\n"
        "buffer_atomic_add v26, v27, s[16:19], 0 offen offset:4\n"
        "s_mov_b64 exec, -1\n"
        "s_nop 1\n"
        "s_branch DY3B_%=\n"
        "SP20_%=:\n"
        "s_sleep 1\n"
        "ds_read_u16 v202, v19 offset:2048\n"
        "s_waitcnt lgkmcnt(0)\n"
        "v_cmp_ne_u32_e64 s[24:25], s30, v202\n"
        "s_nop 4\n"
        "s_cmp_eq_u64 s[24:25], 0\n"
        "s_cbranch_scc1 BK20_%=\n"
        "s_sub_u32 s31, s31, 1\n"
        "s_cbranch_scc0 SP20_%=\n"
        "s_endpgm\n"
        :: "{v[0:15]}"(c0), "{v[16:23]}"(c1), "{v[24:27]}"(c2), "{v[166:173]}"(sp), "{v[30:45]}"(f0), "{v[46:49]}"(f1), "{s15}"(own4), "{s[16:19]}"(srdl), "{s[20:23]}"(sol), "{s[36:39]}"(srdx), "{s[40:43]}"(srdo), "{s[48:55]}"(si)
        : "v28", "v29", "v50", "v51", "v52", "v53", "v54", "v55", "v56", "v57", "v58", "v59", "v60", "v61", "v62", "v63", "v64", "v65", "v66", "v67", "v68", "v69", "v70", "v71", "v72", "v73", "v74", "v75", "v76", "v77", "v78", "v79", "v80", "v81", "v82", "v83", "v84", "v85", "v86", "v87", "v88", "v89", "v90", "v91", "v92", "v93", "v94", "v95", "v96", "v97", "v98", "v99", "v100", "v101", "v102", "v103", "v104", "v105", "v106", "v107", "v108", "v109", "v110", "v111", "v112", "v113", "v114", "v115", "v116", "v117", "v118", "v119", "v120", "v121", "v122", "v123", "v124", "v125", "v126", "v127", "v128", "v129", "v130", "v131", "v132", "v133", "v134", "v135", "v136", "v137", "v138", "v139", "v140", "v141", "v142", "v143", "v144", "v145", "v146", "v147", "v148", "v149", "v150", "v151", "v152", "v153", "v154", "v155", "v156", "v157", "v158", "v159", "v160", "v161", "v162", "v163", "v164", "v165", "v174", "v175", "v176", "v177", "v178", "v179", "v180", "v181", "v182", "v183", "v184", "v185", "v186", "v187", "v188", "v189", "v190", "v191", "v192", "v193", "v194", "v195", "v196", "v197", "v198", "v199", "v200", "v201", "v202", "v203", "v204", "v205", "v206", "v207", "v208", "v209", "v210", "v211", "v212", "v213", "v214", "v215", "v216", "v217", "v218", "v219", "v220", "v221", "v222", "v223", "v224", "v225", "v226", "v227", "v228", "v229", "v230", "v231", "v232", "v233", "v234", "v235", "v236", "v237", "v238", "v239", "v240", "v241", "v242", "v243", "v244", "v245", "v246", "v247", "v248", "v249", "v250", "v251", "v252", "v253", "v254", "v255", "a0", "a1", "a2", "a3", "a4", "a5", "a6", "a7", "a8", "a9", "a10", "a11", "a12", "a13", "a14", "a15", "a16", "a17", "a18", "a19", "a20", "a21", "a22", "a23", "a24", "a25", "a26", "a27", "a28", "a29", "a30", "a31", "a32", "a33", "a34", "a35", "a36", "a37", "a38", "a39", "a40", "a41", "a42", "a43", "a44", "a45", "a46", "a47", "a48", "a49", "a50", "a51", "a52", "a53", "a54", "a55", "a56", "a57", "a58", "a59", "a60", "a61", "a62", "a63", "a64", "a65", "a66", "a67", "a68", "a69", "a70", "a71", "a72", "a73", "a74", "a75", "a76", "a77", "a78", "a79", "a80", "a81", "a82", "a83", "a84", "a85", "a86", "a87", "a88", "a89", "a90", "a91", "a92", "a93", "a94", "a95", "a96", "a97", "a98", "a99", "a100", "a101", "a102", "a103", "a104", "a105", "a106", "a107", "a108", "a109", "a110", "a111", "a112", "a113", "a114", "a115", "a116", "a117", "a118", "a119", "a120", "a121", "a122", "a123", "a124", "a125", "a126", "a127", "a128", "a129", "a130", "a131", "a132", "a133", "a134", "a135", "a136", "a137", "a138", "a139", "a140", "a141", "a142", "a143", "a144", "a145", "a146", "a147", "a148", "a149", "a150", "a151", "a152", "a153", "a154", "a155", "a156", "a157", "a158", "a159", "a160", "a161", "a162", "a163", "a164", "a165", "a166", "a167", "a168", "a169", "a170", "a171", "a172", "a173", "a174", "a175", "a176", "a177", "a178", "a179", "a180", "a181", "a182", "a183", "a184", "a185", "a186", "a187", "a188", "a189", "a190", "a191", "a192", "a193", "a194", "a195", "a196", "a197", "a198", "a199", "a200", "a201", "a202", "a203", "a204", "a205", "a206", "a207", "a208", "a209", "a210", "a211", "a212", "a213", "a214", "a215", "a216", "a217", "a218", "a219", "a220", "a221", "a222", "a223", "a224", "a225", "a226", "a227", "a228", "a229", "a230", "a231", "a232", "a233", "a234", "a235", "a236", "a237", "a238", "a239", "a240", "a241", "a242", "a243", "a244", "a245", "a246", "a247", "a248", "a249", "a250", "a251", "a252", "a253", "a254", "a255", "s24", "s25", "s26", "s27", "s28", "s29", "s30", "s31", "s44", "s45", "s46", "s47", "s56", "s57", "s58", "s59", "s60", "s61", "s62", "s63", "s64", "s65", "s66", "s67", "s68", "s69", "s70", "s71", "s72", "s73", "s74", "s75", "s76", "s77", "s78", "s79", "s80", "s81", "s82", "s83", "s84", "s85", "s86", "s87", "s88", "s89", "s90", "s91", "s92", "s93", "s94", "s95", "vcc", "scc", "memory");
}

extern "C" void kernel_launch(void* const* d_in, const int* in_sizes, int n_in, void* d_out, int out_size, void* d_ws, size_t ws_size, hipStream_t stream) {
    (void)in_sizes; (void)n_in; (void)out_size; (void)ws_size;
    (void)hipFuncSetAttribute((const void*)swin_kernel, hipFuncAttributeMaxDynamicSharedMemorySize, LDS_BYTES);
    swin_kernel<<<GRID, 256, LDS_BYTES, stream>>>((const char*)d_in[0], (const char*)d_in[1], (const char*)d_in[2], (const char*)d_in[3], (const char*)d_in[4], (char*)d_out, (const char*)d_ws);
}
```

```cpp
#define NLC 5
#define WSTEP 32
#define CPL 8
#define SMASK 31914
#define ODDS 1
#define DYN 1
#define EARLY 1
#define TLBW 0
#include <hip/hip_runtime.h>
#include <hip/hip_bf16.h>

#define DI __device__ __forceinline__

using u32x2 = __attribute__((ext_vector_type(2))) unsigned;
using u32x4 = __attribute__((ext_vector_type(4))) unsigned;
using u32x8 = __attribute__((ext_vector_type(8))) unsigned;
using u32x16 = __attribute__((ext_vector_type(16))) unsigned;

constexpr int HH = 511, WW = 511, NWX = 73, NWIN = NWX * NWX, GRID = 256;
constexpr int TOKB = 192, ROWB = WW * TOKB;
constexpr unsigned FRAMEB = (unsigned)HH * ROWB, TOTALB = FRAMEB * 4;
constexpr unsigned FS = 9232, L_BUF = 0, L_STG = 4 * FS, REC = 1584, NREC = DYN ? 27 : 21, L_EXT = L_STG + 4 * 9216, L_MS = L_EXT + 2 * NREC * REC - (DYN ? 18432 : 0), L_STA = L_MS + 18432, L_PSC = L_STA + 512,
                   L_STB = L_PSC + 512, L_AUX = L_STB + 512, L_DUM = L_AUX + 1024, L_FA = L_DUM + 512, LDS_BYTES = 163840;
constexpr unsigned RAWM = 18432, L_GT = L_AUX + 768;
static_assert(L_EXT + 3 * RAWM <= L_MS && ((L_STG >> 10) & 3) == 0 && (L_STG & 1023) < 512 && L_FA + 1024 + 16 <= LDS_BYTES && (NWIN + GRID - 1) / GRID <= NREC, "LDS");

#define LP(T, a) ((__attribute__((address_space(3))) T*)(size_t)(unsigned)(a))
#define SB() __builtin_amdgcn_sched_barrier(0)
#define PIN(v) asm volatile("" : "+v"(v))
DI unsigned raw_lane(unsigned lane, unsigned m, unsigned A, unsigned B, unsigned C) {
    const unsigned n = 64 * m + lane, row = (n * 171) >> 11, pc = n - 12 * row;
    return (row >> 3) * (A & 0xffff) + ((row >> 1) & 3) * (B & 0xffff) + (row & 1) * (C & 0xffff) + (pc ^ ((row >> 2) & 3)) * 16 - 1024 * m;
}
DI u32x4 make_srd(unsigned lo, unsigned hi, unsigned bytes) { return u32x4{lo, hi & 0xffffu, bytes, 0x00020000u}; }
DI void raw_dma(unsigned v0, unsigned v1, unsigned v2, const u32x4& srd, unsigned so, unsigned dst) {
    asm volatile("s_mov_b32 m0, %5\n\ts_nop 0\n\tbuffer_load_dwordx4 %0, %3, %4 offen lds\n\tbuffer_load_dwordx4 %1, %3, %4 offen offset:1024 lds\n\tbuffer_load_dwordx4 %2, %3, %4 offen offset:2048 lds"
                 :: "v"(v0), "v"(v1), "v"(v2), "s"(srd), "s"(so), "s"(dst) : "memory");
}

__global__ __launch_bounds__(256, 1) void swin_kernel(const char* __restrict__ x_, const char* __restrict__ wqkv_t, const char* __restrict__ wo_t, const char* __restrict__ wqkv_s,
                                                           const char* __restrict__ wo_s, char* __restrict__ out_, const char* wf) {
    const unsigned k = __builtin_amdgcn_readfirstlane(threadIdx.x >> 6) & 3, lane = threadIdx.x & 63, k1 = k & 1, k2 = k >> 1;
    u32x2 kx, kl, kr; u32x4 ko;
    const unsigned long long kap = (unsigned long long)__builtin_amdgcn_kernarg_segment_ptr();
    asm volatile("s_load_dwordx2 %0, %4, %5\n\ts_load_dwordx2 %1, %4, %6\n\ts_load_dwordx2 %2, %4, 0x0\n\ts_load_dwordx4 %3, %4, 0x28"
                 : "=&s"(kr), "=&s"(kl), "=&s"(kx), "=&s"(ko) : "s"(kap), "s"(8 + 8 * k), "s"(8 + 16 * k2));
    SB();
    {
        unsigned long long pc; unsigned co = ((((blockIdx.x >> 3) * 4 + k) * CPL + lane / (64 / CPL)) & 511) * 128;
        asm volatile("s_getpc_b64 %0\n.Lswin_pc:\n\ts_mov_b32 m0, %2\n\tv_min_u32 %1, .Lswin_fin-.Lswin_pc, %1\n\tglobal_load_lds_dword %1, %0" : "=&s"(pc), "+v"(co) : "s"(L_DUM + 256) : "memory");
    }
    SB();
#define TS(i)
    const unsigned Bl = 1152 + 2304 * k2, Ql = 4608 - 3456 * k2;
    const unsigned Ar = 13824 - 12288 * k1, Br = k1 ? 384 : Bl, Cr = 576 - 384 * k1, Pr = 27648 - 24576 * k1, Qr = k1 ? 6144 : Ql, cr = 192 - 192 * k1, szr = 55296 - 36864 * k1;
    const unsigned ldr = L_STG + k * RAWM + k2 * RAWM, ldl = k1 * RAWM + k2 * (L_MS - k1 * (L_MS - L_EXT + RAWM));
    static_assert(L_STG + 2 * RAWM == L_EXT, "");
    unsigned vW = (((blockIdx.x >> 3) & 31) * 14 + lane) * 128 + (lane >= 14) * 0x40000000u; PIN(vW);
    unsigned vr[3];
#pragma unroll
    for (int m = 0; m < EARLY; ++m) { vr[m] = raw_lane(lane, m, Ar, Br, Cr); PIN(vr[m]); }
    SB();
    asm volatile("s_waitcnt lgkmcnt(0)" : "+s"(kr), "+s"(kl), "+s"(kx), "+s"(ko));
    const u32x4 srdr = make_srd(kr[0], kr[1], szr);
    asm volatile("s_mov_b32 m0, %2\n\ts_nop 0\n\tbuffer_load_dword %0, %1, 0 offen lds" :: "v"(vW), "s"(srdr), "s"(L_DUM) : "memory");
    SB();
#pragma unroll
    for (int m = EARLY; m < 3; ++m) { vr[m] = raw_lane(lane, m, Ar, Br, Cr); PIN(vr[m]); }
#define RDMA(n) do { SB(); raw_dma(vr[0], vr[1], vr[2], srdr, cr + (n & 1) * Pr + (n >> 1) * Qr, ldr + n * 3072); SB(); } while (0)
    RDMA(0); RDMA(1);
    TS(1);
    const u32x4 srdl = make_srd(kl[0], kl[1], 55296), srdx = make_srd(kx[0], kx[1], TOTALB), srdo = make_srd(ko[0], ko[1], TOTALB);
    const unsigned vb = (blockIdx.x & 7) * (GRID / 8) + (blockIdx.x >> 3);
    const unsigned w0 = (blockIdx.x & 7) * 640 + (blockIdx.x >> 3), nwin = 5120 + (blockIdx.x >> 3), wy0 = w0 / NWX, wx0 = w0 - wy0 * NWX, own4 = (blockIdx.x & 7) * 4;
    static_assert(WSTEP == 32, "");
    const unsigned base0 = wy0 * 7 * ROWB + wx0 * 7 * TOKB;
#define SLOT(n) do { constexpr int a_ = __builtin_popcount(SMASK & ((1 << (n)) - 1)); if ((SMASK >> (n)) & 1) { if (a_ < 4) RDMA((a_ + 2)); else LDMA((a_ - 4)); } } while (0)
    static_assert(__builtin_popcount(SMASK) == 4 + NLC, "");
    const unsigned i = lane & 15, g = lane >> 4, p = i >> 2, t = i & 3, cl = 384 * k1;
    u32x16 c0, f0; u32x8 c1, sp; u32x4 c2;
    const unsigned stg = L_STG + k * 9216, ext = L_EXT + k * 384, exv = L_EXT + NREC * REC + k * 392;
    unsigned vl[3];
#pragma unroll
    for (int m = 0; m < 3; ++m) { vl[m] = raw_lane(lane, m, 13824, Bl, 576); PIN(vl[m]); }
#define LDMA(n) do { SB(); raw_dma(vl[0], vl[1], vl[2], srdl, cl + (n & 1) * 27648 + (n >> 1) * Ql, ldl + n * 3072); SB(); } while (0)
    SLOT(0);
#pragma unroll
    for (int a = 0; a < 2; ++a) {
        const unsigned ra = (8 * p + 4 * a + t) * 192 + ((g ^ ((2 * p + a) & 3)) * 16);
        f0[a] = ldl + ra; f0[2 + a] = ldr + ra; f0[4 + a] = ldr + (8 * g + 4 * a + p) * 192 + ((t ^ ((2 * g + a) & 3)) * 16);
        PIN(f0[a]); PIN(f0[2 + a]); PIN(f0[4 + a]);
    }
    SLOT(1);
#pragma unroll
    for (int a = 0; a < 2; ++a) {
        const unsigned n = 16 * a + 4 * k + p;
        const unsigned w = (DYN && n >= 20 ? nwin - 20 * WSTEP : w0) + n * WSTEP;
        f0[6 + a] = (DYN ? w < NWIN : n < nwin) ? t * FRAMEB + w * (7 * TOKB) + ((w * 57457) >> 22) * (7 * ROWB - NWX * 7 * TOKB) + 6 * ROWB + 6 * TOKB + g * 48 : 0x80000000u; PIN(f0[6 + a]);
        if (a == 0) SLOT(2);
    }
    f0[8] = i == 0 ? L_AUX + 192 + g * 16 : L_DUM; f0[15] = 0;
    SLOT(3);
    {
        const unsigned sh = 2 - 2 * k2, ts = 3072 + 16 * k2, ml = 64 - 48 * k2, xm = 64 * (k == 1), a0 = ldl + g * 256 + (k == 1 ? t * 16 : k1 ? 0 : (i ^ ((g & k & 2) << 2)) * 16);
#pragma unroll
        for (int n = 0; n < 6; ++n) {
            const unsigned z = (32 * (n >> 1) + 4 * (n & 1) + 8 * p + t) >> sh;
            const unsigned z6 = (z * 43) >> 8, tl = z - 6 * z6;
            f0[9 + n] = a0 + (tl * ts + ((z6 * ml) ^ ((tl << 5) & xm))) * k1 + n * 3072 * (1 - k1); PIN(f0[9 + n]);
            if (n == 0) SLOT(4);
            if (n == 2) SLOT(5);
            if (n == 4) SLOT(6);
        }
    }
    SLOT(7);
#pragma unroll
    for (int m = 0; m < 3; ++m) {
        const unsigned n = 64 * m + lane, tf = (n * 171) >> 13, r = n - 48 * tf, q = (r * 43) >> 9;
        const unsigned c = tf * FRAMEB + ((r - 12 * q) ^ tf) * 16;
#pragma unroll
        for (int u = 0; u < 3; ++u) {
            const unsigned px = 16 * u + 4 * k + q;
            c0[3 * u + m] = c + px * TOKB + ((px * 37) >> 8) * (ROWB - 7 * TOKB) - 1024 * ((3 * u + m) % 4); PIN(c0[3 * u + m]);
        }
        if (m == 0) SLOT(8);
        if (m == 1) SLOT(9);
        if (m == 2) SLOT(10);
    }
#pragma unroll
    for (int S = 0; S < 3; ++S) c0[9 + S] = stg + t * 768 + p * TOKB + ((3 * g + S) ^ t) * 16;
    c0[12] = L_BUF + t * FS + g * 256 + p * 64 + k * 16;
    c0[13] = L_BUF + k * FS + lane * 16;
    const unsigned vs = g * TOKB + 12 * i, brk = ROWB - 7 * TOKB, u = (p - g) & 3;
    c0[14] = vs; c0[15] = vs + (g >= 3 ? brk : 0);
    PIN(c0);
    SLOT(11);
    c1[0] = vs + (g >= 2 ? brk : 0); c1[1] = vs + (g >= 1 ? brk : 0);
    c1[2] = u < 3 ? L_STA + t * 128 + g * 32 + u * 8 + k * 2 : L_DUM;
    c1[3] = L_STB + t * 128 + (4 * g + k) * 8 + u * 2;
    c1[4] = L_STA + k * 128 + g * 32;
    c1[5] = L_STB + k * 128 + i * 8;
    c1[6] = L_STA + k * 128 + p * 32 + g * 8 + t * 2;
    c1[7] = ext - REC + (i & 1) * 192 + g * 16;
    c2[0] = exv - REC + 24 * i;
    c2[1] = p == g ? ~0u : 0u;
    c2[2] = L_AUX + (t < 2 ? t : 2) * 192 + g * 16;
    c2[3] = p == g ? L_AUX + 576 + t * 16 : L_AUX + 384;
    PIN(c1); PIN(c2);
    SLOT(12);
    sp[0] = lane * 16; sp[1] = L_MS + (16 * t + ((4 * g + p) ^ ((t & 2) << 2))) * 16;
#pragma unroll
    for (int T = 0; T < 2; ++T) {
        const unsigned n = 16 * T + 4 * t + p;
        sp[2 + T] = n < NREC ? ext + n * REC + g * 16 : L_DUM + (i & 7) * 16; sp[4 + T] = n < NREC && p == g ? exv + n * REC + 384 : L_DUM;
    }
    sp[6] = exv + g * REC + 24 * i; sp[7] = g < (DYN ? 3 : 1) ? exv + ((DYN ? 24 : 20) + g) * REC + 24 * i : L_DUM + 24 * i;
    PIN(sp);
    SLOT(13);
    {
        const unsigned e = 64 * k + lane, y = e - 144, one = (y < 16 ? (y >> 2) & 1 : y >> 5) ? 0x3f800000u : 0x3f80u;
        *LP(unsigned, L_AUX + e * 4) = e < 48 ? 0x3f803f80u : e == 192 ? 0x4000400u : y < 48 && t == (y < 16 ? y >> 3 : (y >> 2) & 3) ? one : 0u;
        *LP(unsigned, L_STA + k * 128 + (lane & 6) * 16 + 24 + (lane & 1) * 4) = 0x3c003c00u;
        *LP(unsigned, L_FA + e * 4) = 0u;
    }
    SLOT(14);
    const u32x4 f1 = {vl[0], vl[1], vl[2], 0u}, sol = {cl + Ql, cl + Ql + 27648, cl + 2 * Ql, cl + 2 * Ql + 27648};
    const unsigned long long gl = (((unsigned long long)ko[3] << 32) | ko[2]) + (blockIdx.x >> 3) * 128;
    const u32x8 si = {(unsigned)nwin, base0, (unsigned)wx0, k * FRAMEB, stg, ldl, (unsigned)gl, (unsigned)(gl >> 32) & 0xffffu};
    asm volatile(
        "s_bitcmp1_b32 s52, 10\n"
        "s_cbranch_scc1 FV_%=\n"
        "s_mov_b32 s24, 0x3e16c740\n"
        "v_mov_b32 v50, 0x3f803f80\n"
        "v_mov_b32 v51, 0x3f803f80\n"
        "v_mov_b32 v52, 0x3f803f80\n"
        "v_mov_b32 v53, 0x3f803f80\n"
        "s_waitcnt vmcnt(15)\n"
        "ds_read_b64_tr_b16 a[72:73], v34 offset:0\n"
        "ds_read_b64_tr_b16 a[74:75], v35 offset:0\n"
        "ds_read_b64_tr_b16 a[76:77], v34 offset:6144\n"
        "ds_read_b64_tr_b16 a[78:79], v35 offset:6144\n"
        "ds_read_b64_tr_b16 a[80:81], v34 offset:12288\n"
        "ds_read_b64_tr_b16 a[82:83], v35 offset:12288\n"
        "s_add_u32 m0, s53, 15360\n"
        "s_nop 0\n"
        "buffer_load_dwordx4 v46, s[16:19], s23 offen lds\n"
        "ds_read_b64_tr_b16 a[84:85], v34 offset:8\n"
        "ds_read_b64_tr_b16 a[86:87], v35 offset:8\n"
        "ds_read_b64_tr_b16 a[88:89], v34 offset:6152\n"
        "ds_read_b64_tr_b16 a[90:91], v35 offset:6152\n"
        "ds_read_b64_tr_b16 a[92:93], v34 offset:12296\n"
        "ds_read_b64_tr_b16 a[94:95], v35 offset:12296\n"
        "buffer_load_dwordx4 v47, s[16:19], s23 offen offset:1024 lds\n"
        "ds_read_b64_tr_b16 a[96:97], v34 offset:64\n"
        "ds_read_b64_tr_b16 a[98:99], v35 offset:64\n"
        "ds_read_b64_tr_b16 a[100:101], v34 offset:6208\n"
        "ds_read_b64_tr_b16 a[102:103], v35 offset:6208\n"
        "ds_read_b64_tr_b16 a[104:105], v34 offset:12352\n"
        "ds_read_b64_tr_b16 a[106:107], v35 offset:12352\n"
        "buffer_load_dwordx4 v48, s[16:19], s23 offen offset:2048 lds\n"
        "ds_read_b64_tr_b16 a[108:109], v34 offset:72\n"
        "ds_read_b64_tr_b16 a[110:111], v35 offset:72\n"
        "ds_read_b64_tr_b16 a[112:113], v34 offset:6216\n"
        "ds_read_b64_tr_b16 a[114:115], v35 offset:6216\n"
        "ds_read_b64_tr_b16 a[116:117], v34 offset:12360\n"
        "ds_read_b64_tr_b16 a[118:119], v35 offset:12360\n"
        "ds_read_b64_tr_b16 a[120:121], v34 offset:128\n"
        "ds_read_b64_tr_b16 a[122:123], v35 offset:128\n"
        "ds_read_b64_tr_b16 a[124:125], v34 offset:6272\n"
        "ds_read_b64_tr_b16 a[126:127], v35 offset:6272\n"
        "ds_read_b64_tr_b16 a[128:129], v34 offset:12416\n"
        "ds_read_b64_tr_b16 a[130:131], v35 offset:12416\n"
        "ds_read_b64_tr_b16 a[132:133], v34 offset:136\n"
        "ds_read_b64_tr_b16 a[134:135], v35 offset:136\n"
        "ds_read_b64_tr_b16 a[136:137], v34 offset:6280\n"
        "ds_read_b64_tr_b16 a[138:139], v35 offset:6280\n"
        "ds_read_b64_tr_b16 a[140:141], v34 offset:12424\n"
        "ds_read_b64_tr_b16 a[142:143], v35 offset:12424\n"
        "s_waitcnt lgkmcnt(15)\n"
        "v_mfma_f32_16x16x32_bf16 v[46:49], a[72:75], v[50:53], 0\n"
        "s_waitcnt vmcnt(12)\n"
        "s_waitcnt vmcnt(6)\n"
        "v_mfma_f32_16x16x32_bf16 v[46:49], a[76:79], v[50:53], v[46:49]\n"
        "v_mfma_f32_16x16x32_bf16 v[46:49], a[80:83], v[50:53], v[46:49]\n"
        "s_waitcnt lgkmcnt(15)\n"
        "s_nop 0\n"
        "v_mfma_f32_16x16x32_bf16 v[54:57], a[92:95], v[50:53], 0\n"
        "s_nop 2\n"
        "v_mfma_f32_16x16x32_bf16 v[54:57], a[88:91], v[50:53], v[54:57]\n"
        "s_waitcnt lgkmcnt(15)\n"
        "v_mul_f32 v46, 0xbc2aaaab, v46\n"
        "v_mul_f32 v47, 0xbc2aaaab, v47\n"
        "v_mul_f32 v48, 0xbc2aaaab, v48\n"
        "v_mfma_f32_16x16x32_bf16 v[54:57], a[84:87], v[50:53], v[54:57]\n"
        "s_waitcnt vmcnt(0)\n"
        "v_mul_f32 v49, 0xbc2aaaab, v49\n"
        "v_cvt_pk_bf16_f32 v62, v46, v47\n"
        "v_cvt_pk_bf16_f32 v63, v48, v49\n"
        "v_mfma_f32_16x16x32_bf16 v[46:49], a[96:99], v[50:53], 0\n"
        "s_waitcnt lgkmcnt(12)\n"
        "ds_read_b128 a[0:3], v32\n"
        "v_mfma_f32_16x16x32_bf16 v[46:49], a[100:103], v[50:53], v[46:49]\n"
        "s_waitcnt lgkmcnt(7)\n"
        "ds_read_b128 a[4:7], v32 offset:64\n"
        "v_mul_f32 v54, 0xbc2aaaab, v54\n"
        "v_mul_f32 v55, 0xbc2aaaab, v55\n"
        "v_mfma_f32_16x16x32_bf16 v[46:49], a[104:107], v[50:53], v[46:49]\n"
        "s_waitcnt lgkmcnt(2)\n"
        "ds_read_b128 a[8:11], v32 offset:128\n"
        "v_mul_f32 v56, 0xbc2aaaab, v56\n"
        "v_mul_f32 v57, 0xbc2aaaab, v57\n"
        "v_mfma_f32_16x16x32_bf16 v[58:61], a[116:119], v[50:53], 0\n"
        "ds_read_b128 a[144:147], v30\n"
        "v_cvt_pk_bf16_f32 v64, v54, v55\n"
        "v_cvt_pk_bf16_f32 v65, v56, v57\n"
        "v_mfma_f32_16x16x32_bf16 v[58:61], a[112:115], v[50:53], v[58:61]\n"
        "ds_read_b128 a[148:151], v30 offset:64\n"
        "v_mul_f32 v46, 0xbc2aaaab, v46\n"
        "v_mul_f32 v47, 0xbc2aaaab, v47\n"
        "v_mfma_f32_16x16x32_bf16 v[58:61], a[108:111], v[50:53], v[58:61]\n"
        "ds_read_b128 a[152:155], v30 offset:128\n"
        "v_mul_f32 v48, 0xbc2aaaab, v48\n"
        "v_mul_f32 v49, 0xbc2aaaab, v49\n"
        "v_mfma_f32_16x16x32_bf16 v[54:57], a[120:123], v[50:53], 0\n"
        "ds_read_b128 a[156:159], v31\n"
        "v_cvt_pk_bf16_f32 v66, v46, v47\n"
        "v_cvt_pk_bf16_f32 v67, v48, v49\n"
        "v_mfma_f32_16x16x32_bf16 v[54:57], a[124:127], v[50:53], v[54:57]\n"
        "ds_read_b128 a[160:163], v31 offset:64\n"
        "v_mul_f32 v58, 0xbc2aaaab, v58\n"
        "v_mul_f32 v59, 0xbc2aaaab, v59\n"
        "v_mfma_f32_16x16x32_bf16 v[54:57], a[128:131], v[50:53], v[54:57]\n"
        "ds_read_b128 a[164:167], v31 offset:128\n"
        "v_mul_f32 v60, 0xbc2aaaab, v60\n"
        "v_mul_f32 v61, 0xbc2aaaab, v61\n"
        "v_mfma_f32_16x16x32_bf16 v[74:77], a[140:143], v[50:53], 0\n"
        "ds_read_b128 a[168:171], v30 offset:6144\n"
        "v_cvt_pk_bf16_f32 v68, v58, v59\n"
        "v_cvt_pk_bf16_f32 v69, v60, v61\n"
        "v_mfma_f32_16x16x32_bf16 v[74:77], a[136:139], v[50:53], v[74:77]\n"
        "ds_read_b128 a[172:175], v30 offset:6208\n"
        "v_mul_f32 v54, 0xbc2aaaab, v54\n"
        "v_mul_f32 v55, 0xbc2aaaab, v55\n"
        "v_mfma_f32_16x16x32_bf16 v[74:77], a[132:135], v[50:53], v[74:77]\n"
        "s_waitcnt lgkmcnt(8)\n"
        "ds_read_b128 a[176:179], v30 offset:6272\n"
        "v_mul_f32 v56, 0xbc2aaaab, v56\n"
        "v_mul_f32 v57, 0xbc2aaaab, v57\n"
        "s_waitcnt lgkmcnt(6)\n"
        "v_mfma_f32_16x16x32_bf16 v[46:49], a[144:147], v[62:65], 0\n"
        "ds_read_b128 a[180:183], v31 offset:6144\n"
        "v_cvt_pk_bf16_f32 v70, v54, v55\n"
        "v_cvt_pk_bf16_f32 v71, v56, v57\n"
        "v_mfma_f32_16x16x32_bf16 v[46:49], a[148:151], v[66:69], v[46:49]\n"
        "ds_read_b128 a[184:187], v31 offset:6208\n"
        "v_mul_f32 v74, 0xbc2aaaab, v74\n"
        "v_mul_f32 v75, 0xbc2aaaab, v75\n"
        "v_cvt_pk_bf16_f32 v72, v74, v75\n"
        "v_mul_f32 v76, 0xbc2aaaab, v76\n"
        "v_mul_f32 v77, 0xbc2aaaab, v77\n"
        "v_cvt_pk_bf16_f32 v73, v76, v77\n"
        "s_nop 1\n"
        "v_mfma_f32_16x16x32_bf16 v[46:49], a[152:155], v[70:73], v[46:49]\n"
        "buffer_load_dwordx4 v[196:199], v36, s[36:39], 0 offen\n"
        "v_mfma_f32_16x16x32_bf16 v[54:57], a[152:155], a[8:11], v[46:49]\n"
        "s_waitcnt lgkmcnt(5)\n"
        "ds_read_b128 a[188:191], v31 offset:6272\n"
        "ds_read_b128 a[192:195], v30 offset:12288\n"
        "v_mfma_f32_16x16x32_bf16 v[54:57], a[148:151], a[4:7], v[54:57]\n"
        "ds_read_b128 a[196:199], v30 offset:12352\n"
        "v_mfma_f32_16x16x32_bf16 v[54:57], a[144:147], a[0:3], v[54:57]\n"
        "s_waitcnt lgkmcnt(5)\n"
        "ds_read_b128 a[200:203], v30 offset:12416\n"
        "v_mfma_f32_16x16x32_bf16 v[50:53], a[156:159], v[62:65], 0\n"
        "s_nop 0\n"
        "ds_read_b128 a[204:207], v31 offset:12288\n"
        "v_mfma_f32_16x16x32_bf16 v[50:53], a[160:163], v[66:69], v[50:53]\n"
        "s_nop 0\n"
        "ds_read_b128 a[208:211], v31 offset:12352\n"
        "v_add_f32 v255, v54, v55\n"
        "v_add_f32 v255, v255, v56\n"
        "v_mfma_f32_16x16x32_bf16 v[50:53], a[164:167], v[70:73], v[50:53]\n"
        "ds_read_b128 a[212:215], v31 offset:12416\n"
        "v_add_f32 v255, v255, v57\n"
        "v_mfma_f32_16x16x32_bf16 v[86:89], a[164:167], a[8:11], v[50:53]\n"
        "ds_read_b128 a[12:15], v33\n"
        "v_mfma_f32_16x16x32_bf16 v[86:89], a[160:163], a[4:7], v[86:89]\n"
        "ds_read_b128 a[16:19], v33 offset:64\n"
        "v_mfma_f32_16x16x32_bf16 v[86:89], a[156:159], a[0:3], v[86:89]\n"
        "buffer_load_dwordx4 v[202:205], v36, s[36:39], 0 offen offset:16\n"
        "v_mfma_f32_16x16x32_bf16 v[74:77], a[168:171], v[62:65], 0\n"
        "s_mov_b32 s78, 0xf0000f\n"
        "ds_read_b128 a[20:23], v33 offset:128\n"
        "ds_read_b128 a[24:27], v32 offset:6144\n"
        "v_mfma_f32_16x16x32_bf16 v[74:77], a[172:175], v[66:69], v[74:77]\n"
        "s_waitcnt lgkmcnt(10)\n"
        "ds_read_b128 a[28:31], v32 offset:6208\n"
        "v_add_f32 v255, v255, v86\n"
        "v_add_f32 v255, v255, v87\n"
        "v_mfma_f32_16x16x32_bf16 v[74:77], a[176:179], v[70:73], v[74:77]\n"
        "s_mov_b32 s79, 0xf0000f00\n"
        "ds_read_b128 a[32:35], v32 offset:6272\n"
        "v_add_f32 v255, v255, v88\n"
        "v_add_f32 v255, v255, v89\n"
        "v_mfma_f32_16x16x32_bf16 v[90:93], a[176:179], a[8:11], v[74:77]\n"
        "s_waitcnt lgkmcnt(9)\n"
        "ds_read_b128 a[36:39], v33 offset:6144\n"
        "v_mfma_f32_16x16x32_bf16 v[90:93], a[172:175], a[4:7], v[90:93]\n"
        "s_mov_b32 s76, 0x11111111\n"
        "ds_read_b128 a[40:43], v33 offset:6208\n"
        "v_mfma_f32_16x16x32_bf16 v[90:93], a[168:171], a[0:3], v[90:93]\n"
        "s_mov_b32 s77, 0x11111111\n"
        "ds_read_b128 a[44:47], v33 offset:6272\n"
        "v_mfma_f32_16x16x32_bf16 v[82:85], a[180:183], v[62:65], 0\n"
        "s_waitcnt lgkmcnt(9)\n"
        "ds_read_b128 a[48:51], v32 offset:12288\n"
        "v_mfma_f32_16x16x32_bf16 v[82:85], a[184:187], v[66:69], v[82:85]\n"
        "s_mov_b32 s56, 0x22222222\n"
        "ds_read_b128 a[52:55], v32 offset:12352\n"
        "v_add_f32 v255, v255, v90\n"
        "v_add_f32 v255, v255, v91\n"
        "v_mfma_f32_16x16x32_bf16 v[82:85], a[188:191], v[70:73], v[82:85]\n"
        "buffer_load_dwordx4 v[208:211], v36, s[36:39], 0 offen offset:32\n"
        "v_add_f32 v255, v255, v92\n"
        "v_mfma_f32_16x16x32_bf16 v[94:97], a[188:191], a[8:11], v[82:85]\n"
        "s_mov_b32 s57, 0x22222222\n"
        "ds_read_b128 a[56:59], v32 offset:12416\n"
        "v_add_f32 v255, v255, v93\n"
        "v_mfma_f32_16x16x32_bf16 v[94:97], a[184:187], a[4:7], v[94:97]\n"
        "s_mov_b32 s58, 0x44444444\n"
        "ds_read_b128 a[60:63], v33 offset:12288\n"
        "ds_read_b128 a[64:67], v33 offset:12352\n"
        "v_mfma_f32_16x16x32_bf16 v[94:97], a[180:183], a[0:3], v[94:97]\n"
        "s_waitcnt lgkmcnt(11)\n"
        "ds_read_b128 a[68:71], v33 offset:12416\n"
        "v_mfma_f32_16x16x32_bf16 v[78:81], a[192:195], v[62:65], 0\n"
        "s_mov_b32 s59, 0x44444444\n"
        "s_nop 1\n"
        "v_mfma_f32_16x16x32_bf16 v[78:81], a[196:199], v[66:69], v[78:81]\n"
        "s_mov_b32 s60, 0x88888888\n"
        "v_add_f32 v255, v255, v94\n"
        "v_add_f32 v255, v255, v95\n"
        "v_add_f32 v255, v255, v96\n"
        "v_mfma_f32_16x16x32_bf16 v[78:81], a[200:203], v[70:73], v[78:81]\n"
        "s_mov_b32 s61, 0x88888888\n"
        "v_add_f32 v255, v255, v97\n"
        "v_mfma_f32_16x16x32_bf16 v[98:101], a[200:203], a[8:11], v[78:81]\n"
        "s_mov_b32 s62, 0xffff\n"
        "v_mfma_f32_16x16x32_bf16 v[98:101], a[196:199], a[4:7], v[98:101]\n"
        "s_mov_b32 s63, 0x0\n"
        "v_mfma_f32_16x16x32_bf16 v[98:101], a[192:195], a[0:3], v[98:101]\n"
        "buffer_load_dwordx4 v[58:61], v37, s[36:39], 0 offen\n"
        "v_mfma_f32_16x16x32_bf16 v[30:33], a[204:207], v[62:65], 0\n"
        "s_waitcnt lgkmcnt(9)\n"
        "v_mfma_f32_16x16x32_bf16 v[30:33], a[208:211], v[66:69], v[30:33]\n"
        "s_mov_b32 s64, 0xffff0000\n"
        "s_nop 0\n"
        "v_mfma_f32_16x16x32_bf16 v[30:33], a[212:215], v[70:73], v[30:33]\n"
        "s_mov_b32 s65, 0x0\n"
        "v_add_f32 v255, v255, v98\n"
        "v_add_f32 v255, v255, v99\n"
        "v_add_f32 v255, v255, v100\n"
        "v_mfma_f32_16x16x32_bf16 v[102:105], a[212:215], a[8:11], v[30:33]\n"
        "s_waitcnt lgkmcnt(0)\n"
        "v_add_f32 v255, v255, v101\n"
        "v_mfma_f32_16x16x32_bf16 v[102:105], a[208:211], a[4:7], v[102:105]\n"
        "s_mov_b32 s66, 0x0\n"
        "v_mfma_f32_16x16x32_bf16 v[102:105], a[204:207], a[0:3], v[102:105]\n"
        "s_mov_b32 s67, 0xffff\n"
        "s_nop 0\n"
        "v_mfma_f32_16x16x32_bf16 v[106:109], a[144:147], a[12:15], v[46:49]\n"
        "s_mov_b32 s68, 0x0\n"
        "s_nop 1\n"
        "v_mfma_f32_16x16x32_bf16 v[106:109], a[148:151], a[16:19], v[106:109]\n"
        "s_mov_b32 s69, 0xffff0000\n"
        "v_add_f32 v255, v255, v102\n"
        "v_add_f32 v255, v255, v103\n"
        "v_add_f32 v255, v255, v104\n"
        "v_mfma_f32_16x16x32_bf16 v[106:109], a[152:155], a[20:23], v[106:109]\n"
        "buffer_load_dwordx4 v[64:67], v37, s[36:39], 0 offen offset:16\n"
        "v_add_f32 v255, v255, v105\n"
        "v_mfma_f32_16x16x32_bf16 v[122:125], a[164:167], a[20:23], v[50:53]\n"
        "s_barrier\n"
        "v_mov_b32 v253, v255\n"
        "v_mfma_f32_16x16x32_bf16 v[122:125], a[160:163], a[16:19], v[122:125]\n"
        "s_mov_b32 s70, 0x0\n"
        "v_permlane32_swap_b32 v255, v253\n"
        "v_add_f32 v255, v255, v253\n"
        "v_mfma_f32_16x16x32_bf16 v[122:125], a[156:159], a[12:15], v[122:125]\n"
        "s_mov_b32 s71, 0xffffffff\n"
        "v_mov_b32 v253, v255\n"
        "v_add_f32 v254, v106, v107\n"
        "v_add_f32 v254, v254, v108\n"
        "v_mfma_f32_16x16x32_bf16 v[126:129], a[168:171], a[12:15], v[74:77]\n"
        "s_mov_b32 s72, 0x0\n"
        "v_permlane16_swap_b32 v255, v253\n"
        "v_add_f32 v255, v255, v253\n"
        "v_mfma_f32_16x16x32_bf16 v[126:129], a[172:175], a[16:19], v[126:129]\n"
        "s_mov_b32 s73, 0x20000\n"
        "v_mul_f32 v255, 0xbac909aa, v255\n"
        "v_add_f32 v254, v254, v109\n"
        "v_fma_f32 v54, v54, s24, v255\n"
        "v_mfma_f32_16x16x32_bf16 v[126:129], a[176:179], a[20:23], v[126:129]\n"
        "s_mov_b64 vcc, s[78:79]\n"
        "v_fma_f32 v55, v55, s24, v255\n"
        "v_fma_f32 v56, v56, s24, v255\n"
        "v_fma_f32 v57, v57, s24, v255\n"
        "v_mfma_f32_16x16x32_bf16 v[118:121], a[188:191], a[20:23], v[82:85]\n"
        "s_mov_b32 s80, 0x3c2aaaab\n"
        "v_fma_f32 v86, v86, s24, v255\n"
        "v_fma_f32 v87, v87, s24, v255\n"
        "v_fma_f32 v88, v88, s24, v255\n"
        "v_mfma_f32_16x16x32_bf16 v[118:121], a[184:187], a[16:19], v[118:121]\n"
        "s_mov_b32 s81, 0x3727c5ac\n"
        "v_fma_f32 v89, v89, s24, v255\n"
        "v_fma_f32 v90, v90, s24, v255\n"
        "v_fma_f32 v91, v91, s24, v255\n"
        "v_mfma_f32_16x16x32_bf16 v[118:121], a[180:183], a[12:15], v[118:121]\n"
        "buffer_load_dwordx4 v[70:73], v37, s[36:39], 0 offen offset:32\n"
        "v_fma_f32 v92, v92, s24, v255\n"
        "v_mfma_f32_16x16x32_bf16 v[130:133], a[192:195], a[12:15], v[78:81]\n"
        "s_mov_b32 s82, 0xc000c000\n"
        "v_fma_f32 v93, v93, s24, v255\n"
        "v_fma_f32 v94, v94, s24, v255\n"
        "v_fma_f32 v95, v95, s24, v255\n"
        "v_mfma_f32_16x16x32_bf16 v[130:133], a[196:199], a[16:19], v[130:133]\n"
        "s_mov_b32 s84, 0x80000000\n"
        "v_fma_f32 v96, v96, s24, v255\n"
        "v_fma_f32 v97, v97, s24, v255\n"
        "v_fma_f32 v98, v98, s24, v255\n"
        "v_mfma_f32_16x16x32_bf16 v[130:133], a[200:203], a[20:23], v[130:133]\n"
        "s_mov_b32 s85, 19\n"
        "v_fma_f32 v99, v99, s24, v255\n"
        "v_fma_f32 v100, v100, s24, v255\n"
        "v_fma_f32 v101, v101, s24, v255\n"
        "v_mfma_f32_16x16x32_bf16 v[134:137], a[212:215], a[20:23], v[30:33]\n"
        "s_mov_b32 s86, 0x80000000\n"
        "v_fma_f32 v102, v102, s24, v255\n"
        "v_fma_f32 v103, v103, s24, v255\n"
        "v_fma_f32 v104, v104, s24, v255\n"
        "v_mfma_f32_16x16x32_bf16 v[134:137], a[208:211], a[16:19], v[134:137]\n"
        "s_mov_b32 s89, 0x80000000\n"
        "v_fma_f32 v105, v105, s24, v255\n"
        "v_cvt_pk_bf16_f32 v110, v54, v55\n"
        "v_cvt_pk_bf16_f32 v111, v56, v57\n"
        "v_mfma_f32_16x16x32_bf16 v[134:137], a[204:207], a[12:15], v[134:137]\n"
        "s_mov_b32 s29, 1\n"
        "v_cvt_pk_bf16_f32 v112, v86, v87\n"
        "v_cvt_pk_bf16_f32 v113, v88, v89\n"
        "v_cvt_pk_bf16_f32 v34, v90, v91\n"
        "v_mfma_f32_16x16x32_bf16 v[54:57], a[144:147], a[24:27], v[46:49]\n"
        "s_mov_b32 s31, 0x400000\n"
        "ds_write_b128 v39, v[110:113]\n"
        "v_cvt_pk_bf16_f32 v35, v92, v93\n"
        "v_mfma_f32_16x16x32_bf16 v[54:57], a[148:151], a[28:31], v[54:57]\n"
        "s_add_u32 m0, s52, 0\n"
        "v_cvt_pk_bf16_f32 v36, v94, v95\n"
        "v_cvt_pk_bf16_f32 v37, v96, v97\n"
        "v_cvt_pk_bf16_f32 v90, v98, v99\n"
        "v_mfma_f32_16x16x32_bf16 v[54:57], a[152:155], a[32:35], v[54:57]\n"
        "buffer_load_dwordx4 v0, s[36:39], s49 offen lds\n"
        "v_cvt_pk_bf16_f32 v91, v100, v101\n"
        "v_mfma_f32_16x16x32_bf16 v[86:89], a[164:167], a[32:35], v[50:53]\n"
        "s_mov_b32 s87, s49\n"
        "ds_write_b128 v39, v[34:37] offset:1024\n"
        "v_cvt_pk_bf16_f32 v92, v102, v103\n"
        "v_mfma_f32_16x16x32_bf16 v[86:89], a[160:163], a[28:31], v[86:89]\n"
        "s_mov_b32 s88, s50\n"
        "v_cvt_pk_bf16_f32 v93, v104, v105\n"
        "v_add_f32 v254, v254, v122\n"
        "v_add_f32 v254, v254, v123\n"
        "v_mfma_f32_16x16x32_bf16 v[86:89], a[156:159], a[24:27], v[86:89]\n"
        "s_mov_b64 s[44:45], s[40:41]\n"
        "ds_write_b128 v39, v[90:93] offset:2048\n"
        "v_add_f32 v254, v254, v124\n"
        "v_mfma_f32_16x16x32_bf16 v[94:97], a[168:171], a[24:27], v[74:77]\n"
        "s_mov_b32 s46, 0\n"
        "v_add_f32 v254, v254, v125\n"
        "v_add_f32 v254, v254, v126\n"
        "v_add_f32 v254, v254, v127\n"
        "v_mfma_f32_16x16x32_bf16 v[94:97], a[172:175], a[28:31], v[94:97]\n"
        "s_mov_b32 s47, s43\n"
        "v_add_f32 v254, v254, v128\n"
        "v_add_f32 v254, v254, v129\n"
        "v_add_f32 v254, v254, v118\n"
        "v_mfma_f32_16x16x32_bf16 v[94:97], a[176:179], a[32:35], v[94:97]\n"
        "buffer_load_dwordx4 v1, s[36:39], s49 offen offset:1024 lds\n"
        "v_add_f32 v254, v254, v119\n"
        "v_mfma_f32_16x16x32_bf16 v[34:37], a[188:191], a[32:35], v[82:85]\n"
        "s_waitcnt lgkmcnt(3)\n"
        "v_add_f32 v254, v254, v120\n"
        "v_add_f32 v254, v254, v121\n"
        "v_add_f32 v254, v254, v130\n"
        "v_mfma_f32_16x16x32_bf16 v[34:37], a[184:187], a[28:31], v[34:37]\n"
        "s_waitcnt lgkmcnt(3)\n"
        "v_add_f32 v254, v254, v131\n"
        "v_add_f32 v254, v254, v132\n"
        "v_add_f32 v254, v254, v133\n"
        "v_mfma_f32_16x16x32_bf16 v[34:37], a[180:183], a[24:27], v[34:37]\n"
        "s_waitcnt lgkmcnt(3)\n"
        "v_add_f32 v254, v254, v134\n"
        "v_add_f32 v254, v254, v135\n"
        "v_add_f32 v254, v254, v136\n"
        "v_mfma_f32_16x16x32_bf16 v[90:93], a[192:195], a[24:27], v[78:81]\n"
        "v_add_f32 v254, v254, v137\n"
        "v_mov_b32 v253, v254\n"
        "v_add_f32 v255, v54, v55\n"
        "v_mfma_f32_16x16x32_bf16 v[90:93], a[196:199], a[28:31], v[90:93]\n"
        "v_permlane32_swap_b32 v254, v253\n"
        "v_add_f32 v254, v254, v253\n"
        "v_mfma_f32_16x16x32_bf16 v[90:93], a[200:203], a[32:35], v[90:93]\n"
        "buffer_load_dwordx4 v2, s[36:39], s49 offen offset:2048 lds\n"
        "v_mov_b32 v253, v254\n"
        "v_mfma_f32_16x16x32_bf16 v[142:145], a[212:215], a[32:35], v[30:33]\n"
        "s_nop 0\n"
        "v_permlane16_swap_b32 v254, v253\n"
        "v_add_f32 v254, v254, v253\n"
        "v_mfma_f32_16x16x32_bf16 v[142:145], a[208:211], a[28:31], v[142:145]\n"
        "v_add_f32 v255, v255, v56\n"
        "v_mul_f32 v254, 0xbac909aa, v254\n"
        "v_add_f32 v255, v255, v57\n"
        "v_mfma_f32_16x16x32_bf16 v[142:145], a[204:207], a[24:27], v[142:145]\n"
        "v_fma_f32 v106, v106, s24, v254\n"
        "v_fma_f32 v107, v107, s24, v254\n"
        "v_fma_f32 v108, v108, s24, v254\n"
        "v_mfma_f32_16x16x32_bf16 v[98:101], a[144:147], a[36:39], v[46:49]\n"
        "v_fma_f32 v109, v109, s24, v254\n"
        "v_fma_f32 v122, v122, s24, v254\n"
        "v_fma_f32 v123, v123, s24, v254\n"
        "v_mfma_f32_16x16x32_bf16 v[98:101], a[148:151], a[40:43], v[98:101]\n"
        "v_fma_f32 v124, v124, s24, v254\n"
        "v_fma_f32 v125, v125, s24, v254\n"
        "v_fma_f32 v126, v126, s24, v254\n"
        "v_mfma_f32_16x16x32_bf16 v[98:101], a[152:155], a[44:47], v[98:101]\n"
        "buffer_load_dwordx4 v3, s[36:39], s49 offen offset:3072 lds\n"
        "v_fma_f32 v127, v127, s24, v254\n"
        "v_mfma_f32_16x16x32_bf16 v[102:105], a[164:167], a[44:47], v[50:53]\n"
        "v_fma_f32 v128, v128, s24, v254\n"
        "v_fma_f32 v129, v129, s24, v254\n"
        "v_fma_f32 v118, v118, s24, v254\n"
        "v_mfma_f32_16x16x32_bf16 v[102:105], a[160:163], a[40:43], v[102:105]\n"
        "v_fma_f32 v119, v119, s24, v254\n"
        "v_fma_f32 v120, v120, s24, v254\n"
        "v_fma_f32 v121, v121, s24, v254\n"
        "v_mfma_f32_16x16x32_bf16 v[102:105], a[156:159], a[36:39], v[102:105]\n"
        "v_fma_f32 v130, v130, s24, v254\n"
        "v_fma_f32 v131, v131, s24, v254\n"
        "v_fma_f32 v132, v132, s24, v254\n"
        "v_mfma_f32_16x16x32_bf16 v[114:117], a[168:171], a[36:39], v[74:77]\n"
        "v_fma_f32 v133, v133, s24, v254\n"
        "v_fma_f32 v134, v134, s24, v254\n"
        "v_fma_f32 v135, v135, s24, v254\n"
        "v_mfma_f32_16x16x32_bf16 v[114:117], a[172:175], a[40:43], v[114:117]\n"
        "s_add_u32 m0, s52, 4096\n"
        "v_fma_f32 v136, v136, s24, v254\n"
        "v_fma_f32 v137, v137, s24, v254\n"
        "v_cvt_pk_bf16_f32 v138, v106, v107\n"
        "v_mfma_f32_16x16x32_bf16 v[114:117], a[176:179], a[44:47], v[114:117]\n"
        "buffer_load_dwordx4 v4, s[36:39], s49 offen lds\n"
        "v_cvt_pk_bf16_f32 v139, v108, v109\n"
        "v_mfma_f32_16x16x32_bf16 v[110:113], a[188:191], a[44:47], v[82:85]\n"
        "v_cvt_pk_bf16_f32 v140, v122, v123\n"
        "v_cvt_pk_bf16_f32 v141, v124, v125\n"
        "v_cvt_pk_bf16_f32 v106, v126, v127\n"
        "v_mfma_f32_16x16x32_bf16 v[110:113], a[184:187], a[40:43], v[110:113]\n"
        "ds_write_b128 v40, v[138:141]\n"
        "v_cvt_pk_bf16_f32 v107, v128, v129\n"
        "v_mfma_f32_16x16x32_bf16 v[110:113], a[180:183], a[36:39], v[110:113]\n"
        "v_cvt_pk_bf16_f32 v108, v118, v119\n"
        "v_cvt_pk_bf16_f32 v109, v120, v121\n"
        "v_cvt_pk_bf16_f32 v122, v130, v131\n"
        "v_mfma_f32_16x16x32_bf16 v[118:121], a[192:195], a[36:39], v[78:81]\n"
        "ds_write_b128 v40, v[106:109] offset:1024\n"
        "v_cvt_pk_bf16_f32 v123, v132, v133\n"
        "v_mfma_f32_16x16x32_bf16 v[118:121], a[196:199], a[40:43], v[118:121]\n"
        "v_cvt_pk_bf16_f32 v124, v134, v135\n"
        "v_cvt_pk_bf16_f32 v125, v136, v137\n"
        "v_add_f32 v255, v255, v86\n"
        "v_mfma_f32_16x16x32_bf16 v[118:121], a[200:203], a[44:47], v[118:121]\n"
        "buffer_load_dwordx4 v5, s[36:39], s49 offen offset:1024 lds\n"
        "v_add_f32 v255, v255, v87\n"
        "v_mfma_f32_16x16x32_bf16 v[106:109], a[212:215], a[44:47], v[30:33]\n"
        "ds_write_b128 v40, v[122:125] offset:2048\n"
        "v_add_f32 v255, v255, v88\n"
        "v_mfma_f32_16x16x32_bf16 v[106:109], a[208:211], a[40:43], v[106:109]\n"
        "v_add_f32 v255, v255, v89\n"
        "v_add_f32 v255, v255, v94\n"
        "v_add_f32 v255, v255, v95\n"
        "v_mfma_f32_16x16x32_bf16 v[106:109], a[204:207], a[36:39], v[106:109]\n"
        "v_add_f32 v255, v255, v96\n"
        "v_add_f32 v255, v255, v97\n"
        "v_add_f32 v255, v255, v34\n"
        "v_mfma_f32_16x16x32_bf16 v[126:129], a[144:147], a[48:51], v[46:49]\n"
        "v_add_f32 v255, v255, v35\n"
        "v_add_f32 v255, v255, v36\n"
        "v_add_f32 v255, v255, v37\n"
        "v_mfma_f32_16x16x32_bf16 v[126:129], a[148:151], a[52:55], v[126:129]\n"
        "v_add_f32 v255, v255, v90\n"
        "v_add_f32 v255, v255, v91\n"
        "v_add_f32 v255, v255, v92\n"
        "v_mfma_f32_16x16x32_bf16 v[126:129], a[152:155], a[56:59], v[126:129]\n"
        "buffer_load_dwordx4 v6, s[36:39], s49 offen offset:2048 lds\n"
        "v_add_f32 v255, v255, v93\n"
        "v_mfma_f32_16x16x32_bf16 v[130:133], a[164:167], a[56:59], v[50:53]\n"
        "v_add_f32 v255, v255, v142\n"
        "v_add_f32 v255, v255, v143\n"
        "v_add_f32 v255, v255, v144\n"
        "v_mfma_f32_16x16x32_bf16 v[130:133], a[160:163], a[52:55], v[130:133]\n"
        "v_add_f32 v255, v255, v145\n"
        "v_mov_b32 v254, v255\n"
        "v_add_f32 v62, v98, v99\n"
        "v_mfma_f32_16x16x32_bf16 v[130:133], a[156:159], a[48:51], v[130:133]\n"
        "v_permlane32_swap_b32 v255, v254\n"
        "v_add_f32 v255, v255, v254\n"
        "v_mfma_f32_16x16x32_bf16 v[122:125], a[168:171], a[48:51], v[74:77]\n"
        "v_mov_b32 v254, v255\n"
        "v_add_f32 v62, v62, v100\n"
        "v_add_f32 v62, v62, v101\n"
        "v_mfma_f32_16x16x32_bf16 v[122:125], a[172:175], a[52:55], v[122:125]\n"
        "v_permlane16_swap_b32 v255, v254\n"
        "v_add_f32 v255, v255, v254\n"
        "v_mfma_f32_16x16x32_bf16 v[122:125], a[176:179], a[56:59], v[122:125]\n"
        "buffer_load_dwordx4 v7, s[36:39], s49 offen offset:3072 lds\n"
        "v_mul_f32 v255, 0xbac909aa, v255\n"
        "v_mfma_f32_16x16x32_bf16 v[134:137], a[188:191], a[56:59], v[82:85]\n"
        "v_fma_f32 v54, v54, s24, v255\n"
        "v_fma_f32 v55, v55, s24, v255\n"
        "v_fma_f32 v56, v56, s24, v255\n"
        "v_mfma_f32_16x16x32_bf16 v[134:137], a[184:187], a[52:55], v[134:137]\n"
        "v_fma_f32 v57, v57, s24, v255\n"
        "v_fma_f32 v86, v86, s24, v255\n"
        "v_fma_f32 v87, v87, s24, v255\n"
        "v_mfma_f32_16x16x32_bf16 v[134:137], a[180:183], a[48:51], v[134:137]\n"
        "v_fma_f32 v88, v88, s24, v255\n"
        "v_fma_f32 v89, v89, s24, v255\n"
        "v_fma_f32 v94, v94, s24, v255\n"
        "v_mfma_f32_16x16x32_bf16 v[146:149], a[192:195], a[48:51], v[78:81]\n"
        "v_fma_f32 v95, v95, s24, v255\n"
        "v_fma_f32 v96, v96, s24, v255\n"
        "v_fma_f32 v97, v97, s24, v255\n"
        "v_mfma_f32_16x16x32_bf16 v[146:149], a[196:199], a[52:55], v[146:149]\n"
        "s_add_u32 m0, s52, 8192\n"
        "v_fma_f32 v34, v34, s24, v255\n"
        "v_fma_f32 v35, v35, s24, v255\n"
        "v_fma_f32 v36, v36, s24, v255\n"
        "v_mfma_f32_16x16x32_bf16 v[146:149], a[200:203], a[56:59], v[146:149]\n"
        "buffer_load_dwordx4 v8, s[36:39], s49 offen lds\n"
        "v_fma_f32 v37, v37, s24, v255\n"
        "v_mfma_f32_16x16x32_bf16 v[150:153], a[212:215], a[56:59], v[30:33]\n"
        "v_fma_f32 v90, v90, s24, v255\n"
        "v_fma_f32 v91, v91, s24, v255\n"
        "v_fma_f32 v92, v92, s24, v255\n"
        "v_mfma_f32_16x16x32_bf16 v[150:153], a[208:211], a[52:55], v[150:153]\n"
        "v_fma_f32 v93, v93, s24, v255\n"
        "v_fma_f32 v142, v142, s24, v255\n"
        "v_fma_f32 v143, v143, s24, v255\n"
        "v_mfma_f32_16x16x32_bf16 v[150:153], a[204:207], a[48:51], v[150:153]\n"
        "v_fma_f32 v144, v144, s24, v255\n"
        "v_fma_f32 v145, v145, s24, v255\n"
        "v_cvt_pk_bf16_f32 v154, v54, v55\n"
        "v_mfma_f32_16x16x32_bf16 v[138:141], a[144:147], a[60:63], v[46:49]\n"
        "v_cvt_pk_bf16_f32 v155, v56, v57\n"
        "v_cvt_pk_bf16_f32 v156, v86, v87\n"
        "v_cvt_pk_bf16_f32 v157, v88, v89\n"
        "v_mfma_f32_16x16x32_bf16 v[138:141], a[148:151], a[64:67], v[138:141]\n"
        "ds_write_b128 v41, v[154:157]\n"
        "v_cvt_pk_bf16_f32 v46, v94, v95\n"
        "v_mfma_f32_16x16x32_bf16 v[138:141], a[152:155], a[68:71], v[138:141]\n"
        "v_cvt_pk_bf16_f32 v47, v96, v97\n"
        "v_cvt_pk_bf16_f32 v48, v34, v35\n"
        "v_cvt_pk_bf16_f32 v49, v36, v37\n"
        "v_mfma_f32_16x16x32_bf16 v[54:57], a[164:167], a[68:71], v[50:53]\n"
        "ds_write_b128 v41, v[46:49] offset:1024\n"
        "v_cvt_pk_bf16_f32 v34, v90, v91\n"
        "v_mfma_f32_16x16x32_bf16 v[54:57], a[160:163], a[64:67], v[54:57]\n"
        "v_cvt_pk_bf16_f32 v35, v92, v93\n"
        "v_cvt_pk_bf16_f32 v36, v142, v143\n"
        "v_cvt_pk_bf16_f32 v37, v144, v145\n"
        "v_mfma_f32_16x16x32_bf16 v[54:57], a[156:159], a[60:63], v[54:57]\n"
        "ds_write_b128 v41, v[34:37] offset:2048\n"
        "v_add_f32 v62, v62, v102\n"
        "v_mfma_f32_16x16x32_bf16 v[38:41], a[168:171], a[60:63], v[74:77]\n"
        "v_add_f32 v62, v62, v103\n"
        "v_add_f32 v62, v62, v104\n"
        "v_add_f32 v62, v62, v105\n"
        "v_mfma_f32_16x16x32_bf16 v[38:41], a[172:175], a[64:67], v[38:41]\n"
        "v_add_f32 v62, v62, v114\n"
        "v_add_f32 v62, v62, v115\n"
        "v_add_f32 v62, v62, v116\n"
        "v_mfma_f32_16x16x32_bf16 v[38:41], a[176:179], a[68:71], v[38:41]\n"
        "v_add_f32 v62, v62, v117\n"
        "v_add_f32 v62, v62, v110\n"
        "v_add_f32 v62, v62, v111\n"
        "v_mfma_f32_16x16x32_bf16 v[46:49], a[188:191], a[68:71], v[82:85]\n"
        "v_add_f32 v62, v62, v112\n"
        "v_add_f32 v62, v62, v113\n"
        "v_add_f32 v62, v62, v118\n"
        "v_mfma_f32_16x16x32_bf16 v[46:49], a[184:187], a[64:67], v[46:49]\n"
        "v_add_f32 v62, v62, v119\n"
        "v_add_f32 v62, v62, v120\n"
        "v_add_f32 v62, v62, v121\n"
        "v_mfma_f32_16x16x32_bf16 v[46:49], a[180:183], a[60:63], v[46:49]\n"
        "v_add_f32 v62, v62, v106\n"
        "v_add_f32 v62, v62, v107\n"
        "v_add_f32 v62, v62, v108\n"
        "v_mfma_f32_16x16x32_bf16 v[34:37], a[192:195], a[60:63], v[78:81]\n"
        "v_add_f32 v62, v62, v109\n"
        "v_mov_b32 v63, v62\n"
        "v_add_f32 v68, v126, v127\n"
        "v_mfma_f32_16x16x32_bf16 v[34:37], a[196:199], a[64:67], v[34:37]\n"
        "v_permlane32_swap_b32 v62, v63\n"
        "v_add_f32 v62, v62, v63\n"
        "v_mfma_f32_16x16x32_bf16 v[34:37], a[200:203], a[68:71], v[34:37]\n"
        "v_mov_b32 v63, v62\n"
        "v_add_f32 v68, v68, v128\n"
        "v_add_f32 v68, v68, v129\n"
        "v_mfma_f32_16x16x32_bf16 v[50:53], a[212:215], a[68:71], v[30:33]\n"
        "v_permlane16_swap_b32 v62, v63\n"
        "v_add_f32 v62, v62, v63\n"
        "v_mfma_f32_16x16x32_bf16 v[50:53], a[208:211], a[64:67], v[50:53]\n"
        "v_mul_f32 v62, 0xbac909aa, v62\n"
        "v_add_f32 v68, v68, v130\n"
        "v_add_f32 v68, v68, v131\n"
        "v_mfma_f32_16x16x32_bf16 v[50:53], a[204:207], a[60:63], v[50:53]\n"
        "v_add_f32 v68, v68, v132\n"
        "v_add_f32 v68, v68, v133\n"
        "v_add_f32 v68, v68, v122\n"
        "v_add_f32 v68, v68, v123\n"
        "v_add_f32 v68, v68, v124\n"
        "v_add_f32 v68, v68, v125\n"
        "v_add_f32 v68, v68, v134\n"
        "v_add_f32 v68, v68, v135\n"
        "v_add_f32 v68, v68, v136\n"
        "v_add_f32 v68, v68, v137\n"
        "v_add_f32 v68, v68, v146\n"
        "v_add_f32 v68, v68, v147\n"
        "v_add_f32 v68, v68, v148\n"
        "v_add_f32 v68, v68, v149\n"
        "v_add_f32 v68, v68, v150\n"
        "v_add_f32 v68, v68, v151\n"
        "v_add_f32 v68, v68, v152\n"
        "v_add_f32 v68, v68, v153\n"
        "v_mov_b32 v69, v68\n"
        "v_add_f32 v94, v138, v139\n"
        "v_add_f32 v94, v94, v140\n"
        "v_permlane32_swap_b32 v68, v69\n"
        "v_add_f32 v68, v68, v69\n"
        "v_mov_b32 v69, v68\n"
        "v_add_f32 v94, v94, v141\n"
        "v_add_f32 v94, v94, v54\n"
        "v_permlane16_swap_b32 v68, v69\n"
        "v_add_f32 v68, v68, v69\n"
        "v_mul_f32 v68, 0xbac909aa, v68\n"
        "v_add_f32 v94, v94, v55\n"
        "v_add_f32 v94, v94, v56\n"
        "v_add_f32 v94, v94, v57\n"
        "v_add_f32 v94, v94, v38\n"
        "v_add_f32 v94, v94, v39\n"
        "v_add_f32 v94, v94, v40\n"
        "v_add_f32 v94, v94, v41\n"
        "v_add_f32 v94, v94, v46\n"
        "v_add_f32 v94, v94, v47\n"
        "v_add_f32 v94, v94, v48\n"
        "v_add_f32 v94, v94, v49\n"
        "v_add_f32 v94, v94, v34\n"
        "v_add_f32 v94, v94, v35\n"
        "v_add_f32 v94, v94, v36\n"
        "v_add_f32 v94, v94, v37\n"
        "v_pk_fma_f32 v[98:99], v[98:99], s[24:25], v[62:63] op_sel_hi:[1,0,0]\n"
        "v_pk_fma_f32 v[100:101], v[100:101], s[24:25], v[62:63] op_sel_hi:[1,0,0]\n"
        "v_pk_fma_f32 v[102:103], v[102:103], s[24:25], v[62:63] op_sel_hi:[1,0,0]\n"
        "v_pk_fma_f32 v[104:105], v[104:105], s[24:25], v[62:63] op_sel_hi:[1,0,0]\n"
        "v_pk_fma_f32 v[114:115], v[114:115], s[24:25], v[62:63] op_sel_hi:[1,0,0]\n"
        "v_pk_fma_f32 v[116:117], v[116:117], s[24:25], v[62:63] op_sel_hi:[1,0,0]\n"
        "v_pk_fma_f32 v[110:111], v[110:111], s[24:25], v[62:63] op_sel_hi:[1,0,0]\n"
        "v_pk_fma_f32 v[112:113], v[112:113], s[24:25], v[62:63] op_sel_hi:[1,0,0]\n"
        "v_pk_fma_f32 v[118:119], v[118:119], s[24:25], v[62:63] op_sel_hi:[1,0,0]\n"
        "v_pk_fma_f32 v[120:121], v[120:121], s[24:25], v[62:63] op_sel_hi:[1,0,0]\n"
        "v_pk_fma_f32 v[106:107], v[106:107], s[24:25], v[62:63] op_sel_hi:[1,0,0]\n"
        "v_pk_fma_f32 v[108:109], v[108:109], s[24:25], v[62:63] op_sel_hi:[1,0,0]\n"
        "v_pk_fma_f32 v[126:127], v[126:127], s[24:25], v[68:69] op_sel_hi:[1,0,0]\n"
        "v_pk_fma_f32 v[128:129], v[128:129], s[24:25], v[68:69] op_sel_hi:[1,0,0]\n"
        "v_pk_fma_f32 v[130:131], v[130:131], s[24:25], v[68:69] op_sel_hi:[1,0,0]\n"
        "v_pk_fma_f32 v[132:133], v[132:133], s[24:25], v[68:69] op_sel_hi:[1,0,0]\n"
        "v_pk_fma_f32 v[122:123], v[122:123], s[24:25], v[68:69] op_sel_hi:[1,0,0]\n"
        "v_pk_fma_f32 v[124:125], v[124:125], s[24:25], v[68:69] op_sel_hi:[1,0,0]\n"
        "v_pk_fma_f32 v[134:135], v[134:135], s[24:25], v[68:69] op_sel_hi:[1,0,0]\n"
        "v_pk_fma_f32 v[136:137], v[136:137], s[24:25], v[68:69] op_sel_hi:[1,0,0]\n"
        "v_pk_fma_f32 v[146:147], v[146:147], s[24:25], v[68:69] op_sel_hi:[1,0,0]\n"
        "v_pk_fma_f32 v[148:149], v[148:149], s[24:25], v[68:69] op_sel_hi:[1,0,0]\n"
        "v_pk_fma_f32 v[150:151], v[150:151], s[24:25], v[68:69] op_sel_hi:[1,0,0]\n"
        "v_pk_fma_f32 v[152:153], v[152:153], s[24:25], v[68:69] op_sel_hi:[1,0,0]\n"
        "v_cvt_pk_bf16_f32 v30, v98, v99\n"
        "v_cvt_pk_bf16_f32 v31, v100, v101\n"
        "v_cvt_pk_bf16_f32 v32, v102, v103\n"
        "v_cvt_pk_bf16_f32 v33, v104, v105\n"
        "v_cvt_pk_bf16_f32 v74, v114, v115\n"
        "v_cvt_pk_bf16_f32 v75, v116, v117\n"
        "v_cvt_pk_bf16_f32 v76, v110, v111\n"
        "v_cvt_pk_bf16_f32 v77, v112, v113\n"
        "v_cvt_pk_bf16_f32 v78, v118, v119\n"
        "v_cvt_pk_bf16_f32 v79, v120, v121\n"
        "v_cvt_pk_bf16_f32 v80, v106, v107\n"
        "v_cvt_pk_bf16_f32 v81, v108, v109\n"
        "v_cvt_pk_bf16_f32 v86, v126, v127\n"
        "v_cvt_pk_bf16_f32 v87, v128, v129\n"
        "v_cvt_pk_bf16_f32 v88, v130, v131\n"
        "v_cvt_pk_bf16_f32 v89, v132, v133\n"
        "v_cvt_pk_bf16_f32 v82, v122, v123\n"
        "v_cvt_pk_bf16_f32 v83, v124, v125\n"
        "v_cvt_pk_bf16_f32 v84, v134, v135\n"
        "v_cvt_pk_bf16_f32 v85, v136, v137\n"
        "v_cvt_pk_bf16_f32 v90, v146, v147\n"
        "v_cvt_pk_bf16_f32 v91, v148, v149\n"
        "v_cvt_pk_bf16_f32 v92, v150, v151\n"
        "v_cvt_pk_bf16_f32 v93, v152, v153\n"
        "v_add_f32 v94, v94, v50\n"
        "ds_write_b128 v42, v[30:33]\n"
        "ds_write_b128 v42, v[74:77] offset:1024\n"
        "ds_write_b128 v42, v[78:81] offset:2048\n"
        "ds_write_b128 v43, v[86:89]\n"
        "ds_write_b128 v43, v[82:85] offset:1024\n"
        "ds_write_b128 v43, v[90:93] offset:2048\n"
        "v_add_f32 v94, v94, v51\n"
        "v_add_f32 v94, v94, v52\n"
        "v_add_f32 v94, v94, v53\n"
        "v_mov_b32 v95, v94\n"
        "s_waitcnt vmcnt(9)\n"
        "s_nop 0\n"
        "v_permlane32_swap_b32 v94, v95\n"
        "v_add_f32 v94, v94, v95\n"
        "v_mov_b32 v95, v94\n"
        "s_nop 1\n"
        "v_permlane16_swap_b32 v94, v95\n"
        "v_add_f32 v94, v94, v95\n"
        "v_mul_f32 v94, 0xbac909aa, v94\n"
        "v_pk_fma_f32 v[138:139], v[138:139], s[24:25], v[94:95] op_sel_hi:[1,0,0]\n"
        "v_pk_fma_f32 v[140:141], v[140:141], s[24:25], v[94:95] op_sel_hi:[1,0,0]\n"
        "v_pk_fma_f32 v[54:55], v[54:55], s[24:25], v[94:95] op_sel_hi:[1,0,0]\n"
        "v_pk_fma_f32 v[56:57], v[56:57], s[24:25], v[94:95] op_sel_hi:[1,0,0]\n"
        "v_pk_fma_f32 v[38:39], v[38:39], s[24:25], v[94:95] op_sel_hi:[1,0,0]\n"
        "v_pk_fma_f32 v[40:41], v[40:41], s[24:25], v[94:95] op_sel_hi:[1,0,0]\n"
        "v_pk_fma_f32 v[46:47], v[46:47], s[24:25], v[94:95] op_sel_hi:[1,0,0]\n"
        "v_pk_fma_f32 v[48:49], v[48:49], s[24:25], v[94:95] op_sel_hi:[1,0,0]\n"
        "v_pk_fma_f32 v[34:35], v[34:35], s[24:25], v[94:95] op_sel_hi:[1,0,0]\n"
        "v_pk_fma_f32 v[36:37], v[36:37], s[24:25], v[94:95] op_sel_hi:[1,0,0]\n"
        "v_pk_fma_f32 v[50:51], v[50:51], s[24:25], v[94:95] op_sel_hi:[1,0,0]\n"
        "v_pk_fma_f32 v[52:53], v[52:53], s[24:25], v[94:95] op_sel_hi:[1,0,0]\n"
        "v_cvt_pk_bf16_f32 v74, v138, v139\n"
        "v_cvt_pk_bf16_f32 v75, v140, v141\n"
        "v_cvt_pk_bf16_f32 v76, v54, v55\n"
        "v_cvt_pk_bf16_f32 v77, v56, v57\n"
        "v_cvt_pk_bf16_f32 v54, v38, v39\n"
        "v_cvt_pk_bf16_f32 v55, v40, v41\n"
        "v_cvt_pk_bf16_f32 v56, v46, v47\n"
        "v_cvt_pk_bf16_f32 v57, v48, v49\n"
        "v_cvt_pk_bf16_f32 v30, v34, v35\n"
        "v_cvt_pk_bf16_f32 v31, v36, v37\n"
        "v_cvt_pk_bf16_f32 v32, v50, v51\n"
        "v_cvt_pk_bf16_f32 v33, v52, v53\n"
        "ds_write_b128 v44, v[74:77]\n"
        "ds_write_b128 v44, v[54:57] offset:1024\n"
        "ds_write_b128 v44, v[30:33] offset:2048\n"
        "s_waitcnt lgkmcnt(0)\n"
        "s_barrier\n"
        "s_branch FE_%=\n"
        "FV_%=:\n"
        "s_bitcmp1_b32 s52, 11\n"
        "s_cbranch_scc1 FG_%=\n"
        "v_mov_b32 v50, 0x3f803f80\n"
        "v_mov_b32 v51, 0x3f803f80\n"
        "v_mov_b32 v52, 0x3f803f80\n"
        "v_mov_b32 v53, 0x3f803f80\n"
        "s_waitcnt vmcnt(15)\n"
        "ds_read_b128 a[0:3], v32\n"
        "ds_read_b128 a[4:7], v32 offset:64\n"
        "ds_read_b128 a[8:11], v32 offset:128\n"
        "s_add_u32 m0, s53, 15360\n"
        "s_nop 0\n"
        "buffer_load_dwordx4 v46, s[16:19], s23 offen lds\n"
        "ds_read_b128 a[12:15], v33\n"
        "ds_read_b128 a[16:19], v33 offset:64\n"
        "ds_read_b128 a[20:23], v33 offset:128\n"
        "buffer_load_dwordx4 v47, s[16:19], s23 offen offset:1024 lds\n"
        "ds_read_b128 a[24:27], v32 offset:6144\n"
        "ds_read_b128 a[28:31], v32 offset:6208\n"
        "ds_read_b128 a[32:35], v32 offset:6272\n"
        "buffer_load_dwordx4 v48, s[16:19], s23 offen offset:2048 lds\n"
        "ds_read_b128 a[36:39], v33 offset:6144\n"
        "ds_read_b128 a[40:43], v33 offset:6208\n"
        "ds_read_b128 a[44:47], v33 offset:6272\n"
        "ds_read_b128 a[48:51], v32 offset:12288\n"
        "ds_read_b128 a[52:55], v32 offset:12352\n"
        "ds_read_b128 a[56:59], v32 offset:12416\n"
        "ds_read_b128 a[60:63], v33 offset:12288\n"
        "ds_read_b128 a[64:67], v33 offset:12352\n"
        "ds_read_b128 a[68:71], v33 offset:12416\n"
        "ds_read_b64_tr_b16 a[72:73], v34 offset:0\n"
        "ds_read_b64_tr_b16 a[74:75], v35 offset:0\n"
        "ds_read_b64_tr_b16 a[76:77], v34 offset:6144\n"
        "ds_read_b64_tr_b16 a[78:79], v35 offset:6144\n"
        "ds_read_b64_tr_b16 a[80:81], v34 offset:12288\n"
        "ds_read_b64_tr_b16 a[82:83], v35 offset:12288\n"
        "s_waitcnt lgkmcnt(15)\n"
        "v_mfma_f32_16x16x32_bf16 v[46:49], a[0:3], v[50:53], 0\n"
        "s_waitcnt vmcnt(12)\n"
        "s_waitcnt vmcnt(6)\n"
        "v_mfma_f32_16x16x32_bf16 v[46:49], a[4:7], v[50:53], v[46:49]\n"
        "s_waitcnt lgkmcnt(15)\n"
        "v_mfma_f32_16x16x32_bf16 v[46:49], a[8:11], v[50:53], v[46:49]\n"
        "s_waitcnt vmcnt(0)\n"
        "s_nop 0\n"
        "v_mfma_f32_16x16x32_bf16 v[54:57], a[20:23], v[50:53], 0\n"
        "s_waitcnt lgkmcnt(15)\n"
        "s_nop 1\n"
        "v_mfma_f32_16x16x32_bf16 v[54:57], a[16:19], v[50:53], v[54:57]\n"
        "s_waitcnt lgkmcnt(12)\n"
        "v_cvt_pk_bf16_f32 v62, v46, v47\n"
        "v_cvt_pk_bf16_f32 v63, v48, v49\n"
        "v_mfma_f32_16x16x32_bf16 v[54:57], a[12:15], v[50:53], v[54:57]\n"
        "s_waitcnt lgkmcnt(9)\n"
        "v_mfma_f32_16x16x32_bf16 v[46:49], a[24:27], v[50:53], 0\n"
        "s_waitcnt lgkmcnt(6)\n"
        "ds_read_b128 a[144:147], v30\n"
        "s_nop 0\n"
        "v_mfma_f32_16x16x32_bf16 v[46:49], a[28:31], v[50:53], v[46:49]\n"
        "s_waitcnt lgkmcnt(1)\n"
        "ds_read_b128 a[148:151], v30 offset:64\n"
        "v_cvt_pk_bf16_f32 v64, v54, v55\n"
        "v_cvt_pk_bf16_f32 v65, v56, v57\n"
        "v_mfma_f32_16x16x32_bf16 v[46:49], a[32:35], v[50:53], v[46:49]\n"
        "s_nop 0\n"
        "ds_read_b128 a[152:155], v30 offset:128\n"
        "v_mfma_f32_16x16x32_bf16 v[54:57], a[44:47], v[50:53], 0\n"
        "s_nop 0\n"
        "ds_read_b128 a[156:159], v31\n"
        "v_mfma_f32_16x16x32_bf16 v[54:57], a[40:43], v[50:53], v[54:57]\n"
        "s_nop 0\n"
        "ds_read_b128 a[160:163], v31 offset:64\n"
        "v_cvt_pk_bf16_f32 v66, v46, v47\n"
        "v_cvt_pk_bf16_f32 v67, v48, v49\n"
        "v_mfma_f32_16x16x32_bf16 v[54:57], a[36:39], v[50:53], v[54:57]\n"
        "s_nop 0\n"
        "ds_read_b128 a[164:167], v31 offset:128\n"
        "v_mfma_f32_16x16x32_bf16 v[46:49], a[48:51], v[50:53], 0\n"
        "s_nop 0\n"
        "ds_read_b128 a[168:171], v30 offset:6144\n"
        "v_mfma_f32_16x16x32_bf16 v[46:49], a[52:55], v[50:53], v[46:49]\n"
        "s_nop 0\n"
        "ds_read_b128 a[172:175], v30 offset:6208\n"
        "v_cvt_pk_bf16_f32 v68, v54, v55\n"
        "v_cvt_pk_bf16_f32 v69, v56, v57\n"
        "v_mfma_f32_16x16x32_bf16 v[46:49], a[56:59], v[50:53], v[46:49]\n"
        "s_nop 0\n"
        "ds_read_b128 a[176:179], v30 offset:6272\n"
        "v_mfma_f32_16x16x32_bf16 v[54:57], a[68:71], v[50:53], 0\n"
        "s_nop 0\n"
        "ds_read_b128 a[180:183], v31 offset:6144\n"
        "v_mfma_f32_16x16x32_bf16 v[54:57], a[64:67], v[50:53], v[54:57]\n"
        "s_nop 0\n"
        "ds_read_b128 a[184:187], v31 offset:6208\n"
        "v_cvt_pk_bf16_f32 v70, v46, v47\n"
        "v_cvt_pk_bf16_f32 v71, v48, v49\n"
        "v_mfma_f32_16x16x32_bf16 v[54:57], a[60:63], v[50:53], v[54:57]\n"
        "ds_read_b128 a[188:191], v31 offset:6272\n"
        "s_waitcnt lgkmcnt(9)\n"
        "v_mfma_f32_16x16x32_bf16 v[46:49], a[144:147], a[72:75], 0\n"
        "s_nop 0\n"
        "ds_read_b128 a[192:195], v30 offset:12288\n"
        "v_mfma_f32_16x16x32_bf16 v[46:49], a[148:151], a[76:79], v[46:49]\n"
        "s_nop 0\n"
        "ds_read_b128 a[196:199], v30 offset:12352\n"
        "v_cvt_pk_bf16_f32 v72, v54, v55\n"
        "v_cvt_pk_bf16_f32 v73, v56, v57\n"
        "v_mfma_f32_16x16x32_bf16 v[46:49], a[152:155], a[80:83], v[46:49]\n"
        "buffer_load_dwordx4 v[196:199], v36, s[36:39], 0 offen\n"
        "v_mfma_f32_16x16x32_bf16 v[50:53], a[152:155], v[70:73], 0\n"
        "s_waitcnt lgkmcnt(8)\n"
        "ds_read_b128 a[200:203], v30 offset:12416\n"
        "ds_read_b128 a[204:207], v31 offset:12288\n"
        "v_mfma_f32_16x16x32_bf16 v[50:53], a[148:151], v[66:69], v[50:53]\n"
        "s_nop 0\n"
        "ds_read_b128 a[208:211], v31 offset:12352\n"
        "v_add_f32 v254, v46, v47\n"
        "v_add_f32 v254, v254, v48\n"
        "v_mfma_f32_16x16x32_bf16 v[50:53], a[144:147], v[62:65], v[50:53]\n"
        "ds_read_b128 a[212:215], v31 offset:12416\n"
        "v_add_f32 v254, v254, v49\n"
        "v_mfma_f32_16x16x32_bf16 v[102:105], a[156:159], a[72:75], 0\n"
        "s_waitcnt lgkmcnt(9)\n"
        "ds_read_b64_tr_b16 a[84:85], v34 offset:8\n"
        "v_mfma_f32_16x16x32_bf16 v[102:105], a[160:163], a[76:79], v[102:105]\n"
        "s_nop 0\n"
        "ds_read_b64_tr_b16 a[86:87], v35 offset:8\n"
        "v_add_f32 v255, v50, v51\n"
        "v_add_f32 v255, v255, v52\n"
        "v_mfma_f32_16x16x32_bf16 v[102:105], a[164:167], a[80:83], v[102:105]\n"
        "ds_read_b64_tr_b16 a[88:89], v34 offset:6152\n"
        "v_add_f32 v255, v255, v53\n"
        "v_mfma_f32_16x16x32_bf16 v[30:33], a[164:167], v[70:73], 0\n"
        "s_waitcnt lgkmcnt(9)\n"
        "ds_read_b64_tr_b16 a[90:91], v35 offset:6152\n"
        "v_mfma_f32_16x16x32_bf16 v[30:33], a[160:163], v[66:69], v[30:33]\n"
        "s_nop 0\n"
        "ds_read_b64_tr_b16 a[92:93], v34 offset:12296\n"
        "v_add_f32 v254, v254, v102\n"
        "v_add_f32 v254, v254, v103\n"
        "v_mfma_f32_16x16x32_bf16 v[30:33], a[156:159], v[62:65], v[30:33]\n"
        "buffer_load_dwordx4 v[202:205], v36, s[36:39], 0 offen offset:16\n"
        "v_add_f32 v254, v254, v104\n"
        "v_mfma_f32_16x16x32_bf16 v[108:111], a[168:171], a[72:75], 0\n"
        "s_mov_b32 s78, 0xf0000f\n"
        "ds_read_b64_tr_b16 a[94:95], v35 offset:12296\n"
        "v_add_f32 v254, v254, v105\n"
        "v_mfma_f32_16x16x32_bf16 v[108:111], a[172:175], a[76:79], v[108:111]\n"
        "s_waitcnt lgkmcnt(9)\n"
        "ds_read_b64_tr_b16 a[96:97], v34 offset:64\n"
        "ds_read_b64_tr_b16 a[98:99], v35 offset:64\n"
        "v_add_f32 v255, v255, v30\n"
        "v_mfma_f32_16x16x32_bf16 v[108:111], a[176:179], a[80:83], v[108:111]\n"
        "s_mov_b32 s79, 0xf0000f00\n"
        "ds_read_b64_tr_b16 a[100:101], v34 offset:6208\n"
        "v_add_f32 v255, v255, v31\n"
        "v_add_f32 v255, v255, v32\n"
        "v_mfma_f32_16x16x32_bf16 v[54:57], a[176:179], v[70:73], 0\n"
        "s_mov_b32 s76, 0x11111111\n"
        "ds_read_b64_tr_b16 a[102:103], v35 offset:6208\n"
        "v_add_f32 v255, v255, v33\n"
        "v_add_f32 v254, v254, v108\n"
        "v_mfma_f32_16x16x32_bf16 v[54:57], a[172:175], v[66:69], v[54:57]\n"
        "s_waitcnt lgkmcnt(10)\n"
        "ds_read_b64_tr_b16 a[104:105], v34 offset:12352\n"
        "v_add_f32 v254, v254, v109\n"
        "v_add_f32 v254, v254, v110\n"
        "v_mfma_f32_16x16x32_bf16 v[54:57], a[168:171], v[62:65], v[54:57]\n"
        "s_mov_b32 s77, 0x11111111\n"
        "ds_read_b64_tr_b16 a[106:107], v35 offset:12352\n"
        "v_add_f32 v254, v254, v111\n"
        "v_mfma_f32_16x16x32_bf16 v[112:115], a[180:183], a[72:75], 0\n"
        "s_mov_b32 s56, 0x22222222\n"
        "ds_read_b64_tr_b16 a[108:109], v34 offset:72\n"
        "v_mfma_f32_16x16x32_bf16 v[112:115], a[184:187], a[76:79], v[112:115]\n"
        "s_mov_b32 s57, 0x22222222\n"
        "ds_read_b64_tr_b16 a[110:111], v35 offset:72\n"
        "v_add_f32 v255, v255, v54\n"
        "v_add_f32 v255, v255, v55\n"
        "v_mfma_f32_16x16x32_bf16 v[112:115], a[188:191], a[80:83], v[112:115]\n"
        "buffer_load_dwordx4 v[208:211], v36, s[36:39], 0 offen offset:32\n"
        "v_add_f32 v255, v255, v56\n"
        "v_mfma_f32_16x16x32_bf16 v[78:81], a[188:191], v[70:73], 0\n"
        "s_mov_b32 s58, 0x44444444\n"
        "ds_read_b64_tr_b16 a[112:113], v34 offset:6216\n"
        "v_add_f32 v255, v255, v57\n"
        "v_mfma_f32_16x16x32_bf16 v[78:81], a[184:187], v[66:69], v[78:81]\n"
        "s_waitcnt lgkmcnt(9)\n"
        "ds_read_b64_tr_b16 a[114:115], v35 offset:6216\n"
        "ds_read_b64_tr_b16 a[116:117], v34 offset:12360\n"
        "v_add_f32 v254, v254, v112\n"
        "v_mfma_f32_16x16x32_bf16 v[78:81], a[180:183], v[62:65], v[78:81]\n"
        "s_mov_b32 s59, 0x44444444\n"
        "ds_read_b64_tr_b16 a[118:119], v35 offset:12360\n"
        "v_add_f32 v254, v254, v113\n"
        "v_add_f32 v254, v254, v114\n"
        "v_mfma_f32_16x16x32_bf16 v[116:119], a[192:195], a[72:75], 0\n"
        "s_mov_b32 s60, 0x88888888\n"
        "ds_read_b64_tr_b16 a[120:121], v34 offset:128\n"
        "v_add_f32 v254, v254, v115\n"
        "v_add_f32 v255, v255, v78\n"
        "v_mfma_f32_16x16x32_bf16 v[116:119], a[196:199], a[76:79], v[116:119]\n"
        "s_mov_b32 s61, 0x88888888\n"
        "ds_read_b64_tr_b16 a[122:123], v35 offset:128\n"
        "v_add_f32 v255, v255, v79\n"
        "v_add_f32 v255, v255, v80\n"
        "v_mfma_f32_16x16x32_bf16 v[116:119], a[200:203], a[80:83], v[116:119]\n"
        "s_mov_b32 s62, 0xffff\n"
        "ds_read_b64_tr_b16 a[124:125], v34 offset:6272\n"
        "v_add_f32 v255, v255, v81\n"
        "v_mfma_f32_16x16x32_bf16 v[82:85], a[200:203], v[70:73], 0\n"
        "s_mov_b32 s63, 0x0\n"
        "ds_read_b64_tr_b16 a[126:127], v35 offset:6272\n"
        "v_mfma_f32_16x16x32_bf16 v[82:85], a[196:199], v[66:69], v[82:85]\n"
        "s_mov_b32 s64, 0xffff0000\n"
        "ds_read_b64_tr_b16 a[128:129], v34 offset:12416\n"
        "v_add_f32 v254, v254, v116\n"
        "v_add_f32 v254, v254, v117\n"
        "v_mfma_f32_16x16x32_bf16 v[82:85], a[192:195], v[62:65], v[82:85]\n"
        "buffer_load_dwordx4 v[58:61], v37, s[36:39], 0 offen\n"
        "v_add_f32 v254, v254, v118\n"
        "v_mfma_f32_16x16x32_bf16 v[126:129], a[204:207], a[72:75], 0\n"
        "s_mov_b32 s65, 0x0\n"
        "ds_read_b64_tr_b16 a[130:131], v35 offset:12416\n"
        "v_add_f32 v254, v254, v119\n"
        "v_mfma_f32_16x16x32_bf16 v[126:129], a[208:211], a[76:79], v[126:129]\n"
        "s_mov_b32 s66, 0x0\n"
        "ds_read_b64_tr_b16 a[132:133], v34 offset:136\n"
        "ds_read_b64_tr_b16 a[134:135], v35 offset:136\n"
        "v_add_f32 v255, v255, v82\n"
        "v_mfma_f32_16x16x32_bf16 v[126:129], a[212:215], a[80:83], v[126:129]\n"
        "s_waitcnt lgkmcnt(14)\n"
        "ds_read_b64_tr_b16 a[136:137], v34 offset:6280\n"
        "v_add_f32 v255, v255, v83\n"
        "v_add_f32 v255, v255, v84\n"
        "v_mfma_f32_16x16x32_bf16 v[122:125], a[212:215], v[70:73], 0\n"
        "s_mov_b32 s67, 0xffff\n"
        "ds_read_b64_tr_b16 a[138:139], v35 offset:6280\n"
        "v_add_f32 v255, v255, v85\n"
        "v_add_f32 v254, v254, v126\n"
        "v_mfma_f32_16x16x32_bf16 v[122:125], a[208:211], v[66:69], v[122:125]\n"
        "s_mov_b32 s68, 0x0\n"
        "ds_read_b64_tr_b16 a[140:141], v34 offset:12424\n"
        "v_add_f32 v254, v254, v127\n"
        "v_add_f32 v254, v254, v128\n"
        "v_mfma_f32_16x16x32_bf16 v[122:125], a[204:207], v[62:65], v[122:125]\n"
        "s_mov_b32 s69, 0xffff0000\n"
        "ds_read_b64_tr_b16 a[142:143], v35 offset:12424\n"
        "v_add_f32 v254, v254, v129\n"
        "v_mov_b32 v253, v254\n"
        "v_mfma_f32_16x16x32_bf16 v[74:77], a[144:147], a[84:87], 0\n"
        "s_mov_b32 s70, 0x0\n"
        "v_permlane32_swap_b32 v254, v253\n"
        "v_add_f32 v254, v254, v253\n"
        "v_mfma_f32_16x16x32_bf16 v[74:77], a[148:151], a[88:91], v[74:77]\n"
        "s_mov_b32 s71, 0xffffffff\n"
        "v_mov_b32 v253, v254\n"
        "v_add_f32 v255, v255, v122\n"
        "v_add_f32 v255, v255, v123\n"
        "v_mfma_f32_16x16x32_bf16 v[74:77], a[152:155], a[92:95], v[74:77]\n"
        "buffer_load_dwordx4 v[64:67], v37, s[36:39], 0 offen offset:16\n"
        "v_add_f32 v255, v255, v124\n"
        "v_mfma_f32_16x16x32_bf16 v[90:93], a[164:167], a[92:95], 0\n"
        "s_mov_b32 s72, 0x0\n"
        "v_permlane16_swap_b32 v254, v253\n"
        "v_add_f32 v254, v254, v253\n"
        "v_mfma_f32_16x16x32_bf16 v[90:93], a[160:163], a[88:91], v[90:93]\n"
        "s_mov_b32 s73, 0x20000\n"
        "v_mul_f32 v254, 0xbc2aaaab, v254\n"
        "v_add_f32 v255, v255, v125\n"
        "v_add_f32 v46, v46, v254\n"
        "v_mfma_f32_16x16x32_bf16 v[90:93], a[156:159], a[84:87], v[90:93]\n"
        "s_mov_b64 vcc, s[78:79]\n"
        "v_add_f32 v47, v47, v254\n"
        "v_add_f32 v48, v48, v254\n"
        "v_add_f32 v49, v49, v254\n"
        "v_mfma_f32_16x16x32_bf16 v[94:97], a[168:171], a[84:87], 0\n"
        "s_mov_b32 s80, 0x3c2aaaab\n"
        "v_add_f32 v102, v102, v254\n"
        "v_add_f32 v103, v103, v254\n"
        "v_add_f32 v104, v104, v254\n"
        "v_mfma_f32_16x16x32_bf16 v[94:97], a[172:175], a[88:91], v[94:97]\n"
        "s_mov_b32 s81, 0x3727c5ac\n"
        "v_add_f32 v105, v105, v254\n"
        "v_add_f32 v108, v108, v254\n"
        "v_add_f32 v109, v109, v254\n"
        "v_mfma_f32_16x16x32_bf16 v[94:97], a[176:179], a[92:95], v[94:97]\n"
        "s_waitcnt lgkmcnt(0)\n"
        "v_add_f32 v110, v110, v254\n"
        "v_add_f32 v111, v111, v254\n"
        "v_add_f32 v112, v112, v254\n"
        "v_mfma_f32_16x16x32_bf16 v[86:89], a[188:191], a[92:95], 0\n"
        "s_barrier\n"
        "v_add_f32 v113, v113, v254\n"
        "v_add_f32 v114, v114, v254\n"
        "v_add_f32 v115, v115, v254\n"
        "v_mfma_f32_16x16x32_bf16 v[86:89], a[184:187], a[88:91], v[86:89]\n"
        "s_mov_b32 s82, 0xc000c000\n"
        "v_add_f32 v116, v116, v254\n"
        "v_add_f32 v117, v117, v254\n"
        "v_add_f32 v118, v118, v254\n"
        "v_mfma_f32_16x16x32_bf16 v[86:89], a[180:183], a[84:87], v[86:89]\n"
        "buffer_load_dwordx4 v[70:73], v37, s[36:39], 0 offen offset:32\n"
        "v_add_f32 v119, v119, v254\n"
        "v_mfma_f32_16x16x32_bf16 v[98:101], a[192:195], a[84:87], 0\n"
        "s_mov_b32 s84, 0x80000000\n"
        "v_add_f32 v126, v126, v254\n"
        "v_add_f32 v127, v127, v254\n"
        "v_add_f32 v128, v128, v254\n"
        "v_mfma_f32_16x16x32_bf16 v[98:101], a[196:199], a[88:91], v[98:101]\n"
        "s_mov_b32 s85, 19\n"
        "v_add_f32 v129, v129, v254\n"
        "v_cvt_pk_bf16_f32 v34, v46, v47\n"
        "v_cvt_pk_bf16_f32 v35, v48, v49\n"
        "v_mfma_f32_16x16x32_bf16 v[98:101], a[200:203], a[92:95], v[98:101]\n"
        "s_mov_b32 s86, 0x80000000\n"
        "v_cvt_pk_bf16_f32 v36, v102, v103\n"
        "v_cvt_pk_bf16_f32 v37, v104, v105\n"
        "v_cvt_pk_bf16_f32 v46, v108, v109\n"
        "v_mfma_f32_16x16x32_bf16 v[106:109], a[212:215], a[92:95], 0\n"
        "s_mov_b32 s89, 0x80000000\n"
        "ds_write_b128 v39, v[34:37]\n"
        "v_cvt_pk_bf16_f32 v47, v110, v111\n"
        "v_mfma_f32_16x16x32_bf16 v[106:109], a[208:211], a[88:91], v[106:109]\n"
        "s_mov_b32 s29, 1\n"
        "v_cvt_pk_bf16_f32 v48, v112, v113\n"
        "v_cvt_pk_bf16_f32 v49, v114, v115\n"
        "v_cvt_pk_bf16_f32 v34, v116, v117\n"
        "v_mfma_f32_16x16x32_bf16 v[106:109], a[204:207], a[84:87], v[106:109]\n"
        "s_mov_b32 s31, 0x400000\n"
        "ds_write_b128 v39, v[46:49] offset:1024\n"
        "v_cvt_pk_bf16_f32 v35, v118, v119\n"
        "v_mfma_f32_16x16x32_bf16 v[46:49], a[144:147], a[96:99], 0\n"
        "s_mov_b32 s87, s49\n"
        "v_cvt_pk_bf16_f32 v36, v126, v127\n"
        "v_cvt_pk_bf16_f32 v37, v128, v129\n"
        "v_mov_b32 v254, v255\n"
        "v_mfma_f32_16x16x32_bf16 v[46:49], a[148:151], a[100:103], v[46:49]\n"
        "s_add_u32 m0, s52, 0\n"
        "ds_write_b128 v39, v[34:37] offset:2048\n"
        "v_add_f32 v253, v74, v75\n"
        "v_mfma_f32_16x16x32_bf16 v[46:49], a[152:155], a[104:107], v[46:49]\n"
        "buffer_load_dwordx4 v0, s[36:39], s49 offen lds\n"
        "v_add_f32 v253, v253, v76\n"
        "v_mfma_f32_16x16x32_bf16 v[102:105], a[164:167], a[104:107], 0\n"
        "s_mov_b32 s88, s50\n"
        "v_permlane32_swap_b32 v255, v254\n"
        "v_add_f32 v255, v255, v254\n"
        "v_mfma_f32_16x16x32_bf16 v[102:105], a[160:163], a[100:103], v[102:105]\n"
        "s_mov_b64 s[44:45], s[40:41]\n"
        "v_mov_b32 v254, v255\n"
        "v_add_f32 v253, v253, v77\n"
        "v_add_f32 v253, v253, v90\n"
        "v_mfma_f32_16x16x32_bf16 v[102:105], a[156:159], a[96:99], v[102:105]\n"
        "s_mov_b32 s46, 0\n"
        "v_permlane16_swap_b32 v255, v254\n"
        "v_add_f32 v255, v255, v254\n"
        "v_mfma_f32_16x16x32_bf16 v[114:117], a[168:171], a[96:99], 0\n"
        "s_mov_b32 s47, s43\n"
        "v_mul_f32 v255, 0xbc2aaaab, v255\n"
        "v_add_f32 v50, v50, v255\n"
        "v_add_f32 v51, v51, v255\n"
        "v_mfma_f32_16x16x32_bf16 v[114:117], a[172:175], a[100:103], v[114:117]\n"
        "s_waitcnt lgkmcnt(3)\n"
        "v_add_f32 v52, v52, v255\n"
        "v_add_f32 v53, v53, v255\n"
        "v_add_f32 v30, v30, v255\n"
        "v_mfma_f32_16x16x32_bf16 v[114:117], a[176:179], a[104:107], v[114:117]\n"
        "buffer_load_dwordx4 v1, s[36:39], s49 offen offset:1024 lds\n"
        "v_add_f32 v31, v31, v255\n"
        "v_mfma_f32_16x16x32_bf16 v[34:37], a[188:191], a[104:107], 0\n"
        "s_waitcnt lgkmcnt(3)\n"
        "v_add_f32 v32, v32, v255\n"
        "v_add_f32 v33, v33, v255\n"
        "v_add_f32 v54, v54, v255\n"
        "v_mfma_f32_16x16x32_bf16 v[34:37], a[184:187], a[100:103], v[34:37]\n"
        "s_waitcnt lgkmcnt(3)\n"
        "v_add_f32 v55, v55, v255\n"
        "v_add_f32 v56, v56, v255\n"
        "v_add_f32 v57, v57, v255\n"
        "v_mfma_f32_16x16x32_bf16 v[34:37], a[180:183], a[96:99], v[34:37]\n"
        "v_add_f32 v78, v78, v255\n"
        "v_add_f32 v79, v79, v255\n"
        "v_add_f32 v80, v80, v255\n"
        "v_mfma_f32_16x16x32_bf16 v[110:113], a[192:195], a[96:99], 0\n"
        "v_add_f32 v81, v81, v255\n"
        "v_add_f32 v82, v82, v255\n"
        "v_add_f32 v83, v83, v255\n"
        "v_mfma_f32_16x16x32_bf16 v[110:113], a[196:199], a[100:103], v[110:113]\n"
        "v_add_f32 v84, v84, v255\n"
        "v_add_f32 v85, v85, v255\n"
        "v_add_f32 v122, v122, v255\n"
        "v_mfma_f32_16x16x32_bf16 v[110:113], a[200:203], a[104:107], v[110:113]\n"
        "buffer_load_dwordx4 v2, s[36:39], s49 offen offset:2048 lds\n"
        "v_add_f32 v123, v123, v255\n"
        "v_mfma_f32_16x16x32_bf16 v[118:121], a[212:215], a[104:107], 0\n"
        "v_add_f32 v124, v124, v255\n"
        "v_add_f32 v125, v125, v255\n"
        "v_cvt_pk_bf16_f32 v126, v50, v51\n"
        "v_mfma_f32_16x16x32_bf16 v[118:121], a[208:211], a[100:103], v[118:121]\n"
        "v_cvt_pk_bf16_f32 v127, v52, v53\n"
        "v_cvt_pk_bf16_f32 v128, v30, v31\n"
        "v_cvt_pk_bf16_f32 v129, v32, v33\n"
        "v_mfma_f32_16x16x32_bf16 v[118:121], a[204:207], a[96:99], v[118:121]\n"
        "ds_write_b128 v38, v[126:129]\n"
        "v_cvt_pk_bf16_f32 v30, v54, v55\n"
        "v_mfma_f32_16x16x32_bf16 v[50:53], a[144:147], a[108:111], 0\n"
        "v_cvt_pk_bf16_f32 v31, v56, v57\n"
        "v_cvt_pk_bf16_f32 v32, v78, v79\n"
        "v_cvt_pk_bf16_f32 v33, v80, v81\n"
        "v_mfma_f32_16x16x32_bf16 v[50:53], a[148:151], a[112:115], v[50:53]\n"
        "ds_write_b128 v38, v[30:33] offset:64\n"
        "v_cvt_pk_bf16_f32 v54, v82, v83\n"
        "v_mfma_f32_16x16x32_bf16 v[50:53], a[152:155], a[116:119], v[50:53]\n"
        "buffer_load_dwordx4 v3, s[36:39], s49 offen offset:3072 lds\n"
        "v_cvt_pk_bf16_f32 v55, v84, v85\n"
        "v_mfma_f32_16x16x32_bf16 v[30:33], a[164:167], a[116:119], 0\n"
        "v_cvt_pk_bf16_f32 v56, v122, v123\n"
        "v_cvt_pk_bf16_f32 v57, v124, v125\n"
        "v_add_f32 v253, v253, v91\n"
        "v_mfma_f32_16x16x32_bf16 v[30:33], a[160:163], a[112:115], v[30:33]\n"
        "ds_write_b128 v38, v[54:57] offset:128\n"
        "v_add_f32 v253, v253, v92\n"
        "v_mfma_f32_16x16x32_bf16 v[30:33], a[156:159], a[108:111], v[30:33]\n"
        "v_add_f32 v253, v253, v93\n"
        "v_add_f32 v253, v253, v94\n"
        "v_add_f32 v253, v253, v95\n"
        "v_mfma_f32_16x16x32_bf16 v[82:85], a[168:171], a[108:111], 0\n"
        "v_add_f32 v253, v253, v96\n"
        "v_add_f32 v253, v253, v97\n"
        "v_add_f32 v253, v253, v86\n"
        "v_mfma_f32_16x16x32_bf16 v[82:85], a[172:175], a[112:115], v[82:85]\n"
        "s_add_u32 m0, s52, 4096\n"
        "v_add_f32 v253, v253, v87\n"
        "v_add_f32 v253, v253, v88\n"
        "v_add_f32 v253, v253, v89\n"
        "v_mfma_f32_16x16x32_bf16 v[82:85], a[176:179], a[116:119], v[82:85]\n"
        "buffer_load_dwordx4 v4, s[36:39], s49 offen lds\n"
        "v_add_f32 v253, v253, v98\n"
        "v_mfma_f32_16x16x32_bf16 v[78:81], a[188:191], a[116:119], 0\n"
        "v_add_f32 v253, v253, v99\n"
        "v_add_f32 v253, v253, v100\n"
        "v_add_f32 v253, v253, v101\n"
        "v_mfma_f32_16x16x32_bf16 v[78:81], a[184:187], a[112:115], v[78:81]\n"
        "v_add_f32 v253, v253, v106\n"
        "v_add_f32 v253, v253, v107\n"
        "v_add_f32 v253, v253, v108\n"
        "v_mfma_f32_16x16x32_bf16 v[78:81], a[180:183], a[108:111], v[78:81]\n"
        "v_add_f32 v253, v253, v109\n"
        "v_mov_b32 v254, v253\n"
        "v_add_f32 v255, v46, v47\n"
        "v_mfma_f32_16x16x32_bf16 v[122:125], a[192:195], a[108:111], 0\n"
        "v_permlane32_swap_b32 v253, v254\n"
        "v_add_f32 v253, v253, v254\n"
        "v_mfma_f32_16x16x32_bf16 v[122:125], a[196:199], a[112:115], v[122:125]\n"
        "v_mov_b32 v254, v253\n"
        "v_add_f32 v255, v255, v48\n"
        "v_add_f32 v255, v255, v49\n"
        "v_mfma_f32_16x16x32_bf16 v[122:125], a[200:203], a[116:119], v[122:125]\n"
        "buffer_load_dwordx4 v5, s[36:39], s49 offen offset:1024 lds\n"
        "v_add_f32 v255, v255, v102\n"
        "v_mfma_f32_16x16x32_bf16 v[54:57], a[212:215], a[116:119], 0\n"
        "v_permlane16_swap_b32 v253, v254\n"
        "v_add_f32 v253, v253, v254\n"
        "v_mfma_f32_16x16x32_bf16 v[54:57], a[208:211], a[112:115], v[54:57]\n"
        "v_mul_f32 v253, 0xbc2aaaab, v253\n"
        "v_add_f32 v74, v74, v253\n"
        "v_add_f32 v75, v75, v253\n"
        "v_mfma_f32_16x16x32_bf16 v[54:57], a[204:207], a[108:111], v[54:57]\n"
        "v_add_f32 v76, v76, v253\n"
        "v_add_f32 v77, v77, v253\n"
        "v_add_f32 v90, v90, v253\n"
        "v_mfma_f32_16x16x32_bf16 v[130:133], a[144:147], a[120:123], 0\n"
        "v_add_f32 v91, v91, v253\n"
        "v_add_f32 v92, v92, v253\n"
        "v_add_f32 v93, v93, v253\n"
        "v_mfma_f32_16x16x32_bf16 v[130:133], a[148:151], a[124:127], v[130:133]\n"
        "v_add_f32 v94, v94, v253\n"
        "v_add_f32 v95, v95, v253\n"
        "v_add_f32 v96, v96, v253\n"
        "v_mfma_f32_16x16x32_bf16 v[130:133], a[152:155], a[128:131], v[130:133]\n"
        "buffer_load_dwordx4 v6, s[36:39], s49 offen offset:2048 lds\n"
        "v_add_f32 v97, v97, v253\n"
        "v_mfma_f32_16x16x32_bf16 v[134:137], a[164:167], a[128:131], 0\n"
        "v_add_f32 v86, v86, v253\n"
        "v_add_f32 v87, v87, v253\n"
        "v_add_f32 v88, v88, v253\n"
        "v_mfma_f32_16x16x32_bf16 v[134:137], a[160:163], a[124:127], v[134:137]\n"
        "v_add_f32 v89, v89, v253\n"
        "v_add_f32 v98, v98, v253\n"
        "v_add_f32 v99, v99, v253\n"
        "v_mfma_f32_16x16x32_bf16 v[134:137], a[156:159], a[120:123], v[134:137]\n"
        "v_add_f32 v100, v100, v253\n"
        "v_add_f32 v101, v101, v253\n"
        "v_add_f32 v106, v106, v253\n"
        "v_mfma_f32_16x16x32_bf16 v[126:129], a[168:171], a[120:123], 0\n"
        "v_add_f32 v107, v107, v253\n"
        "v_add_f32 v108, v108, v253\n"
        "v_add_f32 v109, v109, v253\n"
        "v_mfma_f32_16x16x32_bf16 v[126:129], a[172:175], a[124:127], v[126:129]\n"
        "v_cvt_pk_bf16_f32 v138, v74, v75\n"
        "v_cvt_pk_bf16_f32 v139, v76, v77\n"
        "v_cvt_pk_bf16_f32 v140, v90, v91\n"
        "v_mfma_f32_16x16x32_bf16 v[126:129], a[176:179], a[128:131], v[126:129]\n"
        "buffer_load_dwordx4 v7, s[36:39], s49 offen offset:3072 lds\n"
        "v_cvt_pk_bf16_f32 v141, v92, v93\n"
        "v_mfma_f32_16x16x32_bf16 v[74:77], a[188:191], a[128:131], 0\n"
        "ds_write_b128 v40, v[138:141]\n"
        "v_cvt_pk_bf16_f32 v90, v94, v95\n"
        "v_mfma_f32_16x16x32_bf16 v[74:77], a[184:187], a[124:127], v[74:77]\n"
        "v_cvt_pk_bf16_f32 v91, v96, v97\n"
        "v_cvt_pk_bf16_f32 v92, v86, v87\n"
        "v_cvt_pk_bf16_f32 v93, v88, v89\n"
        "v_mfma_f32_16x16x32_bf16 v[74:77], a[180:183], a[120:123], v[74:77]\n"
        "ds_write_b128 v40, v[90:93] offset:1024\n"
        "v_cvt_pk_bf16_f32 v90, v98, v99\n"
        "v_mfma_f32_16x16x32_bf16 v[86:89], a[192:195], a[120:123], 0\n"
        "v_cvt_pk_bf16_f32 v91, v100, v101\n"
        "v_cvt_pk_bf16_f32 v92, v106, v107\n"
        "v_cvt_pk_bf16_f32 v93, v108, v109\n"
        "v_mfma_f32_16x16x32_bf16 v[86:89], a[196:199], a[124:127], v[86:89]\n"
        "s_add_u32 m0, s52, 8192\n"
        "ds_write_b128 v40, v[90:93] offset:2048\n"
        "v_add_f32 v255, v255, v103\n"
        "v_mfma_f32_16x16x32_bf16 v[86:89], a[200:203], a[128:131], v[86:89]\n"
        "buffer_load_dwordx4 v8, s[36:39], s49 offen lds\n"
        "v_add_f32 v255, v255, v104\n"
        "v_mfma_f32_16x16x32_bf16 v[94:97], a[212:215], a[128:131], 0\n"
        "v_add_f32 v255, v255, v105\n"
        "v_add_f32 v255, v255, v114\n"
        "v_add_f32 v255, v255, v115\n"
        "v_mfma_f32_16x16x32_bf16 v[94:97], a[208:211], a[124:127], v[94:97]\n"
        "v_add_f32 v255, v255, v116\n"
        "v_add_f32 v255, v255, v117\n"
        "v_add_f32 v255, v255, v34\n"
        "v_mfma_f32_16x16x32_bf16 v[94:97], a[204:207], a[120:123], v[94:97]\n"
        "v_add_f32 v255, v255, v35\n"
        "v_add_f32 v255, v255, v36\n"
        "v_add_f32 v255, v255, v37\n"
        "v_mfma_f32_16x16x32_bf16 v[90:93], a[144:147], a[132:135], 0\n"
        "v_add_f32 v255, v255, v110\n"
        "v_add_f32 v255, v255, v111\n"
        "v_add_f32 v255, v255, v112\n"
        "v_mfma_f32_16x16x32_bf16 v[90:93], a[148:151], a[136:139], v[90:93]\n"
        "v_add_f32 v255, v255, v113\n"
        "v_add_f32 v255, v255, v118\n"
        "v_add_f32 v255, v255, v119\n"
        "v_mfma_f32_16x16x32_bf16 v[90:93], a[152:155], a[140:143], v[90:93]\n"
        "v_add_f32 v255, v255, v120\n"
        "v_add_f32 v255, v255, v121\n"
        "v_mov_b32 v254, v255\n"
        "v_mfma_f32_16x16x32_bf16 v[98:101], a[164:167], a[140:143], 0\n"
        "s_nop 0\n"
        "v_permlane32_swap_b32 v255, v254\n"
        "v_add_f32 v255, v255, v254\n"
        "v_mfma_f32_16x16x32_bf16 v[98:101], a[160:163], a[136:139], v[98:101]\n"
        "v_mov_b32 v254, v255\n"
        "v_add_f32 v38, v50, v51\n"
        "v_add_f32 v38, v38, v52\n"
        "v_mfma_f32_16x16x32_bf16 v[98:101], a[156:159], a[132:135], v[98:101]\n"
        "v_permlane16_swap_b32 v255, v254\n"
        "v_add_f32 v255, v255, v254\n"
        "v_mfma_f32_16x16x32_bf16 v[106:109], a[168:171], a[132:135], 0\n"
        "v_mul_f32 v255, 0xbc2aaaab, v255\n"
        "v_add_f32 v38, v38, v53\n"
        "v_add_f32 v46, v46, v255\n"
        "v_mfma_f32_16x16x32_bf16 v[106:109], a[172:175], a[136:139], v[106:109]\n"
        "v_add_f32 v47, v47, v255\n"
        "v_add_f32 v48, v48, v255\n"
        "v_add_f32 v49, v49, v255\n"
        "v_mfma_f32_16x16x32_bf16 v[106:109], a[176:179], a[140:143], v[106:109]\n"
        "v_add_f32 v102, v102, v255\n"
        "v_add_f32 v103, v103, v255\n"
        "v_add_f32 v104, v104, v255\n"
        "v_mfma_f32_16x16x32_bf16 v[138:141], a[188:191], a[140:143], 0\n"
        "v_add_f32 v105, v105, v255\n"
        "v_add_f32 v114, v114, v255\n"
        "v_add_f32 v115, v115, v255\n"
        "v_mfma_f32_16x16x32_bf16 v[138:141], a[184:187], a[136:139], v[138:141]\n"
        "v_add_f32 v116, v116, v255\n"
        "v_add_f32 v117, v117, v255\n"
        "v_add_f32 v34, v34, v255\n"
        "v_mfma_f32_16x16x32_bf16 v[138:141], a[180:183], a[132:135], v[138:141]\n"
        "v_add_f32 v35, v35, v255\n"
        "v_add_f32 v36, v36, v255\n"
        "v_add_f32 v37, v37, v255\n"
        "v_mfma_f32_16x16x32_bf16 v[142:145], a[192:195], a[132:135], 0\n"
        "v_add_f32 v110, v110, v255\n"
        "v_add_f32 v111, v111, v255\n"
        "v_add_f32 v112, v112, v255\n"
        "v_mfma_f32_16x16x32_bf16 v[142:145], a[196:199], a[136:139], v[142:145]\n"
        "v_add_f32 v113, v113, v255\n"
        "v_add_f32 v118, v118, v255\n"
        "v_add_f32 v119, v119, v255\n"
        "v_mfma_f32_16x16x32_bf16 v[142:145], a[200:203], a[140:143], v[142:145]\n"
        "v_add_f32 v120, v120, v255\n"
        "v_add_f32 v121, v121, v255\n"
        "v_cvt_pk_bf16_f32 v146, v46, v47\n"
        "v_mfma_f32_16x16x32_bf16 v[150:153], a[212:215], a[140:143], 0\n"
        "v_cvt_pk_bf16_f32 v147, v48, v49\n"
        "v_cvt_pk_bf16_f32 v148, v102, v103\n"
        "v_cvt_pk_bf16_f32 v149, v104, v105\n"
        "v_mfma_f32_16x16x32_bf16 v[150:153], a[208:211], a[136:139], v[150:153]\n"
        "ds_write_b128 v41, v[146:149]\n"
        "v_cvt_pk_bf16_f32 v46, v114, v115\n"
        "v_mfma_f32_16x16x32_bf16 v[150:153], a[204:207], a[132:135], v[150:153]\n"
        "v_cvt_pk_bf16_f32 v47, v116, v117\n"
        "v_cvt_pk_bf16_f32 v48, v34, v35\n"
        "v_cvt_pk_bf16_f32 v49, v36, v37\n"
        "v_cvt_pk_bf16_f32 v34, v110, v111\n"
        "v_cvt_pk_bf16_f32 v35, v112, v113\n"
        "v_cvt_pk_bf16_f32 v36, v118, v119\n"
        "v_cvt_pk_bf16_f32 v37, v120, v121\n"
        "ds_write_b128 v41, v[46:49] offset:1024\n"
        "ds_write_b128 v41, v[34:37] offset:2048\n"
        "v_add_f32 v38, v38, v30\n"
        "v_add_f32 v38, v38, v31\n"
        "v_add_f32 v38, v38, v32\n"
        "v_add_f32 v38, v38, v33\n"
        "v_add_f32 v38, v38, v82\n"
        "v_add_f32 v38, v38, v83\n"
        "v_add_f32 v38, v38, v84\n"
        "v_add_f32 v38, v38, v85\n"
        "v_add_f32 v38, v38, v78\n"
        "v_add_f32 v38, v38, v79\n"
        "v_add_f32 v38, v38, v80\n"
        "v_add_f32 v38, v38, v81\n"
        "v_add_f32 v38, v38, v122\n"
        "v_add_f32 v38, v38, v123\n"
        "v_add_f32 v38, v38, v124\n"
        "v_add_f32 v38, v38, v125\n"
        "v_add_f32 v38, v38, v54\n"
        "v_add_f32 v38, v38, v55\n"
        "v_add_f32 v38, v38, v56\n"
        "v_add_f32 v38, v38, v57\n"
        "v_mov_b32 v39, v38\n"
        "v_add_f32 v34, v130, v131\n"
        "v_add_f32 v34, v34, v132\n"
        "v_permlane32_swap_b32 v38, v39\n"
        "v_add_f32 v38, v38, v39\n"
        "v_mov_b32 v39, v38\n"
        "v_add_f32 v34, v34, v133\n"
        "v_add_f32 v34, v34, v134\n"
        "v_permlane16_swap_b32 v38, v39\n"
        "v_add_f32 v38, v38, v39\n"
        "v_mul_f32 v38, 0xbc2aaaab, v38\n"
        "v_add_f32 v34, v34, v135\n"
        "v_add_f32 v34, v34, v136\n"
        "v_add_f32 v34, v34, v137\n"
        "v_add_f32 v34, v34, v126\n"
        "v_add_f32 v34, v34, v127\n"
        "v_add_f32 v34, v34, v128\n"
        "v_add_f32 v34, v34, v129\n"
        "v_add_f32 v34, v34, v74\n"
        "v_add_f32 v34, v34, v75\n"
        "v_add_f32 v34, v34, v76\n"
        "v_add_f32 v34, v34, v77\n"
        "v_add_f32 v34, v34, v86\n"
        "v_add_f32 v34, v34, v87\n"
        "v_add_f32 v34, v34, v88\n"
        "v_add_f32 v34, v34, v89\n"
        "v_add_f32 v34, v34, v94\n"
        "v_add_f32 v34, v34, v95\n"
        "v_add_f32 v34, v34, v96\n"
        "v_add_f32 v34, v34, v97\n"
        "v_mov_b32 v35, v34\n"
        "v_add_f32 v62, v90, v91\n"
        "v_add_f32 v62, v62, v92\n"
        "v_permlane32_swap_b32 v34, v35\n"
        "v_add_f32 v34, v34, v35\n"
        "v_mov_b32 v35, v34\n"
        "v_add_f32 v62, v62, v93\n"
        "v_add_f32 v62, v62, v98\n"
        "v_permlane16_swap_b32 v34, v35\n"
        "v_add_f32 v34, v34, v35\n"
        "v_mul_f32 v34, 0xbc2aaaab, v34\n"
        "v_add_f32 v62, v62, v99\n"
        "v_add_f32 v62, v62, v100\n"
        "v_add_f32 v62, v62, v101\n"
        "v_add_f32 v62, v62, v106\n"
        "v_add_f32 v62, v62, v107\n"
        "v_add_f32 v62, v62, v108\n"
        "v_add_f32 v62, v62, v109\n"
        "v_add_f32 v62, v62, v138\n"
        "v_add_f32 v62, v62, v139\n"
        "v_add_f32 v62, v62, v140\n"
        "v_add_f32 v62, v62, v141\n"
        "v_add_f32 v62, v62, v142\n"
        "v_add_f32 v62, v62, v143\n"
        "v_add_f32 v62, v62, v144\n"
        "v_add_f32 v62, v62, v145\n"
        "v_pk_add_f32 v[50:51], v[50:51], v[38:39] op_sel_hi:[1,0]\n"
        "v_pk_add_f32 v[52:53], v[52:53], v[38:39] op_sel_hi:[1,0]\n"
        "v_pk_add_f32 v[30:31], v[30:31], v[38:39] op_sel_hi:[1,0]\n"
        "v_pk_add_f32 v[32:33], v[32:33], v[38:39] op_sel_hi:[1,0]\n"
        "v_pk_add_f32 v[82:83], v[82:83], v[38:39] op_sel_hi:[1,0]\n"
        "v_pk_add_f32 v[84:85], v[84:85], v[38:39] op_sel_hi:[1,0]\n"
        "v_pk_add_f32 v[78:79], v[78:79], v[38:39] op_sel_hi:[1,0]\n"
        "v_pk_add_f32 v[80:81], v[80:81], v[38:39] op_sel_hi:[1,0]\n"
        "v_pk_add_f32 v[122:123], v[122:123], v[38:39] op_sel_hi:[1,0]\n"
        "v_pk_add_f32 v[124:125], v[124:125], v[38:39] op_sel_hi:[1,0]\n"
        "v_pk_add_f32 v[54:55], v[54:55], v[38:39] op_sel_hi:[1,0]\n"
        "v_pk_add_f32 v[56:57], v[56:57], v[38:39] op_sel_hi:[1,0]\n"
        "v_pk_add_f32 v[130:131], v[130:131], v[34:35] op_sel_hi:[1,0]\n"
        "v_pk_add_f32 v[132:133], v[132:133], v[34:35] op_sel_hi:[1,0]\n"
        "v_pk_add_f32 v[134:135], v[134:135], v[34:35] op_sel_hi:[1,0]\n"
        "v_pk_add_f32 v[136:137], v[136:137], v[34:35] op_sel_hi:[1,0]\n"
        "v_pk_add_f32 v[126:127], v[126:127], v[34:35] op_sel_hi:[1,0]\n"
        "v_pk_add_f32 v[128:129], v[128:129], v[34:35] op_sel_hi:[1,0]\n"
        "v_pk_add_f32 v[74:75], v[74:75], v[34:35] op_sel_hi:[1,0]\n"
        "v_pk_add_f32 v[76:77], v[76:77], v[34:35] op_sel_hi:[1,0]\n"
        "v_pk_add_f32 v[86:87], v[86:87], v[34:35] op_sel_hi:[1,0]\n"
        "v_pk_add_f32 v[88:89], v[88:89], v[34:35] op_sel_hi:[1,0]\n"
        "v_pk_add_f32 v[94:95], v[94:95], v[34:35] op_sel_hi:[1,0]\n"
        "v_pk_add_f32 v[96:97], v[96:97], v[34:35] op_sel_hi:[1,0]\n"
        "v_cvt_pk_bf16_f32 v34, v50, v51\n"
        "v_cvt_pk_bf16_f32 v35, v52, v53\n"
        "v_cvt_pk_bf16_f32 v36, v30, v31\n"
        "v_cvt_pk_bf16_f32 v37, v32, v33\n"
        "v_cvt_pk_bf16_f32 v46, v82, v83\n"
        "v_cvt_pk_bf16_f32 v47, v84, v85\n"
        "v_cvt_pk_bf16_f32 v48, v78, v79\n"
        "v_cvt_pk_bf16_f32 v49, v80, v81\n"
        "v_cvt_pk_bf16_f32 v38, v122, v123\n"
        "v_cvt_pk_bf16_f32 v39, v124, v125\n"
        "v_cvt_pk_bf16_f32 v40, v54, v55\n"
        "v_cvt_pk_bf16_f32 v41, v56, v57\n"
        "v_cvt_pk_bf16_f32 v50, v130, v131\n"
        "v_cvt_pk_bf16_f32 v51, v132, v133\n"
        "v_cvt_pk_bf16_f32 v52, v134, v135\n"
        "v_cvt_pk_bf16_f32 v53, v136, v137\n"
        "v_cvt_pk_bf16_f32 v54, v126, v127\n"
        "v_cvt_pk_bf16_f32 v55, v128, v129\n"
        "v_cvt_pk_bf16_f32 v56, v74, v75\n"
        "v_cvt_pk_bf16_f32 v57, v76, v77\n"
        "v_cvt_pk_bf16_f32 v30, v86, v87\n"
        "v_cvt_pk_bf16_f32 v31, v88, v89\n"
        "v_cvt_pk_bf16_f32 v32, v94, v95\n"
        "v_cvt_pk_bf16_f32 v33, v96, v97\n"
        "v_add_f32 v62, v62, v150\n"
        "ds_write_b128 v42, v[34:37]\n"
        "ds_write_b128 v42, v[46:49] offset:1024\n"
        "ds_write_b128 v42, v[38:41] offset:2048\n"
        "ds_write_b128 v43, v[50:53]\n"
        "ds_write_b128 v43, v[54:57] offset:1024\n"
        "ds_write_b128 v43, v[30:33] offset:2048\n"
        "v_add_f32 v62, v62, v151\n"
        "v_add_f32 v62, v62, v152\n"
        "v_add_f32 v62, v62, v153\n"
        "v_mov_b32 v63, v62\n"
        "s_waitcnt vmcnt(9)\n"
        "s_nop 0\n"
        "v_permlane32_swap_b32 v62, v63\n"
        "v_add_f32 v62, v62, v63\n"
        "v_mov_b32 v63, v62\n"
        "s_nop 1\n"
        "v_permlane16_swap_b32 v62, v63\n"
        "v_add_f32 v62, v62, v63\n"
        "v_mul_f32 v62, 0xbc2aaaab, v62\n"
        "v_pk_add_f32 v[90:91], v[90:91], v[62:63] op_sel_hi:[1,0]\n"
        "v_pk_add_f32 v[92:93], v[92:93], v[62:63] op_sel_hi:[1,0]\n"
        "v_pk_add_f32 v[98:99], v[98:99], v[62:63] op_sel_hi:[1,0]\n"
        "v_pk_add_f32 v[100:101], v[100:101], v[62:63] op_sel_hi:[1,0]\n"
        "v_pk_add_f32 v[106:107], v[106:107], v[62:63] op_sel_hi:[1,0]\n"
        "v_pk_add_f32 v[108:109], v[108:109], v[62:63] op_sel_hi:[1,0]\n"
        "v_pk_add_f32 v[138:139], v[138:139], v[62:63] op_sel_hi:[1,0]\n"
        "v_pk_add_f32 v[140:141], v[140:141], v[62:63] op_sel_hi:[1,0]\n"
        "v_pk_add_f32 v[142:143], v[142:143], v[62:63] op_sel_hi:[1,0]\n"
        "v_pk_add_f32 v[144:145], v[144:145], v[62:63] op_sel_hi:[1,0]\n"
        "v_pk_add_f32 v[150:151], v[150:151], v[62:63] op_sel_hi:[1,0]\n"
        "v_pk_add_f32 v[152:153], v[152:153], v[62:63] op_sel_hi:[1,0]\n"
        "v_cvt_pk_bf16_f32 v34, v90, v91\n"
        "v_cvt_pk_bf16_f32 v35, v92, v93\n"
        "v_cvt_pk_bf16_f32 v36, v98, v99\n"
        "v_cvt_pk_bf16_f32 v37, v100, v101\n"
        "v_cvt_pk_bf16_f32 v38, v106, v107\n"
        "v_cvt_pk_bf16_f32 v39, v108, v109\n"
        "v_cvt_pk_bf16_f32 v40, v138, v139\n"
        "v_cvt_pk_bf16_f32 v41, v140, v141\n"
        "v_cvt_pk_bf16_f32 v30, v142, v143\n"
        "v_cvt_pk_bf16_f32 v31, v144, v145\n"
        "v_cvt_pk_bf16_f32 v32, v150, v151\n"
        "v_cvt_pk_bf16_f32 v33, v152, v153\n"
        "ds_write_b128 v44, v[34:37]\n"
        "ds_write_b128 v44, v[38:41] offset:1024\n"
        "ds_write_b128 v44, v[30:33] offset:2048\n"
        "s_waitcnt lgkmcnt(0)\n"
        "s_barrier\n"
        "s_branch FE_%=\n"
        "FG_%=:\n"
        "s_mov_b32 s24, 0x471c4000\n"
        "v_mov_b32 v255, 0x3f803f80\n"
        "v_mov_b32 v255, 0x3f803f80\n"
        "v_mov_b32 v255, 0x3f803f80\n"
        "v_mov_b32 v255, 0x3f803f80\n"
        "s_waitcnt vmcnt(15)\n"
        "ds_read_b64_tr_b16 a[72:73], v34 offset:0\n"
        "ds_read_b64_tr_b16 a[74:75], v35 offset:0\n"
        "ds_read_b64_tr_b16 a[76:77], v34 offset:6144\n"
        "ds_read_b64_tr_b16 a[78:79], v35 offset:6144\n"
        "ds_read_b64_tr_b16 a[80:81], v34 offset:12288\n"
        "ds_read_b64_tr_b16 a[82:83], v35 offset:12288\n"
        "s_add_u32 m0, s53, 15360\n"
        "s_nop 0\n"
        "buffer_load_dwordx4 v46, s[16:19], s23 offen lds\n"
        "ds_read_b64_tr_b16 a[84:85], v34 offset:8\n"
        "ds_read_b64_tr_b16 a[86:87], v35 offset:8\n"
        "ds_read_b64_tr_b16 a[88:89], v34 offset:6152\n"
        "ds_read_b64_tr_b16 a[90:91], v35 offset:6152\n"
        "ds_read_b64_tr_b16 a[92:93], v34 offset:12296\n"
        "ds_read_b64_tr_b16 a[94:95], v35 offset:12296\n"
        "buffer_load_dwordx4 v47, s[16:19], s23 offen offset:1024 lds\n"
        "ds_read_b64_tr_b16 a[96:97], v34 offset:64\n"
        "ds_read_b64_tr_b16 a[98:99], v35 offset:64\n"
        "ds_read_b64_tr_b16 a[100:101], v34 offset:6208\n"
        "ds_read_b64_tr_b16 a[102:103], v35 offset:6208\n"
        "ds_read_b64_tr_b16 a[104:105], v34 offset:12352\n"
        "ds_read_b64_tr_b16 a[106:107], v35 offset:12352\n"
        "buffer_load_dwordx4 v48, s[16:19], s23 offen offset:2048 lds\n"
        "ds_read_b64_tr_b16 a[108:109], v34 offset:72\n"
        "ds_read_b64_tr_b16 a[110:111], v35 offset:72\n"
        "ds_read_b64_tr_b16 a[112:113], v34 offset:6216\n"
        "ds_read_b64_tr_b16 a[114:115], v35 offset:6216\n"
        "ds_read_b64_tr_b16 a[116:117], v34 offset:12360\n"
        "ds_read_b64_tr_b16 a[118:119], v35 offset:12360\n"
        "ds_read_b64_tr_b16 a[120:121], v34 offset:128\n"
        "ds_read_b64_tr_b16 a[122:123], v35 offset:128\n"
        "ds_read_b64_tr_b16 a[124:125], v34 offset:6272\n"
        "ds_read_b64_tr_b16 a[126:127], v35 offset:6272\n"
        "ds_read_b64_tr_b16 a[128:129], v34 offset:12416\n"
        "ds_read_b64_tr_b16 a[130:131], v35 offset:12416\n"
        "ds_read_b64_tr_b16 a[132:133], v34 offset:136\n"
        "ds_read_b64_tr_b16 a[134:135], v35 offset:136\n"
        "ds_read_b64_tr_b16 a[136:137], v34 offset:6280\n"
        "ds_read_b64_tr_b16 a[138:139], v35 offset:6280\n"
        "ds_read_b64_tr_b16 a[140:141], v34 offset:12424\n"
        "ds_read_b64_tr_b16 a[142:143], v35 offset:12424\n"
        "s_waitcnt vmcnt(12)\n"
        "ds_read_b128 a[144:147], v30\n"
        "ds_read_b128 a[148:151], v30 offset:64\n"
        "ds_read_b128 a[152:155], v30 offset:128\n"
        "ds_read_b128 a[156:159], v31\n"
        "ds_read_b128 a[160:163], v31 offset:64\n"
        "ds_read_b128 a[164:167], v31 offset:128\n"
        "s_waitcnt lgkmcnt(15)\n"
        "s_waitcnt lgkmcnt(3)\n"
        "v_mfma_f32_16x16x32_bf16 v[50:53], a[144:147], a[72:75], 0\n"
        "s_waitcnt vmcnt(6)\n"
        "ds_read_b128 a[168:171], v30 offset:6144\n"
        "v_mov_b32 v255, 161888\n"
        "v_mfma_f32_16x16x32_bf16 v[50:53], a[148:151], a[76:79], v[50:53]\n"
        "s_waitcnt vmcnt(0)\n"
        "ds_read_b128 a[172:175], v30 offset:6208\n"
        "ds_read_b128 a[176:179], v30 offset:6272\n"
        "v_mfma_f32_16x16x32_bf16 v[50:53], a[152:155], a[80:83], v[50:53]\n"
        "s_waitcnt lgkmcnt(6)\n"
        "ds_read_b128 a[180:183], v31 offset:6144\n"
        "ds_read_b128 a[184:187], v31 offset:6208\n"
        "s_waitcnt lgkmcnt(5)\n"
        "v_mfma_f32_16x16x32_bf16 v[58:61], a[164:167], a[80:83], 0\n"
        "s_waitcnt lgkmcnt(5)\n"
        "ds_read_b128 a[188:191], v31 offset:6272\n"
        "ds_read_b128 a[192:195], v30 offset:12288\n"
        "v_add_f32 v254, v50, v51\n"
        "v_mfma_f32_16x16x32_bf16 v[58:61], a[160:163], a[76:79], v[58:61]\n"
        "s_waitcnt lgkmcnt(7)\n"
        "ds_read_b128 a[196:199], v30 offset:12352\n"
        "ds_read_b128 a[200:203], v30 offset:12416\n"
        "v_add_f32 v254, v254, v52\n"
        "v_mfma_f32_16x16x32_bf16 v[58:61], a[156:159], a[72:75], v[58:61]\n"
        "s_waitcnt lgkmcnt(9)\n"
        "ds_read_b128 a[204:207], v31 offset:12288\n"
        "v_add_f32 v254, v254, v53\n"
        "s_waitcnt lgkmcnt(7)\n"
        "v_mfma_f32_16x16x32_bf16 v[66:69], a[168:171], a[72:75], 0\n"
        "s_waitcnt lgkmcnt(7)\n"
        "ds_read_b128 a[208:211], v31 offset:12352\n"
        "ds_read_b128 a[212:215], v31 offset:12416\n"
        "v_add_f32 v254, v254, v58\n"
        "v_mfma_f32_16x16x32_bf16 v[66:69], a[172:175], a[76:79], v[66:69]\n"
        "s_waitcnt lgkmcnt(9)\n"
        "v_add_f32 v254, v254, v59\n"
        "v_add_f32 v254, v254, v60\n"
        "v_add_f32 v254, v254, v61\n"
        "v_mfma_f32_16x16x32_bf16 v[66:69], a[176:179], a[80:83], v[66:69]\n"
        "s_waitcnt lgkmcnt(6)\n"
        "s_nop 0\n"
        "v_mfma_f32_16x16x32_bf16 v[62:65], a[188:191], a[80:83], 0\n"
        "s_waitcnt lgkmcnt(3)\n"
        "s_nop 1\n"
        "v_mfma_f32_16x16x32_bf16 v[62:65], a[184:187], a[76:79], v[62:65]\n"
        "s_nop 0\n"
        "v_add_f32 v254, v254, v66\n"
        "v_add_f32 v254, v254, v67\n"
        "v_add_f32 v254, v254, v68\n"
        "v_mfma_f32_16x16x32_bf16 v[62:65], a[180:183], a[72:75], v[62:65]\n"
        "s_nop 0\n"
        "v_add_f32 v254, v254, v69\n"
        "v_mfma_f32_16x16x32_bf16 v[82:85], a[192:195], a[72:75], 0\n"
        "s_nop 2\n"
        "v_mfma_f32_16x16x32_bf16 v[82:85], a[196:199], a[76:79], v[82:85]\n"
        "s_nop 0\n"
        "v_add_f32 v254, v254, v62\n"
        "v_add_f32 v254, v254, v63\n"
        "v_add_f32 v254, v254, v64\n"
        "v_mfma_f32_16x16x32_bf16 v[82:85], a[200:203], a[80:83], v[82:85]\n"
        "s_nop 0\n"
        "v_add_f32 v254, v254, v65\n"
        "s_waitcnt lgkmcnt(0)\n"
        "v_mfma_f32_16x16x32_bf16 v[70:73], a[212:215], a[80:83], 0\n"
        "s_nop 1\n"
        "v_mfma_f32_16x16x32_bf16 v[70:73], a[208:211], a[76:79], v[70:73]\n"
        "s_nop 0\n"
        "v_add_f32 v254, v254, v82\n"
        "v_add_f32 v254, v254, v83\n"
        "v_add_f32 v254, v254, v84\n"
        "v_mfma_f32_16x16x32_bf16 v[70:73], a[204:207], a[72:75], v[70:73]\n"
        "s_nop 0\n"
        "v_add_f32 v254, v254, v85\n"
        "v_mfma_f32_16x16x32_bf16 v[30:33], a[144:147], a[84:87], 0\n"
        "s_nop 2\n"
        "v_mfma_f32_16x16x32_bf16 v[30:33], a[148:151], a[88:91], v[30:33]\n"
        "s_nop 0\n"
        "v_add_f32 v254, v254, v70\n"
        "v_add_f32 v254, v254, v71\n"
        "v_add_f32 v254, v254, v72\n"
        "v_mfma_f32_16x16x32_bf16 v[30:33], a[152:155], a[92:95], v[30:33]\n"
        "buffer_load_dwordx4 v[196:199], v36, s[36:39], 0 offen\n"
        "v_add_f32 v254, v254, v73\n"
        "v_mfma_f32_16x16x32_bf16 v[46:49], a[164:167], a[92:95], 0\n"
        "v_mov_b32 v253, v254\n"
        "v_mfma_f32_16x16x32_bf16 v[46:49], a[160:163], a[88:91], v[46:49]\n"
        "s_nop 0\n"
        "v_permlane32_swap_b32 v254, v253\n"
        "v_add_f32 v254, v254, v253\n"
        "v_mfma_f32_16x16x32_bf16 v[46:49], a[156:159], a[84:87], v[46:49]\n"
        "v_mov_b32 v253, v254\n"
        "v_add_f32 v252, v30, v31\n"
        "v_add_f32 v252, v252, v32\n"
        "v_mfma_f32_16x16x32_bf16 v[122:125], a[168:171], a[84:87], 0\n"
        "v_permlane16_swap_b32 v254, v253\n"
        "v_add_f32 v254, v254, v253\n"
        "v_mfma_f32_16x16x32_bf16 v[122:125], a[172:175], a[88:91], v[122:125]\n"
        "v_mul_f32 v254, 0xbc2aaaab, v254\n"
        "v_add_f32 v252, v252, v33\n"
        "v_add_f32 v50, v50, v254\n"
        "v_mfma_f32_16x16x32_bf16 v[122:125], a[176:179], a[92:95], v[122:125]\n"
        "v_add_f32 v51, v51, v254\n"
        "v_add_f32 v52, v52, v254\n"
        "v_add_f32 v53, v53, v254\n"
        "v_mfma_f32_16x16x32_bf16 v[126:129], a[188:191], a[92:95], 0\n"
        "v_add_f32 v58, v58, v254\n"
        "v_add_f32 v59, v59, v254\n"
        "v_add_f32 v60, v60, v254\n"
        "v_mfma_f32_16x16x32_bf16 v[126:129], a[184:187], a[88:91], v[126:129]\n"
        "v_add_f32 v61, v61, v254\n"
        "v_add_f32 v66, v66, v254\n"
        "v_add_f32 v67, v67, v254\n"
        "v_mfma_f32_16x16x32_bf16 v[126:129], a[180:183], a[84:87], v[126:129]\n"
        "buffer_load_dwordx4 v[202:205], v36, s[36:39], 0 offen offset:16\n"
        "v_add_f32 v68, v68, v254\n"
        "v_mfma_f32_16x16x32_bf16 v[130:133], a[192:195], a[84:87], 0\n"
        "s_mov_b32 s78, 0xf0000f\n"
        "v_add_f32 v69, v69, v254\n"
        "v_add_f32 v62, v62, v254\n"
        "v_add_f32 v63, v63, v254\n"
        "v_mfma_f32_16x16x32_bf16 v[130:133], a[196:199], a[88:91], v[130:133]\n"
        "s_mov_b32 s79, 0xf0000f00\n"
        "v_add_f32 v64, v64, v254\n"
        "v_add_f32 v65, v65, v254\n"
        "v_add_f32 v82, v82, v254\n"
        "v_mfma_f32_16x16x32_bf16 v[130:133], a[200:203], a[92:95], v[130:133]\n"
        "s_mov_b32 s76, 0x11111111\n"
        "v_add_f32 v83, v83, v254\n"
        "v_add_f32 v84, v84, v254\n"
        "v_add_f32 v85, v85, v254\n"
        "v_mfma_f32_16x16x32_bf16 v[134:137], a[212:215], a[92:95], 0\n"
        "s_mov_b32 s77, 0x11111111\n"
        "v_add_f32 v70, v70, v254\n"
        "v_add_f32 v71, v71, v254\n"
        "v_add_f32 v72, v72, v254\n"
        "v_mfma_f32_16x16x32_bf16 v[134:137], a[208:211], a[88:91], v[134:137]\n"
        "s_mov_b32 s56, 0x22222222\n"
        "v_add_f32 v73, v73, v254\n"
        "v_cvt_pk_bf16_f32 v54, v50, v51\n"
        "v_cvt_pk_bf16_f32 v55, v52, v53\n"
        "v_mfma_f32_16x16x32_bf16 v[134:137], a[204:207], a[84:87], v[134:137]\n"
        "s_mov_b32 s57, 0x22222222\n"
        "v_cvt_pk_bf16_f32 v56, v58, v59\n"
        "v_cvt_pk_bf16_f32 v57, v60, v61\n"
        "v_cvt_pk_bf16_f32 v78, v66, v67\n"
        "v_mfma_f32_16x16x32_bf16 v[74:77], a[144:147], a[96:99], 0\n"
        "s_mov_b32 s58, 0x44444444\n"
        "v_cvt_pk_bf16_f32 v79, v68, v69\n"
        "v_cvt_pk_bf16_f32 v80, v62, v63\n"
        "v_cvt_pk_bf16_f32 v81, v64, v65\n"
        "v_mfma_f32_16x16x32_bf16 v[74:77], a[148:151], a[100:103], v[74:77]\n"
        "s_mov_b32 s59, 0x44444444\n"
        "v_cvt_pk_bf16_f32 v50, v82, v83\n"
        "v_cvt_pk_bf16_f32 v51, v84, v85\n"
        "v_cvt_pk_bf16_f32 v52, v70, v71\n"
        "v_mfma_f32_16x16x32_bf16 v[74:77], a[152:155], a[104:107], v[74:77]\n"
        "buffer_load_dwordx4 v[208:211], v36, s[36:39], 0 offen offset:32\n"
        "v_cvt_pk_bf16_f32 v53, v72, v73\n"
        "v_mfma_f32_16x16x32_bf16 v[82:85], a[164:167], a[104:107], 0\n"
        "s_mov_b32 s60, 0x88888888\n"
        "v_add_f32 v252, v252, v46\n"
        "v_add_f32 v252, v252, v47\n"
        "v_add_f32 v252, v252, v48\n"
        "v_mfma_f32_16x16x32_bf16 v[82:85], a[160:163], a[100:103], v[82:85]\n"
        "s_mov_b32 s61, 0x88888888\n"
        "v_add_f32 v252, v252, v49\n"
        "v_add_f32 v252, v252, v122\n"
        "v_add_f32 v252, v252, v123\n"
        "v_mfma_f32_16x16x32_bf16 v[82:85], a[156:159], a[96:99], v[82:85]\n"
        "s_mov_b32 s62, 0xffff\n"
        "v_add_f32 v252, v252, v124\n"
        "v_add_f32 v252, v252, v125\n"
        "v_add_f32 v252, v252, v126\n"
        "v_mfma_f32_16x16x32_bf16 v[86:89], a[168:171], a[96:99], 0\n"
        "s_mov_b32 s63, 0x0\n"
        "v_add_f32 v252, v252, v127\n"
        "v_add_f32 v252, v252, v128\n"
        "v_add_f32 v252, v252, v129\n"
        "v_mfma_f32_16x16x32_bf16 v[86:89], a[172:175], a[100:103], v[86:89]\n"
        "s_mov_b32 s64, 0xffff0000\n"
        "v_add_f32 v252, v252, v130\n"
        "v_add_f32 v252, v252, v131\n"
        "v_add_f32 v252, v252, v132\n"
        "v_mfma_f32_16x16x32_bf16 v[86:89], a[176:179], a[104:107], v[86:89]\n"
        "s_barrier\n"
        "ds_write_b128 v39, v[54:57]\n"
        "v_add_f32 v252, v252, v133\n"
        "v_mfma_f32_16x16x32_bf16 v[98:101], a[188:191], a[104:107], 0\n"
        "s_mov_b32 s65, 0x0\n"
        "ds_write_b128 v39, v[78:81] offset:1024\n"
        "v_add_f32 v252, v252, v134\n"
        "v_mfma_f32_16x16x32_bf16 v[98:101], a[184:187], a[100:103], v[98:101]\n"
        "s_mov_b32 s66, 0x0\n"
        "ds_write_b128 v39, v[50:53] offset:2048\n"
        "v_add_f32 v252, v252, v135\n"
        "v_mfma_f32_16x16x32_bf16 v[98:101], a[180:183], a[96:99], v[98:101]\n"
        "buffer_load_dwordx4 v[58:61], v37, s[36:39], 0 offen\n"
        "v_add_f32 v252, v252, v136\n"
        "v_mfma_f32_16x16x32_bf16 v[102:105], a[192:195], a[96:99], 0\n"
        "s_mov_b32 s67, 0xffff\n"
        "v_add_f32 v252, v252, v137\n"
        "v_mov_b32 v254, v252\n"
        "v_add_f32 v253, v74, v75\n"
        "v_mfma_f32_16x16x32_bf16 v[102:105], a[196:199], a[100:103], v[102:105]\n"
        "s_mov_b32 s68, 0x0\n"
        "v_permlane32_swap_b32 v252, v254\n"
        "v_add_f32 v252, v252, v254\n"
        "v_mfma_f32_16x16x32_bf16 v[102:105], a[200:203], a[104:107], v[102:105]\n"
        "s_mov_b32 s69, 0xffff0000\n"
        "v_mov_b32 v254, v252\n"
        "v_add_f32 v253, v253, v76\n"
        "v_add_f32 v253, v253, v77\n"
        "v_mfma_f32_16x16x32_bf16 v[90:93], a[212:215], a[104:107], 0\n"
        "s_mov_b32 s70, 0x0\n"
        "v_permlane16_swap_b32 v252, v254\n"
        "v_add_f32 v252, v252, v254\n"
        "v_mfma_f32_16x16x32_bf16 v[90:93], a[208:211], a[100:103], v[90:93]\n"
        "s_mov_b32 s71, 0xffffffff\n"
        "v_mul_f32 v252, 0xbc2aaaab, v252\n"
        "v_add_f32 v30, v30, v252\n"
        "v_add_f32 v31, v31, v252\n"
        "v_mfma_f32_16x16x32_bf16 v[90:93], a[204:207], a[96:99], v[90:93]\n"
        "s_mov_b32 s72, 0x0\n"
        "v_add_f32 v32, v32, v252\n"
        "v_add_f32 v33, v33, v252\n"
        "v_add_f32 v46, v46, v252\n"
        "v_mfma_f32_16x16x32_bf16 v[94:97], a[144:147], a[108:111], 0\n"
        "s_mov_b32 s73, 0x20000\n"
        "v_add_f32 v47, v47, v252\n"
        "v_add_f32 v48, v48, v252\n"
        "v_add_f32 v49, v49, v252\n"
        "v_mfma_f32_16x16x32_bf16 v[94:97], a[148:151], a[112:115], v[94:97]\n"
        "s_mov_b64 vcc, s[78:79]\n"
        "v_add_f32 v122, v122, v252\n"
        "v_add_f32 v123, v123, v252\n"
        "v_add_f32 v124, v124, v252\n"
        "v_mfma_f32_16x16x32_bf16 v[94:97], a[152:155], a[116:119], v[94:97]\n"
        "buffer_load_dwordx4 v[64:67], v37, s[36:39], 0 offen offset:16\n"
        "v_add_f32 v125, v125, v252\n"
        "v_mfma_f32_16x16x32_bf16 v[106:109], a[164:167], a[116:119], 0\n"
        "s_mov_b32 s80, 0x3c2aaaab\n"
        "v_add_f32 v126, v126, v252\n"
        "v_add_f32 v127, v127, v252\n"
        "v_add_f32 v128, v128, v252\n"
        "v_mfma_f32_16x16x32_bf16 v[106:109], a[160:163], a[112:115], v[106:109]\n"
        "s_mov_b32 s81, 0x3727c5ac\n"
        "v_add_f32 v129, v129, v252\n"
        "v_add_f32 v130, v130, v252\n"
        "v_add_f32 v131, v131, v252\n"
        "v_mfma_f32_16x16x32_bf16 v[106:109], a[156:159], a[108:111], v[106:109]\n"
        "s_mov_b32 s82, 0xc000c000\n"
        "v_add_f32 v132, v132, v252\n"
        "v_add_f32 v133, v133, v252\n"
        "v_add_f32 v134, v134, v252\n"
        "v_mfma_f32_16x16x32_bf16 v[118:121], a[168:171], a[108:111], 0\n"
        "s_mov_b32 s84, 0x80000000\n"
        "v_add_f32 v135, v135, v252\n"
        "v_add_f32 v136, v136, v252\n"
        "v_add_f32 v137, v137, v252\n"
        "v_mfma_f32_16x16x32_bf16 v[118:121], a[172:175], a[112:115], v[118:121]\n"
        "s_mov_b32 s85, 19\n"
        "v_cvt_pk_bf16_f32 v110, v30, v31\n"
        "v_cvt_pk_bf16_f32 v111, v32, v33\n"
        "v_cvt_pk_bf16_f32 v112, v46, v47\n"
        "v_mfma_f32_16x16x32_bf16 v[118:121], a[176:179], a[116:119], v[118:121]\n"
        "s_mov_b32 s86, 0x80000000\n"
        "v_cvt_pk_bf16_f32 v113, v48, v49\n"
        "v_cvt_pk_bf16_f32 v114, v122, v123\n"
        "v_cvt_pk_bf16_f32 v115, v124, v125\n"
        "v_mfma_f32_16x16x32_bf16 v[46:49], a[188:191], a[116:119], 0\n"
        "s_mov_b32 s89, 0x80000000\n"
        "ds_write_b128 v40, v[110:113]\n"
        "v_cvt_pk_bf16_f32 v116, v126, v127\n"
        "v_mfma_f32_16x16x32_bf16 v[46:49], a[184:187], a[112:115], v[46:49]\n"
        "s_mov_b32 s29, 1\n"
        "v_cvt_pk_bf16_f32 v117, v128, v129\n"
        "v_cvt_pk_bf16_f32 v34, v130, v131\n"
        "v_cvt_pk_bf16_f32 v35, v132, v133\n"
        "v_mfma_f32_16x16x32_bf16 v[46:49], a[180:183], a[108:111], v[46:49]\n"
        "buffer_load_dwordx4 v[70:73], v37, s[36:39], 0 offen offset:32\n"
        "v_cvt_pk_bf16_f32 v36, v134, v135\n"
        "v_mfma_f32_16x16x32_bf16 v[126:129], a[192:195], a[108:111], 0\n"
        "s_mov_b32 s31, 0x400000\n"
        "v_cvt_pk_bf16_f32 v37, v136, v137\n"
        "v_add_f32 v253, v253, v82\n"
        "v_add_f32 v253, v253, v83\n"
        "v_mfma_f32_16x16x32_bf16 v[126:129], a[196:199], a[112:115], v[126:129]\n"
        "s_mov_b32 s87, s49\n"
        "v_add_f32 v253, v253, v84\n"
        "v_add_f32 v253, v253, v85\n"
        "v_add_f32 v253, v253, v86\n"
        "v_mfma_f32_16x16x32_bf16 v[126:129], a[200:203], a[116:119], v[126:129]\n"
        "s_mov_b32 s88, s50\n"
        "v_add_f32 v253, v253, v87\n"
        "v_add_f32 v253, v253, v88\n"
        "v_add_f32 v253, v253, v89\n"
        "v_mfma_f32_16x16x32_bf16 v[30:33], a[212:215], a[116:119], 0\n"
        "s_mov_b64 s[44:45], s[40:41]\n"
        "v_add_f32 v253, v253, v98\n"
        "v_add_f32 v253, v253, v99\n"
        "v_add_f32 v253, v253, v100\n"
        "v_mfma_f32_16x16x32_bf16 v[30:33], a[208:211], a[112:115], v[30:33]\n"
        "s_mov_b32 s46, 0\n"
        "v_add_f32 v253, v253, v101\n"
        "v_add_f32 v253, v253, v102\n"
        "v_add_f32 v253, v253, v103\n"
        "v_mfma_f32_16x16x32_bf16 v[30:33], a[204:207], a[108:111], v[30:33]\n"
        "s_mov_b32 s47, s43\n"
        "v_add_f32 v253, v253, v104\n"
        "v_add_f32 v253, v253, v105\n"
        "v_add_f32 v253, v253, v90\n"
        "v_mfma_f32_16x16x32_bf16 v[130:133], a[144:147], a[120:123], 0\n"
        "v_add_f32 v253, v253, v91\n"
        "v_add_f32 v253, v253, v92\n"
        "v_add_f32 v253, v253, v93\n"
        "v_mfma_f32_16x16x32_bf16 v[130:133], a[148:151], a[124:127], v[130:133]\n"
        "s_add_u32 m0, s52, 0\n"
        "v_mov_b32 v254, v253\n"
        "v_add_f32 v38, v94, v95\n"
        "v_add_f32 v38, v38, v96\n"
        "v_mfma_f32_16x16x32_bf16 v[130:133], a[152:155], a[128:131], v[130:133]\n"
        "buffer_load_dwordx4 v0, s[36:39], s49 offen lds\n"
        "v_add_f32 v38, v38, v97\n"
        "v_mfma_f32_16x16x32_bf16 v[122:125], a[164:167], a[128:131], 0\n"
        "v_permlane32_swap_b32 v253, v254\n"
        "v_add_f32 v253, v253, v254\n"
        "v_mfma_f32_16x16x32_bf16 v[122:125], a[160:163], a[124:127], v[122:125]\n"
        "v_mov_b32 v254, v253\n"
        "v_add_f32 v38, v38, v106\n"
        "v_add_f32 v38, v38, v107\n"
        "v_mfma_f32_16x16x32_bf16 v[122:125], a[156:159], a[120:123], v[122:125]\n"
        "v_permlane16_swap_b32 v253, v254\n"
        "v_add_f32 v253, v253, v254\n"
        "v_mfma_f32_16x16x32_bf16 v[134:137], a[168:171], a[120:123], 0\n"
        "v_mul_f32 v253, 0xbc2aaaab, v253\n"
        "v_add_f32 v74, v74, v253\n"
        "v_add_f32 v75, v75, v253\n"
        "v_mfma_f32_16x16x32_bf16 v[134:137], a[172:175], a[124:127], v[134:137]\n"
        "v_add_f32 v76, v76, v253\n"
        "v_add_f32 v77, v77, v253\n"
        "v_add_f32 v82, v82, v253\n"
        "v_mfma_f32_16x16x32_bf16 v[134:137], a[176:179], a[128:131], v[134:137]\n"
        "buffer_load_dwordx4 v1, s[36:39], s49 offen offset:1024 lds\n"
        "v_add_f32 v83, v83, v253\n"
        "v_mfma_f32_16x16x32_bf16 v[142:145], a[188:191], a[128:131], 0\n"
        "v_add_f32 v84, v84, v253\n"
        "v_add_f32 v85, v85, v253\n"
        "v_add_f32 v86, v86, v253\n"
        "v_mfma_f32_16x16x32_bf16 v[142:145], a[184:187], a[124:127], v[142:145]\n"
        "v_add_f32 v87, v87, v253\n"
        "v_add_f32 v88, v88, v253\n"
        "v_add_f32 v89, v89, v253\n"
        "v_mfma_f32_16x16x32_bf16 v[142:145], a[180:183], a[120:123], v[142:145]\n"
        "v_add_f32 v98, v98, v253\n"
        "v_add_f32 v99, v99, v253\n"
        "v_add_f32 v100, v100, v253\n"
        "v_mfma_f32_16x16x32_bf16 v[146:149], a[192:195], a[120:123], 0\n"
        "v_add_f32 v101, v101, v253\n"
        "v_add_f32 v102, v102, v253\n"
        "v_add_f32 v103, v103, v253\n"
        "v_mfma_f32_16x16x32_bf16 v[146:149], a[196:199], a[124:127], v[146:149]\n"
        "v_add_f32 v104, v104, v253\n"
        "v_add_f32 v105, v105, v253\n"
        "v_add_f32 v90, v90, v253\n"
        "v_mfma_f32_16x16x32_bf16 v[146:149], a[200:203], a[128:131], v[146:149]\n"
        "buffer_load_dwordx4 v2, s[36:39], s49 offen offset:2048 lds\n"
        "v_add_f32 v91, v91, v253\n"
        "v_mfma_f32_16x16x32_bf16 v[150:153], a[212:215], a[128:131], 0\n"
        "v_add_f32 v92, v92, v253\n"
        "v_add_f32 v93, v93, v253\n"
        "v_cvt_pk_bf16_f32 v138, v74, v75\n"
        "v_mfma_f32_16x16x32_bf16 v[150:153], a[208:211], a[124:127], v[150:153]\n"
        "v_cvt_pk_bf16_f32 v139, v76, v77\n"
        "v_cvt_pk_bf16_f32 v140, v82, v83\n"
        "v_cvt_pk_bf16_f32 v141, v84, v85\n"
        "v_mfma_f32_16x16x32_bf16 v[150:153], a[204:207], a[120:123], v[150:153]\n"
        "v_cvt_pk_bf16_f32 v82, v86, v87\n"
        "v_cvt_pk_bf16_f32 v83, v88, v89\n"
        "v_cvt_pk_bf16_f32 v84, v98, v99\n"
        "v_mfma_f32_16x16x32_bf16 v[74:77], a[144:147], a[132:135], 0\n"
        "v_cvt_pk_bf16_f32 v85, v100, v101\n"
        "v_cvt_pk_bf16_f32 v86, v102, v103\n"
        "v_cvt_pk_bf16_f32 v87, v104, v105\n"
        "v_mfma_f32_16x16x32_bf16 v[74:77], a[148:151], a[136:139], v[74:77]\n"
        "v_cvt_pk_bf16_f32 v88, v90, v91\n"
        "v_cvt_pk_bf16_f32 v89, v92, v93\n"
        "v_add_f32 v38, v38, v108\n"
        "v_mfma_f32_16x16x32_bf16 v[74:77], a[152:155], a[140:143], v[74:77]\n"
        "buffer_load_dwordx4 v3, s[36:39], s49 offen offset:3072 lds\n"
        "v_add_f32 v38, v38, v109\n"
        "v_mfma_f32_16x16x32_bf16 v[98:101], a[164:167], a[140:143], 0\n"
        "v_add_f32 v38, v38, v118\n"
        "v_add_f32 v38, v38, v119\n"
        "v_add_f32 v38, v38, v120\n"
        "v_mfma_f32_16x16x32_bf16 v[98:101], a[160:163], a[136:139], v[98:101]\n"
        "v_add_f32 v38, v38, v121\n"
        "v_add_f32 v38, v38, v46\n"
        "v_add_f32 v38, v38, v47\n"
        "v_mfma_f32_16x16x32_bf16 v[98:101], a[156:159], a[132:135], v[98:101]\n"
        "v_add_f32 v38, v38, v48\n"
        "v_add_f32 v38, v38, v49\n"
        "v_add_f32 v38, v38, v126\n"
        "v_mfma_f32_16x16x32_bf16 v[154:157], a[168:171], a[132:135], 0\n"
        "v_add_f32 v38, v38, v127\n"
        "v_add_f32 v38, v38, v128\n"
        "v_add_f32 v38, v38, v129\n"
        "v_mfma_f32_16x16x32_bf16 v[154:157], a[172:175], a[136:139], v[154:157]\n"
        "s_add_u32 m0, s52, 4096\n"
        "v_add_f32 v38, v38, v30\n"
        "v_add_f32 v38, v38, v31\n"
        "v_add_f32 v38, v38, v32\n"
        "v_mfma_f32_16x16x32_bf16 v[154:157], a[176:179], a[140:143], v[154:157]\n"
        "buffer_load_dwordx4 v4, s[36:39], s49 offen lds\n"
        "v_add_f32 v38, v38, v33\n"
        "v_mfma_f32_16x16x32_bf16 v[90:93], a[188:191], a[140:143], 0\n"
        "v_mov_b32 v39, v38\n"
        "v_add_f32 v62, v130, v131\n"
        "v_add_f32 v62, v62, v132\n"
        "v_mfma_f32_16x16x32_bf16 v[90:93], a[184:187], a[136:139], v[90:93]\n"
        "v_permlane32_swap_b32 v38, v39\n"
        "v_add_f32 v38, v38, v39\n"
        "v_mfma_f32_16x16x32_bf16 v[90:93], a[180:183], a[132:135], v[90:93]\n"
        "v_mov_b32 v39, v38\n"
        "v_add_f32 v62, v62, v133\n"
        "v_add_f32 v62, v62, v122\n"
        "v_mfma_f32_16x16x32_bf16 v[158:161], a[192:195], a[132:135], 0\n"
        "v_permlane16_swap_b32 v38, v39\n"
        "v_add_f32 v38, v38, v39\n"
        "v_mfma_f32_16x16x32_bf16 v[158:161], a[196:199], a[136:139], v[158:161]\n"
        "v_mul_f32 v38, 0xbc2aaaab, v38\n"
        "v_add_f32 v62, v62, v123\n"
        "v_add_f32 v62, v62, v124\n"
        "v_mfma_f32_16x16x32_bf16 v[158:161], a[200:203], a[140:143], v[158:161]\n"
        "buffer_load_dwordx4 v5, s[36:39], s49 offen offset:1024 lds\n"
        "v_add_f32 v62, v62, v125\n"
        "v_mfma_f32_16x16x32_bf16 v[102:105], a[212:215], a[140:143], 0\n"
        "v_add_f32 v62, v62, v134\n"
        "v_add_f32 v62, v62, v135\n"
        "v_add_f32 v62, v62, v136\n"
        "v_mfma_f32_16x16x32_bf16 v[102:105], a[208:211], a[136:139], v[102:105]\n"
        "ds_write_b128 v40, v[114:117] offset:1024\n"
        "v_add_f32 v62, v62, v137\n"
        "v_mfma_f32_16x16x32_bf16 v[102:105], a[204:207], a[132:135], v[102:105]\n"
        "ds_write_b128 v40, v[34:37] offset:2048\n"
        "v_add_f32 v62, v62, v142\n"
        "v_pk_add_f32 v[94:95], v[94:95], v[38:39] op_sel_hi:[1,0]\n"
        "v_cvt_pk_bf16_f32 v162, v94, v95\n"
        "v_pk_add_f32 v[96:97], v[96:97], v[38:39] op_sel_hi:[1,0]\n"
        "v_cvt_pk_bf16_f32 v163, v96, v97\n"
        "v_pk_add_f32 v[106:107], v[106:107], v[38:39] op_sel_hi:[1,0]\n"
        "v_cvt_pk_bf16_f32 v164, v106, v107\n"
        "v_pk_add_f32 v[108:109], v[108:109], v[38:39] op_sel_hi:[1,0]\n"
        "v_cvt_pk_bf16_f32 v165, v108, v109\n"
        "v_pk_add_f32 v[118:119], v[118:119], v[38:39] op_sel_hi:[1,0]\n"
        "v_cvt_pk_bf16_f32 v94, v118, v119\n"
        "v_pk_add_f32 v[120:121], v[120:121], v[38:39] op_sel_hi:[1,0]\n"
        "v_cvt_pk_bf16_f32 v95, v120, v121\n"
        "v_pk_add_f32 v[46:47], v[46:47], v[38:39] op_sel_hi:[1,0]\n"
        "v_cvt_pk_bf16_f32 v96, v46, v47\n"
        "v_pk_add_f32 v[48:49], v[48:49], v[38:39] op_sel_hi:[1,0]\n"
        "v_cvt_pk_bf16_f32 v97, v48, v49\n"
        "v_pk_add_f32 v[126:127], v[126:127], v[38:39] op_sel_hi:[1,0]\n"
        "v_cvt_pk_bf16_f32 v46, v126, v127\n"
        "v_pk_add_f32 v[128:129], v[128:129], v[38:39] op_sel_hi:[1,0]\n"
        "v_cvt_pk_bf16_f32 v47, v128, v129\n"
        "v_pk_add_f32 v[30:31], v[30:31], v[38:39] op_sel_hi:[1,0]\n"
        "v_cvt_pk_bf16_f32 v48, v30, v31\n"
        "v_pk_add_f32 v[32:33], v[32:33], v[38:39] op_sel_hi:[1,0]\n"
        "v_cvt_pk_bf16_f32 v49, v32, v33\n"
        "v_add_f32 v62, v62, v143\n"
        "v_add_f32 v62, v62, v144\n"
        "v_add_f32 v62, v62, v145\n"
        "v_add_f32 v62, v62, v146\n"
        "v_add_f32 v62, v62, v147\n"
        "v_add_f32 v62, v62, v148\n"
        "v_add_f32 v62, v62, v149\n"
        "v_add_f32 v62, v62, v150\n"
        "v_add_f32 v62, v62, v151\n"
        "v_add_f32 v62, v62, v152\n"
        "v_add_f32 v62, v62, v153\n"
        "v_mov_b32 v63, v62\n"
        "s_nop 1\n"
        "v_permlane32_swap_b32 v62, v63\n"
        "v_add_f32 v62, v62, v63\n"
        "v_mov_b32 v63, v62\n"
        "s_nop 1\n"
        "v_permlane16_swap_b32 v62, v63\n"
        "v_add_f32 v62, v62, v63\n"
        "v_mul_f32 v62, 0xbc2aaaab, v62\n"
        "v_pk_add_f32 v[130:131], v[130:131], v[62:63] op_sel_hi:[1,0]\n"
        "v_cvt_pk_bf16_f32 v106, v130, v131\n"
        "v_pk_add_f32 v[132:133], v[132:133], v[62:63] op_sel_hi:[1,0]\n"
        "v_cvt_pk_bf16_f32 v107, v132, v133\n"
        "v_pk_add_f32 v[122:123], v[122:123], v[62:63] op_sel_hi:[1,0]\n"
        "v_cvt_pk_bf16_f32 v108, v122, v123\n"
        "v_pk_add_f32 v[124:125], v[124:125], v[62:63] op_sel_hi:[1,0]\n"
        "v_cvt_pk_bf16_f32 v109, v124, v125\n"
        "v_pk_add_f32 v[134:135], v[134:135], v[62:63] op_sel_hi:[1,0]\n"
        "v_cvt_pk_bf16_f32 v30, v134, v135\n"
        "v_pk_add_f32 v[136:137], v[136:137], v[62:63] op_sel_hi:[1,0]\n"
        "v_cvt_pk_bf16_f32 v31, v136, v137\n"
        "v_pk_add_f32 v[142:143], v[142:143], v[62:63] op_sel_hi:[1,0]\n"
        "v_cvt_pk_bf16_f32 v32, v142, v143\n"
        "v_pk_add_f32 v[144:145], v[144:145], v[62:63] op_sel_hi:[1,0]\n"
        "v_cvt_pk_bf16_f32 v33, v144, v145\n"
        "v_pk_add_f32 v[146:147], v[146:147], v[62:63] op_sel_hi:[1,0]\n"
        "v_cvt_pk_bf16_f32 v118, v146, v147\n"
        "v_pk_add_f32 v[148:149], v[148:149], v[62:63] op_sel_hi:[1,0]\n"
        "v_cvt_pk_bf16_f32 v119, v148, v149\n"
        "v_pk_add_f32 v[150:151], v[150:151], v[62:63] op_sel_hi:[1,0]\n"
        "v_cvt_pk_bf16_f32 v120, v150, v151\n"
        "v_pk_add_f32 v[152:153], v[152:153], v[62:63] op_sel_hi:[1,0]\n"
        "v_cvt_pk_bf16_f32 v121, v152, v153\n"
        "v_add_f32 v38, v74, v75\n"
        "v_add_f32 v38, v38, v76\n"
        "v_add_f32 v38, v38, v77\n"
        "v_add_f32 v38, v38, v98\n"
        "v_add_f32 v38, v38, v99\n"
        "v_add_f32 v38, v38, v100\n"
        "v_add_f32 v38, v38, v101\n"
        "v_add_f32 v38, v38, v154\n"
        "v_add_f32 v38, v38, v155\n"
        "v_add_f32 v38, v38, v156\n"
        "v_add_f32 v38, v38, v157\n"
        "v_add_f32 v38, v38, v90\n"
        "v_add_f32 v38, v38, v91\n"
        "v_add_f32 v38, v38, v92\n"
        "v_add_f32 v38, v38, v93\n"
        "v_add_f32 v38, v38, v158\n"
        "v_add_f32 v38, v38, v159\n"
        "v_add_f32 v38, v38, v160\n"
        "v_add_f32 v38, v38, v161\n"
        "v_add_f32 v38, v38, v102\n"
        "v_add_f32 v38, v38, v103\n"
        "v_add_f32 v38, v38, v104\n"
        "v_add_f32 v38, v38, v105\n"
        "v_mov_b32 v39, v38\n"
        "s_nop 1\n"
        "v_permlane32_swap_b32 v38, v39\n"
        "v_add_f32 v38, v38, v39\n"
        "v_mov_b32 v39, v38\n"
        "s_nop 1\n"
        "v_permlane16_swap_b32 v38, v39\n"
        "v_add_f32 v38, v38, v39\n"
        "v_mul_f32 v38, 0xbc2aaaab, v38\n"
        "v_pk_add_f32 v[74:75], v[74:75], v[38:39] op_sel_hi:[1,0]\n"
        "v_cvt_pk_bf16_f32 v122, v74, v75\n"
        "v_pk_add_f32 v[76:77], v[76:77], v[38:39] op_sel_hi:[1,0]\n"
        "v_cvt_pk_bf16_f32 v123, v76, v77\n"
        "v_pk_add_f32 v[98:99], v[98:99], v[38:39] op_sel_hi:[1,0]\n"
        "v_cvt_pk_bf16_f32 v124, v98, v99\n"
        "v_pk_add_f32 v[100:101], v[100:101], v[38:39] op_sel_hi:[1,0]\n"
        "v_cvt_pk_bf16_f32 v125, v100, v101\n"
        "v_pk_add_f32 v[154:155], v[154:155], v[38:39] op_sel_hi:[1,0]\n"
        "v_cvt_pk_bf16_f32 v74, v154, v155\n"
        "v_pk_add_f32 v[156:157], v[156:157], v[38:39] op_sel_hi:[1,0]\n"
        "v_cvt_pk_bf16_f32 v75, v156, v157\n"
        "v_pk_add_f32 v[90:91], v[90:91], v[38:39] op_sel_hi:[1,0]\n"
        "v_cvt_pk_bf16_f32 v76, v90, v91\n"
        "v_pk_add_f32 v[92:93], v[92:93], v[38:39] op_sel_hi:[1,0]\n"
        "v_cvt_pk_bf16_f32 v77, v92, v93\n"
        "v_pk_add_f32 v[158:159], v[158:159], v[38:39] op_sel_hi:[1,0]\n"
        "v_cvt_pk_bf16_f32 v90, v158, v159\n"
        "v_pk_add_f32 v[160:161], v[160:161], v[38:39] op_sel_hi:[1,0]\n"
        "v_cvt_pk_bf16_f32 v91, v160, v161\n"
        "v_pk_add_f32 v[102:103], v[102:103], v[38:39] op_sel_hi:[1,0]\n"
        "v_cvt_pk_bf16_f32 v92, v102, v103\n"
        "v_pk_add_f32 v[104:105], v[104:105], v[38:39] op_sel_hi:[1,0]\n"
        "v_cvt_pk_bf16_f32 v93, v104, v105\n"
        "s_nop 1\n"
        "v_mfma_f32_16x16x32_bf16 v[98:101], v[54:57], v[54:57], 0\n"
        "ds_write_b128 v41, v[138:141]\n"
        "v_mfma_f32_16x16x32_bf16 v[98:101], v[78:81], v[78:81], v[98:101]\n"
        "ds_write_b128 v41, v[82:85] offset:1024\n"
        "v_mfma_f32_16x16x32_bf16 v[98:101], v[50:53], v[50:53], v[98:101]\n"
        "buffer_load_dwordx4 v6, s[36:39], s49 offen offset:2048 lds\n"
        "v_mfma_f32_16x16x32_bf16 v[98:101], v[110:113], v[110:113], v[98:101]\n"
        "ds_write_b128 v41, v[86:89] offset:2048\n"
        "v_mfma_f32_16x16x32_bf16 v[98:101], v[114:117], v[114:117], v[98:101]\n"
        "ds_write_b128 v42, v[162:165]\n"
        "v_mfma_f32_16x16x32_bf16 v[98:101], v[34:37], v[34:37], v[98:101]\n"
        "ds_write_b128 v42, v[94:97] offset:1024\n"
        "v_mfma_f32_16x16x32_bf16 v[98:101], v[138:141], v[138:141], v[98:101]\n"
        "ds_write_b128 v42, v[46:49] offset:2048\n"
        "v_mfma_f32_16x16x32_bf16 v[98:101], v[82:85], v[82:85], v[98:101]\n"
        "ds_write_b128 v43, v[106:109]\n"
        "v_mfma_f32_16x16x32_bf16 v[98:101], v[86:89], v[86:89], v[98:101]\n"
        "buffer_load_dwordx4 v7, s[36:39], s49 offen offset:3072 lds\n"
        "v_mfma_f32_16x16x32_bf16 v[98:101], v[162:165], v[162:165], v[98:101]\n"
        "ds_write_b128 v43, v[30:33] offset:1024\n"
        "v_mfma_f32_16x16x32_bf16 v[98:101], v[94:97], v[94:97], v[98:101]\n"
        "ds_write_b128 v43, v[118:121] offset:2048\n"
        "v_mfma_f32_16x16x32_bf16 v[98:101], v[46:49], v[46:49], v[98:101]\n"
        "ds_write_b128 v44, v[122:125]\n"
        "v_mfma_f32_16x16x32_bf16 v[98:101], v[106:109], v[106:109], v[98:101]\n"
        "ds_write_b128 v44, v[74:77] offset:1024\n"
        "v_mfma_f32_16x16x32_bf16 v[98:101], v[30:33], v[30:33], v[98:101]\n"
        "s_add_u32 m0, s52, 8192\n"
        "ds_write_b128 v44, v[90:93] offset:2048\n"
        "v_mfma_f32_16x16x32_bf16 v[98:101], v[118:121], v[118:121], v[98:101]\n"
        "buffer_load_dwordx4 v8, s[36:39], s49 offen lds\n"
        "v_mfma_f32_16x16x32_bf16 v[98:101], v[122:125], v[122:125], v[98:101]\n"
        "v_mfma_f32_16x16x32_bf16 v[98:101], v[74:77], v[74:77], v[98:101]\n"
        "v_mfma_f32_16x16x32_bf16 v[98:101], v[90:93], v[90:93], v[98:101]\n"
        "s_nop 2\n"
        "s_waitcnt vmcnt(9)\n"
        "s_nop 3\n"
        "v_max3_f32 v254, v98, v99, v100\n"
        "v_max_f32 v254, v254, v101\n"
        "v_cmp_nge_f32_e64 s[74:75], s24, v254\n"
        "v_mov_b32 v254, 161632\n"
        "s_nop 0\n"
        "v_cndmask_b32_e64 v254, v255, v254, s[74:75]\n"
        "v_mov_b32 v255, 0x80008000\n"
        "ds_write_b32 v254, v255\n"
        "s_waitcnt lgkmcnt(0)\n"
        "s_barrier\n"
        "FE_%=:\n"
        "s_mov_b64 s[16:17], s[54:55]\n"
        "s_cmp_eq_u32 s51, 0\n"
        "s_cselect_b32 s18, 128, 0\n"
        "s_mov_b32 s49, 161888\n"
        "s_cselect_b32 s49, 163424, s49\n"
        "s_movk_i32 s22, 1584\n"
        "s_movk_i32 s23, 1584\n"
        "s_mov_b32 s53, 19\n"
        "s_mov_b32 s54, 0x05040302\n"
        "s_mov_b32 s55, 0x07060302\n"
        "v_mov_b32 v28, s80\n"
        "ds_read_b128 v[96:99], v26\n"
        "ds_read_b128 v[100:103], v26 offset:64\n"
        "ds_read_b128 v[92:95], v26 offset:128\n"
        "v_xor_b32 v78, 64, v186\n"
        "ds_read_b128 a[0:3], v186\n"
        "ds_read_b128 a[4:7], v186 offset:1024\n"
        "ds_read_b128 a[8:11], v186 offset:2048\n"
        "ds_read_b128 a[12:15], v186 offset:3072\n"
        "ds_read_b128 a[16:19], v186 offset:4096\n"
        "ds_read_b128 a[20:23], v186 offset:5120\n"
        "ds_read_b128 a[24:27], v186 offset:6144\n"
        "ds_read_b128 a[28:31], v186 offset:7168\n"
        "ds_read_b128 a[32:35], v186 offset:8192\n"
        "v_mfma_f32_16x16x32_bf16 v[152:155], v[196:199], v[196:199], 0\n"
        "ds_read_b128 a[36:39], v186 offset:9216\n"
        "v_mov_b32 v63, 161632\n"
        "v_mfma_f32_16x16x32_bf16 v[152:155], v[202:205], v[202:205], v[152:155]\n"
        "ds_read_b128 a[40:43], v186 offset:10240\n"
        "v_lshrrev_b32 v222, 2, v186\n"
        "v_mfma_f32_16x16x32_bf16 v[152:155], v[208:211], v[208:211], v[152:155]\n"
        "ds_read_b128 a[44:47], v186 offset:11264\n"
        "v_and_b32 v222, 128, v222\n"
        "v_mfma_f32_16x16x32_bf16 v[112:115], v[58:61], v[58:61], 0\n"
        "ds_read_b128 a[48:51], v186 offset:12288\n"
        "v_xor_b32 v222, v222, v186\n"
        "v_mfma_f32_16x16x32_bf16 v[112:115], v[64:67], v[64:67], v[112:115]\n"
        "s_nop 0\n"
        "ds_read_b128 a[52:55], v186 offset:13312\n"
        "v_cndmask_b32_e64 v75, v152, v153, s[56:57]\n"
        "v_mfma_f32_16x16x32_bf16 v[112:115], v[70:73], v[70:73], v[112:115]\n"
        "s_waitcnt lgkmcnt(14)\n"
        "ds_read_b128 a[56:59], v186 offset:14336\n"
        "v_cndmask_b32_e64 v75, v75, v154, s[58:59]\n"
        "v_mfma_f32_16x16x32_bf16 v[158:161], v[92:95], v[70:73], 0\n"
        "ds_read_b128 a[60:63], v186 offset:15360\n"
        "v_cndmask_b32_e64 v75, v75, v155, s[60:61]\n"
        "v_mfma_f32_16x16x32_bf16 v[158:161], v[100:103], v[64:67], v[158:161]\n"
        "ds_read_b128 a[64:67], v186 offset:16384\n"
        "v_cndmask_b32_e64 v255, v112, v113, s[56:57]\n"
        "v_mfma_f32_16x16x32_bf16 v[158:161], v[96:99], v[58:61], v[158:161]\n"
        "s_waitcnt lgkmcnt(14)\n"
        "ds_read_b128 a[68:71], v186 offset:17408\n"
        "v_cndmask_b32_e64 v255, v255, v114, s[58:59]\n"
        "v_mfma_f32_16x16x32_bf16 v[84:87], v[96:99], v[196:199], 0\n"
        "s_waitcnt lgkmcnt(12)\n"
        "ds_read_b128 a[72:75], v186 offset:18432\n"
        "v_cndmask_b32_e64 v255, v255, v115, s[60:61]\n"
        "v_mfma_f32_16x16x32_bf16 v[84:87], v[100:103], v[202:205], v[84:87]\n"
        "s_waitcnt lgkmcnt(10)\n"
        "ds_read_b128 a[76:79], v186 offset:19456\n"
        "v_cndmask_b32_dpp v75, v255, v75, vcc row_ror:4 row_mask:0xf bank_mask:0xf\n"
        "v_mfma_f32_16x16x32_bf16 v[84:87], v[92:95], v[208:211], v[84:87]\n"
        "ds_read_b128 a[80:83], v186 offset:20480\n"
        "v_add_u32 v255, 73792, v186\n"
        "v_mfma_f32_16x16x32_bf16 v[118:121], a[8:11], v[208:211], 0\n"
        "ds_read_b128 a[84:87], v186 offset:21504\n"
        "v_add_u32 v222, 140896, v222\n"
        "v_mfma_f32_16x16x32_bf16 v[118:121], a[4:7], v[202:205], v[118:121]\n"
        "s_waitcnt lgkmcnt(10)\n"
        "ds_read_b128 a[88:91], v186 offset:22528\n"
        "v_cndmask_b32_dpp v90, v158, v84, vcc row_ror:4 row_mask:0xf bank_mask:0xf\n"
        "v_mfma_f32_16x16x32_bf16 v[118:121], a[0:3], v[196:199], v[118:121]\n"
        "ds_read_b128 a[92:95], v186 offset:23552\n"
        "v_mul_f32 v87, s80, v90\n"
        "v_mfma_f32_16x16x32_bf16 v[152:155], a[12:15], v[196:199], 0\n"
        "ds_read_b128 a[96:99], v78 offset:24576\n"
        "v_fma_f32 v87, -v87, v90, v75\n"
        "v_mfma_f32_16x16x32_bf16 v[152:155], a[16:19], v[202:205], v[152:155]\n"
        "s_waitcnt lgkmcnt(10)\n"
        "ds_read_b128 a[100:103], v78 offset:25600\n"
        "v_cvt_pk_bf16_f32 v112, v118, v119\n"
        "v_mfma_f32_16x16x32_bf16 v[152:155], a[20:23], v[208:211], v[152:155]\n"
        "ds_read_b128 a[104:107], v78 offset:26624\n"
        "v_cvt_pk_bf16_f32 v113, v120, v121\n"
        "v_mfma_f32_16x16x32_bf16 v[118:121], a[32:35], v[208:211], 0\n"
        "ds_read_b128 a[108:111], v78 offset:27648\n"
        "v_max_f32 v87, 0, v87\n"
        "v_mfma_f32_16x16x32_bf16 v[118:121], a[28:31], v[202:205], v[118:121]\n"
        "s_waitcnt lgkmcnt(10)\n"
        "ds_read_b128 a[112:115], v78 offset:28672\n"
        "v_cvt_pk_bf16_f32 v114, v152, v153\n"
        "v_mfma_f32_16x16x32_bf16 v[118:121], a[24:27], v[196:199], v[118:121]\n"
        "ds_read_b128 a[116:119], v78 offset:29696\n"
        "v_cvt_pk_bf16_f32 v115, v154, v155\n"
        "v_mfma_f32_16x16x32_bf16 v[74:77], a[36:39], v[196:199], 0\n"
        "ds_read_b128 a[120:123], v186 offset:30720\n"
        "v_fmaak_f32 v87, v28, v87, 0x3727c5ac\n"
        "v_mfma_f32_16x16x32_bf16 v[74:77], a[40:43], v[202:205], v[74:77]\n"
        "s_nop 0\n"
        "ds_read_b128 a[124:127], v186 offset:31744\n"
        "v_cvt_pk_bf16_f32 v152, v118, v119\n"
        "v_mfma_f32_16x16x32_bf16 v[74:77], a[44:47], v[208:211], v[74:77]\n"
        "ds_read_b128 a[128:131], v186 offset:32768\n"
        "v_cvt_pk_bf16_f32 v153, v120, v121\n"
        "v_mfma_f32_16x16x32_bf16 v[118:121], a[56:59], v[208:211], 0\n"
        "ds_read_b128 a[132:135], v186 offset:33792\n"
        "v_cndmask_b32_dpp v254, v159, v85, vcc row_ror:4 row_mask:0xf bank_mask:0xf\n"
        "v_mfma_f32_16x16x32_bf16 v[118:121], a[52:55], v[202:205], v[118:121]\n"
        "s_nop 0\n"
        "ds_read_b128 a[136:139], v186 offset:34816\n"
        "v_cvt_pk_bf16_f32 v154, v74, v75\n"
        "v_mfma_f32_16x16x32_bf16 v[118:121], a[48:51], v[196:199], v[118:121]\n"
        "s_nop 0\n"
        "ds_read_b128 a[140:143], v186 offset:35840\n"
        "v_cvt_pk_bf16_f32 v155, v76, v77\n"
        "v_mfma_f32_16x16x32_bf16 v[158:161], a[60:63], v[196:199], 0\n"
        "s_nop 0\n"
        "v_rsq_f32 v78, v87\n"
        "v_mfma_f32_16x16x32_bf16 v[158:161], a[64:67], v[202:205], v[158:161]\n"
        "s_nop 0\n"
        "v_cvt_pk_bf16_f32 v84, v118, v119\n"
        "v_cvt_pk_bf16_f32 v85, v120, v121\n"
        "v_mfma_f32_16x16x32_bf16 v[158:161], a[68:71], v[208:211], v[158:161]\n"
        "s_nop 1\n"
        "v_mfma_f32_16x16x32_bf16 v[118:121], a[8:11], v[70:73], 0\n"
        "s_nop 2\n"
        "v_mfma_f32_16x16x32_bf16 v[118:121], a[4:7], v[64:67], v[118:121]\n"
        "s_nop 0\n"
        "v_cvt_pk_bf16_f32 v86, v158, v159\n"
        "v_cvt_pk_bf16_f32 v87, v160, v161\n"
        "v_mfma_f32_16x16x32_bf16 v[118:121], a[0:3], v[58:61], v[118:121]\n"
        "s_nop 1\n"
        "v_mfma_f32_16x16x32_bf16 v[158:161], v[196:199], v[112:115], 0\n"
        "s_nop 2\n"
        "v_mfma_f32_16x16x32_bf16 v[158:161], v[202:205], v[152:155], v[158:161]\n"
        "s_nop 0\n"
        "v_cvt_pk_bf16_f32 v152, v118, v119\n"
        "v_cvt_pk_bf16_f32 v153, v120, v121\n"
        "v_mfma_f32_16x16x32_bf16 v[158:161], v[208:211], v[84:87], v[158:161]\n"
        "v_mfma_f32_16x16x32_bf16 v[112:115], a[20:23], v[70:73], 0\n"
        "s_waitcnt lgkmcnt(0)\n"
        "v_mfma_f32_16x16x32_bf16 v[112:115], a[16:19], v[64:67], v[112:115]\n"
        "s_barrier\n"
        "ds_read_b128 a[144:147], v222\n"
        "v_mfma_f32_16x16x32_bf16 v[112:115], a[12:15], v[58:61], v[112:115]\n"
        "s_nop 1\n"
        "v_mfma_f32_16x16x32_bf16 v[84:87], a[24:27], v[58:61], 0\n"
        "s_nop 0\n"
        "ds_read_b128 a[148:151], v222 offset:1024\n"
        "s_nop 0\n"
        "v_mfma_f32_16x16x32_bf16 v[84:87], a[28:31], v[64:67], v[84:87]\n"
        "s_nop 0\n"
        "v_cvt_pk_bf16_f32 v154, v112, v113\n"
        "v_cvt_pk_bf16_f32 v155, v114, v115\n"
        "v_mfma_f32_16x16x32_bf16 v[84:87], a[32:35], v[70:73], v[84:87]\n"
        "s_nop 1\n"
        "v_mfma_f32_16x16x32_bf16 v[118:121], a[44:47], v[70:73], 0\n"
        "s_nop 2\n"
        "v_mfma_f32_16x16x32_bf16 v[118:121], a[40:43], v[64:67], v[118:121]\n"
        "s_nop 0\n"
        "v_cvt_pk_bf16_f32 v112, v84, v85\n"
        "v_cvt_pk_bf16_f32 v113, v86, v87\n"
        "v_mfma_f32_16x16x32_bf16 v[118:121], a[36:39], v[58:61], v[118:121]\n"
        "s_nop 0\n"
        "ds_read_b128 a[152:155], v222 offset:2048\n"
        "v_mfma_f32_16x16x32_bf16 v[84:87], a[48:51], v[58:61], 0\n"
        "s_nop 2\n"
        "v_mfma_f32_16x16x32_bf16 v[84:87], a[52:55], v[64:67], v[84:87]\n"
        "s_nop 0\n"
        "v_cvt_pk_bf16_f32 v114, v118, v119\n"
        "v_cvt_pk_bf16_f32 v115, v120, v121\n"
        "v_mfma_f32_16x16x32_bf16 v[84:87], a[56:59], v[70:73], v[84:87]\n"
        "s_nop 0\n"
        "ds_read_b128 a[156:159], v222 offset:3072\n"
        "v_mfma_f32_16x16x32_bf16 v[74:77], a[68:71], v[70:73], 0\n"
        "s_nop 2\n"
        "v_mfma_f32_16x16x32_bf16 v[74:77], a[64:67], v[64:67], v[74:77]\n"
        "s_nop 0\n"
        "v_cvt_pk_bf16_f32 v118, v84, v85\n"
        "v_cvt_pk_bf16_f32 v119, v86, v87\n"
        "v_mfma_f32_16x16x32_bf16 v[74:77], a[60:63], v[58:61], v[74:77]\n"
        "s_nop 0\n"
        "ds_read_b128 a[160:163], v222 offset:4096\n"
        "v_mfma_f32_16x16x32_bf16 v[84:87], v[196:199], a[72:75], 0\n"
        "s_nop 2\n"
        "v_mfma_f32_16x16x32_bf16 v[84:87], v[202:205], a[76:79], v[84:87]\n"
        "s_nop 0\n"
        "v_cvt_pk_bf16_f32 v120, v74, v75\n"
        "v_cvt_pk_bf16_f32 v121, v76, v77\n"
        "v_mfma_f32_16x16x32_bf16 v[84:87], v[208:211], a[80:83], v[84:87]\n"
        "s_nop 1\n"
        "v_mfma_f32_16x16x32_bf16 v[74:77], v[208:211], a[92:95], 0\n"
        "s_nop 0\n"
        "ds_read_b128 a[164:167], v222 offset:5120\n"
        "s_nop 0\n"
        "v_mfma_f32_16x16x32_bf16 v[74:77], v[202:205], a[88:91], v[74:77]\n"
        "s_nop 0\n"
        "v_cvt_pk_bf16_f32 v42, v84, v85\n"
        "v_cvt_pk_bf16_f32 v43, v86, v87\n"
        "v_mfma_f32_16x16x32_bf16 v[74:77], v[196:199], a[84:87], v[74:77]\n"
        "s_nop 0\n"
        "v_mfma_f32_16x16x32_bf16 v[84:87], v[58:61], v[152:155], 0\n"
        "s_nop 2\n"
        "v_mfma_f32_16x16x32_bf16 v[84:87], v[64:67], v[112:115], v[84:87]\n"
        "s_nop 0\n"
        "ds_read_b128 a[168:171], v222 offset:6144\n"
        "v_cvt_pk_bf16_f32 v46, v74, v75\n"
        "v_mfma_f32_16x16x32_bf16 v[84:87], v[70:73], v[118:121], v[84:87]\n"
        "s_waitcnt vmcnt(0)\n"
        "v_cvt_pk_bf16_f32 v47, v76, v77\n"
        "v_mfma_f32_16x16x32_bf16 v[74:77], v[208:211], a[104:107], 0\n"
        "s_nop 0\n"
        "ds_read_b128 v[126:129], v9\n"
        "v_mfma_f32_16x16x32_bf16 v[74:77], v[202:205], a[100:103], v[74:77]\n"
        "s_nop 0\n"
        "ds_read_b128 v[132:135], v10\n"
        "v_cndmask_b32_dpp v186, v84, v158, vcc row_ror:4 row_mask:0xf bank_mask:0xf\n"
        "v_mfma_f32_16x16x32_bf16 v[74:77], v[196:199], a[96:99], v[74:77]\n"
        "ds_read_b128 v[138:141], v11\n"
        "v_cndmask_b32_dpp v157, v85, v159, vcc row_ror:4 row_mask:0xf bank_mask:0xf\n"
        "v_mfma_f32_16x16x32_bf16 v[82:85], v[196:199], a[108:111], 0\n"
        "ds_read_b128 v[106:109], v9 offset:3072\n"
        "v_cndmask_b32_dpp v81, v86, v160, vcc row_ror:4 row_mask:0xf bank_mask:0xf\n"
        "v_mfma_f32_16x16x32_bf16 v[82:85], v[202:205], a[112:115], v[82:85]\n"
        "ds_read_b128 v[112:115], v10 offset:3072\n"
        "v_cndmask_b32_dpp v91, v87, v161, vcc row_ror:4 row_mask:0xf bank_mask:0xf\n"
        "v_mfma_f32_16x16x32_bf16 v[82:85], v[208:211], a[116:119], v[82:85]\n"
        "ds_read_b128 v[118:121], v11 offset:3072\n"
        "v_mul_f32_dpp v186, v78, v186 quad_perm:[0,0,0,0] row_mask:0xf bank_mask:0xf\n"
        "v_mfma_f32_16x16x32_bf16 v[228:231], v[208:211], a[128:131], 0\n"
        "ds_read_b128 v[146:149], v9 offset:6144\n"
        "v_mul_f32_dpp v157, v78, v157 quad_perm:[1,1,1,1] row_mask:0xf bank_mask:0xf\n"
        "v_mfma_f32_16x16x32_bf16 v[228:231], v[202:205], a[124:127], v[228:231]\n"
        "ds_read_b128 v[152:155], v10 offset:6144\n"
        "v_mul_f32_dpp v81, v78, v81 quad_perm:[2,2,2,2] row_mask:0xf bank_mask:0xf\n"
        "v_mfma_f32_16x16x32_bf16 v[228:231], v[196:199], a[120:123], v[228:231]\n"
        "ds_read_b128 v[158:161], v11 offset:6144\n"
        "v_mul_f32_dpp v91, v78, v91 quad_perm:[3,3,3,3] row_mask:0xf bank_mask:0xf\n"
        "v_mfma_f32_16x16x32_bf16 v[250:253], v[196:199], a[132:135], 0\n"
        "ds_read_b128 a[172:175], v222 offset:7168\n"
        "v_max3_f32 v45, v186, v157, v81\n"
        "v_mfma_f32_16x16x32_bf16 v[250:253], v[202:205], a[136:139], v[250:253]\n"
        "s_waitcnt lgkmcnt(7)\n"
        "ds_read_b128 a[176:179], v222 offset:8192\n"
        "v_max_f32 v45, v45, v91\n"
        "v_mfma_f32_16x16x32_bf16 v[250:253], v[208:211], a[140:143], v[250:253]\n"
        "v_mul_f32 v45, v45, v78\n"
        "v_fma_f32 v186, v186, v78, -v45\n"
        "v_mfma_f32_16x16x32_bf16 v[86:89], v[126:129], v[126:129], 0\n"
        "s_mov_b32 s28, s89\n"
        "v_fma_f32 v157, v157, v78, -v45\n"
        "v_fma_f32 v81, v81, v78, -v45\n"
        "v_mfma_f32_16x16x32_bf16 v[86:89], v[132:135], v[132:135], v[86:89]\n"
        "s_add_u32 s89, s86, s51\n"
        "v_fma_f32 v91, v91, v78, -v45\n"
        "v_cvt_pk_bf16_f32 v36, v74, v75\n"
        "v_mfma_f32_16x16x32_bf16 v[86:89], v[138:141], v[138:141], v[86:89]\n"
        "s_mov_b32 s30, s29\n"
        "v_exp_f32 v186, v186\n"
        "v_mfma_f32_16x16x32_bf16 v[50:53], v[92:95], v[138:141], 0\n"
        "s_add_u32 s29, s29, 1\n"
        "v_exp_f32 v157, v157\n"
        "v_mfma_f32_16x16x32_bf16 v[50:53], v[100:103], v[132:135], v[50:53]\n"
        "s_mov_b32 s86, s87\n"
        "v_exp_f32 v81, v81\n"
        "v_mfma_f32_16x16x32_bf16 v[50:53], v[96:99], v[126:129], v[50:53]\n"
        "s_add_u32 s88, s88, 32\n"
        "v_exp_f32 v91, v91\n"
        "v_mfma_f32_16x16x32_bf16 v[214:217], a[0:3], v[126:129], 0\n"
        "s_add_u32 s87, s87, 43008\n"
        "v_add_f32 v75, v186, v157\n"
        "v_add_f32 v74, v81, v91\n"
        "v_mfma_f32_16x16x32_bf16 v[214:217], a[4:7], v[132:135], v[214:217]\n"
        "s_cmp_ge_u32 s88, 73\n"
        "v_add_f32 v75, v75, v74\n"
        "v_cvt_pk_bf16_f32 v37, v76, v77\n"
        "v_mfma_f32_16x16x32_bf16 v[214:217], a[8:11], v[138:141], v[214:217]\n"
        "s_cselect_b32 s92, 588672, 0\n"
        "v_rcp_f32 v75, v75\n"
        "v_mfma_f32_16x16x32_bf16 v[224:227], a[20:23], v[138:141], 0\n"
        "s_cselect_b32 s94, 73, 0\n"
        "ds_read_b64 v[44:45], v27\n"
        "ds_read_b64 v[38:39], v27\n"
        "v_mfma_f32_16x16x32_bf16 v[224:227], a[16:19], v[132:135], v[224:227]\n"
        "s_sub_u32 s88, s88, s94\n"
        "ds_read_b64 v[32:33], v27\n"
        "v_mul_f32 v186, v186, v75\n"
        "v_mfma_f32_16x16x32_bf16 v[224:227], a[12:15], v[126:129], v[224:227]\n"
        "s_add_u32 s87, s87, s92\n"
        "ds_read_b32 v63, v63\n"
        "v_mul_f32 v157, v157, v75\n"
        "v_mfma_f32_16x16x32_bf16 v[218:221], a[24:27], v[126:129], 0\n"
        "s_cmp_gt_i32 s85, 0\n"
        "v_mul_f32 v81, v81, v75\n"
        "v_mul_f32 v91, v91, v75\n"
        "v_mfma_f32_16x16x32_bf16 v[218:221], a[28:31], v[132:135], v[218:221]\n"
        "s_cselect_b32 s94, s87, 0\n"
        "v_mul_f32_dpp v186, v78, v186 quad_perm:[0,0,0,0] row_mask:0xf bank_mask:0xf\n"
        "v_mul_f32_dpp v157, v78, v157 quad_perm:[1,1,1,1] row_mask:0xf bank_mask:0xf\n"
        "v_mfma_f32_16x16x32_bf16 v[218:221], a[32:35], v[138:141], v[218:221]\n"
        "s_sub_u32 s85, s85, 1\n"
        "v_mul_f32_dpp v81, v78, v81 quad_perm:[2,2,2,2] row_mask:0xf bank_mask:0xf\n"
        "v_mul_f32_dpp v91, v78, v91 quad_perm:[3,3,3,3] row_mask:0xf bank_mask:0xf\n"
        "v_mfma_f32_16x16x32_bf16 v[76:79], a[44:47], v[138:141], 0\n"
        "s_waitcnt lgkmcnt(9)\n"
        "v_cvt_pk_bf16_f32 v75, v186, v157\n"
        "v_cvt_pk_bf16_f32 v74, v81, v91\n"
        "v_mfma_f32_16x16x32_bf16 v[76:79], a[40:43], v[132:135], v[76:79]\n"
        "s_add_u32 s38, s36, s94\n"
        "v_and_b32 v194, v25, v75\n"
        "v_and_b32 v195, v25, v74\n"
        "v_mfma_f32_16x16x32_bf16 v[76:79], a[36:39], v[126:129], v[76:79]\n"
        "s_addc_u32 s39, s37, 0\n"
        "v_mov_b64 v[200:201], v[194:195]\n"
        "v_mov_b64 v[206:207], v[194:195]\n"
        "v_mfma_f32_16x16x32_bf16 v[52:55], a[48:51], v[126:129], 0\n"
        "s_waitcnt lgkmcnt(1)\n"
        "v_cvt_pk_bf16_f32 v40, v82, v83\n"
        "v_cvt_pk_bf16_f32 v41, v84, v85\n"
        "v_mfma_f32_16x16x32_bf16 v[52:55], a[52:55], v[132:135], v[52:55]\n"
        "s_waitcnt lgkmcnt(0)\n"
        "v_readfirstlane_b32 s83, v63\n"
        "v_cvt_pk_bf16_f32 v30, v228, v229\n"
        "v_mfma_f32_16x16x32_bf16 v[52:55], a[56:59], v[138:141], v[52:55]\n"
        "s_waitcnt lgkmcnt(0)\n"
        "v_cvt_pk_bf16_f32 v31, v230, v231\n"
        "v_mov_b64 v[212:213], v[194:195]\n"
        "v_mfma_f32_16x16x32_bf16 v[82:85], a[68:71], v[138:141], 0\n"
        "v_cvt_pk_bf16_f32 v34, v250, v251\n"
        "v_cvt_pk_bf16_f32 v35, v252, v253\n"
        "v_mfma_f32_16x16x32_bf16 v[82:85], a[64:67], v[132:135], v[82:85]\n"
        "ds_read_b128 a[180:183], v222 offset:9216\n"
        "v_and_b32_dpp v56, v75, v25 row_ror:12 row_mask:0xf bank_mask:0xf\n"
        "v_mfma_f32_16x16x32_bf16 v[82:85], a[60:63], v[126:129], v[82:85]\n"
        "ds_read_b128 a[184:187], v222 offset:10240\n"
        "v_and_b32_dpp v57, v74, v25 row_ror:12 row_mask:0xf bank_mask:0xf\n"
        "v_mfma_f32_16x16x32_bf16 v[250:253], v[42:45], v[194:197], 0\n"
        "ds_read_b128 a[188:191], v222 offset:11264\n"
        "v_mov_b64 v[62:63], v[56:57]\n"
        "v_mfma_f32_16x16x32_bf16 v[230:233], v[44:47], v[198:201], 0\n"
        "s_nop 0\n"
        "ds_read_b128 a[192:195], v222 offset:12288\n"
        "v_mov_b64 v[68:69], v[56:57]\n"
        "v_mfma_f32_16x16x32_bf16 v[46:49], v[58:61], a[72:75], 0\n"
        "s_nop 0\n"
        "v_cvt_pk_bf16_f32 v228, v250, v251\n"
        "v_cvt_pk_bf16_f32 v250, v252, v253\n"
        "v_mfma_f32_16x16x32_bf16 v[46:49], v[64:67], a[76:79], v[46:49]\n"
        "v_cvt_pk_bf16_f32 v229, v232, v233\n"
        "v_cvt_pk_bf16_f32 v194, v230, v231\n"
        "v_mfma_f32_16x16x32_bf16 v[46:49], v[70:73], a[80:83], v[46:49]\n"
        "s_add_u32 m0, s52, 0\n"
        "ds_read_b128 a[196:199], v222 offset:13312\n"
        "v_mov_b64 v[74:75], v[56:57]\n"
        "v_mfma_f32_16x16x32_bf16 v[230:233], v[70:73], a[92:95], 0\n"
        "ds_read_b128 a[200:203], v222 offset:14336\n"
        "v_cvt_pk_bf16_f32 v198, v214, v215\n"
        "v_mfma_f32_16x16x32_bf16 v[230:233], v[64:67], a[88:91], v[230:233]\n"
        "global_load_lds_dwordx4 v0, s[38:39]\n"
        "v_cvt_pk_bf16_f32 v42, v46, v47\n"
        "v_mfma_f32_16x16x32_bf16 v[230:233], v[58:61], a[84:87], v[230:233]\n"
        "ds_read_b128 a[204:207], v222 offset:15360\n"
        "v_cvt_pk_bf16_f32 v43, v48, v49\n"
        "v_mfma_f32_16x16x32_bf16 v[166:169], v[36:39], v[200:203], 0\n"
        "ds_read_b128 a[208:211], v222 offset:16384\n"
        "v_cvt_pk_bf16_f32 v199, v216, v217\n"
        "v_mfma_f32_16x16x32_bf16 v[214:217], v[38:41], v[204:207], 0\n"
        "s_nop 0\n"
        "ds_read_b128 a[212:215], v222 offset:17408\n"
        "v_cvt_pk_bf16_f32 v46, v230, v231\n"
        "v_mfma_f32_16x16x32_bf16 v[202:205], v[58:61], a[96:99], 0\n"
        "s_nop 0\n"
        "v_cvt_pk_bf16_f32 v251, v166, v167\n"
        "v_cvt_pk_bf16_f32 v195, v168, v169\n"
        "v_mfma_f32_16x16x32_bf16 v[202:205], v[64:67], a[100:103], v[202:205]\n"
        "v_cvt_pk_bf16_f32 v230, v214, v215\n"
        "v_cvt_pk_bf16_f32 v252, v216, v217\n"
        "v_mfma_f32_16x16x32_bf16 v[202:205], v[70:73], a[104:107], v[202:205]\n"
        "v_cvt_pk_bf16_f32 v47, v232, v233\n"
        "v_cvt_pk_bf16_f32 v200, v224, v225\n"
        "v_mfma_f32_16x16x32_bf16 v[214:217], v[70:73], a[116:119], 0\n"
        "s_nop 0\n"
        "ds_read_b128 a[216:219], v255\n"
        "v_cvt_pk_bf16_f32 v201, v226, v227\n"
        "v_mfma_f32_16x16x32_bf16 v[214:217], v[64:67], a[112:115], v[214:217]\n"
        "global_load_lds_dwordx4 v1, s[38:39] offset:1024\n"
        "v_cvt_pk_bf16_f32 v36, v202, v203\n"
        "v_mfma_f32_16x16x32_bf16 v[214:217], v[58:61], a[108:111], v[214:217]\n"
        "v_cvt_pk_bf16_f32 v37, v204, v205\n"
        "v_cvt_pk_bf16_f32 v226, v218, v219\n"
        "v_mfma_f32_16x16x32_bf16 v[222:225], v[30:33], v[206:209], 0\n"
        "s_nop 0\n"
        "ds_read_b128 a[220:223], v255 offset:1024\n"
        "v_cvt_pk_bf16_f32 v227, v220, v221\n"
        "v_mfma_f32_16x16x32_bf16 v[206:209], v[32:35], v[210:213], 0\n"
        "s_nop 0\n"
        "v_cvt_pk_bf16_f32 v40, v214, v215\n"
        "v_cvt_pk_bf16_f32 v41, v216, v217\n"
        "v_mfma_f32_16x16x32_bf16 v[202:205], v[58:61], a[120:123], 0\n"
        "v_cvt_pk_bf16_f32 v196, v222, v223\n"
        "v_cvt_pk_bf16_f32 v231, v224, v225\n"
        "v_mfma_f32_16x16x32_bf16 v[202:205], v[64:67], a[124:127], v[202:205]\n"
        "ds_write_b128 v12, v[228:231]\n"
        "v_mfma_f32_16x16x32_bf16 v[202:205], v[70:73], a[128:131], v[202:205]\n"
        "s_nop 0\n"
        "v_cvt_pk_bf16_f32 v253, v206, v207\n"
        "v_cvt_pk_bf16_f32 v197, v208, v209\n"
        "v_mfma_f32_16x16x32_bf16 v[208:211], v[70:73], a[140:143], 0\n"
        "s_nop 0\n"
        "ds_write_b128 v12, v[250:253] offset:1024\n"
        "v_mfma_f32_16x16x32_bf16 v[208:211], v[64:67], a[136:139], v[208:211]\n"
        "global_load_lds_dwordx4 v2, s[38:39] offset:2048\n"
        "v_cvt_pk_bf16_f32 v30, v202, v203\n"
        "v_mfma_f32_16x16x32_bf16 v[208:211], v[58:61], a[132:135], v[208:211]\n"
        "ds_write_b128 v12, v[194:197] offset:2048\n"
        "v_mfma_f32_16x16x32_bf16 v[218:221], v[228:231], v[228:231], 0\n"
        "v_cvt_pk_bf16_f32 v228, v76, v77\n"
        "v_cvt_pk_bf16_f32 v229, v78, v79\n"
        "v_mfma_f32_16x16x32_bf16 v[218:221], v[250:253], v[250:253], v[218:221]\n"
        "v_cvt_pk_bf16_f32 v31, v204, v205\n"
        "v_cvt_pk_bf16_f32 v76, v52, v53\n"
        "v_mfma_f32_16x16x32_bf16 v[218:221], v[194:197], v[194:197], v[218:221]\n"
        "v_cvt_pk_bf16_f32 v77, v54, v55\n"
        "v_cvt_pk_bf16_f32 v78, v82, v83\n"
        "v_mfma_f32_16x16x32_bf16 v[202:205], v[106:109], v[106:109], 0\n"
        "v_cvt_pk_bf16_f32 v79, v84, v85\n"
        "v_cvt_pk_bf16_f32 v34, v208, v209\n"
        "v_mfma_f32_16x16x32_bf16 v[202:205], v[112:115], v[112:115], v[202:205]\n"
        "v_cvt_pk_bf16_f32 v35, v210, v211\n"
        "v_fmac_f32_dpp v90, v254, v186 quad_perm:[0,0,0,0] row_mask:0xf bank_mask:0xf\n"
        "v_mfma_f32_16x16x32_bf16 v[202:205], v[118:121], v[118:121], v[202:205]\n"
        "ds_read_b128 a[224:227], v255 offset:2048\n"
        "v_fmac_f32_dpp v90, v254, v157 quad_perm:[1,1,1,1] row_mask:0xf bank_mask:0xf\n"
        "v_mfma_f32_16x16x32_bf16 v[208:211], v[92:95], v[118:121], 0\n"
        "global_load_lds_dwordx4 v3, s[38:39] offset:3072\n"
        "v_cndmask_b32_e64 v186, v218, v219, s[56:57]\n"
        "v_mfma_f32_16x16x32_bf16 v[208:211], v[100:103], v[112:115], v[208:211]\n"
        "ds_read_b128 a[228:231], v255 offset:3088\n"
        "v_fmac_f32_dpp v90, v254, v81 quad_perm:[2,2,2,2] row_mask:0xf bank_mask:0xf\n"
        "v_mfma_f32_16x16x32_bf16 v[208:211], v[96:99], v[106:109], v[208:211]\n"
        "ds_read_b128 a[232:235], v255 offset:4112\n"
        "v_cndmask_b32_e64 v186, v186, v220, s[58:59]\n"
        "v_mfma_f32_16x16x32_bf16 v[82:85], v[42:45], v[56:59], 0\n"
        "ds_read_b128 a[236:239], v255 offset:5136\n"
        "v_cndmask_b32_e64 v186, v186, v221, s[60:61]\n"
        "v_mfma_f32_16x16x32_bf16 v[54:57], v[44:47], v[60:63], 0\n"
        "s_nop 0\n"
        "ds_read_b128 a[240:243], v255 offset:6176\n"
        "v_fmac_f32_dpp v90, v254, v91 quad_perm:[3,3,3,3] row_mask:0xf bank_mask:0xf\n"
        "v_mfma_f32_16x16x32_bf16 v[210:213], v[36:39], v[62:65], 0\n"
        "s_nop 0\n"
        "v_cvt_pk_bf16_f32 v194, v82, v83\n"
        "v_cvt_pk_bf16_f32 v58, v84, v85\n"
        "v_mfma_f32_16x16x32_bf16 v[60:63], v[38:41], v[66:69], 0\n"
        "s_add_u32 m0, s52, 4096\n"
        "v_cvt_pk_bf16_f32 v64, v54, v55\n"
        "v_cvt_pk_bf16_f32 v195, v56, v57\n"
        "v_mfma_f32_16x16x32_bf16 v[54:57], v[30:33], v[68:71], 0\n"
        "v_cvt_pk_bf16_f32 v59, v210, v211\n"
        "v_cvt_pk_bf16_f32 v65, v212, v213\n"
        "v_mfma_f32_16x16x32_bf16 v[68:71], v[32:35], v[72:75], 0\n"
        "global_load_lds_dwordx4 v4, s[38:39]\n"
        "v_cvt_pk_bf16_f32 v196, v60, v61\n"
        "v_mfma_f32_16x16x32_bf16 v[34:37], v[126:129], v[198:201], 0\n"
        "s_nop 0\n"
        "v_cvt_pk_bf16_f32 v60, v62, v63\n"
        "v_cvt_pk_bf16_f32 v66, v54, v55\n"
        "v_mfma_f32_16x16x32_bf16 v[34:37], v[132:135], v[226:229], v[34:37]\n"
        "v_cvt_pk_bf16_f32 v197, v56, v57\n"
        "v_cvt_pk_bf16_f32 v61, v68, v69\n"
        "v_mfma_f32_16x16x32_bf16 v[34:37], v[138:141], v[76:79], v[34:37]\n"
        "ds_write_b128 v12, v[194:197] offset:3072\n"
        "v_mfma_f32_16x16x32_bf16 v[72:75], a[8:11], v[118:121], 0\n"
        "ds_write_b128 v12, v[58:61] offset:4096\n"
        "v_mfma_f32_16x16x32_bf16 v[72:75], a[4:7], v[112:115], v[72:75]\n"
        "v_cvt_pk_bf16_f32 v67, v70, v71\n"
        "v_mul_f32 v78, s80, v90\n"
        "v_mfma_f32_16x16x32_bf16 v[72:75], a[0:3], v[106:109], v[72:75]\n"
        "s_nop 0\n"
        "ds_write_b128 v12, v[64:67] offset:5120\n"
        "v_mfma_f32_16x16x32_bf16 v[68:71], v[194:197], v[194:197], 0\n"
        "s_nop 0\n"
        "ds_read_b128 a[244:247], v255 offset:7200\n"
        "ds_read_b128 a[248:251], v255 offset:8224\n"
        "v_mfma_f32_16x16x32_bf16 v[68:71], v[58:61], v[58:61], v[68:71]\n"
        "global_load_lds_dwordx4 v5, s[38:39] offset:1024\n"
        "v_cvt_pk_bf16_f32 v194, v72, v73\n"
        "v_mfma_f32_16x16x32_bf16 v[68:71], v[64:67], v[64:67], v[68:71]\n"
        "v_cvt_pk_bf16_f32 v195, v74, v75\n"
        "v_cndmask_b32_e64 v222, v202, v203, s[56:57]\n"
        "v_mfma_f32_16x16x32_bf16 v[64:67], a[20:23], v[118:121], 0\n"
        "s_nop 0\n"
        "ds_read_b128 a[252:255], v255 offset:9264\n"
        "ds_read_b128 v[72:75], v255 offset:10288\n"
        "v_mfma_f32_16x16x32_bf16 v[64:67], a[16:19], v[112:115], v[64:67]\n"
        "s_nop 0\n"
        "v_cndmask_b32_e64 v58, v68, v69, s[56:57]\n"
        "v_cndmask_b32_e64 v58, v58, v70, s[58:59]\n"
        "v_mfma_f32_16x16x32_bf16 v[64:67], a[12:15], v[106:109], v[64:67]\n"
        "v_cndmask_b32_e64 v58, v58, v71, s[60:61]\n"
        "v_cndmask_b32_e64 v222, v222, v204, s[58:59]\n"
        "v_mfma_f32_16x16x32_bf16 v[60:63], a[24:27], v[106:109], 0\n"
        "v_cndmask_b32_dpp v186, v58, v186, vcc row_ror:4 row_mask:0xf bank_mask:0xf\n"
        "v_fma_f32 v78, -v78, v90, v186\n"
        "v_mfma_f32_16x16x32_bf16 v[60:63], a[28:31], v[112:115], v[60:63]\n"
        "v_max_f32 v78, 0, v78\n"
        "v_fmaak_f32 v78, v28, v78, 0x3727c5ac\n"
        "v_mfma_f32_16x16x32_bf16 v[60:63], a[32:35], v[118:121], v[60:63]\n"
        "v_sub_f32 v186, 0x47c00000, v186\n"
        "v_bfi_b32 v78, s84, v186, v78\n"
        "v_mfma_f32_16x16x32_bf16 v[56:59], a[44:47], v[118:121], 0\n"
        "global_load_lds_dwordx4 v6, s[38:39] offset:2048\n"
        "v_cvt_pk_bf16_f32 v196, v64, v65\n"
        "v_mfma_f32_16x16x32_bf16 v[56:59], a[40:43], v[112:115], v[56:59]\n"
        "v_rsq_f32 v64, v78\n"
        "v_mfma_f32_16x16x32_bf16 v[56:59], a[36:39], v[106:109], v[56:59]\n"
        "v_cvt_f16_f32 v64, v64\n"
        "v_cvt_pk_bf16_f32 v197, v66, v67\n"
        "v_mfma_f32_16x16x32_bf16 v[76:79], a[48:51], v[106:109], 0\n"
        "ds_write_b16 v18, v64\n"
        "v_mfma_f32_16x16x32_bf16 v[76:79], a[52:55], v[112:115], v[76:79]\n"
        "ds_write_b16 v19, v64\n"
        "v_mfma_f32_16x16x32_bf16 v[76:79], a[56:59], v[118:121], v[76:79]\n"
        "ds_read_b128 v[64:67], v255 offset:11312\n"
        "v_cvt_pk_bf16_f32 v200, v60, v61\n"
        "v_mfma_f32_16x16x32_bf16 v[226:229], a[68:71], v[118:121], 0\n"
        "ds_read_b128 v[68:71], v255 offset:12352\n"
        "v_cvt_pk_bf16_f32 v201, v62, v63\n"
        "v_mfma_f32_16x16x32_bf16 v[226:229], a[64:67], v[112:115], v[226:229]\n"
        "ds_read_b128 v[60:63], v255 offset:13376\n"
        "v_cvt_pk_bf16_f32 v202, v56, v57\n"
        "v_mfma_f32_16x16x32_bf16 v[226:229], a[60:63], v[106:109], v[226:229]\n"
        "global_load_lds_dwordx4 v7, s[38:39] offset:3072\n"
        "v_cvt_pk_bf16_f32 v203, v58, v59\n"
        "v_mfma_f32_16x16x32_bf16 v[82:85], v[146:149], v[146:149], 0\n"
        "ds_read_b128 v[56:59], v255 offset:14400\n"
        "v_cvt_pk_bf16_f32 v210, v76, v77\n"
        "v_mfma_f32_16x16x32_bf16 v[82:85], v[152:155], v[152:155], v[82:85]\n"
        "ds_read_b128 v[52:55], v255 offset:15440\n"
        "v_cvt_pk_bf16_f32 v211, v78, v79\n"
        "v_mfma_f32_16x16x32_bf16 v[82:85], v[158:161], v[158:161], v[82:85]\n"
        "ds_read_b128 v[76:79], v255 offset:16464\n"
        "v_cvt_pk_bf16_f32 v212, v226, v227\n"
        "v_mfma_f32_16x16x32_bf16 v[218:221], v[92:95], v[158:161], 0\n"
        "s_waitcnt lgkmcnt(6)\n"
        "v_cvt_pk_bf16_f32 v213, v228, v229\n"
        "v_cndmask_b32_e64 v222, v222, v205, s[60:61]\n"
        "v_mfma_f32_16x16x32_bf16 v[218:221], v[100:103], v[152:155], v[218:221]\n"
        "s_barrier\n"
        "v_cndmask_b32_e64 v204, v82, v83, s[56:57]\n"
        "v_cndmask_b32_e64 v204, v204, v84, s[58:59]\n"
        "v_mfma_f32_16x16x32_bf16 v[218:221], v[96:99], v[146:149], v[218:221]\n"
        "s_add_u32 m0, s52, 8192\n"
        "v_cndmask_b32_e64 v204, v204, v85, s[60:61]\n"
        "v_cndmask_b32_e64 v205, v86, v87, s[56:57]\n"
        "v_mfma_f32_16x16x32_bf16 v[226:229], v[106:109], v[194:197], 0\n"
        "ds_read_b128 v[84:87], v255 offset:17488\n"
        "v_cndmask_b32_e64 v205, v205, v88, s[58:59]\n"
        "v_mfma_f32_16x16x32_bf16 v[226:229], v[112:115], v[200:203], v[226:229]\n"
        "global_load_lds_dwordx4 v8, s[38:39]\n"
        "v_cndmask_b32_dpp v186, v218, v208, vcc row_ror:4 row_mask:0xf bank_mask:0xf\n"
        "v_mfma_f32_16x16x32_bf16 v[226:229], v[118:121], v[210:213], v[226:229]\n"
        "v_cndmask_b32_e64 v205, v205, v89, s[60:61]\n"
        "v_cndmask_b32_dpp v222, v204, v222, vcc row_ror:4 row_mask:0xf bank_mask:0xf\n"
        "v_mfma_f32_16x16x32_bf16 v[196:199], a[8:11], v[158:161], 0\n"
        "v_cndmask_b32_dpp v186, v186, v50, vcc row_ror:4 row_mask:0xf bank_mask:0xf\n"
        "v_cndmask_b32_dpp v205, v222, v205, vcc row_ror:4 row_mask:0xf bank_mask:0xf\n"
        "v_mfma_f32_16x16x32_bf16 v[196:199], a[4:7], v[152:155], v[196:199]\n"
        "v_mul_f32 v255, s80, v186\n"
        "v_fma_f32 v255, -v255, v186, v205\n"
        "v_mfma_f32_16x16x32_bf16 v[196:199], a[0:3], v[146:149], v[196:199]\n"
        "s_nop 0\n"
        "v_max_f32 v255, 0, v255\n"
        "v_fmaak_f32 v255, v28, v255, 0x3727c5ac\n"
        "v_mfma_f32_16x16x32_bf16 v[202:205], a[12:15], v[146:149], 0\n"
        "s_nop 0\n"
        "v_rsq_f32 v157, v255\n"
        "v_mfma_f32_16x16x32_bf16 v[202:205], a[16:19], v[152:155], v[202:205]\n"
        "s_nop 0\n"
        "v_cvt_pk_bf16_f32 v40, v196, v197\n"
        "v_cvt_pk_bf16_f32 v41, v198, v199\n"
        "v_mfma_f32_16x16x32_bf16 v[202:205], a[20:23], v[158:161], v[202:205]\n"
        "s_waitcnt lgkmcnt(0)\n"
        "v_cndmask_b32_dpp v255, v219, v209, vcc row_ror:4 row_mask:0xf bank_mask:0xf\n"
        "v_mfma_f32_16x16x32_bf16 v[208:211], a[32:35], v[158:161], 0\n"
        "s_nop 0\n"
        "v_cndmask_b32_dpp v255, v255, v51, vcc row_ror:4 row_mask:0xf bank_mask:0xf\n"
        "s_nop 0\n"
        "v_mfma_f32_16x16x32_bf16 v[208:211], a[28:31], v[152:155], v[208:211]\n"
        "s_nop 0\n"
        "v_cvt_pk_bf16_f32 v42, v202, v203\n"
        "v_cvt_pk_bf16_f32 v43, v204, v205\n"
        "v_mfma_f32_16x16x32_bf16 v[208:211], a[24:27], v[146:149], v[208:211]\n"
        "s_nop 1\n"
        "v_mfma_f32_16x16x32_bf16 v[196:199], a[36:39], v[146:149], 0\n"
        "s_nop 2\n"
        "v_mfma_f32_16x16x32_bf16 v[196:199], a[40:43], v[152:155], v[196:199]\n"
        "s_nop 0\n"
        "v_cvt_pk_bf16_f32 v88, v208, v209\n"
        "v_cvt_pk_bf16_f32 v89, v210, v211\n"
        "v_mfma_f32_16x16x32_bf16 v[196:199], a[44:47], v[158:161], v[196:199]\n"
        "s_nop 0\n"
        "v_mfma_f32_16x16x32_bf16 v[202:205], a[56:59], v[158:161], 0\n"
        "s_nop 0\n"
        "ds_read_b128 v[210:213], v13\n"
        "ds_read_b128 v[214:217], v13 offset:1024\n"
        "v_mfma_f32_16x16x32_bf16 v[202:205], a[52:55], v[152:155], v[202:205]\n"
        "s_nop 0\n"
        "ds_read_b128 v[218:221], v13 offset:2048\n"
        "v_cvt_pk_bf16_f32 v90, v196, v197\n"
        "v_mfma_f32_16x16x32_bf16 v[202:205], a[48:51], v[146:149], v[202:205]\n"
        "v_mov_b64 v[224:225], 0\n"
        "s_mov_b64 exec, 1\n"
        "buffer_atomic_add v224, off, s[16:19], 0 sc0\n"
        "buffer_atomic_add v225, off, s[16:19], s15 offset:4 sc0\n"
        "s_mov_b64 exec, -1\n"
        "v_mfma_f32_16x16x32_bf16 v[250:253], a[60:63], v[146:149], 0\n"
        "v_cvt_pk_bf16_f32 v91, v198, v199\n"
        "v_mfma_f32_16x16x32_bf16 v[250:253], a[64:67], v[152:155], v[250:253]\n"
        "v_cvt_pk_bf16_f32 v234, v202, v203\n"
        "v_cvt_pk_bf16_f32 v235, v204, v205\n"
        "v_mfma_f32_16x16x32_bf16 v[250:253], a[68:71], v[158:161], v[250:253]\n"
        "v_mfma_f32_16x16x32_bf16 v[166:169], v[138:141], a[80:83], 0\n"
        "v_mfma_f32_16x16x32_bf16 v[166:169], v[132:135], a[76:79], v[166:169]\n"
        "ds_read_b64_tr_b16 v[48:49], v187\n"
        "ds_read_b64_tr_b16 v[50:51], v187 offset:3072\n"
        "v_mfma_f32_16x16x32_bf16 v[166:169], v[126:129], a[72:75], v[166:169]\n"
        "s_waitcnt lgkmcnt(2)\n"
        "ds_read_b64_tr_b16 v[230:231], v187 offset:6144\n"
        "ds_read_b64_tr_b16 v[232:233], v187 offset:9216\n"
        "v_mfma_f32_16x16x32_bf16 v[178:181], v[126:129], a[84:87], 0\n"
        "ds_read_b64_tr_b16 v[80:81], v187 offset:12288\n"
        "ds_read_b128 v[206:209], v13 offset:3072\n"
        "v_mfma_f32_16x16x32_bf16 v[178:181], v[132:135], a[88:91], v[178:181]\n"
        "ds_read_b128 v[202:205], v13 offset:4096\n"
        "ds_read_b128 v[194:197], v13 offset:5120\n"
        "v_mfma_f32_16x16x32_bf16 v[178:181], v[138:141], a[92:95], v[178:181]\n"
        "ds_read_b128 v[198:201], v20\n"
        "ds_read_b64 v[222:223], v21\n"
        "v_mfma_f32_16x16x32_bf16 v[162:165], v[146:149], v[40:43], 0\n"
        "ds_read_b64_tr_b16 v[82:83], v187 offset:15360\n"
        "v_cvt_pk_bf16_f32 v236, v250, v251\n"
        "v_mfma_f32_16x16x32_bf16 v[162:165], v[152:155], v[88:91], v[162:165]\n"
        "v_cvt_pk_bf16_f32 v237, v252, v253\n"
        "v_cvt_pk_bf16_f32 v42, v166, v167\n"
        "s_nop 0\n"
        "v_mfma_f32_16x16x32_bf16 v[162:165], v[158:161], v[234:237], v[162:165]\n"
        "s_nop 0\n"
        "v_cvt_pk_bf16_f32 v43, v168, v169\n"
        "v_cvt_pk_bf16_f32 v46, v178, v179\n"
        "v_mfma_f32_16x16x32_bf16 v[234:237], a[152:155], v[218:221], 0\n"
        "s_nop 0\n"
        "v_cvt_pk_bf16_f32 v47, v180, v181\n"
        "v_mfma_f32_16x16x32_bf16 v[234:237], a[148:151], v[214:217], v[234:237]\n"
        "s_nop 0\n"
        "v_cndmask_b32_dpp v251, v162, v226, vcc row_ror:4 row_mask:0xf bank_mask:0xf\n"
        "v_cndmask_b32_dpp v253, v163, v227, vcc row_ror:4 row_mask:0xf bank_mask:0xf\n"
        "v_mfma_f32_16x16x32_bf16 v[234:237], a[144:147], v[210:213], v[234:237]\n"
        "v_cndmask_b32_dpp v254, v164, v228, vcc row_ror:4 row_mask:0xf bank_mask:0xf\n"
        "v_cndmask_b32_dpp v252, v165, v229, vcc row_ror:4 row_mask:0xf bank_mask:0xf\n"
        "v_mfma_f32_16x16x32_bf16 v[162:165], a[156:159], v[210:213], 0\n"
        "v_cndmask_b32_dpp v251, v251, v34, vcc row_ror:4 row_mask:0xf bank_mask:0xf\n"
        "v_cndmask_b32_dpp v253, v253, v35, vcc row_ror:4 row_mask:0xf bank_mask:0xf\n"
        "v_mfma_f32_16x16x32_bf16 v[162:165], a[160:163], v[214:217], v[162:165]\n"
        "s_waitcnt lgkmcnt(1)\n"
        "ds_read_b64_tr_b16 v[166:167], v187 offset:8\n"
        "ds_read_b64_tr_b16 v[168:169], v187 offset:3080\n"
        "v_mfma_f32_16x16x32_bf16 v[162:165], a[164:167], v[218:221], v[162:165]\n"
        "s_waitcnt lgkmcnt(2)\n"
        "ds_read_b64_tr_b16 v[88:89], v187 offset:6152\n"
        "ds_read_b64_tr_b16 v[90:91], v187 offset:9224\n"
        "v_mfma_f32_16x16x32_bf16 v[142:145], v[80:83], v[218:221], 0\n"
        "s_barrier\n"
        "ds_read_b64_tr_b16 v[178:179], v187 offset:12296\n"
        "ds_read_b64_tr_b16 v[180:181], v187 offset:15368\n"
        "v_mfma_f32_16x16x32_bf16 v[142:145], v[230:233], v[214:217], v[142:145]\n"
        "v_cvt_pk_bf16_f32 v226, v234, v235\n"
        "v_cvt_pk_bf16_f32 v227, v236, v237\n"
        "v_mfma_f32_16x16x32_bf16 v[142:145], v[48:51], v[210:213], v[142:145]\n"
        "s_nop 0\n"
        "v_cvt_pk_bf16_f32 v228, v162, v163\n"
        "v_cvt_pk_bf16_f32 v229, v164, v165\n"
        "v_mfma_f32_16x16x32_bf16 v[234:237], v[48:51], v[206:209], 0\n"
        "s_nop 0\n"
        "ds_write_b128 v188, v[226:229]\n"
        "v_mfma_f32_16x16x32_bf16 v[234:237], v[230:233], v[202:205], v[234:237]\n"
        "s_nop 0\n"
        "v_cvt_pk_bf16_f32 v48, v142, v143\n"
        "v_cvt_pk_bf16_f32 v49, v144, v145\n"
        "v_mfma_f32_16x16x32_bf16 v[234:237], v[80:83], v[194:197], v[234:237]\n"
        "v_cndmask_b32_dpp v254, v254, v36, vcc row_ror:4 row_mask:0xf bank_mask:0xf\n"
        "v_cndmask_b32_dpp v252, v252, v37, vcc row_ror:4 row_mask:0xf bank_mask:0xf\n"
        "v_mfma_f32_16x16x32_bf16 v[34:37], a[176:179], v[218:221], 0\n"
        "s_nop 0\n"
        "v_mul_f32_dpp v251, v157, v251 quad_perm:[0,0,0,0] row_mask:0xf bank_mask:0xf\n"
        "v_mul_f32_dpp v253, v157, v253 quad_perm:[1,1,1,1] row_mask:0xf bank_mask:0xf\n"
        "v_mfma_f32_16x16x32_bf16 v[34:37], a[172:175], v[214:217], v[34:37]\n"
        "s_waitcnt lgkmcnt(1)\n"
        "v_cvt_pk_bf16_f32 v142, v234, v235\n"
        "v_cvt_pk_bf16_f32 v143, v236, v237\n"
        "v_mfma_f32_16x16x32_bf16 v[34:37], a[168:171], v[210:213], v[34:37]\n"
        "v_mul_f32_dpp v254, v157, v254 quad_perm:[2,2,2,2] row_mask:0xf bank_mask:0xf\n"
        "v_mul_f32_dpp v252, v157, v252 quad_perm:[3,3,3,3] row_mask:0xf bank_mask:0xf\n"
        "v_mfma_f32_16x16x32_bf16 v[174:177], a[180:183], v[210:213], 0\n"
        "v_max3_f32 v51, v251, v253, v254\n"
        "v_max_f32 v51, v51, v252\n"
        "v_mfma_f32_16x16x32_bf16 v[174:177], a[184:187], v[214:217], v[174:177]\n"
        "ds_read_b64_tr_b16 v[182:183], v187 offset:1024\n"
        "v_mul_f32 v51, v51, v157\n"
        "v_mfma_f32_16x16x32_bf16 v[174:177], a[188:191], v[218:221], v[174:177]\n"
        "ds_read_b64_tr_b16 v[184:185], v187 offset:4096\n"
        "v_fma_f32 v251, v251, v157, -v51\n"
        "v_mfma_f32_16x16x32_bf16 v[80:83], v[178:181], v[218:221], 0\n"
        "v_fma_f32 v253, v253, v157, -v51\n"
        "v_fma_f32 v254, v254, v157, -v51\n"
        "v_mfma_f32_16x16x32_bf16 v[80:83], v[88:91], v[214:217], v[80:83]\n"
        "ds_read_b64_tr_b16 v[162:163], v187 offset:7168\n"
        "v_fma_f32 v252, v252, v157, -v51\n"
        "v_mfma_f32_16x16x32_bf16 v[80:83], v[166:169], v[210:213], v[80:83]\n"
        "ds_read_b64_tr_b16 v[164:165], v187 offset:10240\n"
        "v_cvt_pk_bf16_f32 v230, v34, v35\n"
        "v_mfma_f32_16x16x32_bf16 v[234:237], v[166:169], v[206:209], 0\n"
        "ds_read_b64_tr_b16 v[166:167], v187 offset:13312\n"
        "v_cvt_pk_bf16_f32 v231, v36, v37\n"
        "v_mfma_f32_16x16x32_bf16 v[234:237], v[88:91], v[202:205], v[234:237]\n"
        "ds_read_b64_tr_b16 v[168:169], v187 offset:16384\n"
        "v_cvt_pk_bf16_f32 v232, v174, v175\n"
        "v_mfma_f32_16x16x32_bf16 v[234:237], v[178:181], v[194:197], v[234:237]\n"
        "v_exp_f32 v251, v251\n"
        "v_mfma_f32_16x16x32_bf16 v[34:37], a[200:203], v[218:221], 0\n"
        "v_exp_f32 v253, v253\n"
        "v_mfma_f32_16x16x32_bf16 v[34:37], a[196:199], v[214:217], v[34:37]\n"
        "v_exp_f32 v254, v254\n"
        "v_mfma_f32_16x16x32_bf16 v[34:37], a[192:195], v[210:213], v[34:37]\n"
        "v_exp_f32 v252, v252\n"
        "v_mfma_f32_16x16x32_bf16 v[178:181], a[204:207], v[210:213], 0\n"
        "v_add_f32 v90, v251, v253\n"
        "v_add_f32 v89, v254, v252\n"
        "v_mfma_f32_16x16x32_bf16 v[178:181], a[208:211], v[214:217], v[178:181]\n"
        "v_add_f32 v90, v90, v89\n"
        "v_cvt_pk_bf16_f32 v233, v176, v177\n"
        "v_mfma_f32_16x16x32_bf16 v[178:181], a[212:215], v[218:221], v[178:181]\n"
        "v_rcp_f32 v90, v90\n"
        "s_waitcnt lgkmcnt(0)\n"
        "v_mfma_f32_16x16x32_bf16 v[242:245], v[166:169], v[218:221], 0\n"
        "v_mul_f32 v251, v251, v90\n"
        "v_mul_f32 v253, v253, v90\n"
        "v_mfma_f32_16x16x32_bf16 v[242:245], v[162:165], v[214:217], v[242:245]\n"
        "ds_read_b64_tr_b16 v[238:239], v187 offset:1032\n"
        "v_mul_f32 v254, v254, v90\n"
        "v_mfma_f32_16x16x32_bf16 v[242:245], v[182:185], v[210:213], v[242:245]\n"
        "ds_read_b64_tr_b16 v[240:241], v187 offset:4104\n"
        "v_mul_f32 v252, v252, v90\n"
        "v_mfma_f32_16x16x32_bf16 v[174:177], v[182:185], v[206:209], 0\n"
        "ds_read_b64_tr_b16 v[246:247], v187 offset:7176\n"
        "v_mul_f32_dpp v251, v157, v251 quad_perm:[0,0,0,0] row_mask:0xf bank_mask:0xf\n"
        "v_mfma_f32_16x16x32_bf16 v[174:177], v[162:165], v[202:205], v[174:177]\n"
        "ds_read_b64_tr_b16 v[248:249], v187 offset:10248\n"
        "v_mul_f32_dpp v253, v157, v253 quad_perm:[1,1,1,1] row_mask:0xf bank_mask:0xf\n"
        "v_mfma_f32_16x16x32_bf16 v[174:177], v[166:169], v[194:197], v[174:177]\n"
        "ds_read_b64_tr_b16 v[182:183], v187 offset:13320\n"
        "v_mul_f32_dpp v254, v157, v254 quad_perm:[2,2,2,2] row_mask:0xf bank_mask:0xf\n"
        "v_mfma_f32_16x16x32_bf16 v[170:173], v[138:141], a[104:107], 0\n"
        "ds_read_b64_tr_b16 v[184:185], v187 offset:16392\n"
        "v_mul_f32_dpp v252, v157, v252 quad_perm:[3,3,3,3] row_mask:0xf bank_mask:0xf\n"
        "v_mfma_f32_16x16x32_bf16 v[170:173], v[132:135], a[100:103], v[170:173]\n"
        "ds_write_b128 v188, v[230:233] offset:64\n"
        "v_mfma_f32_16x16x32_bf16 v[170:173], v[126:129], a[96:99], v[170:173]\n"
        "v_cvt_pk_bf16_f32 v250, v251, v253\n"
        "v_cvt_pk_bf16_f32 v157, v254, v252\n"
        "v_mfma_f32_16x16x32_bf16 v[88:91], v[126:129], a[108:111], 0\n"
        "v_and_b32 v124, v25, v250\n"
        "v_and_b32 v125, v25, v157\n"
        "v_mfma_f32_16x16x32_bf16 v[88:91], v[132:135], a[112:115], v[88:91]\n"
        "v_mov_b64 v[130:131], v[124:125]\n"
        "v_cvt_pk_bf16_f32 v50, v80, v81\n"
        "v_mfma_f32_16x16x32_bf16 v[88:91], v[138:141], a[116:119], v[88:91]\n"
        "v_cvt_pk_bf16_f32 v51, v82, v83\n"
        "v_cvt_pk_bf16_f32 v144, v234, v235\n"
        "v_mfma_f32_16x16x32_bf16 v[166:169], v[42:45], v[124:127], 0\n"
        "ds_write_b128 v188, v[48:51] offset:192\n"
        "v_mfma_f32_16x16x32_bf16 v[80:83], v[44:47], v[128:131], 0\n"
        "s_waitcnt lgkmcnt(2)\n"
        "v_cvt_pk_bf16_f32 v145, v236, v237\n"
        "v_cvt_pk_bf16_f32 v234, v34, v35\n"
        "v_mfma_f32_16x16x32_bf16 v[48:51], v[182:185], v[218:221], 0\n"
        "ds_write_b128 v189, v[142:145] offset:192\n"
        "v_mfma_f32_16x16x32_bf16 v[48:51], v[246:249], v[214:217], v[48:51]\n"
        "ds_read_b64_tr_b16 v[142:143], v187 offset:2048\n"
        "v_cvt_pk_bf16_f32 v235, v36, v37\n"
        "v_mfma_f32_16x16x32_bf16 v[48:51], v[238:241], v[210:213], v[48:51]\n"
        "ds_read_b64_tr_b16 v[144:145], v187 offset:5120\n"
        "v_cvt_pk_bf16_f32 v236, v178, v179\n"
        "v_mfma_f32_16x16x32_bf16 v[162:165], v[238:241], v[206:209], 0\n"
        "ds_read_b64_tr_b16 v[238:239], v187 offset:8192\n"
        "v_cvt_pk_bf16_f32 v237, v180, v181\n"
        "v_mfma_f32_16x16x32_bf16 v[162:165], v[246:249], v[202:205], v[162:165]\n"
        "ds_write_b128 v188, v[234:237] offset:128\n"
        "v_mfma_f32_16x16x32_bf16 v[162:165], v[182:185], v[194:197], v[162:165]\n"
        "ds_read_b64_tr_b16 v[240:241], v187 offset:11264\n"
        "v_mov_b64 v[136:137], v[124:125]\n"
        "v_mfma_f32_16x16x32_bf16 v[246:249], v[138:141], a[128:131], 0\n"
        "ds_read_b64_tr_b16 v[182:183], v187 offset:14336\n"
        "v_cvt_pk_bf16_f32 v34, v242, v243\n"
        "v_mfma_f32_16x16x32_bf16 v[246:249], v[132:135], a[124:127], v[246:249]\n"
        "ds_read_b64_tr_b16 v[184:185], v187 offset:17408\n"
        "v_cvt_pk_bf16_f32 v35, v244, v245\n"
        "v_mfma_f32_16x16x32_bf16 v[246:249], v[126:129], a[120:123], v[246:249]\n"
        "ds_read_b64_tr_b16 v[178:179], v187 offset:2056\n"
        "v_cvt_pk_bf16_f32 v46, v174, v175\n"
        "v_mfma_f32_16x16x32_bf16 v[242:245], v[126:129], a[132:135], 0\n"
        "ds_read_b64_tr_b16 v[180:181], v187 offset:5128\n"
        "v_cvt_pk_bf16_f32 v47, v176, v177\n"
        "v_mfma_f32_16x16x32_bf16 v[242:245], v[132:135], a[136:139], v[242:245]\n"
        "ds_read_b64_tr_b16 v[126:127], v187 offset:8200\n"
        "v_cvt_pk_bf16_f32 v36, v170, v171\n"
        "v_mfma_f32_16x16x32_bf16 v[242:245], v[138:141], a[140:143], v[242:245]\n"
        "ds_read_b64_tr_b16 v[128:129], v187 offset:11272\n"
        "v_cvt_pk_bf16_f32 v37, v172, v173\n"
        "s_nop 1\n"
        "v_mfma_f32_16x16x32_bf16 v[174:177], v[36:39], v[130:133], 0\n"
        "ds_read_b64_tr_b16 v[170:171], v187 offset:14344\n"
        "v_cvt_pk_bf16_f32 v40, v88, v89\n"
        "v_cvt_pk_bf16_f32 v41, v90, v91\n"
        "s_nop 1\n"
        "v_mfma_f32_16x16x32_bf16 v[88:91], v[38:41], v[134:137], 0\n"
        "ds_read_b64_tr_b16 v[172:173], v187 offset:17416\n"
        "v_cvt_pk_bf16_f32 v36, v48, v49\n"
        "s_waitcnt lgkmcnt(6)\n"
        "v_mfma_f32_16x16x32_bf16 v[132:135], v[182:185], v[218:221], 0\n"
        "v_cvt_pk_bf16_f32 v37, v50, v51\n"
        "v_cvt_pk_bf16_f32 v48, v162, v163\n"
        "v_mfma_f32_16x16x32_bf16 v[132:135], v[238:241], v[214:217], v[132:135]\n"
        "ds_write_b128 v188, v[34:37] offset:256\n"
        "v_mfma_f32_16x16x32_bf16 v[132:135], v[142:145], v[210:213], v[132:135]\n"
        "v_cvt_pk_bf16_f32 v49, v164, v165\n"
        "v_cvt_pk_bf16_f32 v30, v246, v247\n"
        "v_mfma_f32_16x16x32_bf16 v[40:43], v[142:145], v[206:209], 0\n"
        "ds_write_b128 v189, v[46:49] offset:256\n"
        "v_mfma_f32_16x16x32_bf16 v[40:43], v[238:241], v[202:205], v[40:43]\n"
        "v_cvt_pk_bf16_f32 v31, v248, v249\n"
        "v_mov_b64 v[142:143], v[124:125]\n"
        "v_mfma_f32_16x16x32_bf16 v[40:43], v[182:185], v[194:197], v[40:43]\n"
        "s_waitcnt lgkmcnt(2)\n"
        "v_cvt_pk_bf16_f32 v34, v242, v243\n"
        "v_cvt_pk_bf16_f32 v35, v244, v245\n"
        "v_mfma_f32_16x16x32_bf16 v[48:51], v[170:173], v[218:221], 0\n"
        "s_barrier\n"
        "v_cvt_pk_bf16_f32 v164, v132, v133\n"
        "v_cvt_pk_bf16_f32 v165, v134, v135\n"
        "v_mfma_f32_16x16x32_bf16 v[48:51], v[126:129], v[214:217], v[48:51]\n"
        "v_cvt_pk_bf16_f32 v124, v40, v41\n"
        "v_cvt_pk_bf16_f32 v125, v42, v43\n"
        "v_mfma_f32_16x16x32_bf16 v[48:51], v[178:181], v[210:213], v[48:51]\n"
        "v_cvt_pk_bf16_f32 v132, v166, v167\n"
        "v_cvt_pk_bf16_f32 v133, v82, v83\n"
        "v_mfma_f32_16x16x32_bf16 v[40:43], v[178:181], v[206:209], 0\n"
        "s_nop 0\n"
        "v_cvt_pk_bf16_f32 v134, v88, v89\n"
        "v_cvt_pk_bf16_f32 v162, v168, v169\n"
        "v_mfma_f32_16x16x32_bf16 v[40:43], v[126:129], v[202:205], v[40:43]\n"
        "s_nop 0\n"
        "v_cvt_pk_bf16_f32 v166, v48, v49\n"
        "v_cvt_pk_bf16_f32 v167, v50, v51\n"
        "v_mfma_f32_16x16x32_bf16 v[40:43], v[170:173], v[194:197], v[40:43]\n"
        "s_nop 0\n"
        "ds_write_b128 v188, v[164:167] offset:320\n"
        "v_mfma_f32_16x16x32_bf16 v[128:131], v[118:121], a[80:83], 0\n"
        "s_nop 0\n"
        "v_cvt_pk_bf16_f32 v163, v174, v175\n"
        "v_cvt_pk_bf16_f32 v164, v90, v91\n"
        "v_mfma_f32_16x16x32_bf16 v[128:131], v[112:115], a[76:79], v[128:131]\n"
        "s_nop 0\n"
        "v_cvt_pk_bf16_f32 v126, v40, v41\n"
        "v_cvt_pk_bf16_f32 v127, v42, v43\n"
        "v_mfma_f32_16x16x32_bf16 v[128:131], v[106:109], a[72:75], v[128:131]\n"
        "s_nop 0\n"
        "ds_read_b128 v[88:91], v27 offset:64\n"
        "ds_read_b128 v[48:51], v27 offset:128\n"
        "v_mfma_f32_16x16x32_bf16 v[178:181], v[106:109], a[84:87], 0\n"
        "s_nop 0\n"
        "ds_write_b128 v189, v[124:127] offset:320\n"
        "v_mfma_f32_16x16x32_bf16 v[178:181], v[112:115], a[88:91], v[178:181]\n"
        "s_nop 0\n"
        "v_cvt_pk_bf16_f32 v42, v128, v129\n"
        "v_cvt_pk_bf16_f32 v43, v130, v131\n"
        "v_mfma_f32_16x16x32_bf16 v[178:181], v[118:121], a[92:95], v[178:181]\n"
        "v_cvt_pk_bf16_f32 v166, v80, v81\n"
        "v_cvt_pk_bf16_f32 v167, v176, v177\n"
        "v_mfma_f32_16x16x32_bf16 v[80:83], v[30:33], v[136:139], 0\n"
        "s_nop 0\n"
        "v_cvt_f32_f16_sdwa v188, v222 dst_sel:DWORD dst_unused:UNUSED_PAD src0_sel:WORD_0\n"
        "v_lshrrev_b32 v41, 0, v222\n"
        "v_mfma_f32_16x16x32_bf16 v[126:129], v[32:35], v[140:143], 0\n"
        "s_nop 0\n"
        "v_cvt_pk_bf16_f32 v46, v178, v179\n"
        "v_cvt_pk_bf16_f32 v47, v180, v181\n"
        "v_mfma_f32_16x16x32_bf16 v[138:141], v[210:213], v[226:229], 0\n"
        "v_cvt_pk_bf16_f32 v135, v82, v83\n"
        "v_cvt_pk_bf16_f32 v168, v80, v81\n"
        "v_mfma_f32_16x16x32_bf16 v[138:141], v[214:217], v[230:233], v[138:141]\n"
        "s_waitcnt lgkmcnt(1)\n"
        "ds_write_b128 v12, v[132:135]\n"
        "v_mfma_f32_16x16x32_bf16 v[138:141], v[218:221], v[234:237], v[138:141]\n"
        "v_cvt_pk_bf16_f32 v165, v126, v127\n"
        "v_cvt_pk_bf16_f32 v169, v128, v129\n"
        "v_mfma_f32_16x16x32_bf16 v[230:233], v[218:221], a[224:227], 0\n"
        "ds_write_b128 v12, v[162:165] offset:1024\n"
        "v_mfma_f32_16x16x32_bf16 v[230:233], v[214:217], a[220:223], v[230:233]\n"
        "ds_write_b128 v12, v[166:169] offset:2048\n"
        "v_mfma_f32_16x16x32_bf16 v[230:233], v[210:213], a[216:219], v[230:233]\n"
        "s_nop 0\n"
        "v_cndmask_b32_e64 v40, v138, v139, s[56:57]\n"
        "v_cndmask_b32_e64 v40, v40, v140, s[58:59]\n"
        "v_mfma_f32_16x16x32_bf16 v[226:229], v[210:213], a[228:231], 0\n"
        "v_cndmask_b32_e64 v40, v40, v141, s[60:61]\n"
        "v_mul_f32 v40, v40, v188\n"
        "v_mfma_f32_16x16x32_bf16 v[226:229], v[214:217], a[232:235], v[226:229]\n"
        "v_mul_f32 v40, v40, v188\n"
        "v_and_b32_dpp v104, v250, v25 row_ror:12 row_mask:0xf bank_mask:0xf\n"
        "v_mfma_f32_16x16x32_bf16 v[226:229], v[218:221], a[236:239], v[226:229]\n"
        "v_exp_f32 v40, v40\n"
        "v_mfma_f32_16x16x32_bf16 v[140:143], v[210:213], v[88:91], 0\n"
        "v_and_b32_dpp v105, v157, v25 row_ror:12 row_mask:0xf bank_mask:0xf\n"
        "v_cvt_pk_f16_f32 v40, v40, 0\n"
        "v_mfma_f32_16x16x32_bf16 v[34:37], v[210:213], v[48:51], 0\n"
        "ds_write_b64 v190, v[40:41]\n"
        "v_mfma_f32_16x16x32_bf16 v[238:241], v[118:121], a[104:107], 0\n"
        "v_mov_b64 v[110:111], v[104:105]\n"
        "v_cvt_f32_f16_sdwa v188, v198 dst_sel:DWORD dst_unused:UNUSED_PAD src0_sel:WORD_0\n"
        "v_mfma_f32_16x16x32_bf16 v[238:241], v[112:115], a[100:103], v[238:241]\n"
        "v_mul_f32 v230, v230, v188\n"
        "v_mul_f32 v226, v226, v188\n"
        "v_mfma_f32_16x16x32_bf16 v[238:241], v[106:109], a[96:99], v[238:241]\n"
        "v_cvt_pk_f16_f32 v230, v230, v226\n"
        "v_cvt_f32_f16_sdwa v190, v198 dst_sel:DWORD dst_unused:UNUSED_PAD src0_sel:WORD_1\n"
        "v_mfma_f32_16x16x32_bf16 v[182:185], v[106:109], a[108:111], 0\n"
        "v_mul_f32 v231, v231, v190\n"
        "v_mul_f32 v227, v227, v190\n"
        "v_mfma_f32_16x16x32_bf16 v[182:185], v[112:115], a[112:115], v[182:185]\n"
        "v_cvt_pk_f16_f32 v231, v231, v227\n"
        "v_perm_b32 v82, v230, v140, s54\n"
        "v_mfma_f32_16x16x32_bf16 v[182:185], v[118:121], a[116:119], v[182:185]\n"
        "v_perm_b32 v226, v231, v141, s54\n"
        "v_cvt_f32_f16_sdwa v245, v199 dst_sel:DWORD dst_unused:UNUSED_PAD src0_sel:WORD_0\n"
        "v_mfma_f32_16x16x32_bf16 v[138:141], v[42:45], v[104:107], 0\n"
        "v_mul_f32 v232, v232, v245\n"
        "v_mul_f32 v228, v228, v245\n"
        "v_mfma_f32_16x16x32_bf16 v[126:129], v[44:47], v[108:111], 0\n"
        "v_cvt_pk_f16_f32 v232, v232, v228\n"
        "v_perm_b32 v83, v230, v34, s55\n"
        "v_mfma_f32_16x16x32_bf16 v[178:181], v[218:221], a[248:251], 0\n"
        "ds_write_b64 v192, v[82:83]\n"
        "v_mfma_f32_16x16x32_bf16 v[178:181], v[214:217], a[244:247], v[178:181]\n"
        "v_perm_b32 v34, v232, v142, s54\n"
        "v_perm_b32 v227, v231, v35, s55\n"
        "v_mfma_f32_16x16x32_bf16 v[178:181], v[210:213], a[240:243], v[178:181]\n"
        "ds_write_b64 v192, v[226:227] offset:6336\n"
        "v_mfma_f32_16x16x32_bf16 v[234:237], v[210:213], a[252:255], 0\n"
        "v_perm_b32 v35, v232, v36, s55\n"
        "v_cvt_f32_f16_sdwa v187, v199 dst_sel:DWORD dst_unused:UNUSED_PAD src0_sel:WORD_1\n"
        "v_mfma_f32_16x16x32_bf16 v[234:237], v[214:217], v[72:75], v[234:237]\n"
        "ds_write_b64 v192, v[34:35] offset:12672\n"
        "v_mfma_f32_16x16x32_bf16 v[234:237], v[218:221], v[64:67], v[234:237]\n"
        "v_mul_f32 v233, v233, v187\n"
        "v_mul_f32 v229, v229, v187\n"
        "v_mfma_f32_16x16x32_bf16 v[174:177], v[214:217], v[88:91], 0\n"
        "v_cvt_pk_f16_f32 v233, v233, v229\n"
        "v_perm_b32 v198, v233, v143, s54\n"
        "v_mfma_f32_16x16x32_bf16 v[80:83], v[214:217], v[48:51], 0\n"
        "v_perm_b32 v199, v233, v37, s55\n"
        "v_cvt_pk_bf16_f32 v36, v238, v239\n"
        "v_mfma_f32_16x16x32_bf16 v[226:229], v[132:135], v[132:135], 0\n"
        "ds_write_b64 v192, v[198:199] offset:19008\n"
        "v_mfma_f32_16x16x32_bf16 v[226:229], v[162:165], v[162:165], v[226:229]\n"
        "v_cvt_pk_bf16_f32 v37, v240, v241\n"
        "v_cvt_pk_bf16_f32 v40, v182, v183\n"
        "v_mfma_f32_16x16x32_bf16 v[226:229], v[166:169], v[166:169], v[226:229]\n"
        "v_cvt_pk_bf16_f32 v41, v184, v185\n"
        "v_mul_f32 v178, v178, v188\n"
        "v_mfma_f32_16x16x32_bf16 v[230:233], a[152:155], v[194:197], 0\n"
        "v_mul_f32 v234, v234, v188\n"
        "v_mul_f32 v179, v179, v190\n"
        "v_mfma_f32_16x16x32_bf16 v[230:233], a[148:151], v[202:205], v[230:233]\n"
        "v_mul_f32 v235, v235, v190\n"
        "v_cvt_pk_f16_f32 v178, v178, v234\n"
        "v_mfma_f32_16x16x32_bf16 v[230:233], a[144:147], v[206:209], v[230:233]\n"
        "v_cvt_pk_f16_f32 v179, v179, v235\n"
        "v_mul_f32 v180, v180, v245\n"
        "v_mfma_f32_16x16x32_bf16 v[166:169], a[156:159], v[206:209], 0\n"
        "v_mul_f32 v236, v236, v245\n"
        "v_mov_b64 v[116:117], v[104:105]\n"
        "v_mfma_f32_16x16x32_bf16 v[166:169], a[160:163], v[202:205], v[166:169]\n"
        "v_cvt_pk_f16_f32 v180, v180, v236\n"
        "v_perm_b32 v198, v178, v174, s54\n"
        "v_mfma_f32_16x16x32_bf16 v[166:169], a[164:167], v[194:197], v[166:169]\n"
        "v_perm_b32 v134, v179, v175, s54\n"
        "v_perm_b32 v132, v180, v176, s54\n"
        "v_mfma_f32_16x16x32_bf16 v[182:185], v[118:121], a[128:131], 0\n"
        "v_mul_f32 v181, v181, v187\n"
        "v_mul_f32 v237, v237, v187\n"
        "v_mfma_f32_16x16x32_bf16 v[182:185], v[112:115], a[124:127], v[182:185]\n"
        "v_cvt_pk_f16_f32 v181, v181, v237\n"
        "v_perm_b32 v199, v178, v80, s55\n"
        "v_mfma_f32_16x16x32_bf16 v[182:185], v[106:109], a[120:123], v[182:185]\n"
        "ds_write_b64 v192, v[198:199] offset:8\n"
        "v_mfma_f32_16x16x32_bf16 v[142:145], v[106:109], a[132:135], 0\n"
        "v_perm_b32 v106, v181, v177, s54\n"
        "v_perm_b32 v135, v179, v81, s55\n"
        "v_mfma_f32_16x16x32_bf16 v[142:145], v[112:115], a[136:139], v[142:145]\n"
        "ds_write_b64 v192, v[134:135] offset:6344\n"
        "v_mfma_f32_16x16x32_bf16 v[142:145], v[118:121], a[140:143], v[142:145]\n"
        "v_perm_b32 v133, v180, v82, s55\n"
        "v_perm_b32 v107, v181, v83, s55\n"
        "v_mfma_f32_16x16x32_bf16 v[234:237], v[36:39], v[110:113], 0\n"
        "ds_write_b64 v192, v[132:133] offset:12680\n"
        "v_mfma_f32_16x16x32_bf16 v[162:165], v[38:41], v[114:117], 0\n"
        "ds_write_b64 v192, v[106:107] offset:19016\n"
        "v_mfma_f32_16x16x32_bf16 v[132:135], v[218:221], v[56:59], 0\n"
        "v_cvt_pk_bf16_f32 v112, v230, v231\n"
        "v_cvt_pk_bf16_f32 v113, v232, v233\n"
        "v_mfma_f32_16x16x32_bf16 v[132:135], v[214:217], v[60:63], v[132:135]\n"
        "v_cvt_pk_bf16_f32 v114, v166, v167\n"
        "v_cvt_pk_bf16_f32 v115, v168, v169\n"
        "v_mfma_f32_16x16x32_bf16 v[132:135], v[210:213], v[68:71], v[132:135]\n"
        "v_cvt_pk_bf16_f32 v30, v182, v183\n"
        "v_cvt_pk_bf16_f32 v31, v184, v185\n"
        "v_mfma_f32_16x16x32_bf16 v[80:83], v[210:213], v[52:55], 0\n"
        "v_cvt_pk_bf16_f32 v34, v142, v143\n"
        "v_cvt_pk_bf16_f32 v35, v144, v145\n"
        "v_mfma_f32_16x16x32_bf16 v[80:83], v[214:217], v[76:79], v[80:83]\n"
        "s_nop 0\n"
        "v_mov_b64 v[122:123], v[104:105]\n"
        "v_mul_f32 v132, v132, v188\n"
        "v_mfma_f32_16x16x32_bf16 v[80:83], v[218:221], v[84:87], v[80:83]\n"
        "v_mul_f32 v133, v133, v190\n"
        "v_cvt_pk_bf16_f32 v106, v138, v139\n"
        "v_mfma_f32_16x16x32_bf16 v[230:233], v[218:221], v[88:91], 0\n"
        "s_nop 0\n"
        "v_cvt_pk_bf16_f32 v107, v128, v129\n"
        "v_cvt_pk_bf16_f32 v108, v162, v163\n"
        "v_mfma_f32_16x16x32_bf16 v[128:131], v[218:221], v[48:51], 0\n"
        "s_nop 0\n"
        "v_mul_f32 v80, v80, v188\n"
        "v_cvt_pk_f16_f32 v132, v132, v80\n"
        "v_mfma_f32_16x16x32_bf16 v[210:213], v[158:161], a[80:83], 0\n"
        "v_perm_b32 v198, v132, v230, s54\n"
        "v_mul_f32 v81, v81, v190\n"
        "v_mfma_f32_16x16x32_bf16 v[210:213], v[152:155], a[76:79], v[210:213]\n"
        "v_cvt_pk_f16_f32 v133, v133, v81\n"
        "v_perm_b32 v138, v133, v231, s54\n"
        "v_mfma_f32_16x16x32_bf16 v[210:213], v[146:149], a[72:75], v[210:213]\n"
        "v_perm_b32 v199, v132, v128, s55\n"
        "v_perm_b32 v139, v133, v129, s55\n"
        "v_mfma_f32_16x16x32_bf16 v[218:221], v[146:149], a[84:87], 0\n"
        "ds_write_b64 v192, v[198:199] offset:16\n"
        "v_mfma_f32_16x16x32_bf16 v[218:221], v[152:155], a[88:91], v[218:221]\n"
        "ds_write_b64 v192, v[138:139] offset:6352\n"
        "v_mfma_f32_16x16x32_bf16 v[218:221], v[158:161], a[92:95], v[218:221]\n"
        "v_mul_f32 v134, v134, v245\n"
        "v_mul_f32 v82, v82, v245\n"
        "v_mfma_f32_16x16x32_bf16 v[178:181], v[30:33], v[116:119], 0\n"
        "v_cvt_pk_f16_f32 v134, v134, v82\n"
        "v_perm_b32 v118, v134, v232, s54\n"
        "v_mfma_f32_16x16x32_bf16 v[214:217], v[32:35], v[120:123], 0\n"
        "v_perm_b32 v119, v134, v130, s55\n"
        "v_cvt_pk_bf16_f32 v80, v140, v141\n"
        "v_mfma_f32_16x16x32_bf16 v[138:141], a[176:179], v[194:197], 0\n"
        "ds_write_b64 v192, v[118:119] offset:12688\n"
        "v_mfma_f32_16x16x32_bf16 v[138:141], a[172:175], v[202:205], v[138:141]\n"
        "v_cvt_pk_bf16_f32 v81, v234, v235\n"
        "v_cvt_pk_bf16_f32 v82, v164, v165\n"
        "v_mfma_f32_16x16x32_bf16 v[138:141], a[168:171], v[206:209], v[138:141]\n"
        "v_mul_f32 v135, v135, v187\n"
        "v_mul_f32 v83, v83, v187\n"
        "v_mfma_f32_16x16x32_bf16 v[34:37], a[180:183], v[206:209], 0\n"
        "v_cvt_pk_f16_f32 v135, v135, v83\n"
        "v_perm_b32 v118, v135, v233, s54\n"
        "v_mfma_f32_16x16x32_bf16 v[34:37], a[184:187], v[202:205], v[34:37]\n"
        "v_perm_b32 v119, v135, v131, s55\n"
        "v_and_b32_dpp v144, v250, v25 row_ror:8 row_mask:0xf bank_mask:0xf\n"
        "v_mfma_f32_16x16x32_bf16 v[34:37], a[188:191], v[194:197], v[34:37]\n"
        "ds_write_b64 v192, v[118:119] offset:19024\n"
        "v_mfma_f32_16x16x32_bf16 v[230:233], v[158:161], a[104:107], 0\n"
        "v_and_b32_dpp v145, v157, v25 row_ror:8 row_mask:0xf bank_mask:0xf\n"
        "v_cvt_pk_bf16_f32 v166, v126, v127\n"
        "v_mfma_f32_16x16x32_bf16 v[230:233], v[152:155], a[100:103], v[230:233]\n"
        "v_cvt_pk_bf16_f32 v167, v236, v237\n"
        "v_mov_b64 v[150:151], v[144:145]\n"
        "v_mfma_f32_16x16x32_bf16 v[230:233], v[146:149], a[96:99], v[230:233]\n"
        "v_cvt_pk_bf16_f32 v42, v210, v211\n"
        "v_cvt_pk_bf16_f32 v43, v212, v213\n"
        "v_mfma_f32_16x16x32_bf16 v[210:213], v[146:149], a[108:111], 0\n"
        "v_cvt_pk_bf16_f32 v46, v218, v219\n"
        "v_cvt_pk_bf16_f32 v47, v220, v221\n"
        "v_mfma_f32_16x16x32_bf16 v[210:213], v[152:155], a[112:115], v[210:213]\n"
        "v_cvt_pk_bf16_f32 v168, v178, v179\n"
        "v_cvt_pk_bf16_f32 v109, v180, v181\n"
        "v_mfma_f32_16x16x32_bf16 v[210:213], v[158:161], a[116:119], v[210:213]\n"
        "ds_write_b128 v12, v[106:109] offset:3072\n"
        "v_mfma_f32_16x16x32_bf16 v[118:121], v[42:45], v[144:147], 0\n"
        "v_cvt_pk_bf16_f32 v83, v214, v215\n"
        "v_cvt_pk_bf16_f32 v169, v216, v217\n"
        "v_mfma_f32_16x16x32_bf16 v[132:135], v[44:47], v[148:151], 0\n"
        "ds_write_b128 v12, v[80:83] offset:4096\n"
        "v_mfma_f32_16x16x32_bf16 v[126:129], v[194:197], a[224:227], 0\n"
        "ds_write_b128 v12, v[166:169] offset:5120\n"
        "v_mfma_f32_16x16x32_bf16 v[126:129], v[202:205], a[220:223], v[126:129]\n"
        "v_cvt_pk_bf16_f32 v218, v138, v139\n"
        "v_cvt_pk_bf16_f32 v219, v140, v141\n"
        "v_mfma_f32_16x16x32_bf16 v[126:129], v[206:209], a[216:219], v[126:129]\n"
        "v_cvt_pk_bf16_f32 v220, v34, v35\n"
        "v_cvt_pk_bf16_f32 v221, v36, v37\n"
        "v_mfma_f32_16x16x32_bf16 v[140:143], v[206:209], a[228:231], 0\n"
        "v_cvt_pk_bf16_f32 v36, v230, v231\n"
        "v_cvt_pk_bf16_f32 v37, v232, v233\n"
        "v_mfma_f32_16x16x32_bf16 v[140:143], v[202:205], a[232:235], v[140:143]\n"
        "v_cvt_pk_bf16_f32 v40, v210, v211\n"
        "v_cvt_pk_bf16_f32 v41, v212, v213\n"
        "v_mfma_f32_16x16x32_bf16 v[140:143], v[194:197], a[236:239], v[140:143]\n"
        "v_mov_b64 v[156:157], v[144:145]\n"
        "v_cvt_f32_f16_sdwa v187, v200 dst_sel:DWORD dst_unused:UNUSED_PAD src0_sel:WORD_0\n"
        "v_mfma_f32_16x16x32_bf16 v[230:233], v[206:209], v[88:91], 0\n"
        "s_nop 0\n"
        "v_mul_f32 v126, v126, v187\n"
        "v_cvt_f32_f16_sdwa v190, v200 dst_sel:DWORD dst_unused:UNUSED_PAD src0_sel:WORD_1\n"
        "v_mfma_f32_16x16x32_bf16 v[214:217], v[206:209], v[48:51], 0\n"
        "s_nop 0\n"
        "v_mul_f32 v140, v140, v187\n"
        "v_cvt_pk_f16_f32 v126, v126, v140\n"
        "v_mfma_f32_16x16x32_bf16 v[210:213], v[106:109], v[106:109], 0\n"
        "v_perm_b32 v106, v126, v230, s54\n"
        "v_mul_f32 v127, v127, v190\n"
        "v_mfma_f32_16x16x32_bf16 v[210:213], v[80:83], v[80:83], v[210:213]\n"
        "s_nop 0\n"
        "v_perm_b32 v107, v126, v214, s55\n"
        "v_mul_f32 v141, v141, v190\n"
        "v_mfma_f32_16x16x32_bf16 v[210:213], v[166:169], v[166:169], v[210:213]\n"
        "ds_write_b64 v192, v[106:107] offset:25344\n"
        "v_mfma_f32_16x16x32_bf16 v[80:83], v[158:161], a[128:131], 0\n"
        "ds_write_b128 v189, v[112:115]\n"
        "v_mfma_f32_16x16x32_bf16 v[80:83], v[152:155], a[124:127], v[80:83]\n"
        "v_cvt_pk_f16_f32 v127, v127, v141\n"
        "v_perm_b32 v140, v127, v231, s54\n"
        "v_mfma_f32_16x16x32_bf16 v[80:83], v[146:149], a[120:123], v[80:83]\n"
        "ds_write_b128 v189, v[218:221] offset:64\n"
        "v_mfma_f32_16x16x32_bf16 v[106:109], v[146:149], a[132:135], 0\n"
        "v_perm_b32 v141, v127, v215, s55\n"
        "v_cvt_f32_f16_sdwa v188, v201 dst_sel:DWORD dst_unused:UNUSED_PAD src0_sel:WORD_0\n"
        "v_mfma_f32_16x16x32_bf16 v[106:109], v[152:155], a[136:139], v[106:109]\n"
        "ds_write_b64 v192, v[140:141] offset:31680\n"
        "v_mfma_f32_16x16x32_bf16 v[106:109], v[158:161], a[140:143], v[106:109]\n"
        "v_mul_f32 v128, v128, v188\n"
        "v_mul_f32 v142, v142, v188\n"
        "v_mfma_f32_16x16x32_bf16 v[138:141], v[36:39], v[150:153], 0\n"
        "v_cvt_pk_f16_f32 v128, v128, v142\n"
        "v_perm_b32 v152, v128, v232, s54\n"
        "v_mfma_f32_16x16x32_bf16 v[230:233], v[38:41], v[154:157], 0\n"
        "v_perm_b32 v153, v128, v216, s55\n"
        "v_cvt_pk_bf16_f32 v30, v80, v81\n"
        "v_mfma_f32_16x16x32_bf16 v[146:149], a[200:203], v[194:197], 0\n"
        "ds_write_b64 v193, v[152:153]\n"
        "v_mfma_f32_16x16x32_bf16 v[146:149], a[196:199], v[202:205], v[146:149]\n"
        "v_cvt_pk_bf16_f32 v31, v82, v83\n"
        "v_cvt_pk_bf16_f32 v34, v106, v107\n"
        "v_mfma_f32_16x16x32_bf16 v[146:149], a[192:195], v[206:209], v[146:149]\n"
        "v_cvt_pk_bf16_f32 v35, v108, v109\n"
        "v_mov_b64 v[162:163], v[144:145]\n"
        "v_mfma_f32_16x16x32_bf16 v[126:129], a[204:207], v[206:209], 0\n"
        "s_nop 0\n"
        "v_cvt_pk_bf16_f32 v106, v118, v119\n"
        "v_cvt_pk_bf16_f32 v107, v134, v135\n"
        "v_mfma_f32_16x16x32_bf16 v[126:129], a[208:211], v[202:205], v[126:129]\n"
        "s_nop 0\n"
        "v_cvt_pk_bf16_f32 v152, v146, v147\n"
        "v_cvt_pk_bf16_f32 v153, v148, v149\n"
        "v_mfma_f32_16x16x32_bf16 v[126:129], a[212:215], v[194:197], v[126:129]\n"
        "v_cvt_pk_bf16_f32 v108, v230, v231\n"
        "v_cvt_pk_bf16_f32 v198, v120, v121\n"
        "v_mfma_f32_16x16x32_bf16 v[146:149], v[194:197], a[248:251], 0\n"
        "s_nop 0\n"
        "v_cvt_pk_bf16_f32 v199, v138, v139\n"
        "v_cvt_pk_bf16_f32 v200, v232, v233\n"
        "v_mfma_f32_16x16x32_bf16 v[146:149], v[202:205], a[244:247], v[146:149]\n"
        "s_nop 0\n"
        "v_cvt_pk_bf16_f32 v154, v126, v127\n"
        "v_cvt_pk_bf16_f32 v155, v128, v129\n"
        "v_mfma_f32_16x16x32_bf16 v[146:149], v[206:209], a[240:243], v[146:149]\n"
        "s_nop 0\n"
        "ds_write_b128 v189, v[152:155] offset:128\n"
        "v_mfma_f32_16x16x32_bf16 v[120:123], v[206:209], a[252:255], 0\n"
        "s_nop 0\n"
        "v_cvt_pk_bf16_f32 v118, v132, v133\n"
        "v_cvt_pk_bf16_f32 v119, v140, v141\n"
        "v_mfma_f32_16x16x32_bf16 v[120:123], v[202:205], v[72:75], v[120:123]\n"
        "s_nop 0\n"
        "v_mul_f32 v146, v146, v187\n"
        "v_mul_f32 v147, v147, v190\n"
        "v_mfma_f32_16x16x32_bf16 v[120:123], v[194:197], v[64:67], v[120:123]\n"
        "v_mul_f32 v148, v148, v188\n"
        "v_fmac_f32_dpp v186, v255, v251 quad_perm:[0,0,0,0] row_mask:0xf bank_mask:0xf\n"
        "v_mfma_f32_16x16x32_bf16 v[140:143], v[202:205], v[88:91], 0\n"
        "s_nop 0\n"
        "v_cndmask_b32_e64 v189, v210, v211, s[56:57]\n"
        "v_fmac_f32_dpp v186, v255, v253 quad_perm:[1,1,1,1] row_mask:0xf bank_mask:0xf\n"
        "v_mfma_f32_16x16x32_bf16 v[80:83], v[202:205], v[48:51], 0\n"
        "s_nop 0\n"
        "v_mul_f32 v120, v120, v187\n"
        "v_cvt_pk_f16_f32 v146, v146, v120\n"
        "v_mfma_f32_16x16x32_bf16 v[126:129], v[30:33], v[156:159], 0\n"
        "v_perm_b32 v210, v146, v140, s54\n"
        "v_mul_f32 v121, v121, v190\n"
        "v_mfma_f32_16x16x32_bf16 v[132:135], v[32:35], v[160:163], 0\n"
        "s_nop 0\n"
        "v_perm_b32 v211, v146, v80, s55\n"
        "v_cvt_pk_f16_f32 v147, v147, v121\n"
        "v_mfma_f32_16x16x32_bf16 v[158:161], v[194:197], v[56:59], 0\n"
        "ds_write_b64 v192, v[210:211] offset:25352\n"
        "v_mfma_f32_16x16x32_bf16 v[158:161], v[202:205], v[60:63], v[158:161]\n"
        "v_cvt_pk_bf16_f32 v109, v128, v129\n"
        "v_cvt_pk_bf16_f32 v120, v126, v127\n"
        "v_mfma_f32_16x16x32_bf16 v[158:161], v[206:209], v[68:71], v[158:161]\n"
        "ds_write_b128 v12, v[106:109] offset:6144\n"
        "v_mfma_f32_16x16x32_bf16 v[214:217], v[206:209], v[52:55], 0\n"
        "s_waitcnt vmcnt(0)\n"
        "v_cvt_pk_bf16_f32 v201, v132, v133\n"
        "v_cvt_pk_bf16_f32 v121, v134, v135\n"
        "v_mfma_f32_16x16x32_bf16 v[214:217], v[202:205], v[76:79], v[214:217]\n"
        "ds_write_b128 v12, v[198:201] offset:7168\n"
        "v_mfma_f32_16x16x32_bf16 v[214:217], v[194:197], v[84:87], v[214:217]\n"
        "ds_write_b128 v12, v[118:121] offset:8192\n"
        "v_mfma_f32_16x16x32_bf16 v[40:43], v[194:197], v[88:91], 0\n"
        "ds_read_b128 v[126:129], v9\n"
        "v_perm_b32 v210, v147, v141, s54\n"
        "v_mfma_f32_16x16x32_bf16 v[230:233], v[194:197], v[48:51], 0\n"
        "ds_read_b128 v[132:135], v10\n"
        "v_perm_b32 v211, v147, v81, s55\n"
        "v_mfma_f32_16x16x32_bf16 v[34:37], v[106:109], v[106:109], 0\n"
        "ds_write_b64 v192, v[210:211] offset:31688\n"
        "v_mfma_f32_16x16x32_bf16 v[34:37], v[198:201], v[198:201], v[34:37]\n"
        "ds_read_b128 v[138:141], v11\n"
        "v_mul_f32 v122, v122, v188\n"
        "v_mfma_f32_16x16x32_bf16 v[34:37], v[118:121], v[118:121], v[34:37]\n"
        "ds_read_b128 v[106:109], v9 offset:3072\n"
        "v_cvt_pk_f16_f32 v148, v148, v122\n"
        "v_mfma_f32_16x16x32_bf16 v[198:201], v[206:209], v[112:115], 0\n"
        "ds_read_b128 v[112:115], v10 offset:3072\n"
        "v_perm_b32 v208, v148, v142, s54\n"
        "v_mfma_f32_16x16x32_bf16 v[198:201], v[202:205], v[218:221], v[198:201]\n"
        "ds_read_b128 v[118:121], v11 offset:3072\n"
        "v_perm_b32 v209, v148, v82, s55\n"
        "v_mfma_f32_16x16x32_bf16 v[198:201], v[194:197], v[152:155], v[198:201]\n"
        "ds_write_b64 v193, v[208:209] offset:8\n"
        "v_cndmask_b32_e64 v189, v189, v212, s[58:59]\n"
        "v_cndmask_b32_e64 v189, v189, v213, s[60:61]\n"
        "v_cndmask_b32_e64 v202, v226, v227, s[56:57]\n"
        "v_fmac_f32_dpp v186, v255, v254 quad_perm:[2,2,2,2] row_mask:0xf bank_mask:0xf\n"
        "v_cndmask_b32_e64 v202, v202, v228, s[58:59]\n"
        "v_cndmask_b32_e64 v202, v202, v229, s[60:61]\n"
        "v_mul_f32 v158, v158, v187\n"
        "v_mul_f32 v214, v214, v187\n"
        "v_fmac_f32_dpp v186, v255, v252 quad_perm:[3,3,3,3] row_mask:0xf bank_mask:0xf\n"
        "v_cvt_pk_f16_f32 v158, v158, v214\n"
        "v_mul_f32 v203, s80, v186\n"
        "v_cvt_f32_f16_sdwa v255, v222 dst_sel:DWORD dst_unused:UNUSED_PAD src0_sel:WORD_1\n"
        "v_perm_b32 v154, v158, v40, s54\n"
        "v_mul_f32 v159, v159, v190\n"
        "v_mul_f32 v215, v215, v190\n"
        "v_mul_f32 v160, v160, v188\n"
        "v_mul_f32 v216, v216, v188\n"
        "v_cvt_pk_f16_f32 v159, v159, v215\n"
        "v_cvt_pk_f16_f32 v160, v160, v216\n"
        "v_mov_b32 v208, 0x3c003c00\n"
        "v_perm_b32 v204, v159, v41, s54\n"
        "v_lshrrev_b32 v209, 16, v222\n"
        "v_mov_b32 v190, s29\n"
        "v_perm_b32 v152, v160, v42, s54\n"
        "v_cndmask_b32_e64 v80, 0, v208, s[76:77]\n"
        "v_cndmask_b32_e64 v81, 0, v208, s[56:57]\n"
        "v_cndmask_b32_e64 v82, 0, v208, s[58:59]\n"
        "v_cndmask_b32_e64 v83, 0, v208, s[60:61]\n"
        "v_readfirstlane_b32 s20, v224\n"
        "v_readfirstlane_b32 s21, v225\n"
        "v_perm_b32 v155, v158, v230, s55\n"
        "v_perm_b32 v205, v159, v231, s55\n"
        "v_perm_b32 v153, v160, v232, s55\n"
        "ds_write_b64 v192, v[154:155] offset:25360\n"
        "ds_write_b64 v192, v[204:205] offset:31696\n"
        "ds_write_b64 v193, v[152:153] offset:16\n"
        "ds_read_b128 v[146:149], v9 offset:6144\n"
        "v_cndmask_b32_e64 v193, v34, v35, s[56:57]\n"
        "v_cndmask_b32_e64 v193, v193, v36, s[58:59]\n"
        "v_cndmask_b32_e64 v193, v193, v37, s[60:61]\n"
        "ds_read_b128 v[152:155], v10 offset:6144\n"
        "ds_read_b128 v[158:161], v11 offset:6144\n"
        "v_cndmask_b32_dpp v189, v193, v189, vcc row_ror:4 row_mask:0xf bank_mask:0xf\n"
        "v_cndmask_b32_e64 v208, v198, v199, s[56:57]\n"
        "v_cndmask_b32_e64 v208, v208, v200, s[58:59]\n"
        "v_cndmask_b32_dpp v202, v189, v202, vcc row_ror:4 row_mask:0xf bank_mask:0xf\n"
        "v_fma_f32 v203, -v203, v186, v202\n"
        "v_max_f32 v203, 0, v203\n"
        "v_sub_f32 v202, 0x47c00000, v202\n"
        "v_fmaak_f32 v203, v28, v203, 0x3727c5ac\n"
        "v_bfi_b32 v203, s84, v202, v203\n"
        "v_rsq_f32 v193, v203\n"
        "v_cndmask_b32_e64 v208, v208, v201, s[60:61]\n"
        "v_cvt_f16_f32 v193, v193\n"
        "v_mul_f32 v208, v208, v255\n"
        "ds_write_b16 v18, v193\n"
        "ds_write_b16 v19, v193\n"
        "v_mul_f32 v208, v208, v255\n"
        "v_exp_f32 v208, v208\n"
        "ds_write_b16 v22, v190 offset:3072\n"
        "v_cvt_pk_f16_f32 v208, v208, 0\n"
        "ds_write_b64 v191, v[208:209]\n"
        "s_waitcnt lgkmcnt(0)\n"
        "s_nop 7\n"
        ".p2align 6\n"
        "TOP_%=:\n"
        "v_mfma_f32_16x16x32_bf16 v[206:209], v[126:129], v[126:129], 0\n"
        "s_mov_b32 s28, s89\n"
        "ds_read_b128 v[222:225], v20 offset:512\n"
        "s_mov_b32 s30, s29\n"
        "v_mfma_f32_16x16x32_bf16 v[206:209], v[132:135], v[132:135], v[206:209]\n"
        "s_add_u32 s88, s88, 32\n"
        "v_add_u32 v23, s22, v23\n"
        "v_add_u32 v24, s22, v24\n"
        "v_mfma_f32_16x16x32_bf16 v[206:209], v[138:141], v[138:141], v[206:209]\n"
        "s_mov_b32 s22, s23\n"
        "ds_read_b128 v[192:195], v20 offset:528\n"
        "s_add_u32 s89, s86, s51\n"
        "v_mfma_f32_16x16x32_bf16 v[226:229], v[92:95], v[138:141], 0\n"
        "s_add_u32 s29, s29, 1\n"
        "s_add_u32 s26, s28, 392448\n"
        "v_cvt_pk_bf16_f32 v202, v196, v186\n"
        "v_mfma_f32_16x16x32_bf16 v[226:229], v[100:103], v[132:135], v[226:229]\n"
        "s_cmp_ge_u32 s88, 73\n"
        "v_cvt_pk_bf16_f32 v220, v197, v187\n"
        "v_lshlrev_b32 v214, 16, v180\n"
        "v_mfma_f32_16x16x32_bf16 v[226:229], v[96:99], v[126:129], v[226:229]\n"
        "buffer_store_dwordx3 v[200:202], v15, s[40:43], s26 offen offset:768 sc1 nt\n"
        "v_lshlrev_b32 v215, 16, v172\n"
        "v_lshlrev_b32 v216, 16, v144\n"
        "v_lshlrev_b32 v217, 16, v176\n"
        "v_mfma_f32_16x16x32_bf16 v[202:205], a[0:3], v[126:129], 0\n"
        "s_movk_i32 s23, 1584\n"
        "v_cvt_pk_bf16_f32 v212, v198, v188\n"
        "v_lshlrev_b32 v200, 16, v124\n"
        "v_mfma_f32_16x16x32_bf16 v[202:205], a[4:7], v[132:135], v[202:205]\n"
        "s_mov_b32 s86, s87\n"
        "v_lshlrev_b32 v201, 16, v42\n"
        "v_cvt_pk_bf16_f32 v250, v199, v189\n"
        "v_mfma_f32_16x16x32_bf16 v[202:205], a[8:11], v[138:141], v[202:205]\n"
        "s_cselect_b32 s92, 588672, 0\n"
        "v_cndmask_b32_e64 v254, v206, v207, s[56:57]\n"
        "v_cndmask_b32_e64 v254, v254, v208, s[58:59]\n"
        "v_mfma_f32_16x16x32_bf16 v[196:199], a[20:23], v[138:141], 0\n"
        "s_cselect_b32 s94, 73, 0\n"
        "s_add_u32 s27, s28, 490560\n"
        "s_add_u32 s87, s87, 43008\n"
        "v_mfma_f32_16x16x32_bf16 v[196:199], a[16:19], v[132:135], v[196:199]\n"
        "s_waitcnt lgkmcnt(0)\n"
        "v_pk_mul_f16 v186, v222, v182\n"
        "v_pk_mul_f16 v187, v222, v34\n"
        "v_mfma_f32_16x16x32_bf16 v[196:199], a[12:15], v[126:129], v[196:199]\n"
        "buffer_store_dwordx3 v[218:220], v14, s[40:43], s27 offen offset:192 sc1 nt\n"
        "v_pk_mul_f16 v188, v222, v162\n"
        "v_pk_mul_f16 v189, v222, v166\n"
        "v_pk_mul_f16 v190, v222, v244\n"
        "v_mfma_f32_16x16x32_bf16 v[218:221], a[24:27], v[126:129], 0\n"
        "s_add_u32 s26, s28, 588672\n"
        "v_pk_mul_f16 v191, v222, v240\n"
        "v_pk_fma_f16 v186, v223, v183, v186\n"
        "v_mfma_f32_16x16x32_bf16 v[218:221], a[28:31], v[132:135], v[218:221]\n"
        "s_sub_u32 s88, s88, s94\n"
        "v_pk_fma_f16 v187, v223, v35, v187\n"
        "v_pk_fma_f16 v188, v223, v163, v188\n"
        "v_mfma_f32_16x16x32_bf16 v[218:221], a[32:35], v[138:141], v[218:221]\n"
        "buffer_store_dwordx3 v[210:212], v16, s[40:43], s27 offen offset:960 sc1 nt\n"
        "s_add_u32 s87, s87, s92\n"
        "v_pk_fma_f16 v189, v223, v167, v189\n"
        "v_pk_fma_f16 v190, v223, v245, v190\n"
        "v_mfma_f32_16x16x32_bf16 v[210:213], a[44:47], v[138:141], 0\n"
        "s_waitcnt lgkmcnt(0)\n"
        "v_pk_fma_f16 v191, v223, v241, v191\n"
        "v_pk_fma_f16 v186, v224, v184, v186\n"
        "v_mfma_f32_16x16x32_bf16 v[210:213], a[40:43], v[132:135], v[210:213]\n"
        "s_waitcnt lgkmcnt(0)\n"
        "v_pk_fma_f16 v187, v224, v36, v187\n"
        "v_pk_fma_f16 v188, v224, v164, v188\n"
        "v_mfma_f32_16x16x32_bf16 v[210:213], a[36:39], v[126:129], v[210:213]\n"
        "buffer_store_dwordx3 v[248:250], v14, s[40:43], s26 offen offset:384 sc1 nt\n"
        "v_pk_fma_f16 v189, v224, v168, v189\n"
        "v_pk_fma_f16 v190, v224, v246, v190\n"
        "v_pk_fma_f16 v191, v224, v242, v191\n"
        "v_mfma_f32_16x16x32_bf16 v[228:231], a[48:51], v[126:129], 0\n"
        "v_pk_fma_f16 v186, v225, v185, v186\n"
        "v_pk_fma_f16 v187, v225, v37, v187\n"
        "v_mfma_f32_16x16x32_bf16 v[228:231], a[52:55], v[132:135], v[228:231]\n"
        "v_pk_fma_f16 v188, v225, v165, v188\n"
        "v_pk_fma_f16 v189, v225, v169, v189\n"
        "v_mfma_f32_16x16x32_bf16 v[228:231], a[56:59], v[138:141], v[228:231]\n"
        "v_pk_fma_f16 v190, v225, v247, v190\n"
        "v_pk_fma_f16 v191, v225, v243, v191\n"
        "v_mfma_f32_16x16x32_bf16 v[162:165], a[68:71], v[138:141], 0\n"
        "v_pk_fma_f16 v186, v192, v178, v186\n"
        "v_pk_fma_f16 v187, v192, v170, v187\n"
        "v_mfma_f32_16x16x32_bf16 v[162:165], a[64:67], v[132:135], v[162:165]\n"
        "v_cvt_pk_bf16_f32 v222, v202, v203\n"
        "v_cvt_pk_bf16_f32 v223, v204, v205\n"
        "v_mfma_f32_16x16x32_bf16 v[162:165], a[60:63], v[126:129], v[162:165]\n"
        "v_cvt_pk_bf16_f32 v224, v196, v197\n"
        "v_cvt_pk_bf16_f32 v225, v198, v199\n"
        "v_mfma_f32_16x16x32_bf16 v[34:37], v[106:109], v[106:109], 0\n"
        "v_cvt_pk_bf16_f32 v196, v218, v219\n"
        "v_cvt_pk_bf16_f32 v197, v220, v221\n"
        "v_mfma_f32_16x16x32_bf16 v[34:37], v[112:115], v[112:115], v[34:37]\n"
        "v_cvt_pk_bf16_f32 v198, v210, v211\n"
        "v_cvt_pk_bf16_f32 v199, v212, v213\n"
        "v_mfma_f32_16x16x32_bf16 v[34:37], v[118:121], v[118:121], v[34:37]\n"
        "v_cvt_pk_bf16_f32 v166, v228, v229\n"
        "v_cvt_pk_bf16_f32 v167, v230, v231\n"
        "v_mfma_f32_16x16x32_bf16 v[182:185], v[92:95], v[118:121], 0\n"
        "v_cvt_pk_bf16_f32 v168, v162, v163\n"
        "v_cvt_pk_bf16_f32 v169, v164, v165\n"
        "v_mfma_f32_16x16x32_bf16 v[182:185], v[100:103], v[112:115], v[182:185]\n"
        "v_pk_fma_f16 v188, v192, v142, v188\n"
        "v_pk_fma_f16 v189, v192, v174, v189\n"
        "v_mfma_f32_16x16x32_bf16 v[182:185], v[96:99], v[106:109], v[182:185]\n"
        "ds_read_u16 v255, v19 offset:2048\n"
        "v_pk_fma_f16 v190, v192, v122, v190\n"
        "v_mfma_f32_16x16x32_bf16 v[230:233], v[126:129], v[222:225], 0\n"
        "v_pk_fma_f16 v191, v192, v40, v191\n"
        "v_pk_fma_f16 v186, v193, v179, v186\n"
        "v_mfma_f32_16x16x32_bf16 v[230:233], v[132:135], v[196:199], v[230:233]\n"
        "v_pk_fma_f16 v187, v193, v171, v187\n"
        "v_pk_fma_f16 v188, v193, v143, v188\n"
        "v_mfma_f32_16x16x32_bf16 v[230:233], v[138:141], v[166:169], v[230:233]\n"
        "v_pk_fma_f16 v189, v193, v175, v189\n"
        "v_pk_fma_f16 v190, v193, v123, v190\n"
        "v_mfma_f32_16x16x32_bf16 v[162:165], a[8:11], v[118:121], 0\n"
        "v_pk_fma_f16 v191, v193, v41, v191\n"
        "v_pk_fma_f16 v186, v194, v180, v186\n"
        "v_mfma_f32_16x16x32_bf16 v[162:165], a[4:7], v[112:115], v[162:165]\n"
        "v_pk_fma_f16 v187, v194, v172, v187\n"
        "v_pk_fma_f16 v188, v194, v144, v188\n"
        "v_mfma_f32_16x16x32_bf16 v[162:165], a[0:3], v[106:109], v[162:165]\n"
        "v_pk_fma_f16 v189, v194, v176, v189\n"
        "v_pk_fma_f16 v190, v194, v124, v190\n"
        "v_mfma_f32_16x16x32_bf16 v[122:125], a[12:15], v[106:109], 0\n"
        "s_nop 0\n"
        "v_pk_fma_f16 v191, v194, v42, v191\n"
        "v_cndmask_b32_e64 v252, v34, v35, s[56:57]\n"
        "v_mfma_f32_16x16x32_bf16 v[122:125], a[16:19], v[112:115], v[122:125]\n"
        "s_add_u32 s46, s28, 590016\n"
        "v_cvt_pk_bf16_f32 v40, v162, v163\n"
        "v_cvt_pk_bf16_f32 v41, v164, v165\n"
        "v_mfma_f32_16x16x32_bf16 v[122:125], a[20:23], v[118:121], v[122:125]\n"
        "s_waitcnt lgkmcnt(0)\n"
        "v_cmp_ne_u32_e64 s[24:25], s30, v255\n"
        "v_cndmask_b32_e64 v252, v252, v36, s[58:59]\n"
        "v_mfma_f32_16x16x32_f16 v[214:217], v[80:83], v[186:189], v[214:217]\n"
        "s_min_u32 s46, s46, 200540928\n"
        "v_cndmask_b32_e64 v252, v252, v37, s[60:61]\n"
        "v_cndmask_b32_e64 v254, v254, v209, s[60:61]\n"
        "v_mfma_f32_16x16x32_f16 v[198:201], v[80:83], v[188:191], v[198:201]\n"
        "s_cmp_le_i32 s85, 1\n"
        "ds_read_b128 v[234:237], v23\n"
        "v_cvt_pk_bf16_f32 v42, v122, v123\n"
        "v_mfma_f32_16x16x32_bf16 v[162:165], a[32:35], v[118:121], 0\n"
        "s_cselect_b64 s[24:25], -1, s[24:25]\n"
        "v_cvt_pk_bf16_f32 v174, v214, v215\n"
        "v_cvt_pk_bf16_f32 v175, v216, v217\n"
        "v_mfma_f32_16x16x32_bf16 v[162:165], a[28:31], v[112:115], v[162:165]\n"
        "ds_read_b128 v[170:173], v23 offset:64\n"
        "v_cvt_pk_bf16_f32 v176, v200, v201\n"
        "v_mfma_f32_16x16x32_bf16 v[162:165], a[24:27], v[106:109], v[162:165]\n"
        "s_nop 0\n"
        "ds_read_b128 v[34:37], v23 offset:128\n"
        "ds_read_b64 v[30:31], v24 offset:360\n"
        "v_mfma_f32_16x16x32_bf16 v[142:145], a[36:39], v[106:109], 0\n"
        "s_nop 0\n"
        "v_cvt_pk_bf16_f32 v43, v124, v125\n"
        "v_mfma_f32_16x16x32_bf16 v[142:145], a[40:43], v[112:115], v[142:145]\n"
        "s_nop 0\n"
        "v_cvt_pk_bf16_f32 v166, v162, v163\n"
        "v_cvt_pk_bf16_f32 v167, v164, v165\n"
        "v_mfma_f32_16x16x32_bf16 v[142:145], a[44:47], v[118:121], v[142:145]\n"
        "buffer_store_dwordx3 v[174:176], v17, s[44:47], s26 offen offset:1152 sc1 nt\n"
        "v_mfma_f32_16x16x32_bf16 v[178:181], a[56:59], v[118:121], 0\n"
        "s_cmp_lg_u64 s[24:25], 0\n"
        "v_mfma_f32_16x16x32_bf16 v[178:181], a[52:55], v[112:115], v[178:181]\n"
        "s_cbranch_scc1 SP3_%=\n"
        "BK3_%=:\n"
        "v_mfma_f32_16x16x32_bf16 v[178:181], a[48:51], v[106:109], v[178:181]\n"
        "s_cmp_gt_i32 s85, 0\n"
        "ds_read_b128 v[190:193], v13\n"
        "ds_read_b128 v[214:217], v13 offset:1024\n"
        "v_mfma_f32_16x16x32_bf16 v[122:125], a[60:63], v[106:109], 0\n"
        "s_cselect_b32 s94, s87, 0\n"
        "ds_read_b128 v[218:221], v13 offset:2048\n"
        "ds_read_b128 v[210:213], v13 offset:3072\n"
        "v_mfma_f32_16x16x32_bf16 v[122:125], a[64:67], v[112:115], v[122:125]\n"
        "s_add_u32 s38, s36, s94\n"
        "ds_read_b128 v[202:205], v13 offset:4096\n"
        "ds_read_b128 v[186:189], v13 offset:5120\n"
        "v_mfma_f32_16x16x32_bf16 v[122:125], a[68:71], v[118:121], v[122:125]\n"
        "s_addc_u32 s39, s37, 0\n"
        "ds_read_b128 v[198:201], v13 offset:6144\n"
        "ds_read_b128 v[206:209], v13 offset:7168\n"
        "v_mfma_f32_16x16x32_bf16 v[240:243], v[146:149], v[146:149], 0\n"
        "s_waitcnt lgkmcnt(8)\n"
        "ds_read_b128 v[222:225], v13 offset:8192\n"
        "ds_read_b128 v[194:197], v20\n"
        "v_mfma_f32_16x16x32_bf16 v[240:243], v[152:155], v[152:155], v[240:243]\n"
        "s_sub_u32 s85, s85, 1\n"
        "ds_read_b64 v[238:239], v20 offset:16\n"
        "ds_read_u16 v255, v22\n"
        "v_mfma_f32_16x16x32_bf16 v[240:243], v[158:161], v[158:161], v[240:243]\n"
        "ds_read_b64 v[164:165], v21\n"
        "v_cvt_pk_bf16_f32 v168, v142, v143\n"
        "v_mfma_f32_16x16x32_bf16 v[174:177], v[92:95], v[158:161], 0\n"
        "s_add_u32 m0, s52, 0\n"
        "v_cvt_pk_bf16_f32 v169, v144, v145\n"
        "v_cvt_pk_bf16_f32 v142, v178, v179\n"
        "v_mfma_f32_16x16x32_bf16 v[174:177], v[100:103], v[152:155], v[174:177]\n"
        "v_cvt_pk_bf16_f32 v143, v180, v181\n"
        "v_cvt_pk_bf16_f32 v144, v122, v123\n"
        "v_mfma_f32_16x16x32_bf16 v[174:177], v[96:99], v[146:149], v[174:177]\n"
        "global_load_lds_dwordx4 v0, s[38:39]\n"
        "v_cvt_pk_bf16_f32 v145, v124, v125\n"
        "v_mfma_f32_16x16x32_bf16 v[122:125], v[106:109], v[40:43], 0\n"
        "s_waitcnt lgkmcnt(10)\n"
        "v_cndmask_b32_e64 v251, v240, v241, s[56:57]\n"
        "v_cndmask_b32_e64 v251, v251, v242, s[58:59]\n"
        "v_mfma_f32_16x16x32_bf16 v[122:125], v[112:115], v[166:169], v[122:125]\n"
        "s_waitcnt lgkmcnt(7)\n"
        "v_cndmask_b32_e64 v251, v251, v243, s[60:61]\n"
        "v_cndmask_b32_dpp v253, v174, v182, vcc row_ror:4 row_mask:0xf bank_mask:0xf\n"
        "v_mfma_f32_16x16x32_bf16 v[122:125], v[118:121], v[142:145], v[122:125]\n"
        "v_mov_b32 v250, s29\n"
        "v_cndmask_b32_dpp v252, v251, v252, vcc row_ror:4 row_mask:0xf bank_mask:0xf\n"
        "v_mfma_f32_16x16x32_bf16 v[40:43], v[234:237], v[190:193], 0\n"
        "ds_write_b16 v22, v250 offset:3584\n"
        "v_mfma_f32_16x16x32_bf16 v[40:43], v[170:173], v[214:217], v[40:43]\n"
        "s_waitcnt lgkmcnt(1)\n"
        "v_cndmask_b32_dpp v254, v252, v254, vcc row_ror:4 row_mask:0xf bank_mask:0xf\n"
        "v_cndmask_b32_dpp v253, v253, v226, vcc row_ror:4 row_mask:0xf bank_mask:0xf\n"
        "v_mfma_f32_16x16x32_bf16 v[40:43], v[34:37], v[218:221], v[40:43]\n"
        "v_mul_f32 v252, s80, v253\n"
        "v_fma_f32 v252, -v252, v253, v254\n"
        "v_mfma_f32_16x16x32_bf16 v[166:169], v[34:37], v[186:189], 0\n"
        "v_max_f32 v252, 0, v252\n"
        "v_fmaak_f32 v252, v28, v252, 0x3727c5ac\n"
        "v_mfma_f32_16x16x32_bf16 v[166:169], v[170:173], v[202:205], v[166:169]\n"
        "global_load_lds_dwordx4 v1, s[38:39] offset:1024\n"
        "v_readlane_b32 s93, v31, 1\n"
        "v_mfma_f32_16x16x32_bf16 v[166:169], v[234:237], v[210:213], v[166:169]\n"
        "v_rsq_f32 v250, v252\n"
        "v_mfma_f32_16x16x32_bf16 v[142:145], v[234:237], v[198:201], 0\n"
        "s_nop 0\n"
        "v_cndmask_b32_e64 v30, 0, v30, s[72:73]\n"
        "v_cndmask_b32_dpp v251, v175, v183, vcc row_ror:4 row_mask:0xf bank_mask:0xf\n"
        "v_mfma_f32_16x16x32_bf16 v[142:145], v[170:173], v[206:209], v[142:145]\n"
        "s_nop 0\n"
        "v_cndmask_b32_dpp v251, v251, v227, vcc row_ror:4 row_mask:0xf bank_mask:0xf\n"
        "v_cndmask_b32_e64 v252, v40, v166, s[64:65]\n"
        "v_mfma_f32_16x16x32_bf16 v[142:145], v[34:37], v[222:225], v[142:145]\n"
        "s_nop 0\n"
        "v_cndmask_b32_e64 v254, v41, v167, s[64:65]\n"
        "v_mfma_f32_16x16x32_bf16 v[34:37], a[8:11], v[158:161], 0\n"
        "s_nop 2\n"
        "v_mfma_f32_16x16x32_bf16 v[34:37], a[4:7], v[152:155], v[34:37]\n"
        "s_nop 0\n"
        "v_cndmask_b32_e64 v252, v252, v142, s[70:71]\n"
        "v_cndmask_b32_e64 v254, v254, v143, s[70:71]\n"
        "v_mfma_f32_16x16x32_bf16 v[34:37], a[0:3], v[146:149], v[34:37]\n"
        "s_nop 0\n"
        "v_cvt_pk_f16_f32 v252, v252, v254\n"
        "v_pk_mul_f16 v252, v252, v255 op_sel_hi:[1,0]\n"
        "v_mfma_f32_16x16x32_bf16 v[40:43], a[12:15], v[146:149], 0\n"
        "global_load_lds_dwordx4 v2, s[38:39] offset:2048\n"
        "v_pk_mul_f16 v252, v252, s93 op_sel_hi:[1,0]\n"
        "v_mfma_f32_16x16x32_bf16 v[40:43], a[16:19], v[152:155], v[40:43]\n"
        "s_nop 0\n"
        "v_cvt_pk_bf16_f32 v166, v34, v35\n"
        "v_cvt_pk_bf16_f32 v167, v36, v37\n"
        "v_mfma_f32_16x16x32_bf16 v[40:43], a[20:23], v[158:161], v[40:43]\n"
        "s_nop 0\n"
        "v_exp_f16_sdwa v254, v252 dst_sel:WORD_1 dst_unused:UNUSED_PAD src0_sel:WORD_1\n"
        "v_mfma_f32_16x16x32_bf16 v[34:37], a[32:35], v[158:161], 0\n"
        "s_nop 0\n"
        "v_exp_f16_sdwa v229, v252 dst_sel:WORD_0 dst_unused:UNUSED_PAD src0_sel:WORD_0\n"
        "v_mfma_f32_16x16x32_bf16 v[34:37], a[28:31], v[152:155], v[34:37]\n"
        "s_nop 0\n"
        "v_cndmask_b32_e64 v229, v229, v30, s[68:69]\n"
        "v_cvt_pk_bf16_f32 v168, v40, v41\n"
        "v_mfma_f32_16x16x32_bf16 v[34:37], a[24:27], v[146:149], v[34:37]\n"
        "s_nop 0\n"
        "v_cvt_pk_bf16_f32 v169, v42, v43\n"
        "v_mfma_f32_16x16x32_bf16 v[40:43], a[36:39], v[146:149], 0\n"
        "s_nop 2\n"
        "v_mfma_f32_16x16x32_bf16 v[40:43], a[40:43], v[152:155], v[40:43]\n"
        "s_nop 0\n"
        "v_cvt_pk_bf16_f32 v170, v34, v35\n"
        "v_cvt_pk_bf16_f32 v171, v36, v37\n"
        "v_mfma_f32_16x16x32_bf16 v[40:43], a[44:47], v[158:161], v[40:43]\n"
        "global_load_lds_dwordx4 v3, s[38:39] offset:3072\n"
        "s_nop 0\n"
        "v_mfma_f32_16x16x32_bf16 v[142:145], a[56:59], v[158:161], 0\n"
        "s_nop 2\n"
        "v_mfma_f32_16x16x32_bf16 v[142:145], a[52:55], v[152:155], v[142:145]\n"
        "s_nop 0\n"
        "v_cvt_pk_bf16_f32 v172, v40, v41\n"
        "v_cvt_pk_bf16_f32 v173, v42, v43\n"
        "v_mfma_f32_16x16x32_bf16 v[142:145], a[48:51], v[146:149], v[142:145]\n"
        "s_nop 1\n"
        "v_mfma_f32_16x16x32_bf16 v[40:43], a[60:63], v[146:149], 0\n"
        "s_nop 2\n"
        "v_mfma_f32_16x16x32_bf16 v[40:43], a[64:67], v[152:155], v[40:43]\n"
        "s_nop 0\n"
        "v_cvt_pk_bf16_f32 v34, v142, v143\n"
        "v_cvt_pk_bf16_f32 v35, v144, v145\n"
        "v_mfma_f32_16x16x32_bf16 v[40:43], a[68:71], v[158:161], v[40:43]\n"
        "s_add_u32 m0, s52, 4096\n"
        "s_nop 0\n"
        "v_mfma_f32_16x16x32_bf16 v[142:145], a[152:155], v[218:221], 0\n"
        "s_nop 2\n"
        "v_mfma_f32_16x16x32_bf16 v[142:145], a[148:151], v[214:217], v[142:145]\n"
        "global_load_lds_dwordx4 v4, s[38:39]\n"
        "v_cvt_pk_bf16_f32 v36, v40, v41\n"
        "v_mfma_f32_16x16x32_bf16 v[142:145], a[144:147], v[190:193], v[142:145]\n"
        "s_nop 0\n"
        "v_cvt_pk_bf16_f32 v37, v42, v43\n"
        "v_mfma_f32_16x16x32_bf16 v[40:43], v[146:149], v[166:169], 0\n"
        "s_nop 2\n"
        "v_mfma_f32_16x16x32_bf16 v[40:43], v[152:155], v[170:173], v[40:43]\n"
        "s_nop 0\n"
        "v_cvt_pk_bf16_f32 v170, v142, v143\n"
        "v_cvt_pk_bf16_f32 v171, v144, v145\n"
        "v_mfma_f32_16x16x32_bf16 v[40:43], v[158:161], v[34:37], v[40:43]\n"
        "s_nop 1\n"
        "v_mfma_f32_16x16x32_bf16 v[34:37], a[164:167], v[218:221], 0\n"
        "s_nop 2\n"
        "v_mfma_f32_16x16x32_bf16 v[34:37], a[160:163], v[214:217], v[34:37]\n"
        "s_nop 0\n"
        "v_cndmask_b32_dpp v252, v40, v122, vcc row_ror:4 row_mask:0xf bank_mask:0xf\n"
        "v_cndmask_b32_dpp v247, v41, v123, vcc row_ror:4 row_mask:0xf bank_mask:0xf\n"
        "v_mfma_f32_16x16x32_bf16 v[34:37], a[156:159], v[190:193], v[34:37]\n"
        "v_cndmask_b32_dpp v245, v42, v124, vcc row_ror:4 row_mask:0xf bank_mask:0xf\n"
        "v_cndmask_b32_dpp v249, v43, v125, vcc row_ror:4 row_mask:0xf bank_mask:0xf\n"
        "v_mfma_f32_16x16x32_bf16 v[142:145], a[168:171], v[190:193], 0\n"
        "global_load_lds_dwordx4 v5, s[38:39] offset:1024\n"
        "v_cndmask_b32_dpp v252, v252, v230, vcc row_ror:4 row_mask:0xf bank_mask:0xf\n"
        "v_mfma_f32_16x16x32_bf16 v[142:145], a[172:175], v[214:217], v[142:145]\n"
        "v_cndmask_b32_dpp v247, v247, v231, vcc row_ror:4 row_mask:0xf bank_mask:0xf\n"
        "v_cndmask_b32_dpp v245, v245, v232, vcc row_ror:4 row_mask:0xf bank_mask:0xf\n"
        "v_mfma_f32_16x16x32_bf16 v[142:145], a[176:179], v[218:221], v[142:145]\n"
        "v_cndmask_b32_dpp v249, v249, v233, vcc row_ror:4 row_mask:0xf bank_mask:0xf\n"
        "v_mul_f32_dpp v252, v250, v252 quad_perm:[0,0,0,0] row_mask:0xf bank_mask:0xf\n"
        "v_mfma_f32_16x16x32_bf16 v[40:43], a[188:191], v[218:221], 0\n"
        "v_mul_f32_dpp v247, v250, v247 quad_perm:[1,1,1,1] row_mask:0xf bank_mask:0xf\n"
        "v_mul_f32_dpp v245, v250, v245 quad_perm:[2,2,2,2] row_mask:0xf bank_mask:0xf\n"
        "v_mfma_f32_16x16x32_bf16 v[40:43], a[184:187], v[214:217], v[40:43]\n"
        "v_mul_f32_dpp v249, v250, v249 quad_perm:[3,3,3,3] row_mask:0xf bank_mask:0xf\n"
        "v_max3_f32 v248, v252, v247, v245\n"
        "v_mfma_f32_16x16x32_bf16 v[40:43], a[180:183], v[190:193], v[40:43]\n"
        "v_max_f32 v248, v248, v249\n"
        "v_mul_f32 v248, v248, v250\n"
        "v_mfma_f32_16x16x32_bf16 v[166:169], a[192:195], v[190:193], 0\n"
        "v_fma_f32 v252, v252, v250, -v248\n"
        "v_fma_f32 v247, v247, v250, -v248\n"
        "v_mfma_f32_16x16x32_bf16 v[166:169], a[196:199], v[214:217], v[166:169]\n"
        "v_fma_f32 v245, v245, v250, -v248\n"
        "v_fma_f32 v249, v249, v250, -v248\n"
        "v_mfma_f32_16x16x32_bf16 v[166:169], a[200:203], v[218:221], v[166:169]\n"
        "global_load_lds_dwordx4 v6, s[38:39] offset:2048\n"
        "v_cvt_pk_bf16_f32 v172, v34, v35\n"
        "v_mfma_f32_16x16x32_bf16 v[122:125], a[212:215], v[218:221], 0\n"
        "v_exp_f32 v252, v252\n"
        "v_mfma_f32_16x16x32_bf16 v[122:125], a[208:211], v[214:217], v[122:125]\n"
        "v_cvt_pk_bf16_f32 v173, v36, v37\n"
        "v_cvt_pk_bf16_f32 v34, v142, v143\n"
        "v_mfma_f32_16x16x32_bf16 v[122:125], a[204:207], v[190:193], v[122:125]\n"
        "v_cvt_pk_bf16_f32 v35, v144, v145\n"
        "v_cvt_pk_bf16_f32 v36, v40, v41\n"
        "v_mfma_f32_16x16x32_bf16 v[174:177], v[126:129], a[72:75], 0\n"
        "v_cvt_pk_bf16_f32 v37, v42, v43\n"
        "v_cvt_pk_bf16_f32 v40, v166, v167\n"
        "v_mfma_f32_16x16x32_bf16 v[174:177], v[132:135], a[76:79], v[174:177]\n"
        "s_nop 0\n"
        "v_cvt_pk_bf16_f32 v41, v168, v169\n"
        "v_cvt_pk_bf16_f32 v42, v122, v123\n"
        "v_mfma_f32_16x16x32_bf16 v[174:177], v[138:141], a[80:83], v[174:177]\n"
        "v_cvt_pk_bf16_f32 v43, v124, v125\n"
        "v_mfma_f32_16x16x32_bf16 v[142:145], v[138:141], a[92:95], 0\n"
        "v_exp_f32 v247, v247\n"
        "v_mfma_f32_16x16x32_bf16 v[142:145], v[132:135], a[88:91], v[142:145]\n"
        "global_load_lds_dwordx4 v7, s[38:39] offset:3072\n"
        "v_add_f32 v246, v252, v247\n"
        "v_mfma_f32_16x16x32_bf16 v[142:145], v[126:129], a[84:87], v[142:145]\n"
        "v_exp_f32 v245, v245\n"
        "v_mfma_f32_16x16x32_bf16 v[178:181], v[190:193], v[170:173], 0\n"
        "v_exp_f32 v249, v249\n"
        "v_mfma_f32_16x16x32_bf16 v[178:181], v[214:217], v[34:37], v[178:181]\n"
        "v_add_f32 v248, v245, v249\n"
        "v_add_f32 v246, v246, v248\n"
        "v_mfma_f32_16x16x32_bf16 v[178:181], v[218:221], v[40:43], v[178:181]\n"
        "v_rcp_f32 v246, v246\n"
        "v_mfma_f32_16x16x32_bf16 v[166:169], v[186:189], v[40:43], 0\n"
        "v_mul_f32 v252, v252, v246\n"
        "v_mul_f32 v247, v247, v246\n"
        "v_mfma_f32_16x16x32_bf16 v[166:169], v[202:205], v[34:37], v[166:169]\n"
        "s_add_u32 m0, s52, 8192\n"
        "v_mul_f32 v245, v245, v246\n"
        "v_mul_f32 v249, v249, v246\n"
        "v_mfma_f32_16x16x32_bf16 v[166:169], v[210:213], v[170:173], v[166:169]\n"
        "v_mul_f32_dpp v252, v250, v252 quad_perm:[0,0,0,0] row_mask:0xf bank_mask:0xf\n"
        "v_mul_f32_dpp v247, v250, v247 quad_perm:[1,1,1,1] row_mask:0xf bank_mask:0xf\n"
        "v_mfma_f32_16x16x32_bf16 v[182:185], v[198:201], v[170:173], 0\n"
        "global_load_lds_dwordx4 v8, s[38:39]\n"
        "v_mul_f32_dpp v245, v250, v245 quad_perm:[2,2,2,2] row_mask:0xf bank_mask:0xf\n"
        "v_mfma_f32_16x16x32_bf16 v[182:185], v[206:209], v[34:37], v[182:185]\n"
        "v_mul_f32_dpp v249, v250, v249 quad_perm:[3,3,3,3] row_mask:0xf bank_mask:0xf\n"
        "v_cvt_pk_bf16_f32 v250, v252, v247\n"
        "v_mfma_f32_16x16x32_bf16 v[182:185], v[222:225], v[40:43], v[182:185]\n"
        "v_cvt_pk_bf16_f32 v248, v245, v249\n"
        "v_and_b32 v124, v25, v250\n"
        "v_mfma_f32_16x16x32_bf16 v[170:173], v[138:141], a[104:107], 0\n"
        "v_and_b32 v125, v25, v248\n"
        "v_mov_b64 v[130:131], v[124:125]\n"
        "v_mfma_f32_16x16x32_bf16 v[170:173], v[132:135], a[100:103], v[170:173]\n"
        "v_cvt_pk_bf16_f32 v42, v174, v175\n"
        "v_cvt_pk_bf16_f32 v43, v176, v177\n"
        "v_mfma_f32_16x16x32_bf16 v[170:173], v[126:129], a[96:99], v[170:173]\n"
        "v_cvt_pk_bf16_f32 v46, v142, v143\n"
        "v_cvt_pk_bf16_f32 v47, v144, v145\n"
        "v_mfma_f32_16x16x32_bf16 v[174:177], v[126:129], a[108:111], 0\n"
        "s_nop 0\n"
        "v_mov_b64 v[136:137], v[124:125]\n"
        "v_mov_b64 v[142:143], v[124:125]\n"
        "v_mfma_f32_16x16x32_bf16 v[174:177], v[132:135], a[112:115], v[174:177]\n"
        "s_nop 0\n"
        "v_cvt_pk_bf16_f32 v36, v170, v171\n"
        "v_cvt_pk_bf16_f32 v37, v172, v173\n"
        "v_mfma_f32_16x16x32_bf16 v[174:177], v[138:141], a[116:119], v[174:177]\n"
        "ds_read_u16 v246, v19 offset:2560\n"
        "v_and_b32_dpp v104, v250, v25 row_ror:12 row_mask:0xf bank_mask:0xf\n"
        "v_mfma_f32_16x16x32_bf16 v[234:237], v[42:45], v[124:127], 0\n"
        "s_nop 0\n"
        "v_and_b32_dpp v105, v248, v25 row_ror:12 row_mask:0xf bank_mask:0xf\n"
        "v_mov_b64 v[110:111], v[104:105]\n"
        "v_mfma_f32_16x16x32_bf16 v[122:125], v[44:47], v[128:131], 0\n"
        "s_nop 0\n"
        "v_cvt_pk_bf16_f32 v40, v174, v175\n"
        "v_cvt_pk_bf16_f32 v41, v176, v177\n"
        "v_mfma_f32_16x16x32_bf16 v[230:233], v[138:141], a[128:131], 0\n"
        "v_cvt_pk_bf16_f32 v170, v234, v235\n"
        "v_cvt_pk_bf16_f32 v234, v236, v237\n"
        "v_mfma_f32_16x16x32_bf16 v[230:233], v[132:135], a[124:127], v[230:233]\n"
        "s_nop 0\n"
        "v_cvt_pk_bf16_f32 v174, v122, v123\n"
        "v_cvt_pk_bf16_f32 v171, v124, v125\n"
        "v_mfma_f32_16x16x32_bf16 v[230:233], v[126:129], a[120:123], v[230:233]\n"
        "v_fmac_f32_dpp v253, v251, v252 quad_perm:[0,0,0,0] row_mask:0xf bank_mask:0xf\n"
        "v_and_b32_dpp v144, v250, v25 row_ror:8 row_mask:0xf bank_mask:0xf\n"
        "v_mfma_f32_16x16x32_bf16 v[240:243], v[126:129], a[132:135], 0\n"
        "s_nop 0\n"
        "v_and_b32_dpp v145, v248, v25 row_ror:8 row_mask:0xf bank_mask:0xf\n"
        "v_mov_b64 v[116:117], v[104:105]\n"
        "v_mfma_f32_16x16x32_bf16 v[240:243], v[132:135], a[136:139], v[240:243]\n"
        "s_nop 0\n"
        "v_cvt_pk_bf16_f32 v30, v230, v231\n"
        "v_cvt_pk_bf16_f32 v31, v232, v233\n"
        "v_mfma_f32_16x16x32_bf16 v[240:243], v[138:141], a[140:143], v[240:243]\n"
        "s_waitcnt lgkmcnt(0)\n"
        "v_cmp_ne_u32_e64 s[24:25], s29, v246\n"
        "v_mov_b64 v[122:123], v[104:105]\n"
        "v_mfma_f32_16x16x32_bf16 v[230:233], v[36:39], v[130:133], 0\n"
        "v_mov_b64 v[150:151], v[144:145]\n"
        "v_fmac_f32_dpp v253, v251, v247 quad_perm:[1,1,1,1] row_mask:0xf bank_mask:0xf\n"
        "v_mfma_f32_16x16x32_bf16 v[124:127], v[38:41], v[134:137], 0\n"
        "s_cmp_lg_u64 s[24:25], 0\n"
        "v_cvt_pk_bf16_f32 v34, v240, v241\n"
        "v_cvt_pk_bf16_f32 v35, v242, v243\n"
        "v_mfma_f32_16x16x32_bf16 v[128:131], v[118:121], a[80:83], 0\n"
        "s_cbranch_scc1 SP10_%=\n"
        "BK10_%=:\n"
        "v_mfma_f32_16x16x32_bf16 v[128:131], v[112:115], a[76:79], v[128:131]\n"
        "v_cvt_pk_bf16_f32 v235, v230, v231\n"
        "v_cvt_pk_bf16_f32 v175, v232, v233\n"
        "v_mfma_f32_16x16x32_bf16 v[128:131], v[106:109], a[72:75], v[128:131]\n"
        "v_cvt_pk_bf16_f32 v172, v124, v125\n"
        "v_cvt_pk_bf16_f32 v236, v126, v127\n"
        "v_mfma_f32_16x16x32_bf16 v[132:135], v[106:109], a[84:87], 0\n"
        "s_nop 0\n"
        "v_mov_b64 v[156:157], v[144:145]\n"
        "v_fmac_f32_dpp v253, v251, v245 quad_perm:[2,2,2,2] row_mask:0xf bank_mask:0xf\n"
        "v_mfma_f32_16x16x32_bf16 v[132:135], v[112:115], a[88:91], v[132:135]\n"
        "s_nop 0\n"
        "v_cvt_pk_bf16_f32 v42, v128, v129\n"
        "v_cvt_pk_bf16_f32 v43, v130, v131\n"
        "v_mfma_f32_16x16x32_bf16 v[132:135], v[118:121], a[92:95], v[132:135]\n"
        "v_mov_b64 v[162:163], v[144:145]\n"
        "v_fmac_f32_dpp v253, v251, v249 quad_perm:[3,3,3,3] row_mask:0xf bank_mask:0xf\n"
        "v_mfma_f32_16x16x32_bf16 v[128:131], v[30:33], v[136:139], 0\n"
        "s_nop 0\n"
        "v_cvt_pk_f16_f32 v126, v178, v179\n"
        "v_cvt_pk_f16_f32 v127, v180, v181\n"
        "v_mfma_f32_16x16x32_bf16 v[136:139], v[32:35], v[140:143], 0\n"
        "s_nop 0\n"
        "v_cvt_pk_bf16_f32 v46, v132, v133\n"
        "v_cvt_pk_bf16_f32 v47, v134, v135\n"
        "v_mfma_f32_16x16x32_bf16 v[132:135], v[118:121], a[104:107], 0\n"
        "v_cvt_pk_bf16_f32 v173, v130, v131\n"
        "v_cvt_pk_bf16_f32 v176, v128, v129\n"
        "v_mfma_f32_16x16x32_bf16 v[132:135], v[112:115], a[100:103], v[132:135]\n"
        "s_nop 0\n"
        "v_cvt_pk_bf16_f32 v237, v136, v137\n"
        "v_cvt_pk_bf16_f32 v177, v138, v139\n"
        "v_mfma_f32_16x16x32_bf16 v[132:135], v[106:109], a[96:99], v[132:135]\n"
        "s_nop 0\n"
        "ds_write_b128 v12, v[170:173]\n"
        "v_mfma_f32_16x16x32_bf16 v[138:141], v[106:109], a[108:111], 0\n"
        "s_nop 0\n"
        "v_pk_mul_f16 v126, v126, v164 op_sel_hi:[1,0]\n"
        "v_pk_mul_f16 v127, v127, v164 op_sel_hi:[1,0]\n"
        "v_mfma_f32_16x16x32_bf16 v[138:141], v[112:115], a[112:115], v[138:141]\n"
        "s_nop 0\n"
        "v_cvt_pk_bf16_f32 v36, v132, v133\n"
        "v_cvt_pk_bf16_f32 v37, v134, v135\n"
        "v_mfma_f32_16x16x32_bf16 v[138:141], v[118:121], a[116:119], v[138:141]\n"
        "s_nop 0\n"
        "ds_write_b128 v12, v[234:237] offset:1024\n"
        "v_mfma_f32_16x16x32_bf16 v[128:131], v[42:45], v[104:107], 0\n"
        "s_nop 0\n"
        "ds_write_b128 v12, v[174:177] offset:2048\n"
        "v_mfma_f32_16x16x32_bf16 v[178:181], v[44:47], v[108:111], 0\n"
        "s_nop 0\n"
        "v_pk_mul_f16 v126, v126, v194\n"
        "v_cvt_pk_bf16_f32 v40, v138, v139\n"
        "v_mfma_f32_16x16x32_bf16 v[136:139], v[170:173], v[170:173], 0\n"
        "v_cvt_pk_bf16_f32 v41, v140, v141\n"
        "v_pk_mul_f16 v127, v127, v195\n"
        "v_mfma_f32_16x16x32_bf16 v[136:139], v[234:237], v[234:237], v[136:139]\n"
        "v_exp_f16_sdwa v126, v126 dst_sel:WORD_0 dst_unused:UNUSED_PRESERVE src0_sel:WORD_0\n"
        "v_mfma_f32_16x16x32_bf16 v[136:139], v[174:177], v[174:177], v[136:139]\n"
        "v_exp_f16_sdwa v127, v127 dst_sel:WORD_0 dst_unused:UNUSED_PRESERVE src0_sel:WORD_0\n"
        "v_mfma_f32_16x16x32_bf16 v[132:135], v[118:121], a[128:131], 0\n"
        "v_cvt_pk_bf16_f32 v170, v128, v129\n"
        "v_cvt_pk_bf16_f32 v140, v130, v131\n"
        "v_mfma_f32_16x16x32_bf16 v[132:135], v[112:115], a[124:127], v[132:135]\n"
        "v_cvt_pk_bf16_f32 v130, v178, v179\n"
        "v_cvt_pk_bf16_f32 v171, v180, v181\n"
        "v_mfma_f32_16x16x32_bf16 v[132:135], v[106:109], a[120:123], v[132:135]\n"
        "s_nop 0\n"
        "v_exp_f16_sdwa v126, v126 dst_sel:WORD_1 dst_unused:UNUSED_PRESERVE src0_sel:WORD_1\n"
        "v_mfma_f32_16x16x32_bf16 v[172:175], v[106:109], a[132:135], 0\n"
        "s_nop 0\n"
        "v_exp_f16_sdwa v127, v127 dst_sel:WORD_1 dst_unused:UNUSED_PRESERVE src0_sel:WORD_1\n"
        "v_mfma_f32_16x16x32_bf16 v[172:175], v[112:115], a[136:139], v[172:175]\n"
        "s_nop 0\n"
        "v_mul_f32 v252, s80, v253\n"
        "v_cvt_pk_bf16_f32 v30, v132, v133\n"
        "v_mfma_f32_16x16x32_bf16 v[172:175], v[118:121], a[140:143], v[172:175]\n"
        "v_cvt_pk_bf16_f32 v31, v134, v135\n"
        "v_pk_add_f16 v226, v126, v127\n"
        "v_mfma_f32_16x16x32_bf16 v[132:135], v[36:39], v[110:113], 0\n"
        "s_nop 0\n"
        "v_cvt_pk_f16_f32 v128, v166, v167\n"
        "v_cvt_pk_f16_f32 v129, v168, v169\n"
        "v_mfma_f32_16x16x32_bf16 v[104:107], v[38:41], v[114:117], 0\n"
        "s_nop 0\n"
        "v_cvt_pk_bf16_f32 v34, v172, v173\n"
        "v_cvt_pk_bf16_f32 v35, v174, v175\n"
        "v_mfma_f32_16x16x32_bf16 v[108:111], v[158:161], a[80:83], 0\n"
        "v_cvt_pk_bf16_f32 v141, v132, v133\n"
        "v_cvt_pk_bf16_f32 v131, v134, v135\n"
        "v_mfma_f32_16x16x32_bf16 v[108:111], v[152:155], a[76:79], v[108:111]\n"
        "s_nop 0\n"
        "v_cvt_pk_bf16_f32 v172, v104, v105\n"
        "v_cvt_pk_bf16_f32 v142, v106, v107\n"
        "v_mfma_f32_16x16x32_bf16 v[108:111], v[146:149], a[72:75], v[108:111]\n"
        "v_pk_mul_f16 v128, v128, v164 op_sel_hi:[1,0]\n"
        "v_pk_mul_f16 v129, v129, v164 op_sel_hi:[1,0]\n"
        "v_mfma_f32_16x16x32_bf16 v[104:107], v[146:149], a[84:87], 0\n"
        "s_nop 0\n"
        "v_pk_mul_f16 v128, v128, v196\n"
        "v_pk_mul_f16 v129, v129, v197\n"
        "v_mfma_f32_16x16x32_bf16 v[104:107], v[152:155], a[88:91], v[104:107]\n"
        "s_nop 0\n"
        "v_cvt_pk_bf16_f32 v42, v108, v109\n"
        "v_cvt_pk_bf16_f32 v43, v110, v111\n"
        "v_mfma_f32_16x16x32_bf16 v[104:107], v[158:161], a[92:95], v[104:107]\n"
        "v_exp_f16_sdwa v128, v128 dst_sel:WORD_0 dst_unused:UNUSED_PRESERVE src0_sel:WORD_0\n"
        "v_mfma_f32_16x16x32_bf16 v[112:115], v[30:33], v[116:119], 0\n"
        "v_exp_f16_sdwa v129, v129 dst_sel:WORD_0 dst_unused:UNUSED_PRESERVE src0_sel:WORD_0\n"
        "v_mfma_f32_16x16x32_bf16 v[116:119], v[32:35], v[120:123], 0\n"
        "s_nop 0\n"
        "v_exp_f16_sdwa v128, v128 dst_sel:WORD_1 dst_unused:UNUSED_PRESERVE src0_sel:WORD_1\n"
        "v_mfma_f32_16x16x32_bf16 v[108:111], v[158:161], a[104:107], 0\n"
        "s_nop 0\n"
        "v_cvt_pk_bf16_f32 v46, v104, v105\n"
        "v_cvt_pk_bf16_f32 v47, v106, v107\n"
        "v_mfma_f32_16x16x32_bf16 v[108:111], v[152:155], a[100:103], v[108:111]\n"
        "v_cvt_pk_bf16_f32 v173, v114, v115\n"
        "v_cvt_pk_bf16_f32 v132, v112, v113\n"
        "v_mfma_f32_16x16x32_bf16 v[108:111], v[146:149], a[96:99], v[108:111]\n"
        "s_nop 0\n"
        "ds_write_b128 v12, v[170:173] offset:3072\n"
        "v_mfma_f32_16x16x32_bf16 v[112:115], v[146:149], a[108:111], 0\n"
        "s_nop 0\n"
        "v_cvt_pk_bf16_f32 v143, v116, v117\n"
        "v_cvt_pk_bf16_f32 v133, v118, v119\n"
        "v_mfma_f32_16x16x32_bf16 v[112:115], v[152:155], a[112:115], v[112:115]\n"
        "s_nop 0\n"
        "v_cvt_pk_bf16_f32 v36, v108, v109\n"
        "v_cvt_pk_bf16_f32 v37, v110, v111\n"
        "v_mfma_f32_16x16x32_bf16 v[112:115], v[158:161], a[116:119], v[112:115]\n"
        "ds_write_b128 v12, v[140:143] offset:4096\n"
        "v_mfma_f32_16x16x32_bf16 v[104:107], v[42:45], v[144:147], 0\n"
        "ds_write_b128 v12, v[130:133] offset:5120\n"
        "v_mfma_f32_16x16x32_bf16 v[108:111], v[44:47], v[148:151], 0\n"
        "s_nop 0\n"
        "v_exp_f16_sdwa v129, v129 dst_sel:WORD_1 dst_unused:UNUSED_PRESERVE src0_sel:WORD_1\n"
        "v_mfma_f32_16x16x32_bf16 v[116:119], v[170:173], v[170:173], 0\n"
        "s_nop 0\n"
        "v_cvt_pk_bf16_f32 v40, v112, v113\n"
        "v_cvt_pk_bf16_f32 v41, v114, v115\n"
        "v_mfma_f32_16x16x32_bf16 v[116:119], v[140:143], v[140:143], v[116:119]\n"
        "v_pk_add_f16 v226, v226, v128\n"
        "v_pk_add_f16 v226, v226, v129\n"
        "v_mfma_f32_16x16x32_bf16 v[116:119], v[130:133], v[130:133], v[116:119]\n"
        "v_cvt_pk_bf16_f32 v112, v104, v105\n"
        "v_cvt_pk_bf16_f32 v130, v106, v107\n"
        "v_mfma_f32_16x16x32_bf16 v[120:123], v[158:161], a[128:131], 0\n"
        "v_cvt_pk_bf16_f32 v106, v108, v109\n"
        "v_cvt_pk_bf16_f32 v113, v110, v111\n"
        "v_mfma_f32_16x16x32_bf16 v[120:123], v[152:155], a[124:127], v[120:123]\n"
        "v_cvt_pk_f16_f32 v104, v182, v183\n"
        "v_cvt_pk_f16_f32 v105, v184, v185\n"
        "v_mfma_f32_16x16x32_bf16 v[120:123], v[146:149], a[120:123], v[120:123]\n"
        "v_pk_mul_f16 v104, v104, v164 op_sel_hi:[1,0]\n"
        "v_pk_mul_f16 v105, v105, v164 op_sel_hi:[1,0]\n"
        "v_mfma_f32_16x16x32_bf16 v[140:143], v[146:149], a[132:135], 0\n"
        "s_nop 0\n"
        "v_pk_mul_f16 v104, v104, v238\n"
        "v_pk_mul_f16 v105, v105, v239\n"
        "v_mfma_f32_16x16x32_bf16 v[140:143], v[152:155], a[136:139], v[140:143]\n"
        "s_nop 0\n"
        "v_cvt_pk_bf16_f32 v30, v120, v121\n"
        "v_cvt_pk_bf16_f32 v31, v122, v123\n"
        "v_mfma_f32_16x16x32_bf16 v[140:143], v[158:161], a[140:143], v[140:143]\n"
        "v_cndmask_b32_e64 v250, v136, v137, s[56:57]\n"
        "v_cndmask_b32_e64 v250, v250, v138, s[58:59]\n"
        "v_mfma_f32_16x16x32_bf16 v[132:135], v[36:39], v[150:153], 0\n"
        "s_nop 0\n"
        "v_cndmask_b32_e64 v251, v116, v117, s[56:57]\n"
        "v_cndmask_b32_e64 v251, v251, v118, s[58:59]\n"
        "v_mfma_f32_16x16x32_bf16 v[108:111], v[38:41], v[154:157], 0\n"
        "s_nop 0\n"
        "v_cvt_pk_bf16_f32 v34, v140, v141\n"
        "v_cvt_pk_bf16_f32 v35, v142, v143\n"
        "v_mfma_f32_16x16x32_bf16 v[120:123], a[152:155], v[186:189], 0\n"
        "v_cvt_pk_bf16_f32 v131, v132, v133\n"
        "v_cvt_pk_bf16_f32 v107, v134, v135\n"
        "v_mfma_f32_16x16x32_bf16 v[120:123], a[148:151], v[202:205], v[120:123]\n"
        "s_nop 0\n"
        "v_cvt_pk_bf16_f32 v114, v108, v109\n"
        "v_cvt_pk_bf16_f32 v132, v110, v111\n"
        "v_mfma_f32_16x16x32_bf16 v[120:123], a[144:147], v[210:213], v[120:123]\n"
        "s_nop 0\n"
        "v_cndmask_b32_e64 v251, v251, v119, s[60:61]\n"
        "v_cndmask_b32_e64 v250, v250, v139, s[60:61]\n"
        "v_mfma_f32_16x16x32_bf16 v[116:119], v[30:33], v[156:159], 0\n"
        "s_nop 0\n"
        "v_exp_f16_sdwa v104, v104 dst_sel:WORD_0 dst_unused:UNUSED_PRESERVE src0_sel:WORD_0\n"
        "v_mfma_f32_16x16x32_bf16 v[40:43], v[32:35], v[160:163], 0\n"
        "s_nop 0\n"
        "v_cvt_pk_bf16_f32 v110, v120, v121\n"
        "v_cvt_pk_bf16_f32 v111, v122, v123\n"
        "v_mfma_f32_16x16x32_bf16 v[34:37], a[156:159], v[210:213], 0\n"
        "s_nop 0\n"
        "v_cvt_pk_bf16_f32 v115, v118, v119\n"
        "v_cvt_pk_bf16_f32 v108, v116, v117\n"
        "v_mfma_f32_16x16x32_bf16 v[34:37], a[160:163], v[202:205], v[34:37]\n"
        "v_cvt_pk_bf16_f32 v133, v40, v41\n"
        "v_cvt_pk_bf16_f32 v109, v42, v43\n"
        "v_mfma_f32_16x16x32_bf16 v[34:37], a[164:167], v[186:189], v[34:37]\n"
        "ds_write_b128 v12, v[112:115] offset:6144\n"
        "v_mfma_f32_16x16x32_bf16 v[40:43], a[176:179], v[186:189], 0\n"
        "v_exp_f16_sdwa v105, v105 dst_sel:WORD_0 dst_unused:UNUSED_PRESERVE src0_sel:WORD_0\n"
        "v_mfma_f32_16x16x32_bf16 v[40:43], a[172:175], v[202:205], v[40:43]\n"
        "v_exp_f16_sdwa v104, v104 dst_sel:WORD_1 dst_unused:UNUSED_PRESERVE src0_sel:WORD_1\n"
        "v_mfma_f32_16x16x32_bf16 v[40:43], a[168:171], v[210:213], v[40:43]\n"
        "s_nop 0\n"
        "ds_write_b128 v12, v[130:133] offset:7168\n"
        "v_mfma_f32_16x16x32_bf16 v[120:123], v[112:115], v[112:115], 0\n"
        "s_nop 0\n"
        "v_cvt_pk_bf16_f32 v112, v34, v35\n"
        "v_cvt_pk_bf16_f32 v113, v36, v37\n"
        "v_mfma_f32_16x16x32_bf16 v[120:123], v[130:133], v[130:133], v[120:123]\n"
        "s_nop 0\n"
        "v_cvt_pk_bf16_f32 v118, v40, v41\n"
        "v_cvt_pk_bf16_f32 v119, v42, v43\n"
        "v_mfma_f32_16x16x32_bf16 v[120:123], v[106:109], v[106:109], v[120:123]\n"
        "ds_write_b128 v12, v[106:109] offset:8192\n"
        "v_mfma_f32_16x16x32_bf16 v[34:37], a[188:191], v[186:189], 0\n"
        "s_cmp_le_i32 s85, 0\n"
        "v_exp_f16_sdwa v105, v105 dst_sel:WORD_1 dst_unused:UNUSED_PRESERVE src0_sel:WORD_1\n"
        "v_mfma_f32_16x16x32_bf16 v[34:37], a[184:187], v[202:205], v[34:37]\n"
        "s_cbranch_scc1 DY3_%=\n"
        "DY3B_%=:\n"
        "v_mfma_f32_16x16x32_bf16 v[34:37], a[180:183], v[210:213], v[34:37]\n"
        "s_nop 0\n"
        "v_cndmask_b32_e64 v249, v120, v121, s[56:57]\n"
        "v_cndmask_b32_e64 v249, v249, v122, s[58:59]\n"
        "v_mfma_f32_16x16x32_bf16 v[40:43], a[192:195], v[210:213], 0\n"
        "v_cndmask_b32_e64 v249, v249, v123, s[60:61]\n"
        "v_pk_add_f16 v226, v226, v104\n"
        "v_mfma_f32_16x16x32_bf16 v[40:43], a[196:199], v[202:205], v[40:43]\n"
        "s_nop 0\n"
        "v_cvt_pk_bf16_f32 v120, v34, v35\n"
        "v_cvt_pk_bf16_f32 v121, v36, v37\n"
        "v_mfma_f32_16x16x32_bf16 v[40:43], a[200:203], v[186:189], v[40:43]\n"
        "v_cndmask_b32_dpp v251, v249, v251, vcc row_ror:4 row_mask:0xf bank_mask:0xf\n"
        "v_pk_add_f16 v226, v226, v105\n"
        "v_mfma_f32_16x16x32_bf16 v[34:37], a[212:215], v[186:189], 0\n"
        "s_nop 0\n"
        "v_mov_b32 v249, s29\n"
        "v_cndmask_b32_dpp v250, v251, v250, vcc row_ror:4 row_mask:0xf bank_mask:0xf\n"
        "v_mfma_f32_16x16x32_bf16 v[34:37], a[208:211], v[202:205], v[34:37]\n"
        "s_nop 0\n"
        "v_cvt_pk_bf16_f32 v130, v40, v41\n"
        "v_cvt_pk_bf16_f32 v131, v42, v43\n"
        "v_mfma_f32_16x16x32_bf16 v[34:37], a[204:207], v[210:213], v[34:37]\n"
        "v_fma_f32 v252, -v252, v253, v250\n"
        "v_max_f32 v252, 0, v252\n"
        "v_mfma_f32_16x16x32_bf16 v[40:43], a[144:147], v[198:201], 0\n"
        "s_nop 0\n"
        "v_sub_f32 v250, 0x47c00000, v250\n"
        "v_fmaak_f32 v252, v28, v252, 0x3727c5ac\n"
        "v_mfma_f32_16x16x32_bf16 v[40:43], a[148:151], v[206:209], v[40:43]\n"
        "s_nop 0\n"
        "v_cvt_pk_bf16_f32 v132, v34, v35\n"
        "v_cvt_pk_bf16_f32 v133, v36, v37\n"
        "v_mfma_f32_16x16x32_bf16 v[40:43], a[152:155], v[222:225], v[40:43]\n"
        "s_nop 0\n"
        "v_bfi_b32 v252, s84, v250, v252\n"
        "v_mfma_f32_16x16x32_bf16 v[114:117], v[190:193], v[110:113], 0\n"
        "s_nop 0\n"
        "v_rsq_f32 v253, v252\n"
        "v_mfma_f32_16x16x32_bf16 v[114:117], v[214:217], v[118:121], v[114:117]\n"
        "s_nop 0\n"
        "v_cvt_f16_f32 v253, v253\n"
        "v_cvt_pk_bf16_f32 v34, v40, v41\n"
        "v_mfma_f32_16x16x32_bf16 v[114:117], v[218:221], v[130:133], v[114:117]\n"
        "ds_write_b16 v18, v253\n"
        "v_mfma_f32_16x16x32_bf16 v[122:125], v[186:189], v[130:133], 0\n"
        "ds_write_b16 v19, v253\n"
        "v_mfma_f32_16x16x32_bf16 v[122:125], v[202:205], v[118:121], v[122:125]\n"
        "s_nop 0\n"
        "ds_write_b16 v22, v249 offset:3072\n"
        "v_mfma_f32_16x16x32_bf16 v[122:125], v[210:213], v[110:113], v[122:125]\n"
        "s_nop 0\n"
        "v_cvt_pk_f16_f32 v108, v114, v115\n"
        "v_cvt_pk_f16_f32 v109, v116, v117\n"
        "v_mfma_f32_16x16x32_bf16 v[114:117], v[198:201], v[110:113], 0\n"
        "v_pk_mul_f16 v108, v108, v164 op_sel:[0,1]\n"
        "v_pk_mul_f16 v109, v109, v164 op_sel:[0,1]\n"
        "v_mfma_f32_16x16x32_bf16 v[114:117], v[206:209], v[118:121], v[114:117]\n"
        "v_pk_mul_f16 v108, v108, v194\n"
        "v_pk_mul_f16 v109, v109, v195\n"
        "v_mfma_f32_16x16x32_bf16 v[114:117], v[222:225], v[130:133], v[114:117]\n"
        "v_exp_f16_sdwa v108, v108 dst_sel:WORD_0 dst_unused:UNUSED_PRESERVE src0_sel:WORD_0\n"
        "v_mfma_f32_16x16x32_bf16 v[118:121], a[164:167], v[222:225], 0\n"
        "v_exp_f16_sdwa v109, v109 dst_sel:WORD_0 dst_unused:UNUSED_PRESERVE src0_sel:WORD_0\n"
        "v_mfma_f32_16x16x32_bf16 v[118:121], a[160:163], v[206:209], v[118:121]\n"
        "v_exp_f16_sdwa v108, v108 dst_sel:WORD_1 dst_unused:UNUSED_PRESERVE src0_sel:WORD_1\n"
        "v_mfma_f32_16x16x32_bf16 v[118:121], a[156:159], v[198:201], v[118:121]\n"
        "v_exp_f16_sdwa v109, v109 dst_sel:WORD_1 dst_unused:UNUSED_PRESERVE src0_sel:WORD_1\n"
        "v_mfma_f32_16x16x32_bf16 v[138:141], a[168:171], v[198:201], 0\n"
        "v_cvt_pk_bf16_f32 v35, v42, v43\n"
        "v_pk_add_f16 v227, v108, v109\n"
        "v_mfma_f32_16x16x32_bf16 v[138:141], a[172:175], v[206:209], v[138:141]\n"
        "v_cvt_pk_f16_f32 v110, v122, v123\n"
        "v_cvt_pk_f16_f32 v111, v124, v125\n"
        "v_mfma_f32_16x16x32_bf16 v[138:141], a[176:179], v[222:225], v[138:141]\n"
        "v_pk_mul_f16 v110, v110, v164 op_sel:[0,1]\n"
        "v_pk_mul_f16 v111, v111, v164 op_sel:[0,1]\n"
        "v_mfma_f32_16x16x32_bf16 v[134:137], a[188:191], v[222:225], 0\n"
        "v_pk_mul_f16 v110, v110, v196\n"
        "v_pk_mul_f16 v111, v111, v197\n"
        "v_mfma_f32_16x16x32_bf16 v[134:137], a[184:187], v[206:209], v[134:137]\n"
        "v_exp_f16_sdwa v110, v110 dst_sel:WORD_0 dst_unused:UNUSED_PRESERVE src0_sel:WORD_0\n"
        "v_mfma_f32_16x16x32_bf16 v[134:137], a[180:183], v[198:201], v[134:137]\n"
        "v_exp_f16_sdwa v111, v111 dst_sel:WORD_0 dst_unused:UNUSED_PRESERVE src0_sel:WORD_0\n"
        "v_mfma_f32_16x16x32_bf16 v[130:133], a[192:195], v[198:201], 0\n"
        "v_exp_f16_sdwa v110, v110 dst_sel:WORD_1 dst_unused:UNUSED_PRESERVE src0_sel:WORD_1\n"
        "v_mfma_f32_16x16x32_bf16 v[130:133], a[196:199], v[206:209], v[130:133]\n"
        "v_cvt_pk_bf16_f32 v36, v118, v119\n"
        "v_cvt_pk_bf16_f32 v37, v120, v121\n"
        "v_mfma_f32_16x16x32_bf16 v[130:133], a[200:203], v[222:225], v[130:133]\n"
        "v_cvt_pk_bf16_f32 v122, v138, v139\n"
        "v_cvt_pk_bf16_f32 v123, v140, v141\n"
        "v_mfma_f32_16x16x32_bf16 v[40:43], a[212:215], v[222:225], 0\n"
        "s_nop 0\n"
        "v_cvt_pk_bf16_f32 v124, v134, v135\n"
        "v_cvt_pk_bf16_f32 v125, v136, v137\n"
        "v_mfma_f32_16x16x32_bf16 v[40:43], a[208:211], v[206:209], v[40:43]\n"
        "s_nop 0\n"
        "v_cvt_pk_bf16_f32 v134, v130, v131\n"
        "v_cvt_pk_bf16_f32 v135, v132, v133\n"
        "v_mfma_f32_16x16x32_bf16 v[40:43], a[204:207], v[198:201], v[40:43]\n"
        "s_nop 0\n"
        "v_exp_f16_sdwa v111, v111 dst_sel:WORD_1 dst_unused:UNUSED_PRESERVE src0_sel:WORD_1\n"
        "v_mfma_f32_16x16x32_bf16 v[118:121], v[190:193], a[216:219], 0\n"
        "s_nop 0\n"
        "v_pk_add_f16 v227, v227, v110\n"
        "v_pk_add_f16 v227, v227, v111\n"
        "v_mfma_f32_16x16x32_bf16 v[118:121], v[214:217], a[220:223], v[118:121]\n"
        "s_nop 0\n"
        "v_cvt_pk_bf16_f32 v136, v40, v41\n"
        "v_cvt_pk_bf16_f32 v137, v42, v43\n"
        "v_mfma_f32_16x16x32_bf16 v[118:121], v[218:221], a[224:227], v[118:121]\n"
        "v_cvt_pk_f16_f32 v112, v114, v115\n"
        "v_cvt_pk_f16_f32 v113, v116, v117\n"
        "v_mfma_f32_16x16x32_bf16 v[130:133], v[186:189], a[224:227], 0\n"
        "v_pk_mul_f16 v112, v112, v164 op_sel:[0,1]\n"
        "v_pk_mul_f16 v113, v113, v164 op_sel:[0,1]\n"
        "v_mfma_f32_16x16x32_bf16 v[130:133], v[202:205], a[220:223], v[130:133]\n"
        "v_pk_mul_f16 v112, v112, v238\n"
        "v_pk_mul_f16 v113, v113, v239\n"
        "v_mfma_f32_16x16x32_bf16 v[130:133], v[210:213], a[216:219], v[130:133]\n"
        "v_exp_f16_sdwa v112, v112 dst_sel:WORD_0 dst_unused:UNUSED_PRESERVE src0_sel:WORD_0\n"
        "v_mfma_f32_16x16x32_bf16 v[40:43], v[198:201], a[216:219], 0\n"
        "v_exp_f16_sdwa v113, v113 dst_sel:WORD_0 dst_unused:UNUSED_PRESERVE src0_sel:WORD_0\n"
        "v_mfma_f32_16x16x32_bf16 v[40:43], v[206:209], a[220:223], v[40:43]\n"
        "v_exp_f16_sdwa v112, v112 dst_sel:WORD_1 dst_unused:UNUSED_PRESERVE src0_sel:WORD_1\n"
        "v_mfma_f32_16x16x32_bf16 v[40:43], v[222:225], a[224:227], v[40:43]\n"
        "v_exp_f16_sdwa v113, v113 dst_sel:WORD_1 dst_unused:UNUSED_PRESERVE src0_sel:WORD_1\n"
        "v_mfma_f32_16x16x32_bf16 v[114:117], v[190:193], v[34:37], 0\n"
        "v_pk_add_f16 v227, v227, v112\n"
        "v_pk_add_f16 v227, v227, v113\n"
        "v_mfma_f32_16x16x32_bf16 v[114:117], v[214:217], v[122:125], v[114:117]\n"
        "v_cvt_pk_f16_f32 v182, v118, v119\n"
        "v_cvt_pk_f16_f32 v183, v120, v121\n"
        "v_mfma_f32_16x16x32_bf16 v[114:117], v[218:221], v[134:137], v[114:117]\n"
        "v_cvt_pk_f16_f32 v184, v130, v131\n"
        "v_cvt_pk_f16_f32 v185, v132, v133\n"
        "v_mfma_f32_16x16x32_bf16 v[118:121], v[186:189], v[134:137], 0\n"
        "s_nop 0\n"
        "v_cvt_pk_f16_f32 v178, v40, v41\n"
        "v_cvt_pk_f16_f32 v179, v42, v43\n"
        "v_mfma_f32_16x16x32_bf16 v[118:121], v[202:205], v[122:125], v[118:121]\n"
        "s_nop 0\n"
        "v_cvt_pk_f16_f32 v230, v114, v115\n"
        "v_cvt_pk_f16_f32 v231, v116, v117\n"
        "v_mfma_f32_16x16x32_bf16 v[118:121], v[210:213], v[34:37], v[118:121]\n"
        "v_pk_mul_f16 v230, v230, v165 op_sel_hi:[1,0]\n"
        "v_pk_mul_f16 v231, v231, v165 op_sel_hi:[1,0]\n"
        "v_mfma_f32_16x16x32_bf16 v[40:43], v[198:201], v[34:37], 0\n"
        "v_pk_mul_f16 v230, v230, v194\n"
        "v_pk_mul_f16 v231, v231, v195\n"
        "v_mfma_f32_16x16x32_bf16 v[40:43], v[206:209], v[122:125], v[40:43]\n"
        "v_exp_f16_sdwa v230, v230 dst_sel:WORD_0 dst_unused:UNUSED_PRESERVE src0_sel:WORD_0\n"
        "v_mfma_f32_16x16x32_bf16 v[40:43], v[222:225], v[134:137], v[40:43]\n"
        "v_exp_f16_sdwa v231, v231 dst_sel:WORD_0 dst_unused:UNUSED_PRESERVE src0_sel:WORD_0\n"
        "v_mfma_f32_16x16x32_bf16 v[34:37], v[222:225], a[236:239], 0\n"
        "v_exp_f16_sdwa v230, v230 dst_sel:WORD_1 dst_unused:UNUSED_PRESERVE src0_sel:WORD_1\n"
        "v_mfma_f32_16x16x32_bf16 v[34:37], v[206:209], a[232:235], v[34:37]\n"
        "v_exp_f16_sdwa v231, v231 dst_sel:WORD_1 dst_unused:UNUSED_PRESERVE src0_sel:WORD_1\n"
        "v_mfma_f32_16x16x32_bf16 v[34:37], v[198:201], a[228:231], v[34:37]\n"
        "v_pk_add_f16 v228, v230, v231\n"
        "v_cvt_pk_f16_f32 v232, v118, v119\n"
        "v_mfma_f32_16x16x32_bf16 v[130:133], v[210:213], a[228:231], 0\n"
        "v_cndmask_b32_e64 v106, 0, v254, s[62:63]\n"
        "v_cndmask_b32_e64 v114, 0, v254, s[64:65]\n"
        "v_mfma_f32_16x16x32_bf16 v[130:133], v[202:205], a[232:235], v[130:133]\n"
        "v_cndmask_b32_e64 v236, 0, v254, s[66:67]\n"
        "v_pk_add_f16 v226, v226, v106\n"
        "v_mfma_f32_16x16x32_bf16 v[130:133], v[186:189], a[236:239], v[130:133]\n"
        "v_pk_add_f16 v227, v227, v114\n"
        "v_cvt_pk_f16_f32 v233, v120, v121\n"
        "v_mfma_f32_16x16x32_bf16 v[116:119], v[218:221], a[236:239], 0\n"
        "v_pk_mul_f16 v232, v232, v165 op_sel_hi:[1,0]\n"
        "v_pk_mul_f16 v233, v233, v165 op_sel_hi:[1,0]\n"
        "v_mfma_f32_16x16x32_bf16 v[116:119], v[214:217], a[232:235], v[116:119]\n"
        "v_pk_mul_f16 v232, v232, v196\n"
        "v_pk_mul_f16 v233, v233, v197\n"
        "v_mfma_f32_16x16x32_bf16 v[116:119], v[190:193], a[228:231], v[116:119]\n"
        "v_exp_f16_sdwa v232, v232 dst_sel:WORD_0 dst_unused:UNUSED_PRESERVE src0_sel:WORD_0\n"
        "v_mfma_f32_16x16x32_bf16 v[154:157], v[190:193], a[240:243], 0\n"
        "v_exp_f16_sdwa v233, v233 dst_sel:WORD_0 dst_unused:UNUSED_PRESERVE src0_sel:WORD_0\n"
        "v_mfma_f32_16x16x32_bf16 v[154:157], v[214:217], a[244:247], v[154:157]\n"
        "v_exp_f16_sdwa v232, v232 dst_sel:WORD_1 dst_unused:UNUSED_PRESERVE src0_sel:WORD_1\n"
        "v_mfma_f32_16x16x32_bf16 v[154:157], v[218:221], a[248:251], v[154:157]\n"
        "v_exp_f16_sdwa v233, v233 dst_sel:WORD_1 dst_unused:UNUSED_PRESERVE src0_sel:WORD_1\n"
        "v_mfma_f32_16x16x32_bf16 v[120:123], v[186:189], a[248:251], 0\n"
        "v_pk_add_f16 v228, v228, v232\n"
        "v_pk_add_f16 v228, v228, v233\n"
        "v_mfma_f32_16x16x32_bf16 v[120:123], v[202:205], a[244:247], v[120:123]\n"
        "v_cvt_pk_f16_f32 v234, v40, v41\n"
        "v_cvt_pk_f16_f32 v235, v42, v43\n"
        "v_mfma_f32_16x16x32_bf16 v[120:123], v[210:213], a[240:243], v[120:123]\n"
        "v_pk_mul_f16 v234, v234, v165 op_sel_hi:[1,0]\n"
        "v_pk_mul_f16 v235, v235, v165 op_sel_hi:[1,0]\n"
        "v_mfma_f32_16x16x32_bf16 v[150:153], v[198:201], a[240:243], 0\n"
        "v_pk_mul_f16 v234, v234, v238\n"
        "v_pk_mul_f16 v235, v235, v239\n"
        "v_mfma_f32_16x16x32_bf16 v[150:153], v[206:209], a[244:247], v[150:153]\n"
        "v_exp_f16_sdwa v234, v234 dst_sel:WORD_0 dst_unused:UNUSED_PRESERVE src0_sel:WORD_0\n"
        "v_mfma_f32_16x16x32_bf16 v[150:153], v[222:225], a[248:251], v[150:153]\n"
        "v_exp_f16_sdwa v235, v235 dst_sel:WORD_0 dst_unused:UNUSED_PRESERVE src0_sel:WORD_0\n"
        "v_mfma_f32_16x16x32_bf16 v[134:137], v[222:225], v[64:67], 0\n"
        "v_exp_f16_sdwa v234, v234 dst_sel:WORD_1 dst_unused:UNUSED_PRESERVE src0_sel:WORD_1\n"
        "v_mfma_f32_16x16x32_bf16 v[134:137], v[206:209], v[72:75], v[134:137]\n"
        "v_exp_f16_sdwa v235, v235 dst_sel:WORD_1 dst_unused:UNUSED_PRESERVE src0_sel:WORD_1\n"
        "v_mfma_f32_16x16x32_bf16 v[134:137], v[198:201], a[252:255], v[134:137]\n"
        "v_pk_add_f16 v228, v228, v234\n"
        "v_pk_add_f16 v228, v228, v235\n"
        "v_mfma_f32_16x16x32_bf16 v[146:149], v[210:213], a[252:255], 0\n"
        "v_pk_add_f16 v228, v228, v236\n"
        "v_cvt_pk_f16_f32 v170, v34, v35\n"
        "v_mfma_f32_16x16x32_bf16 v[146:149], v[202:205], v[72:75], v[146:149]\n"
        "ds_read2_b32 v[180:181], v24 offset0:0 offset1:0\n"
        "ds_read2_b32 v[172:173], v24 offset0:1 offset1:1\n"
        "v_mfma_f32_16x16x32_bf16 v[146:149], v[186:189], v[64:67], v[146:149]\n"
        "ds_read2_b32 v[144:145], v24 offset0:2 offset1:2\n"
        "ds_read2_b32 v[176:177], v24 offset0:3 offset1:3\n"
        "v_mfma_f32_16x16x32_bf16 v[138:141], v[218:221], v[64:67], 0\n"
        "ds_read2_b32 v[124:125], v24 offset0:4 offset1:4\n"
        "v_mov_b32 v107, 0\n"
        "v_mfma_f32_16x16x32_bf16 v[138:141], v[214:217], v[72:75], v[138:141]\n"
        "ds_read2_b32 v[42:43], v24 offset0:5 offset1:5\n"
        "v_cvt_pk_f16_f32 v171, v36, v37\n"
        "v_mfma_f32_16x16x32_bf16 v[138:141], v[190:193], a[252:255], v[138:141]\n"
        "v_cvt_pk_f16_f32 v36, v130, v131\n"
        "v_cvt_pk_f16_f32 v37, v132, v133\n"
        "v_mfma_f32_16x16x32_f16 v[246:249], v[80:83], v[226:229], 0\n"
        "v_cvt_pk_f16_f32 v34, v116, v117\n"
        "v_cvt_pk_f16_f32 v35, v118, v119\n"
        "v_mfma_f32_16x16x32_bf16 v[158:161], v[190:193], v[68:71], 0\n"
        "v_cvt_pk_f16_f32 v162, v154, v155\n"
        "v_cvt_pk_f16_f32 v163, v156, v157\n"
        "v_mfma_f32_16x16x32_bf16 v[158:161], v[214:217], v[60:63], v[158:161]\n"
        "v_cvt_pk_f16_f32 v164, v120, v121\n"
        "v_cvt_pk_f16_f32 v165, v122, v123\n"
        "v_mfma_f32_16x16x32_bf16 v[158:161], v[218:221], v[56:59], v[158:161]\n"
        "v_cvt_pk_f16_f32 v142, v150, v151\n"
        "v_cvt_pk_f16_f32 v143, v152, v153\n"
        "v_mfma_f32_16x16x32_bf16 v[120:123], v[186:189], v[56:59], 0\n"
        "v_cvt_pk_f16_f32 v174, v134, v135\n"
        "v_cvt_pk_f16_f32 v175, v136, v137\n"
        "v_mfma_f32_16x16x32_bf16 v[120:123], v[202:205], v[60:63], v[120:123]\n"
        "v_cvt_pk_f16_f32 v168, v146, v147\n"
        "v_cvt_pk_f16_f32 v169, v148, v149\n"
        "v_mfma_f32_16x16x32_bf16 v[120:123], v[210:213], v[68:71], v[120:123]\n"
        "s_waitcnt lgkmcnt(0)\n"
        "v_rcp_f32 v246, v246\n"
        "v_mfma_f32_16x16x32_bf16 v[134:137], v[198:201], v[68:71], 0\n"
        "v_rcp_f32 v247, v247\n"
        "v_mfma_f32_16x16x32_bf16 v[134:137], v[206:209], v[60:63], v[134:137]\n"
        "v_cvt_pk_f16_f32 v254, v246, v247\n"
        "v_pk_mul_f16 v126, v126, v254 op_sel_hi:[1,0]\n"
        "v_mfma_f32_16x16x32_bf16 v[134:137], v[222:225], v[56:59], v[134:137]\n"
        "v_pk_mul_f16 v127, v127, v254 op_sel_hi:[1,0]\n"
        "v_pk_mul_f16 v128, v128, v254 op_sel_hi:[1,0]\n"
        "v_mfma_f32_16x16x32_bf16 v[116:119], v[222:225], v[84:87], 0\n"
        "v_pk_mul_f16 v129, v129, v254 op_sel_hi:[1,0]\n"
        "v_pk_mul_f16 v104, v104, v254 op_sel_hi:[1,0]\n"
        "v_mfma_f32_16x16x32_bf16 v[116:119], v[206:209], v[76:79], v[116:119]\n"
        "v_pk_mul_f16 v105, v105, v254 op_sel_hi:[1,0]\n"
        "v_pk_mul_f16 v106, v106, v254 op_sel_hi:[1,0]\n"
        "v_mfma_f32_16x16x32_bf16 v[116:119], v[198:201], v[52:55], v[116:119]\n"
        "v_pk_mul_f16 v126, v126, v194\n"
        "v_pk_mul_f16 v127, v127, v195\n"
        "v_mfma_f32_16x16x32_bf16 v[130:133], v[210:213], v[52:55], 0\n"
        "v_pk_mul_f16 v128, v128, v196\n"
        "v_pk_mul_f16 v129, v129, v197\n"
        "v_mfma_f32_16x16x32_bf16 v[130:133], v[202:205], v[76:79], v[130:133]\n"
        "v_pk_mul_f16 v104, v104, v238\n"
        "v_pk_mul_f16 v105, v105, v239\n"
        "v_mfma_f32_16x16x32_bf16 v[130:133], v[186:189], v[84:87], v[130:133]\n"
        "v_mov_b32 v115, 0\n"
        "v_cvt_pk_f16_f32 v166, v138, v139\n"
        "v_mfma_f32_16x16x32_bf16 v[146:149], v[218:221], v[84:87], 0\n"
        "v_cvt_pk_f16_f32 v167, v140, v141\n"
        "v_add_f32_dpp v249, v249, v249 row_ror:8 row_mask:0xf bank_mask:0xf\n"
        "v_mfma_f32_16x16x32_bf16 v[146:149], v[214:217], v[76:79], v[146:149]\n"
        "v_rcp_f32 v248, v248\n"
        "v_mfma_f32_16x16x32_bf16 v[146:149], v[190:193], v[52:55], v[146:149]\n"
        "v_add_f32_dpp v249, v249, v249 row_ror:4 row_mask:0xf bank_mask:0xf\n"
        "v_pk_mul_f16 v108, v108, v254 op_sel:[0,1]\n"
        "v_mfma_f32_16x16x32_f16 v[150:153], v[126:129], v[182:185], 0\n"
        "v_pk_mul_f16 v109, v109, v254 op_sel:[0,1]\n"
        "v_pk_mul_f16 v110, v110, v254 op_sel:[0,1]\n"
        "v_mfma_f32_16x16x32_f16 v[150:153], v[104:107], v[178:181], v[150:153]\n"
        "v_pk_mul_f16 v111, v111, v254 op_sel:[0,1]\n"
        "v_pk_mul_f16 v112, v112, v254 op_sel:[0,1]\n"
        "v_mfma_f32_16x16x32_bf16 v[150:153], v[190:193], v[88:91], v[150:153]\n"
        "v_pk_mul_f16 v113, v113, v254 op_sel:[0,1]\n"
        "v_pk_mul_f16 v114, v114, v254 op_sel:[0,1]\n"
        "v_mfma_f32_16x16x32_bf16 v[138:141], v[190:193], v[48:51], 0\n"
        "v_pk_sub_u16 v253, v254, s83\n"
        "v_pk_mul_f16 v108, v108, v194\n"
        "v_mfma_f32_16x16x32_f16 v[138:141], v[104:107], v[170:173], v[138:141]\n"
        "v_pk_mul_f16 v109, v109, v195\n"
        "v_pk_mul_f16 v110, v110, v196\n"
        "v_mfma_f32_16x16x32_f16 v[138:141], v[126:129], v[34:37], v[138:141]\n"
        "v_pk_mul_f16 v111, v111, v197\n"
        "v_cvt_pk_f16_f32 v244, v158, v159\n"
        "v_mfma_f32_16x16x32_f16 v[154:157], v[126:129], v[162:165], 0\n"
        "v_cvt_pk_f16_f32 v245, v160, v161\n"
        "v_cvt_pk_f16_f32 v246, v120, v121\n"
        "v_mfma_f32_16x16x32_f16 v[154:157], v[104:107], v[142:145], v[154:157]\n"
        "v_cvt_pk_f16_f32 v247, v122, v123\n"
        "v_cvt_pk_f16_f32 v122, v134, v135\n"
        "v_mfma_f32_16x16x32_bf16 v[154:157], v[214:217], v[88:91], v[154:157]\n"
        "v_cvt_pk_f16_f32 v123, v136, v137\n"
        "v_pk_mul_f16 v112, v112, v238\n"
        "v_mfma_f32_16x16x32_bf16 v[134:137], v[214:217], v[48:51], 0\n"
        "v_add_f32_dpp v249, v249, v249 row_ror:2 row_mask:0xf bank_mask:0xf\n"
        "v_cvt_pk_f16_f32 v40, v116, v117\n"
        "v_mfma_f32_16x16x32_f16 v[134:137], v[104:107], v[174:177], v[134:137]\n"
        "v_cvt_pk_f16_f32 v41, v118, v119\n"
        "v_cvt_pk_f16_f32 v242, v130, v131\n"
        "v_mfma_f32_16x16x32_f16 v[134:137], v[126:129], v[166:169], v[134:137]\n"
        "v_cvt_pk_f16_f32 v243, v132, v133\n"
        "v_cvt_pk_f16_f32 v240, v146, v147\n"
        "v_mfma_f32_16x16x32_f16 v[116:119], v[126:129], v[244:247], 0\n"
        "v_cvt_pk_f16_f32 v241, v148, v149\n"
        "v_pk_mul_f16 v113, v113, v239\n"
        "v_mfma_f32_16x16x32_f16 v[116:119], v[104:107], v[122:125], v[116:119]\n"
        "v_add_f32_dpp v249, v249, v249 row_ror:1 row_mask:0xf bank_mask:0xf\n"
        "v_cvt_pk_bf16_f32 v158, v150, v138\n"
        "v_mfma_f32_16x16x32_bf16 v[116:119], v[218:221], v[88:91], v[116:119]\n"
        "v_rcp_f32 v249, v249\n"
        "v_mfma_f32_16x16x32_bf16 v[130:133], v[218:221], v[48:51], 0\n"
        "v_cvt_pk_f16_f32 v254, v249, v248\n"
        "v_mul_f16 v229, v229, v254\n"
        "v_mfma_f32_16x16x32_f16 v[130:133], v[104:107], v[40:43], v[130:133]\n"
        "v_pk_sub_u16 v252, v254, s83\n"
        "v_mul_f16 v229, v229, v255\n"
        "v_mfma_f32_16x16x32_f16 v[130:133], v[126:129], v[240:243], v[130:133]\n"
        "s_add_u32 s90, s89, 0\n"
        "ds_write_b16 v22, v229 offset:512\n"
        "v_mfma_f32_16x16x32_f16 v[104:107], v[108:111], v[182:185], 0\n"
        "v_bitop3_b32 v253, v253, v252, s82 bitop3:0xa8\n"
        "v_cmp_ne_u32_e64 s[74:75], 0, v253\n"
        "v_mfma_f32_16x16x32_f16 v[104:107], v[112:115], v[178:181], v[104:107]\n"
        "s_nop 0\n"
        "v_cvt_pk_bf16_f32 v159, v154, v134\n"
        "v_cvt_pk_bf16_f32 v160, v116, v130\n"
        "v_mfma_f32_16x16x32_bf16 v[104:107], v[210:213], v[88:91], v[104:107]\n"
        "s_cmp_lg_u64 s[74:75], 0\n"
        "v_cvt_pk_bf16_f32 v226, v151, v139\n"
        "v_cvt_pk_bf16_f32 v227, v155, v135\n"
        "v_mfma_f32_16x16x32_bf16 v[126:129], v[210:213], v[48:51], 0\n"
        "s_cbranch_scc1 SLOW_%=\n"
        "v_mfma_f32_16x16x32_f16 v[126:129], v[112:115], v[170:173], v[126:129]\n"
        "v_cvt_pk_bf16_f32 v228, v117, v131\n"
        "v_mov_b32 v237, 0\n"
        "v_mfma_f32_16x16x32_f16 v[126:129], v[108:111], v[34:37], v[126:129]\n"
        "s_add_u32 s91, s89, 98112\n"
        "v_cvt_pk_bf16_f32 v150, v152, v140\n"
        "v_cvt_pk_bf16_f32 v151, v156, v136\n"
        "v_mfma_f32_16x16x32_f16 v[146:149], v[108:111], v[162:165], 0\n"
        "v_cvt_pk_bf16_f32 v152, v118, v132\n"
        "v_pk_mul_f16 v230, v230, v254 op_sel:[0,1]\n"
        "v_mfma_f32_16x16x32_f16 v[146:149], v[112:115], v[142:145], v[146:149]\n"
        "v_cvt_pk_bf16_f32 v138, v153, v141\n"
        "v_cvt_pk_bf16_f32 v139, v157, v137\n"
        "v_mfma_f32_16x16x32_bf16 v[146:149], v[202:205], v[88:91], v[146:149]\n"
        "buffer_store_dwordx3 v[158:160], v14, s[40:43], s90 offen sc1 nt\n"
        "v_cvt_pk_bf16_f32 v140, v119, v133\n"
        "v_pk_mul_f16 v231, v231, v254 op_sel:[0,1]\n"
        "v_pk_mul_f16 v232, v232, v254 op_sel:[0,1]\n"
        "v_mfma_f32_16x16x32_bf16 v[130:133], v[202:205], v[48:51], 0\n"
        "v_pk_mul_f16 v233, v233, v254 op_sel:[0,1]\n"
        "v_pk_mul_f16 v234, v234, v254 op_sel:[0,1]\n"
        "v_mfma_f32_16x16x32_f16 v[130:133], v[112:115], v[174:177], v[130:133]\n"
        "v_pk_mul_f16 v235, v235, v254 op_sel:[0,1]\n"
        "v_pk_mul_f16 v236, v236, v254 op_sel:[0,1]\n"
        "v_mfma_f32_16x16x32_f16 v[130:133], v[108:111], v[166:169], v[130:133]\n"
        "buffer_store_dwordx3 v[226:228], v15, s[40:43], s90 offen offset:768 sc1 nt\n"
        "v_pk_mul_f16 v230, v230, v194\n"
        "v_pk_mul_f16 v231, v231, v195\n"
        "v_pk_mul_f16 v232, v232, v196\n"
        "v_mfma_f32_16x16x32_f16 v[134:137], v[108:111], v[244:247], 0\n"
        "v_pk_mul_f16 v233, v233, v197\n"
        "v_pk_mul_f16 v234, v234, v238\n"
        "v_mfma_f32_16x16x32_f16 v[134:137], v[112:115], v[122:125], v[134:137]\n"
        "v_pk_mul_f16 v235, v235, v239\n"
        "v_cvt_pk_bf16_f32 v154, v104, v126\n"
        "v_mfma_f32_16x16x32_bf16 v[134:137], v[186:189], v[88:91], v[134:137]\n"
        "buffer_store_dwordx3 v[150:152], v14, s[40:43], s91 offen offset:192 sc1 nt\n"
        "v_cvt_pk_bf16_f32 v194, v105, v127\n"
        "v_cvt_pk_bf16_f32 v212, v106, v128\n"
        "v_cvt_pk_bf16_f32 v155, v146, v130\n"
        "v_mfma_f32_16x16x32_bf16 v[116:119], v[186:189], v[48:51], 0\n"
        "v_cvt_pk_bf16_f32 v195, v147, v131\n"
        "v_cvt_pk_bf16_f32 v213, v148, v132\n"
        "v_mfma_f32_16x16x32_f16 v[116:119], v[112:115], v[40:43], v[116:119]\n"
        "v_cvt_pk_bf16_f32 v202, v107, v129\n"
        "v_cvt_pk_bf16_f32 v203, v149, v133\n"
        "v_mfma_f32_16x16x32_f16 v[116:119], v[108:111], v[240:243], v[116:119]\n"
        "buffer_store_dwordx3 v[138:140], v16, s[40:43], s91 offen offset:960 sc1 nt\n"
        "s_nop 0\n"
        "v_mfma_f32_16x16x32_f16 v[146:149], v[230:233], v[182:185], 0\n"
        "s_nop 2\n"
        "v_mfma_f32_16x16x32_f16 v[146:149], v[234:237], v[178:181], v[146:149]\n"
        "s_waitcnt vmcnt(4)\n"
        "v_cvt_pk_bf16_f32 v156, v134, v116\n"
        "v_cvt_pk_bf16_f32 v196, v135, v117\n"
        "v_mfma_f32_16x16x32_bf16 v[146:149], v[198:201], v[88:91], v[146:149]\n"
        "s_add_u32 s90, s89, 196224\n"
        "ds_read_b128 v[126:129], v9\n"
        "v_cvt_pk_bf16_f32 v214, v136, v118\n"
        "v_mfma_f32_16x16x32_bf16 v[150:153], v[198:201], v[48:51], 0\n"
        "ds_read_b128 v[132:135], v10\n"
        "v_cvt_pk_bf16_f32 v204, v137, v119\n"
        "v_mfma_f32_16x16x32_f16 v[150:153], v[234:237], v[170:173], v[150:153]\n"
        "ds_read_b128 v[138:141], v11\n"
        "v_mfma_f32_16x16x32_f16 v[150:153], v[230:233], v[34:37], v[150:153]\n"
        "s_add_u32 s91, s89, 294336\n"
        "ds_read_b128 v[106:109], v9 offset:3072\n"
        "v_mfma_f32_16x16x32_f16 v[186:189], v[230:233], v[162:165], 0\n"
        "s_nop 0\n"
        "ds_read_b128 v[112:115], v10 offset:3072\n"
        "v_mfma_f32_16x16x32_f16 v[186:189], v[234:237], v[142:145], v[186:189]\n"
        "s_nop 0\n"
        "ds_read_b128 v[118:121], v11 offset:3072\n"
        "v_cvt_pk_bf16_f32 v200, v146, v150\n"
        "v_mfma_f32_16x16x32_bf16 v[186:189], v[206:209], v[88:91], v[186:189]\n"
        "buffer_store_dwordx3 v[154:156], v14, s[40:43], s90 offen offset:384 sc1 nt\n"
        "v_cvt_pk_bf16_f32 v218, v147, v151\n"
        "v_cvt_pk_bf16_f32 v210, v148, v152\n"
        "v_cvt_pk_bf16_f32 v248, v149, v153\n"
        "v_mfma_f32_16x16x32_bf16 v[190:193], v[206:209], v[48:51], 0\n"
        "ds_read_b128 v[146:149], v9 offset:6144\n"
        "ds_read_b128 v[152:155], v10 offset:6144\n"
        "v_mfma_f32_16x16x32_f16 v[190:193], v[234:237], v[174:177], v[190:193]\n"
        "ds_read_b128 v[158:161], v11 offset:6144\n"
        "v_mfma_f32_16x16x32_f16 v[190:193], v[230:233], v[166:169], v[190:193]\n"
        "buffer_store_dwordx3 v[194:196], v17, s[40:43], s90 offen offset:1152 sc1 nt\n"
        "s_add_u32 s90, s89, 392448\n"
        "s_nop 0\n"
        "v_mfma_f32_16x16x32_f16 v[196:199], v[230:233], v[244:247], 0\n"
        "s_waitcnt lgkmcnt(6)\n"
        "s_nop 0\n"
        "v_mfma_f32_16x16x32_f16 v[196:199], v[234:237], v[122:125], v[196:199]\n"
        "s_cmp_ge_i32 s85, 0\n"
        "v_cvt_pk_bf16_f32 v201, v186, v190\n"
        "v_cvt_pk_bf16_f32 v219, v187, v191\n"
        "v_mfma_f32_16x16x32_bf16 v[196:199], v[222:225], v[88:91], v[196:199]\n"
        "buffer_store_dwordx3 v[212:214], v14, s[40:43], s91 offen offset:576 sc1 nt\n"
        "v_cvt_pk_bf16_f32 v211, v188, v192\n"
        "v_cvt_pk_bf16_f32 v249, v189, v193\n"
        "v_mfma_f32_16x16x32_bf16 v[186:189], v[222:225], v[48:51], 0\n"
        "v_mfma_f32_16x16x32_f16 v[186:189], v[234:237], v[40:43], v[186:189]\n"
        "v_mfma_f32_16x16x32_f16 v[186:189], v[230:233], v[240:243], v[186:189]\n"
        "buffer_store_dwordx3 v[202:204], v14, s[40:43], s90 offen sc1 nt\n"
        "s_cbranch_scc1 TOP_%=\n"
        "LAST_%=:\n"
        "s_waitcnt lgkmcnt(0)\n"
        "s_nop 7\n"
        "s_mov_b32 s28, s89\n"
        "s_add_u32 s26, s28, 392448\n"
        "v_cvt_pk_bf16_f32 v202, v196, v186\n"
        "buffer_store_dwordx3 v[200:202], v15, s[40:43], s26 offen offset:768 sc1 nt\n"
        "s_add_u32 s27, s28, 490560\n"
        "v_cvt_pk_bf16_f32 v220, v197, v187\n"
        "buffer_store_dwordx3 v[218:220], v14, s[40:43], s27 offen offset:192 sc1 nt\n"
        "v_cvt_pk_bf16_f32 v212, v198, v188\n"
        "buffer_store_dwordx3 v[210:212], v16, s[40:43], s27 offen offset:960 sc1 nt\n"
        "s_add_u32 s26, s28, 588672\n"
        "v_cvt_pk_bf16_f32 v250, v199, v189\n"
        "buffer_store_dwordx3 v[248:250], v14, s[40:43], s26 offen offset:384 sc1 nt\n"
        "ds_read_u16 v255, v19 offset:2048\n"
        "s_waitcnt lgkmcnt(0)\n"
        "s_mov_b32 s30, s29\n"
        "v_cmp_ne_u32_e64 s[24:25], s30, v255\n"
        "s_nop 3\n"
        "s_cmp_lg_u64 s[24:25], 0\n"
        "s_cbranch_scc1 SP20_%=\n"
        "BK20_%=:\n"
        "ds_read_b128 v[190:193], v13\n"
        "ds_read_b128 v[214:217], v13 offset:1024\n"
        "ds_read_b128 v[218:221], v13 offset:2048\n"
        "ds_read_b128 v[210:213], v13 offset:3072\n"
        "ds_read_b128 v[202:205], v13 offset:4096\n"
        "ds_read_b128 v[186:189], v13 offset:5120\n"
        "ds_read_b128 v[198:201], v13 offset:6144\n"
        "ds_read_b128 v[206:209], v13 offset:7168\n"
        "ds_read_b128 v[222:225], v13 offset:8192\n"
        "ds_read_b128 v[104:107], v20\n"
        "ds_read_b64 v[30:31], v20 offset:16\n"
        "ds_read_u16 v255, v22\n"
        "ds_read_b64 v[46:47], v21\n"
        "v_mov_b32 v26, 0\n"
        "s_mov_b64 exec, 1\n"
        "buffer_atomic_add v26, off, s[16:19], s15 offset:4 sc0\n"
        "s_mov_b64 exec, -1\n"
        "s_waitcnt lgkmcnt(10)\n"
        "v_mfma_f32_16x16x32_bf16 v[112:115], a[152:155], v[218:221], 0\n"
        "s_add_u32 s89, s86, s51\n"
        "ds_read_b128 v[138:141], v20 offset:512\n"
        "v_add_u32 v23, s22, v23\n"
        "v_mfma_f32_16x16x32_bf16 v[112:115], a[148:151], v[214:217], v[112:115]\n"
        "s_add_u32 s29, s29, 1\n"
        "ds_read_b128 v[194:197], v20 offset:528\n"
        "v_add_u32 v24, s22, v24\n"
        "v_mfma_f32_16x16x32_bf16 v[112:115], a[144:147], v[190:193], v[112:115]\n"
        "s_mov_b32 s22, s23\n"
        "ds_read_b128 v[154:157], v23\n"
        "v_lshlrev_b32 v134, 16, v180\n"
        "v_mfma_f32_16x16x32_bf16 v[108:111], a[164:167], v[218:221], 0\n"
        "s_movk_i32 s23, 1584\n"
        "ds_read_b128 v[130:133], v23 offset:64\n"
        "v_lshlrev_b32 v135, 16, v172\n"
        "v_mfma_f32_16x16x32_bf16 v[108:111], a[160:163], v[214:217], v[108:111]\n"
        "s_mov_b32 s86, s87\n"
        "ds_read_b128 v[116:119], v23 offset:128\n"
        "v_lshlrev_b32 v136, 16, v144\n"
        "v_mfma_f32_16x16x32_bf16 v[108:111], a[156:159], v[190:193], v[108:111]\n"
        "s_add_u32 s88, s88, 32\n"
        "ds_read_b64 v[234:235], v24 offset:360\n"
        "v_lshlrev_b32 v137, 16, v176\n"
        "v_mfma_f32_16x16x32_bf16 v[150:153], a[168:171], v[190:193], 0\n"
        "s_add_u32 s87, s87, 43008\n"
        "v_lshlrev_b32 v120, 16, v124\n"
        "v_lshlrev_b32 v121, 16, v42\n"
        "v_mfma_f32_16x16x32_bf16 v[150:153], a[172:175], v[214:217], v[150:153]\n"
        "s_cmp_ge_u32 s88, 73\n"
        "v_cvt_pk_bf16_f32 v126, v112, v113\n"
        "v_cvt_pk_bf16_f32 v127, v114, v115\n"
        "v_mfma_f32_16x16x32_bf16 v[150:153], a[176:179], v[218:221], v[150:153]\n"
        "s_cselect_b32 s92, 588672, 0\n"
        "v_cvt_pk_bf16_f32 v128, v108, v109\n"
        "v_cvt_pk_bf16_f32 v129, v110, v111\n"
        "v_mfma_f32_16x16x32_bf16 v[226:229], a[188:191], v[218:221], 0\n"
        "s_waitcnt lgkmcnt(4)\n"
        "v_pk_mul_f16 v110, v138, v182\n"
        "v_pk_mul_f16 v111, v138, v34\n"
        "v_mfma_f32_16x16x32_bf16 v[226:229], a[184:187], v[214:217], v[226:229]\n"
        "s_cselect_b32 s94, 73, 0\n"
        "v_pk_mul_f16 v112, v138, v162\n"
        "v_pk_mul_f16 v113, v138, v166\n"
        "v_mfma_f32_16x16x32_bf16 v[226:229], a[180:183], v[190:193], v[226:229]\n"
        "s_sub_u32 s88, s88, s94\n"
        "v_pk_mul_f16 v114, v138, v244\n"
        "v_pk_mul_f16 v115, v138, v240\n"
        "v_mfma_f32_16x16x32_bf16 v[146:149], a[192:195], v[190:193], 0\n"
        "s_add_u32 s87, s87, s92\n"
        "v_pk_fma_f16 v110, v139, v183, v110\n"
        "v_pk_fma_f16 v111, v139, v35, v111\n"
        "v_mfma_f32_16x16x32_bf16 v[146:149], a[196:199], v[214:217], v[146:149]\n"
        "s_waitcnt lgkmcnt(0)\n"
        "v_pk_fma_f16 v112, v139, v163, v112\n"
        "v_pk_fma_f16 v113, v139, v167, v113\n"
        "v_mfma_f32_16x16x32_bf16 v[146:149], a[200:203], v[218:221], v[146:149]\n"
        "s_cmp_gt_i32 s85, 0\n"
        "v_pk_fma_f16 v114, v139, v245, v114\n"
        "v_pk_fma_f16 v115, v139, v241, v115\n"
        "v_mfma_f32_16x16x32_bf16 v[158:161], a[212:215], v[218:221], 0\n"
        "s_cselect_b32 s94, s87, 0\n"
        "v_readlane_b32 s93, v235, 1\n"
        "v_cndmask_b32_e64 v234, 0, v234, s[72:73]\n"
        "v_mfma_f32_16x16x32_bf16 v[158:161], a[208:211], v[214:217], v[158:161]\n"
        "s_sub_u32 s85, s85, 1\n"
        "v_pk_fma_f16 v110, v140, v184, v110\n"
        "v_pk_fma_f16 v111, v140, v36, v111\n"
        "v_mfma_f32_16x16x32_bf16 v[158:161], a[204:207], v[190:193], v[158:161]\n"
        "s_waitcnt lgkmcnt(0)\n"
        "v_pk_fma_f16 v112, v140, v164, v112\n"
        "v_pk_fma_f16 v113, v140, v168, v113\n"
        "v_mfma_f32_16x16x32_bf16 v[230:233], v[154:157], v[190:193], 0\n"
        "s_waitcnt lgkmcnt(0)\n"
        "v_pk_fma_f16 v114, v140, v246, v114\n"
        "v_pk_fma_f16 v115, v140, v242, v115\n"
        "v_mfma_f32_16x16x32_bf16 v[230:233], v[130:133], v[214:217], v[230:233]\n"
        "v_pk_fma_f16 v110, v141, v185, v110\n"
        "v_pk_fma_f16 v111, v141, v37, v111\n"
        "v_mfma_f32_16x16x32_bf16 v[230:233], v[116:119], v[218:221], v[230:233]\n"
        "v_pk_fma_f16 v112, v141, v165, v112\n"
        "v_pk_fma_f16 v113, v141, v169, v113\n"
        "v_mfma_f32_16x16x32_bf16 v[162:165], v[116:119], v[186:189], 0\n"
        "v_pk_fma_f16 v114, v141, v247, v114\n"
        "v_pk_fma_f16 v115, v141, v243, v115\n"
        "v_mfma_f32_16x16x32_bf16 v[162:165], v[130:133], v[202:205], v[162:165]\n"
        "v_pk_fma_f16 v110, v194, v178, v110\n"
        "v_pk_fma_f16 v111, v194, v170, v111\n"
        "v_mfma_f32_16x16x32_bf16 v[162:165], v[154:157], v[210:213], v[162:165]\n"
        "v_pk_fma_f16 v112, v194, v142, v112\n"
        "v_pk_fma_f16 v113, v194, v174, v113\n"
        "v_mfma_f32_16x16x32_bf16 v[138:141], v[154:157], v[198:201], 0\n"
        "v_pk_fma_f16 v114, v194, v122, v114\n"
        "v_pk_fma_f16 v115, v194, v40, v115\n"
        "v_mfma_f32_16x16x32_bf16 v[138:141], v[130:133], v[206:209], v[138:141]\n"
        "v_pk_fma_f16 v110, v195, v179, v110\n"
        "v_pk_fma_f16 v111, v195, v171, v111\n"
        "v_mfma_f32_16x16x32_bf16 v[138:141], v[116:119], v[222:225], v[138:141]\n"
        "v_pk_fma_f16 v112, v195, v143, v112\n"
        "v_pk_fma_f16 v113, v195, v175, v113\n"
        "v_mfma_f32_16x16x32_bf16 v[130:133], v[190:193], v[126:129], 0\n"
        "v_pk_fma_f16 v114, v195, v123, v114\n"
        "v_pk_fma_f16 v115, v195, v41, v115\n"
        "v_cvt_pk_bf16_f32 v34, v150, v151\n"
        "v_cvt_pk_bf16_f32 v35, v152, v153\n"
        "v_cvt_pk_bf16_f32 v36, v226, v227\n"
        "v_cvt_pk_bf16_f32 v37, v228, v229\n"
        "s_nop 1\n"
        "v_mfma_f32_16x16x32_bf16 v[130:133], v[214:217], v[34:37], v[130:133]\n"
        "v_pk_fma_f16 v110, v196, v180, v110\n"
        "v_pk_fma_f16 v111, v196, v172, v111\n"
        "v_cvt_pk_bf16_f32 v116, v146, v147\n"
        "v_cvt_pk_bf16_f32 v117, v148, v149\n"
        "v_cvt_pk_bf16_f32 v118, v158, v159\n"
        "v_cvt_pk_bf16_f32 v119, v160, v161\n"
        "s_nop 1\n"
        "v_mfma_f32_16x16x32_bf16 v[130:133], v[218:221], v[116:119], v[130:133]\n"
        "v_pk_fma_f16 v112, v196, v144, v112\n"
        "v_pk_fma_f16 v113, v196, v176, v113\n"
        "v_mfma_f32_16x16x32_bf16 v[140:143], v[186:189], v[116:119], 0\n"
        "v_pk_fma_f16 v114, v196, v124, v114\n"
        "v_pk_fma_f16 v115, v196, v42, v115\n"
        "v_mfma_f32_16x16x32_bf16 v[140:143], v[202:205], v[34:37], v[140:143]\n"
        "v_cndmask_b32_e64 v253, v230, v162, s[64:65]\n"
        "v_cndmask_b32_e64 v254, v231, v163, s[64:65]\n"
        "v_mfma_f32_16x16x32_bf16 v[140:143], v[210:213], v[126:129], v[140:143]\n"
        "v_cndmask_b32_e64 v253, v253, v138, s[70:71]\n"
        "v_cndmask_b32_e64 v254, v254, v139, s[70:71]\n"
        "v_mfma_f32_16x16x32_bf16 v[122:125], v[198:201], v[126:129], 0\n"
        "v_cvt_pk_f16_f32 v253, v253, v254\n"
        "v_pk_mul_f16 v253, v253, v255 op_sel_hi:[1,0]\n"
        "v_mfma_f32_16x16x32_bf16 v[122:125], v[206:209], v[34:37], v[122:125]\n"
        "s_add_u32 s46, s28, 590016\n"
        "v_pk_mul_f16 v253, v253, s93 op_sel_hi:[1,0]\n"
        "v_cvt_pk_f16_f32 v108, v130, v131\n"
        "v_mfma_f32_16x16x32_bf16 v[122:125], v[222:225], v[116:119], v[122:125]\n"
        "v_exp_f16_sdwa v254, v253 dst_sel:WORD_1 dst_unused:UNUSED_PAD src0_sel:WORD_1\n"
        "v_mfma_f32_16x16x32_f16 v[134:137], v[80:83], v[110:113], v[134:137]\n"
        "s_min_u32 s46, s46, 200540928\n"
        "v_exp_f16_sdwa v43, v253 dst_sel:WORD_0 dst_unused:UNUSED_PAD src0_sel:WORD_0\n"
        "v_mfma_f32_16x16x32_f16 v[118:121], v[80:83], v[112:115], v[118:121]\n"
        "v_cndmask_b32_e64 v43, v43, v234, s[68:69]\n"
        "v_cvt_pk_f16_f32 v109, v132, v133\n"
        "v_mfma_f32_16x16x32_bf16 v[130:133], a[152:155], v[186:189], 0\n"
        "v_pk_mul_f16 v108, v108, v46 op_sel_hi:[1,0]\n"
        "v_pk_mul_f16 v109, v109, v46 op_sel_hi:[1,0]\n"
        "v_mfma_f32_16x16x32_bf16 v[130:133], a[148:151], v[202:205], v[130:133]\n"
        "v_pk_mul_f16 v108, v108, v104\n"
        "v_pk_mul_f16 v109, v109, v105\n"
        "v_mfma_f32_16x16x32_bf16 v[130:133], a[144:147], v[210:213], v[130:133]\n"
        "v_exp_f16_sdwa v108, v108 dst_sel:WORD_0 dst_unused:UNUSED_PRESERVE src0_sel:WORD_0\n"
        "v_mfma_f32_16x16x32_bf16 v[112:115], a[156:159], v[210:213], 0\n"
        "v_exp_f16_sdwa v109, v109 dst_sel:WORD_0 dst_unused:UNUSED_PRESERVE src0_sel:WORD_0\n"
        "v_mfma_f32_16x16x32_bf16 v[112:115], a[160:163], v[202:205], v[112:115]\n"
        "v_exp_f16_sdwa v108, v108 dst_sel:WORD_1 dst_unused:UNUSED_PRESERVE src0_sel:WORD_1\n"
        "v_mfma_f32_16x16x32_bf16 v[112:115], a[164:167], v[186:189], v[112:115]\n"
        "v_exp_f16_sdwa v109, v109 dst_sel:WORD_1 dst_unused:UNUSED_PRESERVE src0_sel:WORD_1\n"
        "v_mfma_f32_16x16x32_bf16 v[126:129], a[176:179], v[186:189], 0\n"
        "v_pk_add_f16 v40, v108, v109\n"
        "v_cvt_pk_bf16_f32 v34, v134, v135\n"
        "v_mfma_f32_16x16x32_bf16 v[126:129], a[172:175], v[202:205], v[126:129]\n"
        "v_cvt_pk_bf16_f32 v35, v136, v137\n"
        "v_cvt_pk_bf16_f32 v36, v120, v121\n"
        "v_mfma_f32_16x16x32_bf16 v[126:129], a[168:171], v[210:213], v[126:129]\n"
        "buffer_store_dwordx3 v[34:36], v17, s[44:47], s26 offen offset:1152 sc1 nt\n"
        "v_cvt_pk_f16_f32 v110, v140, v141\n"
        "v_cvt_pk_f16_f32 v111, v142, v143\n"
        "v_pk_mul_f16 v110, v110, v46 op_sel_hi:[1,0]\n"
        "v_mfma_f32_16x16x32_bf16 v[116:119], a[188:191], v[186:189], 0\n"
        "v_pk_mul_f16 v111, v111, v46 op_sel_hi:[1,0]\n"
        "v_pk_mul_f16 v110, v110, v106\n"
        "v_mfma_f32_16x16x32_bf16 v[116:119], a[184:187], v[202:205], v[116:119]\n"
        "v_pk_mul_f16 v111, v111, v107\n"
        "v_cvt_pk_f16_f32 v34, v122, v123\n"
        "v_mfma_f32_16x16x32_bf16 v[116:119], a[180:183], v[210:213], v[116:119]\n"
        "v_exp_f16_sdwa v110, v110 dst_sel:WORD_0 dst_unused:UNUSED_PRESERVE src0_sel:WORD_0\n"
        "v_mfma_f32_16x16x32_bf16 v[138:141], a[192:195], v[210:213], 0\n"
        "v_exp_f16_sdwa v111, v111 dst_sel:WORD_0 dst_unused:UNUSED_PRESERVE src0_sel:WORD_0\n"
        "v_mfma_f32_16x16x32_bf16 v[138:141], a[196:199], v[202:205], v[138:141]\n"
        "v_exp_f16_sdwa v110, v110 dst_sel:WORD_1 dst_unused:UNUSED_PRESERVE src0_sel:WORD_1\n"
        "v_mfma_f32_16x16x32_bf16 v[138:141], a[200:203], v[186:189], v[138:141]\n"
        "v_exp_f16_sdwa v111, v111 dst_sel:WORD_1 dst_unused:UNUSED_PRESERVE src0_sel:WORD_1\n"
        "v_mfma_f32_16x16x32_bf16 v[120:123], a[212:215], v[186:189], 0\n"
        "v_pk_add_f16 v40, v40, v110\n"
        "v_pk_add_f16 v40, v40, v111\n"
        "v_mfma_f32_16x16x32_bf16 v[120:123], a[208:211], v[202:205], v[120:123]\n"
        "v_cvt_pk_f16_f32 v35, v124, v125\n"
        "v_pk_mul_f16 v34, v34, v46 op_sel_hi:[1,0]\n"
        "v_mfma_f32_16x16x32_bf16 v[120:123], a[204:207], v[210:213], v[120:123]\n"
        "v_pk_mul_f16 v35, v35, v46 op_sel_hi:[1,0]\n"
        "v_pk_mul_f16 v34, v34, v30\n"
        "v_mfma_f32_16x16x32_bf16 v[142:145], v[190:193], a[216:219], 0\n"
        "v_pk_mul_f16 v35, v35, v31\n"
        "v_cvt_pk_bf16_f32 v134, v130, v131\n"
        "v_mfma_f32_16x16x32_bf16 v[142:145], v[214:217], a[220:223], v[142:145]\n"
        "v_exp_f16_sdwa v34, v34 dst_sel:WORD_0 dst_unused:UNUSED_PRESERVE src0_sel:WORD_0\n"
        "v_mfma_f32_16x16x32_bf16 v[142:145], v[218:221], a[224:227], v[142:145]\n"
        "v_exp_f16_sdwa v35, v35 dst_sel:WORD_0 dst_unused:UNUSED_PRESERVE src0_sel:WORD_0\n"
        "v_mfma_f32_16x16x32_bf16 v[146:149], v[186:189], a[224:227], 0\n"
        "v_exp_f16_sdwa v34, v34 dst_sel:WORD_1 dst_unused:UNUSED_PRESERVE src0_sel:WORD_1\n"
        "v_mfma_f32_16x16x32_bf16 v[146:149], v[202:205], a[220:223], v[146:149]\n"
        "v_exp_f16_sdwa v35, v35 dst_sel:WORD_1 dst_unused:UNUSED_PRESERVE src0_sel:WORD_1\n"
        "v_mfma_f32_16x16x32_bf16 v[146:149], v[210:213], a[216:219], v[146:149]\n"
        "v_pk_add_f16 v40, v40, v34\n"
        "v_pk_add_f16 v40, v40, v35\n"
        "v_mfma_f32_16x16x32_bf16 v[150:153], v[198:201], a[216:219], 0\n"
        "v_cvt_pk_bf16_f32 v135, v132, v133\n"
        "v_cvt_pk_bf16_f32 v136, v112, v113\n"
        "v_mfma_f32_16x16x32_bf16 v[150:153], v[206:209], a[220:223], v[150:153]\n"
        "v_cvt_pk_bf16_f32 v137, v114, v115\n"
        "v_cvt_pk_bf16_f32 v130, v126, v127\n"
        "v_mfma_f32_16x16x32_bf16 v[150:153], v[222:225], a[224:227], v[150:153]\n"
        "v_cvt_pk_bf16_f32 v131, v128, v129\n"
        "v_cvt_pk_bf16_f32 v132, v116, v117\n"
        "v_mfma_f32_16x16x32_bf16 v[154:157], v[190:193], v[134:137], 0\n"
        "v_cvt_pk_bf16_f32 v133, v118, v119\n"
        "v_cvt_pk_bf16_f32 v124, v138, v139\n"
        "s_nop 0\n"
        "v_mfma_f32_16x16x32_bf16 v[154:157], v[214:217], v[130:133], v[154:157]\n"
        "v_cvt_pk_bf16_f32 v125, v140, v141\n"
        "v_cvt_pk_bf16_f32 v126, v120, v121\n"
        "v_cvt_pk_bf16_f32 v127, v122, v123\n"
        "s_nop 1\n"
        "v_mfma_f32_16x16x32_bf16 v[154:157], v[218:221], v[124:127], v[154:157]\n"
        "v_cvt_pk_f16_f32 v120, v142, v143\n"
        "v_cvt_pk_f16_f32 v121, v144, v145\n"
        "v_mfma_f32_16x16x32_bf16 v[142:145], v[186:189], v[124:127], 0\n"
        "v_cvt_pk_f16_f32 v122, v146, v147\n"
        "v_cvt_pk_f16_f32 v123, v148, v149\n"
        "v_mfma_f32_16x16x32_bf16 v[142:145], v[202:205], v[130:133], v[142:145]\n"
        "s_nop 0\n"
        "v_cvt_pk_f16_f32 v112, v150, v151\n"
        "v_cvt_pk_f16_f32 v116, v154, v155\n"
        "v_mfma_f32_16x16x32_bf16 v[142:145], v[210:213], v[134:137], v[142:145]\n"
        "v_cvt_pk_f16_f32 v117, v156, v157\n"
        "v_pk_mul_f16 v116, v116, v46 op_sel:[0,1]\n"
        "v_mfma_f32_16x16x32_bf16 v[138:141], v[198:201], v[134:137], 0\n"
        "v_pk_mul_f16 v117, v117, v46 op_sel:[0,1]\n"
        "v_pk_mul_f16 v116, v116, v104\n"
        "v_mfma_f32_16x16x32_bf16 v[138:141], v[206:209], v[130:133], v[138:141]\n"
        "v_pk_mul_f16 v117, v117, v105\n"
        "v_cvt_pk_f16_f32 v113, v152, v153\n"
        "v_mfma_f32_16x16x32_bf16 v[138:141], v[222:225], v[124:127], v[138:141]\n"
        "v_exp_f16_sdwa v116, v116 dst_sel:WORD_0 dst_unused:UNUSED_PRESERVE src0_sel:WORD_0\n"
        "v_mfma_f32_16x16x32_bf16 v[130:133], a[144:147], v[198:201], 0\n"
        "v_exp_f16_sdwa v117, v117 dst_sel:WORD_0 dst_unused:UNUSED_PRESERVE src0_sel:WORD_0\n"
        "v_mfma_f32_16x16x32_bf16 v[130:133], a[148:151], v[206:209], v[130:133]\n"
        "v_exp_f16_sdwa v116, v116 dst_sel:WORD_1 dst_unused:UNUSED_PRESERVE src0_sel:WORD_1\n"
        "v_mfma_f32_16x16x32_bf16 v[130:133], a[152:155], v[222:225], v[130:133]\n"
        "v_exp_f16_sdwa v117, v117 dst_sel:WORD_1 dst_unused:UNUSED_PRESERVE src0_sel:WORD_1\n"
        "v_mfma_f32_16x16x32_bf16 v[124:127], a[164:167], v[222:225], 0\n"
        "v_pk_add_f16 v41, v116, v117\n"
        "v_cvt_pk_f16_f32 v118, v142, v143\n"
        "v_mfma_f32_16x16x32_bf16 v[124:127], a[160:163], v[206:209], v[124:127]\n"
        "v_cvt_pk_f16_f32 v119, v144, v145\n"
        "v_pk_mul_f16 v118, v118, v46 op_sel:[0,1]\n"
        "v_mfma_f32_16x16x32_bf16 v[124:127], a[156:159], v[198:201], v[124:127]\n"
        "v_pk_mul_f16 v119, v119, v46 op_sel:[0,1]\n"
        "v_pk_mul_f16 v118, v118, v106\n"
        "v_mfma_f32_16x16x32_bf16 v[134:137], a[168:171], v[198:201], 0\n"
        "v_pk_mul_f16 v119, v119, v107\n"
        "v_cvt_pk_f16_f32 v128, v138, v139\n"
        "v_mfma_f32_16x16x32_bf16 v[134:137], a[172:175], v[206:209], v[134:137]\n"
        "v_exp_f16_sdwa v118, v118 dst_sel:WORD_0 dst_unused:UNUSED_PRESERVE src0_sel:WORD_0\n"
        "v_mfma_f32_16x16x32_bf16 v[134:137], a[176:179], v[222:225], v[134:137]\n"
        "v_exp_f16_sdwa v119, v119 dst_sel:WORD_0 dst_unused:UNUSED_PRESERVE src0_sel:WORD_0\n"
        "v_mfma_f32_16x16x32_bf16 v[148:151], a[188:191], v[222:225], 0\n"
        "v_exp_f16_sdwa v118, v118 dst_sel:WORD_1 dst_unused:UNUSED_PRESERVE src0_sel:WORD_1\n"
        "v_mfma_f32_16x16x32_bf16 v[148:151], a[184:187], v[206:209], v[148:151]\n"
        "v_exp_f16_sdwa v119, v119 dst_sel:WORD_1 dst_unused:UNUSED_PRESERVE src0_sel:WORD_1\n"
        "v_mfma_f32_16x16x32_bf16 v[148:151], a[180:183], v[198:201], v[148:151]\n"
        "v_pk_add_f16 v41, v41, v118\n"
        "v_pk_add_f16 v41, v41, v119\n"
        "v_mfma_f32_16x16x32_bf16 v[156:159], a[192:195], v[198:201], 0\n"
        "v_cvt_pk_f16_f32 v129, v140, v141\n"
        "v_pk_mul_f16 v128, v128, v46 op_sel:[0,1]\n"
        "v_mfma_f32_16x16x32_bf16 v[156:159], a[196:199], v[206:209], v[156:159]\n"
        "v_pk_mul_f16 v129, v129, v46 op_sel:[0,1]\n"
        "v_pk_mul_f16 v128, v128, v30\n"
        "v_mfma_f32_16x16x32_bf16 v[156:159], a[200:203], v[222:225], v[156:159]\n"
        "v_pk_mul_f16 v129, v129, v31\n"
        "v_cvt_pk_bf16_f32 v144, v130, v131\n"
        "v_mfma_f32_16x16x32_bf16 v[138:141], a[212:215], v[222:225], 0\n"
        "v_exp_f16_sdwa v128, v128 dst_sel:WORD_0 dst_unused:UNUSED_PRESERVE src0_sel:WORD_0\n"
        "v_mfma_f32_16x16x32_bf16 v[138:141], a[208:211], v[206:209], v[138:141]\n"
        "v_exp_f16_sdwa v129, v129 dst_sel:WORD_0 dst_unused:UNUSED_PRESERVE src0_sel:WORD_0\n"
        "v_mfma_f32_16x16x32_bf16 v[138:141], a[204:207], v[198:201], v[138:141]\n"
        "v_exp_f16_sdwa v128, v128 dst_sel:WORD_1 dst_unused:UNUSED_PRESERVE src0_sel:WORD_1\n"
        "v_mfma_f32_16x16x32_bf16 v[152:155], v[222:225], a[236:239], 0\n"
        "v_exp_f16_sdwa v129, v129 dst_sel:WORD_1 dst_unused:UNUSED_PRESERVE src0_sel:WORD_1\n"
        "v_mfma_f32_16x16x32_bf16 v[152:155], v[206:209], a[232:235], v[152:155]\n"
        "v_pk_add_f16 v41, v41, v128\n"
        "v_pk_add_f16 v41, v41, v129\n"
        "v_mfma_f32_16x16x32_bf16 v[152:155], v[198:201], a[228:231], v[152:155]\n"
        "v_cvt_pk_bf16_f32 v145, v132, v133\n"
        "v_cvt_pk_bf16_f32 v146, v124, v125\n"
        "v_mfma_f32_16x16x32_bf16 v[160:163], v[210:213], a[228:231], 0\n"
        "v_cvt_pk_bf16_f32 v147, v126, v127\n"
        "v_cvt_pk_bf16_f32 v164, v134, v135\n"
        "v_mfma_f32_16x16x32_bf16 v[160:163], v[202:205], a[232:235], v[160:163]\n"
        "v_cvt_pk_bf16_f32 v165, v136, v137\n"
        "v_cvt_pk_bf16_f32 v166, v148, v149\n"
        "v_mfma_f32_16x16x32_bf16 v[160:163], v[186:189], a[236:239], v[160:163]\n"
        "v_cvt_pk_bf16_f32 v167, v150, v151\n"
        "v_cvt_pk_bf16_f32 v134, v156, v157\n"
        "v_mfma_f32_16x16x32_bf16 v[130:133], v[218:221], a[236:239], 0\n"
        "v_cvt_pk_bf16_f32 v135, v158, v159\n"
        "v_cvt_pk_bf16_f32 v136, v138, v139\n"
        "v_mfma_f32_16x16x32_bf16 v[130:133], v[214:217], a[232:235], v[130:133]\n"
        "v_cvt_pk_bf16_f32 v137, v140, v141\n"
        "v_cvt_pk_f16_f32 v124, v152, v153\n"
        "v_mfma_f32_16x16x32_bf16 v[130:133], v[190:193], a[228:231], v[130:133]\n"
        "s_nop 0\n"
        "v_cvt_pk_f16_f32 v125, v154, v155\n"
        "v_cvt_pk_f16_f32 v142, v160, v161\n"
        "v_mfma_f32_16x16x32_bf16 v[148:151], v[190:193], v[144:147], 0\n"
        "s_nop 0\n"
        "v_cvt_pk_f16_f32 v143, v162, v163\n"
        "v_mfma_f32_16x16x32_bf16 v[148:151], v[214:217], v[164:167], v[148:151]\n"
        "s_nop 0\n"
        "v_cvt_pk_f16_f32 v140, v130, v131\n"
        "v_cvt_pk_f16_f32 v141, v132, v133\n"
        "v_mfma_f32_16x16x32_bf16 v[148:151], v[218:221], v[134:137], v[148:151]\n"
        "s_nop 1\n"
        "v_mfma_f32_16x16x32_bf16 v[152:155], v[186:189], v[134:137], 0\n"
        "s_nop 2\n"
        "v_mfma_f32_16x16x32_bf16 v[152:155], v[202:205], v[164:167], v[152:155]\n"
        "s_nop 0\n"
        "v_cvt_pk_f16_f32 v132, v148, v149\n"
        "v_cvt_pk_f16_f32 v133, v150, v151\n"
        "v_mfma_f32_16x16x32_bf16 v[152:155], v[210:213], v[144:147], v[152:155]\n"
        "v_pk_mul_f16 v132, v132, v47 op_sel_hi:[1,0]\n"
        "v_pk_mul_f16 v133, v133, v47 op_sel_hi:[1,0]\n"
        "v_mfma_f32_16x16x32_bf16 v[160:163], v[198:201], v[144:147], 0\n"
        "v_pk_mul_f16 v132, v132, v104\n"
        "v_pk_mul_f16 v133, v133, v105\n"
        "v_mfma_f32_16x16x32_bf16 v[160:163], v[206:209], v[164:167], v[160:163]\n"
        "v_exp_f16_sdwa v132, v132 dst_sel:WORD_0 dst_unused:UNUSED_PRESERVE src0_sel:WORD_0\n"
        "v_mfma_f32_16x16x32_bf16 v[160:163], v[222:225], v[134:137], v[160:163]\n"
        "v_exp_f16_sdwa v133, v133 dst_sel:WORD_0 dst_unused:UNUSED_PRESERVE src0_sel:WORD_0\n"
        "v_mfma_f32_16x16x32_bf16 v[174:177], v[190:193], a[240:243], 0\n"
        "v_exp_f16_sdwa v132, v132 dst_sel:WORD_1 dst_unused:UNUSED_PRESERVE src0_sel:WORD_1\n"
        "v_mfma_f32_16x16x32_bf16 v[174:177], v[214:217], a[244:247], v[174:177]\n"
        "v_exp_f16_sdwa v133, v133 dst_sel:WORD_1 dst_unused:UNUSED_PRESERVE src0_sel:WORD_1\n"
        "v_mfma_f32_16x16x32_bf16 v[174:177], v[218:221], a[248:251], v[174:177]\n"
        "v_pk_add_f16 v42, v132, v133\n"
        "v_cvt_pk_f16_f32 v134, v152, v153\n"
        "v_mfma_f32_16x16x32_bf16 v[178:181], v[186:189], a[248:251], 0\n"
        "v_cndmask_b32_e64 v36, 0, v254, s[62:63]\n"
        "v_cndmask_b32_e64 v130, 0, v254, s[64:65]\n"
        "v_mfma_f32_16x16x32_bf16 v[178:181], v[202:205], a[244:247], v[178:181]\n"
        "v_cndmask_b32_e64 v138, 0, v254, s[66:67]\n"
        "v_pk_add_f16 v40, v40, v36\n"
        "v_mfma_f32_16x16x32_bf16 v[178:181], v[210:213], a[240:243], v[178:181]\n"
        "v_pk_add_f16 v41, v41, v130\n"
        "v_cvt_pk_f16_f32 v135, v154, v155\n"
        "v_mfma_f32_16x16x32_bf16 v[226:229], v[198:201], a[240:243], 0\n"
        "v_pk_mul_f16 v134, v134, v47 op_sel_hi:[1,0]\n"
        "v_pk_mul_f16 v135, v135, v47 op_sel_hi:[1,0]\n"
        "v_mfma_f32_16x16x32_bf16 v[226:229], v[206:209], a[244:247], v[226:229]\n"
        "v_pk_mul_f16 v134, v134, v106\n"
        "v_pk_mul_f16 v135, v135, v107\n"
        "v_mfma_f32_16x16x32_bf16 v[226:229], v[222:225], a[248:251], v[226:229]\n"
        "v_exp_f16_sdwa v134, v134 dst_sel:WORD_0 dst_unused:UNUSED_PRESERVE src0_sel:WORD_0\n"
        "v_mfma_f32_16x16x32_bf16 v[170:173], v[222:225], v[64:67], 0\n"
        "v_exp_f16_sdwa v135, v135 dst_sel:WORD_0 dst_unused:UNUSED_PRESERVE src0_sel:WORD_0\n"
        "v_mfma_f32_16x16x32_bf16 v[170:173], v[206:209], v[72:75], v[170:173]\n"
        "v_exp_f16_sdwa v134, v134 dst_sel:WORD_1 dst_unused:UNUSED_PRESERVE src0_sel:WORD_1\n"
        "v_mfma_f32_16x16x32_bf16 v[170:173], v[198:201], a[252:255], v[170:173]\n"
        "v_exp_f16_sdwa v135, v135 dst_sel:WORD_1 dst_unused:UNUSED_PRESERVE src0_sel:WORD_1\n"
        "v_mfma_f32_16x16x32_bf16 v[182:185], v[210:213], a[252:255], 0\n"
        "v_pk_add_f16 v42, v42, v134\n"
        "v_pk_add_f16 v42, v42, v135\n"
        "v_mfma_f32_16x16x32_bf16 v[182:185], v[202:205], v[72:75], v[182:185]\n"
        "ds_read2_b32 v[114:115], v24 offset0:0 offset1:0\n"
        "ds_read2_b32 v[126:127], v24 offset0:1 offset1:1\n"
        "v_mfma_f32_16x16x32_bf16 v[182:185], v[186:189], v[64:67], v[182:185]\n"
        "ds_read2_b32 v[146:147], v24 offset0:2 offset1:2\n"
        "ds_read2_b32 v[158:159], v24 offset0:3 offset1:3\n"
        "v_mfma_f32_16x16x32_bf16 v[234:237], v[218:221], v[64:67], 0\n"
        "ds_read2_b32 v[154:155], v24 offset0:4 offset1:4\n"
        "v_mov_b32 v37, 0\n"
        "v_mfma_f32_16x16x32_bf16 v[234:237], v[214:217], v[72:75], v[234:237]\n"
        "ds_read2_b32 v[150:151], v24 offset0:5 offset1:5\n"
        "v_cvt_pk_f16_f32 v136, v160, v161\n"
        "v_mfma_f32_16x16x32_bf16 v[234:237], v[190:193], a[252:255], v[234:237]\n"
        "v_cvt_pk_f16_f32 v137, v162, v163\n"
        "v_pk_mul_f16 v136, v136, v47 op_sel_hi:[1,0]\n"
        "v_mfma_f32_16x16x32_bf16 v[230:233], v[190:193], v[68:71], 0\n"
        "v_pk_mul_f16 v137, v137, v47 op_sel_hi:[1,0]\n"
        "v_pk_mul_f16 v136, v136, v30\n"
        "v_mfma_f32_16x16x32_bf16 v[230:233], v[214:217], v[60:63], v[230:233]\n"
        "v_pk_mul_f16 v137, v137, v31\n"
        "v_cvt_pk_f16_f32 v164, v174, v175\n"
        "v_mfma_f32_16x16x32_bf16 v[230:233], v[218:221], v[56:59], v[230:233]\n"
        "v_exp_f16_sdwa v136, v136 dst_sel:WORD_0 dst_unused:UNUSED_PRESERVE src0_sel:WORD_0\n"
        "v_mfma_f32_16x16x32_bf16 v[194:197], v[186:189], v[56:59], 0\n"
        "v_exp_f16_sdwa v137, v137 dst_sel:WORD_0 dst_unused:UNUSED_PRESERVE src0_sel:WORD_0\n"
        "v_mfma_f32_16x16x32_bf16 v[194:197], v[202:205], v[60:63], v[194:197]\n"
        "v_exp_f16_sdwa v136, v136 dst_sel:WORD_1 dst_unused:UNUSED_PRESERVE src0_sel:WORD_1\n"
        "v_mfma_f32_16x16x32_bf16 v[194:197], v[210:213], v[68:71], v[194:197]\n"
        "v_exp_f16_sdwa v137, v137 dst_sel:WORD_1 dst_unused:UNUSED_PRESERVE src0_sel:WORD_1\n"
        "v_mfma_f32_16x16x32_bf16 v[238:241], v[198:201], v[68:71], 0\n"
        "s_waitcnt lgkmcnt(0)\n"
        "v_lshlrev_b32 v160, 16, v114\n"
        "v_lshlrev_b32 v161, 16, v126\n"
        "v_mfma_f32_16x16x32_bf16 v[238:241], v[206:209], v[60:63], v[238:241]\n"
        "v_lshlrev_b32 v162, 16, v146\n"
        "v_lshlrev_b32 v163, 16, v158\n"
        "v_mfma_f32_16x16x32_bf16 v[238:241], v[222:225], v[56:59], v[238:241]\n"
        "v_lshlrev_b32 v168, 16, v154\n"
        "v_lshlrev_b32 v169, 16, v150\n"
        "v_pk_add_f16 v42, v42, v136\n"
        "v_pk_add_f16 v42, v42, v137\n"
        "v_pk_add_f16 v42, v42, v138\n"
        "s_nop 1\n"
        "v_mfma_f32_16x16x32_f16 v[246:249], v[80:83], v[40:43], 0\n"
        "v_cvt_pk_f16_f32 v165, v176, v177\n"
        "v_cvt_pk_f16_f32 v166, v178, v179\n"
        "v_mfma_f32_16x16x32_bf16 v[242:245], v[222:225], v[84:87], 0\n"
        "v_cvt_pk_f16_f32 v167, v180, v181\n"
        "v_cvt_pk_f16_f32 v144, v226, v227\n"
        "v_mfma_f32_16x16x32_bf16 v[242:245], v[206:209], v[76:79], v[242:245]\n"
        "v_cvt_pk_f16_f32 v145, v228, v229\n"
        "v_cvt_pk_f16_f32 v156, v170, v171\n"
        "v_mfma_f32_16x16x32_bf16 v[242:245], v[198:201], v[52:55], v[242:245]\n"
        "s_waitcnt vmcnt(1)\n"
        "v_cvt_pk_f16_f32 v157, v172, v173\n"
        "v_cvt_pk_f16_f32 v176, v182, v183\n"
        "v_mfma_f32_16x16x32_bf16 v[178:181], v[210:213], v[52:55], 0\n"
        "v_readfirstlane_b32 s50, v26\n"
        "s_lshl_b32 s92, s15, 3\n"
        "s_add_u32 s92, s92, s48\n"
        "v_mov_b32 v27, s49\n"
        "s_nop 0\n"
        "s_cmp_eq_u32 s50, s21\n"
        "s_cselect_b32 s92, s92, 0x10000\n"
        "v_mov_b32 v26, s92\n"
        "ds_write_b32 v27, v26 offset:8\n"
        "v_mfma_f32_16x16x32_bf16 v[178:181], v[202:205], v[76:79], v[178:181]\n"
        "v_cvt_pk_f16_f32 v177, v184, v185\n"
        "v_cvt_pk_f16_f32 v174, v234, v235\n"
        "v_mfma_f32_16x16x32_bf16 v[178:181], v[186:189], v[84:87], v[178:181]\n"
        "v_mov_b32 v131, 0\n"
        "v_cvt_pk_f16_f32 v175, v236, v237\n"
        "v_mfma_f32_16x16x32_bf16 v[226:229], v[218:221], v[84:87], 0\n"
        "v_cvt_pk_f16_f32 v170, v230, v231\n"
        "v_cvt_pk_f16_f32 v171, v232, v233\n"
        "v_mfma_f32_16x16x32_bf16 v[226:229], v[214:217], v[76:79], v[226:229]\n"
        "v_cvt_pk_f16_f32 v172, v194, v195\n"
        "v_cvt_pk_f16_f32 v173, v196, v197\n"
        "v_mfma_f32_16x16x32_bf16 v[226:229], v[190:193], v[52:55], v[226:229]\n"
        "v_cvt_pk_f16_f32 v152, v238, v239\n"
        "v_cvt_pk_f16_f32 v153, v240, v241\n"
        "v_rcp_f32 v246, v246\n"
        "v_rcp_f32 v247, v247\n"
        "s_nop 0\n"
        "v_cvt_pk_f16_f32 v254, v246, v247\n"
        "v_pk_mul_f16 v108, v108, v254 op_sel_hi:[1,0]\n"
        "v_pk_mul_f16 v108, v108, v104\n"
        "v_pk_mul_f16 v109, v109, v254 op_sel_hi:[1,0]\n"
        "v_pk_mul_f16 v109, v109, v105\n"
        "v_pk_mul_f16 v110, v110, v254 op_sel_hi:[1,0]\n"
        "v_pk_mul_f16 v110, v110, v106\n"
        "v_pk_mul_f16 v111, v111, v254 op_sel_hi:[1,0]\n"
        "v_pk_mul_f16 v111, v111, v107\n"
        "s_nop 1\n"
        "v_mfma_f32_16x16x32_f16 v[182:185], v[108:111], v[120:123], 0\n"
        "v_add_f32_dpp v249, v249, v249 row_ror:8 row_mask:0xf bank_mask:0xf\n"
        "v_pk_mul_f16 v34, v34, v254 op_sel_hi:[1,0]\n"
        "v_pk_mul_f16 v36, v36, v254 op_sel_hi:[1,0]\n"
        "v_pk_mul_f16 v34, v34, v30\n"
        "v_pk_mul_f16 v35, v35, v254 op_sel_hi:[1,0]\n"
        "v_pk_mul_f16 v35, v35, v31\n"
        "s_nop 1\n"
        "v_mfma_f32_16x16x32_f16 v[182:185], v[34:37], v[112:115], v[182:185]\n"
        "v_rcp_f32 v248, v248\n"
        "v_mfma_f32_16x16x32_bf16 v[182:185], v[190:193], v[88:91], v[182:185]\n"
        "v_add_f32_dpp v249, v249, v249 row_ror:4 row_mask:0xf bank_mask:0xf\n"
        "v_pk_mul_f16 v116, v116, v254 op_sel:[0,1]\n"
        "v_mfma_f32_16x16x32_bf16 v[230:233], v[190:193], v[48:51], 0\n"
        "v_pk_mul_f16 v117, v117, v254 op_sel:[0,1]\n"
        "v_pk_mul_f16 v118, v118, v254 op_sel:[0,1]\n"
        "v_mfma_f32_16x16x32_f16 v[230:233], v[34:37], v[124:127], v[230:233]\n"
        "v_pk_mul_f16 v119, v119, v254 op_sel:[0,1]\n"
        "v_pk_mul_f16 v128, v128, v254 op_sel:[0,1]\n"
        "v_mfma_f32_16x16x32_f16 v[230:233], v[108:111], v[140:143], v[230:233]\n"
        "v_pk_mul_f16 v129, v129, v254 op_sel:[0,1]\n"
        "v_pk_mul_f16 v130, v130, v254 op_sel:[0,1]\n"
        "v_mfma_f32_16x16x32_f16 v[194:197], v[108:111], v[164:167], 0\n"
        "v_pk_sub_u16 v247, v254, s83\n"
        "v_pk_mul_f16 v116, v116, v104\n"
        "v_mfma_f32_16x16x32_f16 v[194:197], v[34:37], v[144:147], v[194:197]\n"
        "v_pk_mul_f16 v117, v117, v105\n"
        "v_pk_mul_f16 v118, v118, v106\n"
        "v_mfma_f32_16x16x32_bf16 v[194:197], v[214:217], v[88:91], v[194:197]\n"
        "v_pk_mul_f16 v119, v119, v107\n"
        "v_add_f32_dpp v249, v249, v249 row_ror:2 row_mask:0xf bank_mask:0xf\n"
        "v_mfma_f32_16x16x32_bf16 v[250:253], v[214:217], v[48:51], 0\n"
        "v_pk_mul_f16 v128, v128, v30\n"
        "v_add_f32_dpp v249, v249, v249 row_ror:1 row_mask:0xf bank_mask:0xf\n"
        "v_mfma_f32_16x16x32_f16 v[250:253], v[34:37], v[156:159], v[250:253]\n"
        "v_rcp_f32 v249, v249\n"
        "v_mfma_f32_16x16x32_f16 v[250:253], v[108:111], v[174:177], v[250:253]\n"
        "v_pk_mul_f16 v129, v129, v31\n"
        "v_cvt_pk_f16_f32 v254, v249, v248\n"
        "v_mfma_f32_16x16x32_f16 v[234:237], v[108:111], v[170:173], 0\n"
        "v_cvt_pk_f16_f32 v148, v242, v243\n"
        "v_cvt_pk_f16_f32 v149, v244, v245\n"
        "v_mfma_f32_16x16x32_f16 v[234:237], v[34:37], v[152:155], v[234:237]\n"
        "v_pk_mul_f16 v132, v132, v254 op_sel:[0,1]\n"
        "v_pk_mul_f16 v133, v133, v254 op_sel:[0,1]\n"
        "v_mfma_f32_16x16x32_bf16 v[234:237], v[218:221], v[88:91], v[234:237]\n"
        "v_mov_b32 v139, 0\n"
        "v_pk_mul_f16 v134, v134, v254 op_sel:[0,1]\n"
        "v_mfma_f32_16x16x32_bf16 v[242:245], v[218:221], v[48:51], 0\n"
        "v_pk_mul_f16 v135, v135, v254 op_sel:[0,1]\n"
        "v_pk_mul_f16 v136, v136, v254 op_sel:[0,1]\n"
        "v_mfma_f32_16x16x32_f16 v[242:245], v[34:37], v[148:151], v[242:245]\n"
        "s_add_u32 s90, s89, 0\n"
        "v_pk_mul_f16 v137, v137, v254 op_sel:[0,1]\n"
        "v_mul_f16 v43, v43, v254\n"
        "v_cvt_pk_f16_f32 v240, v178, v179\n"
        "v_cvt_pk_f16_f32 v241, v180, v181\n"
        "v_cvt_pk_f16_f32 v238, v226, v227\n"
        "v_cvt_pk_f16_f32 v239, v228, v229\n"
        "s_nop 1\n"
        "v_mfma_f32_16x16x32_f16 v[242:245], v[108:111], v[238:241], v[242:245]\n"
        "v_mul_f16 v43, v43, v255\n"
        "v_pk_mul_f16 v138, v138, v254 op_sel:[0,1]\n"
        "v_mfma_f32_16x16x32_f16 v[108:111], v[116:119], v[120:123], 0\n"
        "ds_write_b16 v22, v43 offset:512\n"
        "v_mfma_f32_16x16x32_f16 v[108:111], v[128:131], v[112:115], v[108:111]\n"
        "v_pk_sub_u16 v255, v254, s83\n"
        "v_pk_mul_f16 v132, v132, v104\n"
        "v_mfma_f32_16x16x32_bf16 v[108:111], v[210:213], v[88:91], v[108:111]\n"
        "v_pk_mul_f16 v133, v133, v105\n"
        "v_pk_mul_f16 v134, v134, v106\n"
        "v_mfma_f32_16x16x32_bf16 v[178:181], v[210:213], v[48:51], 0\n"
        "v_pk_mul_f16 v135, v135, v107\n"
        "v_pk_mul_f16 v136, v136, v30\n"
        "v_mfma_f32_16x16x32_f16 v[178:181], v[128:131], v[124:127], v[178:181]\n"
        "v_pk_mul_f16 v137, v137, v31\n"
        "v_bitop3_b32 v247, v247, v255, s82 bitop3:0xa8\n"
        "v_mfma_f32_16x16x32_f16 v[178:181], v[116:119], v[140:143], v[178:181]\n"
        "s_add_u32 s91, s89, 98112\n"
        "v_cmp_ne_u32_e64 s[74:75], 0, v247\n"
        "v_cvt_pk_bf16_f32 v104, v182, v230\n"
        "v_mfma_f32_16x16x32_f16 v[40:43], v[116:119], v[164:167], 0\n"
        "s_nop 0\n"
        "v_cvt_pk_bf16_f32 v105, v194, v250\n"
        "s_cmp_lg_u64 s[74:75], 0\n"
        "v_mfma_f32_16x16x32_f16 v[40:43], v[128:131], v[144:147], v[40:43]\n"
        "s_cbranch_scc1 SLOW_%=\n"
        "v_mfma_f32_16x16x32_bf16 v[40:43], v[202:205], v[88:91], v[40:43]\n"
        "ds_read_b128 v[34:37], v20 offset:512\n"
        "ds_read_b128 v[190:193], v20 offset:528\n"
        "v_mfma_f32_16x16x32_bf16 v[210:213], v[202:205], v[48:51], 0\n"
        "v_cvt_pk_bf16_f32 v106, v234, v242\n"
        "v_cvt_pk_bf16_f32 v228, v183, v231\n"
        "v_mfma_f32_16x16x32_f16 v[210:213], v[128:131], v[156:159], v[210:213]\n"
        "v_cvt_pk_bf16_f32 v229, v195, v251\n"
        "v_cvt_pk_bf16_f32 v230, v235, v243\n"
        "v_mfma_f32_16x16x32_f16 v[210:213], v[116:119], v[174:177], v[210:213]\n"
        "buffer_store_dwordx3 v[104:106], v14, s[40:43], s90 offen sc1 nt\n"
        "v_cvt_pk_bf16_f32 v194, v184, v232\n"
        "v_cvt_pk_bf16_f32 v195, v196, v252\n"
        "v_cvt_pk_bf16_f32 v196, v236, v244\n"
        "v_mfma_f32_16x16x32_f16 v[202:205], v[116:119], v[170:173], 0\n"
        "v_cvt_pk_bf16_f32 v214, v185, v233\n"
        "v_cvt_pk_bf16_f32 v215, v197, v253\n"
        "v_mfma_f32_16x16x32_f16 v[202:205], v[128:131], v[152:155], v[202:205]\n"
        "v_cvt_pk_bf16_f32 v216, v237, v245\n"
        "v_cvt_pk_bf16_f32 v218, v108, v178\n"
        "v_mfma_f32_16x16x32_bf16 v[202:205], v[186:189], v[88:91], v[202:205]\n"
        "buffer_store_dwordx3 v[228:230], v15, s[40:43], s90 offen offset:768 sc1 nt\n"
        "v_cvt_pk_bf16_f32 v219, v40, v210\n"
        "v_cvt_pk_bf16_f32 v226, v109, v179\n"
        "v_cvt_pk_bf16_f32 v227, v41, v211\n"
        "v_mfma_f32_16x16x32_bf16 v[182:185], v[186:189], v[48:51], 0\n"
        "s_waitcnt lgkmcnt(0)\n"
        "v_pk_mul_f16 v104, v34, v120\n"
        "v_pk_mul_f16 v105, v34, v140\n"
        "v_mfma_f32_16x16x32_f16 v[182:185], v[128:131], v[148:151], v[182:185]\n"
        "v_pk_mul_f16 v106, v34, v164\n"
        "v_pk_mul_f16 v107, v34, v174\n"
        "v_mfma_f32_16x16x32_f16 v[182:185], v[116:119], v[238:241], v[182:185]\n"
        "buffer_store_dwordx3 v[194:196], v14, s[40:43], s91 offen offset:192 sc1 nt\n"
        "v_pk_mul_f16 v108, v34, v170\n"
        "v_pk_mul_f16 v109, v34, v238\n"
        "v_pk_fma_f16 v104, v35, v121, v104\n"
        "v_mfma_f32_16x16x32_f16 v[116:119], v[132:135], v[120:123], 0\n"
        "v_pk_fma_f16 v105, v35, v141, v105\n"
        "v_pk_fma_f16 v106, v35, v165, v106\n"
        "v_mfma_f32_16x16x32_f16 v[116:119], v[136:139], v[112:115], v[116:119]\n"
        "s_add_u32 s90, s89, 196224\n"
        "v_pk_fma_f16 v107, v35, v175, v107\n"
        "v_pk_fma_f16 v108, v35, v171, v108\n"
        "v_mfma_f32_16x16x32_bf16 v[116:119], v[198:201], v[88:91], v[116:119]\n"
        "buffer_store_dwordx3 v[214:216], v16, s[40:43], s91 offen offset:960 sc1 nt\n"
        "v_pk_fma_f16 v109, v35, v239, v109\n"
        "v_pk_fma_f16 v104, v36, v122, v104\n"
        "v_pk_fma_f16 v105, v36, v142, v105\n"
        "v_mfma_f32_16x16x32_bf16 v[128:131], v[198:201], v[48:51], 0\n"
        "v_pk_fma_f16 v106, v36, v166, v106\n"
        "v_pk_fma_f16 v107, v36, v176, v107\n"
        "v_mfma_f32_16x16x32_f16 v[128:131], v[136:139], v[124:127], v[128:131]\n"
        "v_pk_fma_f16 v108, v36, v172, v108\n"
        "v_pk_fma_f16 v109, v36, v240, v109\n"
        "v_mfma_f32_16x16x32_f16 v[128:131], v[132:135], v[140:143], v[128:131]\n"
        "s_add_u32 s91, s89, 294336\n"
        "v_pk_fma_f16 v104, v37, v123, v104\n"
        "v_pk_fma_f16 v105, v37, v143, v105\n"
        "v_mfma_f32_16x16x32_f16 v[120:123], v[132:135], v[164:167], 0\n"
        "v_pk_fma_f16 v106, v37, v167, v106\n"
        "v_pk_fma_f16 v107, v37, v177, v107\n"
        "v_mfma_f32_16x16x32_f16 v[120:123], v[136:139], v[144:147], v[120:123]\n"
        "v_pk_fma_f16 v108, v37, v173, v108\n"
        "v_pk_fma_f16 v109, v37, v241, v109\n"
        "v_mfma_f32_16x16x32_bf16 v[120:123], v[206:209], v[88:91], v[120:123]\n"
        "v_pk_fma_f16 v104, v190, v112, v104\n"
        "v_pk_fma_f16 v105, v190, v124, v105\n"
        "v_mfma_f32_16x16x32_bf16 v[34:37], v[206:209], v[48:51], 0\n"
        "v_pk_fma_f16 v106, v190, v144, v106\n"
        "v_cvt_pk_bf16_f32 v220, v202, v182\n"
        "v_mfma_f32_16x16x32_f16 v[34:37], v[136:139], v[156:159], v[34:37]\n"
        "v_pk_fma_f16 v107, v190, v156, v107\n"
        "v_cvt_pk_bf16_f32 v228, v203, v183\n"
        "v_mfma_f32_16x16x32_f16 v[34:37], v[132:135], v[174:177], v[34:37]\n"
        "buffer_store_dwordx3 v[218:220], v14, s[40:43], s90 offen offset:384 sc1 nt\n"
        "v_pk_fma_f16 v108, v190, v152, v108\n"
        "v_cvt_pk_bf16_f32 v164, v110, v180\n"
        "v_cvt_pk_bf16_f32 v165, v42, v212\n"
        "v_mfma_f32_16x16x32_f16 v[140:143], v[132:135], v[170:173], 0\n"
        "v_cvt_pk_bf16_f32 v166, v204, v184\n"
        "v_pk_fma_f16 v109, v190, v148, v109\n"
        "v_mfma_f32_16x16x32_f16 v[140:143], v[136:139], v[152:155], v[140:143]\n"
        "v_cvt_pk_bf16_f32 v110, v111, v181\n"
        "v_cvt_pk_bf16_f32 v111, v43, v213\n"
        "v_mfma_f32_16x16x32_bf16 v[140:143], v[222:225], v[88:91], v[140:143]\n"
        "buffer_store_dwordx3 v[226:228], v17, s[40:43], s90 offen offset:1152 sc1 nt\n"
        "s_add_u32 s90, s89, 392448\n"
        "v_cvt_pk_bf16_f32 v112, v205, v185\n"
        "v_pk_fma_f16 v104, v191, v113, v104\n"
        "v_mfma_f32_16x16x32_bf16 v[40:43], v[222:225], v[48:51], 0\n"
        "v_pk_fma_f16 v105, v191, v125, v105\n"
        "v_pk_fma_f16 v106, v191, v145, v106\n"
        "v_mfma_f32_16x16x32_f16 v[40:43], v[136:139], v[148:151], v[40:43]\n"
        "v_pk_fma_f16 v107, v191, v157, v107\n"
        "v_pk_fma_f16 v108, v191, v153, v108\n"
        "v_mfma_f32_16x16x32_f16 v[40:43], v[132:135], v[238:241], v[40:43]\n"
        "buffer_store_dwordx3 v[164:166], v14, s[40:43], s91 offen offset:576 sc1 nt\n"
        "v_pk_fma_f16 v109, v191, v149, v109\n"
        "v_pk_fma_f16 v104, v192, v114, v104\n"
        "v_pk_fma_f16 v105, v192, v126, v105\n"
        "v_pk_fma_f16 v106, v192, v146, v106\n"
        "v_pk_fma_f16 v107, v192, v158, v107\n"
        "s_nop 1\n"
        "v_mfma_f32_16x16x32_f16 v[160:163], v[80:83], v[104:107], v[160:163]\n"
        "buffer_store_dwordx3 v[110:112], v14, s[40:43], s90 offen sc1 nt\n"
        "v_pk_fma_f16 v108, v192, v154, v108\n"
        "v_pk_fma_f16 v109, v192, v150, v109\n"
        "v_cvt_pk_bf16_f32 v110, v116, v128\n"
        "s_nop 0\n"
        "v_mfma_f32_16x16x32_f16 v[166:169], v[80:83], v[106:109], v[166:169]\n"
        "s_add_u32 s46, s89, 590016\n"
        "v_cvt_pk_bf16_f32 v104, v117, v129\n"
        "v_cvt_pk_bf16_f32 v114, v118, v130\n"
        "v_cvt_pk_bf16_f32 v108, v119, v131\n"
        "v_cvt_pk_bf16_f32 v111, v120, v34\n"
        "v_cvt_pk_bf16_f32 v105, v121, v35\n"
        "v_cvt_pk_bf16_f32 v112, v140, v40\n"
        "buffer_store_dwordx3 v[110:112], v15, s[40:43], s90 offen offset:768 sc1 nt\n"
        "v_cvt_pk_bf16_f32 v115, v122, v36\n"
        "v_cvt_pk_bf16_f32 v109, v123, v37\n"
        "v_cvt_pk_bf16_f32 v106, v141, v41\n"
        "s_add_u32 s91, s89, 490560\n"
        "v_cvt_pk_bf16_f32 v116, v142, v42\n"
        "buffer_store_dwordx3 v[104:106], v14, s[40:43], s91 offen offset:192 sc1 nt\n"
        "s_min_u32 s46, s46, 200540928\n"
        "v_cvt_pk_bf16_f32 v110, v143, v43\n"
        "v_cvt_pk_bf16_f32 v34, v160, v161\n"
        "v_cvt_pk_bf16_f32 v35, v162, v163\n"
        "v_cvt_pk_bf16_f32 v36, v168, v169\n"
        "buffer_store_dwordx3 v[114:116], v16, s[40:43], s91 offen offset:960 sc1 nt\n"
        "s_add_u32 s90, s89, 588672\n"
        "buffer_store_dwordx3 v[108:110], v14, s[40:43], s90 offen offset:384 sc1 nt\n"
        "buffer_store_dwordx3 v[34:36], v17, s[44:47], s90 offen offset:1152 sc1 nt\n"
        "FIN_%=:\n"
        "s_waitcnt lgkmcnt(0)\n"
        "s_barrier\n"
        "v_mov_b32 v27, 163432\n"
        "ds_read_b32 v27, v27\n"
        "s_waitcnt lgkmcnt(0)\n"
        "v_readfirstlane_b32 s50, v27\n"
        "s_nop 3\n"
        "s_cmp_lt_u32 s50, 5329\n"
        "s_cbranch_scc1 POST_%=\n"
        ".Lswin_fin:\n"
        "s_endpgm\n"
        "POST_%=:\n"
        "s_barrier\n"
        "s_mul_i32 s87, s50, 1344\n"
        "s_mul_i32 s92, s50, 57457\n"
        "s_lshr_b32 s92, s92, 22\n"
        "s_mul_i32 s92, s92, 588672\n"
        "s_add_u32 s87, s87, s92\n"
        "s_lshr_b32 s23, s15, 2\n"
        "s_add_u32 s23, s23, 20\n"
        "s_sub_u32 s23, s23, s53\n"
        "s_mul_i32 s23, s23, 1584\n"
        "s_mov_b32 s22, 0\n"
        "s_mov_b32 s48, 0x10000\n"
        "s_mov_b32 s18, 0\n"
        "s_add_u32 s38, s36, s87\n"
        "s_addc_u32 s39, s37, 0\n"
        "s_add_u32 m0, s52, 0\n"
        "s_nop 0\n"
        "global_load_lds_dwordx4 v0, s[38:39]\n"
        "global_load_lds_dwordx4 v1, s[38:39] offset:1024\n"
        "global_load_lds_dwordx4 v2, s[38:39] offset:2048\n"
        "global_load_lds_dwordx4 v3, s[38:39] offset:3072\n"
        "s_add_u32 m0, s52, 4096\n"
        "s_nop 0\n"
        "global_load_lds_dwordx4 v4, s[38:39]\n"
        "global_load_lds_dwordx4 v5, s[38:39] offset:1024\n"
        "global_load_lds_dwordx4 v6, s[38:39] offset:2048\n"
        "global_load_lds_dwordx4 v7, s[38:39] offset:3072\n"
        "s_add_u32 m0, s52, 8192\n"
        "s_nop 0\n"
        "global_load_lds_dwordx4 v8, s[38:39]\n"
        "s_waitcnt vmcnt(0)\n"
        "ds_read_b128 v[126:129], v9\n"
        "ds_read_b128 v[132:135], v10\n"
        "ds_read_b128 v[138:141], v11\n"
        "ds_read_b128 v[106:109], v9 offset:3072\n"
        "ds_read_b128 v[112:115], v10 offset:3072\n"
        "ds_read_b128 v[118:121], v11 offset:3072\n"
        "ds_read_b128 v[146:149], v9 offset:6144\n"
        "ds_read_b128 v[152:155], v10 offset:6144\n"
        "ds_read_b128 v[158:161], v11 offset:6144\n"
        "s_mov_b32 s86, 0x80000000\n"
        "s_mov_b32 s89, 0x80000000\n"
        "s_mov_b32 s85, 0\n"
        "s_sub_u32 s29, s29, 1\n"
        "s_waitcnt lgkmcnt(0)\n"
        "s_nop 7\n"
        "s_branch TOP_%=\n"
        "SLOW_%=:\n"
        "s_waitcnt lgkmcnt(0)\n"
        "s_nop 15\n"
        "v_mbcnt_lo_u32_b32 v255, -1, 0\n"
        "v_mbcnt_hi_u32_b32 v255, -1, v255\n"
        "v_and_b32 v254, 15, v255\n"
        "v_mul_u32_u24 v252, 24, v254\n"
        "v_sub_u32 v253, v24, v252\n"
        "v_lshrrev_b32 v250, 4, v255\n"
        "v_mul_u32_u24 v252, 96, v250\n"
        "v_add_u32 v253, v253, v252\n"
        "ds_read_b64 v[132:133], v253\n"
        "ds_read_b64 v[30:31], v253 offset:24\n"
        "ds_read_b64 v[34:35], v253 offset:48\n"
        "ds_read_b64 v[46:47], v253 offset:72\n"
        "ds_read_b64 v[40:41], v253 offset:8\n"
        "ds_read_b64 v[136:137], v253 offset:32\n"
        "ds_read_b64 v[104:105], v253 offset:56\n"
        "ds_read_b64 v[144:145], v253 offset:80\n"
        "ds_read_b64 v[124:125], v253 offset:16\n"
        "ds_read_b64 v[116:117], v253 offset:40\n"
        "ds_read_b64 v[108:109], v253 offset:64\n"
        "ds_read_b64 v[140:141], v253 offset:88\n"
        "v_mfma_f32_16x16x32_bf16 v[120:123], v[190:193], v[190:193], 0\n"
        "v_mov_b32 v253, 0x05040100\n"
        "v_subrev_u32 v248, 159328, v20\n"
        "v_mfma_f32_16x16x32_bf16 v[120:123], v[214:217], v[214:217], v[120:123]\n"
        "v_mov_b32 v249, 161888\n"
        "v_mov_b32 v166, 0x3f803f80\n"
        "v_mfma_f32_16x16x32_bf16 v[120:123], v[218:221], v[218:221], v[120:123]\n"
        "v_mov_b32 v167, 0x3f803f80\n"
        "v_mov_b32 v168, 0x3f803f80\n"
        "v_mov_b32 v169, 0x3f803f80\n"
        "s_nop 1\n"
        "v_mfma_f32_16x16x32_bf16 v[112:115], v[166:169], v[218:221], 0\n"
        "v_mov_b32 v255, 0xff800000\n"
        "v_mov_b32 v139, 0\n"
        "v_mfma_f32_16x16x32_bf16 v[112:115], v[166:169], v[214:217], v[112:115]\n"
        "s_waitcnt lgkmcnt(11)\n"
        "v_mov_b32 v43, 0\n"
        "v_mov_b32 v143, 0\n"
        "v_mfma_f32_16x16x32_bf16 v[112:115], v[166:169], v[190:193], v[112:115]\n"
        "s_waitcnt lgkmcnt(10)\n"
        "v_mov_b32 v119, 0\n"
        "v_mov_b32 v107, 0\n"
        "v_mfma_f32_16x16x32_bf16 v[128:131], v[210:213], v[210:213], 0\n"
        "s_waitcnt lgkmcnt(9)\n"
        "v_mov_b32 v37, 0\n"
        "v_mov_b32 v111, 0\n"
        "v_mfma_f32_16x16x32_bf16 v[128:131], v[202:205], v[202:205], v[128:131]\n"
        "s_waitcnt lgkmcnt(8)\n"
        "v_mov_b32 v147, 0\n"
        "v_mov_b32 v135, 0\n"
        "v_mfma_f32_16x16x32_bf16 v[128:131], v[186:189], v[186:189], v[128:131]\n"
        "s_waitcnt lgkmcnt(7)\n"
        "v_mov_b32 v127, 0\n"
        "v_and_b32 v248, 384, v248\n"
        "v_mfma_f32_16x16x32_bf16 v[162:165], v[166:169], v[186:189], 0\n"
        "s_waitcnt lgkmcnt(6)\n"
        "v_lshlrev_b32 v248, 1, v248\n"
        "v_add_u32 v248, 160864, v248\n"
        "v_mfma_f32_16x16x32_bf16 v[162:165], v[166:169], v[202:205], v[162:165]\n"
        "s_waitcnt lgkmcnt(5)\n"
        "v_lshlrev_b32 v251, 2, v254\n"
        "v_lshlrev_b32 v252, 4, v250\n"
        "v_mfma_f32_16x16x32_bf16 v[162:165], v[166:169], v[210:213], v[162:165]\n"
        "s_waitcnt lgkmcnt(4)\n"
        "v_add_u32 v251, v251, v248\n"
        "v_add_u32 v252, v252, v248\n"
        "v_mfma_f32_16x16x32_bf16 v[152:155], v[198:201], v[198:201], 0\n"
        "s_waitcnt lgkmcnt(3)\n"
        "v_cndmask_b32 v249, v249, v251, vcc\n"
        "v_cndmask_b32_e64 v254, v120, v121, s[56:57]\n"
        "v_mfma_f32_16x16x32_bf16 v[152:155], v[206:209], v[206:209], v[152:155]\n"
        "s_waitcnt lgkmcnt(2)\n"
        "v_cndmask_b32_e64 v254, v254, v122, s[58:59]\n"
        "v_cndmask_b32_e64 v254, v254, v123, s[60:61]\n"
        "v_mfma_f32_16x16x32_bf16 v[152:155], v[222:225], v[222:225], v[152:155]\n"
        "s_waitcnt lgkmcnt(1)\n"
        "v_mul_f32 v254, s80, v254\n"
        "v_mul_f32 v250, s80, v112\n"
        "v_mfma_f32_16x16x32_bf16 v[156:159], v[166:169], v[222:225], 0\n"
        "s_waitcnt lgkmcnt(0)\n"
        "v_fma_f32 v250, -v250, v250, v254\n"
        "v_max_f32 v250, 0, v250\n"
        "v_mfma_f32_16x16x32_bf16 v[156:159], v[166:169], v[206:209], v[156:159]\n"
        "v_add_f32 v250, s81, v250\n"
        "v_perm_b32 v120, v133, v132, v253\n"
        "v_mfma_f32_16x16x32_bf16 v[156:159], v[166:169], v[198:201], v[156:159]\n"
        "v_rsq_f32 v250, v250\n"
        "v_perm_b32 v121, v31, v30, v253\n"
        "v_perm_b32 v122, v35, v34, v253\n"
        "v_perm_b32 v123, v47, v46, v253\n"
        "s_nop 1\n"
        "v_mfma_f32_16x16x32_bf16 v[170:173], v[120:123], v[120:123], 0\n"
        "ds_write_b32 v249, v250\n"
        "v_perm_b32 v112, v41, v40, v253\n"
        "v_perm_b32 v113, v137, v136, v253\n"
        "v_perm_b32 v114, v105, v104, v253\n"
        "v_perm_b32 v115, v145, v144, v253\n"
        "s_nop 1\n"
        "v_mfma_f32_16x16x32_bf16 v[170:173], v[112:115], v[112:115], v[170:173]\n"
        "v_perm_b32 v148, v125, v124, v253\n"
        "v_perm_b32 v149, v117, v116, v253\n"
        "v_perm_b32 v150, v109, v108, v253\n"
        "v_perm_b32 v151, v141, v140, v253\n"
        "s_nop 1\n"
        "v_mfma_f32_16x16x32_bf16 v[170:173], v[148:151], v[148:151], v[170:173]\n"
        "v_cndmask_b32_e64 v254, v128, v129, s[56:57]\n"
        "v_cndmask_b32_e64 v254, v254, v130, s[58:59]\n"
        "v_mfma_f32_16x16x32_bf16 v[158:161], v[166:169], v[148:151], 0\n"
        "v_cndmask_b32_e64 v254, v254, v131, s[60:61]\n"
        "v_mul_f32 v254, s80, v254\n"
        "v_mfma_f32_16x16x32_bf16 v[158:161], v[166:169], v[112:115], v[158:161]\n"
        "v_mul_f32 v253, s80, v162\n"
        "v_fma_f32 v253, -v253, v253, v254\n"
        "v_mfma_f32_16x16x32_bf16 v[158:161], v[166:169], v[120:123], v[158:161]\n"
        "v_max_f32 v253, 0, v253\n"
        "v_add_f32 v253, s81, v253\n"
        "v_mfma_f32_16x16x32_bf16 v[130:133], v[190:193], a[216:219], 0\n"
        "v_rsq_f32 v253, v253\n"
        "v_mfma_f32_16x16x32_bf16 v[130:133], v[214:217], a[220:223], v[130:133]\n"
        "ds_write_b32 v249, v253 offset:64\n"
        "v_mfma_f32_16x16x32_bf16 v[130:133], v[218:221], a[224:227], v[130:133]\n"
        "v_cndmask_b32_e64 v254, v152, v153, s[56:57]\n"
        "v_cndmask_b32_e64 v254, v254, v154, s[58:59]\n"
        "v_mfma_f32_16x16x32_bf16 v[164:167], v[186:189], a[224:227], 0\n"
        "v_cndmask_b32_e64 v254, v254, v155, s[60:61]\n"
        "v_mul_f32 v254, s80, v254\n"
        "v_mfma_f32_16x16x32_bf16 v[164:167], v[202:205], a[220:223], v[164:167]\n"
        "v_mul_f32 v253, s80, v156\n"
        "v_fma_f32 v253, -v253, v253, v254\n"
        "v_mfma_f32_16x16x32_bf16 v[164:167], v[210:213], a[216:219], v[164:167]\n"
        "v_max_f32 v253, 0, v253\n"
        "v_add_f32 v253, s81, v253\n"
        "v_mfma_f32_16x16x32_bf16 v[160:163], v[198:201], a[216:219], 0\n"
        "v_rsq_f32 v253, v253\n"
        "v_mfma_f32_16x16x32_bf16 v[160:163], v[206:209], a[220:223], v[160:163]\n"
        "ds_write_b32 v249, v253 offset:128\n"
        "v_mfma_f32_16x16x32_bf16 v[160:163], v[222:225], a[224:227], v[160:163]\n"
        "v_cndmask_b32_e64 v253, v170, v171, s[56:57]\n"
        "v_cndmask_b32_e64 v253, v253, v172, s[58:59]\n"
        "v_mfma_f32_16x16x32_bf16 v[152:155], v[148:151], a[224:227], 0\n"
        "v_cndmask_b32_e64 v253, v253, v173, s[60:61]\n"
        "v_mul_f32 v253, s80, v253\n"
        "v_mfma_f32_16x16x32_bf16 v[152:155], v[112:115], a[220:223], v[152:155]\n"
        "v_mul_f32 v254, s80, v158\n"
        "v_fma_f32 v254, -v254, v254, v253\n"
        "v_mfma_f32_16x16x32_bf16 v[152:155], v[120:123], a[216:219], v[152:155]\n"
        "v_max_f32 v254, 0, v254\n"
        "v_add_f32 v254, s81, v254\n"
        "v_mfma_f32_16x16x32_bf16 v[156:159], v[198:201], a[228:231], 0\n"
        "v_rsq_f32 v254, v254\n"
        "v_mfma_f32_16x16x32_bf16 v[156:159], v[206:209], a[232:235], v[156:159]\n"
        "ds_write_b32 v249, v254 offset:192\n"
        "v_mfma_f32_16x16x32_bf16 v[156:159], v[222:225], a[236:239], v[156:159]\n"
        "v_cvt_pk_bf16_f32 v128, v130, v131\n"
        "v_cvt_pk_bf16_f32 v129, v132, v133\n"
        "v_mfma_f32_16x16x32_bf16 v[168:171], v[186:189], a[236:239], 0\n"
        "v_cvt_pk_bf16_f32 v130, v164, v165\n"
        "v_cvt_pk_bf16_f32 v131, v166, v167\n"
        "v_mfma_f32_16x16x32_bf16 v[168:171], v[202:205], a[232:235], v[168:171]\n"
        "v_cvt_pk_bf16_f32 v136, v160, v161\n"
        "v_cvt_pk_bf16_f32 v137, v162, v163\n"
        "v_mfma_f32_16x16x32_bf16 v[168:171], v[210:213], a[228:231], v[168:171]\n"
        "s_nop 0\n"
        "v_cvt_pk_bf16_f32 v138, v152, 0\n"
        "v_cvt_pk_bf16_f32 v40, v156, v157\n"
        "v_mfma_f32_16x16x32_bf16 v[160:163], v[190:193], a[228:231], 0\n"
        "s_nop 0\n"
        "v_cvt_pk_bf16_f32 v41, v158, v159\n"
        "v_mfma_f32_16x16x32_bf16 v[160:163], v[214:217], a[232:235], v[160:163]\n"
        "s_nop 0\n"
        "v_cvt_pk_bf16_f32 v154, v168, v169\n"
        "v_cvt_pk_bf16_f32 v155, v170, v171\n"
        "v_mfma_f32_16x16x32_bf16 v[160:163], v[218:221], a[236:239], v[160:163]\n"
        "s_nop 1\n"
        "v_mfma_f32_16x16x32_bf16 v[156:159], v[148:151], a[236:239], 0\n"
        "s_nop 2\n"
        "v_mfma_f32_16x16x32_bf16 v[156:159], v[112:115], a[232:235], v[156:159]\n"
        "s_nop 0\n"
        "v_cvt_pk_bf16_f32 v152, v160, v161\n"
        "v_cvt_pk_bf16_f32 v153, v162, v163\n"
        "v_mfma_f32_16x16x32_bf16 v[156:159], v[120:123], a[228:231], v[156:159]\n"
        "s_nop 1\n"
        "v_mfma_f32_16x16x32_bf16 v[162:165], v[190:193], a[240:243], 0\n"
        "s_nop 2\n"
        "v_mfma_f32_16x16x32_bf16 v[162:165], v[214:217], a[244:247], v[162:165]\n"
        "s_nop 0\n"
        "v_cvt_pk_bf16_f32 v42, v156, 0\n"
        "v_mfma_f32_16x16x32_bf16 v[162:165], v[218:221], a[248:251], v[162:165]\n"
        "s_nop 1\n"
        "v_mfma_f32_16x16x32_bf16 v[166:169], v[186:189], a[248:251], 0\n"
        "s_nop 2\n"
        "v_mfma_f32_16x16x32_bf16 v[166:169], v[202:205], a[244:247], v[166:169]\n"
        "s_nop 0\n"
        "v_cvt_pk_bf16_f32 v160, v162, v163\n"
        "v_cvt_pk_bf16_f32 v161, v164, v165\n"
        "v_mfma_f32_16x16x32_bf16 v[166:169], v[210:213], a[240:243], v[166:169]\n"
        "s_nop 1\n"
        "v_mfma_f32_16x16x32_bf16 v[156:159], v[198:201], a[240:243], 0\n"
        "s_nop 2\n"
        "v_mfma_f32_16x16x32_bf16 v[156:159], v[206:209], a[244:247], v[156:159]\n"
        "s_nop 0\n"
        "v_cvt_pk_bf16_f32 v162, v166, v167\n"
        "v_cvt_pk_bf16_f32 v163, v168, v169\n"
        "v_mfma_f32_16x16x32_bf16 v[156:159], v[222:225], a[248:251], v[156:159]\n"
        "s_nop 1\n"
        "v_mfma_f32_16x16x32_bf16 v[164:167], v[148:151], a[248:251], 0\n"
        "s_nop 2\n"
        "v_mfma_f32_16x16x32_bf16 v[164:167], v[112:115], a[244:247], v[164:167]\n"
        "s_nop 0\n"
        "v_cvt_pk_bf16_f32 v140, v156, v157\n"
        "v_cvt_pk_bf16_f32 v141, v158, v159\n"
        "v_mfma_f32_16x16x32_bf16 v[164:167], v[120:123], a[240:243], v[164:167]\n"
        "s_nop 1\n"
        "v_mfma_f32_16x16x32_bf16 v[156:159], v[198:201], a[252:255], 0\n"
        "s_nop 2\n"
        "v_mfma_f32_16x16x32_bf16 v[156:159], v[206:209], v[72:75], v[156:159]\n"
        "s_nop 0\n"
        "v_cvt_pk_bf16_f32 v142, v164, 0\n"
        "v_mfma_f32_16x16x32_bf16 v[156:159], v[222:225], v[64:67], v[156:159]\n"
        "s_nop 1\n"
        "v_mfma_f32_16x16x32_bf16 v[164:167], v[186:189], v[64:67], 0\n"
        "s_nop 2\n"
        "v_mfma_f32_16x16x32_bf16 v[164:167], v[202:205], v[72:75], v[164:167]\n"
        "s_nop 0\n"
        "v_cvt_pk_bf16_f32 v116, v156, v157\n"
        "v_cvt_pk_bf16_f32 v117, v158, v159\n"
        "v_mfma_f32_16x16x32_bf16 v[164:167], v[210:213], a[252:255], v[164:167]\n"
        "s_nop 1\n"
        "v_mfma_f32_16x16x32_bf16 v[168:171], v[190:193], a[252:255], 0\n"
        "s_nop 2\n"
        "v_mfma_f32_16x16x32_bf16 v[168:171], v[214:217], v[72:75], v[168:171]\n"
        "s_nop 0\n"
        "v_cvt_pk_bf16_f32 v158, v164, v165\n"
        "v_cvt_pk_bf16_f32 v159, v166, v167\n"
        "v_mfma_f32_16x16x32_bf16 v[168:171], v[218:221], v[64:67], v[168:171]\n"
        "s_nop 1\n"
        "v_mfma_f32_16x16x32_bf16 v[164:167], v[148:151], v[64:67], 0\n"
        "s_nop 2\n"
        "v_mfma_f32_16x16x32_bf16 v[164:167], v[112:115], v[72:75], v[164:167]\n"
        "s_nop 0\n"
        "v_cvt_pk_bf16_f32 v156, v168, v169\n"
        "v_cvt_pk_bf16_f32 v157, v170, v171\n"
        "v_mfma_f32_16x16x32_bf16 v[164:167], v[120:123], a[252:255], v[164:167]\n"
        "s_nop 1\n"
        "v_mfma_f32_16x16x32_bf16 v[168:171], v[190:193], v[68:71], 0\n"
        "s_nop 2\n"
        "v_mfma_f32_16x16x32_bf16 v[168:171], v[214:217], v[60:63], v[168:171]\n"
        "s_nop 0\n"
        "v_cvt_pk_bf16_f32 v118, v164, 0\n"
        "v_mfma_f32_16x16x32_bf16 v[168:171], v[218:221], v[56:59], v[168:171]\n"
        "s_nop 1\n"
        "v_mfma_f32_16x16x32_bf16 v[176:179], v[186:189], v[56:59], 0\n"
        "s_nop 2\n"
        "v_mfma_f32_16x16x32_bf16 v[176:179], v[202:205], v[60:63], v[176:179]\n"
        "s_nop 0\n"
        "v_cvt_pk_bf16_f32 v164, v168, v169\n"
        "v_cvt_pk_bf16_f32 v165, v170, v171\n"
        "v_mfma_f32_16x16x32_bf16 v[176:179], v[210:213], v[68:71], v[176:179]\n"
        "s_nop 1\n"
        "v_mfma_f32_16x16x32_bf16 v[172:175], v[198:201], v[68:71], 0\n"
        "s_nop 2\n"
        "v_mfma_f32_16x16x32_bf16 v[172:175], v[206:209], v[60:63], v[172:175]\n"
        "s_nop 0\n"
        "v_cvt_pk_bf16_f32 v166, v176, v177\n"
        "v_cvt_pk_bf16_f32 v167, v178, v179\n"
        "v_mfma_f32_16x16x32_bf16 v[172:175], v[222:225], v[56:59], v[172:175]\n"
        "s_nop 1\n"
        "v_mfma_f32_16x16x32_bf16 v[168:171], v[148:151], v[56:59], 0\n"
        "s_nop 2\n"
        "v_mfma_f32_16x16x32_bf16 v[168:171], v[112:115], v[60:63], v[168:171]\n"
        "s_nop 0\n"
        "v_cvt_pk_bf16_f32 v104, v172, v173\n"
        "v_cvt_pk_bf16_f32 v105, v174, v175\n"
        "v_mfma_f32_16x16x32_bf16 v[168:171], v[120:123], v[68:71], v[168:171]\n"
        "s_nop 1\n"
        "v_mfma_f32_16x16x32_bf16 v[172:175], v[198:201], v[52:55], 0\n"
        "s_nop 2\n"
        "v_mfma_f32_16x16x32_bf16 v[172:175], v[206:209], v[76:79], v[172:175]\n"
        "s_nop 0\n"
        "v_cvt_pk_bf16_f32 v106, v168, 0\n"
        "v_mfma_f32_16x16x32_bf16 v[172:175], v[222:225], v[84:87], v[172:175]\n"
        "v_mfma_f32_16x16x32_bf16 v[230:233], v[186:189], v[84:87], 0\n"
        "v_mfma_f32_16x16x32_bf16 v[230:233], v[202:205], v[76:79], v[230:233]\n"
        "ds_read_b128 v[168:171], v252\n"
        "ds_read_b128 v[176:179], v252 offset:64\n"
        "v_mfma_f32_16x16x32_bf16 v[230:233], v[210:213], v[52:55], v[230:233]\n"
        "ds_read_b128 v[180:183], v252 offset:128\n"
        "ds_read_b32 v250, v251\n"
        "v_mfma_f32_16x16x32_bf16 v[226:229], v[190:193], v[52:55], 0\n"
        "ds_read_b32 v252, v251 offset:64\n"
        "ds_read_b32 v254, v251 offset:128\n"
        "v_mfma_f32_16x16x32_bf16 v[226:229], v[214:217], v[76:79], v[226:229]\n"
        "ds_read_b32 v253, v251 offset:192\n"
        "v_cvt_pk_bf16_f32 v34, v172, v173\n"
        "v_mfma_f32_16x16x32_bf16 v[226:229], v[218:221], v[84:87], v[226:229]\n"
        "s_nop 0\n"
        "v_cvt_pk_bf16_f32 v35, v174, v175\n"
        "v_cvt_pk_bf16_f32 v174, v230, v231\n"
        "v_mfma_f32_16x16x32_bf16 v[194:197], v[148:151], v[84:87], 0\n"
        "s_nop 0\n"
        "v_cvt_pk_bf16_f32 v175, v232, v233\n"
        "v_mfma_f32_16x16x32_bf16 v[194:197], v[112:115], v[76:79], v[194:197]\n"
        "s_nop 0\n"
        "v_cvt_pk_bf16_f32 v172, v226, v227\n"
        "v_cvt_pk_bf16_f32 v173, v228, v229\n"
        "v_mfma_f32_16x16x32_bf16 v[194:197], v[120:123], v[52:55], v[194:197]\n"
        "s_nop 1\n"
        "v_mfma_f32_16x16x32_bf16 v[226:229], a[144:147], v[190:193], 0\n"
        "s_nop 2\n"
        "v_mfma_f32_16x16x32_bf16 v[226:229], a[148:151], v[214:217], v[226:229]\n"
        "s_nop 0\n"
        "v_cvt_pk_bf16_f32 v36, v194, 0\n"
        "v_mfma_f32_16x16x32_bf16 v[226:229], a[152:155], v[218:221], v[226:229]\n"
        "s_nop 1\n"
        "v_mfma_f32_16x16x32_bf16 v[194:197], a[164:167], v[218:221], 0\n"
        "s_waitcnt lgkmcnt(0)\n"
        "s_nop 1\n"
        "v_mfma_f32_16x16x32_bf16 v[194:197], a[160:163], v[214:217], v[194:197]\n"
        "s_nop 0\n"
        "v_cvt_pk_bf16_f32 v238, v226, v227\n"
        "v_cvt_pk_bf16_f32 v239, v228, v229\n"
        "v_mfma_f32_16x16x32_bf16 v[194:197], a[156:159], v[190:193], v[194:197]\n"
        "s_nop 1\n"
        "v_mfma_f32_16x16x32_bf16 v[226:229], a[168:171], v[190:193], 0\n"
        "s_nop 2\n"
        "v_mfma_f32_16x16x32_bf16 v[226:229], a[172:175], v[214:217], v[226:229]\n"
        "s_nop 0\n"
        "v_cvt_pk_bf16_f32 v240, v194, v195\n"
        "v_cvt_pk_bf16_f32 v241, v196, v197\n"
        "v_mfma_f32_16x16x32_bf16 v[226:229], a[176:179], v[218:221], v[226:229]\n"
        "s_nop 1\n"
        "v_mfma_f32_16x16x32_bf16 v[194:197], a[188:191], v[218:221], 0\n"
        "s_nop 2\n"
        "v_mfma_f32_16x16x32_bf16 v[194:197], a[184:187], v[214:217], v[194:197]\n"
        "s_nop 0\n"
        "v_cvt_pk_bf16_f32 v234, v226, v227\n"
        "v_cvt_pk_bf16_f32 v235, v228, v229\n"
        "v_mfma_f32_16x16x32_bf16 v[194:197], a[180:183], v[190:193], v[194:197]\n"
        "s_nop 1\n"
        "v_mfma_f32_16x16x32_bf16 v[226:229], a[192:195], v[190:193], 0\n"
        "s_nop 2\n"
        "v_mfma_f32_16x16x32_bf16 v[226:229], a[196:199], v[214:217], v[226:229]\n"
        "s_nop 0\n"
        "v_cvt_pk_bf16_f32 v236, v194, v195\n"
        "v_cvt_pk_bf16_f32 v237, v196, v197\n"
        "v_mfma_f32_16x16x32_bf16 v[226:229], a[200:203], v[218:221], v[226:229]\n"
        "s_nop 1\n"
        "v_mfma_f32_16x16x32_bf16 v[230:233], a[212:215], v[218:221], 0\n"
        "s_nop 2\n"
        "v_mfma_f32_16x16x32_bf16 v[230:233], a[208:211], v[214:217], v[230:233]\n"
        "s_nop 0\n"
        "v_cvt_pk_bf16_f32 v194, v226, v227\n"
        "v_cvt_pk_bf16_f32 v195, v228, v229\n"
        "v_mfma_f32_16x16x32_bf16 v[230:233], a[204:207], v[190:193], v[230:233]\n"
        "s_nop 1\n"
        "v_mfma_f32_16x16x32_bf16 v[226:229], v[190:193], v[238:241], 0\n"
        "s_nop 2\n"
        "v_mfma_f32_16x16x32_bf16 v[226:229], v[214:217], v[234:237], v[226:229]\n"
        "s_nop 0\n"
        "v_cvt_pk_bf16_f32 v196, v230, v231\n"
        "v_cvt_pk_bf16_f32 v197, v232, v233\n"
        "s_nop 1\n"
        "v_mfma_f32_16x16x32_bf16 v[226:229], v[218:221], v[194:197], v[226:229]\n"
        "s_nop 1\n"
        "v_mfma_f32_16x16x32_bf16 v[230:233], v[186:189], v[194:197], 0\n"
        "s_nop 2\n"
        "v_mfma_f32_16x16x32_bf16 v[230:233], v[202:205], v[234:237], v[230:233]\n"
        "s_nop 0\n"
        "v_mul_f32 v226, v226, v168\n"
        "v_mul_f32 v227, v227, v169\n"
        "v_mfma_f32_16x16x32_bf16 v[230:233], v[210:213], v[238:241], v[230:233]\n"
        "s_nop 0\n"
        "v_mul_f32 v228, v228, v170\n"
        "v_mul_f32 v229, v229, v171\n"
        "v_mfma_f32_16x16x32_bf16 v[242:245], v[198:201], v[238:241], 0\n"
        "s_nop 0\n"
        "v_max3_f32 v251, v226, v227, v228\n"
        "v_mfma_f32_16x16x32_bf16 v[242:245], v[206:209], v[234:237], v[242:245]\n"
        "s_nop 0\n"
        "v_mul_f32 v230, v230, v176\n"
        "v_mul_f32 v231, v231, v177\n"
        "v_mfma_f32_16x16x32_bf16 v[242:245], v[222:225], v[194:197], v[242:245]\n"
        "v_mul_f32 v232, v232, v178\n"
        "v_mul_f32 v233, v233, v179\n"
        "v_mfma_f32_16x16x32_bf16 v[246:249], v[148:151], v[194:197], 0\n"
        "s_nop 0\n"
        "v_max3_f32 v251, v251, v229, v230\n"
        "v_max3_f32 v251, v251, v231, v232\n"
        "v_mfma_f32_16x16x32_bf16 v[246:249], v[112:115], v[234:237], v[246:249]\n"
        "s_nop 0\n"
        "v_mul_f32 v242, v242, v180\n"
        "v_mul_f32 v243, v243, v181\n"
        "v_mfma_f32_16x16x32_bf16 v[246:249], v[120:123], v[238:241], v[246:249]\n"
        "s_nop 0\n"
        "v_mul_f32 v244, v244, v182\n"
        "v_mul_f32 v245, v245, v183\n"
        "v_max3_f32 v251, v251, v233, v242\n"
        "v_max3_f32 v251, v251, v243, v244\n"
        "s_nop 2\n"
        "v_mul_f32 v246, v246, v253\n"
        "v_cndmask_b32_e64 v246, v255, v246, s[62:63]\n"
        "v_max3_f32 v251, v251, v245, v246\n"
        "v_mov_b32 v249, v251\n"
        "s_nop 1\n"
        "v_permlane32_swap_b32 v251, v249\n"
        "v_max_f32 v251, v251, v249\n"
        "v_mov_b32 v249, v251\n"
        "s_nop 1\n"
        "v_permlane16_swap_b32 v251, v249\n"
        "v_max_f32 v251, v251, v249\n"
        "v_mul_f32 v251, v251, v250\n"
        "v_fma_f32 v226, v226, v250, -v251\n"
        "v_exp_f32 v226, v226\n"
        "v_fma_f32 v227, v227, v250, -v251\n"
        "v_exp_f32 v227, v227\n"
        "s_nop 0\n"
        "v_add_f32 v249, v226, v227\n"
        "v_mul_f32 v226, v226, v168\n"
        "v_fma_f32 v246, v246, v250, -v251\n"
        "v_exp_f32 v246, v246\n"
        "v_fma_f32 v245, v245, v250, -v251\n"
        "v_exp_f32 v245, v245\n"
        "v_fma_f32 v244, v244, v250, -v251\n"
        "v_exp_f32 v244, v244\n"
        "v_fma_f32 v243, v243, v250, -v251\n"
        "v_exp_f32 v243, v243\n"
        "v_fma_f32 v242, v242, v250, -v251\n"
        "v_exp_f32 v242, v242\n"
        "v_fma_f32 v233, v233, v250, -v251\n"
        "v_exp_f32 v233, v233\n"
        "v_fma_f32 v232, v232, v250, -v251\n"
        "v_exp_f32 v232, v232\n"
        "v_fma_f32 v231, v231, v250, -v251\n"
        "v_exp_f32 v231, v231\n"
        "v_fma_f32 v230, v230, v250, -v251\n"
        "v_exp_f32 v230, v230\n"
        "v_fma_f32 v229, v229, v250, -v251\n"
        "v_exp_f32 v229, v229\n"
        "v_fma_f32 v228, v228, v250, -v251\n"
        "v_exp_f32 v228, v228\n"
        "s_nop 0\n"
        "v_add_f32 v249, v249, v228\n"
        "v_add_f32 v249, v249, v229\n"
        "v_add_f32 v249, v249, v230\n"
        "v_add_f32 v249, v249, v231\n"
        "v_add_f32 v249, v249, v232\n"
        "v_add_f32 v249, v249, v233\n"
        "v_add_f32 v249, v249, v242\n"
        "v_add_f32 v249, v249, v243\n"
        "v_add_f32 v249, v249, v244\n"
        "v_add_f32 v249, v249, v245\n"
        "v_add_f32 v249, v249, v246\n"
        "v_mov_b32 v251, v249\n"
        "s_nop 1\n"
        "v_permlane32_swap_b32 v249, v251\n"
        "v_add_f32 v249, v249, v251\n"
        "v_mov_b32 v251, v249\n"
        "s_nop 1\n"
        "v_permlane16_swap_b32 v249, v251\n"
        "v_add_f32 v249, v249, v251\n"
        "v_rcp_f32 v249, v249\n"
        "s_nop 0\n"
        "v_mul_f32 v226, v226, v249\n"
        "v_mul_f32 v227, v227, v169\n"
        "v_mul_f32 v227, v227, v249\n"
        "v_cvt_pk_bf16_f32 v234, v226, v227\n"
        "v_mul_f32 v228, v228, v170\n"
        "v_mul_f32 v228, v228, v249\n"
        "v_mul_f32 v229, v229, v171\n"
        "v_mul_f32 v229, v229, v249\n"
        "v_cvt_pk_bf16_f32 v235, v228, v229\n"
        "v_mul_f32 v230, v230, v176\n"
        "v_mul_f32 v230, v230, v249\n"
        "v_mul_f32 v231, v231, v177\n"
        "v_mul_f32 v231, v231, v249\n"
        "v_cvt_pk_bf16_f32 v236, v230, v231\n"
        "v_mul_f32 v232, v232, v178\n"
        "v_mul_f32 v232, v232, v249\n"
        "v_mul_f32 v233, v233, v179\n"
        "v_mul_f32 v233, v233, v249\n"
        "v_cvt_pk_bf16_f32 v237, v232, v233\n"
        "s_nop 1\n"
        "v_mfma_f32_16x16x32_bf16 v[194:197], v[234:237], v[128:131], 0\n"
        "v_mul_f32 v242, v242, v180\n"
        "v_mul_f32 v243, v243, v181\n"
        "v_mul_f32 v242, v242, v249\n"
        "v_mul_f32 v243, v243, v249\n"
        "v_cvt_pk_bf16_f32 v108, v242, v243\n"
        "v_mul_f32 v244, v244, v182\n"
        "v_mul_f32 v244, v244, v249\n"
        "v_mul_f32 v245, v245, v183\n"
        "v_mul_f32 v245, v245, v249\n"
        "v_cvt_pk_bf16_f32 v109, v244, v245\n"
        "v_mul_f32 v246, v246, v253\n"
        "v_mul_f32 v246, v246, v249\n"
        "v_cvt_pk_bf16_f32 v110, v246, 0\n"
        "s_nop 1\n"
        "v_mfma_f32_16x16x32_bf16 v[194:197], v[108:111], v[136:139], v[194:197]\n"
        "v_mfma_f32_16x16x32_bf16 v[194:197], v[190:193], v[88:91], v[194:197]\n"
        "v_mfma_f32_16x16x32_bf16 v[226:229], v[190:193], v[48:51], 0\n"
        "v_mfma_f32_16x16x32_bf16 v[226:229], v[108:111], v[40:43], v[226:229]\n"
        "v_mfma_f32_16x16x32_bf16 v[226:229], v[234:237], v[152:155], v[226:229]\n"
        "s_add_u32 s90, s89, 0\n"
        "s_nop 0\n"
        "v_mfma_f32_16x16x32_bf16 v[230:233], v[234:237], v[160:163], 0\n"
        "s_nop 2\n"
        "v_mfma_f32_16x16x32_bf16 v[230:233], v[108:111], v[140:143], v[230:233]\n"
        "s_nop 0\n"
        "v_cvt_pk_bf16_f32 v124, v194, v226\n"
        "v_cvt_pk_bf16_f32 v238, v195, v227\n"
        "v_mfma_f32_16x16x32_bf16 v[230:233], v[214:217], v[88:91], v[230:233]\n"
        "v_cvt_pk_bf16_f32 v132, v196, v228\n"
        "v_cvt_pk_bf16_f32 v144, v197, v229\n"
        "v_mfma_f32_16x16x32_bf16 v[194:197], v[214:217], v[48:51], 0\n"
        "s_add_u32 s91, s89, 98112\n"
        "v_mfma_f32_16x16x32_bf16 v[194:197], v[108:111], v[116:119], v[194:197]\n"
        "v_mfma_f32_16x16x32_bf16 v[194:197], v[234:237], v[156:159], v[194:197]\n"
        "s_nop 1\n"
        "v_mfma_f32_16x16x32_bf16 v[226:229], v[234:237], v[164:167], 0\n"
        "s_nop 2\n"
        "v_mfma_f32_16x16x32_bf16 v[226:229], v[108:111], v[104:107], v[226:229]\n"
        "s_nop 0\n"
        "v_cvt_pk_bf16_f32 v125, v230, v194\n"
        "v_cvt_pk_bf16_f32 v239, v231, v195\n"
        "v_mfma_f32_16x16x32_bf16 v[226:229], v[218:221], v[88:91], v[226:229]\n"
        "v_cvt_pk_bf16_f32 v133, v232, v196\n"
        "v_cvt_pk_bf16_f32 v145, v233, v197\n"
        "v_mfma_f32_16x16x32_bf16 v[194:197], v[218:221], v[48:51], 0\n"
        "v_mfma_f32_16x16x32_bf16 v[194:197], v[108:111], v[34:37], v[194:197]\n"
        "v_mfma_f32_16x16x32_bf16 v[194:197], v[234:237], v[172:175], v[194:197]\n"
        "s_nop 1\n"
        "v_mfma_f32_16x16x32_bf16 v[108:111], a[144:147], v[210:213], 0\n"
        "s_nop 2\n"
        "v_mfma_f32_16x16x32_bf16 v[108:111], a[148:151], v[202:205], v[108:111]\n"
        "s_nop 0\n"
        "v_cvt_pk_bf16_f32 v126, v226, v194\n"
        "v_cvt_pk_bf16_f32 v240, v227, v195\n"
        "v_mfma_f32_16x16x32_bf16 v[108:111], a[152:155], v[186:189], v[108:111]\n"
        "s_nop 0\n"
        "v_cvt_pk_bf16_f32 v134, v228, v196\n"
        "v_cvt_pk_bf16_f32 v146, v229, v197\n"
        "v_mfma_f32_16x16x32_bf16 v[226:229], a[164:167], v[186:189], 0\n"
        "s_nop 1\n"
        "v_mfma_f32_16x16x32_bf16 v[226:229], a[160:163], v[202:205], v[226:229]\n"
        "s_nop 0\n"
        "v_cvt_pk_bf16_f32 v194, v108, v109\n"
        "v_cvt_pk_bf16_f32 v195, v110, v111\n"
        "v_mfma_f32_16x16x32_bf16 v[226:229], a[156:159], v[210:213], v[226:229]\n"
        "s_nop 0\n"
        "v_mfma_f32_16x16x32_bf16 v[108:111], a[168:171], v[210:213], 0\n"
        "buffer_store_dwordx3 v[124:126], v14, s[40:43], s90 offen sc1 nt\n"
        "s_nop 2\n"
        "v_mfma_f32_16x16x32_bf16 v[108:111], a[172:175], v[202:205], v[108:111]\n"
        "s_nop 0\n"
        "v_cvt_pk_bf16_f32 v196, v226, v227\n"
        "v_cvt_pk_bf16_f32 v197, v228, v229\n"
        "v_mfma_f32_16x16x32_bf16 v[108:111], a[176:179], v[186:189], v[108:111]\n"
        "buffer_store_dwordx3 v[238:240], v15, s[40:43], s90 offen offset:768 sc1 nt\n"
        "s_nop 0\n"
        "v_mfma_f32_16x16x32_bf16 v[234:237], a[188:191], v[186:189], 0\n"
        "s_nop 2\n"
        "v_mfma_f32_16x16x32_bf16 v[234:237], a[184:187], v[202:205], v[234:237]\n"
        "buffer_store_dwordx3 v[132:134], v14, s[40:43], s91 offen offset:192 sc1 nt\n"
        "v_cvt_pk_bf16_f32 v226, v108, v109\n"
        "v_cvt_pk_bf16_f32 v227, v110, v111\n"
        "v_mfma_f32_16x16x32_bf16 v[234:237], a[180:183], v[210:213], v[234:237]\n"
        "s_nop 0\n"
        "v_mfma_f32_16x16x32_bf16 v[230:233], a[192:195], v[210:213], 0\n"
        "buffer_store_dwordx3 v[144:146], v16, s[40:43], s91 offen offset:960 sc1 nt\n"
        "s_nop 2\n"
        "v_mfma_f32_16x16x32_bf16 v[230:233], a[196:199], v[202:205], v[230:233]\n"
        "s_nop 0\n"
        "v_cvt_pk_bf16_f32 v228, v234, v235\n"
        "v_cvt_pk_bf16_f32 v229, v236, v237\n"
        "v_mfma_f32_16x16x32_bf16 v[230:233], a[200:203], v[186:189], v[230:233]\n"
        "s_nop 1\n"
        "v_mfma_f32_16x16x32_bf16 v[108:111], a[212:215], v[186:189], 0\n"
        "s_nop 2\n"
        "v_mfma_f32_16x16x32_bf16 v[108:111], a[208:211], v[202:205], v[108:111]\n"
        "s_nop 0\n"
        "v_cvt_pk_bf16_f32 v234, v230, v231\n"
        "v_cvt_pk_bf16_f32 v235, v232, v233\n"
        "v_mfma_f32_16x16x32_bf16 v[108:111], a[204:207], v[210:213], v[108:111]\n"
        "s_nop 1\n"
        "v_mfma_f32_16x16x32_bf16 v[238:241], v[190:193], v[194:197], 0\n"
        "s_nop 2\n"
        "v_mfma_f32_16x16x32_bf16 v[238:241], v[214:217], v[226:229], v[238:241]\n"
        "s_nop 0\n"
        "v_cvt_pk_bf16_f32 v236, v108, v109\n"
        "v_cvt_pk_bf16_f32 v237, v110, v111\n"
        "s_nop 1\n"
        "v_mfma_f32_16x16x32_bf16 v[238:241], v[218:221], v[234:237], v[238:241]\n"
        "s_nop 1\n"
        "v_mfma_f32_16x16x32_bf16 v[108:111], v[186:189], v[234:237], 0\n"
        "s_nop 2\n"
        "v_mfma_f32_16x16x32_bf16 v[108:111], v[202:205], v[226:229], v[108:111]\n"
        "s_nop 0\n"
        "v_mul_f32 v238, v238, v168\n"
        "v_mul_f32 v239, v239, v169\n"
        "v_mfma_f32_16x16x32_bf16 v[108:111], v[210:213], v[194:197], v[108:111]\n"
        "s_nop 0\n"
        "v_mul_f32 v240, v240, v170\n"
        "v_mul_f32 v241, v241, v171\n"
        "v_mfma_f32_16x16x32_bf16 v[230:233], v[198:201], v[194:197], 0\n"
        "s_nop 0\n"
        "v_max3_f32 v250, v238, v239, v240\n"
        "v_mfma_f32_16x16x32_bf16 v[230:233], v[206:209], v[226:229], v[230:233]\n"
        "s_nop 0\n"
        "v_mul_f32 v108, v108, v176\n"
        "v_mul_f32 v109, v109, v177\n"
        "v_mfma_f32_16x16x32_bf16 v[230:233], v[222:225], v[234:237], v[230:233]\n"
        "v_mul_f32 v110, v110, v178\n"
        "v_mul_f32 v111, v111, v179\n"
        "v_mfma_f32_16x16x32_bf16 v[242:245], v[148:151], v[234:237], 0\n"
        "s_nop 0\n"
        "v_max3_f32 v250, v250, v241, v108\n"
        "v_max3_f32 v250, v250, v109, v110\n"
        "v_mfma_f32_16x16x32_bf16 v[242:245], v[112:115], v[226:229], v[242:245]\n"
        "s_nop 0\n"
        "v_mul_f32 v230, v230, v180\n"
        "v_mul_f32 v231, v231, v181\n"
        "v_mfma_f32_16x16x32_bf16 v[242:245], v[120:123], v[194:197], v[242:245]\n"
        "s_nop 0\n"
        "v_mul_f32 v232, v232, v182\n"
        "v_mul_f32 v233, v233, v183\n"
        "v_max3_f32 v250, v250, v111, v230\n"
        "v_max3_f32 v250, v250, v231, v232\n"
        "s_nop 2\n"
        "v_mul_f32 v242, v242, v253\n"
        "v_cndmask_b32_e64 v242, v255, v242, s[62:63]\n"
        "v_max3_f32 v250, v250, v233, v242\n"
        "v_mov_b32 v251, v250\n"
        "s_nop 1\n"
        "v_permlane32_swap_b32 v250, v251\n"
        "v_max_f32 v250, v250, v251\n"
        "v_mov_b32 v251, v250\n"
        "s_nop 1\n"
        "v_permlane16_swap_b32 v250, v251\n"
        "v_max_f32 v250, v250, v251\n"
        "v_mul_f32 v250, v250, v252\n"
        "v_fma_f32 v238, v238, v252, -v250\n"
        "v_exp_f32 v238, v238\n"
        "v_fma_f32 v239, v239, v252, -v250\n"
        "v_exp_f32 v239, v239\n"
        "s_nop 0\n"
        "v_add_f32 v251, v238, v239\n"
        "v_mul_f32 v238, v238, v168\n"
        "v_fma_f32 v242, v242, v252, -v250\n"
        "v_exp_f32 v242, v242\n"
        "v_fma_f32 v233, v233, v252, -v250\n"
        "v_exp_f32 v233, v233\n"
        "v_fma_f32 v232, v232, v252, -v250\n"
        "v_exp_f32 v232, v232\n"
        "v_fma_f32 v231, v231, v252, -v250\n"
        "v_exp_f32 v231, v231\n"
        "v_fma_f32 v230, v230, v252, -v250\n"
        "v_exp_f32 v230, v230\n"
        "v_fma_f32 v111, v111, v252, -v250\n"
        "v_exp_f32 v111, v111\n"
        "v_fma_f32 v110, v110, v252, -v250\n"
        "v_exp_f32 v110, v110\n"
        "v_fma_f32 v109, v109, v252, -v250\n"
        "v_exp_f32 v109, v109\n"
        "v_fma_f32 v108, v108, v252, -v250\n"
        "v_exp_f32 v108, v108\n"
        "v_fma_f32 v241, v241, v252, -v250\n"
        "v_exp_f32 v241, v241\n"
        "v_fma_f32 v240, v240, v252, -v250\n"
        "v_exp_f32 v240, v240\n"
        "s_nop 0\n"
        "v_add_f32 v251, v251, v240\n"
        "v_add_f32 v251, v251, v241\n"
        "v_add_f32 v251, v251, v108\n"
        "v_add_f32 v251, v251, v109\n"
        "v_add_f32 v251, v251, v110\n"
        "v_add_f32 v251, v251, v111\n"
        "v_add_f32 v251, v251, v230\n"
        "v_add_f32 v251, v251, v231\n"
        "v_add_f32 v251, v251, v232\n"
        "v_add_f32 v251, v251, v233\n"
        "v_add_f32 v251, v251, v242\n"
        "v_mov_b32 v252, v251\n"
        "s_nop 1\n"
        "v_permlane32_swap_b32 v251, v252\n"
        "v_add_f32 v251, v251, v252\n"
        "v_mov_b32 v252, v251\n"
        "s_nop 1\n"
        "v_permlane16_swap_b32 v251, v252\n"
        "v_add_f32 v251, v251, v252\n"
        "v_rcp_f32 v251, v251\n"
        "s_nop 0\n"
        "v_mul_f32 v238, v238, v251\n"
        "v_mul_f32 v239, v239, v169\n"
        "v_mul_f32 v239, v239, v251\n"
        "v_cvt_pk_bf16_f32 v226, v238, v239\n"
        "v_mul_f32 v240, v240, v170\n"
        "v_mul_f32 v240, v240, v251\n"
        "v_mul_f32 v241, v241, v171\n"
        "v_mul_f32 v241, v241, v251\n"
        "v_cvt_pk_bf16_f32 v227, v240, v241\n"
        "v_mul_f32 v108, v108, v176\n"
        "v_mul_f32 v108, v108, v251\n"
        "v_mul_f32 v109, v109, v177\n"
        "v_mul_f32 v109, v109, v251\n"
        "v_cvt_pk_bf16_f32 v228, v108, v109\n"
        "v_mul_f32 v110, v110, v178\n"
        "v_mul_f32 v110, v110, v251\n"
        "v_mul_f32 v111, v111, v179\n"
        "v_mul_f32 v111, v111, v251\n"
        "v_cvt_pk_bf16_f32 v229, v110, v111\n"
        "s_nop 1\n"
        "v_mfma_f32_16x16x32_bf16 v[108:111], v[226:229], v[128:131], 0\n"
        "v_mul_f32 v230, v230, v180\n"
        "v_mul_f32 v231, v231, v181\n"
        "v_mul_f32 v230, v230, v251\n"
        "v_mul_f32 v231, v231, v251\n"
        "v_cvt_pk_bf16_f32 v144, v230, v231\n"
        "v_mul_f32 v232, v232, v182\n"
        "v_mul_f32 v232, v232, v251\n"
        "v_mul_f32 v233, v233, v183\n"
        "v_mul_f32 v233, v233, v251\n"
        "v_cvt_pk_bf16_f32 v145, v232, v233\n"
        "v_mul_f32 v242, v242, v253\n"
        "v_mul_f32 v242, v242, v251\n"
        "v_cvt_pk_bf16_f32 v146, v242, 0\n"
        "s_nop 1\n"
        "v_mfma_f32_16x16x32_bf16 v[108:111], v[144:147], v[136:139], v[108:111]\n"
        "v_mfma_f32_16x16x32_bf16 v[108:111], v[210:213], v[88:91], v[108:111]\n"
        "v_mfma_f32_16x16x32_bf16 v[230:233], v[210:213], v[48:51], 0\n"
        "v_mfma_f32_16x16x32_bf16 v[230:233], v[144:147], v[40:43], v[230:233]\n"
        "v_mfma_f32_16x16x32_bf16 v[230:233], v[226:229], v[152:155], v[230:233]\n"
        "s_add_u32 s90, s89, 196224\n"
        "s_nop 0\n"
        "v_mfma_f32_16x16x32_bf16 v[194:197], v[226:229], v[160:163], 0\n"
        "s_nop 2\n"
        "v_mfma_f32_16x16x32_bf16 v[194:197], v[144:147], v[140:143], v[194:197]\n"
        "s_nop 0\n"
        "v_cvt_pk_bf16_f32 v238, v108, v230\n"
        "v_cvt_pk_bf16_f32 v234, v109, v231\n"
        "v_mfma_f32_16x16x32_bf16 v[194:197], v[202:205], v[88:91], v[194:197]\n"
        "v_cvt_pk_bf16_f32 v132, v110, v232\n"
        "v_cvt_pk_bf16_f32 v124, v111, v233\n"
        "v_mfma_f32_16x16x32_bf16 v[230:233], v[202:205], v[48:51], 0\n"
        "s_add_u32 s91, s89, 294336\n"
        "v_mfma_f32_16x16x32_bf16 v[230:233], v[144:147], v[116:119], v[230:233]\n"
        "v_mfma_f32_16x16x32_bf16 v[230:233], v[226:229], v[156:159], v[230:233]\n"
        "s_nop 1\n"
        "v_mfma_f32_16x16x32_bf16 v[108:111], v[226:229], v[164:167], 0\n"
        "s_nop 2\n"
        "v_mfma_f32_16x16x32_bf16 v[108:111], v[144:147], v[104:107], v[108:111]\n"
        "s_nop 0\n"
        "v_cvt_pk_bf16_f32 v239, v194, v230\n"
        "v_cvt_pk_bf16_f32 v235, v195, v231\n"
        "v_mfma_f32_16x16x32_bf16 v[108:111], v[186:189], v[88:91], v[108:111]\n"
        "v_cvt_pk_bf16_f32 v133, v196, v232\n"
        "v_cvt_pk_bf16_f32 v125, v197, v233\n"
        "v_mfma_f32_16x16x32_bf16 v[194:197], v[186:189], v[48:51], 0\n"
        "v_mfma_f32_16x16x32_bf16 v[194:197], v[144:147], v[34:37], v[194:197]\n"
        "v_mfma_f32_16x16x32_bf16 v[194:197], v[226:229], v[172:175], v[194:197]\n"
        "s_nop 1\n"
        "v_mfma_f32_16x16x32_bf16 v[144:147], a[144:147], v[198:201], 0\n"
        "s_nop 2\n"
        "v_mfma_f32_16x16x32_bf16 v[144:147], a[148:151], v[206:209], v[144:147]\n"
        "s_nop 0\n"
        "v_cvt_pk_bf16_f32 v240, v108, v194\n"
        "v_cvt_pk_bf16_f32 v236, v109, v195\n"
        "v_mfma_f32_16x16x32_bf16 v[144:147], a[152:155], v[222:225], v[144:147]\n"
        "s_nop 0\n"
        "v_cvt_pk_bf16_f32 v134, v110, v196\n"
        "v_cvt_pk_bf16_f32 v126, v111, v197\n"
        "v_mfma_f32_16x16x32_bf16 v[194:197], a[164:167], v[222:225], 0\n"
        "s_nop 1\n"
        "v_mfma_f32_16x16x32_bf16 v[194:197], a[160:163], v[206:209], v[194:197]\n"
        "s_nop 0\n"
        "v_cvt_pk_bf16_f32 v226, v144, v145\n"
        "v_cvt_pk_bf16_f32 v227, v146, v147\n"
        "v_mfma_f32_16x16x32_bf16 v[194:197], a[156:159], v[198:201], v[194:197]\n"
        "s_nop 0\n"
        "v_mfma_f32_16x16x32_bf16 v[108:111], a[168:171], v[198:201], 0\n"
        "buffer_store_dwordx3 v[238:240], v14, s[40:43], s90 offen offset:384 sc1 nt\n"
        "s_nop 2\n"
        "v_mfma_f32_16x16x32_bf16 v[108:111], a[172:175], v[206:209], v[108:111]\n"
        "s_nop 0\n"
        "v_cvt_pk_bf16_f32 v228, v194, v195\n"
        "v_cvt_pk_bf16_f32 v229, v196, v197\n"
        "v_mfma_f32_16x16x32_bf16 v[108:111], a[176:179], v[222:225], v[108:111]\n"
        "buffer_store_dwordx3 v[234:236], v17, s[40:43], s90 offen offset:1152 sc1 nt\n"
        "s_add_u32 s90, s89, 392448\n"
        "v_mfma_f32_16x16x32_bf16 v[194:197], a[188:191], v[222:225], 0\n"
        "s_nop 2\n"
        "v_mfma_f32_16x16x32_bf16 v[194:197], a[184:187], v[206:209], v[194:197]\n"
        "buffer_store_dwordx3 v[132:134], v14, s[40:43], s91 offen offset:576 sc1 nt\n"
        "v_cvt_pk_bf16_f32 v144, v108, v109\n"
        "v_cvt_pk_bf16_f32 v145, v110, v111\n"
        "v_mfma_f32_16x16x32_bf16 v[194:197], a[180:183], v[198:201], v[194:197]\n"
        "s_nop 0\n"
        "v_mfma_f32_16x16x32_bf16 v[108:111], a[192:195], v[198:201], 0\n"
        "buffer_store_dwordx3 v[124:126], v14, s[40:43], s90 offen sc1 nt\n"
        "s_nop 2\n"
        "v_mfma_f32_16x16x32_bf16 v[108:111], a[196:199], v[206:209], v[108:111]\n"
        "s_nop 0\n"
        "v_cvt_pk_bf16_f32 v146, v194, v195\n"
        "v_cvt_pk_bf16_f32 v147, v196, v197\n"
        "v_mfma_f32_16x16x32_bf16 v[108:111], a[200:203], v[222:225], v[108:111]\n"
        "s_nop 1\n"
        "v_mfma_f32_16x16x32_bf16 v[194:197], a[212:215], v[222:225], 0\n"
        "s_nop 2\n"
        "v_mfma_f32_16x16x32_bf16 v[194:197], a[208:211], v[206:209], v[194:197]\n"
        "s_nop 0\n"
        "v_cvt_pk_bf16_f32 v230, v108, v109\n"
        "v_cvt_pk_bf16_f32 v231, v110, v111\n"
        "v_mfma_f32_16x16x32_bf16 v[194:197], a[204:207], v[198:201], v[194:197]\n"
        "s_nop 1\n"
        "v_mfma_f32_16x16x32_bf16 v[108:111], v[190:193], v[226:229], 0\n"
        "s_nop 2\n"
        "v_mfma_f32_16x16x32_bf16 v[108:111], v[214:217], v[144:147], v[108:111]\n"
        "s_nop 0\n"
        "v_cvt_pk_bf16_f32 v232, v194, v195\n"
        "v_cvt_pk_bf16_f32 v233, v196, v197\n"
        "s_nop 1\n"
        "v_mfma_f32_16x16x32_bf16 v[108:111], v[218:221], v[230:233], v[108:111]\n"
        "s_nop 1\n"
        "v_mfma_f32_16x16x32_bf16 v[234:237], v[186:189], v[230:233], 0\n"
        "s_nop 2\n"
        "v_mfma_f32_16x16x32_bf16 v[234:237], v[202:205], v[144:147], v[234:237]\n"
        "s_nop 0\n"
        "v_mul_f32 v108, v108, v168\n"
        "v_mul_f32 v109, v109, v169\n"
        "v_mfma_f32_16x16x32_bf16 v[234:237], v[210:213], v[226:229], v[234:237]\n"
        "s_nop 0\n"
        "v_mul_f32 v110, v110, v170\n"
        "v_mul_f32 v111, v111, v171\n"
        "v_mfma_f32_16x16x32_bf16 v[238:241], v[198:201], v[226:229], 0\n"
        "s_nop 0\n"
        "v_max3_f32 v251, v108, v109, v110\n"
        "v_mfma_f32_16x16x32_bf16 v[238:241], v[206:209], v[144:147], v[238:241]\n"
        "s_nop 0\n"
        "v_mul_f32 v234, v234, v176\n"
        "v_mul_f32 v235, v235, v177\n"
        "v_mfma_f32_16x16x32_bf16 v[238:241], v[222:225], v[230:233], v[238:241]\n"
        "v_mul_f32 v236, v236, v178\n"
        "v_mul_f32 v237, v237, v179\n"
        "v_mfma_f32_16x16x32_bf16 v[194:197], v[148:151], v[230:233], 0\n"
        "s_nop 0\n"
        "v_max3_f32 v251, v251, v111, v234\n"
        "v_max3_f32 v251, v251, v235, v236\n"
        "v_mfma_f32_16x16x32_bf16 v[194:197], v[112:115], v[144:147], v[194:197]\n"
        "s_nop 0\n"
        "v_mul_f32 v238, v238, v180\n"
        "v_mul_f32 v239, v239, v181\n"
        "v_mfma_f32_16x16x32_bf16 v[194:197], v[120:123], v[226:229], v[194:197]\n"
        "s_nop 0\n"
        "v_mul_f32 v240, v240, v182\n"
        "v_mul_f32 v241, v241, v183\n"
        "v_max3_f32 v251, v251, v237, v238\n"
        "v_max3_f32 v251, v251, v239, v240\n"
        "s_nop 2\n"
        "v_mul_f32 v194, v194, v253\n"
        "v_cndmask_b32_e64 v194, v255, v194, s[62:63]\n"
        "v_max3_f32 v251, v251, v241, v194\n"
        "v_mov_b32 v252, v251\n"
        "s_nop 1\n"
        "v_permlane32_swap_b32 v251, v252\n"
        "v_max_f32 v251, v251, v252\n"
        "v_mov_b32 v252, v251\n"
        "s_nop 1\n"
        "v_permlane16_swap_b32 v251, v252\n"
        "v_max_f32 v251, v251, v252\n"
        "v_mul_f32 v251, v251, v254\n"
        "v_fma_f32 v108, v108, v254, -v251\n"
        "v_exp_f32 v108, v108\n"
        "v_fma_f32 v109, v109, v254, -v251\n"
        "v_exp_f32 v109, v109\n"
        "s_nop 0\n"
        "v_add_f32 v252, v108, v109\n"
        "v_mul_f32 v108, v108, v168\n"
        "v_fma_f32 v194, v194, v254, -v251\n"
        "v_exp_f32 v194, v194\n"
        "v_fma_f32 v241, v241, v254, -v251\n"
        "v_exp_f32 v241, v241\n"
        "v_fma_f32 v240, v240, v254, -v251\n"
        "v_exp_f32 v240, v240\n"
        "v_fma_f32 v239, v239, v254, -v251\n"
        "v_exp_f32 v239, v239\n"
        "v_fma_f32 v238, v238, v254, -v251\n"
        "v_exp_f32 v238, v238\n"
        "v_fma_f32 v237, v237, v254, -v251\n"
        "v_exp_f32 v237, v237\n"
        "v_fma_f32 v236, v236, v254, -v251\n"
        "v_exp_f32 v236, v236\n"
        "v_fma_f32 v235, v235, v254, -v251\n"
        "v_exp_f32 v235, v235\n"
        "v_fma_f32 v234, v234, v254, -v251\n"
        "v_exp_f32 v234, v234\n"
        "v_fma_f32 v111, v111, v254, -v251\n"
        "v_exp_f32 v111, v111\n"
        "v_fma_f32 v110, v110, v254, -v251\n"
        "v_exp_f32 v110, v110\n"
        "s_nop 0\n"
        "v_add_f32 v252, v252, v110\n"
        "v_add_f32 v252, v252, v111\n"
        "v_add_f32 v252, v252, v234\n"
        "v_add_f32 v252, v252, v235\n"
        "v_add_f32 v252, v252, v236\n"
        "v_add_f32 v252, v252, v237\n"
        "v_add_f32 v252, v252, v238\n"
        "v_add_f32 v252, v252, v239\n"
        "v_add_f32 v252, v252, v240\n"
        "v_add_f32 v252, v252, v241\n"
        "v_add_f32 v252, v252, v194\n"
        "v_mov_b32 v254, v252\n"
        "s_nop 1\n"
        "v_permlane32_swap_b32 v252, v254\n"
        "v_add_f32 v252, v252, v254\n"
        "v_mov_b32 v254, v252\n"
        "s_nop 1\n"
        "v_permlane16_swap_b32 v252, v254\n"
        "v_add_f32 v252, v252, v254\n"
        "v_rcp_f32 v252, v252\n"
        "s_nop 0\n"
        "v_mul_f32 v108, v108, v252\n"
        "v_mul_f32 v109, v109, v169\n"
        "v_mul_f32 v109, v109, v252\n"
        "v_cvt_pk_bf16_f32 v226, v108, v109\n"
        "v_mul_f32 v110, v110, v170\n"
        "v_mul_f32 v110, v110, v252\n"
        "v_mul_f32 v111, v111, v171\n"
        "v_mul_f32 v111, v111, v252\n"
        "v_cvt_pk_bf16_f32 v227, v110, v111\n"
        "v_mul_f32 v234, v234, v176\n"
        "v_mul_f32 v234, v234, v252\n"
        "v_mul_f32 v235, v235, v177\n"
        "v_mul_f32 v235, v235, v252\n"
        "v_cvt_pk_bf16_f32 v228, v234, v235\n"
        "v_mul_f32 v236, v236, v178\n"
        "v_mul_f32 v236, v236, v252\n"
        "v_mul_f32 v237, v237, v179\n"
        "v_mul_f32 v237, v237, v252\n"
        "v_cvt_pk_bf16_f32 v229, v236, v237\n"
        "s_nop 1\n"
        "v_mfma_f32_16x16x32_bf16 v[108:111], v[226:229], v[128:131], 0\n"
        "v_mul_f32 v238, v238, v180\n"
        "v_mul_f32 v239, v239, v181\n"
        "v_mul_f32 v238, v238, v252\n"
        "v_mul_f32 v239, v239, v252\n"
        "v_cvt_pk_bf16_f32 v132, v238, v239\n"
        "v_mul_f32 v240, v240, v182\n"
        "v_mul_f32 v240, v240, v252\n"
        "v_mul_f32 v241, v241, v183\n"
        "v_mul_f32 v241, v241, v252\n"
        "v_cvt_pk_bf16_f32 v133, v240, v241\n"
        "v_mul_f32 v194, v194, v253\n"
        "v_mul_f32 v194, v194, v252\n"
        "v_cvt_pk_bf16_f32 v134, v194, 0\n"
        "s_nop 1\n"
        "v_mfma_f32_16x16x32_bf16 v[108:111], v[132:135], v[136:139], v[108:111]\n"
        "v_mfma_f32_16x16x32_bf16 v[108:111], v[198:201], v[88:91], v[108:111]\n"
        "v_mfma_f32_16x16x32_bf16 v[194:197], v[198:201], v[48:51], 0\n"
        "v_mfma_f32_16x16x32_bf16 v[194:197], v[132:135], v[40:43], v[194:197]\n"
        "v_mfma_f32_16x16x32_bf16 v[194:197], v[226:229], v[152:155], v[194:197]\n"
        "s_nop 1\n"
        "v_mfma_f32_16x16x32_bf16 v[144:147], v[226:229], v[160:163], 0\n"
        "s_nop 2\n"
        "v_mfma_f32_16x16x32_bf16 v[144:147], v[132:135], v[140:143], v[144:147]\n"
        "s_add_u32 s91, s89, 490560\n"
        "v_cvt_pk_bf16_f32 v238, v108, v194\n"
        "v_cvt_pk_bf16_f32 v124, v109, v195\n"
        "v_mfma_f32_16x16x32_bf16 v[144:147], v[206:209], v[88:91], v[144:147]\n"
        "v_cvt_pk_bf16_f32 v230, v110, v196\n"
        "v_cvt_pk_bf16_f32 v234, v111, v197\n"
        "v_mfma_f32_16x16x32_bf16 v[108:111], v[206:209], v[48:51], 0\n"
        "v_mfma_f32_16x16x32_bf16 v[108:111], v[132:135], v[116:119], v[108:111]\n"
        "v_mfma_f32_16x16x32_bf16 v[108:111], v[226:229], v[156:159], v[108:111]\n"
        "s_nop 1\n"
        "v_mfma_f32_16x16x32_bf16 v[194:197], v[226:229], v[164:167], 0\n"
        "s_nop 2\n"
        "v_mfma_f32_16x16x32_bf16 v[194:197], v[132:135], v[104:107], v[194:197]\n"
        "s_nop 0\n"
        "v_cvt_pk_bf16_f32 v239, v144, v108\n"
        "v_cvt_pk_bf16_f32 v125, v145, v109\n"
        "v_mfma_f32_16x16x32_bf16 v[194:197], v[222:225], v[88:91], v[194:197]\n"
        "v_cvt_pk_bf16_f32 v231, v146, v110\n"
        "v_cvt_pk_bf16_f32 v235, v147, v111\n"
        "v_mfma_f32_16x16x32_bf16 v[108:111], v[222:225], v[48:51], 0\n"
        "v_mfma_f32_16x16x32_bf16 v[108:111], v[132:135], v[34:37], v[108:111]\n"
        "v_mfma_f32_16x16x32_bf16 v[108:111], v[226:229], v[172:175], v[108:111]\n"
        "s_nop 1\n"
        "v_mfma_f32_16x16x32_bf16 v[144:147], a[144:147], v[120:123], 0\n"
        "s_nop 2\n"
        "v_mfma_f32_16x16x32_bf16 v[144:147], a[148:151], v[112:115], v[144:147]\n"
        "s_nop 0\n"
        "v_cvt_pk_bf16_f32 v240, v194, v108\n"
        "v_cvt_pk_bf16_f32 v126, v195, v109\n"
        "v_mfma_f32_16x16x32_bf16 v[144:147], a[152:155], v[148:151], v[144:147]\n"
        "s_nop 0\n"
        "v_cvt_pk_bf16_f32 v232, v196, v110\n"
        "v_cvt_pk_bf16_f32 v236, v197, v111\n"
        "v_mfma_f32_16x16x32_bf16 v[132:135], a[164:167], v[148:151], 0\n"
        "s_nop 1\n"
        "v_mfma_f32_16x16x32_bf16 v[132:135], a[160:163], v[112:115], v[132:135]\n"
        "s_nop 0\n"
        "v_cvt_pk_bf16_f32 v108, v144, v145\n"
        "v_cvt_pk_bf16_f32 v109, v146, v147\n"
        "v_mfma_f32_16x16x32_bf16 v[132:135], a[156:159], v[120:123], v[132:135]\n"
        "s_nop 0\n"
        "v_mfma_f32_16x16x32_bf16 v[194:197], a[168:171], v[120:123], 0\n"
        "buffer_store_dwordx3 v[238:240], v15, s[40:43], s90 offen offset:768 sc1 nt\n"
        "s_add_u32 s90, s89, 588672\n"
        "s_nop 1\n"
        "v_mfma_f32_16x16x32_bf16 v[194:197], a[172:175], v[112:115], v[194:197]\n"
        "s_nop 0\n"
        "v_cvt_pk_bf16_f32 v110, v132, v133\n"
        "v_cvt_pk_bf16_f32 v111, v134, v135\n"
        "v_mfma_f32_16x16x32_bf16 v[194:197], a[176:179], v[148:151], v[194:197]\n"
        "buffer_store_dwordx3 v[124:126], v14, s[40:43], s91 offen offset:192 sc1 nt\n"
        "s_nop 0\n"
        "v_mfma_f32_16x16x32_bf16 v[132:135], a[188:191], v[148:151], 0\n"
        "s_nop 2\n"
        "v_mfma_f32_16x16x32_bf16 v[132:135], a[184:187], v[112:115], v[132:135]\n"
        "buffer_store_dwordx3 v[230:232], v16, s[40:43], s91 offen offset:960 sc1 nt\n"
        "v_cvt_pk_bf16_f32 v144, v194, v195\n"
        "v_cvt_pk_bf16_f32 v145, v196, v197\n"
        "v_mfma_f32_16x16x32_bf16 v[132:135], a[180:183], v[120:123], v[132:135]\n"
        "s_nop 0\n"
        "v_mfma_f32_16x16x32_bf16 v[194:197], a[192:195], v[120:123], 0\n"
        "buffer_store_dwordx3 v[234:236], v14, s[40:43], s90 offen offset:384 sc1 nt\n"
        "s_nop 2\n"
        "v_mfma_f32_16x16x32_bf16 v[194:197], a[196:199], v[112:115], v[194:197]\n"
        "s_nop 0\n"
        "v_cvt_pk_bf16_f32 v146, v132, v133\n"
        "v_cvt_pk_bf16_f32 v147, v134, v135\n"
        "v_mfma_f32_16x16x32_bf16 v[194:197], a[200:203], v[148:151], v[194:197]\n"
        "s_nop 1\n"
        "v_mfma_f32_16x16x32_bf16 v[132:135], a[212:215], v[148:151], 0\n"
        "s_nop 2\n"
        "v_mfma_f32_16x16x32_bf16 v[132:135], a[208:211], v[112:115], v[132:135]\n"
        "s_nop 0\n"
        "v_cvt_pk_bf16_f32 v226, v194, v195\n"
        "v_cvt_pk_bf16_f32 v227, v196, v197\n"
        "v_mfma_f32_16x16x32_bf16 v[132:135], a[204:207], v[120:123], v[132:135]\n"
        "s_nop 1\n"
        "v_mfma_f32_16x16x32_bf16 v[194:197], v[190:193], v[108:111], 0\n"
        "s_nop 2\n"
        "v_mfma_f32_16x16x32_bf16 v[194:197], v[214:217], v[144:147], v[194:197]\n"
        "s_nop 0\n"
        "v_cvt_pk_bf16_f32 v228, v132, v133\n"
        "v_cvt_pk_bf16_f32 v229, v134, v135\n"
        "s_nop 1\n"
        "v_mfma_f32_16x16x32_bf16 v[194:197], v[218:221], v[226:229], v[194:197]\n"
        "s_nop 1\n"
        "v_mfma_f32_16x16x32_bf16 v[190:193], v[186:189], v[226:229], 0\n"
        "s_nop 2\n"
        "v_mfma_f32_16x16x32_bf16 v[190:193], v[202:205], v[144:147], v[190:193]\n"
        "s_nop 0\n"
        "v_mul_f32 v194, v194, v168\n"
        "v_mul_f32 v195, v195, v169\n"
        "v_mfma_f32_16x16x32_bf16 v[190:193], v[210:213], v[108:111], v[190:193]\n"
        "s_nop 0\n"
        "v_mul_f32 v196, v196, v170\n"
        "v_mul_f32 v197, v197, v171\n"
        "v_mfma_f32_16x16x32_bf16 v[184:187], v[198:201], v[108:111], 0\n"
        "s_nop 0\n"
        "v_max3_f32 v254, v194, v195, v196\n"
        "v_mfma_f32_16x16x32_bf16 v[184:187], v[206:209], v[144:147], v[184:187]\n"
        "s_nop 0\n"
        "v_mul_f32 v190, v190, v176\n"
        "v_mul_f32 v191, v191, v177\n"
        "v_mfma_f32_16x16x32_bf16 v[184:187], v[222:225], v[226:229], v[184:187]\n"
        "v_mul_f32 v192, v192, v178\n"
        "v_mul_f32 v193, v193, v179\n"
        "v_mfma_f32_16x16x32_bf16 v[132:135], v[148:151], v[226:229], 0\n"
        "s_nop 0\n"
        "v_max3_f32 v254, v254, v197, v190\n"
        "v_max3_f32 v254, v254, v191, v192\n"
        "v_mfma_f32_16x16x32_bf16 v[132:135], v[112:115], v[144:147], v[132:135]\n"
        "s_nop 0\n"
        "v_mul_f32 v184, v184, v180\n"
        "v_mul_f32 v185, v185, v181\n"
        "v_mfma_f32_16x16x32_bf16 v[132:135], v[120:123], v[108:111], v[132:135]\n"
        "s_nop 0\n"
        "v_mul_f32 v186, v186, v182\n"
        "v_mul_f32 v187, v187, v183\n"
        "v_max3_f32 v254, v254, v193, v184\n"
        "v_max3_f32 v254, v254, v185, v186\n"
        "s_nop 2\n"
        "v_mul_f32 v132, v132, v253\n"
        "v_cndmask_b32_e64 v132, v255, v132, s[62:63]\n"
        "v_max3_f32 v254, v254, v187, v132\n"
        "v_mov_b32 v255, v254\n"
        "s_nop 1\n"
        "v_permlane32_swap_b32 v254, v255\n"
        "v_max_f32 v254, v254, v255\n"
        "v_mov_b32 v255, v254\n"
        "s_nop 1\n"
        "v_permlane16_swap_b32 v254, v255\n"
        "v_max_f32 v254, v254, v255\n"
        "v_mul_f32 v254, v254, v253\n"
        "v_fma_f32 v194, v194, v253, -v254\n"
        "v_exp_f32 v194, v194\n"
        "v_fma_f32 v195, v195, v253, -v254\n"
        "v_exp_f32 v195, v195\n"
        "s_nop 0\n"
        "v_add_f32 v255, v194, v195\n"
        "v_mul_f32 v194, v194, v168\n"
        "v_fma_f32 v132, v132, v253, -v254\n"
        "v_exp_f32 v132, v132\n"
        "v_fma_f32 v187, v187, v253, -v254\n"
        "v_exp_f32 v187, v187\n"
        "v_fma_f32 v186, v186, v253, -v254\n"
        "v_exp_f32 v186, v186\n"
        "v_fma_f32 v185, v185, v253, -v254\n"
        "v_exp_f32 v185, v185\n"
        "v_fma_f32 v184, v184, v253, -v254\n"
        "v_exp_f32 v184, v184\n"
        "v_fma_f32 v193, v193, v253, -v254\n"
        "v_exp_f32 v193, v193\n"
        "v_fma_f32 v192, v192, v253, -v254\n"
        "v_exp_f32 v192, v192\n"
        "v_fma_f32 v191, v191, v253, -v254\n"
        "v_exp_f32 v191, v191\n"
        "v_fma_f32 v190, v190, v253, -v254\n"
        "v_exp_f32 v190, v190\n"
        "v_fma_f32 v197, v197, v253, -v254\n"
        "v_exp_f32 v197, v197\n"
        "v_fma_f32 v196, v196, v253, -v254\n"
        "v_exp_f32 v196, v196\n"
        "s_nop 0\n"
        "v_add_f32 v255, v255, v196\n"
        "v_add_f32 v255, v255, v197\n"
        "v_add_f32 v255, v255, v190\n"
        "v_add_f32 v255, v255, v191\n"
        "v_add_f32 v255, v255, v192\n"
        "v_add_f32 v255, v255, v193\n"
        "v_add_f32 v255, v255, v184\n"
        "v_add_f32 v255, v255, v185\n"
        "v_add_f32 v255, v255, v186\n"
        "v_add_f32 v255, v255, v187\n"
        "v_add_f32 v255, v255, v132\n"
        "v_mov_b32 v254, v255\n"
        "s_nop 1\n"
        "v_permlane32_swap_b32 v255, v254\n"
        "v_add_f32 v255, v255, v254\n"
        "v_mov_b32 v254, v255\n"
        "s_nop 1\n"
        "v_permlane16_swap_b32 v255, v254\n"
        "v_add_f32 v255, v255, v254\n"
        "v_rcp_f32 v255, v255\n"
        "s_nop 0\n"
        "v_mul_f32 v194, v194, v255\n"
        "v_mul_f32 v195, v195, v169\n"
        "v_mul_f32 v195, v195, v255\n"
        "v_cvt_pk_bf16_f32 v144, v194, v195\n"
        "v_mul_f32 v196, v196, v170\n"
        "v_mul_f32 v196, v196, v255\n"
        "v_mul_f32 v197, v197, v171\n"
        "v_mul_f32 v197, v197, v255\n"
        "v_cvt_pk_bf16_f32 v145, v196, v197\n"
        "v_mul_f32 v190, v190, v176\n"
        "v_mul_f32 v190, v190, v255\n"
        "v_mul_f32 v191, v191, v177\n"
        "v_mul_f32 v191, v191, v255\n"
        "v_cvt_pk_bf16_f32 v146, v190, v191\n"
        "v_mul_f32 v192, v192, v178\n"
        "v_mul_f32 v192, v192, v255\n"
        "v_mul_f32 v193, v193, v179\n"
        "v_mul_f32 v193, v193, v255\n"
        "v_cvt_pk_bf16_f32 v147, v192, v193\n"
        "s_nop 1\n"
        "v_mfma_f32_16x16x32_bf16 v[108:111], v[144:147], v[128:131], 0\n"
        "v_mul_f32 v184, v184, v180\n"
        "v_mul_f32 v185, v185, v181\n"
        "v_mul_f32 v184, v184, v255\n"
        "v_mul_f32 v185, v185, v255\n"
        "v_cvt_pk_bf16_f32 v124, v184, v185\n"
        "v_mul_f32 v186, v186, v182\n"
        "v_mul_f32 v186, v186, v255\n"
        "v_mul_f32 v187, v187, v183\n"
        "v_mul_f32 v187, v187, v255\n"
        "v_cvt_pk_bf16_f32 v125, v186, v187\n"
        "v_mul_f32 v132, v132, v253\n"
        "v_mul_f32 v132, v132, v255\n"
        "v_cvt_pk_bf16_f32 v126, v132, 0\n"
        "s_nop 1\n"
        "v_mfma_f32_16x16x32_bf16 v[108:111], v[124:127], v[136:139], v[108:111]\n"
        "v_mfma_f32_16x16x32_bf16 v[108:111], v[120:123], v[88:91], v[108:111]\n"
        "v_mfma_f32_16x16x32_bf16 v[128:131], v[120:123], v[48:51], 0\n"
        "v_mfma_f32_16x16x32_bf16 v[128:131], v[124:127], v[40:43], v[128:131]\n"
        "v_mfma_f32_16x16x32_bf16 v[128:131], v[144:147], v[152:155], v[128:131]\n"
        "s_nop 1\n"
        "v_mfma_f32_16x16x32_bf16 v[120:123], v[144:147], v[160:163], 0\n"
        "s_nop 2\n"
        "v_mfma_f32_16x16x32_bf16 v[120:123], v[124:127], v[140:143], v[120:123]\n"
        "s_nop 0\n"
        "v_cvt_pk_bf16_f32 v130, v108, v128\n"
        "v_mfma_f32_16x16x32_bf16 v[120:123], v[112:115], v[88:91], v[120:123]\n"
        "v_mfma_f32_16x16x32_bf16 v[108:111], v[112:115], v[48:51], 0\n"
        "v_mfma_f32_16x16x32_bf16 v[108:111], v[124:127], v[116:119], v[108:111]\n"
        "v_mfma_f32_16x16x32_bf16 v[108:111], v[144:147], v[156:159], v[108:111]\n"
        "s_nop 1\n"
        "v_mfma_f32_16x16x32_bf16 v[40:43], v[144:147], v[164:167], 0\n"
        "s_nop 2\n"
        "v_mfma_f32_16x16x32_bf16 v[40:43], v[124:127], v[104:107], v[40:43]\n"
        "s_nop 0\n"
        "v_cvt_pk_bf16_f32 v131, v120, v108\n"
        "v_mfma_f32_16x16x32_bf16 v[40:43], v[148:151], v[88:91], v[40:43]\n"
        "v_mfma_f32_16x16x32_bf16 v[104:107], v[148:151], v[48:51], 0\n"
        "v_mfma_f32_16x16x32_bf16 v[104:107], v[124:127], v[34:37], v[104:107]\n"
        "s_add_u32 s46, s89, 590016\n"
        "v_mfma_f32_16x16x32_bf16 v[104:107], v[144:147], v[172:175], v[104:107]\n"
        "s_nop 2\n"
        "s_min_u32 s46, s46, 200540928\n"
        "s_nop 3\n"
        "v_cvt_pk_bf16_f32 v132, v40, v104\n"
        "buffer_store_dwordx3 v[130:132], v17, s[44:47], s90 offen offset:1152 sc1 nt\n"
        "s_nop 15\n"
        "s_cmp_lt_i32 s85, -1\n"
        "s_cbranch_scc1 FIN_%=\n"
        "s_mov_b32 s89, 0x80000000\n"
        "s_waitcnt vmcnt(0)\n"
        "ds_read_b128 v[126:129], v9\n"
        "ds_read_b128 v[132:135], v10\n"
        "ds_read_b128 v[138:141], v11\n"
        "ds_read_b128 v[106:109], v9 offset:3072\n"
        "ds_read_b128 v[112:115], v10 offset:3072\n"
        "ds_read_b128 v[118:121], v11 offset:3072\n"
        "ds_read_b128 v[146:149], v9 offset:6144\n"
        "ds_read_b128 v[152:155], v10 offset:6144\n"
        "ds_read_b128 v[158:161], v11 offset:6144\n"
        "s_waitcnt lgkmcnt(0)\n"
        "s_nop 7\n"
        "s_cmp_ge_i32 s85, 0\n"
        "s_cbranch_scc1 TOP_%=\n"
        "s_branch LAST_%=\n"
        "SP3_%=:\n"
        "ds_read_u16 v255, v19 offset:2048\n"
        "s_waitcnt lgkmcnt(0)\n"
        "v_cmp_ne_u32_e64 s[24:25], s30, v255\n"
        "s_nop 4\n"
        "s_cmp_eq_u64 s[24:25], 0\n"
        "s_cbranch_scc1 MR3_%=\n"
        "s_sleep 1\n"
        "s_sub_u32 s31, s31, 1\n"
        "s_cbranch_scc0 SP3_%=\n"
        "s_endpgm\n"
        "MR3_%=:\n"
        "s_cmp_gt_i32 s85, 1\n"
        "s_cbranch_scc1 DY2B_%=\n"
        "s_cmp_gt_i32 s85, 0\n"
        "s_cbranch_scc1 DY1B_%=\n"
        "v_mov_b32 v27, 163424\n"
        "ds_read_b32 v27, v27\n"
        "s_waitcnt lgkmcnt(0)\n"
        "v_readfirstlane_b32 s50, v27\n"
        "s_nop 3\n"
        "s_lshl_b32 s92, s50, 5\n"
        "s_add_u32 s92, s92, s48\n"
        "s_cmp_ge_u32 s92, 5329\n"
        "s_cbranch_scc1 DY1F_%=\n"
        "s_mul_i32 s87, s92, 1344\n"
        "s_mul_i32 s92, s92, 57457\n"
        "s_lshr_b32 s92, s92, 22\n"
        "s_mul_i32 s92, s92, 588672\n"
        "s_add_u32 s87, s87, s92\n"
        "s_add_u32 s85, s85, 1\n"
        "s_add_u32 s50, s50, 20\n"
        "s_sub_u32 s23, s50, s53\n"
        "s_mul_i32 s23, s23, 1584\n"
        "s_mov_b32 s53, s50\n"
        "s_branch DY1B_%=\n"
        "DY1F_%=:\n"
        "s_cmp_ge_u32 s92, 5521\n"
        "s_cbranch_scc1 DY1B_%=\n"
        "s_sleep 8\n"
        "s_add_u32 s92, s92, 32\n"
        "s_branch DY1F_%=\n"
        "DY1B_%=:\n"
        "v_mov_b32 v26, 1\n"
        "s_mov_b64 exec, 1\n"
        "buffer_atomic_add v26, off, s[16:19], 0 sc0\n"
        "s_mov_b64 exec, -1\n"
        "DY2B_%=:\n"
        "s_branch BK3_%=\n"
        "SP10_%=:\n"
        "ds_read_u16 v246, v19 offset:2560\n"
        "s_waitcnt lgkmcnt(0)\n"
        "v_cmp_ne_u32_e64 s[24:25], s29, v246\n"
        "s_nop 4\n"
        "s_cmp_eq_u64 s[24:25], 0\n"
        "s_cbranch_scc1 BK10_%=\n"
        "s_sleep 1\n"
        "s_sub_u32 s31, s31, 1\n"
        "s_cbranch_scc0 SP10_%=\n"
        "s_endpgm\n"
        "DY3_%=:\n"
        "s_waitcnt vmcnt(9)\n"
        "v_readfirstlane_b32 s50, v26\n"
        "s_nop 3\n"
        "s_sub_u32 s50, s50, s20\n"
        "s_min_u32 s50, s50, 31\n"
        "v_mov_b32 v27, s49\n"
        "v_mov_b32 v26, s50\n"
        "ds_write_b32 v27, v26\n"
        "s_lshl_b32 s50, s50, 2\n"
        "s_waitcnt lgkmcnt(0)\n"
        "v_mov_b32 v27, s50\n"
        "v_mov_b32 v26, 1\n"
        "s_mov_b64 exec, 1\n"
        "buffer_atomic_add v26, v27, s[16:19], 0 offen offset:4\n"
        "s_mov_b64 exec, -1\n"
        "s_nop 1\n"
        "s_branch DY3B_%=\n"
        "SP20_%=:\n"
        "ds_read_u16 v255, v19 offset:2048\n"
        "s_waitcnt lgkmcnt(0)\n"
        "v_cmp_ne_u32_e64 s[24:25], s30, v255\n"
        "s_nop 4\n"
        "s_cmp_eq_u64 s[24:25], 0\n"
        "s_cbranch_scc1 BK20_%=\n"
        "s_sleep 1\n"
        "s_sub_u32 s31, s31, 1\n"
        "s_cbranch_scc0 SP20_%=\n"
        "s_endpgm\n"
        :: "{v[0:15]}"(c0), "{v[16:23]}"(c1), "{v[24:27]}"(c2), "{v[186:193]}"(sp), "{v[30:45]}"(f0), "{v[46:49]}"(f1), "{s15}"(own4), "{s[16:19]}"(srdl), "{s[20:23]}"(sol), "{s[36:39]}"(srdx), "{s[40:43]}"(srdo), "{s[48:55]}"(si)
        : "v28", "v29", "v50", "v51", "v52", "v53", "v54", "v55", "v56", "v57", "v58", "v59", "v60", "v61", "v62", "v63", "v64", "v65", "v66", "v67", "v68", "v69", "v70", "v71", "v72", "v73", "v74", "v75", "v76", "v77", "v78", "v79", "v80", "v81", "v82", "v83", "v84", "v85", "v86", "v87", "v88", "v89", "v90", "v91", "v92", "v93", "v94", "v95", "v96", "v97", "v98", "v99", "v100", "v101", "v102", "v103", "v104", "v105", "v106", "v107", "v108", "v109", "v110", "v111", "v112", "v113", "v114", "v115", "v116", "v117", "v118", "v119", "v120", "v121", "v122", "v123", "v124", "v125", "v126", "v127", "v128", "v129", "v130", "v131", "v132", "v133", "v134", "v135", "v136", "v137", "v138", "v139", "v140", "v141", "v142", "v143", "v144", "v145", "v146", "v147", "v148", "v149", "v150", "v151", "v152", "v153", "v154", "v155", "v156", "v157", "v158", "v159", "v160", "v161", "v162", "v163", "v164", "v165", "v166", "v167", "v168", "v169", "v170", "v171", "v172", "v173", "v174", "v175", "v176", "v177", "v178", "v179", "v180", "v181", "v182", "v183", "v184", "v185", "v194", "v195", "v196", "v197", "v198", "v199", "v200", "v201", "v202", "v203", "v204", "v205", "v206", "v207", "v208", "v209", "v210", "v211", "v212", "v213", "v214", "v215", "v216", "v217", "v218", "v219", "v220", "v221", "v222", "v223", "v224", "v225", "v226", "v227", "v228", "v229", "v230", "v231", "v232", "v233", "v234", "v235", "v236", "v237", "v238", "v239", "v240", "v241", "v242", "v243", "v244", "v245", "v246", "v247", "v248", "v249", "v250", "v251", "v252", "v253", "v254", "v255", "a0", "a1", "a2", "a3", "a4", "a5", "a6", "a7", "a8", "a9", "a10", "a11", "a12", "a13", "a14", "a15", "a16", "a17", "a18", "a19", "a20", "a21", "a22", "a23", "a24", "a25", "a26", "a27", "a28", "a29", "a30", "a31", "a32", "a33", "a34", "a35", "a36", "a37", "a38", "a39", "a40", "a41", "a42", "a43", "a44", "a45", "a46", "a47", "a48", "a49", "a50", "a51", "a52", "a53", "a54", "a55", "a56", "a57", "a58", "a59", "a60", "a61", "a62", "a63", "a64", "a65", "a66", "a67", "a68", "a69", "a70", "a71", "a72", "a73", "a74", "a75", "a76", "a77", "a78", "a79", "a80", "a81", "a82", "a83", "a84", "a85", "a86", "a87", "a88", "a89", "a90", "a91", "a92", "a93", "a94", "a95", "a96", "a97", "a98", "a99", "a100", "a101", "a102", "a103", "a104", "a105", "a106", "a107", "a108", "a109", "a110", "a111", "a112", "a113", "a114", "a115", "a116", "a117", "a118", "a119", "a120", "a121", "a122", "a123", "a124", "a125", "a126", "a127", "a128", "a129", "a130", "a131", "a132", "a133", "a134", "a135", "a136", "a137", "a138", "a139", "a140", "a141", "a142", "a143", "a144", "a145", "a146", "a147", "a148", "a149", "a150", "a151", "a152", "a153", "a154", "a155", "a156", "a157", "a158", "a159", "a160", "a161", "a162", "a163", "a164", "a165", "a166", "a167", "a168", "a169", "a170", "a171", "a172", "a173", "a174", "a175", "a176", "a177", "a178", "a179", "a180", "a181", "a182", "a183", "a184", "a185", "a186", "a187", "a188", "a189", "a190", "a191", "a192", "a193", "a194", "a195", "a196", "a197", "a198", "a199", "a200", "a201", "a202", "a203", "a204", "a205", "a206", "a207", "a208", "a209", "a210", "a211", "a212", "a213", "a214", "a215", "a216", "a217", "a218", "a219", "a220", "a221", "a222", "a223", "a224", "a225", "a226", "a227", "a228", "a229", "a230", "a231", "a232", "a233", "a234", "a235", "a236", "a237", "a238", "a239", "a240", "a241", "a242", "a243", "a244", "a245", "a246", "a247", "a248", "a249", "a250", "a251", "a252", "a253", "a254", "a255", "s24", "s25", "s26", "s27", "s28", "s29", "s30", "s31", "s44", "s45", "s46", "s47", "s56", "s57", "s58", "s59", "s60", "s61", "s62", "s63", "s64", "s65", "s66", "s67", "s68", "s69", "s70", "s71", "s72", "s73", "s74", "s75", "s76", "s77", "s78", "s79", "s80", "s81", "s82", "s83", "s84", "s85", "s86", "s87", "s88", "s89", "s90", "s91", "s92", "s93", "s94", "s95", "vcc", "scc", "memory");
}

extern "C" void kernel_launch(void* const* d_in, const int* in_sizes, int n_in, void* d_out, int out_size, void* d_ws, size_t ws_size, hipStream_t stream) {
    (void)in_sizes; (void)n_in; (void)out_size; (void)ws_size;
    (void)hipFuncSetAttribute((const void*)swin_kernel, hipFuncAttributeMaxDynamicSharedMemorySize, LDS_BYTES);
    swin_kernel<<<GRID, 256, LDS_BYTES, stream>>>((const char*)d_in[0], (const char*)d_in[1], (const char*)d_in[2], (const char*)d_in[3], (const char*)d_in[4], (char*)d_out, (const char*)d_ws);
}
```

```cpp
#define NLC 5
#define WSTEP 32
#define CPL 8
#define SMASK 31914
#define ODDS 1
#define DYN 1
#define EARLY 1
#define SAX 0
#include <hip/hip_runtime.h>
#include <hip/hip_bf16.h>

#define DI __device__ __forceinline__

using u32x2 = __attribute__((ext_vector_type(2))) unsigned;
using u32x4 = __attribute__((ext_vector_type(4))) unsigned;
using u32x8 = __attribute__((ext_vector_type(8))) unsigned;
using u32x16 = __attribute__((ext_vector_type(16))) unsigned;

constexpr int HH = 511, WW = 511, NWX = 73, NWIN = NWX * NWX, GRID = 256;
constexpr int TOKB = 192, ROWB = WW * TOKB;
constexpr unsigned FRAMEB = (unsigned)HH * ROWB, TOTALB = FRAMEB * 4;
constexpr unsigned FS = 9232, L_BUF = 0, L_STG = 4 * FS, REC = 1584, NREC = DYN ? 27 : 21, L_EXT = L_STG + 4 * 9216, L_MS = L_EXT + 2 * NREC * REC - (DYN ? 18432 : 0), L_STA = L_MS + 18432, L_PSC = L_STA + 512,
                   L_STB = L_PSC + 512, L_AUX = L_STB + 512, L_DUM = L_AUX + 1024, L_FA = L_DUM + 512, LDS_BYTES = 163840;
constexpr unsigned RAWM = 18432, L_GT = L_AUX + 768;
static_assert(L_EXT + 3 * RAWM <= L_MS && ((L_STG >> 10) & 3) == 0 && (L_STG & 1023) < 512 && L_FA + 1024 + 16 <= LDS_BYTES && (NWIN + GRID - 1) / GRID <= NREC, "LDS");

#define LP(T, a) ((__attribute__((address_space(3))) T*)(size_t)(unsigned)(a))
#define SB() __builtin_amdgcn_sched_barrier(0)
#define PIN(v) asm volatile("" : "+v"(v))
DI unsigned raw_lane(unsigned lane, unsigned m, unsigned A, unsigned B, unsigned C) {
    const unsigned n = 64 * m + lane, row = (n * 171) >> 11, pc = n - 12 * row;
    return (row >> 3) * (A & 0xffff) + ((row >> 1) & 3) * (B & 0xffff) + (row & 1) * (C & 0xffff) + (pc ^ ((row >> 2) & 3)) * 16 - 1024 * m;
}
DI u32x4 make_srd(unsigned lo, unsigned hi, unsigned bytes) { return u32x4{lo, hi & 0xffffu, bytes, 0x00020000u}; }
DI void raw_dma(unsigned v0, unsigned v1, unsigned v2, const u32x4& srd, unsigned so, unsigned dst) {
    asm volatile("s_mov_b32 m0, %5\n\ts_nop 0\n\tbuffer_load_dwordx4 %0, %3, %4 offen lds\n\tbuffer_load_dwordx4 %1, %3, %4 offen offset:1024 lds\n\tbuffer_load_dwordx4 %2, %3, %4 offen offset:2048 lds"
                 :: "v"(v0), "v"(v1), "v"(v2), "s"(srd), "s"(so), "s"(dst) : "memory");
}

__global__ __launch_bounds__(256, 1) void swin_kernel(const char* __restrict__ x_, const char* __restrict__ wqkv_t, const char* __restrict__ wo_t, const char* __restrict__ wqkv_s,
                                                           const char* __restrict__ wo_s, char* __restrict__ out_, const char* wf) {
    const unsigned k = __builtin_amdgcn_readfirstlane(threadIdx.x >> 6) & 3, lane = threadIdx.x & 63, k1 = k & 1, k2 = k >> 1;
    u32x2 kx, kl, kr; u32x4 ko;
    const unsigned long long kap = (unsigned long long)__builtin_amdgcn_kernarg_segment_ptr();
    asm volatile("s_load_dwordx2 %0, %4, %5\n\ts_load_dwordx2 %1, %4, %6\n\ts_load_dwordx2 %2, %4, 0x0\n\ts_load_dwordx4 %3, %4, 0x28"
                 : "=&s"(kr), "=&s"(kl), "=&s"(kx), "=&s"(ko) : "s"(kap), "s"(8 + 8 * k), "s"(8 + 16 * k2));
    SB();
    {
        unsigned long long pc; unsigned co = ((((blockIdx.x >> 3) * 4 + k) * CPL + lane / (64 / CPL)) & 511) * 128;
        asm volatile("s_getpc_b64 %0\n.Lswin_pc:\n\ts_mov_b32 m0, %2\n\tv_min_u32 %1, .Lswin_fin-.Lswin_pc, %1\n\tglobal_load_lds_dword %1, %0" : "=&s"(pc), "+v"(co) : "s"(L_DUM + 256) : "memory");
    }
    SB();
#define TS(i)
    const unsigned Bl = 1152 + 2304 * k2, Ql = 4608 - 3456 * k2;
    const unsigned Ar = 13824 - 12288 * k1, Br = k1 ? 384 : Bl, Cr = 576 - 384 * k1, Pr = 27648 - 24576 * k1, Qr = k1 ? 6144 : Ql, cr = 192 - 192 * k1, szr = 55296 - 36864 * k1;
    const unsigned ldr = L_STG + k * RAWM + k2 * RAWM, ldl = k1 * RAWM + k2 * (L_MS - k1 * (L_MS - L_EXT + RAWM));
    static_assert(L_STG + 2 * RAWM == L_EXT, "");
    unsigned vW = (((blockIdx.x >> 3) & 31) * 14 + lane) * 128 + (lane >= 14) * 0x40000000u; PIN(vW);
    unsigned vr[3];
#pragma unroll
    for (int m = 0; m < EARLY; ++m) { vr[m] = raw_lane(lane, m, Ar, Br, Cr); PIN(vr[m]); }
    SB();
    asm volatile("s_waitcnt lgkmcnt(0)" : "+s"(kr), "+s"(kl), "+s"(kx), "+s"(ko));
    const u32x4 srdr = make_srd(kr[0], kr[1], szr);
    asm volatile("s_mov_b32 m0, %2\n\ts_nop 0\n\tbuffer_load_dword %0, %1, 0 offen lds" :: "v"(vW), "s"(srdr), "s"(L_DUM) : "memory");
    SB();
#pragma unroll
    for (int m = EARLY; m < 3; ++m) { vr[m] = raw_lane(lane, m, Ar, Br, Cr); PIN(vr[m]); }
#define RDMA(n) do { SB(); raw_dma(vr[0], vr[1], vr[2], srdr, cr + (n & 1) * Pr + (n >> 1) * Qr, ldr + n * 3072); SB(); } while (0)
    RDMA(0); RDMA(1);
    TS(1);
    const u32x4 srdl = make_srd(kl[0], kl[1], 55296), srdx = make_srd(kx[0], kx[1], TOTALB), srdo = make_srd(ko[0], ko[1], TOTALB);
    const unsigned vb = (blockIdx.x & 7) * (GRID / 8) + (blockIdx.x >> 3);
    const unsigned w0 = (blockIdx.x & 7) * 640 + (blockIdx.x >> 3), nwin = 5120 + (blockIdx.x >> 3), wy0 = w0 / NWX, wx0 = w0 - wy0 * NWX, own4 = (blockIdx.x & 7) * 4;
    static_assert(WSTEP == 32, "");
    const unsigned base0 = wy0 * 7 * ROWB + wx0 * 7 * TOKB;
#define SLOT(n) do { constexpr int a_ = __builtin_popcount(SMASK & ((1 << (n)) - 1)); if ((SMASK >> (n)) & 1) { if (a_ < 4) RDMA((a_ + 2)); else LDMA((a_ - 4)); } } while (0)
    static_assert(__builtin_popcount(SMASK) == 4 + NLC, "");
    const unsigned i = lane & 15, g = lane >> 4, p = i >> 2, t = i & 3, cl = 384 * k1;
    u32x16 c0, f0; u32x8 c1, sp; u32x4 c2;
    const unsigned stg = L_STG + k * 9216, ext = L_EXT + k * 384, exv = L_EXT + NREC * REC + k * 392;
    unsigned vl[3];
#pragma unroll
    for (int m = 0; m < 3; ++m) { vl[m] = raw_lane(lane, m, 13824, Bl, 576); PIN(vl[m]); }
#define LDMA(n) do { SB(); raw_dma(vl[0], vl[1], vl[2], srdl, cl + (n & 1) * 27648 + (n >> 1) * Ql, ldl + n * 3072); SB(); } while (0)
    SLOT(0);
#pragma unroll
    for (int a = 0; a < 2; ++a) {
        const unsigned ra = (8 * p + 4 * a + t) * 192 + ((g ^ ((2 * p + a) & 3)) * 16);
        f0[a] = ldl + ra; f0[2 + a] = ldr + ra; f0[4 + a] = ldr + (8 * g + 4 * a + p) * 192 + ((t ^ ((2 * g + a) & 3)) * 16);
        PIN(f0[a]); PIN(f0[2 + a]); PIN(f0[4 + a]);
    }
    SLOT(1);
#pragma unroll
    for (int a = 0; a < 2; ++a) {
        const unsigned n = 16 * a + 4 * k + p;
        const unsigned w = (DYN && n >= 20 ? nwin - 20 * WSTEP : w0) + n * WSTEP;
        f0[6 + a] = (DYN ? w < NWIN : n < nwin) ? t * FRAMEB + w * (7 * TOKB) + ((w * 57457) >> 22) * (7 * ROWB - NWX * 7 * TOKB) + 6 * ROWB + 6 * TOKB + g * 48 : 0x80000000u; PIN(f0[6 + a]);
        if (a == 0) SLOT(2);
    }
    f0[8] = i == 0 ? L_AUX + 192 + g * 16 : L_DUM; f0[15] = 0;
    SLOT(3);
    {
        const unsigned sh = 2 - 2 * k2, ts = 3072 + 16 * k2, ml = 64 - 48 * k2, xm = 64 * (k == 1), a0 = ldl + g * 256 + (k == 1 ? t * 16 : k1 ? 0 : (i ^ ((g & k & 2) << 2)) * 16);
#pragma unroll
        for (int n = 0; n < 6; ++n) {
            const unsigned z = (32 * (n >> 1) + 4 * (n & 1) + 8 * p + t) >> sh;
            const unsigned z6 = (z * 43) >> 8, tl = z - 6 * z6;
            f0[9 + n] = a0 + (tl * ts + ((z6 * ml) ^ ((tl << 5) & xm))) * k1 + n * 3072 * (1 - k1); PIN(f0[9 + n]);
            if (n == 0) SLOT(4);
            if (n == 2) SLOT(5);
            if (n == 4) SLOT(6);
        }
    }
    SLOT(7);
#pragma unroll
    for (int m = 0; m < 3; ++m) {
        const unsigned n = 64 * m + lane, tf = (n * 171) >> 13, r = n - 48 * tf, q = (r * 43) >> 9;
        const unsigned c = tf * FRAMEB + ((r - 12 * q) ^ tf) * 16;
#pragma unroll
        for (int u = 0; u < 3; ++u) {
            const unsigned px = 16 * u + 4 * k + q;
            c0[3 * u + m] = c + px * TOKB + ((px * 37) >> 8) * (ROWB - 7 * TOKB) - 1024 * ((3 * u + m) % 4); PIN(c0[3 * u + m]);
        }
        if (m == 0) SLOT(8);
        if (m == 1) SLOT(9);
        if (m == 2) SLOT(10);
    }
#pragma unroll
    for (int S = 0; S < 3; ++S) c0[9 + S] = stg + t * 768 + p * TOKB + ((3 * g + S) ^ t) * 16;
    c0[12] = L_BUF + t * FS + g * 256 + p * 64 + k * 16;
    c0[13] = L_BUF + k * FS + lane * 16;
    const unsigned vs = g * TOKB + 12 * i, brk = ROWB - 7 * TOKB, u = (p - g) & 3;
    c0[14] = vs; c0[15] = vs + (g >= 3 ? brk : 0);
    PIN(c0);
    SLOT(11);
    c1[0] = vs + (g >= 2 ? brk : 0); c1[1] = vs + (g >= 1 ? brk : 0);
    c1[2] = u < 3 ? L_STA + t * 128 + g * 32 + u * 8 + k * 2 : L_DUM;
    c1[3] = L_STB + t * 128 + (4 * g + k) * 8 + u * 2;
    c1[4] = L_STA + k * 128 + g * 32;
    c1[5] = L_STB + k * 128 + i * 8;
    c1[6] = L_STA + k * 128 + p * 32 + g * 8 + t * 2;
    c1[7] = ext - REC + (i & 1) * 192 + g * 16;
    c2[0] = exv - REC + 24 * i;
    c2[1] = p == g ? ~0u : 0u;
    c2[2] = SAX ? L_AUX + (t >> 1) * 192 + (g & 1) * 32 + (t & 1) * 4 + (g >> 1) * 56 : L_AUX + (t < 2 ? t : 2) * 192 + g * 16;
    c2[3] = p == g ? L_AUX + 576 + t * 16 : L_AUX + 384;
    PIN(c1); PIN(c2);
    SLOT(12);
    sp[0] = lane * 16; sp[1] = L_MS + (16 * t + ((4 * g + p) ^ ((t & 2) << 2))) * 16;
#pragma unroll
    for (int T = 0; T < 2; ++T) {
        const unsigned n = 16 * T + 4 * t + p;
        sp[2 + T] = n < NREC ? ext + n * REC + g * 16 : L_DUM + (i & 7) * 16; sp[4 + T] = n < NREC && p == g ? exv + n * REC + 384 : L_DUM;
    }
    sp[6] = exv + g * REC + 24 * i; sp[7] = g < (DYN ? 3 : 1) ? exv + ((DYN ? 24 : 20) + g) * REC + 24 * i : L_DUM + 24 * i;
    PIN(sp);
    SLOT(13);
    {
        const unsigned e = 64 * k + lane, y = e - 144, one = (y < 16 ? (y >> 2) & 1 : y >> 5) ? 0x3f800000u : 0x3f80u;
        *LP(unsigned, L_AUX + e * 4) = e < 48 ? 0x3f803f80u : e == 192 ? 0x4000400u : y < 48 && t == (y < 16 ? y >> 3 : (y >> 2) & 3) ? one : 0u;
        *LP(unsigned, L_STA + k * 128 + (lane & 6) * 16 + 24 + (lane & 1) * 4) = 0x3c003c00u;
        *LP(unsigned, L_FA + e * 4) = 0u;
    }
    SLOT(14);
    const u32x4 f1 = {vl[0], vl[1], vl[2], 0u}, sol = {cl + Ql, cl + Ql + 27648, cl + 2 * Ql, cl + 2 * Ql + 27648};
    const unsigned long long gl = (((unsigned long long)ko[3] << 32) | ko[2]) + (blockIdx.x >> 3) * 128;
    const u32x8 si = {(unsigned)nwin, base0, (unsigned)wx0, k * FRAMEB, stg, ldl, (unsigned)gl, (unsigned)(gl >> 32) & 0xffffu};
    asm volatile(
        "s_bitcmp1_b32 s52, 10\n"
        "s_cbranch_scc1 FV_%=\n"
        "s_mov_b32 s24, 0x3e16c740\n"
        "v_mov_b32 v50, 0x3f803f80\n"
        "v_mov_b32 v51, 0x3f803f80\n"
        "v_mov_b32 v52, 0x3f803f80\n"
        "v_mov_b32 v53, 0x3f803f80\n"
        "s_waitcnt vmcnt(15)\n"
        "ds_read_b64_tr_b16 a[72:73], v34 offset:0\n"
        "ds_read_b64_tr_b16 a[74:75], v35 offset:0\n"
        "ds_read_b64_tr_b16 a[76:77], v34 offset:6144\n"
        "ds_read_b64_tr_b16 a[78:79], v35 offset:6144\n"
        "ds_read_b64_tr_b16 a[80:81], v34 offset:12288\n"
        "ds_read_b64_tr_b16 a[82:83], v35 offset:12288\n"
        "s_add_u32 m0, s53, 15360\n"
        "s_nop 0\n"
        "buffer_load_dwordx4 v46, s[16:19], s23 offen lds\n"
        "ds_read_b64_tr_b16 a[84:85], v34 offset:8\n"
        "ds_read_b64_tr_b16 a[86:87], v35 offset:8\n"
        "ds_read_b64_tr_b16 a[88:89], v34 offset:6152\n"
        "ds_read_b64_tr_b16 a[90:91], v35 offset:6152\n"
        "ds_read_b64_tr_b16 a[92:93], v34 offset:12296\n"
        "ds_read_b64_tr_b16 a[94:95], v35 offset:12296\n"
        "buffer_load_dwordx4 v47, s[16:19], s23 offen offset:1024 lds\n"
        "ds_read_b64_tr_b16 a[96:97], v34 offset:64\n"
        "ds_read_b64_tr_b16 a[98:99], v35 offset:64\n"
        "ds_read_b64_tr_b16 a[100:101], v34 offset:6208\n"
        "ds_read_b64_tr_b16 a[102:103], v35 offset:6208\n"
        "ds_read_b64_tr_b16 a[104:105], v34 offset:12352\n"
        "ds_read_b64_tr_b16 a[106:107], v35 offset:12352\n"
        "buffer_load_dwordx4 v48, s[16:19], s23 offen offset:2048 lds\n"
        "ds_read_b64_tr_b16 a[108:109], v34 offset:72\n"
        "ds_read_b64_tr_b16 a[110:111], v35 offset:72\n"
        "ds_read_b64_tr_b16 a[112:113], v34 offset:6216\n"
        "ds_read_b64_tr_b16 a[114:115], v35 offset:6216\n"
        "ds_read_b64_tr_b16 a[116:117], v34 offset:12360\n"
        "ds_read_b64_tr_b16 a[118:119], v35 offset:12360\n"
        "ds_read_b64_tr_b16 a[120:121], v34 offset:128\n"
        "ds_read_b64_tr_b16 a[122:123], v35 offset:128\n"
        "ds_read_b64_tr_b16 a[124:125], v34 offset:6272\n"
        "ds_read_b64_tr_b16 a[126:127], v35 offset:6272\n"
        "ds_read_b64_tr_b16 a[128:129], v34 offset:12416\n"
        "ds_read_b64_tr_b16 a[130:131], v35 offset:12416\n"
        "ds_read_b64_tr_b16 a[132:133], v34 offset:136\n"
        "ds_read_b64_tr_b16 a[134:135], v35 offset:136\n"
        "ds_read_b64_tr_b16 a[136:137], v34 offset:6280\n"
        "ds_read_b64_tr_b16 a[138:139], v35 offset:6280\n"
        "ds_read_b64_tr_b16 a[140:141], v34 offset:12424\n"
        "ds_read_b64_tr_b16 a[142:143], v35 offset:12424\n"
        "s_waitcnt lgkmcnt(15)\n"
        "v_mfma_f32_16x16x32_bf16 v[46:49], a[72:75], v[50:53], 0\n"
        "s_waitcnt vmcnt(12)\n"
        "s_waitcnt vmcnt(6)\n"
        "v_mfma_f32_16x16x32_bf16 v[46:49], a[76:79], v[50:53], v[46:49]\n"
        "v_mfma_f32_16x16x32_bf16 v[46:49], a[80:83], v[50:53], v[46:49]\n"
        "s_waitcnt lgkmcnt(15)\n"
        "s_nop 0\n"
        "v_mfma_f32_16x16x32_bf16 v[54:57], a[92:95], v[50:53], 0\n"
        "s_nop 2\n"
        "v_mfma_f32_16x16x32_bf16 v[54:57], a[88:91], v[50:53], v[54:57]\n"
        "s_waitcnt lgkmcnt(15)\n"
        "v_mul_f32 v46, 0xbc2aaaab, v46\n"
        "v_mul_f32 v47, 0xbc2aaaab, v47\n"
        "v_mul_f32 v48, 0xbc2aaaab, v48\n"
        "v_mfma_f32_16x16x32_bf16 v[54:57], a[84:87], v[50:53], v[54:57]\n"
        "s_waitcnt vmcnt(0)\n"
        "v_mul_f32 v49, 0xbc2aaaab, v49\n"
        "v_cvt_pk_bf16_f32 v62, v46, v47\n"
        "v_cvt_pk_bf16_f32 v63, v48, v49\n"
        "v_mfma_f32_16x16x32_bf16 v[46:49], a[96:99], v[50:53], 0\n"
        "s_waitcnt lgkmcnt(12)\n"
        "ds_read_b128 a[0:3], v32\n"
        "v_mfma_f32_16x16x32_bf16 v[46:49], a[100:103], v[50:53], v[46:49]\n"
        "s_waitcnt lgkmcnt(7)\n"
        "ds_read_b128 a[4:7], v32 offset:64\n"
        "v_mul_f32 v54, 0xbc2aaaab, v54\n"
        "v_mul_f32 v55, 0xbc2aaaab, v55\n"
        "v_mfma_f32_16x16x32_bf16 v[46:49], a[104:107], v[50:53], v[46:49]\n"
        "s_waitcnt lgkmcnt(2)\n"
        "ds_read_b128 a[8:11], v32 offset:128\n"
        "v_mul_f32 v56, 0xbc2aaaab, v56\n"
        "v_mul_f32 v57, 0xbc2aaaab, v57\n"
        "v_mfma_f32_16x16x32_bf16 v[58:61], a[116:119], v[50:53], 0\n"
        "ds_read_b128 a[144:147], v30\n"
        "v_cvt_pk_bf16_f32 v64, v54, v55\n"
        "v_cvt_pk_bf16_f32 v65, v56, v57\n"
        "v_mfma_f32_16x16x32_bf16 v[58:61], a[112:115], v[50:53], v[58:61]\n"
        "ds_read_b128 a[148:151], v30 offset:64\n"
        "v_mul_f32 v46, 0xbc2aaaab, v46\n"
        "v_mul_f32 v47, 0xbc2aaaab, v47\n"
        "v_mfma_f32_16x16x32_bf16 v[58:61], a[108:111], v[50:53], v[58:61]\n"
        "ds_read_b128 a[152:155], v30 offset:128\n"
        "v_mul_f32 v48, 0xbc2aaaab, v48\n"
        "v_mul_f32 v49, 0xbc2aaaab, v49\n"
        "v_mfma_f32_16x16x32_bf16 v[54:57], a[120:123], v[50:53], 0\n"
        "ds_read_b128 a[156:159], v31\n"
        "v_cvt_pk_bf16_f32 v66, v46, v47\n"
        "v_cvt_pk_bf16_f32 v67, v48, v49\n"
        "v_mfma_f32_16x16x32_bf16 v[54:57], a[124:127], v[50:53], v[54:57]\n"
        "ds_read_b128 a[160:163], v31 offset:64\n"
        "v_mul_f32 v58, 0xbc2aaaab, v58\n"
        "v_mul_f32 v59, 0xbc2aaaab, v59\n"
        "v_mfma_f32_16x16x32_bf16 v[54:57], a[128:131], v[50:53], v[54:57]\n"
        "ds_read_b128 a[164:167], v31 offset:128\n"
        "v_mul_f32 v60, 0xbc2aaaab, v60\n"
        "v_mul_f32 v61, 0xbc2aaaab, v61\n"
        "v_mfma_f32_16x16x32_bf16 v[74:77], a[140:143], v[50:53], 0\n"
        "ds_read_b128 a[168:171], v30 offset:6144\n"
        "v_cvt_pk_bf16_f32 v68, v58, v59\n"
        "v_cvt_pk_bf16_f32 v69, v60, v61\n"
        "v_mfma_f32_16x16x32_bf16 v[74:77], a[136:139], v[50:53], v[74:77]\n"
        "ds_read_b128 a[172:175], v30 offset:6208\n"
        "v_mul_f32 v54, 0xbc2aaaab, v54\n"
        "v_mul_f32 v55, 0xbc2aaaab, v55\n"
        "v_mfma_f32_16x16x32_bf16 v[74:77], a[132:135], v[50:53], v[74:77]\n"
        "s_waitcnt lgkmcnt(8)\n"
        "ds_read_b128 a[176:179], v30 offset:6272\n"
        "v_mul_f32 v56, 0xbc2aaaab, v56\n"
        "v_mul_f32 v57, 0xbc2aaaab, v57\n"
        "s_waitcnt lgkmcnt(6)\n"
        "v_mfma_f32_16x16x32_bf16 v[46:49], a[144:147], v[62:65], 0\n"
        "ds_read_b128 a[180:183], v31 offset:6144\n"
        "v_cvt_pk_bf16_f32 v70, v54, v55\n"
        "v_cvt_pk_bf16_f32 v71, v56, v57\n"
        "v_mfma_f32_16x16x32_bf16 v[46:49], a[148:151], v[66:69], v[46:49]\n"
        "ds_read_b128 a[184:187], v31 offset:6208\n"
        "v_mul_f32 v74, 0xbc2aaaab, v74\n"
        "v_mul_f32 v75, 0xbc2aaaab, v75\n"
        "v_cvt_pk_bf16_f32 v72, v74, v75\n"
        "v_mul_f32 v76, 0xbc2aaaab, v76\n"
        "v_mul_f32 v77, 0xbc2aaaab, v77\n"
        "v_cvt_pk_bf16_f32 v73, v76, v77\n"
        "s_nop 1\n"
        "v_mfma_f32_16x16x32_bf16 v[46:49], a[152:155], v[70:73], v[46:49]\n"
        "s_waitcnt lgkmcnt(5)\n"
        "ds_read_b128 a[188:191], v31 offset:6272\n"
        "v_mfma_f32_16x16x32_bf16 v[54:57], a[152:155], a[8:11], v[46:49]\n"
        "buffer_load_dwordx4 v[196:199], v36, s[36:39], 0 offen\n"
        "v_mfma_f32_16x16x32_bf16 v[54:57], a[148:151], a[4:7], v[54:57]\n"
        "ds_read_b128 a[192:195], v30 offset:12288\n"
        "ds_read_b128 a[196:199], v30 offset:12352\n"
        "v_mfma_f32_16x16x32_bf16 v[54:57], a[144:147], a[0:3], v[54:57]\n"
        "s_waitcnt lgkmcnt(5)\n"
        "ds_read_b128 a[200:203], v30 offset:12416\n"
        "v_mfma_f32_16x16x32_bf16 v[50:53], a[156:159], v[62:65], 0\n"
        "s_nop 0\n"
        "ds_read_b128 a[204:207], v31 offset:12288\n"
        "v_mfma_f32_16x16x32_bf16 v[50:53], a[160:163], v[66:69], v[50:53]\n"
        "s_nop 0\n"
        "ds_read_b128 a[208:211], v31 offset:12352\n"
        "v_add_f32 v255, v54, v55\n"
        "v_add_f32 v255, v255, v56\n"
        "v_mfma_f32_16x16x32_bf16 v[50:53], a[164:167], v[70:73], v[50:53]\n"
        "ds_read_b128 a[212:215], v31 offset:12416\n"
        "v_add_f32 v255, v255, v57\n"
        "v_mfma_f32_16x16x32_bf16 v[86:89], a[164:167], a[8:11], v[50:53]\n"
        "ds_read_b128 a[12:15], v33\n"
        "v_mfma_f32_16x16x32_bf16 v[86:89], a[160:163], a[4:7], v[86:89]\n"
        "ds_read_b128 a[16:19], v33 offset:64\n"
        "v_mfma_f32_16x16x32_bf16 v[86:89], a[156:159], a[0:3], v[86:89]\n"
        "ds_read_b128 a[20:23], v33 offset:128\n"
        "v_mfma_f32_16x16x32_bf16 v[74:77], a[168:171], v[62:65], 0\n"
        "s_waitcnt lgkmcnt(9)\n"
        "buffer_load_dwordx4 v[202:205], v36, s[36:39], 0 offen offset:16\n"
        "v_mfma_f32_16x16x32_bf16 v[74:77], a[172:175], v[66:69], v[74:77]\n"
        "s_mov_b32 s78, 0xf0000f\n"
        "ds_read_b128 a[24:27], v32 offset:6144\n"
        "ds_read_b128 a[28:31], v32 offset:6208\n"
        "v_add_f32 v255, v255, v86\n"
        "v_mfma_f32_16x16x32_bf16 v[74:77], a[176:179], v[70:73], v[74:77]\n"
        "s_mov_b32 s79, 0xf0000f00\n"
        "ds_read_b128 a[32:35], v32 offset:6272\n"
        "v_add_f32 v255, v255, v87\n"
        "v_add_f32 v255, v255, v88\n"
        "v_mfma_f32_16x16x32_bf16 v[90:93], a[176:179], a[8:11], v[74:77]\n"
        "s_waitcnt lgkmcnt(9)\n"
        "ds_read_b128 a[36:39], v33 offset:6144\n"
        "v_add_f32 v255, v255, v89\n"
        "v_mfma_f32_16x16x32_bf16 v[90:93], a[172:175], a[4:7], v[90:93]\n"
        "s_mov_b32 s76, 0x11111111\n"
        "ds_read_b128 a[40:43], v33 offset:6208\n"
        "v_mfma_f32_16x16x32_bf16 v[90:93], a[168:171], a[0:3], v[90:93]\n"
        "s_mov_b32 s77, 0x11111111\n"
        "ds_read_b128 a[44:47], v33 offset:6272\n"
        "v_mfma_f32_16x16x32_bf16 v[82:85], a[180:183], v[62:65], 0\n"
        "s_waitcnt lgkmcnt(9)\n"
        "ds_read_b128 a[48:51], v32 offset:12288\n"
        "v_mfma_f32_16x16x32_bf16 v[82:85], a[184:187], v[66:69], v[82:85]\n"
        "s_mov_b32 s56, 0x22222222\n"
        "ds_read_b128 a[52:55], v32 offset:12352\n"
        "v_add_f32 v255, v255, v90\n"
        "v_add_f32 v255, v255, v91\n"
        "v_mfma_f32_16x16x32_bf16 v[82:85], a[188:191], v[70:73], v[82:85]\n"
        "s_mov_b32 s57, 0x22222222\n"
        "ds_read_b128 a[56:59], v32 offset:12416\n"
        "v_add_f32 v255, v255, v92\n"
        "v_add_f32 v255, v255, v93\n"
        "v_mfma_f32_16x16x32_bf16 v[94:97], a[188:191], a[8:11], v[82:85]\n"
        "s_mov_b32 s58, 0x44444444\n"
        "buffer_load_dwordx4 v[208:211], v36, s[36:39], 0 offen offset:32\n"
        "v_mfma_f32_16x16x32_bf16 v[94:97], a[184:187], a[4:7], v[94:97]\n"
        "s_waitcnt lgkmcnt(9)\n"
        "ds_read_b128 a[60:63], v33 offset:12288\n"
        "ds_read_b128 a[64:67], v33 offset:12352\n"
        "v_mfma_f32_16x16x32_bf16 v[94:97], a[180:183], a[0:3], v[94:97]\n"
        "s_mov_b32 s59, 0x44444444\n"
        "ds_read_b128 a[68:71], v33 offset:12416\n"
        "v_mfma_f32_16x16x32_bf16 v[78:81], a[192:195], v[62:65], 0\n"
        "s_mov_b32 s60, 0x88888888\n"
        "s_nop 1\n"
        "v_mfma_f32_16x16x32_bf16 v[78:81], a[196:199], v[66:69], v[78:81]\n"
        "s_mov_b32 s61, 0x88888888\n"
        "v_add_f32 v255, v255, v94\n"
        "v_add_f32 v255, v255, v95\n"
        "v_add_f32 v255, v255, v96\n"
        "v_mfma_f32_16x16x32_bf16 v[78:81], a[200:203], v[70:73], v[78:81]\n"
        "s_mov_b32 s62, 0xffff\n"
        "v_add_f32 v255, v255, v97\n"
        "v_mfma_f32_16x16x32_bf16 v[98:101], a[200:203], a[8:11], v[78:81]\n"
        "s_mov_b32 s63, 0x0\n"
        "v_mfma_f32_16x16x32_bf16 v[98:101], a[196:199], a[4:7], v[98:101]\n"
        "s_waitcnt lgkmcnt(9)\n"
        "v_mfma_f32_16x16x32_bf16 v[98:101], a[192:195], a[0:3], v[98:101]\n"
        "s_mov_b32 s64, 0xffff0000\n"
        "buffer_load_dwordx4 v[58:61], v37, s[36:39], 0 offen\n"
        "v_mfma_f32_16x16x32_bf16 v[30:33], a[204:207], v[62:65], 0\n"
        "s_mov_b32 s65, 0x0\n"
        "s_nop 1\n"
        "v_mfma_f32_16x16x32_bf16 v[30:33], a[208:211], v[66:69], v[30:33]\n"
        "s_mov_b32 s66, 0x0\n"
        "v_add_f32 v255, v255, v98\n"
        "v_add_f32 v255, v255, v99\n"
        "v_add_f32 v255, v255, v100\n"
        "v_mfma_f32_16x16x32_bf16 v[30:33], a[212:215], v[70:73], v[30:33]\n"
        "s_mov_b32 s67, 0xffff\n"
        "v_add_f32 v255, v255, v101\n"
        "v_mfma_f32_16x16x32_bf16 v[106:109], a[212:215], a[8:11], v[30:33]\n"
        "s_waitcnt lgkmcnt(0)\n"
        "v_mfma_f32_16x16x32_bf16 v[106:109], a[208:211], a[4:7], v[106:109]\n"
        "s_mov_b32 s68, 0x0\n"
        "v_mfma_f32_16x16x32_bf16 v[106:109], a[204:207], a[0:3], v[106:109]\n"
        "s_mov_b32 s69, 0xffff0000\n"
        "s_nop 0\n"
        "v_mfma_f32_16x16x32_bf16 v[102:105], a[144:147], a[12:15], v[46:49]\n"
        "s_mov_b32 s70, 0x0\n"
        "s_nop 1\n"
        "v_mfma_f32_16x16x32_bf16 v[102:105], a[148:151], a[16:19], v[102:105]\n"
        "s_mov_b32 s71, 0xffffffff\n"
        "v_add_f32 v255, v255, v106\n"
        "v_add_f32 v255, v255, v107\n"
        "v_add_f32 v255, v255, v108\n"
        "v_mfma_f32_16x16x32_bf16 v[102:105], a[152:155], a[20:23], v[102:105]\n"
        "s_barrier\n"
        "buffer_load_dwordx4 v[64:67], v37, s[36:39], 0 offen offset:16\n"
        "v_add_f32 v255, v255, v109\n"
        "v_mov_b32 v253, v255\n"
        "v_mfma_f32_16x16x32_bf16 v[114:117], a[164:167], a[20:23], v[50:53]\n"
        "s_mov_b32 s72, 0x0\n"
        "v_permlane32_swap_b32 v255, v253\n"
        "v_add_f32 v255, v255, v253\n"
        "v_mfma_f32_16x16x32_bf16 v[114:117], a[160:163], a[16:19], v[114:117]\n"
        "s_mov_b32 s73, 0x20000\n"
        "v_mov_b32 v253, v255\n"
        "v_add_f32 v254, v102, v103\n"
        "v_add_f32 v254, v254, v104\n"
        "v_mfma_f32_16x16x32_bf16 v[114:117], a[156:159], a[12:15], v[114:117]\n"
        "s_mov_b64 vcc, s[78:79]\n"
        "v_permlane16_swap_b32 v255, v253\n"
        "v_add_f32 v255, v255, v253\n"
        "v_mfma_f32_16x16x32_bf16 v[118:121], a[168:171], a[12:15], v[74:77]\n"
        "s_mov_b32 s80, 0x3c2aaaab\n"
        "v_mul_f32 v255, 0xbac909aa, v255\n"
        "v_add_f32 v254, v254, v105\n"
        "v_fma_f32 v54, v54, s24, v255\n"
        "v_mfma_f32_16x16x32_bf16 v[118:121], a[172:175], a[16:19], v[118:121]\n"
        "s_mov_b32 s81, 0x3727c5ac\n"
        "v_fma_f32 v55, v55, s24, v255\n"
        "v_fma_f32 v56, v56, s24, v255\n"
        "v_fma_f32 v57, v57, s24, v255\n"
        "v_mfma_f32_16x16x32_bf16 v[118:121], a[176:179], a[20:23], v[118:121]\n"
        "s_mov_b32 s82, 0xc000c000\n"
        "v_fma_f32 v86, v86, s24, v255\n"
        "v_fma_f32 v87, v87, s24, v255\n"
        "v_fma_f32 v88, v88, s24, v255\n"
        "v_mfma_f32_16x16x32_bf16 v[110:113], a[188:191], a[20:23], v[82:85]\n"
        "s_mov_b32 s84, 0x80000000\n"
        "v_fma_f32 v89, v89, s24, v255\n"
        "v_fma_f32 v90, v90, s24, v255\n"
        "v_fma_f32 v91, v91, s24, v255\n"
        "v_mfma_f32_16x16x32_bf16 v[110:113], a[184:187], a[16:19], v[110:113]\n"
        "s_mov_b32 s85, 19\n"
        "v_fma_f32 v92, v92, s24, v255\n"
        "v_fma_f32 v93, v93, s24, v255\n"
        "v_fma_f32 v94, v94, s24, v255\n"
        "v_mfma_f32_16x16x32_bf16 v[110:113], a[180:183], a[12:15], v[110:113]\n"
        "s_mov_b32 s86, 0x80000000\n"
        "buffer_load_dwordx4 v[70:73], v37, s[36:39], 0 offen offset:32\n"
        "v_fma_f32 v95, v95, s24, v255\n"
        "v_fma_f32 v96, v96, s24, v255\n"
        "v_mfma_f32_16x16x32_bf16 v[122:125], a[192:195], a[12:15], v[78:81]\n"
        "s_mov_b32 s89, 0x80000000\n"
        "v_fma_f32 v97, v97, s24, v255\n"
        "v_fma_f32 v98, v98, s24, v255\n"
        "v_fma_f32 v99, v99, s24, v255\n"
        "v_mfma_f32_16x16x32_bf16 v[122:125], a[196:199], a[16:19], v[122:125]\n"
        "s_mov_b32 s29, 1\n"
        "v_fma_f32 v100, v100, s24, v255\n"
        "v_fma_f32 v101, v101, s24, v255\n"
        "v_fma_f32 v106, v106, s24, v255\n"
        "v_mfma_f32_16x16x32_bf16 v[122:125], a[200:203], a[20:23], v[122:125]\n"
        "s_mov_b32 s31, 0x400000\n"
        "v_fma_f32 v107, v107, s24, v255\n"
        "v_fma_f32 v108, v108, s24, v255\n"
        "v_fma_f32 v109, v109, s24, v255\n"
        "v_mfma_f32_16x16x32_bf16 v[130:133], a[212:215], a[20:23], v[30:33]\n"
        "s_mov_b32 s87, s49\n"
        "v_cvt_pk_bf16_f32 v34, v54, v55\n"
        "v_cvt_pk_bf16_f32 v35, v56, v57\n"
        "v_cvt_pk_bf16_f32 v36, v86, v87\n"
        "v_mfma_f32_16x16x32_bf16 v[130:133], a[208:211], a[16:19], v[130:133]\n"
        "s_mov_b32 s88, s50\n"
        "v_cvt_pk_bf16_f32 v37, v88, v89\n"
        "v_cvt_pk_bf16_f32 v54, v90, v91\n"
        "v_cvt_pk_bf16_f32 v55, v92, v93\n"
        "v_mfma_f32_16x16x32_bf16 v[130:133], a[204:207], a[12:15], v[130:133]\n"
        "s_mov_b64 s[44:45], s[40:41]\n"
        "ds_write_b128 v39, v[34:37]\n"
        "v_cvt_pk_bf16_f32 v56, v94, v95\n"
        "v_mfma_f32_16x16x32_bf16 v[34:37], a[144:147], a[24:27], v[46:49]\n"
        "s_mov_b32 s46, 0\n"
        "v_cvt_pk_bf16_f32 v57, v96, v97\n"
        "v_cvt_pk_bf16_f32 v86, v98, v99\n"
        "v_cvt_pk_bf16_f32 v87, v100, v101\n"
        "v_mfma_f32_16x16x32_bf16 v[34:37], a[148:151], a[28:31], v[34:37]\n"
        "s_add_u32 m0, s52, 0\n"
        "ds_write_b128 v39, v[54:57] offset:1024\n"
        "v_cvt_pk_bf16_f32 v88, v106, v107\n"
        "v_mfma_f32_16x16x32_bf16 v[34:37], a[152:155], a[32:35], v[34:37]\n"
        "s_mov_b32 s47, s43\n"
        "buffer_load_dwordx4 v0, s[36:39], s49 offen lds\n"
        "v_cvt_pk_bf16_f32 v89, v108, v109\n"
        "v_add_f32 v254, v254, v114\n"
        "v_mfma_f32_16x16x32_bf16 v[54:57], a[164:167], a[32:35], v[50:53]\n"
        "s_waitcnt lgkmcnt(2)\n"
        "ds_write_b128 v39, v[86:89] offset:2048\n"
        "v_add_f32 v254, v254, v115\n"
        "v_mfma_f32_16x16x32_bf16 v[54:57], a[160:163], a[28:31], v[54:57]\n"
        "s_waitcnt lgkmcnt(3)\n"
        "v_add_f32 v254, v254, v116\n"
        "v_add_f32 v254, v254, v117\n"
        "v_add_f32 v254, v254, v118\n"
        "v_mfma_f32_16x16x32_bf16 v[54:57], a[156:159], a[24:27], v[54:57]\n"
        "s_waitcnt lgkmcnt(3)\n"
        "v_add_f32 v254, v254, v119\n"
        "v_add_f32 v254, v254, v120\n"
        "v_add_f32 v254, v254, v121\n"
        "v_mfma_f32_16x16x32_bf16 v[86:89], a[168:171], a[24:27], v[74:77]\n"
        "v_add_f32 v254, v254, v110\n"
        "v_add_f32 v254, v254, v111\n"
        "v_add_f32 v254, v254, v112\n"
        "v_mfma_f32_16x16x32_bf16 v[86:89], a[172:175], a[28:31], v[86:89]\n"
        "v_add_f32 v254, v254, v113\n"
        "v_add_f32 v254, v254, v122\n"
        "v_add_f32 v254, v254, v123\n"
        "v_mfma_f32_16x16x32_bf16 v[86:89], a[176:179], a[32:35], v[86:89]\n"
        "buffer_load_dwordx4 v1, s[36:39], s49 offen offset:1024 lds\n"
        "v_add_f32 v254, v254, v124\n"
        "v_add_f32 v254, v254, v125\n"
        "v_mfma_f32_16x16x32_bf16 v[126:129], a[188:191], a[32:35], v[82:85]\n"
        "v_add_f32 v254, v254, v130\n"
        "v_add_f32 v254, v254, v131\n"
        "v_add_f32 v254, v254, v132\n"
        "v_mfma_f32_16x16x32_bf16 v[126:129], a[184:187], a[28:31], v[126:129]\n"
        "v_add_f32 v254, v254, v133\n"
        "v_mov_b32 v253, v254\n"
        "v_add_f32 v255, v34, v35\n"
        "v_mfma_f32_16x16x32_bf16 v[126:129], a[180:183], a[24:27], v[126:129]\n"
        "v_permlane32_swap_b32 v254, v253\n"
        "v_add_f32 v254, v254, v253\n"
        "v_mfma_f32_16x16x32_bf16 v[134:137], a[192:195], a[24:27], v[78:81]\n"
        "v_mov_b32 v253, v254\n"
        "v_add_f32 v255, v255, v36\n"
        "v_add_f32 v255, v255, v37\n"
        "v_mfma_f32_16x16x32_bf16 v[134:137], a[196:199], a[28:31], v[134:137]\n"
        "v_permlane16_swap_b32 v254, v253\n"
        "v_add_f32 v254, v254, v253\n"
        "v_mfma_f32_16x16x32_bf16 v[134:137], a[200:203], a[32:35], v[134:137]\n"
        "buffer_load_dwordx4 v2, s[36:39], s49 offen offset:2048 lds\n"
        "v_mul_f32 v254, 0xbac909aa, v254\n"
        "v_fma_f32 v102, v102, s24, v254\n"
        "v_mfma_f32_16x16x32_bf16 v[138:141], a[212:215], a[32:35], v[30:33]\n"
        "v_fma_f32 v103, v103, s24, v254\n"
        "v_fma_f32 v104, v104, s24, v254\n"
        "v_fma_f32 v105, v105, s24, v254\n"
        "v_mfma_f32_16x16x32_bf16 v[138:141], a[208:211], a[28:31], v[138:141]\n"
        "v_fma_f32 v114, v114, s24, v254\n"
        "v_fma_f32 v115, v115, s24, v254\n"
        "v_fma_f32 v116, v116, s24, v254\n"
        "v_mfma_f32_16x16x32_bf16 v[138:141], a[204:207], a[24:27], v[138:141]\n"
        "v_fma_f32 v117, v117, s24, v254\n"
        "v_fma_f32 v118, v118, s24, v254\n"
        "v_fma_f32 v119, v119, s24, v254\n"
        "v_mfma_f32_16x16x32_bf16 v[90:93], a[144:147], a[36:39], v[46:49]\n"
        "v_fma_f32 v120, v120, s24, v254\n"
        "v_fma_f32 v121, v121, s24, v254\n"
        "v_fma_f32 v110, v110, s24, v254\n"
        "v_mfma_f32_16x16x32_bf16 v[90:93], a[148:151], a[40:43], v[90:93]\n"
        "v_fma_f32 v111, v111, s24, v254\n"
        "v_fma_f32 v112, v112, s24, v254\n"
        "v_fma_f32 v113, v113, s24, v254\n"
        "v_mfma_f32_16x16x32_bf16 v[90:93], a[152:155], a[44:47], v[90:93]\n"
        "buffer_load_dwordx4 v3, s[36:39], s49 offen offset:3072 lds\n"
        "v_fma_f32 v122, v122, s24, v254\n"
        "v_fma_f32 v123, v123, s24, v254\n"
        "v_mfma_f32_16x16x32_bf16 v[94:97], a[164:167], a[44:47], v[50:53]\n"
        "v_fma_f32 v124, v124, s24, v254\n"
        "v_fma_f32 v125, v125, s24, v254\n"
        "v_fma_f32 v130, v130, s24, v254\n"
        "v_mfma_f32_16x16x32_bf16 v[94:97], a[160:163], a[40:43], v[94:97]\n"
        "v_fma_f32 v131, v131, s24, v254\n"
        "v_fma_f32 v132, v132, s24, v254\n"
        "v_fma_f32 v133, v133, s24, v254\n"
        "v_mfma_f32_16x16x32_bf16 v[94:97], a[156:159], a[36:39], v[94:97]\n"
        "v_cvt_pk_bf16_f32 v100, v102, v103\n"
        "v_cvt_pk_bf16_f32 v101, v104, v105\n"
        "v_cvt_pk_bf16_f32 v102, v114, v115\n"
        "v_mfma_f32_16x16x32_bf16 v[106:109], a[168:171], a[36:39], v[74:77]\n"
        "v_cvt_pk_bf16_f32 v103, v116, v117\n"
        "v_cvt_pk_bf16_f32 v98, v118, v119\n"
        "v_cvt_pk_bf16_f32 v99, v120, v121\n"
        "v_mfma_f32_16x16x32_bf16 v[106:109], a[172:175], a[40:43], v[106:109]\n"
        "s_add_u32 m0, s52, 4096\n"
        "ds_write_b128 v40, v[100:103]\n"
        "v_cvt_pk_bf16_f32 v100, v110, v111\n"
        "v_mfma_f32_16x16x32_bf16 v[106:109], a[176:179], a[44:47], v[106:109]\n"
        "buffer_load_dwordx4 v4, s[36:39], s49 offen lds\n"
        "v_cvt_pk_bf16_f32 v101, v112, v113\n"
        "v_cvt_pk_bf16_f32 v110, v122, v123\n"
        "v_mfma_f32_16x16x32_bf16 v[102:105], a[188:191], a[44:47], v[82:85]\n"
        "ds_write_b128 v40, v[98:101] offset:1024\n"
        "v_cvt_pk_bf16_f32 v111, v124, v125\n"
        "v_mfma_f32_16x16x32_bf16 v[102:105], a[184:187], a[40:43], v[102:105]\n"
        "v_cvt_pk_bf16_f32 v112, v130, v131\n"
        "v_cvt_pk_bf16_f32 v113, v132, v133\n"
        "v_add_f32 v255, v255, v54\n"
        "v_mfma_f32_16x16x32_bf16 v[102:105], a[180:183], a[36:39], v[102:105]\n"
        "ds_write_b128 v40, v[110:113] offset:2048\n"
        "v_add_f32 v255, v255, v55\n"
        "v_mfma_f32_16x16x32_bf16 v[110:113], a[192:195], a[36:39], v[78:81]\n"
        "v_add_f32 v255, v255, v56\n"
        "v_add_f32 v255, v255, v57\n"
        "v_add_f32 v255, v255, v86\n"
        "v_mfma_f32_16x16x32_bf16 v[110:113], a[196:199], a[40:43], v[110:113]\n"
        "v_add_f32 v255, v255, v87\n"
        "v_add_f32 v255, v255, v88\n"
        "v_add_f32 v255, v255, v89\n"
        "v_mfma_f32_16x16x32_bf16 v[110:113], a[200:203], a[44:47], v[110:113]\n"
        "buffer_load_dwordx4 v5, s[36:39], s49 offen offset:1024 lds\n"
        "v_add_f32 v255, v255, v126\n"
        "v_add_f32 v255, v255, v127\n"
        "v_mfma_f32_16x16x32_bf16 v[98:101], a[212:215], a[44:47], v[30:33]\n"
        "v_add_f32 v255, v255, v128\n"
        "v_add_f32 v255, v255, v129\n"
        "v_add_f32 v255, v255, v134\n"
        "v_mfma_f32_16x16x32_bf16 v[98:101], a[208:211], a[40:43], v[98:101]\n"
        "v_add_f32 v255, v255, v135\n"
        "v_add_f32 v255, v255, v136\n"
        "v_add_f32 v255, v255, v137\n"
        "v_mfma_f32_16x16x32_bf16 v[98:101], a[204:207], a[36:39], v[98:101]\n"
        "v_add_f32 v255, v255, v138\n"
        "v_add_f32 v255, v255, v139\n"
        "v_add_f32 v255, v255, v140\n"
        "v_mfma_f32_16x16x32_bf16 v[118:121], a[144:147], a[48:51], v[46:49]\n"
        "v_add_f32 v255, v255, v141\n"
        "v_mov_b32 v254, v255\n"
        "v_add_f32 v62, v90, v91\n"
        "v_mfma_f32_16x16x32_bf16 v[118:121], a[148:151], a[52:55], v[118:121]\n"
        "v_permlane32_swap_b32 v255, v254\n"
        "v_add_f32 v255, v255, v254\n"
        "v_mfma_f32_16x16x32_bf16 v[118:121], a[152:155], a[56:59], v[118:121]\n"
        "buffer_load_dwordx4 v6, s[36:39], s49 offen offset:2048 lds\n"
        "v_mov_b32 v254, v255\n"
        "v_add_f32 v62, v62, v92\n"
        "v_mfma_f32_16x16x32_bf16 v[122:125], a[164:167], a[56:59], v[50:53]\n"
        "v_permlane16_swap_b32 v255, v254\n"
        "v_add_f32 v255, v255, v254\n"
        "v_mfma_f32_16x16x32_bf16 v[122:125], a[160:163], a[52:55], v[122:125]\n"
        "v_mul_f32 v255, 0xbac909aa, v255\n"
        "v_add_f32 v62, v62, v93\n"
        "v_fma_f32 v34, v34, s24, v255\n"
        "v_mfma_f32_16x16x32_bf16 v[122:125], a[156:159], a[48:51], v[122:125]\n"
        "v_fma_f32 v35, v35, s24, v255\n"
        "v_fma_f32 v36, v36, s24, v255\n"
        "v_fma_f32 v37, v37, s24, v255\n"
        "v_mfma_f32_16x16x32_bf16 v[114:117], a[168:171], a[48:51], v[74:77]\n"
        "v_fma_f32 v54, v54, s24, v255\n"
        "v_fma_f32 v55, v55, s24, v255\n"
        "v_fma_f32 v56, v56, s24, v255\n"
        "v_mfma_f32_16x16x32_bf16 v[114:117], a[172:175], a[52:55], v[114:117]\n"
        "v_fma_f32 v57, v57, s24, v255\n"
        "v_fma_f32 v86, v86, s24, v255\n"
        "v_fma_f32 v87, v87, s24, v255\n"
        "v_mfma_f32_16x16x32_bf16 v[114:117], a[176:179], a[56:59], v[114:117]\n"
        "buffer_load_dwordx4 v7, s[36:39], s49 offen offset:3072 lds\n"
        "v_fma_f32 v88, v88, s24, v255\n"
        "v_fma_f32 v89, v89, s24, v255\n"
        "v_mfma_f32_16x16x32_bf16 v[130:133], a[188:191], a[56:59], v[82:85]\n"
        "v_fma_f32 v126, v126, s24, v255\n"
        "v_fma_f32 v127, v127, s24, v255\n"
        "v_fma_f32 v128, v128, s24, v255\n"
        "v_mfma_f32_16x16x32_bf16 v[130:133], a[184:187], a[52:55], v[130:133]\n"
        "v_fma_f32 v129, v129, s24, v255\n"
        "v_fma_f32 v134, v134, s24, v255\n"
        "v_fma_f32 v135, v135, s24, v255\n"
        "v_mfma_f32_16x16x32_bf16 v[130:133], a[180:183], a[48:51], v[130:133]\n"
        "v_fma_f32 v136, v136, s24, v255\n"
        "v_fma_f32 v137, v137, s24, v255\n"
        "v_fma_f32 v138, v138, s24, v255\n"
        "v_mfma_f32_16x16x32_bf16 v[142:145], a[192:195], a[48:51], v[78:81]\n"
        "v_fma_f32 v139, v139, s24, v255\n"
        "v_fma_f32 v140, v140, s24, v255\n"
        "v_fma_f32 v141, v141, s24, v255\n"
        "v_mfma_f32_16x16x32_bf16 v[142:145], a[196:199], a[52:55], v[142:145]\n"
        "s_add_u32 m0, s52, 8192\n"
        "v_cvt_pk_bf16_f32 v146, v34, v35\n"
        "v_cvt_pk_bf16_f32 v147, v36, v37\n"
        "v_cvt_pk_bf16_f32 v148, v54, v55\n"
        "v_mfma_f32_16x16x32_bf16 v[142:145], a[200:203], a[56:59], v[142:145]\n"
        "buffer_load_dwordx4 v8, s[36:39], s49 offen lds\n"
        "v_cvt_pk_bf16_f32 v149, v56, v57\n"
        "v_cvt_pk_bf16_f32 v34, v86, v87\n"
        "v_mfma_f32_16x16x32_bf16 v[54:57], a[212:215], a[56:59], v[30:33]\n"
        "ds_write_b128 v41, v[146:149]\n"
        "v_cvt_pk_bf16_f32 v35, v88, v89\n"
        "v_mfma_f32_16x16x32_bf16 v[54:57], a[208:211], a[52:55], v[54:57]\n"
        "v_cvt_pk_bf16_f32 v36, v126, v127\n"
        "v_cvt_pk_bf16_f32 v37, v128, v129\n"
        "v_cvt_pk_bf16_f32 v86, v134, v135\n"
        "v_mfma_f32_16x16x32_bf16 v[54:57], a[204:207], a[48:51], v[54:57]\n"
        "ds_write_b128 v41, v[34:37] offset:1024\n"
        "v_cvt_pk_bf16_f32 v87, v136, v137\n"
        "v_mfma_f32_16x16x32_bf16 v[34:37], a[144:147], a[60:63], v[46:49]\n"
        "v_cvt_pk_bf16_f32 v88, v138, v139\n"
        "v_cvt_pk_bf16_f32 v89, v140, v141\n"
        "v_add_f32 v62, v62, v94\n"
        "v_mfma_f32_16x16x32_bf16 v[34:37], a[148:151], a[64:67], v[34:37]\n"
        "ds_write_b128 v41, v[86:89] offset:2048\n"
        "v_add_f32 v62, v62, v95\n"
        "v_mfma_f32_16x16x32_bf16 v[34:37], a[152:155], a[68:71], v[34:37]\n"
        "v_add_f32 v62, v62, v96\n"
        "v_add_f32 v62, v62, v97\n"
        "v_add_f32 v62, v62, v106\n"
        "v_mfma_f32_16x16x32_bf16 v[46:49], a[164:167], a[68:71], v[50:53]\n"
        "v_add_f32 v62, v62, v107\n"
        "v_add_f32 v62, v62, v108\n"
        "v_add_f32 v62, v62, v109\n"
        "v_mfma_f32_16x16x32_bf16 v[46:49], a[160:163], a[64:67], v[46:49]\n"
        "v_add_f32 v62, v62, v102\n"
        "v_add_f32 v62, v62, v103\n"
        "v_add_f32 v62, v62, v104\n"
        "v_mfma_f32_16x16x32_bf16 v[46:49], a[156:159], a[60:63], v[46:49]\n"
        "v_add_f32 v62, v62, v105\n"
        "v_add_f32 v62, v62, v110\n"
        "v_add_f32 v62, v62, v111\n"
        "v_mfma_f32_16x16x32_bf16 v[50:53], a[168:171], a[60:63], v[74:77]\n"
        "v_add_f32 v62, v62, v112\n"
        "v_add_f32 v62, v62, v113\n"
        "v_add_f32 v62, v62, v98\n"
        "v_mfma_f32_16x16x32_bf16 v[50:53], a[172:175], a[64:67], v[50:53]\n"
        "v_add_f32 v62, v62, v99\n"
        "v_add_f32 v62, v62, v100\n"
        "v_add_f32 v62, v62, v101\n"
        "v_mfma_f32_16x16x32_bf16 v[50:53], a[176:179], a[68:71], v[50:53]\n"
        "v_mov_b32 v63, v62\n"
        "v_add_f32 v68, v118, v119\n"
        "v_add_f32 v68, v68, v120\n"
        "v_mfma_f32_16x16x32_bf16 v[74:77], a[188:191], a[68:71], v[82:85]\n"
        "v_permlane32_swap_b32 v62, v63\n"
        "v_add_f32 v62, v62, v63\n"
        "v_mfma_f32_16x16x32_bf16 v[74:77], a[184:187], a[64:67], v[74:77]\n"
        "v_mov_b32 v63, v62\n"
        "v_add_f32 v68, v68, v121\n"
        "v_add_f32 v68, v68, v122\n"
        "v_mfma_f32_16x16x32_bf16 v[74:77], a[180:183], a[60:63], v[74:77]\n"
        "v_permlane16_swap_b32 v62, v63\n"
        "v_add_f32 v62, v62, v63\n"
        "v_mfma_f32_16x16x32_bf16 v[38:41], a[192:195], a[60:63], v[78:81]\n"
        "v_mul_f32 v62, 0xbac909aa, v62\n"
        "v_add_f32 v68, v68, v123\n"
        "v_add_f32 v68, v68, v124\n"
        "v_mfma_f32_16x16x32_bf16 v[38:41], a[196:199], a[64:67], v[38:41]\n"
        "v_add_f32 v68, v68, v125\n"
        "v_add_f32 v68, v68, v114\n"
        "v_add_f32 v68, v68, v115\n"
        "v_mfma_f32_16x16x32_bf16 v[38:41], a[200:203], a[68:71], v[38:41]\n"
        "v_add_f32 v68, v68, v116\n"
        "v_add_f32 v68, v68, v117\n"
        "v_add_f32 v68, v68, v130\n"
        "v_mfma_f32_16x16x32_bf16 v[78:81], a[212:215], a[68:71], v[30:33]\n"
        "v_add_f32 v68, v68, v131\n"
        "v_add_f32 v68, v68, v132\n"
        "v_add_f32 v68, v68, v133\n"
        "v_mfma_f32_16x16x32_bf16 v[78:81], a[208:211], a[64:67], v[78:81]\n"
        "v_add_f32 v68, v68, v142\n"
        "v_add_f32 v68, v68, v143\n"
        "v_add_f32 v68, v68, v144\n"
        "v_mfma_f32_16x16x32_bf16 v[78:81], a[204:207], a[60:63], v[78:81]\n"
        "v_add_f32 v68, v68, v145\n"
        "v_add_f32 v68, v68, v54\n"
        "v_add_f32 v68, v68, v55\n"
        "v_add_f32 v68, v68, v56\n"
        "v_add_f32 v68, v68, v57\n"
        "v_mov_b32 v69, v68\n"
        "v_add_f32 v126, v34, v35\n"
        "v_add_f32 v126, v126, v36\n"
        "v_permlane32_swap_b32 v68, v69\n"
        "v_add_f32 v68, v68, v69\n"
        "v_mov_b32 v69, v68\n"
        "v_add_f32 v126, v126, v37\n"
        "v_add_f32 v126, v126, v46\n"
        "v_permlane16_swap_b32 v68, v69\n"
        "v_add_f32 v68, v68, v69\n"
        "v_mul_f32 v68, 0xbac909aa, v68\n"
        "v_add_f32 v126, v126, v47\n"
        "v_add_f32 v126, v126, v48\n"
        "v_add_f32 v126, v126, v49\n"
        "v_add_f32 v126, v126, v50\n"
        "v_add_f32 v126, v126, v51\n"
        "v_add_f32 v126, v126, v52\n"
        "v_add_f32 v126, v126, v53\n"
        "v_add_f32 v126, v126, v74\n"
        "v_add_f32 v126, v126, v75\n"
        "v_add_f32 v126, v126, v76\n"
        "v_add_f32 v126, v126, v77\n"
        "v_add_f32 v126, v126, v38\n"
        "v_add_f32 v126, v126, v39\n"
        "v_add_f32 v126, v126, v40\n"
        "v_add_f32 v126, v126, v41\n"
        "v_pk_fma_f32 v[90:91], v[90:91], s[24:25], v[62:63] op_sel_hi:[1,0,0]\n"
        "v_pk_fma_f32 v[92:93], v[92:93], s[24:25], v[62:63] op_sel_hi:[1,0,0]\n"
        "v_pk_fma_f32 v[94:95], v[94:95], s[24:25], v[62:63] op_sel_hi:[1,0,0]\n"
        "v_pk_fma_f32 v[96:97], v[96:97], s[24:25], v[62:63] op_sel_hi:[1,0,0]\n"
        "v_pk_fma_f32 v[106:107], v[106:107], s[24:25], v[62:63] op_sel_hi:[1,0,0]\n"
        "v_pk_fma_f32 v[108:109], v[108:109], s[24:25], v[62:63] op_sel_hi:[1,0,0]\n"
        "v_pk_fma_f32 v[102:103], v[102:103], s[24:25], v[62:63] op_sel_hi:[1,0,0]\n"
        "v_pk_fma_f32 v[104:105], v[104:105], s[24:25], v[62:63] op_sel_hi:[1,0,0]\n"
        "v_pk_fma_f32 v[110:111], v[110:111], s[24:25], v[62:63] op_sel_hi:[1,0,0]\n"
        "v_pk_fma_f32 v[112:113], v[112:113], s[24:25], v[62:63] op_sel_hi:[1,0,0]\n"
        "v_pk_fma_f32 v[98:99], v[98:99], s[24:25], v[62:63] op_sel_hi:[1,0,0]\n"
        "v_pk_fma_f32 v[100:101], v[100:101], s[24:25], v[62:63] op_sel_hi:[1,0,0]\n"
        "v_pk_fma_f32 v[118:119], v[118:119], s[24:25], v[68:69] op_sel_hi:[1,0,0]\n"
        "v_pk_fma_f32 v[120:121], v[120:121], s[24:25], v[68:69] op_sel_hi:[1,0,0]\n"
        "v_pk_fma_f32 v[122:123], v[122:123], s[24:25], v[68:69] op_sel_hi:[1,0,0]\n"
        "v_pk_fma_f32 v[124:125], v[124:125], s[24:25], v[68:69] op_sel_hi:[1,0,0]\n"
        "v_pk_fma_f32 v[114:115], v[114:115], s[24:25], v[68:69] op_sel_hi:[1,0,0]\n"
        "v_pk_fma_f32 v[116:117], v[116:117], s[24:25], v[68:69] op_sel_hi:[1,0,0]\n"
        "v_pk_fma_f32 v[130:131], v[130:131], s[24:25], v[68:69] op_sel_hi:[1,0,0]\n"
        "v_pk_fma_f32 v[132:133], v[132:133], s[24:25], v[68:69] op_sel_hi:[1,0,0]\n"
        "v_pk_fma_f32 v[142:143], v[142:143], s[24:25], v[68:69] op_sel_hi:[1,0,0]\n"
        "v_pk_fma_f32 v[144:145], v[144:145], s[24:25], v[68:69] op_sel_hi:[1,0,0]\n"
        "v_pk_fma_f32 v[54:55], v[54:55], s[24:25], v[68:69] op_sel_hi:[1,0,0]\n"
        "v_pk_fma_f32 v[56:57], v[56:57], s[24:25], v[68:69] op_sel_hi:[1,0,0]\n"
        "v_cvt_pk_bf16_f32 v30, v90, v91\n"
        "v_cvt_pk_bf16_f32 v31, v92, v93\n"
        "v_cvt_pk_bf16_f32 v32, v94, v95\n"
        "v_cvt_pk_bf16_f32 v33, v96, v97\n"
        "v_cvt_pk_bf16_f32 v82, v106, v107\n"
        "v_cvt_pk_bf16_f32 v83, v108, v109\n"
        "v_cvt_pk_bf16_f32 v84, v102, v103\n"
        "v_cvt_pk_bf16_f32 v85, v104, v105\n"
        "v_cvt_pk_bf16_f32 v86, v110, v111\n"
        "v_cvt_pk_bf16_f32 v87, v112, v113\n"
        "v_cvt_pk_bf16_f32 v88, v98, v99\n"
        "v_cvt_pk_bf16_f32 v89, v100, v101\n"
        "v_cvt_pk_bf16_f32 v94, v118, v119\n"
        "v_cvt_pk_bf16_f32 v95, v120, v121\n"
        "v_cvt_pk_bf16_f32 v96, v122, v123\n"
        "v_cvt_pk_bf16_f32 v97, v124, v125\n"
        "v_cvt_pk_bf16_f32 v90, v114, v115\n"
        "v_cvt_pk_bf16_f32 v91, v116, v117\n"
        "v_cvt_pk_bf16_f32 v92, v130, v131\n"
        "v_cvt_pk_bf16_f32 v93, v132, v133\n"
        "v_cvt_pk_bf16_f32 v98, v142, v143\n"
        "v_cvt_pk_bf16_f32 v99, v144, v145\n"
        "v_cvt_pk_bf16_f32 v100, v54, v55\n"
        "v_cvt_pk_bf16_f32 v101, v56, v57\n"
        "v_add_f32 v126, v126, v78\n"
        "ds_write_b128 v42, v[30:33]\n"
        "ds_write_b128 v42, v[82:85] offset:1024\n"
        "ds_write_b128 v42, v[86:89] offset:2048\n"
        "ds_write_b128 v43, v[94:97]\n"
        "ds_write_b128 v43, v[90:93] offset:1024\n"
        "ds_write_b128 v43, v[98:101] offset:2048\n"
        "v_add_f32 v126, v126, v79\n"
        "v_add_f32 v126, v126, v80\n"
        "v_add_f32 v126, v126, v81\n"
        "v_mov_b32 v127, v126\n"
        "s_waitcnt vmcnt(9)\n"
        "s_nop 0\n"
        "v_permlane32_swap_b32 v126, v127\n"
        "v_add_f32 v126, v126, v127\n"
        "v_mov_b32 v127, v126\n"
        "s_nop 1\n"
        "v_permlane16_swap_b32 v126, v127\n"
        "v_add_f32 v126, v126, v127\n"
        "v_mul_f32 v126, 0xbac909aa, v126\n"
        "v_pk_fma_f32 v[34:35], v[34:35], s[24:25], v[126:127] op_sel_hi:[1,0,0]\n"
        "v_pk_fma_f32 v[36:37], v[36:37], s[24:25], v[126:127] op_sel_hi:[1,0,0]\n"
        "v_pk_fma_f32 v[46:47], v[46:47], s[24:25], v[126:127] op_sel_hi:[1,0,0]\n"
        "v_pk_fma_f32 v[48:49], v[48:49], s[24:25], v[126:127] op_sel_hi:[1,0,0]\n"
        "v_pk_fma_f32 v[50:51], v[50:51], s[24:25], v[126:127] op_sel_hi:[1,0,0]\n"
        "v_pk_fma_f32 v[52:53], v[52:53], s[24:25], v[126:127] op_sel_hi:[1,0,0]\n"
        "v_pk_fma_f32 v[74:75], v[74:75], s[24:25], v[126:127] op_sel_hi:[1,0,0]\n"
        "v_pk_fma_f32 v[76:77], v[76:77], s[24:25], v[126:127] op_sel_hi:[1,0,0]\n"
        "v_pk_fma_f32 v[38:39], v[38:39], s[24:25], v[126:127] op_sel_hi:[1,0,0]\n"
        "v_pk_fma_f32 v[40:41], v[40:41], s[24:25], v[126:127] op_sel_hi:[1,0,0]\n"
        "v_pk_fma_f32 v[78:79], v[78:79], s[24:25], v[126:127] op_sel_hi:[1,0,0]\n"
        "v_pk_fma_f32 v[80:81], v[80:81], s[24:25], v[126:127] op_sel_hi:[1,0,0]\n"
        "v_cvt_pk_bf16_f32 v54, v34, v35\n"
        "v_cvt_pk_bf16_f32 v55, v36, v37\n"
        "v_cvt_pk_bf16_f32 v56, v46, v47\n"
        "v_cvt_pk_bf16_f32 v57, v48, v49\n"
        "v_cvt_pk_bf16_f32 v34, v50, v51\n"
        "v_cvt_pk_bf16_f32 v35, v52, v53\n"
        "v_cvt_pk_bf16_f32 v36, v74, v75\n"
        "v_cvt_pk_bf16_f32 v37, v76, v77\n"
        "v_cvt_pk_bf16_f32 v30, v38, v39\n"
        "v_cvt_pk_bf16_f32 v31, v40, v41\n"
        "v_cvt_pk_bf16_f32 v32, v78, v79\n"
        "v_cvt_pk_bf16_f32 v33, v80, v81\n"
        "ds_write_b128 v44, v[54:57]\n"
        "ds_write_b128 v44, v[34:37] offset:1024\n"
        "ds_write_b128 v44, v[30:33] offset:2048\n"
        "s_waitcnt lgkmcnt(0)\n"
        "s_barrier\n"
        "s_branch FE_%=\n"
        "FV_%=:\n"
        "s_bitcmp1_b32 s52, 11\n"
        "s_cbranch_scc1 FG_%=\n"
        "v_mov_b32 v50, 0x3f803f80\n"
        "v_mov_b32 v51, 0x3f803f80\n"
        "v_mov_b32 v52, 0x3f803f80\n"
        "v_mov_b32 v53, 0x3f803f80\n"
        "s_waitcnt vmcnt(15)\n"
        "ds_read_b128 a[0:3], v32\n"
        "ds_read_b128 a[4:7], v32 offset:64\n"
        "ds_read_b128 a[8:11], v32 offset:128\n"
        "s_add_u32 m0, s53, 15360\n"
        "s_nop 0\n"
        "buffer_load_dwordx4 v46, s[16:19], s23 offen lds\n"
        "ds_read_b128 a[12:15], v33\n"
        "ds_read_b128 a[16:19], v33 offset:64\n"
        "ds_read_b128 a[20:23], v33 offset:128\n"
        "buffer_load_dwordx4 v47, s[16:19], s23 offen offset:1024 lds\n"
        "ds_read_b128 a[24:27], v32 offset:6144\n"
        "ds_read_b128 a[28:31], v32 offset:6208\n"
        "ds_read_b128 a[32:35], v32 offset:6272\n"
        "buffer_load_dwordx4 v48, s[16:19], s23 offen offset:2048 lds\n"
        "ds_read_b128 a[36:39], v33 offset:6144\n"
        "ds_read_b128 a[40:43], v33 offset:6208\n"
        "ds_read_b128 a[44:47], v33 offset:6272\n"
        "ds_read_b128 a[48:51], v32 offset:12288\n"
        "ds_read_b128 a[52:55], v32 offset:12352\n"
        "ds_read_b128 a[56:59], v32 offset:12416\n"
        "ds_read_b128 a[60:63], v33 offset:12288\n"
        "ds_read_b128 a[64:67], v33 offset:12352\n"
        "ds_read_b128 a[68:71], v33 offset:12416\n"
        "ds_read_b64_tr_b16 a[72:73], v34 offset:0\n"
        "ds_read_b64_tr_b16 a[74:75], v35 offset:0\n"
        "ds_read_b64_tr_b16 a[76:77], v34 offset:6144\n"
        "ds_read_b64_tr_b16 a[78:79], v35 offset:6144\n"
        "ds_read_b64_tr_b16 a[80:81], v34 offset:12288\n"
        "ds_read_b64_tr_b16 a[82:83], v35 offset:12288\n"
        "s_waitcnt lgkmcnt(15)\n"
        "v_mfma_f32_16x16x32_bf16 v[54:57], a[0:3], v[50:53], 0\n"
        "s_waitcnt vmcnt(12)\n"
        "s_waitcnt vmcnt(6)\n"
        "v_mfma_f32_16x16x32_bf16 v[54:57], a[4:7], v[50:53], v[54:57]\n"
        "s_waitcnt lgkmcnt(15)\n"
        "v_mfma_f32_16x16x32_bf16 v[54:57], a[8:11], v[50:53], v[54:57]\n"
        "s_waitcnt vmcnt(0)\n"
        "s_nop 0\n"
        "v_mfma_f32_16x16x32_bf16 v[58:61], a[20:23], v[50:53], 0\n"
        "s_waitcnt lgkmcnt(15)\n"
        "s_nop 1\n"
        "v_mfma_f32_16x16x32_bf16 v[58:61], a[16:19], v[50:53], v[58:61]\n"
        "s_waitcnt lgkmcnt(12)\n"
        "v_cvt_pk_bf16_f32 v46, v54, v55\n"
        "v_cvt_pk_bf16_f32 v47, v56, v57\n"
        "v_mfma_f32_16x16x32_bf16 v[58:61], a[12:15], v[50:53], v[58:61]\n"
        "s_waitcnt lgkmcnt(9)\n"
        "v_mfma_f32_16x16x32_bf16 v[54:57], a[24:27], v[50:53], 0\n"
        "s_waitcnt lgkmcnt(6)\n"
        "ds_read_b128 a[144:147], v30\n"
        "s_nop 0\n"
        "v_mfma_f32_16x16x32_bf16 v[54:57], a[28:31], v[50:53], v[54:57]\n"
        "s_waitcnt lgkmcnt(1)\n"
        "ds_read_b128 a[148:151], v30 offset:64\n"
        "v_cvt_pk_bf16_f32 v48, v58, v59\n"
        "v_cvt_pk_bf16_f32 v49, v60, v61\n"
        "v_mfma_f32_16x16x32_bf16 v[54:57], a[32:35], v[50:53], v[54:57]\n"
        "s_nop 0\n"
        "ds_read_b128 a[152:155], v30 offset:128\n"
        "v_mfma_f32_16x16x32_bf16 v[58:61], a[44:47], v[50:53], 0\n"
        "s_nop 0\n"
        "ds_read_b128 a[156:159], v31\n"
        "v_mfma_f32_16x16x32_bf16 v[58:61], a[40:43], v[50:53], v[58:61]\n"
        "s_nop 0\n"
        "ds_read_b128 a[160:163], v31 offset:64\n"
        "v_cvt_pk_bf16_f32 v62, v54, v55\n"
        "v_cvt_pk_bf16_f32 v63, v56, v57\n"
        "v_mfma_f32_16x16x32_bf16 v[58:61], a[36:39], v[50:53], v[58:61]\n"
        "s_nop 0\n"
        "ds_read_b128 a[164:167], v31 offset:128\n"
        "v_mfma_f32_16x16x32_bf16 v[54:57], a[48:51], v[50:53], 0\n"
        "s_nop 0\n"
        "ds_read_b128 a[168:171], v30 offset:6144\n"
        "v_mfma_f32_16x16x32_bf16 v[54:57], a[52:55], v[50:53], v[54:57]\n"
        "s_nop 0\n"
        "ds_read_b128 a[172:175], v30 offset:6208\n"
        "v_cvt_pk_bf16_f32 v64, v58, v59\n"
        "v_cvt_pk_bf16_f32 v65, v60, v61\n"
        "v_mfma_f32_16x16x32_bf16 v[54:57], a[56:59], v[50:53], v[54:57]\n"
        "s_nop 0\n"
        "ds_read_b128 a[176:179], v30 offset:6272\n"
        "v_mfma_f32_16x16x32_bf16 v[58:61], a[68:71], v[50:53], 0\n"
        "s_nop 0\n"
        "ds_read_b128 a[180:183], v31 offset:6144\n"
        "v_mfma_f32_16x16x32_bf16 v[58:61], a[64:67], v[50:53], v[58:61]\n"
        "s_nop 0\n"
        "ds_read_b128 a[184:187], v31 offset:6208\n"
        "v_cvt_pk_bf16_f32 v66, v54, v55\n"
        "v_cvt_pk_bf16_f32 v67, v56, v57\n"
        "v_mfma_f32_16x16x32_bf16 v[58:61], a[60:63], v[50:53], v[58:61]\n"
        "ds_read_b128 a[188:191], v31 offset:6272\n"
        "s_waitcnt lgkmcnt(9)\n"
        "v_mfma_f32_16x16x32_bf16 v[50:53], a[144:147], a[72:75], 0\n"
        "s_nop 0\n"
        "ds_read_b128 a[192:195], v30 offset:12288\n"
        "v_mfma_f32_16x16x32_bf16 v[50:53], a[148:151], a[76:79], v[50:53]\n"
        "s_nop 0\n"
        "ds_read_b128 a[196:199], v30 offset:12352\n"
        "v_cvt_pk_bf16_f32 v68, v58, v59\n"
        "v_cvt_pk_bf16_f32 v69, v60, v61\n"
        "v_mfma_f32_16x16x32_bf16 v[50:53], a[152:155], a[80:83], v[50:53]\n"
        "ds_read_b128 a[200:203], v30 offset:12416\n"
        "v_mfma_f32_16x16x32_bf16 v[54:57], a[152:155], v[66:69], 0\n"
        "s_waitcnt lgkmcnt(9)\n"
        "buffer_load_dwordx4 v[196:199], v36, s[36:39], 0 offen\n"
        "v_mfma_f32_16x16x32_bf16 v[54:57], a[148:151], v[62:65], v[54:57]\n"
        "s_nop 0\n"
        "ds_read_b128 a[204:207], v31 offset:12288\n"
        "ds_read_b128 a[208:211], v31 offset:12352\n"
        "v_add_f32 v254, v50, v51\n"
        "v_mfma_f32_16x16x32_bf16 v[54:57], a[144:147], v[46:49], v[54:57]\n"
        "ds_read_b128 a[212:215], v31 offset:12416\n"
        "v_add_f32 v254, v254, v52\n"
        "v_add_f32 v254, v254, v53\n"
        "v_mfma_f32_16x16x32_bf16 v[98:101], a[156:159], a[72:75], 0\n"
        "s_waitcnt lgkmcnt(9)\n"
        "ds_read_b64_tr_b16 a[84:85], v34 offset:8\n"
        "v_mfma_f32_16x16x32_bf16 v[98:101], a[160:163], a[76:79], v[98:101]\n"
        "ds_read_b64_tr_b16 a[86:87], v35 offset:8\n"
        "v_add_f32 v255, v54, v55\n"
        "v_add_f32 v255, v255, v56\n"
        "v_mfma_f32_16x16x32_bf16 v[98:101], a[164:167], a[80:83], v[98:101]\n"
        "ds_read_b64_tr_b16 a[88:89], v34 offset:6152\n"
        "v_add_f32 v255, v255, v57\n"
        "v_mfma_f32_16x16x32_bf16 v[30:33], a[164:167], v[66:69], 0\n"
        "s_waitcnt lgkmcnt(9)\n"
        "ds_read_b64_tr_b16 a[90:91], v35 offset:6152\n"
        "v_mfma_f32_16x16x32_bf16 v[30:33], a[160:163], v[62:65], v[30:33]\n"
        "s_nop 0\n"
        "ds_read_b64_tr_b16 a[92:93], v34 offset:12296\n"
        "v_add_f32 v254, v254, v98\n"
        "v_add_f32 v254, v254, v99\n"
        "v_mfma_f32_16x16x32_bf16 v[30:33], a[156:159], v[46:49], v[30:33]\n"
        "ds_read_b64_tr_b16 a[94:95], v35 offset:12296\n"
        "v_add_f32 v254, v254, v100\n"
        "v_add_f32 v254, v254, v101\n"
        "v_mfma_f32_16x16x32_bf16 v[104:107], a[168:171], a[72:75], 0\n"
        "s_waitcnt lgkmcnt(9)\n"
        "buffer_load_dwordx4 v[202:205], v36, s[36:39], 0 offen offset:16\n"
        "v_mfma_f32_16x16x32_bf16 v[104:107], a[172:175], a[76:79], v[104:107]\n"
        "s_mov_b32 s78, 0xf0000f\n"
        "ds_read_b64_tr_b16 a[96:97], v34 offset:64\n"
        "ds_read_b64_tr_b16 a[98:99], v35 offset:64\n"
        "v_add_f32 v255, v255, v30\n"
        "v_mfma_f32_16x16x32_bf16 v[104:107], a[176:179], a[80:83], v[104:107]\n"
        "s_mov_b32 s79, 0xf0000f00\n"
        "ds_read_b64_tr_b16 a[100:101], v34 offset:6208\n"
        "v_add_f32 v255, v255, v31\n"
        "v_add_f32 v255, v255, v32\n"
        "v_mfma_f32_16x16x32_bf16 v[74:77], a[176:179], v[66:69], 0\n"
        "s_mov_b32 s76, 0x11111111\n"
        "ds_read_b64_tr_b16 a[102:103], v35 offset:6208\n"
        "v_add_f32 v255, v255, v33\n"
        "v_add_f32 v254, v254, v104\n"
        "v_mfma_f32_16x16x32_bf16 v[74:77], a[172:175], v[62:65], v[74:77]\n"
        "s_waitcnt lgkmcnt(10)\n"
        "ds_read_b64_tr_b16 a[104:105], v34 offset:12352\n"
        "v_add_f32 v254, v254, v105\n"
        "v_add_f32 v254, v254, v106\n"
        "v_mfma_f32_16x16x32_bf16 v[74:77], a[168:171], v[46:49], v[74:77]\n"
        "s_mov_b32 s77, 0x11111111\n"
        "ds_read_b64_tr_b16 a[106:107], v35 offset:12352\n"
        "v_add_f32 v254, v254, v107\n"
        "v_mfma_f32_16x16x32_bf16 v[108:111], a[180:183], a[72:75], 0\n"
        "s_mov_b32 s56, 0x22222222\n"
        "ds_read_b64_tr_b16 a[108:109], v34 offset:72\n"
        "v_mfma_f32_16x16x32_bf16 v[108:111], a[184:187], a[76:79], v[108:111]\n"
        "s_mov_b32 s57, 0x22222222\n"
        "ds_read_b64_tr_b16 a[110:111], v35 offset:72\n"
        "v_add_f32 v255, v255, v74\n"
        "v_add_f32 v255, v255, v75\n"
        "v_mfma_f32_16x16x32_bf16 v[108:111], a[188:191], a[80:83], v[108:111]\n"
        "s_mov_b32 s58, 0x44444444\n"
        "ds_read_b64_tr_b16 a[112:113], v34 offset:6216\n"
        "v_add_f32 v255, v255, v76\n"
        "v_add_f32 v255, v255, v77\n"
        "v_mfma_f32_16x16x32_bf16 v[78:81], a[188:191], v[66:69], 0\n"
        "s_waitcnt lgkmcnt(9)\n"
        "buffer_load_dwordx4 v[208:211], v36, s[36:39], 0 offen offset:32\n"
        "v_mfma_f32_16x16x32_bf16 v[78:81], a[184:187], v[62:65], v[78:81]\n"
        "s_mov_b32 s59, 0x44444444\n"
        "ds_read_b64_tr_b16 a[114:115], v35 offset:6216\n"
        "ds_read_b64_tr_b16 a[116:117], v34 offset:12360\n"
        "v_add_f32 v254, v254, v108\n"
        "v_mfma_f32_16x16x32_bf16 v[78:81], a[180:183], v[46:49], v[78:81]\n"
        "s_mov_b32 s60, 0x88888888\n"
        "ds_read_b64_tr_b16 a[118:119], v35 offset:12360\n"
        "v_add_f32 v254, v254, v109\n"
        "v_add_f32 v254, v254, v110\n"
        "v_mfma_f32_16x16x32_bf16 v[112:115], a[192:195], a[72:75], 0\n"
        "s_mov_b32 s61, 0x88888888\n"
        "ds_read_b64_tr_b16 a[120:121], v34 offset:128\n"
        "v_add_f32 v254, v254, v111\n"
        "v_add_f32 v255, v255, v78\n"
        "v_mfma_f32_16x16x32_bf16 v[112:115], a[196:199], a[76:79], v[112:115]\n"
        "s_mov_b32 s62, 0xffff\n"
        "ds_read_b64_tr_b16 a[122:123], v35 offset:128\n"
        "v_add_f32 v255, v255, v79\n"
        "v_add_f32 v255, v255, v80\n"
        "v_mfma_f32_16x16x32_bf16 v[112:115], a[200:203], a[80:83], v[112:115]\n"
        "s_mov_b32 s63, 0x0\n"
        "ds_read_b64_tr_b16 a[124:125], v34 offset:6272\n"
        "v_add_f32 v255, v255, v81\n"
        "v_mfma_f32_16x16x32_bf16 v[126:129], a[200:203], v[66:69], 0\n"
        "s_mov_b32 s64, 0xffff0000\n"
        "ds_read_b64_tr_b16 a[126:127], v35 offset:6272\n"
        "v_mfma_f32_16x16x32_bf16 v[126:129], a[196:199], v[62:65], v[126:129]\n"
        "s_mov_b32 s65, 0x0\n"
        "ds_read_b64_tr_b16 a[128:129], v34 offset:12416\n"
        "v_add_f32 v254, v254, v112\n"
        "v_add_f32 v254, v254, v113\n"
        "v_mfma_f32_16x16x32_bf16 v[126:129], a[192:195], v[46:49], v[126:129]\n"
        "s_mov_b32 s66, 0x0\n"
        "ds_read_b64_tr_b16 a[130:131], v35 offset:12416\n"
        "v_add_f32 v254, v254, v114\n"
        "v_add_f32 v254, v254, v115\n"
        "v_mfma_f32_16x16x32_bf16 v[122:125], a[204:207], a[72:75], 0\n"
        "s_waitcnt lgkmcnt(12)\n"
        "buffer_load_dwordx4 v[58:61], v37, s[36:39], 0 offen\n"
        "v_mfma_f32_16x16x32_bf16 v[122:125], a[208:211], a[76:79], v[122:125]\n"
        "s_mov_b32 s67, 0xffff\n"
        "ds_read_b64_tr_b16 a[132:133], v34 offset:136\n"
        "ds_read_b64_tr_b16 a[134:135], v35 offset:136\n"
        "v_add_f32 v255, v255, v126\n"
        "v_mfma_f32_16x16x32_bf16 v[122:125], a[212:215], a[80:83], v[122:125]\n"
        "s_mov_b32 s68, 0x0\n"
        "ds_read_b64_tr_b16 a[136:137], v34 offset:6280\n"
        "v_add_f32 v255, v255, v127\n"
        "v_add_f32 v255, v255, v128\n"
        "v_mfma_f32_16x16x32_bf16 v[118:121], a[212:215], v[66:69], 0\n"
        "s_mov_b32 s69, 0xffff0000\n"
        "ds_read_b64_tr_b16 a[138:139], v35 offset:6280\n"
        "v_add_f32 v255, v255, v129\n"
        "v_add_f32 v254, v254, v122\n"
        "v_mfma_f32_16x16x32_bf16 v[118:121], a[208:211], v[62:65], v[118:121]\n"
        "s_mov_b32 s70, 0x0\n"
        "ds_read_b64_tr_b16 a[140:141], v34 offset:12424\n"
        "v_add_f32 v254, v254, v123\n"
        "v_add_f32 v254, v254, v124\n"
        "v_mfma_f32_16x16x32_bf16 v[118:121], a[204:207], v[46:49], v[118:121]\n"
        "s_mov_b32 s71, 0xffffffff\n"
        "ds_read_b64_tr_b16 a[142:143], v35 offset:12424\n"
        "v_add_f32 v254, v254, v125\n"
        "v_mov_b32 v253, v254\n"
        "v_mfma_f32_16x16x32_bf16 v[46:49], a[144:147], a[84:87], 0\n"
        "s_mov_b32 s72, 0x0\n"
        "v_permlane32_swap_b32 v254, v253\n"
        "v_add_f32 v254, v254, v253\n"
        "v_mfma_f32_16x16x32_bf16 v[46:49], a[148:151], a[88:91], v[46:49]\n"
        "s_mov_b32 s73, 0x20000\n"
        "v_mov_b32 v253, v254\n"
        "v_add_f32 v255, v255, v118\n"
        "v_add_f32 v255, v255, v119\n"
        "v_mfma_f32_16x16x32_bf16 v[46:49], a[152:155], a[92:95], v[46:49]\n"
        "s_mov_b64 vcc, s[78:79]\n"
        "buffer_load_dwordx4 v[64:67], v37, s[36:39], 0 offen offset:16\n"
        "v_add_f32 v255, v255, v120\n"
        "v_add_f32 v255, v255, v121\n"
        "v_mfma_f32_16x16x32_bf16 v[86:89], a[164:167], a[92:95], 0\n"
        "s_mov_b32 s80, 0x3c2aaaab\n"
        "v_permlane16_swap_b32 v254, v253\n"
        "v_add_f32 v254, v254, v253\n"
        "v_mfma_f32_16x16x32_bf16 v[86:89], a[160:163], a[88:91], v[86:89]\n"
        "s_mov_b32 s81, 0x3727c5ac\n"
        "v_mul_f32 v254, 0xbc2aaaab, v254\n"
        "v_add_f32 v50, v50, v254\n"
        "v_add_f32 v51, v51, v254\n"
        "v_mfma_f32_16x16x32_bf16 v[86:89], a[156:159], a[84:87], v[86:89]\n"
        "s_mov_b32 s82, 0xc000c000\n"
        "v_add_f32 v52, v52, v254\n"
        "v_add_f32 v53, v53, v254\n"
        "v_add_f32 v98, v98, v254\n"
        "v_mfma_f32_16x16x32_bf16 v[90:93], a[168:171], a[84:87], 0\n"
        "s_mov_b32 s84, 0x80000000\n"
        "v_add_f32 v99, v99, v254\n"
        "v_add_f32 v100, v100, v254\n"
        "v_add_f32 v101, v101, v254\n"
        "v_mfma_f32_16x16x32_bf16 v[90:93], a[172:175], a[88:91], v[90:93]\n"
        "s_mov_b32 s85, 19\n"
        "v_add_f32 v104, v104, v254\n"
        "v_add_f32 v105, v105, v254\n"
        "v_add_f32 v106, v106, v254\n"
        "v_mfma_f32_16x16x32_bf16 v[90:93], a[176:179], a[92:95], v[90:93]\n"
        "s_waitcnt lgkmcnt(0)\n"
        "v_add_f32 v107, v107, v254\n"
        "v_add_f32 v108, v108, v254\n"
        "v_add_f32 v109, v109, v254\n"
        "v_mfma_f32_16x16x32_bf16 v[82:85], a[188:191], a[92:95], 0\n"
        "s_barrier\n"
        "v_add_f32 v110, v110, v254\n"
        "v_add_f32 v111, v111, v254\n"
        "v_add_f32 v112, v112, v254\n"
        "v_mfma_f32_16x16x32_bf16 v[82:85], a[184:187], a[88:91], v[82:85]\n"
        "s_mov_b32 s86, 0x80000000\n"
        "v_add_f32 v113, v113, v254\n"
        "v_add_f32 v114, v114, v254\n"
        "v_add_f32 v115, v115, v254\n"
        "v_mfma_f32_16x16x32_bf16 v[82:85], a[180:183], a[84:87], v[82:85]\n"
        "s_mov_b32 s89, 0x80000000\n"
        "buffer_load_dwordx4 v[70:73], v37, s[36:39], 0 offen offset:32\n"
        "v_add_f32 v122, v122, v254\n"
        "v_add_f32 v123, v123, v254\n"
        "v_mfma_f32_16x16x32_bf16 v[94:97], a[192:195], a[84:87], 0\n"
        "s_mov_b32 s29, 1\n"
        "v_add_f32 v124, v124, v254\n"
        "v_add_f32 v125, v125, v254\n"
        "v_cvt_pk_bf16_f32 v34, v50, v51\n"
        "v_mfma_f32_16x16x32_bf16 v[94:97], a[196:199], a[88:91], v[94:97]\n"
        "s_mov_b32 s31, 0x400000\n"
        "v_cvt_pk_bf16_f32 v35, v52, v53\n"
        "v_cvt_pk_bf16_f32 v36, v98, v99\n"
        "v_cvt_pk_bf16_f32 v37, v100, v101\n"
        "v_mfma_f32_16x16x32_bf16 v[94:97], a[200:203], a[92:95], v[94:97]\n"
        "s_mov_b32 s87, s49\n"
        "ds_write_b128 v39, v[34:37]\n"
        "v_cvt_pk_bf16_f32 v34, v104, v105\n"
        "v_mfma_f32_16x16x32_bf16 v[102:105], a[212:215], a[92:95], 0\n"
        "s_mov_b32 s88, s50\n"
        "v_cvt_pk_bf16_f32 v35, v106, v107\n"
        "v_cvt_pk_bf16_f32 v36, v108, v109\n"
        "v_cvt_pk_bf16_f32 v37, v110, v111\n"
        "v_mfma_f32_16x16x32_bf16 v[102:105], a[208:211], a[88:91], v[102:105]\n"
        "s_mov_b64 s[44:45], s[40:41]\n"
        "ds_write_b128 v39, v[34:37] offset:1024\n"
        "v_cvt_pk_bf16_f32 v34, v112, v113\n"
        "v_mfma_f32_16x16x32_bf16 v[102:105], a[204:207], a[84:87], v[102:105]\n"
        "s_mov_b32 s46, 0\n"
        "v_cvt_pk_bf16_f32 v35, v114, v115\n"
        "v_cvt_pk_bf16_f32 v36, v122, v123\n"
        "v_cvt_pk_bf16_f32 v37, v124, v125\n"
        "v_mfma_f32_16x16x32_bf16 v[50:53], a[144:147], a[96:99], 0\n"
        "s_mov_b32 s47, s43\n"
        "ds_write_b128 v39, v[34:37] offset:2048\n"
        "v_mov_b32 v254, v255\n"
        "v_mfma_f32_16x16x32_bf16 v[50:53], a[148:151], a[100:103], v[50:53]\n"
        "s_add_u32 m0, s52, 0\n"
        "v_permlane32_swap_b32 v255, v254\n"
        "v_add_f32 v255, v255, v254\n"
        "v_mfma_f32_16x16x32_bf16 v[50:53], a[152:155], a[104:107], v[50:53]\n"
        "s_waitcnt lgkmcnt(3)\n"
        "buffer_load_dwordx4 v0, s[36:39], s49 offen lds\n"
        "v_mov_b32 v254, v255\n"
        "v_add_f32 v253, v46, v47\n"
        "v_mfma_f32_16x16x32_bf16 v[98:101], a[164:167], a[104:107], 0\n"
        "s_waitcnt lgkmcnt(3)\n"
        "v_permlane16_swap_b32 v255, v254\n"
        "v_add_f32 v255, v255, v254\n"
        "v_mfma_f32_16x16x32_bf16 v[98:101], a[160:163], a[100:103], v[98:101]\n"
        "s_waitcnt lgkmcnt(3)\n"
        "v_add_f32 v253, v253, v48\n"
        "v_mul_f32 v255, 0xbc2aaaab, v255\n"
        "v_add_f32 v253, v253, v49\n"
        "v_mfma_f32_16x16x32_bf16 v[98:101], a[156:159], a[96:99], v[98:101]\n"
        "v_add_f32 v54, v54, v255\n"
        "v_add_f32 v55, v55, v255\n"
        "v_add_f32 v56, v56, v255\n"
        "v_mfma_f32_16x16x32_bf16 v[110:113], a[168:171], a[96:99], 0\n"
        "v_add_f32 v57, v57, v255\n"
        "v_add_f32 v30, v30, v255\n"
        "v_add_f32 v31, v31, v255\n"
        "v_mfma_f32_16x16x32_bf16 v[110:113], a[172:175], a[100:103], v[110:113]\n"
        "v_add_f32 v32, v32, v255\n"
        "v_add_f32 v33, v33, v255\n"
        "v_add_f32 v74, v74, v255\n"
        "v_mfma_f32_16x16x32_bf16 v[110:113], a[176:179], a[104:107], v[110:113]\n"
        "buffer_load_dwordx4 v1, s[36:39], s49 offen offset:1024 lds\n"
        "v_add_f32 v75, v75, v255\n"
        "v_add_f32 v76, v76, v255\n"
        "v_mfma_f32_16x16x32_bf16 v[34:37], a[188:191], a[104:107], 0\n"
        "v_add_f32 v77, v77, v255\n"
        "v_add_f32 v78, v78, v255\n"
        "v_add_f32 v79, v79, v255\n"
        "v_mfma_f32_16x16x32_bf16 v[34:37], a[184:187], a[100:103], v[34:37]\n"
        "v_add_f32 v80, v80, v255\n"
        "v_add_f32 v81, v81, v255\n"
        "v_add_f32 v126, v126, v255\n"
        "v_mfma_f32_16x16x32_bf16 v[34:37], a[180:183], a[96:99], v[34:37]\n"
        "v_add_f32 v127, v127, v255\n"
        "v_add_f32 v128, v128, v255\n"
        "v_add_f32 v129, v129, v255\n"
        "v_mfma_f32_16x16x32_bf16 v[106:109], a[192:195], a[96:99], 0\n"
        "v_add_f32 v118, v118, v255\n"
        "v_add_f32 v119, v119, v255\n"
        "v_add_f32 v120, v120, v255\n"
        "v_mfma_f32_16x16x32_bf16 v[106:109], a[196:199], a[100:103], v[106:109]\n"
        "v_add_f32 v121, v121, v255\n"
        "v_cvt_pk_bf16_f32 v122, v54, v55\n"
        "v_cvt_pk_bf16_f32 v123, v56, v57\n"
        "v_mfma_f32_16x16x32_bf16 v[106:109], a[200:203], a[104:107], v[106:109]\n"
        "buffer_load_dwordx4 v2, s[36:39], s49 offen offset:2048 lds\n"
        "v_cvt_pk_bf16_f32 v124, v30, v31\n"
        "v_cvt_pk_bf16_f32 v125, v32, v33\n"
        "v_mfma_f32_16x16x32_bf16 v[114:117], a[212:215], a[104:107], 0\n"
        "ds_write_b128 v38, v[122:125]\n"
        "v_cvt_pk_bf16_f32 v30, v74, v75\n"
        "v_mfma_f32_16x16x32_bf16 v[114:117], a[208:211], a[100:103], v[114:117]\n"
        "v_cvt_pk_bf16_f32 v31, v76, v77\n"
        "v_cvt_pk_bf16_f32 v32, v78, v79\n"
        "v_cvt_pk_bf16_f32 v33, v80, v81\n"
        "v_mfma_f32_16x16x32_bf16 v[114:117], a[204:207], a[96:99], v[114:117]\n"
        "ds_write_b128 v38, v[30:33] offset:64\n"
        "v_cvt_pk_bf16_f32 v54, v126, v127\n"
        "v_mfma_f32_16x16x32_bf16 v[30:33], a[144:147], a[108:111], 0\n"
        "v_cvt_pk_bf16_f32 v55, v128, v129\n"
        "v_cvt_pk_bf16_f32 v56, v118, v119\n"
        "v_cvt_pk_bf16_f32 v57, v120, v121\n"
        "v_mfma_f32_16x16x32_bf16 v[30:33], a[148:151], a[112:115], v[30:33]\n"
        "ds_write_b128 v38, v[54:57] offset:128\n"
        "v_add_f32 v253, v253, v86\n"
        "v_mfma_f32_16x16x32_bf16 v[30:33], a[152:155], a[116:119], v[30:33]\n"
        "buffer_load_dwordx4 v3, s[36:39], s49 offen offset:3072 lds\n"
        "v_add_f32 v253, v253, v87\n"
        "v_add_f32 v253, v253, v88\n"
        "v_mfma_f32_16x16x32_bf16 v[54:57], a[164:167], a[116:119], 0\n"
        "v_add_f32 v253, v253, v89\n"
        "v_add_f32 v253, v253, v90\n"
        "v_add_f32 v253, v253, v91\n"
        "v_mfma_f32_16x16x32_bf16 v[54:57], a[160:163], a[112:115], v[54:57]\n"
        "v_add_f32 v253, v253, v92\n"
        "v_add_f32 v253, v253, v93\n"
        "v_add_f32 v253, v253, v82\n"
        "v_mfma_f32_16x16x32_bf16 v[54:57], a[156:159], a[108:111], v[54:57]\n"
        "v_add_f32 v253, v253, v83\n"
        "v_add_f32 v253, v253, v84\n"
        "v_add_f32 v253, v253, v85\n"
        "v_mfma_f32_16x16x32_bf16 v[118:121], a[168:171], a[108:111], 0\n"
        "v_add_f32 v253, v253, v94\n"
        "v_add_f32 v253, v253, v95\n"
        "v_add_f32 v253, v253, v96\n"
        "v_mfma_f32_16x16x32_bf16 v[118:121], a[172:175], a[112:115], v[118:121]\n"
        "s_add_u32 m0, s52, 4096\n"
        "v_add_f32 v253, v253, v97\n"
        "v_add_f32 v253, v253, v102\n"
        "v_add_f32 v253, v253, v103\n"
        "v_mfma_f32_16x16x32_bf16 v[118:121], a[176:179], a[116:119], v[118:121]\n"
        "buffer_load_dwordx4 v4, s[36:39], s49 offen lds\n"
        "v_add_f32 v253, v253, v104\n"
        "v_add_f32 v253, v253, v105\n"
        "v_mfma_f32_16x16x32_bf16 v[78:81], a[188:191], a[116:119], 0\n"
        "v_mov_b32 v254, v253\n"
        "v_add_f32 v255, v50, v51\n"
        "v_add_f32 v255, v255, v52\n"
        "v_mfma_f32_16x16x32_bf16 v[78:81], a[184:187], a[112:115], v[78:81]\n"
        "v_permlane32_swap_b32 v253, v254\n"
        "v_add_f32 v253, v253, v254\n"
        "v_mfma_f32_16x16x32_bf16 v[78:81], a[180:183], a[108:111], v[78:81]\n"
        "v_mov_b32 v254, v253\n"
        "v_add_f32 v255, v255, v53\n"
        "v_add_f32 v255, v255, v98\n"
        "v_mfma_f32_16x16x32_bf16 v[122:125], a[192:195], a[108:111], 0\n"
        "v_permlane16_swap_b32 v253, v254\n"
        "v_add_f32 v253, v253, v254\n"
        "v_mfma_f32_16x16x32_bf16 v[122:125], a[196:199], a[112:115], v[122:125]\n"
        "v_mul_f32 v253, 0xbc2aaaab, v253\n"
        "v_add_f32 v46, v46, v253\n"
        "v_add_f32 v47, v47, v253\n"
        "v_mfma_f32_16x16x32_bf16 v[122:125], a[200:203], a[116:119], v[122:125]\n"
        "buffer_load_dwordx4 v5, s[36:39], s49 offen offset:1024 lds\n"
        "v_add_f32 v48, v48, v253\n"
        "v_add_f32 v49, v49, v253\n"
        "v_mfma_f32_16x16x32_bf16 v[74:77], a[212:215], a[116:119], 0\n"
        "v_add_f32 v86, v86, v253\n"
        "v_add_f32 v87, v87, v253\n"
        "v_add_f32 v88, v88, v253\n"
        "v_mfma_f32_16x16x32_bf16 v[74:77], a[208:211], a[112:115], v[74:77]\n"
        "v_add_f32 v89, v89, v253\n"
        "v_add_f32 v90, v90, v253\n"
        "v_add_f32 v91, v91, v253\n"
        "v_mfma_f32_16x16x32_bf16 v[74:77], a[204:207], a[108:111], v[74:77]\n"
        "v_add_f32 v92, v92, v253\n"
        "v_add_f32 v93, v93, v253\n"
        "v_add_f32 v82, v82, v253\n"
        "v_mfma_f32_16x16x32_bf16 v[126:129], a[144:147], a[120:123], 0\n"
        "v_add_f32 v83, v83, v253\n"
        "v_add_f32 v84, v84, v253\n"
        "v_add_f32 v85, v85, v253\n"
        "v_mfma_f32_16x16x32_bf16 v[126:129], a[148:151], a[124:127], v[126:129]\n"
        "v_add_f32 v94, v94, v253\n"
        "v_add_f32 v95, v95, v253\n"
        "v_add_f32 v96, v96, v253\n"
        "v_mfma_f32_16x16x32_bf16 v[126:129], a[152:155], a[128:131], v[126:129]\n"
        "buffer_load_dwordx4 v6, s[36:39], s49 offen offset:2048 lds\n"
        "v_add_f32 v97, v97, v253\n"
        "v_add_f32 v102, v102, v253\n"
        "v_mfma_f32_16x16x32_bf16 v[130:133], a[164:167], a[128:131], 0\n"
        "v_add_f32 v103, v103, v253\n"
        "v_add_f32 v104, v104, v253\n"
        "v_add_f32 v105, v105, v253\n"
        "v_mfma_f32_16x16x32_bf16 v[130:133], a[160:163], a[124:127], v[130:133]\n"
        "v_cvt_pk_bf16_f32 v134, v46, v47\n"
        "v_cvt_pk_bf16_f32 v135, v48, v49\n"
        "v_cvt_pk_bf16_f32 v136, v86, v87\n"
        "v_mfma_f32_16x16x32_bf16 v[130:133], a[156:159], a[120:123], v[130:133]\n"
        "v_cvt_pk_bf16_f32 v137, v88, v89\n"
        "v_cvt_pk_bf16_f32 v86, v90, v91\n"
        "v_cvt_pk_bf16_f32 v87, v92, v93\n"
        "v_mfma_f32_16x16x32_bf16 v[46:49], a[168:171], a[120:123], 0\n"
        "ds_write_b128 v40, v[134:137]\n"
        "v_cvt_pk_bf16_f32 v88, v82, v83\n"
        "v_mfma_f32_16x16x32_bf16 v[46:49], a[172:175], a[124:127], v[46:49]\n"
        "v_cvt_pk_bf16_f32 v89, v84, v85\n"
        "v_cvt_pk_bf16_f32 v90, v94, v95\n"
        "v_cvt_pk_bf16_f32 v91, v96, v97\n"
        "v_mfma_f32_16x16x32_bf16 v[46:49], a[176:179], a[128:131], v[46:49]\n"
        "ds_write_b128 v40, v[86:89] offset:1024\n"
        "v_cvt_pk_bf16_f32 v92, v102, v103\n"
        "v_mfma_f32_16x16x32_bf16 v[82:85], a[188:191], a[128:131], 0\n"
        "buffer_load_dwordx4 v7, s[36:39], s49 offen offset:3072 lds\n"
        "v_cvt_pk_bf16_f32 v93, v104, v105\n"
        "v_add_f32 v255, v255, v99\n"
        "v_mfma_f32_16x16x32_bf16 v[82:85], a[184:187], a[124:127], v[82:85]\n"
        "ds_write_b128 v40, v[90:93] offset:2048\n"
        "v_add_f32 v255, v255, v100\n"
        "v_mfma_f32_16x16x32_bf16 v[82:85], a[180:183], a[120:123], v[82:85]\n"
        "v_add_f32 v255, v255, v101\n"
        "v_add_f32 v255, v255, v110\n"
        "v_add_f32 v255, v255, v111\n"
        "v_mfma_f32_16x16x32_bf16 v[86:89], a[192:195], a[120:123], 0\n"
        "v_add_f32 v255, v255, v112\n"
        "v_add_f32 v255, v255, v113\n"
        "v_add_f32 v255, v255, v34\n"
        "v_mfma_f32_16x16x32_bf16 v[86:89], a[196:199], a[124:127], v[86:89]\n"
        "s_add_u32 m0, s52, 8192\n"
        "v_add_f32 v255, v255, v35\n"
        "v_add_f32 v255, v255, v36\n"
        "v_add_f32 v255, v255, v37\n"
        "v_mfma_f32_16x16x32_bf16 v[86:89], a[200:203], a[128:131], v[86:89]\n"
        "buffer_load_dwordx4 v8, s[36:39], s49 offen lds\n"
        "v_add_f32 v255, v255, v106\n"
        "v_add_f32 v255, v255, v107\n"
        "v_mfma_f32_16x16x32_bf16 v[94:97], a[212:215], a[128:131], 0\n"
        "v_add_f32 v255, v255, v108\n"
        "v_add_f32 v255, v255, v109\n"
        "v_add_f32 v255, v255, v114\n"
        "v_mfma_f32_16x16x32_bf16 v[94:97], a[208:211], a[124:127], v[94:97]\n"
        "v_add_f32 v255, v255, v115\n"
        "v_add_f32 v255, v255, v116\n"
        "v_add_f32 v255, v255, v117\n"
        "v_mfma_f32_16x16x32_bf16 v[94:97], a[204:207], a[120:123], v[94:97]\n"
        "v_mov_b32 v254, v255\n"
        "v_add_f32 v38, v30, v31\n"
        "v_add_f32 v38, v38, v32\n"
        "v_mfma_f32_16x16x32_bf16 v[90:93], a[144:147], a[132:135], 0\n"
        "v_permlane32_swap_b32 v255, v254\n"
        "v_add_f32 v255, v255, v254\n"
        "v_mfma_f32_16x16x32_bf16 v[90:93], a[148:151], a[136:139], v[90:93]\n"
        "v_mov_b32 v254, v255\n"
        "v_add_f32 v38, v38, v33\n"
        "v_add_f32 v38, v38, v54\n"
        "v_mfma_f32_16x16x32_bf16 v[90:93], a[152:155], a[140:143], v[90:93]\n"
        "v_permlane16_swap_b32 v255, v254\n"
        "v_add_f32 v255, v255, v254\n"
        "v_mfma_f32_16x16x32_bf16 v[102:105], a[164:167], a[140:143], 0\n"
        "v_mul_f32 v255, 0xbc2aaaab, v255\n"
        "v_add_f32 v50, v50, v255\n"
        "v_add_f32 v51, v51, v255\n"
        "v_mfma_f32_16x16x32_bf16 v[102:105], a[160:163], a[136:139], v[102:105]\n"
        "v_add_f32 v52, v52, v255\n"
        "v_add_f32 v53, v53, v255\n"
        "v_add_f32 v98, v98, v255\n"
        "v_mfma_f32_16x16x32_bf16 v[102:105], a[156:159], a[132:135], v[102:105]\n"
        "v_add_f32 v99, v99, v255\n"
        "v_add_f32 v100, v100, v255\n"
        "v_add_f32 v101, v101, v255\n"
        "v_mfma_f32_16x16x32_bf16 v[134:137], a[168:171], a[132:135], 0\n"
        "v_add_f32 v110, v110, v255\n"
        "v_add_f32 v111, v111, v255\n"
        "v_add_f32 v112, v112, v255\n"
        "v_mfma_f32_16x16x32_bf16 v[134:137], a[172:175], a[136:139], v[134:137]\n"
        "v_add_f32 v113, v113, v255\n"
        "v_add_f32 v34, v34, v255\n"
        "v_add_f32 v35, v35, v255\n"
        "v_mfma_f32_16x16x32_bf16 v[134:137], a[176:179], a[140:143], v[134:137]\n"
        "v_add_f32 v36, v36, v255\n"
        "v_add_f32 v37, v37, v255\n"
        "v_add_f32 v106, v106, v255\n"
        "v_mfma_f32_16x16x32_bf16 v[142:145], a[188:191], a[140:143], 0\n"
        "v_add_f32 v107, v107, v255\n"
        "v_add_f32 v108, v108, v255\n"
        "v_add_f32 v109, v109, v255\n"
        "v_mfma_f32_16x16x32_bf16 v[142:145], a[184:187], a[136:139], v[142:145]\n"
        "v_add_f32 v114, v114, v255\n"
        "v_add_f32 v115, v115, v255\n"
        "v_add_f32 v116, v116, v255\n"
        "v_mfma_f32_16x16x32_bf16 v[142:145], a[180:183], a[132:135], v[142:145]\n"
        "v_add_f32 v117, v117, v255\n"
        "v_cvt_pk_bf16_f32 v138, v50, v51\n"
        "v_cvt_pk_bf16_f32 v139, v52, v53\n"
        "v_mfma_f32_16x16x32_bf16 v[50:53], a[192:195], a[132:135], 0\n"
        "v_cvt_pk_bf16_f32 v140, v98, v99\n"
        "v_cvt_pk_bf16_f32 v141, v100, v101\n"
        "v_cvt_pk_bf16_f32 v98, v110, v111\n"
        "v_mfma_f32_16x16x32_bf16 v[50:53], a[196:199], a[136:139], v[50:53]\n"
        "ds_write_b128 v41, v[138:141]\n"
        "v_cvt_pk_bf16_f32 v99, v112, v113\n"
        "v_mfma_f32_16x16x32_bf16 v[50:53], a[200:203], a[140:143], v[50:53]\n"
        "v_cvt_pk_bf16_f32 v100, v34, v35\n"
        "v_cvt_pk_bf16_f32 v101, v36, v37\n"
        "v_cvt_pk_bf16_f32 v34, v106, v107\n"
        "v_mfma_f32_16x16x32_bf16 v[110:113], a[212:215], a[140:143], 0\n"
        "ds_write_b128 v41, v[98:101] offset:1024\n"
        "v_cvt_pk_bf16_f32 v35, v108, v109\n"
        "v_mfma_f32_16x16x32_bf16 v[110:113], a[208:211], a[136:139], v[110:113]\n"
        "v_cvt_pk_bf16_f32 v36, v114, v115\n"
        "v_cvt_pk_bf16_f32 v37, v116, v117\n"
        "v_add_f32 v38, v38, v55\n"
        "v_mfma_f32_16x16x32_bf16 v[110:113], a[204:207], a[132:135], v[110:113]\n"
        "ds_write_b128 v41, v[34:37] offset:2048\n"
        "v_add_f32 v38, v38, v56\n"
        "v_add_f32 v38, v38, v57\n"
        "v_add_f32 v38, v38, v118\n"
        "v_add_f32 v38, v38, v119\n"
        "v_add_f32 v38, v38, v120\n"
        "v_add_f32 v38, v38, v121\n"
        "v_add_f32 v38, v38, v78\n"
        "v_add_f32 v38, v38, v79\n"
        "v_add_f32 v38, v38, v80\n"
        "v_add_f32 v38, v38, v81\n"
        "v_add_f32 v38, v38, v122\n"
        "v_add_f32 v38, v38, v123\n"
        "v_add_f32 v38, v38, v124\n"
        "v_add_f32 v38, v38, v125\n"
        "v_add_f32 v38, v38, v74\n"
        "v_add_f32 v38, v38, v75\n"
        "v_add_f32 v38, v38, v76\n"
        "v_add_f32 v38, v38, v77\n"
        "v_mov_b32 v39, v38\n"
        "v_add_f32 v34, v126, v127\n"
        "v_add_f32 v34, v34, v128\n"
        "v_permlane32_swap_b32 v38, v39\n"
        "v_add_f32 v38, v38, v39\n"
        "v_mov_b32 v39, v38\n"
        "v_add_f32 v34, v34, v129\n"
        "v_add_f32 v34, v34, v130\n"
        "v_permlane16_swap_b32 v38, v39\n"
        "v_add_f32 v38, v38, v39\n"
        "v_mul_f32 v38, 0xbc2aaaab, v38\n"
        "v_add_f32 v34, v34, v131\n"
        "v_add_f32 v34, v34, v132\n"
        "v_add_f32 v34, v34, v133\n"
        "v_add_f32 v34, v34, v46\n"
        "v_add_f32 v34, v34, v47\n"
        "v_add_f32 v34, v34, v48\n"
        "v_add_f32 v34, v34, v49\n"
        "v_add_f32 v34, v34, v82\n"
        "v_add_f32 v34, v34, v83\n"
        "v_add_f32 v34, v34, v84\n"
        "v_add_f32 v34, v34, v85\n"
        "v_add_f32 v34, v34, v86\n"
        "v_add_f32 v34, v34, v87\n"
        "v_add_f32 v34, v34, v88\n"
        "v_add_f32 v34, v34, v89\n"
        "v_add_f32 v34, v34, v94\n"
        "v_add_f32 v34, v34, v95\n"
        "v_add_f32 v34, v34, v96\n"
        "v_add_f32 v34, v34, v97\n"
        "v_mov_b32 v35, v34\n"
        "v_add_f32 v62, v90, v91\n"
        "v_add_f32 v62, v62, v92\n"
        "v_permlane32_swap_b32 v34, v35\n"
        "v_add_f32 v34, v34, v35\n"
        "v_mov_b32 v35, v34\n"
        "v_add_f32 v62, v62, v93\n"
        "v_add_f32 v62, v62, v102\n"
        "v_permlane16_swap_b32 v34, v35\n"
        "v_add_f32 v34, v34, v35\n"
        "v_mul_f32 v34, 0xbc2aaaab, v34\n"
        "v_add_f32 v62, v62, v103\n"
        "v_add_f32 v62, v62, v104\n"
        "v_add_f32 v62, v62, v105\n"
        "v_add_f32 v62, v62, v134\n"
        "v_add_f32 v62, v62, v135\n"
        "v_add_f32 v62, v62, v136\n"
        "v_add_f32 v62, v62, v137\n"
        "v_add_f32 v62, v62, v142\n"
        "v_add_f32 v62, v62, v143\n"
        "v_add_f32 v62, v62, v144\n"
        "v_add_f32 v62, v62, v145\n"
        "v_add_f32 v62, v62, v50\n"
        "v_add_f32 v62, v62, v51\n"
        "v_add_f32 v62, v62, v52\n"
        "v_add_f32 v62, v62, v53\n"
        "v_pk_add_f32 v[30:31], v[30:31], v[38:39] op_sel_hi:[1,0]\n"
        "v_pk_add_f32 v[32:33], v[32:33], v[38:39] op_sel_hi:[1,0]\n"
        "v_pk_add_f32 v[54:55], v[54:55], v[38:39] op_sel_hi:[1,0]\n"
        "v_pk_add_f32 v[56:57], v[56:57], v[38:39] op_sel_hi:[1,0]\n"
        "v_pk_add_f32 v[118:119], v[118:119], v[38:39] op_sel_hi:[1,0]\n"
        "v_pk_add_f32 v[120:121], v[120:121], v[38:39] op_sel_hi:[1,0]\n"
        "v_pk_add_f32 v[78:79], v[78:79], v[38:39] op_sel_hi:[1,0]\n"
        "v_pk_add_f32 v[80:81], v[80:81], v[38:39] op_sel_hi:[1,0]\n"
        "v_pk_add_f32 v[122:123], v[122:123], v[38:39] op_sel_hi:[1,0]\n"
        "v_pk_add_f32 v[124:125], v[124:125], v[38:39] op_sel_hi:[1,0]\n"
        "v_pk_add_f32 v[74:75], v[74:75], v[38:39] op_sel_hi:[1,0]\n"
        "v_pk_add_f32 v[76:77], v[76:77], v[38:39] op_sel_hi:[1,0]\n"
        "v_pk_add_f32 v[126:127], v[126:127], v[34:35] op_sel_hi:[1,0]\n"
        "v_pk_add_f32 v[128:129], v[128:129], v[34:35] op_sel_hi:[1,0]\n"
        "v_pk_add_f32 v[130:131], v[130:131], v[34:35] op_sel_hi:[1,0]\n"
        "v_pk_add_f32 v[132:133], v[132:133], v[34:35] op_sel_hi:[1,0]\n"
        "v_pk_add_f32 v[46:47], v[46:47], v[34:35] op_sel_hi:[1,0]\n"
        "v_pk_add_f32 v[48:49], v[48:49], v[34:35] op_sel_hi:[1,0]\n"
        "v_pk_add_f32 v[82:83], v[82:83], v[34:35] op_sel_hi:[1,0]\n"
        "v_pk_add_f32 v[84:85], v[84:85], v[34:35] op_sel_hi:[1,0]\n"
        "v_pk_add_f32 v[86:87], v[86:87], v[34:35] op_sel_hi:[1,0]\n"
        "v_pk_add_f32 v[88:89], v[88:89], v[34:35] op_sel_hi:[1,0]\n"
        "v_pk_add_f32 v[94:95], v[94:95], v[34:35] op_sel_hi:[1,0]\n"
        "v_pk_add_f32 v[96:97], v[96:97], v[34:35] op_sel_hi:[1,0]\n"
        "v_cvt_pk_bf16_f32 v34, v30, v31\n"
        "v_cvt_pk_bf16_f32 v35, v32, v33\n"
        "v_cvt_pk_bf16_f32 v36, v54, v55\n"
        "v_cvt_pk_bf16_f32 v37, v56, v57\n"
        "v_cvt_pk_bf16_f32 v54, v118, v119\n"
        "v_cvt_pk_bf16_f32 v55, v120, v121\n"
        "v_cvt_pk_bf16_f32 v56, v78, v79\n"
        "v_cvt_pk_bf16_f32 v57, v80, v81\n"
        "v_cvt_pk_bf16_f32 v38, v122, v123\n"
        "v_cvt_pk_bf16_f32 v39, v124, v125\n"
        "v_cvt_pk_bf16_f32 v40, v74, v75\n"
        "v_cvt_pk_bf16_f32 v41, v76, v77\n"
        "v_cvt_pk_bf16_f32 v74, v126, v127\n"
        "v_cvt_pk_bf16_f32 v75, v128, v129\n"
        "v_cvt_pk_bf16_f32 v76, v130, v131\n"
        "v_cvt_pk_bf16_f32 v77, v132, v133\n"
        "v_cvt_pk_bf16_f32 v78, v46, v47\n"
        "v_cvt_pk_bf16_f32 v79, v48, v49\n"
        "v_cvt_pk_bf16_f32 v80, v82, v83\n"
        "v_cvt_pk_bf16_f32 v81, v84, v85\n"
        "v_cvt_pk_bf16_f32 v30, v86, v87\n"
        "v_cvt_pk_bf16_f32 v31, v88, v89\n"
        "v_cvt_pk_bf16_f32 v32, v94, v95\n"
        "v_cvt_pk_bf16_f32 v33, v96, v97\n"
        "v_add_f32 v62, v62, v110\n"
        "ds_write_b128 v42, v[34:37]\n"
        "ds_write_b128 v42, v[54:57] offset:1024\n"
        "ds_write_b128 v42, v[38:41] offset:2048\n"
        "ds_write_b128 v43, v[74:77]\n"
        "ds_write_b128 v43, v[78:81] offset:1024\n"
        "ds_write_b128 v43, v[30:33] offset:2048\n"
        "v_add_f32 v62, v62, v111\n"
        "v_add_f32 v62, v62, v112\n"
        "v_add_f32 v62, v62, v113\n"
        "v_mov_b32 v63, v62\n"
        "s_waitcnt vmcnt(9)\n"
        "s_nop 0\n"
        "v_permlane32_swap_b32 v62, v63\n"
        "v_add_f32 v62, v62, v63\n"
        "v_mov_b32 v63, v62\n"
        "s_nop 1\n"
        "v_permlane16_swap_b32 v62, v63\n"
        "v_add_f32 v62, v62, v63\n"
        "v_mul_f32 v62, 0xbc2aaaab, v62\n"
        "v_pk_add_f32 v[90:91], v[90:91], v[62:63] op_sel_hi:[1,0]\n"
        "v_pk_add_f32 v[92:93], v[92:93], v[62:63] op_sel_hi:[1,0]\n"
        "v_pk_add_f32 v[102:103], v[102:103], v[62:63] op_sel_hi:[1,0]\n"
        "v_pk_add_f32 v[104:105], v[104:105], v[62:63] op_sel_hi:[1,0]\n"
        "v_pk_add_f32 v[134:135], v[134:135], v[62:63] op_sel_hi:[1,0]\n"
        "v_pk_add_f32 v[136:137], v[136:137], v[62:63] op_sel_hi:[1,0]\n"
        "v_pk_add_f32 v[142:143], v[142:143], v[62:63] op_sel_hi:[1,0]\n"
        "v_pk_add_f32 v[144:145], v[144:145], v[62:63] op_sel_hi:[1,0]\n"
        "v_pk_add_f32 v[50:51], v[50:51], v[62:63] op_sel_hi:[1,0]\n"
        "v_pk_add_f32 v[52:53], v[52:53], v[62:63] op_sel_hi:[1,0]\n"
        "v_pk_add_f32 v[110:111], v[110:111], v[62:63] op_sel_hi:[1,0]\n"
        "v_pk_add_f32 v[112:113], v[112:113], v[62:63] op_sel_hi:[1,0]\n"
        "v_cvt_pk_bf16_f32 v34, v90, v91\n"
        "v_cvt_pk_bf16_f32 v35, v92, v93\n"
        "v_cvt_pk_bf16_f32 v36, v102, v103\n"
        "v_cvt_pk_bf16_f32 v37, v104, v105\n"
        "v_cvt_pk_bf16_f32 v38, v134, v135\n"
        "v_cvt_pk_bf16_f32 v39, v136, v137\n"
        "v_cvt_pk_bf16_f32 v40, v142, v143\n"
        "v_cvt_pk_bf16_f32 v41, v144, v145\n"
        "v_cvt_pk_bf16_f32 v30, v50, v51\n"
        "v_cvt_pk_bf16_f32 v31, v52, v53\n"
        "v_cvt_pk_bf16_f32 v32, v110, v111\n"
        "v_cvt_pk_bf16_f32 v33, v112, v113\n"
        "ds_write_b128 v44, v[34:37]\n"
        "ds_write_b128 v44, v[38:41] offset:1024\n"
        "ds_write_b128 v44, v[30:33] offset:2048\n"
        "s_waitcnt lgkmcnt(0)\n"
        "s_barrier\n"
        "s_branch FE_%=\n"
        "FG_%=:\n"
        "s_mov_b32 s24, 0x471c4000\n"
        "v_mov_b32 v255, 0x3f803f80\n"
        "v_mov_b32 v255, 0x3f803f80\n"
        "v_mov_b32 v255, 0x3f803f80\n"
        "v_mov_b32 v255, 0x3f803f80\n"
        "s_waitcnt vmcnt(15)\n"
        "ds_read_b64_tr_b16 a[72:73], v34 offset:0\n"
        "ds_read_b64_tr_b16 a[74:75], v35 offset:0\n"
        "ds_read_b64_tr_b16 a[76:77], v34 offset:6144\n"
        "ds_read_b64_tr_b16 a[78:79], v35 offset:6144\n"
        "ds_read_b64_tr_b16 a[80:81], v34 offset:12288\n"
        "ds_read_b64_tr_b16 a[82:83], v35 offset:12288\n"
        "s_add_u32 m0, s53, 15360\n"
        "s_nop 0\n"
        "buffer_load_dwordx4 v46, s[16:19], s23 offen lds\n"
        "ds_read_b64_tr_b16 a[84:85], v34 offset:8\n"
        "ds_read_b64_tr_b16 a[86:87], v35 offset:8\n"
        "ds_read_b64_tr_b16 a[88:89], v34 offset:6152\n"
        "ds_read_b64_tr_b16 a[90:91], v35 offset:6152\n"
        "ds_read_b64_tr_b16 a[92:93], v34 offset:12296\n"
        "ds_read_b64_tr_b16 a[94:95], v35 offset:12296\n"
        "buffer_load_dwordx4 v47, s[16:19], s23 offen offset:1024 lds\n"
        "ds_read_b64_tr_b16 a[96:97], v34 offset:64\n"
        "ds_read_b64_tr_b16 a[98:99], v35 offset:64\n"
        "ds_read_b64_tr_b16 a[100:101], v34 offset:6208\n"
        "ds_read_b64_tr_b16 a[102:103], v35 offset:6208\n"
        "ds_read_b64_tr_b16 a[104:105], v34 offset:12352\n"
        "ds_read_b64_tr_b16 a[106:107], v35 offset:12352\n"
        "buffer_load_dwordx4 v48, s[16:19], s23 offen offset:2048 lds\n"
        "ds_read_b64_tr_b16 a[108:109], v34 offset:72\n"
        "ds_read_b64_tr_b16 a[110:111], v35 offset:72\n"
        "ds_read_b64_tr_b16 a[112:113], v34 offset:6216\n"
        "ds_read_b64_tr_b16 a[114:115], v35 offset:6216\n"
        "ds_read_b64_tr_b16 a[116:117], v34 offset:12360\n"
        "ds_read_b64_tr_b16 a[118:119], v35 offset:12360\n"
        "ds_read_b64_tr_b16 a[120:121], v34 offset:128\n"
        "ds_read_b64_tr_b16 a[122:123], v35 offset:128\n"
        "ds_read_b64_tr_b16 a[124:125], v34 offset:6272\n"
        "ds_read_b64_tr_b16 a[126:127], v35 offset:6272\n"
        "ds_read_b64_tr_b16 a[128:129], v34 offset:12416\n"
        "ds_read_b64_tr_b16 a[130:131], v35 offset:12416\n"
        "ds_read_b64_tr_b16 a[132:133], v34 offset:136\n"
        "ds_read_b64_tr_b16 a[134:135], v35 offset:136\n"
        "ds_read_b64_tr_b16 a[136:137], v34 offset:6280\n"
        "ds_read_b64_tr_b16 a[138:139], v35 offset:6280\n"
        "ds_read_b64_tr_b16 a[140:141], v34 offset:12424\n"
        "ds_read_b64_tr_b16 a[142:143], v35 offset:12424\n"
        "s_waitcnt vmcnt(12)\n"
        "ds_read_b128 a[144:147], v30\n"
        "ds_read_b128 a[148:151], v30 offset:64\n"
        "ds_read_b128 a[152:155], v30 offset:128\n"
        "ds_read_b128 a[156:159], v31\n"
        "ds_read_b128 a[160:163], v31 offset:64\n"
        "ds_read_b128 a[164:167], v31 offset:128\n"
        "s_waitcnt lgkmcnt(15)\n"
        "s_waitcnt lgkmcnt(3)\n"
        "v_mfma_f32_16x16x32_bf16 v[50:53], a[144:147], a[72:75], 0\n"
        "s_waitcnt vmcnt(6)\n"
        "ds_read_b128 a[168:171], v30 offset:6144\n"
        "v_mov_b32 v255, 161888\n"
        "v_mfma_f32_16x16x32_bf16 v[50:53], a[148:151], a[76:79], v[50:53]\n"
        "s_waitcnt vmcnt(0)\n"
        "ds_read_b128 a[172:175], v30 offset:6208\n"
        "ds_read_b128 a[176:179], v30 offset:6272\n"
        "v_mfma_f32_16x16x32_bf16 v[50:53], a[152:155], a[80:83], v[50:53]\n"
        "s_waitcnt lgkmcnt(6)\n"
        "ds_read_b128 a[180:183], v31 offset:6144\n"
        "ds_read_b128 a[184:187], v31 offset:6208\n"
        "s_waitcnt lgkmcnt(5)\n"
        "v_mfma_f32_16x16x32_bf16 v[58:61], a[164:167], a[80:83], 0\n"
        "s_waitcnt lgkmcnt(5)\n"
        "ds_read_b128 a[188:191], v31 offset:6272\n"
        "ds_read_b128 a[192:195], v30 offset:12288\n"
        "v_add_f32 v254, v50, v51\n"
        "v_mfma_f32_16x16x32_bf16 v[58:61], a[160:163], a[76:79], v[58:61]\n"
        "s_waitcnt lgkmcnt(7)\n"
        "ds_read_b128 a[196:199], v30 offset:12352\n"
        "ds_read_b128 a[200:203], v30 offset:12416\n"
        "v_add_f32 v254, v254, v52\n"
        "v_mfma_f32_16x16x32_bf16 v[58:61], a[156:159], a[72:75], v[58:61]\n"
        "s_waitcnt lgkmcnt(9)\n"
        "ds_read_b128 a[204:207], v31 offset:12288\n"
        "v_add_f32 v254, v254, v53\n"
        "s_waitcnt lgkmcnt(7)\n"
        "v_mfma_f32_16x16x32_bf16 v[66:69], a[168:171], a[72:75], 0\n"
        "s_waitcnt lgkmcnt(7)\n"
        "ds_read_b128 a[208:211], v31 offset:12352\n"
        "ds_read_b128 a[212:215], v31 offset:12416\n"
        "v_add_f32 v254, v254, v58\n"
        "v_mfma_f32_16x16x32_bf16 v[66:69], a[172:175], a[76:79], v[66:69]\n"
        "s_waitcnt lgkmcnt(9)\n"
        "v_add_f32 v254, v254, v59\n"
        "v_add_f32 v254, v254, v60\n"
        "v_add_f32 v254, v254, v61\n"
        "v_mfma_f32_16x16x32_bf16 v[66:69], a[176:179], a[80:83], v[66:69]\n"
        "s_waitcnt lgkmcnt(6)\n"
        "s_nop 0\n"
        "v_mfma_f32_16x16x32_bf16 v[62:65], a[188:191], a[80:83], 0\n"
        "s_waitcnt lgkmcnt(3)\n"
        "s_nop 1\n"
        "v_mfma_f32_16x16x32_bf16 v[62:65], a[184:187], a[76:79], v[62:65]\n"
        "s_nop 0\n"
        "v_add_f32 v254, v254, v66\n"
        "v_add_f32 v254, v254, v67\n"
        "v_add_f32 v254, v254, v68\n"
        "v_mfma_f32_16x16x32_bf16 v[62:65], a[180:183], a[72:75], v[62:65]\n"
        "s_nop 0\n"
        "v_add_f32 v254, v254, v69\n"
        "v_mfma_f32_16x16x32_bf16 v[82:85], a[192:195], a[72:75], 0\n"
        "s_nop 2\n"
        "v_mfma_f32_16x16x32_bf16 v[82:85], a[196:199], a[76:79], v[82:85]\n"
        "s_nop 0\n"
        "v_add_f32 v254, v254, v62\n"
        "v_add_f32 v254, v254, v63\n"
        "v_add_f32 v254, v254, v64\n"
        "v_mfma_f32_16x16x32_bf16 v[82:85], a[200:203], a[80:83], v[82:85]\n"
        "s_nop 0\n"
        "v_add_f32 v254, v254, v65\n"
        "s_waitcnt lgkmcnt(0)\n"
        "v_mfma_f32_16x16x32_bf16 v[70:73], a[212:215], a[80:83], 0\n"
        "s_nop 1\n"
        "v_mfma_f32_16x16x32_bf16 v[70:73], a[208:211], a[76:79], v[70:73]\n"
        "s_nop 0\n"
        "v_add_f32 v254, v254, v82\n"
        "v_add_f32 v254, v254, v83\n"
        "v_add_f32 v254, v254, v84\n"
        "v_mfma_f32_16x16x32_bf16 v[70:73], a[204:207], a[72:75], v[70:73]\n"
        "s_nop 0\n"
        "v_add_f32 v254, v254, v85\n"
        "v_mfma_f32_16x16x32_bf16 v[30:33], a[144:147], a[84:87], 0\n"
        "s_nop 2\n"
        "v_mfma_f32_16x16x32_bf16 v[30:33], a[148:151], a[88:91], v[30:33]\n"
        "s_nop 0\n"
        "v_add_f32 v254, v254, v70\n"
        "v_add_f32 v254, v254, v71\n"
        "v_add_f32 v254, v254, v72\n"
        "v_mfma_f32_16x16x32_bf16 v[30:33], a[152:155], a[92:95], v[30:33]\n"
        "buffer_load_dwordx4 v[196:199], v36, s[36:39], 0 offen\n"
        "v_add_f32 v254, v254, v73\n"
        "v_mov_b32 v253, v254\n"
        "v_mfma_f32_16x16x32_bf16 v[46:49], a[164:167], a[92:95], 0\n"
        "s_nop 0\n"
        "v_permlane32_swap_b32 v254, v253\n"
        "v_add_f32 v254, v254, v253\n"
        "v_mfma_f32_16x16x32_bf16 v[46:49], a[160:163], a[88:91], v[46:49]\n"
        "v_mov_b32 v253, v254\n"
        "v_add_f32 v252, v30, v31\n"
        "v_add_f32 v252, v252, v32\n"
        "v_mfma_f32_16x16x32_bf16 v[46:49], a[156:159], a[84:87], v[46:49]\n"
        "v_permlane16_swap_b32 v254, v253\n"
        "v_add_f32 v254, v254, v253\n"
        "v_mfma_f32_16x16x32_bf16 v[108:111], a[168:171], a[84:87], 0\n"
        "v_mul_f32 v254, 0xbc2aaaab, v254\n"
        "v_add_f32 v252, v252, v33\n"
        "v_add_f32 v50, v50, v254\n"
        "v_mfma_f32_16x16x32_bf16 v[108:111], a[172:175], a[88:91], v[108:111]\n"
        "v_add_f32 v51, v51, v254\n"
        "v_add_f32 v52, v52, v254\n"
        "v_add_f32 v53, v53, v254\n"
        "v_mfma_f32_16x16x32_bf16 v[108:111], a[176:179], a[92:95], v[108:111]\n"
        "v_add_f32 v58, v58, v254\n"
        "v_add_f32 v59, v59, v254\n"
        "v_add_f32 v60, v60, v254\n"
        "v_mfma_f32_16x16x32_bf16 v[118:121], a[188:191], a[92:95], 0\n"
        "v_add_f32 v61, v61, v254\n"
        "v_add_f32 v66, v66, v254\n"
        "v_add_f32 v67, v67, v254\n"
        "v_mfma_f32_16x16x32_bf16 v[118:121], a[184:187], a[88:91], v[118:121]\n"
        "v_add_f32 v68, v68, v254\n"
        "v_add_f32 v69, v69, v254\n"
        "v_add_f32 v62, v62, v254\n"
        "v_mfma_f32_16x16x32_bf16 v[118:121], a[180:183], a[84:87], v[118:121]\n"
        "buffer_load_dwordx4 v[202:205], v36, s[36:39], 0 offen offset:16\n"
        "v_add_f32 v63, v63, v254\n"
        "v_add_f32 v64, v64, v254\n"
        "v_mfma_f32_16x16x32_bf16 v[122:125], a[192:195], a[84:87], 0\n"
        "s_mov_b32 s78, 0xf0000f\n"
        "v_add_f32 v65, v65, v254\n"
        "v_add_f32 v82, v82, v254\n"
        "v_add_f32 v83, v83, v254\n"
        "v_mfma_f32_16x16x32_bf16 v[122:125], a[196:199], a[88:91], v[122:125]\n"
        "s_mov_b32 s79, 0xf0000f00\n"
        "v_add_f32 v84, v84, v254\n"
        "v_add_f32 v85, v85, v254\n"
        "v_add_f32 v70, v70, v254\n"
        "v_mfma_f32_16x16x32_bf16 v[122:125], a[200:203], a[92:95], v[122:125]\n"
        "s_mov_b32 s76, 0x11111111\n"
        "v_add_f32 v71, v71, v254\n"
        "v_add_f32 v72, v72, v254\n"
        "v_add_f32 v73, v73, v254\n"
        "v_mfma_f32_16x16x32_bf16 v[126:129], a[212:215], a[92:95], 0\n"
        "s_mov_b32 s77, 0x11111111\n"
        "v_cvt_pk_bf16_f32 v54, v50, v51\n"
        "v_cvt_pk_bf16_f32 v55, v52, v53\n"
        "v_cvt_pk_bf16_f32 v56, v58, v59\n"
        "v_mfma_f32_16x16x32_bf16 v[126:129], a[208:211], a[88:91], v[126:129]\n"
        "s_mov_b32 s56, 0x22222222\n"
        "v_cvt_pk_bf16_f32 v57, v60, v61\n"
        "v_cvt_pk_bf16_f32 v78, v66, v67\n"
        "v_cvt_pk_bf16_f32 v79, v68, v69\n"
        "v_mfma_f32_16x16x32_bf16 v[126:129], a[204:207], a[84:87], v[126:129]\n"
        "s_mov_b32 s57, 0x22222222\n"
        "v_cvt_pk_bf16_f32 v80, v62, v63\n"
        "v_cvt_pk_bf16_f32 v81, v64, v65\n"
        "v_cvt_pk_bf16_f32 v50, v82, v83\n"
        "v_mfma_f32_16x16x32_bf16 v[74:77], a[144:147], a[96:99], 0\n"
        "s_mov_b32 s58, 0x44444444\n"
        "v_cvt_pk_bf16_f32 v51, v84, v85\n"
        "v_cvt_pk_bf16_f32 v52, v70, v71\n"
        "v_cvt_pk_bf16_f32 v53, v72, v73\n"
        "v_mfma_f32_16x16x32_bf16 v[74:77], a[148:151], a[100:103], v[74:77]\n"
        "s_mov_b32 s59, 0x44444444\n"
        "v_add_f32 v252, v252, v46\n"
        "v_add_f32 v252, v252, v47\n"
        "v_add_f32 v252, v252, v48\n"
        "v_mfma_f32_16x16x32_bf16 v[74:77], a[152:155], a[104:107], v[74:77]\n"
        "s_mov_b32 s60, 0x88888888\n"
        "buffer_load_dwordx4 v[208:211], v36, s[36:39], 0 offen offset:32\n"
        "v_add_f32 v252, v252, v49\n"
        "v_add_f32 v252, v252, v108\n"
        "v_mfma_f32_16x16x32_bf16 v[82:85], a[164:167], a[104:107], 0\n"
        "s_mov_b32 s61, 0x88888888\n"
        "v_add_f32 v252, v252, v109\n"
        "v_add_f32 v252, v252, v110\n"
        "v_add_f32 v252, v252, v111\n"
        "v_mfma_f32_16x16x32_bf16 v[82:85], a[160:163], a[100:103], v[82:85]\n"
        "s_mov_b32 s62, 0xffff\n"
        "v_add_f32 v252, v252, v118\n"
        "v_add_f32 v252, v252, v119\n"
        "v_add_f32 v252, v252, v120\n"
        "v_mfma_f32_16x16x32_bf16 v[82:85], a[156:159], a[96:99], v[82:85]\n"
        "s_mov_b32 s63, 0x0\n"
        "v_add_f32 v252, v252, v121\n"
        "v_add_f32 v252, v252, v122\n"
        "v_add_f32 v252, v252, v123\n"
        "v_mfma_f32_16x16x32_bf16 v[86:89], a[168:171], a[96:99], 0\n"
        "s_mov_b32 s64, 0xffff0000\n"
        "v_add_f32 v252, v252, v124\n"
        "v_add_f32 v252, v252, v125\n"
        "v_add_f32 v252, v252, v126\n"
        "v_mfma_f32_16x16x32_bf16 v[86:89], a[172:175], a[100:103], v[86:89]\n"
        "s_mov_b32 s65, 0x0\n"
        "v_add_f32 v252, v252, v127\n"
        "v_add_f32 v252, v252, v128\n"
        "v_add_f32 v252, v252, v129\n"
        "v_mfma_f32_16x16x32_bf16 v[86:89], a[176:179], a[104:107], v[86:89]\n"
        "s_barrier\n"
        "ds_write_b128 v39, v[54:57]\n"
        "v_mov_b32 v254, v252\n"
        "v_mfma_f32_16x16x32_bf16 v[94:97], a[188:191], a[104:107], 0\n"
        "s_mov_b32 s66, 0x0\n"
        "ds_write_b128 v39, v[78:81] offset:1024\n"
        "v_add_f32 v253, v74, v75\n"
        "v_mfma_f32_16x16x32_bf16 v[94:97], a[184:187], a[100:103], v[94:97]\n"
        "s_mov_b32 s67, 0xffff\n"
        "ds_write_b128 v39, v[50:53] offset:2048\n"
        "v_add_f32 v253, v253, v76\n"
        "v_mfma_f32_16x16x32_bf16 v[94:97], a[180:183], a[96:99], v[94:97]\n"
        "s_mov_b32 s68, 0x0\n"
        "buffer_load_dwordx4 v[58:61], v37, s[36:39], 0 offen\n"
        "v_add_f32 v253, v253, v77\n"
        "v_add_f32 v253, v253, v82\n"
        "v_mfma_f32_16x16x32_bf16 v[134:137], a[192:195], a[96:99], 0\n"
        "s_mov_b32 s69, 0xffff0000\n"
        "v_permlane32_swap_b32 v252, v254\n"
        "v_add_f32 v252, v252, v254\n"
        "v_mfma_f32_16x16x32_bf16 v[134:137], a[196:199], a[100:103], v[134:137]\n"
        "s_mov_b32 s70, 0x0\n"
        "v_mov_b32 v254, v252\n"
        "v_add_f32 v253, v253, v83\n"
        "v_add_f32 v253, v253, v84\n"
        "v_mfma_f32_16x16x32_bf16 v[134:137], a[200:203], a[104:107], v[134:137]\n"
        "s_mov_b32 s71, 0xffffffff\n"
        "v_permlane16_swap_b32 v252, v254\n"
        "v_add_f32 v252, v252, v254\n"
        "v_mfma_f32_16x16x32_bf16 v[130:133], a[212:215], a[104:107], 0\n"
        "s_mov_b32 s72, 0x0\n"
        "v_mul_f32 v252, 0xbc2aaaab, v252\n"
        "v_add_f32 v30, v30, v252\n"
        "v_add_f32 v31, v31, v252\n"
        "v_mfma_f32_16x16x32_bf16 v[130:133], a[208:211], a[100:103], v[130:133]\n"
        "s_mov_b32 s73, 0x20000\n"
        "v_add_f32 v32, v32, v252\n"
        "v_add_f32 v33, v33, v252\n"
        "v_add_f32 v46, v46, v252\n"
        "v_mfma_f32_16x16x32_bf16 v[130:133], a[204:207], a[96:99], v[130:133]\n"
        "s_mov_b64 vcc, s[78:79]\n"
        "v_add_f32 v47, v47, v252\n"
        "v_add_f32 v48, v48, v252\n"
        "v_add_f32 v49, v49, v252\n"
        "v_mfma_f32_16x16x32_bf16 v[90:93], a[144:147], a[108:111], 0\n"
        "s_mov_b32 s80, 0x3c2aaaab\n"
        "v_add_f32 v108, v108, v252\n"
        "v_add_f32 v109, v109, v252\n"
        "v_add_f32 v110, v110, v252\n"
        "v_mfma_f32_16x16x32_bf16 v[90:93], a[148:151], a[112:115], v[90:93]\n"
        "s_mov_b32 s81, 0x3727c5ac\n"
        "v_add_f32 v111, v111, v252\n"
        "v_add_f32 v118, v118, v252\n"
        "v_add_f32 v119, v119, v252\n"
        "v_mfma_f32_16x16x32_bf16 v[90:93], a[152:155], a[116:119], v[90:93]\n"
        "s_mov_b32 s82, 0xc000c000\n"
        "buffer_load_dwordx4 v[64:67], v37, s[36:39], 0 offen offset:16\n"
        "v_add_f32 v120, v120, v252\n"
        "v_add_f32 v121, v121, v252\n"
        "v_mfma_f32_16x16x32_bf16 v[98:101], a[164:167], a[116:119], 0\n"
        "s_mov_b32 s84, 0x80000000\n"
        "v_add_f32 v122, v122, v252\n"
        "v_add_f32 v123, v123, v252\n"
        "v_add_f32 v124, v124, v252\n"
        "v_mfma_f32_16x16x32_bf16 v[98:101], a[160:163], a[112:115], v[98:101]\n"
        "s_mov_b32 s85, 19\n"
        "v_add_f32 v125, v125, v252\n"
        "v_add_f32 v126, v126, v252\n"
        "v_add_f32 v127, v127, v252\n"
        "v_mfma_f32_16x16x32_bf16 v[98:101], a[156:159], a[108:111], v[98:101]\n"
        "s_mov_b32 s86, 0x80000000\n"
        "v_add_f32 v128, v128, v252\n"
        "v_add_f32 v129, v129, v252\n"
        "v_cvt_pk_bf16_f32 v102, v30, v31\n"
        "v_mfma_f32_16x16x32_bf16 v[114:117], a[168:171], a[108:111], 0\n"
        "s_mov_b32 s89, 0x80000000\n"
        "v_cvt_pk_bf16_f32 v103, v32, v33\n"
        "v_cvt_pk_bf16_f32 v104, v46, v47\n"
        "v_cvt_pk_bf16_f32 v105, v48, v49\n"
        "v_mfma_f32_16x16x32_bf16 v[114:117], a[172:175], a[112:115], v[114:117]\n"
        "s_mov_b32 s29, 1\n"
        "ds_write_b128 v40, v[102:105]\n"
        "v_cvt_pk_bf16_f32 v106, v108, v109\n"
        "v_mfma_f32_16x16x32_bf16 v[114:117], a[176:179], a[116:119], v[114:117]\n"
        "s_mov_b32 s31, 0x400000\n"
        "v_cvt_pk_bf16_f32 v107, v110, v111\n"
        "v_cvt_pk_bf16_f32 v108, v118, v119\n"
        "v_cvt_pk_bf16_f32 v109, v120, v121\n"
        "v_mfma_f32_16x16x32_bf16 v[110:113], a[188:191], a[116:119], 0\n"
        "s_mov_b32 s87, s49\n"
        "v_cvt_pk_bf16_f32 v46, v122, v123\n"
        "v_cvt_pk_bf16_f32 v47, v124, v125\n"
        "v_cvt_pk_bf16_f32 v48, v126, v127\n"
        "v_mfma_f32_16x16x32_bf16 v[110:113], a[184:187], a[112:115], v[110:113]\n"
        "s_mov_b32 s88, s50\n"
        "v_cvt_pk_bf16_f32 v49, v128, v129\n"
        "v_add_f32 v253, v253, v85\n"
        "v_add_f32 v253, v253, v86\n"
        "v_mfma_f32_16x16x32_bf16 v[110:113], a[180:183], a[108:111], v[110:113]\n"
        "s_mov_b64 s[44:45], s[40:41]\n"
        "buffer_load_dwordx4 v[70:73], v37, s[36:39], 0 offen offset:32\n"
        "v_add_f32 v253, v253, v87\n"
        "v_add_f32 v253, v253, v88\n"
        "v_mfma_f32_16x16x32_bf16 v[118:121], a[192:195], a[108:111], 0\n"
        "s_mov_b32 s46, 0\n"
        "v_add_f32 v253, v253, v89\n"
        "v_add_f32 v253, v253, v94\n"
        "v_add_f32 v253, v253, v95\n"
        "v_mfma_f32_16x16x32_bf16 v[118:121], a[196:199], a[112:115], v[118:121]\n"
        "s_mov_b32 s47, s43\n"
        "v_add_f32 v253, v253, v96\n"
        "v_add_f32 v253, v253, v97\n"
        "v_add_f32 v253, v253, v134\n"
        "v_mfma_f32_16x16x32_bf16 v[118:121], a[200:203], a[116:119], v[118:121]\n"
        "v_add_f32 v253, v253, v135\n"
        "v_add_f32 v253, v253, v136\n"
        "v_add_f32 v253, v253, v137\n"
        "v_mfma_f32_16x16x32_bf16 v[30:33], a[212:215], a[116:119], 0\n"
        "v_add_f32 v253, v253, v130\n"
        "v_add_f32 v253, v253, v131\n"
        "v_add_f32 v253, v253, v132\n"
        "v_mfma_f32_16x16x32_bf16 v[30:33], a[208:211], a[112:115], v[30:33]\n"
        "v_add_f32 v253, v253, v133\n"
        "v_mov_b32 v254, v253\n"
        "v_add_f32 v38, v90, v91\n"
        "v_mfma_f32_16x16x32_bf16 v[30:33], a[204:207], a[108:111], v[30:33]\n"
        "v_permlane32_swap_b32 v253, v254\n"
        "v_add_f32 v253, v253, v254\n"
        "v_mfma_f32_16x16x32_bf16 v[122:125], a[144:147], a[120:123], 0\n"
        "v_mov_b32 v254, v253\n"
        "v_add_f32 v38, v38, v92\n"
        "v_add_f32 v38, v38, v93\n"
        "v_mfma_f32_16x16x32_bf16 v[122:125], a[148:151], a[124:127], v[122:125]\n"
        "s_add_u32 m0, s52, 0\n"
        "v_permlane16_swap_b32 v253, v254\n"
        "v_add_f32 v253, v253, v254\n"
        "v_mfma_f32_16x16x32_bf16 v[122:125], a[152:155], a[128:131], v[122:125]\n"
        "buffer_load_dwordx4 v0, s[36:39], s49 offen lds\n"
        "v_mul_f32 v253, 0xbc2aaaab, v253\n"
        "v_add_f32 v74, v74, v253\n"
        "v_mfma_f32_16x16x32_bf16 v[34:37], a[164:167], a[128:131], 0\n"
        "v_add_f32 v75, v75, v253\n"
        "v_add_f32 v76, v76, v253\n"
        "v_add_f32 v77, v77, v253\n"
        "v_mfma_f32_16x16x32_bf16 v[34:37], a[160:163], a[124:127], v[34:37]\n"
        "v_add_f32 v82, v82, v253\n"
        "v_add_f32 v83, v83, v253\n"
        "v_add_f32 v84, v84, v253\n"
        "v_mfma_f32_16x16x32_bf16 v[34:37], a[156:159], a[120:123], v[34:37]\n"
        "v_add_f32 v85, v85, v253\n"
        "v_add_f32 v86, v86, v253\n"
        "v_add_f32 v87, v87, v253\n"
        "v_mfma_f32_16x16x32_bf16 v[146:149], a[168:171], a[120:123], 0\n"
        "v_add_f32 v88, v88, v253\n"
        "v_add_f32 v89, v89, v253\n"
        "v_add_f32 v94, v94, v253\n"
        "v_mfma_f32_16x16x32_bf16 v[146:149], a[172:175], a[124:127], v[146:149]\n"
        "v_add_f32 v95, v95, v253\n"
        "v_add_f32 v96, v96, v253\n"
        "v_add_f32 v97, v97, v253\n"
        "v_mfma_f32_16x16x32_bf16 v[146:149], a[176:179], a[128:131], v[146:149]\n"
        "buffer_load_dwordx4 v1, s[36:39], s49 offen offset:1024 lds\n"
        "v_add_f32 v134, v134, v253\n"
        "v_add_f32 v135, v135, v253\n"
        "v_mfma_f32_16x16x32_bf16 v[138:141], a[188:191], a[128:131], 0\n"
        "v_add_f32 v136, v136, v253\n"
        "v_add_f32 v137, v137, v253\n"
        "v_add_f32 v130, v130, v253\n"
        "v_mfma_f32_16x16x32_bf16 v[138:141], a[184:187], a[124:127], v[138:141]\n"
        "v_add_f32 v131, v131, v253\n"
        "v_add_f32 v132, v132, v253\n"
        "v_add_f32 v133, v133, v253\n"
        "v_mfma_f32_16x16x32_bf16 v[138:141], a[180:183], a[120:123], v[138:141]\n"
        "v_cvt_pk_bf16_f32 v126, v74, v75\n"
        "v_cvt_pk_bf16_f32 v127, v76, v77\n"
        "v_cvt_pk_bf16_f32 v128, v82, v83\n"
        "v_mfma_f32_16x16x32_bf16 v[142:145], a[192:195], a[120:123], 0\n"
        "v_cvt_pk_bf16_f32 v129, v84, v85\n"
        "v_cvt_pk_bf16_f32 v74, v86, v87\n"
        "v_cvt_pk_bf16_f32 v75, v88, v89\n"
        "v_mfma_f32_16x16x32_bf16 v[142:145], a[196:199], a[124:127], v[142:145]\n"
        "v_cvt_pk_bf16_f32 v76, v94, v95\n"
        "v_cvt_pk_bf16_f32 v77, v96, v97\n"
        "v_cvt_pk_bf16_f32 v82, v134, v135\n"
        "v_mfma_f32_16x16x32_bf16 v[142:145], a[200:203], a[128:131], v[142:145]\n"
        "buffer_load_dwordx4 v2, s[36:39], s49 offen offset:2048 lds\n"
        "v_cvt_pk_bf16_f32 v83, v136, v137\n"
        "v_cvt_pk_bf16_f32 v84, v130, v131\n"
        "v_mfma_f32_16x16x32_bf16 v[134:137], a[212:215], a[128:131], 0\n"
        "v_cvt_pk_bf16_f32 v85, v132, v133\n"
        "v_add_f32 v38, v38, v98\n"
        "v_add_f32 v38, v38, v99\n"
        "v_mfma_f32_16x16x32_bf16 v[134:137], a[208:211], a[124:127], v[134:137]\n"
        "v_add_f32 v38, v38, v100\n"
        "v_add_f32 v38, v38, v101\n"
        "v_add_f32 v38, v38, v114\n"
        "v_mfma_f32_16x16x32_bf16 v[134:137], a[204:207], a[120:123], v[134:137]\n"
        "v_add_f32 v38, v38, v115\n"
        "v_add_f32 v38, v38, v116\n"
        "v_add_f32 v38, v38, v117\n"
        "v_mfma_f32_16x16x32_bf16 v[86:89], a[144:147], a[132:135], 0\n"
        "v_add_f32 v38, v38, v110\n"
        "v_add_f32 v38, v38, v111\n"
        "v_add_f32 v38, v38, v112\n"
        "v_mfma_f32_16x16x32_bf16 v[86:89], a[148:151], a[136:139], v[86:89]\n"
        "v_add_f32 v38, v38, v113\n"
        "v_add_f32 v38, v38, v118\n"
        "v_add_f32 v38, v38, v119\n"
        "v_mfma_f32_16x16x32_bf16 v[86:89], a[152:155], a[140:143], v[86:89]\n"
        "buffer_load_dwordx4 v3, s[36:39], s49 offen offset:3072 lds\n"
        "v_add_f32 v38, v38, v120\n"
        "v_add_f32 v38, v38, v121\n"
        "v_mfma_f32_16x16x32_bf16 v[130:133], a[164:167], a[140:143], 0\n"
        "v_add_f32 v38, v38, v30\n"
        "v_add_f32 v38, v38, v31\n"
        "v_add_f32 v38, v38, v32\n"
        "v_mfma_f32_16x16x32_bf16 v[130:133], a[160:163], a[136:139], v[130:133]\n"
        "v_add_f32 v38, v38, v33\n"
        "v_mov_b32 v39, v38\n"
        "v_add_f32 v62, v122, v123\n"
        "v_mfma_f32_16x16x32_bf16 v[130:133], a[156:159], a[132:135], v[130:133]\n"
        "v_permlane32_swap_b32 v38, v39\n"
        "v_add_f32 v38, v38, v39\n"
        "v_mfma_f32_16x16x32_bf16 v[154:157], a[168:171], a[132:135], 0\n"
        "v_mov_b32 v39, v38\n"
        "v_add_f32 v62, v62, v124\n"
        "v_add_f32 v62, v62, v125\n"
        "v_mfma_f32_16x16x32_bf16 v[154:157], a[172:175], a[136:139], v[154:157]\n"
        "s_add_u32 m0, s52, 4096\n"
        "v_permlane16_swap_b32 v38, v39\n"
        "v_add_f32 v38, v38, v39\n"
        "v_mfma_f32_16x16x32_bf16 v[154:157], a[176:179], a[140:143], v[154:157]\n"
        "buffer_load_dwordx4 v4, s[36:39], s49 offen lds\n"
        "v_mul_f32 v38, 0xbc2aaaab, v38\n"
        "v_add_f32 v62, v62, v34\n"
        "v_mfma_f32_16x16x32_bf16 v[94:97], a[188:191], a[140:143], 0\n"
        "v_add_f32 v62, v62, v35\n"
        "v_add_f32 v62, v62, v36\n"
        "v_add_f32 v62, v62, v37\n"
        "v_mfma_f32_16x16x32_bf16 v[94:97], a[184:187], a[136:139], v[94:97]\n"
        "v_add_f32 v62, v62, v146\n"
        "v_add_f32 v62, v62, v147\n"
        "v_add_f32 v62, v62, v148\n"
        "v_mfma_f32_16x16x32_bf16 v[94:97], a[180:183], a[132:135], v[94:97]\n"
        "v_add_f32 v62, v62, v149\n"
        "v_add_f32 v62, v62, v138\n"
        "v_add_f32 v62, v62, v139\n"
        "v_mfma_f32_16x16x32_bf16 v[158:161], a[192:195], a[132:135], 0\n"
        "v_add_f32 v62, v62, v140\n"
        "v_add_f32 v62, v62, v141\n"
        "v_add_f32 v62, v62, v142\n"
        "v_mfma_f32_16x16x32_bf16 v[158:161], a[196:199], a[136:139], v[158:161]\n"
        "v_add_f32 v62, v62, v143\n"
        "v_add_f32 v62, v62, v144\n"
        "v_add_f32 v62, v62, v145\n"
        "v_mfma_f32_16x16x32_bf16 v[158:161], a[200:203], a[140:143], v[158:161]\n"
        "buffer_load_dwordx4 v5, s[36:39], s49 offen offset:1024 lds\n"
        "v_add_f32 v62, v62, v134\n"
        "v_add_f32 v62, v62, v135\n"
        "v_mfma_f32_16x16x32_bf16 v[150:153], a[212:215], a[140:143], 0\n"
        "v_add_f32 v62, v62, v136\n"
        "v_add_f32 v62, v62, v137\n"
        "v_mov_b32 v63, v62\n"
        "v_mfma_f32_16x16x32_bf16 v[150:153], a[208:211], a[136:139], v[150:153]\n"
        "ds_write_b128 v40, v[106:109] offset:1024\n"
        "v_add_f32 v68, v86, v87\n"
        "v_mfma_f32_16x16x32_bf16 v[150:153], a[204:207], a[132:135], v[150:153]\n"
        "ds_write_b128 v40, v[46:49] offset:2048\n"
        "v_add_f32 v68, v68, v88\n"
        "v_pk_add_f32 v[90:91], v[90:91], v[38:39] op_sel_hi:[1,0]\n"
        "v_cvt_pk_bf16_f32 v162, v90, v91\n"
        "v_pk_add_f32 v[92:93], v[92:93], v[38:39] op_sel_hi:[1,0]\n"
        "v_cvt_pk_bf16_f32 v163, v92, v93\n"
        "v_pk_add_f32 v[98:99], v[98:99], v[38:39] op_sel_hi:[1,0]\n"
        "v_cvt_pk_bf16_f32 v164, v98, v99\n"
        "v_pk_add_f32 v[100:101], v[100:101], v[38:39] op_sel_hi:[1,0]\n"
        "v_cvt_pk_bf16_f32 v165, v100, v101\n"
        "v_pk_add_f32 v[114:115], v[114:115], v[38:39] op_sel_hi:[1,0]\n"
        "v_cvt_pk_bf16_f32 v98, v114, v115\n"
        "v_pk_add_f32 v[116:117], v[116:117], v[38:39] op_sel_hi:[1,0]\n"
        "v_cvt_pk_bf16_f32 v99, v116, v117\n"
        "v_pk_add_f32 v[110:111], v[110:111], v[38:39] op_sel_hi:[1,0]\n"
        "v_cvt_pk_bf16_f32 v100, v110, v111\n"
        "v_pk_add_f32 v[112:113], v[112:113], v[38:39] op_sel_hi:[1,0]\n"
        "v_cvt_pk_bf16_f32 v101, v112, v113\n"
        "v_pk_add_f32 v[118:119], v[118:119], v[38:39] op_sel_hi:[1,0]\n"
        "v_cvt_pk_bf16_f32 v90, v118, v119\n"
        "v_pk_add_f32 v[120:121], v[120:121], v[38:39] op_sel_hi:[1,0]\n"
        "v_cvt_pk_bf16_f32 v91, v120, v121\n"
        "v_pk_add_f32 v[30:31], v[30:31], v[38:39] op_sel_hi:[1,0]\n"
        "v_cvt_pk_bf16_f32 v92, v30, v31\n"
        "v_pk_add_f32 v[32:33], v[32:33], v[38:39] op_sel_hi:[1,0]\n"
        "v_cvt_pk_bf16_f32 v93, v32, v33\n"
        "v_permlane32_swap_b32 v62, v63\n"
        "v_add_f32 v62, v62, v63\n"
        "v_mov_b32 v63, v62\n"
        "s_nop 1\n"
        "v_permlane16_swap_b32 v62, v63\n"
        "v_add_f32 v62, v62, v63\n"
        "v_mul_f32 v62, 0xbc2aaaab, v62\n"
        "v_pk_add_f32 v[122:123], v[122:123], v[62:63] op_sel_hi:[1,0]\n"
        "v_cvt_pk_bf16_f32 v110, v122, v123\n"
        "v_pk_add_f32 v[124:125], v[124:125], v[62:63] op_sel_hi:[1,0]\n"
        "v_cvt_pk_bf16_f32 v111, v124, v125\n"
        "v_pk_add_f32 v[34:35], v[34:35], v[62:63] op_sel_hi:[1,0]\n"
        "v_cvt_pk_bf16_f32 v112, v34, v35\n"
        "v_pk_add_f32 v[36:37], v[36:37], v[62:63] op_sel_hi:[1,0]\n"
        "v_cvt_pk_bf16_f32 v113, v36, v37\n"
        "v_pk_add_f32 v[146:147], v[146:147], v[62:63] op_sel_hi:[1,0]\n"
        "v_cvt_pk_bf16_f32 v30, v146, v147\n"
        "v_pk_add_f32 v[148:149], v[148:149], v[62:63] op_sel_hi:[1,0]\n"
        "v_cvt_pk_bf16_f32 v31, v148, v149\n"
        "v_pk_add_f32 v[138:139], v[138:139], v[62:63] op_sel_hi:[1,0]\n"
        "v_cvt_pk_bf16_f32 v32, v138, v139\n"
        "v_pk_add_f32 v[140:141], v[140:141], v[62:63] op_sel_hi:[1,0]\n"
        "v_cvt_pk_bf16_f32 v33, v140, v141\n"
        "v_pk_add_f32 v[142:143], v[142:143], v[62:63] op_sel_hi:[1,0]\n"
        "v_cvt_pk_bf16_f32 v34, v142, v143\n"
        "v_pk_add_f32 v[144:145], v[144:145], v[62:63] op_sel_hi:[1,0]\n"
        "v_cvt_pk_bf16_f32 v35, v144, v145\n"
        "v_pk_add_f32 v[134:135], v[134:135], v[62:63] op_sel_hi:[1,0]\n"
        "v_cvt_pk_bf16_f32 v36, v134, v135\n"
        "v_pk_add_f32 v[136:137], v[136:137], v[62:63] op_sel_hi:[1,0]\n"
        "v_cvt_pk_bf16_f32 v37, v136, v137\n"
        "v_add_f32 v68, v68, v89\n"
        "v_add_f32 v68, v68, v130\n"
        "v_add_f32 v68, v68, v131\n"
        "v_add_f32 v68, v68, v132\n"
        "v_add_f32 v68, v68, v133\n"
        "v_add_f32 v68, v68, v154\n"
        "v_add_f32 v68, v68, v155\n"
        "v_add_f32 v68, v68, v156\n"
        "v_add_f32 v68, v68, v157\n"
        "v_add_f32 v68, v68, v94\n"
        "v_add_f32 v68, v68, v95\n"
        "v_add_f32 v68, v68, v96\n"
        "v_add_f32 v68, v68, v97\n"
        "v_add_f32 v68, v68, v158\n"
        "v_add_f32 v68, v68, v159\n"
        "v_add_f32 v68, v68, v160\n"
        "v_add_f32 v68, v68, v161\n"
        "v_add_f32 v68, v68, v150\n"
        "v_add_f32 v68, v68, v151\n"
        "v_add_f32 v68, v68, v152\n"
        "v_add_f32 v68, v68, v153\n"
        "v_mov_b32 v69, v68\n"
        "s_nop 1\n"
        "v_permlane32_swap_b32 v68, v69\n"
        "v_add_f32 v68, v68, v69\n"
        "v_mov_b32 v69, v68\n"
        "s_nop 1\n"
        "v_permlane16_swap_b32 v68, v69\n"
        "v_add_f32 v68, v68, v69\n"
        "v_mul_f32 v68, 0xbc2aaaab, v68\n"
        "v_pk_add_f32 v[86:87], v[86:87], v[68:69] op_sel_hi:[1,0]\n"
        "v_cvt_pk_bf16_f32 v114, v86, v87\n"
        "v_pk_add_f32 v[88:89], v[88:89], v[68:69] op_sel_hi:[1,0]\n"
        "v_cvt_pk_bf16_f32 v115, v88, v89\n"
        "v_pk_add_f32 v[130:131], v[130:131], v[68:69] op_sel_hi:[1,0]\n"
        "v_cvt_pk_bf16_f32 v116, v130, v131\n"
        "v_pk_add_f32 v[132:133], v[132:133], v[68:69] op_sel_hi:[1,0]\n"
        "v_cvt_pk_bf16_f32 v117, v132, v133\n"
        "v_pk_add_f32 v[154:155], v[154:155], v[68:69] op_sel_hi:[1,0]\n"
        "v_cvt_pk_bf16_f32 v86, v154, v155\n"
        "v_pk_add_f32 v[156:157], v[156:157], v[68:69] op_sel_hi:[1,0]\n"
        "v_cvt_pk_bf16_f32 v87, v156, v157\n"
        "v_pk_add_f32 v[94:95], v[94:95], v[68:69] op_sel_hi:[1,0]\n"
        "v_cvt_pk_bf16_f32 v88, v94, v95\n"
        "v_pk_add_f32 v[96:97], v[96:97], v[68:69] op_sel_hi:[1,0]\n"
        "v_cvt_pk_bf16_f32 v89, v96, v97\n"
        "v_pk_add_f32 v[158:159], v[158:159], v[68:69] op_sel_hi:[1,0]\n"
        "v_cvt_pk_bf16_f32 v94, v158, v159\n"
        "v_pk_add_f32 v[160:161], v[160:161], v[68:69] op_sel_hi:[1,0]\n"
        "v_cvt_pk_bf16_f32 v95, v160, v161\n"
        "v_pk_add_f32 v[150:151], v[150:151], v[68:69] op_sel_hi:[1,0]\n"
        "v_cvt_pk_bf16_f32 v96, v150, v151\n"
        "v_pk_add_f32 v[152:153], v[152:153], v[68:69] op_sel_hi:[1,0]\n"
        "v_cvt_pk_bf16_f32 v97, v152, v153\n"
        "s_nop 1\n"
        "v_mfma_f32_16x16x32_bf16 v[118:121], v[54:57], v[54:57], 0\n"
        "ds_write_b128 v41, v[126:129]\n"
        "v_mfma_f32_16x16x32_bf16 v[118:121], v[78:81], v[78:81], v[118:121]\n"
        "ds_write_b128 v41, v[74:77] offset:1024\n"
        "v_mfma_f32_16x16x32_bf16 v[118:121], v[50:53], v[50:53], v[118:121]\n"
        "buffer_load_dwordx4 v6, s[36:39], s49 offen offset:2048 lds\n"
        "v_mfma_f32_16x16x32_bf16 v[118:121], v[102:105], v[102:105], v[118:121]\n"
        "ds_write_b128 v41, v[82:85] offset:2048\n"
        "v_mfma_f32_16x16x32_bf16 v[118:121], v[106:109], v[106:109], v[118:121]\n"
        "ds_write_b128 v42, v[162:165]\n"
        "v_mfma_f32_16x16x32_bf16 v[118:121], v[46:49], v[46:49], v[118:121]\n"
        "ds_write_b128 v42, v[98:101] offset:1024\n"
        "v_mfma_f32_16x16x32_bf16 v[118:121], v[126:129], v[126:129], v[118:121]\n"
        "ds_write_b128 v42, v[90:93] offset:2048\n"
        "v_mfma_f32_16x16x32_bf16 v[118:121], v[74:77], v[74:77], v[118:121]\n"
        "ds_write_b128 v43, v[110:113]\n"
        "v_mfma_f32_16x16x32_bf16 v[118:121], v[82:85], v[82:85], v[118:121]\n"
        "buffer_load_dwordx4 v7, s[36:39], s49 offen offset:3072 lds\n"
        "v_mfma_f32_16x16x32_bf16 v[118:121], v[162:165], v[162:165], v[118:121]\n"
        "ds_write_b128 v43, v[30:33] offset:1024\n"
        "v_mfma_f32_16x16x32_bf16 v[118:121], v[98:101], v[98:101], v[118:121]\n"
        "ds_write_b128 v43, v[34:37] offset:2048\n"
        "v_mfma_f32_16x16x32_bf16 v[118:121], v[90:93], v[90:93], v[118:121]\n"
        "ds_write_b128 v44, v[114:117]\n"
        "v_mfma_f32_16x16x32_bf16 v[118:121], v[110:113], v[110:113], v[118:121]\n"
        "ds_write_b128 v44, v[86:89] offset:1024\n"
        "v_mfma_f32_16x16x32_bf16 v[118:121], v[30:33], v[30:33], v[118:121]\n"
        "s_add_u32 m0, s52, 8192\n"
        "ds_write_b128 v44, v[94:97] offset:2048\n"
        "v_mfma_f32_16x16x32_bf16 v[118:121], v[34:37], v[34:37], v[118:121]\n"
        "buffer_load_dwordx4 v8, s[36:39], s49 offen lds\n"
        "v_mfma_f32_16x16x32_bf16 v[118:121], v[114:117], v[114:117], v[118:121]\n"
        "v_mfma_f32_16x16x32_bf16 v[118:121], v[86:89], v[86:89], v[118:121]\n"
        "v_mfma_f32_16x16x32_bf16 v[118:121], v[94:97], v[94:97], v[118:121]\n"
        "s_nop 2\n"
        "s_waitcnt vmcnt(9)\n"
        "s_nop 3\n"
        "v_max3_f32 v254, v118, v119, v120\n"
        "v_max_f32 v254, v254, v121\n"
        "v_cmp_nge_f32_e64 s[74:75], s24, v254\n"
        "v_mov_b32 v254, 161632\n"
        "s_nop 0\n"
        "v_cndmask_b32_e64 v254, v255, v254, s[74:75]\n"
        "v_mov_b32 v255, 0x80008000\n"
        "ds_write_b32 v254, v255\n"
        "s_waitcnt lgkmcnt(0)\n"
        "s_barrier\n"
        "FE_%=:\n"
        "s_mov_b64 s[16:17], s[54:55]\n"
        "s_cmp_eq_u32 s51, 0\n"
        "s_cselect_b32 s18, 128, 0\n"
        "s_cselect_b32 s49, 1, -100\n"
        "s_sub_u32 s48, s48, 32\n"
        "s_movk_i32 s22, 1584\n"
        "s_movk_i32 s23, 1584\n"
        "s_mov_b32 s53, 0\n"
        "s_mov_b32 s54, 0x05040302\n"
        "s_mov_b32 s55, 0x07060302\n"
        "v_mov_b32 v28, s80\n"
        "ds_read_b128 v[96:99], v26\n"
        "ds_read_b128 v[100:103], v26 offset:64\n"
        "ds_read_b128 v[92:95], v26 offset:128\n"
        "v_xor_b32 v78, 64, v186\n"
        "ds_read_b128 a[0:3], v186\n"
        "ds_read_b128 a[4:7], v186 offset:1024\n"
        "ds_read_b128 a[8:11], v186 offset:2048\n"
        "ds_read_b128 a[12:15], v186 offset:3072\n"
        "ds_read_b128 a[16:19], v186 offset:4096\n"
        "ds_read_b128 a[20:23], v186 offset:5120\n"
        "ds_read_b128 a[24:27], v186 offset:6144\n"
        "ds_read_b128 a[28:31], v186 offset:7168\n"
        "ds_read_b128 a[32:35], v186 offset:8192\n"
        "v_mfma_f32_16x16x32_bf16 v[152:155], v[196:199], v[196:199], 0\n"
        "ds_read_b128 a[36:39], v186 offset:9216\n"
        "v_mov_b32 v63, 161632\n"
        "v_mfma_f32_16x16x32_bf16 v[152:155], v[202:205], v[202:205], v[152:155]\n"
        "ds_read_b128 a[40:43], v186 offset:10240\n"
        "v_lshrrev_b32 v222, 2, v186\n"
        "v_mfma_f32_16x16x32_bf16 v[152:155], v[208:211], v[208:211], v[152:155]\n"
        "ds_read_b128 a[44:47], v186 offset:11264\n"
        "v_and_b32 v222, 128, v222\n"
        "v_mfma_f32_16x16x32_bf16 v[112:115], v[58:61], v[58:61], 0\n"
        "ds_read_b128 a[48:51], v186 offset:12288\n"
        "v_xor_b32 v222, v222, v186\n"
        "v_mfma_f32_16x16x32_bf16 v[112:115], v[64:67], v[64:67], v[112:115]\n"
        "s_nop 0\n"
        "ds_read_b128 a[52:55], v186 offset:13312\n"
        "v_cndmask_b32_e64 v75, v152, v153, s[56:57]\n"
        "v_mfma_f32_16x16x32_bf16 v[112:115], v[70:73], v[70:73], v[112:115]\n"
        "s_waitcnt lgkmcnt(14)\n"
        "ds_read_b128 a[56:59], v186 offset:14336\n"
        "v_cndmask_b32_e64 v75, v75, v154, s[58:59]\n"
        "v_mfma_f32_16x16x32_bf16 v[158:161], v[92:95], v[70:73], 0\n"
        "ds_read_b128 a[60:63], v186 offset:15360\n"
        "v_cndmask_b32_e64 v75, v75, v155, s[60:61]\n"
        "v_mfma_f32_16x16x32_bf16 v[158:161], v[100:103], v[64:67], v[158:161]\n"
        "ds_read_b128 a[64:67], v186 offset:16384\n"
        "v_cndmask_b32_e64 v255, v112, v113, s[56:57]\n"
        "v_mfma_f32_16x16x32_bf16 v[158:161], v[96:99], v[58:61], v[158:161]\n"
        "s_waitcnt lgkmcnt(14)\n"
        "ds_read_b128 a[68:71], v186 offset:17408\n"
        "v_cndmask_b32_e64 v255, v255, v114, s[58:59]\n"
        "v_mfma_f32_16x16x32_bf16 v[118:121], v[96:99], v[196:199], 0\n"
        "s_waitcnt lgkmcnt(12)\n"
        "ds_read_b128 a[72:75], v186 offset:18432\n"
        "v_cndmask_b32_e64 v255, v255, v115, s[60:61]\n"
        "v_mfma_f32_16x16x32_bf16 v[118:121], v[100:103], v[202:205], v[118:121]\n"
        "s_waitcnt lgkmcnt(10)\n"
        "ds_read_b128 a[76:79], v186 offset:19456\n"
        "v_cndmask_b32_dpp v75, v255, v75, vcc row_ror:4 row_mask:0xf bank_mask:0xf\n"
        "v_mfma_f32_16x16x32_bf16 v[118:121], v[92:95], v[208:211], v[118:121]\n"
        "ds_read_b128 a[80:83], v186 offset:20480\n"
        "v_add_u32 v255, 73792, v186\n"
        "v_mfma_f32_16x16x32_bf16 v[84:87], a[8:11], v[208:211], 0\n"
        "ds_read_b128 a[84:87], v186 offset:21504\n"
        "v_add_u32 v222, 140896, v222\n"
        "v_mfma_f32_16x16x32_bf16 v[84:87], a[4:7], v[202:205], v[84:87]\n"
        "s_waitcnt lgkmcnt(10)\n"
        "ds_read_b128 a[88:91], v186 offset:22528\n"
        "v_cndmask_b32_dpp v90, v158, v118, vcc row_ror:4 row_mask:0xf bank_mask:0xf\n"
        "v_mfma_f32_16x16x32_bf16 v[84:87], a[0:3], v[196:199], v[84:87]\n"
        "ds_read_b128 a[92:95], v186 offset:23552\n"
        "v_mul_f32 v118, s80, v90\n"
        "v_mfma_f32_16x16x32_bf16 v[152:155], a[12:15], v[196:199], 0\n"
        "ds_read_b128 a[96:99], v78 offset:24576\n"
        "v_fma_f32 v118, -v118, v90, v75\n"
        "v_mfma_f32_16x16x32_bf16 v[152:155], a[16:19], v[202:205], v[152:155]\n"
        "s_waitcnt lgkmcnt(10)\n"
        "ds_read_b128 a[100:103], v78 offset:25600\n"
        "v_cvt_pk_bf16_f32 v112, v84, v85\n"
        "v_mfma_f32_16x16x32_bf16 v[152:155], a[20:23], v[208:211], v[152:155]\n"
        "ds_read_b128 a[104:107], v78 offset:26624\n"
        "v_cvt_pk_bf16_f32 v113, v86, v87\n"
        "v_mfma_f32_16x16x32_bf16 v[84:87], a[32:35], v[208:211], 0\n"
        "ds_read_b128 a[108:111], v78 offset:27648\n"
        "v_max_f32 v118, 0, v118\n"
        "v_mfma_f32_16x16x32_bf16 v[84:87], a[28:31], v[202:205], v[84:87]\n"
        "s_waitcnt lgkmcnt(10)\n"
        "ds_read_b128 a[112:115], v78 offset:28672\n"
        "v_cvt_pk_bf16_f32 v114, v152, v153\n"
        "v_mfma_f32_16x16x32_bf16 v[84:87], a[24:27], v[196:199], v[84:87]\n"
        "ds_read_b128 a[116:119], v78 offset:29696\n"
        "v_cvt_pk_bf16_f32 v115, v154, v155\n"
        "v_mfma_f32_16x16x32_bf16 v[74:77], a[36:39], v[196:199], 0\n"
        "ds_read_b128 a[120:123], v186 offset:30720\n"
        "v_fmaak_f32 v118, v28, v118, 0x3727c5ac\n"
        "v_mfma_f32_16x16x32_bf16 v[74:77], a[40:43], v[202:205], v[74:77]\n"
        "s_nop 0\n"
        "ds_read_b128 a[124:127], v186 offset:31744\n"
        "v_cvt_pk_bf16_f32 v152, v84, v85\n"
        "v_mfma_f32_16x16x32_bf16 v[74:77], a[44:47], v[208:211], v[74:77]\n"
        "ds_read_b128 a[128:131], v186 offset:32768\n"
        "v_cvt_pk_bf16_f32 v153, v86, v87\n"
        "v_mfma_f32_16x16x32_bf16 v[84:87], a[56:59], v[208:211], 0\n"
        "ds_read_b128 a[132:135], v186 offset:33792\n"
        "v_cndmask_b32_dpp v254, v159, v119, vcc row_ror:4 row_mask:0xf bank_mask:0xf\n"
        "v_mfma_f32_16x16x32_bf16 v[84:87], a[52:55], v[202:205], v[84:87]\n"
        "s_nop 0\n"
        "ds_read_b128 a[136:139], v186 offset:34816\n"
        "v_cvt_pk_bf16_f32 v154, v74, v75\n"
        "v_mfma_f32_16x16x32_bf16 v[84:87], a[48:51], v[196:199], v[84:87]\n"
        "s_nop 0\n"
        "ds_read_b128 a[140:143], v186 offset:35840\n"
        "v_cvt_pk_bf16_f32 v155, v76, v77\n"
        "v_mfma_f32_16x16x32_bf16 v[158:161], a[60:63], v[196:199], 0\n"
        "s_nop 0\n"
        "v_rsq_f32 v78, v118\n"
        "v_mfma_f32_16x16x32_bf16 v[158:161], a[64:67], v[202:205], v[158:161]\n"
        "s_nop 0\n"
        "v_cvt_pk_bf16_f32 v118, v84, v85\n"
        "v_cvt_pk_bf16_f32 v119, v86, v87\n"
        "v_mfma_f32_16x16x32_bf16 v[158:161], a[68:71], v[208:211], v[158:161]\n"
        "s_nop 1\n"
        "v_mfma_f32_16x16x32_bf16 v[84:87], a[8:11], v[70:73], 0\n"
        "s_nop 2\n"
        "v_mfma_f32_16x16x32_bf16 v[84:87], a[4:7], v[64:67], v[84:87]\n"
        "s_nop 0\n"
        "v_cvt_pk_bf16_f32 v120, v158, v159\n"
        "v_cvt_pk_bf16_f32 v121, v160, v161\n"
        "v_mfma_f32_16x16x32_bf16 v[84:87], a[0:3], v[58:61], v[84:87]\n"
        "s_nop 1\n"
        "v_mfma_f32_16x16x32_bf16 v[158:161], v[196:199], v[112:115], 0\n"
        "s_nop 2\n"
        "v_mfma_f32_16x16x32_bf16 v[158:161], v[202:205], v[152:155], v[158:161]\n"
        "s_nop 0\n"
        "v_cvt_pk_bf16_f32 v152, v84, v85\n"
        "v_cvt_pk_bf16_f32 v153, v86, v87\n"
        "v_mfma_f32_16x16x32_bf16 v[158:161], v[208:211], v[118:121], v[158:161]\n"
        "v_mfma_f32_16x16x32_bf16 v[112:115], a[20:23], v[70:73], 0\n"
        "s_waitcnt lgkmcnt(0)\n"
        "v_mfma_f32_16x16x32_bf16 v[112:115], a[16:19], v[64:67], v[112:115]\n"
        "s_barrier\n"
        "ds_read_b128 a[144:147], v222\n"
        "v_mfma_f32_16x16x32_bf16 v[112:115], a[12:15], v[58:61], v[112:115]\n"
        "s_nop 1\n"
        "v_mfma_f32_16x16x32_bf16 v[118:121], a[24:27], v[58:61], 0\n"
        "s_nop 0\n"
        "ds_read_b128 a[148:151], v222 offset:1024\n"
        "s_nop 0\n"
        "v_mfma_f32_16x16x32_bf16 v[118:121], a[28:31], v[64:67], v[118:121]\n"
        "s_nop 0\n"
        "v_cvt_pk_bf16_f32 v154, v112, v113\n"
        "v_cvt_pk_bf16_f32 v155, v114, v115\n"
        "v_mfma_f32_16x16x32_bf16 v[118:121], a[32:35], v[70:73], v[118:121]\n"
        "s_nop 1\n"
        "v_mfma_f32_16x16x32_bf16 v[84:87], a[44:47], v[70:73], 0\n"
        "s_nop 2\n"
        "v_mfma_f32_16x16x32_bf16 v[84:87], a[40:43], v[64:67], v[84:87]\n"
        "s_nop 0\n"
        "v_cvt_pk_bf16_f32 v112, v118, v119\n"
        "v_cvt_pk_bf16_f32 v113, v120, v121\n"
        "v_mfma_f32_16x16x32_bf16 v[84:87], a[36:39], v[58:61], v[84:87]\n"
        "s_nop 0\n"
        "ds_read_b128 a[152:155], v222 offset:2048\n"
        "v_mfma_f32_16x16x32_bf16 v[118:121], a[48:51], v[58:61], 0\n"
        "s_nop 2\n"
        "v_mfma_f32_16x16x32_bf16 v[118:121], a[52:55], v[64:67], v[118:121]\n"
        "s_nop 0\n"
        "v_cvt_pk_bf16_f32 v114, v84, v85\n"
        "v_cvt_pk_bf16_f32 v115, v86, v87\n"
        "v_mfma_f32_16x16x32_bf16 v[118:121], a[56:59], v[70:73], v[118:121]\n"
        "s_nop 0\n"
        "ds_read_b128 a[156:159], v222 offset:3072\n"
        "v_mfma_f32_16x16x32_bf16 v[74:77], a[68:71], v[70:73], 0\n"
        "s_nop 2\n"
        "v_mfma_f32_16x16x32_bf16 v[74:77], a[64:67], v[64:67], v[74:77]\n"
        "s_nop 0\n"
        "v_cvt_pk_bf16_f32 v84, v118, v119\n"
        "v_cvt_pk_bf16_f32 v85, v120, v121\n"
        "v_mfma_f32_16x16x32_bf16 v[74:77], a[60:63], v[58:61], v[74:77]\n"
        "s_nop 0\n"
        "ds_read_b128 a[160:163], v222 offset:4096\n"
        "v_mfma_f32_16x16x32_bf16 v[118:121], v[196:199], a[72:75], 0\n"
        "s_nop 2\n"
        "v_mfma_f32_16x16x32_bf16 v[118:121], v[202:205], a[76:79], v[118:121]\n"
        "s_nop 0\n"
        "v_cvt_pk_bf16_f32 v86, v74, v75\n"
        "v_cvt_pk_bf16_f32 v87, v76, v77\n"
        "v_mfma_f32_16x16x32_bf16 v[118:121], v[208:211], a[80:83], v[118:121]\n"
        "s_nop 1\n"
        "v_mfma_f32_16x16x32_bf16 v[74:77], v[208:211], a[92:95], 0\n"
        "s_nop 0\n"
        "ds_read_b128 a[164:167], v222 offset:5120\n"
        "s_nop 0\n"
        "v_mfma_f32_16x16x32_bf16 v[74:77], v[202:205], a[88:91], v[74:77]\n"
        "s_nop 0\n"
        "v_cvt_pk_bf16_f32 v42, v118, v119\n"
        "v_cvt_pk_bf16_f32 v43, v120, v121\n"
        "v_mfma_f32_16x16x32_bf16 v[74:77], v[196:199], a[84:87], v[74:77]\n"
        "s_nop 0\n"
        "v_mfma_f32_16x16x32_bf16 v[118:121], v[58:61], v[152:155], 0\n"
        "s_nop 2\n"
        "v_mfma_f32_16x16x32_bf16 v[118:121], v[64:67], v[112:115], v[118:121]\n"
        "s_nop 0\n"
        "ds_read_b128 a[168:171], v222 offset:6144\n"
        "v_cvt_pk_bf16_f32 v46, v74, v75\n"
        "v_mfma_f32_16x16x32_bf16 v[118:121], v[70:73], v[84:87], v[118:121]\n"
        "s_waitcnt vmcnt(0)\n"
        "v_cvt_pk_bf16_f32 v47, v76, v77\n"
        "v_mfma_f32_16x16x32_bf16 v[74:77], v[208:211], a[104:107], 0\n"
        "s_nop 0\n"
        "ds_read_b128 v[126:129], v9\n"
        "v_mfma_f32_16x16x32_bf16 v[74:77], v[202:205], a[100:103], v[74:77]\n"
        "s_nop 0\n"
        "ds_read_b128 v[132:135], v10\n"
        "v_cndmask_b32_dpp v186, v118, v158, vcc row_ror:4 row_mask:0xf bank_mask:0xf\n"
        "v_mfma_f32_16x16x32_bf16 v[74:77], v[196:199], a[96:99], v[74:77]\n"
        "ds_read_b128 v[138:141], v11\n"
        "v_cndmask_b32_dpp v151, v119, v159, vcc row_ror:4 row_mask:0xf bank_mask:0xf\n"
        "v_mfma_f32_16x16x32_bf16 v[82:85], v[196:199], a[108:111], 0\n"
        "ds_read_b128 v[106:109], v9 offset:3072\n"
        "v_cndmask_b32_dpp v81, v120, v160, vcc row_ror:4 row_mask:0xf bank_mask:0xf\n"
        "v_mfma_f32_16x16x32_bf16 v[82:85], v[202:205], a[112:115], v[82:85]\n"
        "ds_read_b128 v[112:115], v10 offset:3072\n"
        "v_cndmask_b32_dpp v91, v121, v161, vcc row_ror:4 row_mask:0xf bank_mask:0xf\n"
        "v_mfma_f32_16x16x32_bf16 v[82:85], v[208:211], a[116:119], v[82:85]\n"
        "ds_read_b128 v[118:121], v11 offset:3072\n"
        "v_mul_f32_dpp v186, v78, v186 quad_perm:[0,0,0,0] row_mask:0xf bank_mask:0xf\n"
        "v_mfma_f32_16x16x32_bf16 v[228:231], v[208:211], a[128:131], 0\n"
        "ds_read_b128 v[146:149], v9 offset:6144\n"
        "v_mul_f32_dpp v151, v78, v151 quad_perm:[1,1,1,1] row_mask:0xf bank_mask:0xf\n"
        "v_mfma_f32_16x16x32_bf16 v[228:231], v[202:205], a[124:127], v[228:231]\n"
        "ds_read_b128 v[152:155], v10 offset:6144\n"
        "v_mul_f32_dpp v81, v78, v81 quad_perm:[2,2,2,2] row_mask:0xf bank_mask:0xf\n"
        "v_mfma_f32_16x16x32_bf16 v[228:231], v[196:199], a[120:123], v[228:231]\n"
        "ds_read_b128 v[158:161], v11 offset:6144\n"
        "v_mul_f32_dpp v91, v78, v91 quad_perm:[3,3,3,3] row_mask:0xf bank_mask:0xf\n"
        "v_mfma_f32_16x16x32_bf16 v[250:253], v[196:199], a[132:135], 0\n"
        "ds_read_b128 a[172:175], v222 offset:7168\n"
        "v_max3_f32 v45, v186, v151, v81\n"
        "v_mfma_f32_16x16x32_bf16 v[250:253], v[202:205], a[136:139], v[250:253]\n"
        "s_waitcnt lgkmcnt(7)\n"
        "ds_read_b128 a[176:179], v222 offset:8192\n"
        "v_max_f32 v45, v45, v91\n"
        "v_mfma_f32_16x16x32_bf16 v[250:253], v[208:211], a[140:143], v[250:253]\n"
        "v_mul_f32 v45, v45, v78\n"
        "v_fma_f32 v186, v186, v78, -v45\n"
        "v_mfma_f32_16x16x32_bf16 v[86:89], v[126:129], v[126:129], 0\n"
        "s_mov_b32 s28, s89\n"
        "v_fma_f32 v151, v151, v78, -v45\n"
        "v_fma_f32 v81, v81, v78, -v45\n"
        "v_mfma_f32_16x16x32_bf16 v[86:89], v[132:135], v[132:135], v[86:89]\n"
        "s_add_u32 s89, s86, s51\n"
        "v_fma_f32 v91, v91, v78, -v45\n"
        "v_cvt_pk_bf16_f32 v36, v74, v75\n"
        "v_mfma_f32_16x16x32_bf16 v[86:89], v[138:141], v[138:141], v[86:89]\n"
        "s_mov_b32 s30, s29\n"
        "v_exp_f32 v186, v186\n"
        "v_mfma_f32_16x16x32_bf16 v[50:53], v[92:95], v[138:141], 0\n"
        "s_add_u32 s29, s29, 1\n"
        "v_exp_f32 v151, v151\n"
        "v_mfma_f32_16x16x32_bf16 v[50:53], v[100:103], v[132:135], v[50:53]\n"
        "s_mov_b32 s86, s87\n"
        "v_exp_f32 v81, v81\n"
        "v_mfma_f32_16x16x32_bf16 v[50:53], v[96:99], v[126:129], v[50:53]\n"
        "s_add_u32 s88, s88, 32\n"
        "v_exp_f32 v91, v91\n"
        "v_mfma_f32_16x16x32_bf16 v[214:217], a[0:3], v[126:129], 0\n"
        "s_add_u32 s87, s87, 43008\n"
        "v_add_f32 v75, v186, v151\n"
        "v_add_f32 v74, v81, v91\n"
        "v_mfma_f32_16x16x32_bf16 v[214:217], a[4:7], v[132:135], v[214:217]\n"
        "s_cmp_ge_u32 s88, 73\n"
        "v_add_f32 v75, v75, v74\n"
        "v_cvt_pk_bf16_f32 v37, v76, v77\n"
        "v_mfma_f32_16x16x32_bf16 v[214:217], a[8:11], v[138:141], v[214:217]\n"
        "s_cselect_b32 s92, 588672, 0\n"
        "v_rcp_f32 v75, v75\n"
        "v_mfma_f32_16x16x32_bf16 v[224:227], a[20:23], v[138:141], 0\n"
        "s_cselect_b32 s94, 73, 0\n"
        "ds_read_b64 v[44:45], v27\n"
        "ds_read_b64 v[38:39], v27\n"
        "v_mfma_f32_16x16x32_bf16 v[224:227], a[16:19], v[132:135], v[224:227]\n"
        "s_sub_u32 s88, s88, s94\n"
        "ds_read_b64 v[32:33], v27\n"
        "v_mul_f32 v186, v186, v75\n"
        "v_mfma_f32_16x16x32_bf16 v[224:227], a[12:15], v[126:129], v[224:227]\n"
        "s_add_u32 s87, s87, s92\n"
        "ds_read_b32 v63, v63\n"
        "v_mul_f32 v151, v151, v75\n"
        "v_mfma_f32_16x16x32_bf16 v[218:221], a[24:27], v[126:129], 0\n"
        "s_cmp_gt_i32 s85, 0\n"
        "v_mul_f32 v81, v81, v75\n"
        "v_mul_f32 v91, v91, v75\n"
        "v_mfma_f32_16x16x32_bf16 v[218:221], a[28:31], v[132:135], v[218:221]\n"
        "s_cselect_b32 s94, s87, 0\n"
        "v_mul_f32_dpp v186, v78, v186 quad_perm:[0,0,0,0] row_mask:0xf bank_mask:0xf\n"
        "v_mul_f32_dpp v151, v78, v151 quad_perm:[1,1,1,1] row_mask:0xf bank_mask:0xf\n"
        "v_mfma_f32_16x16x32_bf16 v[218:221], a[32:35], v[138:141], v[218:221]\n"
        "s_sub_u32 s85, s85, 1\n"
        "v_mul_f32_dpp v81, v78, v81 quad_perm:[2,2,2,2] row_mask:0xf bank_mask:0xf\n"
        "v_mul_f32_dpp v91, v78, v91 quad_perm:[3,3,3,3] row_mask:0xf bank_mask:0xf\n"
        "v_mfma_f32_16x16x32_bf16 v[76:79], a[44:47], v[138:141], 0\n"
        "s_waitcnt lgkmcnt(9)\n"
        "v_cvt_pk_bf16_f32 v75, v186, v151\n"
        "v_cvt_pk_bf16_f32 v74, v81, v91\n"
        "v_mfma_f32_16x16x32_bf16 v[76:79], a[40:43], v[132:135], v[76:79]\n"
        "s_add_u32 s38, s36, s94\n"
        "v_and_b32 v194, v25, v75\n"
        "v_and_b32 v195, v25, v74\n"
        "v_mfma_f32_16x16x32_bf16 v[76:79], a[36:39], v[126:129], v[76:79]\n"
        "s_addc_u32 s39, s37, 0\n"
        "v_mov_b64 v[200:201], v[194:195]\n"
        "v_mov_b64 v[206:207], v[194:195]\n"
        "v_mfma_f32_16x16x32_bf16 v[52:55], a[48:51], v[126:129], 0\n"
        "s_waitcnt lgkmcnt(1)\n"
        "v_cvt_pk_bf16_f32 v40, v82, v83\n"
        "v_cvt_pk_bf16_f32 v41, v84, v85\n"
        "v_mfma_f32_16x16x32_bf16 v[52:55], a[52:55], v[132:135], v[52:55]\n"
        "s_waitcnt lgkmcnt(0)\n"
        "v_readfirstlane_b32 s83, v63\n"
        "v_cvt_pk_bf16_f32 v30, v228, v229\n"
        "v_mfma_f32_16x16x32_bf16 v[52:55], a[56:59], v[138:141], v[52:55]\n"
        "s_waitcnt lgkmcnt(0)\n"
        "v_cvt_pk_bf16_f32 v31, v230, v231\n"
        "v_mov_b64 v[212:213], v[194:195]\n"
        "v_mfma_f32_16x16x32_bf16 v[82:85], a[68:71], v[138:141], 0\n"
        "v_cvt_pk_bf16_f32 v34, v250, v251\n"
        "v_cvt_pk_bf16_f32 v35, v252, v253\n"
        "v_mfma_f32_16x16x32_bf16 v[82:85], a[64:67], v[132:135], v[82:85]\n"
        "ds_read_b128 a[180:183], v222 offset:9216\n"
        "v_and_b32_dpp v56, v75, v25 row_ror:12 row_mask:0xf bank_mask:0xf\n"
        "v_mfma_f32_16x16x32_bf16 v[82:85], a[60:63], v[126:129], v[82:85]\n"
        "ds_read_b128 a[184:187], v222 offset:10240\n"
        "v_and_b32_dpp v57, v74, v25 row_ror:12 row_mask:0xf bank_mask:0xf\n"
        "v_mfma_f32_16x16x32_bf16 v[250:253], v[42:45], v[194:197], 0\n"
        "ds_read_b128 a[188:191], v222 offset:11264\n"
        "v_mov_b64 v[62:63], v[56:57]\n"
        "v_mfma_f32_16x16x32_bf16 v[230:233], v[44:47], v[198:201], 0\n"
        "s_nop 0\n"
        "ds_read_b128 a[192:195], v222 offset:12288\n"
        "v_mov_b64 v[68:69], v[56:57]\n"
        "v_mfma_f32_16x16x32_bf16 v[46:49], v[58:61], a[72:75], 0\n"
        "s_nop 0\n"
        "v_cvt_pk_bf16_f32 v228, v250, v251\n"
        "v_cvt_pk_bf16_f32 v250, v252, v253\n"
        "v_mfma_f32_16x16x32_bf16 v[46:49], v[64:67], a[76:79], v[46:49]\n"
        "v_cvt_pk_bf16_f32 v229, v232, v233\n"
        "v_cvt_pk_bf16_f32 v194, v230, v231\n"
        "v_mfma_f32_16x16x32_bf16 v[46:49], v[70:73], a[80:83], v[46:49]\n"
        "s_add_u32 m0, s52, 0\n"
        "ds_read_b128 a[196:199], v222 offset:13312\n"
        "v_mov_b64 v[74:75], v[56:57]\n"
        "v_mfma_f32_16x16x32_bf16 v[230:233], v[70:73], a[92:95], 0\n"
        "ds_read_b128 a[200:203], v222 offset:14336\n"
        "v_cvt_pk_bf16_f32 v198, v214, v215\n"
        "v_mfma_f32_16x16x32_bf16 v[230:233], v[64:67], a[88:91], v[230:233]\n"
        "global_load_lds_dwordx4 v0, s[38:39]\n"
        "v_cvt_pk_bf16_f32 v42, v46, v47\n"
        "v_mfma_f32_16x16x32_bf16 v[230:233], v[58:61], a[84:87], v[230:233]\n"
        "ds_read_b128 a[204:207], v222 offset:15360\n"
        "v_cvt_pk_bf16_f32 v43, v48, v49\n"
        "v_mfma_f32_16x16x32_bf16 v[166:169], v[36:39], v[200:203], 0\n"
        "ds_read_b128 a[208:211], v222 offset:16384\n"
        "v_cvt_pk_bf16_f32 v199, v216, v217\n"
        "v_mfma_f32_16x16x32_bf16 v[214:217], v[38:41], v[204:207], 0\n"
        "s_nop 0\n"
        "ds_read_b128 a[212:215], v222 offset:17408\n"
        "v_cvt_pk_bf16_f32 v46, v230, v231\n"
        "v_mfma_f32_16x16x32_bf16 v[202:205], v[58:61], a[96:99], 0\n"
        "s_nop 0\n"
        "v_cvt_pk_bf16_f32 v251, v166, v167\n"
        "v_cvt_pk_bf16_f32 v195, v168, v169\n"
        "v_mfma_f32_16x16x32_bf16 v[202:205], v[64:67], a[100:103], v[202:205]\n"
        "v_cvt_pk_bf16_f32 v230, v214, v215\n"
        "v_cvt_pk_bf16_f32 v252, v216, v217\n"
        "v_mfma_f32_16x16x32_bf16 v[202:205], v[70:73], a[104:107], v[202:205]\n"
        "v_cvt_pk_bf16_f32 v47, v232, v233\n"
        "v_cvt_pk_bf16_f32 v200, v224, v225\n"
        "v_mfma_f32_16x16x32_bf16 v[214:217], v[70:73], a[116:119], 0\n"
        "ds_read_b128 a[216:219], v255\n"
        "v_cvt_pk_bf16_f32 v201, v226, v227\n"
        "v_mfma_f32_16x16x32_bf16 v[214:217], v[64:67], a[112:115], v[214:217]\n"
        "s_nop 0\n"
        "global_load_lds_dwordx4 v1, s[38:39] offset:1024\n"
        "v_cvt_pk_bf16_f32 v36, v202, v203\n"
        "v_mfma_f32_16x16x32_bf16 v[214:217], v[58:61], a[108:111], v[214:217]\n"
        "v_cvt_pk_bf16_f32 v37, v204, v205\n"
        "v_cvt_pk_bf16_f32 v226, v218, v219\n"
        "v_mfma_f32_16x16x32_bf16 v[222:225], v[30:33], v[206:209], 0\n"
        "s_nop 0\n"
        "ds_read_b128 a[220:223], v255 offset:1024\n"
        "v_cvt_pk_bf16_f32 v227, v220, v221\n"
        "v_mfma_f32_16x16x32_bf16 v[206:209], v[32:35], v[210:213], 0\n"
        "s_nop 0\n"
        "v_cvt_pk_bf16_f32 v40, v214, v215\n"
        "v_cvt_pk_bf16_f32 v41, v216, v217\n"
        "v_mfma_f32_16x16x32_bf16 v[202:205], v[58:61], a[120:123], 0\n"
        "v_cvt_pk_bf16_f32 v196, v222, v223\n"
        "v_cvt_pk_bf16_f32 v231, v224, v225\n"
        "v_mfma_f32_16x16x32_bf16 v[202:205], v[64:67], a[124:127], v[202:205]\n"
        "ds_write_b128 v12, v[228:231]\n"
        "v_mfma_f32_16x16x32_bf16 v[202:205], v[70:73], a[128:131], v[202:205]\n"
        "v_cvt_pk_bf16_f32 v253, v206, v207\n"
        "v_cvt_pk_bf16_f32 v197, v208, v209\n"
        "v_mfma_f32_16x16x32_bf16 v[208:211], v[70:73], a[140:143], 0\n"
        "ds_write_b128 v12, v[250:253] offset:1024\n"
        "v_mfma_f32_16x16x32_bf16 v[208:211], v[64:67], a[136:139], v[208:211]\n"
        "ds_write_b128 v12, v[194:197] offset:2048\n"
        "v_mfma_f32_16x16x32_bf16 v[208:211], v[58:61], a[132:135], v[208:211]\n"
        "global_load_lds_dwordx4 v2, s[38:39] offset:2048\n"
        "v_cvt_pk_bf16_f32 v30, v202, v203\n"
        "v_mfma_f32_16x16x32_bf16 v[218:221], v[228:231], v[228:231], 0\n"
        "v_cvt_pk_bf16_f32 v228, v76, v77\n"
        "v_cvt_pk_bf16_f32 v229, v78, v79\n"
        "v_mfma_f32_16x16x32_bf16 v[218:221], v[250:253], v[250:253], v[218:221]\n"
        "v_cvt_pk_bf16_f32 v31, v204, v205\n"
        "v_cvt_pk_bf16_f32 v76, v52, v53\n"
        "v_mfma_f32_16x16x32_bf16 v[218:221], v[194:197], v[194:197], v[218:221]\n"
        "v_cvt_pk_bf16_f32 v77, v54, v55\n"
        "v_cvt_pk_bf16_f32 v78, v82, v83\n"
        "v_mfma_f32_16x16x32_bf16 v[202:205], v[106:109], v[106:109], 0\n"
        "v_cvt_pk_bf16_f32 v79, v84, v85\n"
        "v_cvt_pk_bf16_f32 v34, v208, v209\n"
        "v_mfma_f32_16x16x32_bf16 v[202:205], v[112:115], v[112:115], v[202:205]\n"
        "v_cvt_pk_bf16_f32 v35, v210, v211\n"
        "v_fmac_f32_dpp v90, v254, v186 quad_perm:[0,0,0,0] row_mask:0xf bank_mask:0xf\n"
        "v_mfma_f32_16x16x32_bf16 v[202:205], v[118:121], v[118:121], v[202:205]\n"
        "ds_read_b128 a[224:227], v255 offset:2048\n"
        "v_fmac_f32_dpp v90, v254, v151 quad_perm:[1,1,1,1] row_mask:0xf bank_mask:0xf\n"
        "v_mfma_f32_16x16x32_bf16 v[208:211], v[92:95], v[118:121], 0\n"
        "global_load_lds_dwordx4 v3, s[38:39] offset:3072\n"
        "v_cndmask_b32_e64 v186, v218, v219, s[56:57]\n"
        "v_mfma_f32_16x16x32_bf16 v[208:211], v[100:103], v[112:115], v[208:211]\n"
        "ds_read_b128 a[228:231], v255 offset:3088\n"
        "v_fmac_f32_dpp v90, v254, v81 quad_perm:[2,2,2,2] row_mask:0xf bank_mask:0xf\n"
        "v_mfma_f32_16x16x32_bf16 v[208:211], v[96:99], v[106:109], v[208:211]\n"
        "ds_read_b128 a[232:235], v255 offset:4112\n"
        "v_cndmask_b32_e64 v186, v186, v220, s[58:59]\n"
        "v_mfma_f32_16x16x32_bf16 v[82:85], v[42:45], v[56:59], 0\n"
        "ds_read_b128 a[236:239], v255 offset:5136\n"
        "v_cndmask_b32_e64 v186, v186, v221, s[60:61]\n"
        "v_mfma_f32_16x16x32_bf16 v[194:197], v[44:47], v[60:63], 0\n"
        "s_nop 0\n"
        "ds_read_b128 a[240:243], v255 offset:6176\n"
        "v_fmac_f32_dpp v90, v254, v91 quad_perm:[3,3,3,3] row_mask:0xf bank_mask:0xf\n"
        "v_mfma_f32_16x16x32_bf16 v[210:213], v[36:39], v[62:65], 0\n"
        "s_nop 0\n"
        "v_cvt_pk_bf16_f32 v54, v82, v83\n"
        "v_cvt_pk_bf16_f32 v58, v84, v85\n"
        "v_mfma_f32_16x16x32_bf16 v[60:63], v[38:41], v[66:69], 0\n"
        "s_add_u32 m0, s52, 4096\n"
        "v_cvt_pk_bf16_f32 v64, v194, v195\n"
        "v_cvt_pk_bf16_f32 v55, v196, v197\n"
        "v_mfma_f32_16x16x32_bf16 v[194:197], v[30:33], v[68:71], 0\n"
        "v_cvt_pk_bf16_f32 v59, v210, v211\n"
        "v_cvt_pk_bf16_f32 v65, v212, v213\n"
        "v_mfma_f32_16x16x32_bf16 v[68:71], v[32:35], v[72:75], 0\n"
        "global_load_lds_dwordx4 v4, s[38:39]\n"
        "v_cvt_pk_bf16_f32 v56, v60, v61\n"
        "v_mfma_f32_16x16x32_bf16 v[34:37], v[126:129], v[198:201], 0\n"
        "s_nop 0\n"
        "v_cvt_pk_bf16_f32 v60, v62, v63\n"
        "v_cvt_pk_bf16_f32 v66, v194, v195\n"
        "v_mfma_f32_16x16x32_bf16 v[34:37], v[132:135], v[226:229], v[34:37]\n"
        "v_cvt_pk_bf16_f32 v57, v196, v197\n"
        "v_cvt_pk_bf16_f32 v61, v68, v69\n"
        "v_mfma_f32_16x16x32_bf16 v[34:37], v[138:141], v[76:79], v[34:37]\n"
        "ds_write_b128 v12, v[54:57] offset:3072\n"
        "v_mfma_f32_16x16x32_bf16 v[72:75], a[8:11], v[118:121], 0\n"
        "ds_write_b128 v12, v[58:61] offset:4096\n"
        "v_mfma_f32_16x16x32_bf16 v[72:75], a[4:7], v[112:115], v[72:75]\n"
        "v_cvt_pk_bf16_f32 v67, v70, v71\n"
        "v_mul_f32 v78, s80, v90\n"
        "v_mfma_f32_16x16x32_bf16 v[72:75], a[0:3], v[106:109], v[72:75]\n"
        "ds_write_b128 v12, v[64:67] offset:5120\n"
        "v_mfma_f32_16x16x32_bf16 v[68:71], v[54:57], v[54:57], 0\n"
        "s_nop 0\n"
        "ds_read_b128 a[244:247], v255 offset:7200\n"
        "ds_read_b128 a[248:251], v255 offset:8224\n"
        "v_mfma_f32_16x16x32_bf16 v[68:71], v[58:61], v[58:61], v[68:71]\n"
        "s_nop 0\n"
        "global_load_lds_dwordx4 v5, s[38:39] offset:1024\n"
        "v_cvt_pk_bf16_f32 v196, v72, v73\n"
        "v_mfma_f32_16x16x32_bf16 v[68:71], v[64:67], v[64:67], v[68:71]\n"
        "v_cvt_pk_bf16_f32 v197, v74, v75\n"
        "v_cndmask_b32_e64 v222, v202, v203, s[56:57]\n"
        "v_mfma_f32_16x16x32_bf16 v[64:67], a[20:23], v[118:121], 0\n"
        "s_nop 0\n"
        "ds_read_b128 a[252:255], v255 offset:9264\n"
        "ds_read_b128 v[72:75], v255 offset:10288\n"
        "v_mfma_f32_16x16x32_bf16 v[64:67], a[16:19], v[112:115], v[64:67]\n"
        "s_nop 0\n"
        "v_cndmask_b32_e64 v58, v68, v69, s[56:57]\n"
        "v_cndmask_b32_e64 v58, v58, v70, s[58:59]\n"
        "v_mfma_f32_16x16x32_bf16 v[64:67], a[12:15], v[106:109], v[64:67]\n"
        "v_cndmask_b32_e64 v58, v58, v71, s[60:61]\n"
        "v_cndmask_b32_e64 v222, v222, v204, s[58:59]\n"
        "v_mfma_f32_16x16x32_bf16 v[60:63], a[24:27], v[106:109], 0\n"
        "v_cndmask_b32_dpp v186, v58, v186, vcc row_ror:4 row_mask:0xf bank_mask:0xf\n"
        "v_fma_f32 v78, -v78, v90, v186\n"
        "v_mfma_f32_16x16x32_bf16 v[60:63], a[28:31], v[112:115], v[60:63]\n"
        "v_max_f32 v78, 0, v78\n"
        "v_fmaak_f32 v78, v28, v78, 0x3727c5ac\n"
        "v_mfma_f32_16x16x32_bf16 v[60:63], a[32:35], v[118:121], v[60:63]\n"
        "v_sub_f32 v186, 0x47c00000, v186\n"
        "v_bfi_b32 v78, s84, v186, v78\n"
        "v_mfma_f32_16x16x32_bf16 v[56:59], a[44:47], v[118:121], 0\n"
        "global_load_lds_dwordx4 v6, s[38:39] offset:2048\n"
        "v_cvt_pk_bf16_f32 v198, v64, v65\n"
        "v_mfma_f32_16x16x32_bf16 v[56:59], a[40:43], v[112:115], v[56:59]\n"
        "v_rsq_f32 v64, v78\n"
        "v_mfma_f32_16x16x32_bf16 v[56:59], a[36:39], v[106:109], v[56:59]\n"
        "v_cvt_f16_f32 v64, v64\n"
        "v_cvt_pk_bf16_f32 v199, v66, v67\n"
        "v_mfma_f32_16x16x32_bf16 v[76:79], a[48:51], v[106:109], 0\n"
        "ds_write_b16 v18, v64\n"
        "v_mfma_f32_16x16x32_bf16 v[76:79], a[52:55], v[112:115], v[76:79]\n"
        "ds_write_b16 v19, v64\n"
        "v_mfma_f32_16x16x32_bf16 v[76:79], a[56:59], v[118:121], v[76:79]\n"
        "ds_read_b128 v[64:67], v255 offset:11312\n"
        "v_cvt_pk_bf16_f32 v200, v60, v61\n"
        "v_mfma_f32_16x16x32_bf16 v[226:229], a[68:71], v[118:121], 0\n"
        "ds_read_b128 v[68:71], v255 offset:12352\n"
        "v_cvt_pk_bf16_f32 v201, v62, v63\n"
        "v_mfma_f32_16x16x32_bf16 v[226:229], a[64:67], v[112:115], v[226:229]\n"
        "ds_read_b128 v[60:63], v255 offset:13376\n"
        "v_cvt_pk_bf16_f32 v202, v56, v57\n"
        "v_mfma_f32_16x16x32_bf16 v[226:229], a[60:63], v[106:109], v[226:229]\n"
        "global_load_lds_dwordx4 v7, s[38:39] offset:3072\n"
        "v_cvt_pk_bf16_f32 v203, v58, v59\n"
        "v_mfma_f32_16x16x32_bf16 v[82:85], v[146:149], v[146:149], 0\n"
        "ds_read_b128 v[56:59], v255 offset:14400\n"
        "v_cvt_pk_bf16_f32 v210, v76, v77\n"
        "v_mfma_f32_16x16x32_bf16 v[82:85], v[152:155], v[152:155], v[82:85]\n"
        "ds_read_b128 v[52:55], v255 offset:15440\n"
        "v_cvt_pk_bf16_f32 v211, v78, v79\n"
        "v_mfma_f32_16x16x32_bf16 v[82:85], v[158:161], v[158:161], v[82:85]\n"
        "ds_read_b128 v[76:79], v255 offset:16464\n"
        "v_cvt_pk_bf16_f32 v212, v226, v227\n"
        "v_mfma_f32_16x16x32_bf16 v[218:221], v[92:95], v[158:161], 0\n"
        "s_waitcnt lgkmcnt(6)\n"
        "v_cvt_pk_bf16_f32 v213, v228, v229\n"
        "v_cndmask_b32_e64 v222, v222, v205, s[60:61]\n"
        "v_mfma_f32_16x16x32_bf16 v[218:221], v[100:103], v[152:155], v[218:221]\n"
        "s_barrier\n"
        "v_cndmask_b32_e64 v204, v82, v83, s[56:57]\n"
        "v_cndmask_b32_e64 v204, v204, v84, s[58:59]\n"
        "v_mfma_f32_16x16x32_bf16 v[218:221], v[96:99], v[146:149], v[218:221]\n"
        "s_add_u32 m0, s52, 8192\n"
        "v_cndmask_b32_e64 v204, v204, v85, s[60:61]\n"
        "v_cndmask_b32_e64 v205, v86, v87, s[56:57]\n"
        "v_mfma_f32_16x16x32_bf16 v[226:229], v[106:109], v[196:199], 0\n"
        "ds_read_b128 v[84:87], v255 offset:17488\n"
        "v_cndmask_b32_e64 v205, v205, v88, s[58:59]\n"
        "v_mfma_f32_16x16x32_bf16 v[226:229], v[112:115], v[200:203], v[226:229]\n"
        "global_load_lds_dwordx4 v8, s[38:39]\n"
        "v_cndmask_b32_dpp v186, v218, v208, vcc row_ror:4 row_mask:0xf bank_mask:0xf\n"
        "v_mfma_f32_16x16x32_bf16 v[226:229], v[118:121], v[210:213], v[226:229]\n"
        "v_cndmask_b32_e64 v205, v205, v89, s[60:61]\n"
        "v_cndmask_b32_dpp v222, v204, v222, vcc row_ror:4 row_mask:0xf bank_mask:0xf\n"
        "v_mfma_f32_16x16x32_bf16 v[196:199], a[8:11], v[158:161], 0\n"
        "v_cndmask_b32_dpp v186, v186, v50, vcc row_ror:4 row_mask:0xf bank_mask:0xf\n"
        "v_cndmask_b32_dpp v205, v222, v205, vcc row_ror:4 row_mask:0xf bank_mask:0xf\n"
        "v_mfma_f32_16x16x32_bf16 v[196:199], a[4:7], v[152:155], v[196:199]\n"
        "v_mul_f32 v255, s80, v186\n"
        "v_fma_f32 v255, -v255, v186, v205\n"
        "v_mfma_f32_16x16x32_bf16 v[196:199], a[0:3], v[146:149], v[196:199]\n"
        "s_nop 0\n"
        "v_max_f32 v255, 0, v255\n"
        "v_fmaak_f32 v255, v28, v255, 0x3727c5ac\n"
        "v_mfma_f32_16x16x32_bf16 v[202:205], a[12:15], v[146:149], 0\n"
        "s_nop 0\n"
        "v_rsq_f32 v151, v255\n"
        "v_mfma_f32_16x16x32_bf16 v[202:205], a[16:19], v[152:155], v[202:205]\n"
        "s_nop 0\n"
        "v_cvt_pk_bf16_f32 v40, v196, v197\n"
        "v_cvt_pk_bf16_f32 v41, v198, v199\n"
        "v_mfma_f32_16x16x32_bf16 v[202:205], a[20:23], v[158:161], v[202:205]\n"
        "s_waitcnt lgkmcnt(0)\n"
        "v_cndmask_b32_dpp v255, v219, v209, vcc row_ror:4 row_mask:0xf bank_mask:0xf\n"
        "v_mfma_f32_16x16x32_bf16 v[208:211], a[32:35], v[158:161], 0\n"
        "s_nop 0\n"
        "v_cndmask_b32_dpp v255, v255, v51, vcc row_ror:4 row_mask:0xf bank_mask:0xf\n"
        "s_nop 0\n"
        "v_mfma_f32_16x16x32_bf16 v[208:211], a[28:31], v[152:155], v[208:211]\n"
        "s_nop 0\n"
        "v_cvt_pk_bf16_f32 v42, v202, v203\n"
        "v_cvt_pk_bf16_f32 v43, v204, v205\n"
        "v_mfma_f32_16x16x32_bf16 v[208:211], a[24:27], v[146:149], v[208:211]\n"
        "s_nop 1\n"
        "v_mfma_f32_16x16x32_bf16 v[196:199], a[36:39], v[146:149], 0\n"
        "s_nop 2\n"
        "v_mfma_f32_16x16x32_bf16 v[196:199], a[40:43], v[152:155], v[196:199]\n"
        "s_nop 0\n"
        "v_cvt_pk_bf16_f32 v88, v208, v209\n"
        "v_cvt_pk_bf16_f32 v89, v210, v211\n"
        "v_mfma_f32_16x16x32_bf16 v[196:199], a[44:47], v[158:161], v[196:199]\n"
        "s_nop 0\n"
        "v_mfma_f32_16x16x32_bf16 v[202:205], a[56:59], v[158:161], 0\n"
        "s_nop 0\n"
        "ds_read_b128 v[210:213], v13\n"
        "ds_read_b128 v[214:217], v13 offset:1024\n"
        "v_mfma_f32_16x16x32_bf16 v[202:205], a[52:55], v[152:155], v[202:205]\n"
        "s_nop 0\n"
        "ds_read_b128 v[218:221], v13 offset:2048\n"
        "v_cvt_pk_bf16_f32 v90, v196, v197\n"
        "v_mfma_f32_16x16x32_bf16 v[202:205], a[48:51], v[146:149], v[202:205]\n"
        "v_mov_b64 v[224:225], 0\n"
        "s_mov_b64 exec, 1\n"
        "buffer_atomic_add v224, off, s[16:19], 0 sc0\n"
        "buffer_atomic_add v225, off, s[16:19], s15 offset:4 sc0\n"
        "s_mov_b64 exec, -1\n"
        "v_mfma_f32_16x16x32_bf16 v[250:253], a[60:63], v[146:149], 0\n"
        "v_cvt_pk_bf16_f32 v91, v198, v199\n"
        "v_mfma_f32_16x16x32_bf16 v[250:253], a[64:67], v[152:155], v[250:253]\n"
        "v_cvt_pk_bf16_f32 v234, v202, v203\n"
        "v_cvt_pk_bf16_f32 v235, v204, v205\n"
        "v_mfma_f32_16x16x32_bf16 v[250:253], a[68:71], v[158:161], v[250:253]\n"
        "v_mfma_f32_16x16x32_bf16 v[166:169], v[138:141], a[80:83], 0\n"
        "v_mfma_f32_16x16x32_bf16 v[166:169], v[132:135], a[76:79], v[166:169]\n"
        "ds_read_b64_tr_b16 v[48:49], v187\n"
        "ds_read_b64_tr_b16 v[50:51], v187 offset:3072\n"
        "v_mfma_f32_16x16x32_bf16 v[166:169], v[126:129], a[72:75], v[166:169]\n"
        "s_waitcnt lgkmcnt(2)\n"
        "ds_read_b64_tr_b16 v[230:231], v187 offset:6144\n"
        "ds_read_b64_tr_b16 v[232:233], v187 offset:9216\n"
        "v_mfma_f32_16x16x32_bf16 v[178:181], v[126:129], a[84:87], 0\n"
        "ds_read_b64_tr_b16 v[80:81], v187 offset:12288\n"
        "ds_read_b128 v[206:209], v13 offset:3072\n"
        "v_mfma_f32_16x16x32_bf16 v[178:181], v[132:135], a[88:91], v[178:181]\n"
        "ds_read_b128 v[202:205], v13 offset:4096\n"
        "ds_read_b128 v[194:197], v13 offset:5120\n"
        "v_mfma_f32_16x16x32_bf16 v[178:181], v[138:141], a[92:95], v[178:181]\n"
        "ds_read_b128 v[198:201], v20\n"
        "ds_read_b64 v[222:223], v21\n"
        "v_mfma_f32_16x16x32_bf16 v[162:165], v[146:149], v[40:43], 0\n"
        "ds_read_b64_tr_b16 v[82:83], v187 offset:15360\n"
        "v_cvt_pk_bf16_f32 v236, v250, v251\n"
        "v_mfma_f32_16x16x32_bf16 v[162:165], v[152:155], v[88:91], v[162:165]\n"
        "v_cvt_pk_bf16_f32 v237, v252, v253\n"
        "v_cvt_pk_bf16_f32 v42, v166, v167\n"
        "s_nop 0\n"
        "v_mfma_f32_16x16x32_bf16 v[162:165], v[158:161], v[234:237], v[162:165]\n"
        "s_nop 0\n"
        "v_cvt_pk_bf16_f32 v43, v168, v169\n"
        "v_cvt_pk_bf16_f32 v46, v178, v179\n"
        "v_mfma_f32_16x16x32_bf16 v[234:237], a[152:155], v[218:221], 0\n"
        "s_nop 0\n"
        "v_cvt_pk_bf16_f32 v47, v180, v181\n"
        "v_mfma_f32_16x16x32_bf16 v[234:237], a[148:151], v[214:217], v[234:237]\n"
        "s_nop 0\n"
        "v_cndmask_b32_dpp v251, v162, v226, vcc row_ror:4 row_mask:0xf bank_mask:0xf\n"
        "v_cndmask_b32_dpp v253, v163, v227, vcc row_ror:4 row_mask:0xf bank_mask:0xf\n"
        "v_mfma_f32_16x16x32_bf16 v[234:237], a[144:147], v[210:213], v[234:237]\n"
        "v_cndmask_b32_dpp v254, v164, v228, vcc row_ror:4 row_mask:0xf bank_mask:0xf\n"
        "v_cndmask_b32_dpp v252, v165, v229, vcc row_ror:4 row_mask:0xf bank_mask:0xf\n"
        "v_mfma_f32_16x16x32_bf16 v[162:165], a[156:159], v[210:213], 0\n"
        "v_cndmask_b32_dpp v251, v251, v34, vcc row_ror:4 row_mask:0xf bank_mask:0xf\n"
        "v_cndmask_b32_dpp v253, v253, v35, vcc row_ror:4 row_mask:0xf bank_mask:0xf\n"
        "v_mfma_f32_16x16x32_bf16 v[162:165], a[160:163], v[214:217], v[162:165]\n"
        "s_waitcnt lgkmcnt(1)\n"
        "ds_read_b64_tr_b16 v[166:167], v187 offset:8\n"
        "ds_read_b64_tr_b16 v[168:169], v187 offset:3080\n"
        "v_mfma_f32_16x16x32_bf16 v[162:165], a[164:167], v[218:221], v[162:165]\n"
        "s_waitcnt lgkmcnt(2)\n"
        "ds_read_b64_tr_b16 v[88:89], v187 offset:6152\n"
        "ds_read_b64_tr_b16 v[90:91], v187 offset:9224\n"
        "v_mfma_f32_16x16x32_bf16 v[142:145], v[80:83], v[218:221], 0\n"
        "s_barrier\n"
        "ds_read_b64_tr_b16 v[178:179], v187 offset:12296\n"
        "ds_read_b64_tr_b16 v[180:181], v187 offset:15368\n"
        "v_mfma_f32_16x16x32_bf16 v[142:145], v[230:233], v[214:217], v[142:145]\n"
        "v_cvt_pk_bf16_f32 v226, v234, v235\n"
        "v_cvt_pk_bf16_f32 v227, v236, v237\n"
        "v_mfma_f32_16x16x32_bf16 v[142:145], v[48:51], v[210:213], v[142:145]\n"
        "s_nop 0\n"
        "v_cvt_pk_bf16_f32 v228, v162, v163\n"
        "v_cvt_pk_bf16_f32 v229, v164, v165\n"
        "v_mfma_f32_16x16x32_bf16 v[234:237], v[48:51], v[206:209], 0\n"
        "s_nop 0\n"
        "ds_write_b128 v188, v[226:229]\n"
        "v_mfma_f32_16x16x32_bf16 v[234:237], v[230:233], v[202:205], v[234:237]\n"
        "s_nop 0\n"
        "v_cvt_pk_bf16_f32 v48, v142, v143\n"
        "v_cvt_pk_bf16_f32 v49, v144, v145\n"
        "v_mfma_f32_16x16x32_bf16 v[234:237], v[80:83], v[194:197], v[234:237]\n"
        "v_cndmask_b32_dpp v254, v254, v36, vcc row_ror:4 row_mask:0xf bank_mask:0xf\n"
        "v_cndmask_b32_dpp v252, v252, v37, vcc row_ror:4 row_mask:0xf bank_mask:0xf\n"
        "v_mfma_f32_16x16x32_bf16 v[34:37], a[176:179], v[218:221], 0\n"
        "s_nop 0\n"
        "v_mul_f32_dpp v251, v151, v251 quad_perm:[0,0,0,0] row_mask:0xf bank_mask:0xf\n"
        "v_mul_f32_dpp v253, v151, v253 quad_perm:[1,1,1,1] row_mask:0xf bank_mask:0xf\n"
        "v_mfma_f32_16x16x32_bf16 v[34:37], a[172:175], v[214:217], v[34:37]\n"
        "s_waitcnt lgkmcnt(1)\n"
        "v_cvt_pk_bf16_f32 v142, v234, v235\n"
        "v_cvt_pk_bf16_f32 v143, v236, v237\n"
        "v_mfma_f32_16x16x32_bf16 v[34:37], a[168:171], v[210:213], v[34:37]\n"
        "v_mul_f32_dpp v254, v151, v254 quad_perm:[2,2,2,2] row_mask:0xf bank_mask:0xf\n"
        "v_mul_f32_dpp v252, v151, v252 quad_perm:[3,3,3,3] row_mask:0xf bank_mask:0xf\n"
        "v_mfma_f32_16x16x32_bf16 v[174:177], a[180:183], v[210:213], 0\n"
        "v_max3_f32 v51, v251, v253, v254\n"
        "v_max_f32 v51, v51, v252\n"
        "v_mfma_f32_16x16x32_bf16 v[174:177], a[184:187], v[214:217], v[174:177]\n"
        "ds_read_b64_tr_b16 v[182:183], v187 offset:1024\n"
        "v_mul_f32 v51, v51, v151\n"
        "v_mfma_f32_16x16x32_bf16 v[174:177], a[188:191], v[218:221], v[174:177]\n"
        "ds_read_b64_tr_b16 v[184:185], v187 offset:4096\n"
        "v_fma_f32 v251, v251, v151, -v51\n"
        "v_mfma_f32_16x16x32_bf16 v[80:83], v[178:181], v[218:221], 0\n"
        "v_fma_f32 v253, v253, v151, -v51\n"
        "v_fma_f32 v254, v254, v151, -v51\n"
        "v_mfma_f32_16x16x32_bf16 v[80:83], v[88:91], v[214:217], v[80:83]\n"
        "ds_read_b64_tr_b16 v[162:163], v187 offset:7168\n"
        "v_fma_f32 v252, v252, v151, -v51\n"
        "v_mfma_f32_16x16x32_bf16 v[80:83], v[166:169], v[210:213], v[80:83]\n"
        "ds_read_b64_tr_b16 v[164:165], v187 offset:10240\n"
        "v_cvt_pk_bf16_f32 v230, v34, v35\n"
        "v_mfma_f32_16x16x32_bf16 v[234:237], v[166:169], v[206:209], 0\n"
        "ds_read_b64_tr_b16 v[166:167], v187 offset:13312\n"
        "v_cvt_pk_bf16_f32 v231, v36, v37\n"
        "v_mfma_f32_16x16x32_bf16 v[234:237], v[88:91], v[202:205], v[234:237]\n"
        "ds_read_b64_tr_b16 v[168:169], v187 offset:16384\n"
        "v_cvt_pk_bf16_f32 v232, v174, v175\n"
        "v_mfma_f32_16x16x32_bf16 v[234:237], v[178:181], v[194:197], v[234:237]\n"
        "v_exp_f32 v251, v251\n"
        "v_mfma_f32_16x16x32_bf16 v[34:37], a[200:203], v[218:221], 0\n"
        "v_exp_f32 v253, v253\n"
        "v_mfma_f32_16x16x32_bf16 v[34:37], a[196:199], v[214:217], v[34:37]\n"
        "v_exp_f32 v254, v254\n"
        "v_mfma_f32_16x16x32_bf16 v[34:37], a[192:195], v[210:213], v[34:37]\n"
        "v_exp_f32 v252, v252\n"
        "v_mfma_f32_16x16x32_bf16 v[178:181], a[204:207], v[210:213], 0\n"
        "v_add_f32 v90, v251, v253\n"
        "v_add_f32 v89, v254, v252\n"
        "v_mfma_f32_16x16x32_bf16 v[178:181], a[208:211], v[214:217], v[178:181]\n"
        "v_add_f32 v90, v90, v89\n"
        "v_cvt_pk_bf16_f32 v233, v176, v177\n"
        "v_mfma_f32_16x16x32_bf16 v[178:181], a[212:215], v[218:221], v[178:181]\n"
        "v_rcp_f32 v90, v90\n"
        "s_waitcnt lgkmcnt(0)\n"
        "v_mfma_f32_16x16x32_bf16 v[242:245], v[166:169], v[218:221], 0\n"
        "v_mul_f32 v251, v251, v90\n"
        "v_mul_f32 v253, v253, v90\n"
        "v_mfma_f32_16x16x32_bf16 v[242:245], v[162:165], v[214:217], v[242:245]\n"
        "ds_read_b64_tr_b16 v[238:239], v187 offset:1032\n"
        "v_mul_f32 v254, v254, v90\n"
        "v_mfma_f32_16x16x32_bf16 v[242:245], v[182:185], v[210:213], v[242:245]\n"
        "ds_read_b64_tr_b16 v[240:241], v187 offset:4104\n"
        "v_mul_f32 v252, v252, v90\n"
        "v_mfma_f32_16x16x32_bf16 v[174:177], v[182:185], v[206:209], 0\n"
        "ds_read_b64_tr_b16 v[246:247], v187 offset:7176\n"
        "v_mul_f32_dpp v251, v151, v251 quad_perm:[0,0,0,0] row_mask:0xf bank_mask:0xf\n"
        "v_mfma_f32_16x16x32_bf16 v[174:177], v[162:165], v[202:205], v[174:177]\n"
        "ds_read_b64_tr_b16 v[248:249], v187 offset:10248\n"
        "v_mul_f32_dpp v253, v151, v253 quad_perm:[1,1,1,1] row_mask:0xf bank_mask:0xf\n"
        "v_mfma_f32_16x16x32_bf16 v[174:177], v[166:169], v[194:197], v[174:177]\n"
        "ds_read_b64_tr_b16 v[182:183], v187 offset:13320\n"
        "v_mul_f32_dpp v254, v151, v254 quad_perm:[2,2,2,2] row_mask:0xf bank_mask:0xf\n"
        "v_mfma_f32_16x16x32_bf16 v[170:173], v[138:141], a[104:107], 0\n"
        "ds_read_b64_tr_b16 v[184:185], v187 offset:16392\n"
        "v_mul_f32_dpp v252, v151, v252 quad_perm:[3,3,3,3] row_mask:0xf bank_mask:0xf\n"
        "v_mfma_f32_16x16x32_bf16 v[170:173], v[132:135], a[100:103], v[170:173]\n"
        "ds_write_b128 v188, v[230:233] offset:64\n"
        "v_mfma_f32_16x16x32_bf16 v[170:173], v[126:129], a[96:99], v[170:173]\n"
        "v_cvt_pk_bf16_f32 v250, v251, v253\n"
        "v_cvt_pk_bf16_f32 v151, v254, v252\n"
        "v_mfma_f32_16x16x32_bf16 v[88:91], v[126:129], a[108:111], 0\n"
        "v_and_b32 v124, v25, v250\n"
        "v_and_b32 v125, v25, v151\n"
        "v_mfma_f32_16x16x32_bf16 v[88:91], v[132:135], a[112:115], v[88:91]\n"
        "v_mov_b64 v[130:131], v[124:125]\n"
        "v_cvt_pk_bf16_f32 v50, v80, v81\n"
        "v_mfma_f32_16x16x32_bf16 v[88:91], v[138:141], a[116:119], v[88:91]\n"
        "v_cvt_pk_bf16_f32 v51, v82, v83\n"
        "v_cvt_pk_bf16_f32 v144, v234, v235\n"
        "v_mfma_f32_16x16x32_bf16 v[166:169], v[42:45], v[124:127], 0\n"
        "ds_write_b128 v188, v[48:51] offset:192\n"
        "v_mfma_f32_16x16x32_bf16 v[80:83], v[44:47], v[128:131], 0\n"
        "s_waitcnt lgkmcnt(2)\n"
        "v_cvt_pk_bf16_f32 v145, v236, v237\n"
        "v_cvt_pk_bf16_f32 v234, v34, v35\n"
        "v_mfma_f32_16x16x32_bf16 v[48:51], v[182:185], v[218:221], 0\n"
        "ds_write_b128 v189, v[142:145] offset:192\n"
        "v_mfma_f32_16x16x32_bf16 v[48:51], v[246:249], v[214:217], v[48:51]\n"
        "ds_read_b64_tr_b16 v[142:143], v187 offset:2048\n"
        "v_cvt_pk_bf16_f32 v235, v36, v37\n"
        "v_mfma_f32_16x16x32_bf16 v[48:51], v[238:241], v[210:213], v[48:51]\n"
        "ds_read_b64_tr_b16 v[144:145], v187 offset:5120\n"
        "v_cvt_pk_bf16_f32 v236, v178, v179\n"
        "v_mfma_f32_16x16x32_bf16 v[162:165], v[238:241], v[206:209], 0\n"
        "ds_read_b64_tr_b16 v[238:239], v187 offset:8192\n"
        "v_cvt_pk_bf16_f32 v237, v180, v181\n"
        "v_mfma_f32_16x16x32_bf16 v[162:165], v[246:249], v[202:205], v[162:165]\n"
        "ds_write_b128 v188, v[234:237] offset:128\n"
        "v_mfma_f32_16x16x32_bf16 v[162:165], v[182:185], v[194:197], v[162:165]\n"
        "ds_read_b64_tr_b16 v[240:241], v187 offset:11264\n"
        "v_mov_b64 v[136:137], v[124:125]\n"
        "v_mfma_f32_16x16x32_bf16 v[246:249], v[138:141], a[128:131], 0\n"
        "ds_read_b64_tr_b16 v[182:183], v187 offset:14336\n"
        "v_cvt_pk_bf16_f32 v34, v242, v243\n"
        "v_mfma_f32_16x16x32_bf16 v[246:249], v[132:135], a[124:127], v[246:249]\n"
        "ds_read_b64_tr_b16 v[184:185], v187 offset:17408\n"
        "v_cvt_pk_bf16_f32 v35, v244, v245\n"
        "v_mfma_f32_16x16x32_bf16 v[246:249], v[126:129], a[120:123], v[246:249]\n"
        "ds_read_b64_tr_b16 v[178:179], v187 offset:2056\n"
        "v_cvt_pk_bf16_f32 v46, v174, v175\n"
        "v_mfma_f32_16x16x32_bf16 v[242:245], v[126:129], a[132:135], 0\n"
        "ds_read_b64_tr_b16 v[180:181], v187 offset:5128\n"
        "v_cvt_pk_bf16_f32 v47, v176, v177\n"
        "v_mfma_f32_16x16x32_bf16 v[242:245], v[132:135], a[136:139], v[242:245]\n"
        "ds_read_b64_tr_b16 v[126:127], v187 offset:8200\n"
        "v_cvt_pk_bf16_f32 v36, v170, v171\n"
        "v_mfma_f32_16x16x32_bf16 v[242:245], v[138:141], a[140:143], v[242:245]\n"
        "ds_read_b64_tr_b16 v[128:129], v187 offset:11272\n"
        "v_cvt_pk_bf16_f32 v37, v172, v173\n"
        "s_nop 1\n"
        "v_mfma_f32_16x16x32_bf16 v[174:177], v[36:39], v[130:133], 0\n"
        "ds_read_b64_tr_b16 v[170:171], v187 offset:14344\n"
        "v_cvt_pk_bf16_f32 v40, v88, v89\n"
        "v_cvt_pk_bf16_f32 v41, v90, v91\n"
        "s_nop 1\n"
        "v_mfma_f32_16x16x32_bf16 v[88:91], v[38:41], v[134:137], 0\n"
        "ds_read_b64_tr_b16 v[172:173], v187 offset:17416\n"
        "v_cvt_pk_bf16_f32 v36, v48, v49\n"
        "s_waitcnt lgkmcnt(6)\n"
        "v_mfma_f32_16x16x32_bf16 v[132:135], v[182:185], v[218:221], 0\n"
        "v_cvt_pk_bf16_f32 v37, v50, v51\n"
        "v_cvt_pk_bf16_f32 v48, v162, v163\n"
        "v_mfma_f32_16x16x32_bf16 v[132:135], v[238:241], v[214:217], v[132:135]\n"
        "ds_write_b128 v188, v[34:37] offset:256\n"
        "v_mfma_f32_16x16x32_bf16 v[132:135], v[142:145], v[210:213], v[132:135]\n"
        "v_cvt_pk_bf16_f32 v49, v164, v165\n"
        "v_cvt_pk_bf16_f32 v30, v246, v247\n"
        "v_mfma_f32_16x16x32_bf16 v[40:43], v[142:145], v[206:209], 0\n"
        "ds_write_b128 v189, v[46:49] offset:256\n"
        "v_mfma_f32_16x16x32_bf16 v[40:43], v[238:241], v[202:205], v[40:43]\n"
        "v_cvt_pk_bf16_f32 v31, v248, v249\n"
        "v_mov_b64 v[142:143], v[124:125]\n"
        "v_mfma_f32_16x16x32_bf16 v[40:43], v[182:185], v[194:197], v[40:43]\n"
        "s_waitcnt lgkmcnt(2)\n"
        "v_cvt_pk_bf16_f32 v34, v242, v243\n"
        "v_cvt_pk_bf16_f32 v35, v244, v245\n"
        "v_mfma_f32_16x16x32_bf16 v[48:51], v[170:173], v[218:221], 0\n"
        "s_barrier\n"
        "v_cvt_pk_bf16_f32 v164, v132, v133\n"
        "v_cvt_pk_bf16_f32 v165, v134, v135\n"
        "v_mfma_f32_16x16x32_bf16 v[48:51], v[126:129], v[214:217], v[48:51]\n"
        "v_cvt_pk_bf16_f32 v124, v40, v41\n"
        "v_cvt_pk_bf16_f32 v125, v42, v43\n"
        "v_mfma_f32_16x16x32_bf16 v[48:51], v[178:181], v[210:213], v[48:51]\n"
        "v_cvt_pk_bf16_f32 v132, v166, v167\n"
        "v_cvt_pk_bf16_f32 v133, v82, v83\n"
        "v_mfma_f32_16x16x32_bf16 v[40:43], v[178:181], v[206:209], 0\n"
        "s_nop 0\n"
        "v_cvt_pk_bf16_f32 v134, v88, v89\n"
        "v_cvt_pk_bf16_f32 v162, v168, v169\n"
        "v_mfma_f32_16x16x32_bf16 v[40:43], v[126:129], v[202:205], v[40:43]\n"
        "s_nop 0\n"
        "v_cvt_pk_bf16_f32 v166, v48, v49\n"
        "v_cvt_pk_bf16_f32 v167, v50, v51\n"
        "v_mfma_f32_16x16x32_bf16 v[40:43], v[170:173], v[194:197], v[40:43]\n"
        "s_nop 0\n"
        "ds_write_b128 v188, v[164:167] offset:320\n"
        "v_mfma_f32_16x16x32_bf16 v[128:131], v[118:121], a[80:83], 0\n"
        "s_nop 0\n"
        "v_cvt_pk_bf16_f32 v163, v174, v175\n"
        "v_cvt_pk_bf16_f32 v164, v90, v91\n"
        "v_mfma_f32_16x16x32_bf16 v[128:131], v[112:115], a[76:79], v[128:131]\n"
        "s_nop 0\n"
        "v_cvt_pk_bf16_f32 v126, v40, v41\n"
        "v_cvt_pk_bf16_f32 v127, v42, v43\n"
        "v_mfma_f32_16x16x32_bf16 v[128:131], v[106:109], a[72:75], v[128:131]\n"
        "s_nop 0\n"
        "ds_read_b128 v[88:91], v27 offset:64\n"
        "ds_read_b128 v[48:51], v27 offset:128\n"
        "v_mfma_f32_16x16x32_bf16 v[178:181], v[106:109], a[84:87], 0\n"
        "s_nop 0\n"
        "ds_write_b128 v189, v[124:127] offset:320\n"
        "v_mfma_f32_16x16x32_bf16 v[178:181], v[112:115], a[88:91], v[178:181]\n"
        "s_nop 0\n"
        "v_cvt_pk_bf16_f32 v42, v128, v129\n"
        "v_cvt_pk_bf16_f32 v43, v130, v131\n"
        "v_mfma_f32_16x16x32_bf16 v[178:181], v[118:121], a[92:95], v[178:181]\n"
        "v_cvt_pk_bf16_f32 v166, v80, v81\n"
        "v_cvt_pk_bf16_f32 v167, v176, v177\n"
        "v_mfma_f32_16x16x32_bf16 v[80:83], v[30:33], v[136:139], 0\n"
        "s_nop 0\n"
        "v_cvt_f32_f16_sdwa v188, v222 dst_sel:DWORD dst_unused:UNUSED_PAD src0_sel:WORD_0\n"
        "v_lshrrev_b32 v41, 0, v222\n"
        "v_mfma_f32_16x16x32_bf16 v[126:129], v[32:35], v[140:143], 0\n"
        "s_nop 0\n"
        "v_cvt_pk_bf16_f32 v46, v178, v179\n"
        "v_cvt_pk_bf16_f32 v47, v180, v181\n"
        "v_mfma_f32_16x16x32_bf16 v[138:141], v[210:213], v[226:229], 0\n"
        "v_cvt_pk_bf16_f32 v135, v82, v83\n"
        "v_cvt_pk_bf16_f32 v168, v80, v81\n"
        "v_mfma_f32_16x16x32_bf16 v[138:141], v[214:217], v[230:233], v[138:141]\n"
        "s_waitcnt lgkmcnt(1)\n"
        "ds_write_b128 v12, v[132:135]\n"
        "v_mfma_f32_16x16x32_bf16 v[138:141], v[218:221], v[234:237], v[138:141]\n"
        "v_cvt_pk_bf16_f32 v165, v126, v127\n"
        "v_cvt_pk_bf16_f32 v169, v128, v129\n"
        "v_mfma_f32_16x16x32_bf16 v[230:233], v[218:221], a[224:227], 0\n"
        "ds_write_b128 v12, v[162:165] offset:1024\n"
        "v_mfma_f32_16x16x32_bf16 v[230:233], v[214:217], a[220:223], v[230:233]\n"
        "ds_write_b128 v12, v[166:169] offset:2048\n"
        "v_mfma_f32_16x16x32_bf16 v[230:233], v[210:213], a[216:219], v[230:233]\n"
        "s_nop 0\n"
        "v_cndmask_b32_e64 v40, v138, v139, s[56:57]\n"
        "v_cndmask_b32_e64 v40, v40, v140, s[58:59]\n"
        "v_mfma_f32_16x16x32_bf16 v[226:229], v[210:213], a[228:231], 0\n"
        "v_cndmask_b32_e64 v40, v40, v141, s[60:61]\n"
        "v_mul_f32 v40, v40, v188\n"
        "v_mfma_f32_16x16x32_bf16 v[226:229], v[214:217], a[232:235], v[226:229]\n"
        "v_mul_f32 v40, v40, v188\n"
        "v_and_b32_dpp v104, v250, v25 row_ror:12 row_mask:0xf bank_mask:0xf\n"
        "v_mfma_f32_16x16x32_bf16 v[226:229], v[218:221], a[236:239], v[226:229]\n"
        "v_exp_f32 v40, v40\n"
        "v_mfma_f32_16x16x32_bf16 v[140:143], v[210:213], v[88:91], 0\n"
        "v_and_b32_dpp v105, v151, v25 row_ror:12 row_mask:0xf bank_mask:0xf\n"
        "v_cvt_pk_f16_f32 v40, v40, 0\n"
        "v_mfma_f32_16x16x32_bf16 v[34:37], v[210:213], v[48:51], 0\n"
        "ds_write_b64 v190, v[40:41]\n"
        "v_mfma_f32_16x16x32_bf16 v[238:241], v[118:121], a[104:107], 0\n"
        "v_mov_b64 v[110:111], v[104:105]\n"
        "v_cvt_f32_f16_sdwa v188, v198 dst_sel:DWORD dst_unused:UNUSED_PAD src0_sel:WORD_0\n"
        "v_mfma_f32_16x16x32_bf16 v[238:241], v[112:115], a[100:103], v[238:241]\n"
        "v_mul_f32 v230, v230, v188\n"
        "v_mul_f32 v226, v226, v188\n"
        "v_mfma_f32_16x16x32_bf16 v[238:241], v[106:109], a[96:99], v[238:241]\n"
        "v_cvt_pk_f16_f32 v230, v230, v226\n"
        "v_cvt_f32_f16_sdwa v190, v198 dst_sel:DWORD dst_unused:UNUSED_PAD src0_sel:WORD_1\n"
        "v_mfma_f32_16x16x32_bf16 v[182:185], v[106:109], a[108:111], 0\n"
        "v_mul_f32 v231, v231, v190\n"
        "v_mul_f32 v227, v227, v190\n"
        "v_mfma_f32_16x16x32_bf16 v[182:185], v[112:115], a[112:115], v[182:185]\n"
        "v_cvt_pk_f16_f32 v231, v231, v227\n"
        "v_perm_b32 v82, v230, v140, s54\n"
        "v_mfma_f32_16x16x32_bf16 v[182:185], v[118:121], a[116:119], v[182:185]\n"
        "v_perm_b32 v226, v231, v141, s54\n"
        "v_cvt_f32_f16_sdwa v245, v199 dst_sel:DWORD dst_unused:UNUSED_PAD src0_sel:WORD_0\n"
        "v_mfma_f32_16x16x32_bf16 v[138:141], v[42:45], v[104:107], 0\n"
        "v_mul_f32 v232, v232, v245\n"
        "v_mul_f32 v228, v228, v245\n"
        "v_mfma_f32_16x16x32_bf16 v[126:129], v[44:47], v[108:111], 0\n"
        "v_cvt_pk_f16_f32 v232, v232, v228\n"
        "v_perm_b32 v83, v230, v34, s55\n"
        "v_mfma_f32_16x16x32_bf16 v[178:181], v[218:221], a[248:251], 0\n"
        "ds_write_b64 v192, v[82:83]\n"
        "v_mfma_f32_16x16x32_bf16 v[178:181], v[214:217], a[244:247], v[178:181]\n"
        "v_perm_b32 v34, v232, v142, s54\n"
        "v_perm_b32 v227, v231, v35, s55\n"
        "v_mfma_f32_16x16x32_bf16 v[178:181], v[210:213], a[240:243], v[178:181]\n"
        "ds_write_b64 v192, v[226:227] offset:6336\n"
        "v_mfma_f32_16x16x32_bf16 v[234:237], v[210:213], a[252:255], 0\n"
        "v_perm_b32 v35, v232, v36, s55\n"
        "v_cvt_f32_f16_sdwa v187, v199 dst_sel:DWORD dst_unused:UNUSED_PAD src0_sel:WORD_1\n"
        "v_mfma_f32_16x16x32_bf16 v[234:237], v[214:217], v[72:75], v[234:237]\n"
        "ds_write_b64 v192, v[34:35] offset:12672\n"
        "v_mfma_f32_16x16x32_bf16 v[234:237], v[218:221], v[64:67], v[234:237]\n"
        "v_mul_f32 v233, v233, v187\n"
        "v_mul_f32 v229, v229, v187\n"
        "v_mfma_f32_16x16x32_bf16 v[174:177], v[214:217], v[88:91], 0\n"
        "v_cvt_pk_f16_f32 v233, v233, v229\n"
        "v_perm_b32 v198, v233, v143, s54\n"
        "v_mfma_f32_16x16x32_bf16 v[80:83], v[214:217], v[48:51], 0\n"
        "v_perm_b32 v199, v233, v37, s55\n"
        "v_cvt_pk_bf16_f32 v36, v238, v239\n"
        "v_mfma_f32_16x16x32_bf16 v[226:229], v[132:135], v[132:135], 0\n"
        "ds_write_b64 v192, v[198:199] offset:19008\n"
        "v_mfma_f32_16x16x32_bf16 v[226:229], v[162:165], v[162:165], v[226:229]\n"
        "v_cvt_pk_bf16_f32 v37, v240, v241\n"
        "v_cvt_pk_bf16_f32 v40, v182, v183\n"
        "v_mfma_f32_16x16x32_bf16 v[226:229], v[166:169], v[166:169], v[226:229]\n"
        "v_cvt_pk_bf16_f32 v41, v184, v185\n"
        "v_mul_f32 v178, v178, v188\n"
        "v_mfma_f32_16x16x32_bf16 v[230:233], a[152:155], v[194:197], 0\n"
        "v_mul_f32 v234, v234, v188\n"
        "v_mul_f32 v179, v179, v190\n"
        "v_mfma_f32_16x16x32_bf16 v[230:233], a[148:151], v[202:205], v[230:233]\n"
        "v_mul_f32 v235, v235, v190\n"
        "v_cvt_pk_f16_f32 v178, v178, v234\n"
        "v_mfma_f32_16x16x32_bf16 v[230:233], a[144:147], v[206:209], v[230:233]\n"
        "v_cvt_pk_f16_f32 v179, v179, v235\n"
        "v_mul_f32 v180, v180, v245\n"
        "v_mfma_f32_16x16x32_bf16 v[166:169], a[156:159], v[206:209], 0\n"
        "v_mul_f32 v236, v236, v245\n"
        "v_mov_b64 v[116:117], v[104:105]\n"
        "v_mfma_f32_16x16x32_bf16 v[166:169], a[160:163], v[202:205], v[166:169]\n"
        "v_cvt_pk_f16_f32 v180, v180, v236\n"
        "v_perm_b32 v198, v178, v174, s54\n"
        "v_mfma_f32_16x16x32_bf16 v[166:169], a[164:167], v[194:197], v[166:169]\n"
        "v_perm_b32 v134, v179, v175, s54\n"
        "v_perm_b32 v132, v180, v176, s54\n"
        "v_mfma_f32_16x16x32_bf16 v[182:185], v[118:121], a[128:131], 0\n"
        "v_mul_f32 v181, v181, v187\n"
        "v_mul_f32 v237, v237, v187\n"
        "v_mfma_f32_16x16x32_bf16 v[182:185], v[112:115], a[124:127], v[182:185]\n"
        "v_cvt_pk_f16_f32 v181, v181, v237\n"
        "v_perm_b32 v199, v178, v80, s55\n"
        "v_mfma_f32_16x16x32_bf16 v[182:185], v[106:109], a[120:123], v[182:185]\n"
        "ds_write_b64 v192, v[198:199] offset:8\n"
        "v_mfma_f32_16x16x32_bf16 v[142:145], v[106:109], a[132:135], 0\n"
        "v_perm_b32 v106, v181, v177, s54\n"
        "v_perm_b32 v135, v179, v81, s55\n"
        "v_mfma_f32_16x16x32_bf16 v[142:145], v[112:115], a[136:139], v[142:145]\n"
        "ds_write_b64 v192, v[134:135] offset:6344\n"
        "v_mfma_f32_16x16x32_bf16 v[142:145], v[118:121], a[140:143], v[142:145]\n"
        "v_perm_b32 v133, v180, v82, s55\n"
        "v_perm_b32 v107, v181, v83, s55\n"
        "v_mfma_f32_16x16x32_bf16 v[234:237], v[36:39], v[110:113], 0\n"
        "ds_write_b64 v192, v[132:133] offset:12680\n"
        "v_mfma_f32_16x16x32_bf16 v[162:165], v[38:41], v[114:117], 0\n"
        "ds_write_b64 v192, v[106:107] offset:19016\n"
        "v_mfma_f32_16x16x32_bf16 v[132:135], v[218:221], v[56:59], 0\n"
        "v_cvt_pk_bf16_f32 v112, v230, v231\n"
        "v_cvt_pk_bf16_f32 v113, v232, v233\n"
        "v_mfma_f32_16x16x32_bf16 v[132:135], v[214:217], v[60:63], v[132:135]\n"
        "v_cvt_pk_bf16_f32 v114, v166, v167\n"
        "v_cvt_pk_bf16_f32 v115, v168, v169\n"
        "v_mfma_f32_16x16x32_bf16 v[132:135], v[210:213], v[68:71], v[132:135]\n"
        "v_cvt_pk_bf16_f32 v30, v182, v183\n"
        "v_cvt_pk_bf16_f32 v31, v184, v185\n"
        "v_mfma_f32_16x16x32_bf16 v[80:83], v[210:213], v[52:55], 0\n"
        "v_cvt_pk_bf16_f32 v34, v142, v143\n"
        "v_cvt_pk_bf16_f32 v35, v144, v145\n"
        "v_mfma_f32_16x16x32_bf16 v[80:83], v[214:217], v[76:79], v[80:83]\n"
        "s_nop 0\n"
        "v_mov_b64 v[122:123], v[104:105]\n"
        "v_mul_f32 v132, v132, v188\n"
        "v_mfma_f32_16x16x32_bf16 v[80:83], v[218:221], v[84:87], v[80:83]\n"
        "v_mul_f32 v133, v133, v190\n"
        "v_cvt_pk_bf16_f32 v106, v138, v139\n"
        "v_mfma_f32_16x16x32_bf16 v[230:233], v[218:221], v[88:91], 0\n"
        "s_nop 0\n"
        "v_cvt_pk_bf16_f32 v107, v128, v129\n"
        "v_cvt_pk_bf16_f32 v108, v162, v163\n"
        "v_mfma_f32_16x16x32_bf16 v[128:131], v[218:221], v[48:51], 0\n"
        "s_nop 0\n"
        "v_mul_f32 v80, v80, v188\n"
        "v_cvt_pk_f16_f32 v132, v132, v80\n"
        "v_mfma_f32_16x16x32_bf16 v[210:213], v[158:161], a[80:83], 0\n"
        "v_perm_b32 v138, v132, v230, s54\n"
        "v_mul_f32 v81, v81, v190\n"
        "v_mfma_f32_16x16x32_bf16 v[210:213], v[152:155], a[76:79], v[210:213]\n"
        "v_cvt_pk_f16_f32 v133, v133, v81\n"
        "v_perm_b32 v198, v133, v231, s54\n"
        "v_mfma_f32_16x16x32_bf16 v[210:213], v[146:149], a[72:75], v[210:213]\n"
        "v_perm_b32 v139, v132, v128, s55\n"
        "v_perm_b32 v199, v133, v129, s55\n"
        "v_mfma_f32_16x16x32_bf16 v[218:221], v[146:149], a[84:87], 0\n"
        "ds_write_b64 v192, v[138:139] offset:16\n"
        "v_mfma_f32_16x16x32_bf16 v[218:221], v[152:155], a[88:91], v[218:221]\n"
        "ds_write_b64 v192, v[198:199] offset:6352\n"
        "v_mfma_f32_16x16x32_bf16 v[218:221], v[158:161], a[92:95], v[218:221]\n"
        "v_mul_f32 v134, v134, v245\n"
        "v_mul_f32 v82, v82, v245\n"
        "v_mfma_f32_16x16x32_bf16 v[178:181], v[30:33], v[116:119], 0\n"
        "v_cvt_pk_f16_f32 v134, v134, v82\n"
        "v_perm_b32 v118, v134, v232, s54\n"
        "v_mfma_f32_16x16x32_bf16 v[214:217], v[32:35], v[120:123], 0\n"
        "v_perm_b32 v119, v134, v130, s55\n"
        "v_cvt_pk_bf16_f32 v80, v140, v141\n"
        "v_mfma_f32_16x16x32_bf16 v[138:141], a[176:179], v[194:197], 0\n"
        "ds_write_b64 v192, v[118:119] offset:12688\n"
        "v_mfma_f32_16x16x32_bf16 v[138:141], a[172:175], v[202:205], v[138:141]\n"
        "v_cvt_pk_bf16_f32 v81, v234, v235\n"
        "v_cvt_pk_bf16_f32 v82, v164, v165\n"
        "v_mfma_f32_16x16x32_bf16 v[138:141], a[168:171], v[206:209], v[138:141]\n"
        "v_mul_f32 v135, v135, v187\n"
        "v_mul_f32 v83, v83, v187\n"
        "v_mfma_f32_16x16x32_bf16 v[34:37], a[180:183], v[206:209], 0\n"
        "v_cvt_pk_f16_f32 v135, v135, v83\n"
        "v_perm_b32 v118, v135, v233, s54\n"
        "v_mfma_f32_16x16x32_bf16 v[34:37], a[184:187], v[202:205], v[34:37]\n"
        "v_perm_b32 v119, v135, v131, s55\n"
        "v_and_b32_dpp v144, v250, v25 row_ror:8 row_mask:0xf bank_mask:0xf\n"
        "v_mfma_f32_16x16x32_bf16 v[34:37], a[188:191], v[194:197], v[34:37]\n"
        "ds_write_b64 v192, v[118:119] offset:19024\n"
        "v_mfma_f32_16x16x32_bf16 v[230:233], v[158:161], a[104:107], 0\n"
        "v_and_b32_dpp v145, v151, v25 row_ror:8 row_mask:0xf bank_mask:0xf\n"
        "v_cvt_pk_bf16_f32 v166, v126, v127\n"
        "v_mfma_f32_16x16x32_bf16 v[230:233], v[152:155], a[100:103], v[230:233]\n"
        "v_cvt_pk_bf16_f32 v167, v236, v237\n"
        "v_mov_b64 v[150:151], v[144:145]\n"
        "v_mfma_f32_16x16x32_bf16 v[230:233], v[146:149], a[96:99], v[230:233]\n"
        "v_cvt_pk_bf16_f32 v42, v210, v211\n"
        "v_cvt_pk_bf16_f32 v43, v212, v213\n"
        "v_mfma_f32_16x16x32_bf16 v[210:213], v[146:149], a[108:111], 0\n"
        "v_cvt_pk_bf16_f32 v46, v218, v219\n"
        "v_cvt_pk_bf16_f32 v47, v220, v221\n"
        "v_mfma_f32_16x16x32_bf16 v[210:213], v[152:155], a[112:115], v[210:213]\n"
        "v_cvt_pk_bf16_f32 v168, v178, v179\n"
        "v_cvt_pk_bf16_f32 v109, v180, v181\n"
        "v_mfma_f32_16x16x32_bf16 v[210:213], v[158:161], a[116:119], v[210:213]\n"
        "ds_write_b128 v12, v[106:109] offset:3072\n"
        "v_mfma_f32_16x16x32_bf16 v[118:121], v[42:45], v[144:147], 0\n"
        "v_cvt_pk_bf16_f32 v83, v214, v215\n"
        "v_cvt_pk_bf16_f32 v169, v216, v217\n"
        "v_mfma_f32_16x16x32_bf16 v[132:135], v[44:47], v[148:151], 0\n"
        "ds_write_b128 v12, v[80:83] offset:4096\n"
        "v_mfma_f32_16x16x32_bf16 v[126:129], v[194:197], a[224:227], 0\n"
        "ds_write_b128 v12, v[166:169] offset:5120\n"
        "v_mfma_f32_16x16x32_bf16 v[126:129], v[202:205], a[220:223], v[126:129]\n"
        "v_cvt_pk_bf16_f32 v218, v138, v139\n"
        "v_cvt_pk_bf16_f32 v219, v140, v141\n"
        "v_mfma_f32_16x16x32_bf16 v[126:129], v[206:209], a[216:219], v[126:129]\n"
        "v_cvt_pk_bf16_f32 v220, v34, v35\n"
        "v_cvt_pk_bf16_f32 v221, v36, v37\n"
        "v_mfma_f32_16x16x32_bf16 v[140:143], v[206:209], a[228:231], 0\n"
        "v_cvt_pk_bf16_f32 v36, v230, v231\n"
        "v_cvt_pk_bf16_f32 v37, v232, v233\n"
        "v_mfma_f32_16x16x32_bf16 v[140:143], v[202:205], a[232:235], v[140:143]\n"
        "v_cvt_pk_bf16_f32 v40, v210, v211\n"
        "v_cvt_pk_bf16_f32 v41, v212, v213\n"
        "v_mfma_f32_16x16x32_bf16 v[140:143], v[194:197], a[236:239], v[140:143]\n"
        "v_mov_b64 v[156:157], v[144:145]\n"
        "v_cvt_f32_f16_sdwa v187, v200 dst_sel:DWORD dst_unused:UNUSED_PAD src0_sel:WORD_0\n"
        "v_mfma_f32_16x16x32_bf16 v[230:233], v[206:209], v[88:91], 0\n"
        "s_nop 0\n"
        "v_mul_f32 v126, v126, v187\n"
        "v_cvt_f32_f16_sdwa v190, v200 dst_sel:DWORD dst_unused:UNUSED_PAD src0_sel:WORD_1\n"
        "v_mfma_f32_16x16x32_bf16 v[214:217], v[206:209], v[48:51], 0\n"
        "s_nop 0\n"
        "v_mul_f32 v140, v140, v187\n"
        "v_cvt_pk_f16_f32 v126, v126, v140\n"
        "v_mfma_f32_16x16x32_bf16 v[210:213], v[106:109], v[106:109], 0\n"
        "v_perm_b32 v106, v126, v230, s54\n"
        "v_mul_f32 v127, v127, v190\n"
        "v_mfma_f32_16x16x32_bf16 v[210:213], v[80:83], v[80:83], v[210:213]\n"
        "s_nop 0\n"
        "v_perm_b32 v107, v126, v214, s55\n"
        "v_mul_f32 v141, v141, v190\n"
        "v_mfma_f32_16x16x32_bf16 v[210:213], v[166:169], v[166:169], v[210:213]\n"
        "ds_write_b64 v192, v[106:107] offset:25344\n"
        "v_mfma_f32_16x16x32_bf16 v[80:83], v[158:161], a[128:131], 0\n"
        "ds_write_b128 v189, v[112:115]\n"
        "v_mfma_f32_16x16x32_bf16 v[80:83], v[152:155], a[124:127], v[80:83]\n"
        "v_cvt_pk_f16_f32 v127, v127, v141\n"
        "v_perm_b32 v140, v127, v231, s54\n"
        "v_mfma_f32_16x16x32_bf16 v[80:83], v[146:149], a[120:123], v[80:83]\n"
        "ds_write_b128 v189, v[218:221] offset:64\n"
        "v_mfma_f32_16x16x32_bf16 v[106:109], v[146:149], a[132:135], 0\n"
        "v_perm_b32 v141, v127, v215, s55\n"
        "v_cvt_f32_f16_sdwa v188, v201 dst_sel:DWORD dst_unused:UNUSED_PAD src0_sel:WORD_0\n"
        "v_mfma_f32_16x16x32_bf16 v[106:109], v[152:155], a[136:139], v[106:109]\n"
        "ds_write_b64 v192, v[140:141] offset:31680\n"
        "v_mfma_f32_16x16x32_bf16 v[106:109], v[158:161], a[140:143], v[106:109]\n"
        "v_mul_f32 v128, v128, v188\n"
        "v_mul_f32 v142, v142, v188\n"
        "v_mfma_f32_16x16x32_bf16 v[138:141], v[36:39], v[150:153], 0\n"
        "v_cvt_pk_f16_f32 v128, v128, v142\n"
        "v_perm_b32 v152, v128, v232, s54\n"
        "v_mfma_f32_16x16x32_bf16 v[148:151], v[38:41], v[154:157], 0\n"
        "v_perm_b32 v153, v128, v216, s55\n"
        "v_cvt_pk_bf16_f32 v30, v80, v81\n"
        "v_mfma_f32_16x16x32_bf16 v[198:201], a[200:203], v[194:197], 0\n"
        "ds_write_b64 v193, v[152:153]\n"
        "v_mfma_f32_16x16x32_bf16 v[198:201], a[196:199], v[202:205], v[198:201]\n"
        "v_cvt_pk_bf16_f32 v31, v82, v83\n"
        "v_cvt_pk_bf16_f32 v34, v106, v107\n"
        "v_mfma_f32_16x16x32_bf16 v[198:201], a[192:195], v[206:209], v[198:201]\n"
        "v_cvt_pk_bf16_f32 v35, v108, v109\n"
        "v_mov_b64 v[162:163], v[144:145]\n"
        "v_mfma_f32_16x16x32_bf16 v[126:129], a[204:207], v[206:209], 0\n"
        "s_nop 0\n"
        "v_cvt_pk_bf16_f32 v106, v118, v119\n"
        "v_cvt_pk_bf16_f32 v107, v134, v135\n"
        "v_mfma_f32_16x16x32_bf16 v[126:129], a[208:211], v[202:205], v[126:129]\n"
        "s_nop 0\n"
        "v_cvt_pk_bf16_f32 v152, v198, v199\n"
        "v_cvt_pk_bf16_f32 v153, v200, v201\n"
        "v_mfma_f32_16x16x32_bf16 v[126:129], a[212:215], v[194:197], v[126:129]\n"
        "v_cvt_pk_bf16_f32 v108, v148, v149\n"
        "v_cvt_pk_bf16_f32 v198, v120, v121\n"
        "v_mfma_f32_16x16x32_bf16 v[146:149], v[194:197], a[248:251], 0\n"
        "s_nop 0\n"
        "v_cvt_pk_bf16_f32 v199, v138, v139\n"
        "v_cvt_pk_bf16_f32 v200, v150, v151\n"
        "v_mfma_f32_16x16x32_bf16 v[146:149], v[202:205], a[244:247], v[146:149]\n"
        "s_nop 0\n"
        "v_cvt_pk_bf16_f32 v154, v126, v127\n"
        "v_cvt_pk_bf16_f32 v155, v128, v129\n"
        "v_mfma_f32_16x16x32_bf16 v[146:149], v[206:209], a[240:243], v[146:149]\n"
        "s_nop 0\n"
        "ds_write_b128 v189, v[152:155] offset:128\n"
        "v_mfma_f32_16x16x32_bf16 v[120:123], v[206:209], a[252:255], 0\n"
        "s_nop 0\n"
        "v_cvt_pk_bf16_f32 v118, v132, v133\n"
        "v_cvt_pk_bf16_f32 v119, v140, v141\n"
        "v_mfma_f32_16x16x32_bf16 v[120:123], v[202:205], v[72:75], v[120:123]\n"
        "s_nop 0\n"
        "v_mul_f32 v146, v146, v187\n"
        "v_mul_f32 v147, v147, v190\n"
        "v_mfma_f32_16x16x32_bf16 v[120:123], v[194:197], v[64:67], v[120:123]\n"
        "v_mul_f32 v148, v148, v188\n"
        "v_fmac_f32_dpp v186, v255, v251 quad_perm:[0,0,0,0] row_mask:0xf bank_mask:0xf\n"
        "v_mfma_f32_16x16x32_bf16 v[140:143], v[202:205], v[88:91], 0\n"
        "s_nop 0\n"
        "v_cndmask_b32_e64 v189, v210, v211, s[56:57]\n"
        "v_fmac_f32_dpp v186, v255, v253 quad_perm:[1,1,1,1] row_mask:0xf bank_mask:0xf\n"
        "v_mfma_f32_16x16x32_bf16 v[40:43], v[202:205], v[48:51], 0\n"
        "s_nop 0\n"
        "v_mul_f32 v120, v120, v187\n"
        "v_cvt_pk_f16_f32 v146, v146, v120\n"
        "v_mfma_f32_16x16x32_bf16 v[126:129], v[30:33], v[156:159], 0\n"
        "v_perm_b32 v210, v146, v140, s54\n"
        "v_mul_f32 v121, v121, v190\n"
        "v_mfma_f32_16x16x32_bf16 v[132:135], v[32:35], v[160:163], 0\n"
        "s_nop 0\n"
        "v_perm_b32 v211, v146, v40, s55\n"
        "v_cvt_pk_f16_f32 v147, v147, v121\n"
        "v_mfma_f32_16x16x32_bf16 v[158:161], v[194:197], v[56:59], 0\n"
        "ds_write_b64 v192, v[210:211] offset:25352\n"
        "v_mfma_f32_16x16x32_bf16 v[158:161], v[202:205], v[60:63], v[158:161]\n"
        "v_cvt_pk_bf16_f32 v109, v128, v129\n"
        "v_cvt_pk_bf16_f32 v120, v126, v127\n"
        "v_mfma_f32_16x16x32_bf16 v[158:161], v[206:209], v[68:71], v[158:161]\n"
        "ds_write_b128 v12, v[106:109] offset:6144\n"
        "v_mfma_f32_16x16x32_bf16 v[214:217], v[206:209], v[52:55], 0\n"
        "s_waitcnt vmcnt(0)\n"
        "v_cvt_pk_bf16_f32 v201, v132, v133\n"
        "v_cvt_pk_bf16_f32 v121, v134, v135\n"
        "v_mfma_f32_16x16x32_bf16 v[214:217], v[202:205], v[76:79], v[214:217]\n"
        "ds_write_b128 v12, v[198:201] offset:7168\n"
        "v_mfma_f32_16x16x32_bf16 v[214:217], v[194:197], v[84:87], v[214:217]\n"
        "ds_write_b128 v12, v[118:121] offset:8192\n"
        "v_mfma_f32_16x16x32_bf16 v[80:83], v[194:197], v[88:91], 0\n"
        "ds_read_b128 v[126:129], v9\n"
        "v_perm_b32 v210, v147, v141, s54\n"
        "v_mfma_f32_16x16x32_bf16 v[230:233], v[194:197], v[48:51], 0\n"
        "ds_read_b128 v[132:135], v10\n"
        "v_perm_b32 v211, v147, v41, s55\n"
        "v_mfma_f32_16x16x32_bf16 v[34:37], v[106:109], v[106:109], 0\n"
        "ds_write_b64 v192, v[210:211] offset:31688\n"
        "v_mfma_f32_16x16x32_bf16 v[34:37], v[198:201], v[198:201], v[34:37]\n"
        "ds_read_b128 v[138:141], v11\n"
        "v_mul_f32 v122, v122, v188\n"
        "v_mfma_f32_16x16x32_bf16 v[34:37], v[118:121], v[118:121], v[34:37]\n"
        "ds_read_b128 v[106:109], v9 offset:3072\n"
        "v_cvt_pk_f16_f32 v148, v148, v122\n"
        "v_mfma_f32_16x16x32_bf16 v[198:201], v[206:209], v[112:115], 0\n"
        "ds_read_b128 v[112:115], v10 offset:3072\n"
        "v_perm_b32 v208, v148, v142, s54\n"
        "v_mfma_f32_16x16x32_bf16 v[198:201], v[202:205], v[218:221], v[198:201]\n"
        "ds_read_b128 v[118:121], v11 offset:3072\n"
        "v_perm_b32 v209, v148, v42, s55\n"
        "v_mfma_f32_16x16x32_bf16 v[198:201], v[194:197], v[152:155], v[198:201]\n"
        "ds_write_b64 v193, v[208:209] offset:8\n"
        "v_cndmask_b32_e64 v189, v189, v212, s[58:59]\n"
        "v_cndmask_b32_e64 v189, v189, v213, s[60:61]\n"
        "v_cndmask_b32_e64 v202, v226, v227, s[56:57]\n"
        "v_fmac_f32_dpp v186, v255, v254 quad_perm:[2,2,2,2] row_mask:0xf bank_mask:0xf\n"
        "v_cndmask_b32_e64 v202, v202, v228, s[58:59]\n"
        "v_cndmask_b32_e64 v202, v202, v229, s[60:61]\n"
        "v_mul_f32 v158, v158, v187\n"
        "v_mul_f32 v214, v214, v187\n"
        "v_fmac_f32_dpp v186, v255, v252 quad_perm:[3,3,3,3] row_mask:0xf bank_mask:0xf\n"
        "v_cvt_pk_f16_f32 v158, v158, v214\n"
        "v_mul_f32 v203, s80, v186\n"
        "v_cvt_f32_f16_sdwa v255, v222 dst_sel:DWORD dst_unused:UNUSED_PAD src0_sel:WORD_1\n"
        "v_readfirstlane_b32 s20, v224\n"
        "v_perm_b32 v154, v158, v80, s54\n"
        "v_mul_f32 v159, v159, v190\n"
        "v_mul_f32 v215, v215, v190\n"
        "v_mul_f32 v160, v160, v188\n"
        "v_mul_f32 v216, v216, v188\n"
        "v_cvt_pk_f16_f32 v159, v159, v215\n"
        "v_cvt_pk_f16_f32 v160, v160, v216\n"
        "v_mov_b32 v208, 0x3c003c00\n"
        "v_perm_b32 v204, v159, v81, s54\n"
        "v_lshrrev_b32 v209, 16, v222\n"
        "v_mov_b32 v190, s29\n"
        "v_perm_b32 v152, v160, v82, s54\n"
        "v_cndmask_b32_e64 v80, 0, v208, s[76:77]\n"
        "v_cndmask_b32_e64 v81, 0, v208, s[56:57]\n"
        "v_cndmask_b32_e64 v82, 0, v208, s[58:59]\n"
        "v_cndmask_b32_e64 v83, 0, v208, s[60:61]\n"
        "v_readfirstlane_b32 s21, v225\n"
        "v_perm_b32 v155, v158, v230, s55\n"
        "v_perm_b32 v205, v159, v231, s55\n"
        "v_perm_b32 v153, v160, v232, s55\n"
        "ds_write_b64 v192, v[154:155] offset:25360\n"
        "ds_write_b64 v192, v[204:205] offset:31696\n"
        "ds_write_b64 v193, v[152:153] offset:16\n"
        "ds_read_b128 v[146:149], v9 offset:6144\n"
        "v_cndmask_b32_e64 v193, v34, v35, s[56:57]\n"
        "v_cndmask_b32_e64 v193, v193, v36, s[58:59]\n"
        "v_cndmask_b32_e64 v193, v193, v37, s[60:61]\n"
        "ds_read_b128 v[152:155], v10 offset:6144\n"
        "ds_read_b128 v[158:161], v11 offset:6144\n"
        "v_cndmask_b32_dpp v189, v193, v189, vcc row_ror:4 row_mask:0xf bank_mask:0xf\n"
        "v_cndmask_b32_e64 v208, v198, v199, s[56:57]\n"
        "v_cndmask_b32_e64 v208, v208, v200, s[58:59]\n"
        "v_cndmask_b32_dpp v202, v189, v202, vcc row_ror:4 row_mask:0xf bank_mask:0xf\n"
        "v_fma_f32 v203, -v203, v186, v202\n"
        "v_max_f32 v203, 0, v203\n"
        "v_sub_f32 v202, 0x47c00000, v202\n"
        "v_fmaak_f32 v203, v28, v203, 0x3727c5ac\n"
        "v_bfi_b32 v203, s84, v202, v203\n"
        "v_rsq_f32 v193, v203\n"
        "v_cndmask_b32_e64 v208, v208, v201, s[60:61]\n"
        "v_cvt_f16_f32 v193, v193\n"
        "v_mul_f32 v208, v208, v255\n"
        "ds_write_b16 v18, v193\n"
        "ds_write_b16 v19, v193\n"
        "v_mul_f32 v208, v208, v255\n"
        "v_exp_f32 v208, v208\n"
        "ds_write_b16 v22, v190 offset:3072\n"
        "v_cvt_pk_f16_f32 v208, v208, 0\n"
        "s_add_u32 s20, s20, -1\n"
        "ds_write_b64 v191, v[208:209]\n"
        "s_waitcnt lgkmcnt(0)\n"
        "s_nop 7\n"
        ".p2align 6\n"
        "TOP_%=:\n"
        "v_mfma_f32_16x16x32_bf16 v[202:205], v[126:129], v[126:129], 0\n"
        "s_mov_b32 s28, s89\n"
        "ds_read_b128 v[218:221], v20 offset:512\n"
        "s_mov_b32 s30, s29\n"
        "v_mfma_f32_16x16x32_bf16 v[202:205], v[132:135], v[132:135], v[202:205]\n"
        "s_add_u32 s88, s88, 32\n"
        "v_add_u32 v23, s22, v23\n"
        "v_add_u32 v24, s22, v24\n"
        "v_mfma_f32_16x16x32_bf16 v[202:205], v[138:141], v[138:141], v[202:205]\n"
        "s_mov_b32 s22, s23\n"
        "ds_read_b128 v[188:191], v20 offset:528\n"
        "s_add_u32 s89, s86, s51\n"
        "v_mfma_f32_16x16x32_bf16 v[222:225], v[92:95], v[138:141], 0\n"
        "s_add_u32 s29, s29, 1\n"
        "s_add_u32 s26, s28, 392448\n"
        "v_cvt_pk_bf16_f32 v198, v192, v182\n"
        "v_mfma_f32_16x16x32_bf16 v[222:225], v[100:103], v[132:135], v[222:225]\n"
        "s_cmp_ge_u32 s88, 73\n"
        "v_cvt_pk_bf16_f32 v216, v193, v183\n"
        "v_lshlrev_b32 v210, 16, v176\n"
        "v_mfma_f32_16x16x32_bf16 v[222:225], v[96:99], v[126:129], v[222:225]\n"
        "buffer_store_dwordx3 v[196:198], v15, s[40:43], s26 offen offset:768 sc1 nt\n"
        "v_lshlrev_b32 v211, 16, v168\n"
        "v_lshlrev_b32 v212, 16, v144\n"
        "v_lshlrev_b32 v213, 16, v172\n"
        "v_mfma_f32_16x16x32_bf16 v[198:201], a[0:3], v[126:129], 0\n"
        "s_movk_i32 s23, 1584\n"
        "v_cvt_pk_bf16_f32 v208, v194, v184\n"
        "v_lshlrev_b32 v196, 16, v124\n"
        "v_mfma_f32_16x16x32_bf16 v[198:201], a[4:7], v[132:135], v[198:201]\n"
        "s_mov_b32 s86, s87\n"
        "v_lshlrev_b32 v197, 16, v42\n"
        "v_cvt_pk_bf16_f32 v250, v195, v185\n"
        "v_mfma_f32_16x16x32_bf16 v[198:201], a[8:11], v[138:141], v[198:201]\n"
        "s_cselect_b32 s92, 588672, 0\n"
        "v_cndmask_b32_e64 v254, v202, v203, s[56:57]\n"
        "v_cndmask_b32_e64 v254, v254, v204, s[58:59]\n"
        "v_mfma_f32_16x16x32_bf16 v[192:195], a[20:23], v[138:141], 0\n"
        "s_cselect_b32 s94, 73, 0\n"
        "s_add_u32 s27, s28, 490560\n"
        "s_add_u32 s87, s87, 43008\n"
        "v_mfma_f32_16x16x32_bf16 v[192:195], a[16:19], v[132:135], v[192:195]\n"
        "s_waitcnt lgkmcnt(0)\n"
        "v_pk_mul_f16 v182, v218, v178\n"
        "v_pk_mul_f16 v183, v218, v34\n"
        "v_mfma_f32_16x16x32_bf16 v[192:195], a[12:15], v[126:129], v[192:195]\n"
        "buffer_store_dwordx3 v[214:216], v14, s[40:43], s27 offen offset:192 sc1 nt\n"
        "v_pk_mul_f16 v184, v218, v162\n"
        "v_pk_mul_f16 v185, v218, v244\n"
        "v_pk_mul_f16 v186, v218, v240\n"
        "v_mfma_f32_16x16x32_bf16 v[214:217], a[24:27], v[126:129], 0\n"
        "s_add_u32 s26, s28, 588672\n"
        "v_pk_mul_f16 v187, v218, v236\n"
        "v_pk_fma_f16 v182, v219, v179, v182\n"
        "v_mfma_f32_16x16x32_bf16 v[214:217], a[28:31], v[132:135], v[214:217]\n"
        "s_sub_u32 s88, s88, s94\n"
        "v_pk_fma_f16 v183, v219, v35, v183\n"
        "v_pk_fma_f16 v184, v219, v163, v184\n"
        "v_mfma_f32_16x16x32_bf16 v[214:217], a[32:35], v[138:141], v[214:217]\n"
        "buffer_store_dwordx3 v[206:208], v16, s[40:43], s27 offen offset:960 sc1 nt\n"
        "s_add_u32 s87, s87, s92\n"
        "v_pk_fma_f16 v185, v219, v245, v185\n"
        "v_pk_fma_f16 v186, v219, v241, v186\n"
        "v_mfma_f32_16x16x32_bf16 v[206:209], a[44:47], v[138:141], 0\n"
        "s_waitcnt lgkmcnt(0)\n"
        "v_pk_fma_f16 v187, v219, v237, v187\n"
        "v_pk_fma_f16 v182, v220, v180, v182\n"
        "v_mfma_f32_16x16x32_bf16 v[206:209], a[40:43], v[132:135], v[206:209]\n"
        "s_waitcnt lgkmcnt(0)\n"
        "v_pk_fma_f16 v183, v220, v36, v183\n"
        "v_pk_fma_f16 v184, v220, v164, v184\n"
        "v_mfma_f32_16x16x32_bf16 v[206:209], a[36:39], v[126:129], v[206:209]\n"
        "buffer_store_dwordx3 v[248:250], v14, s[40:43], s26 offen offset:384 sc1 nt\n"
        "v_pk_fma_f16 v185, v220, v246, v185\n"
        "v_pk_fma_f16 v186, v220, v242, v186\n"
        "v_pk_fma_f16 v187, v220, v238, v187\n"
        "v_mfma_f32_16x16x32_bf16 v[224:227], a[48:51], v[126:129], 0\n"
        "v_pk_fma_f16 v182, v221, v181, v182\n"
        "v_pk_fma_f16 v183, v221, v37, v183\n"
        "v_mfma_f32_16x16x32_bf16 v[224:227], a[52:55], v[132:135], v[224:227]\n"
        "v_pk_fma_f16 v184, v221, v165, v184\n"
        "v_pk_fma_f16 v185, v221, v247, v185\n"
        "v_mfma_f32_16x16x32_bf16 v[224:227], a[56:59], v[138:141], v[224:227]\n"
        "v_pk_fma_f16 v186, v221, v243, v186\n"
        "v_pk_fma_f16 v187, v221, v239, v187\n"
        "v_mfma_f32_16x16x32_bf16 v[162:165], a[68:71], v[138:141], 0\n"
        "v_pk_fma_f16 v182, v188, v174, v182\n"
        "v_pk_fma_f16 v183, v188, v166, v183\n"
        "v_mfma_f32_16x16x32_bf16 v[162:165], a[64:67], v[132:135], v[162:165]\n"
        "v_cvt_pk_bf16_f32 v218, v198, v199\n"
        "v_cvt_pk_bf16_f32 v219, v200, v201\n"
        "v_mfma_f32_16x16x32_bf16 v[162:165], a[60:63], v[126:129], v[162:165]\n"
        "v_cvt_pk_bf16_f32 v220, v192, v193\n"
        "v_cvt_pk_bf16_f32 v221, v194, v195\n"
        "v_mfma_f32_16x16x32_bf16 v[34:37], v[106:109], v[106:109], 0\n"
        "v_cvt_pk_bf16_f32 v192, v214, v215\n"
        "v_cvt_pk_bf16_f32 v193, v216, v217\n"
        "v_mfma_f32_16x16x32_bf16 v[34:37], v[112:115], v[112:115], v[34:37]\n"
        "v_cvt_pk_bf16_f32 v194, v206, v207\n"
        "v_cvt_pk_bf16_f32 v195, v208, v209\n"
        "v_mfma_f32_16x16x32_bf16 v[34:37], v[118:121], v[118:121], v[34:37]\n"
        "v_cvt_pk_bf16_f32 v178, v224, v225\n"
        "v_cvt_pk_bf16_f32 v179, v226, v227\n"
        "v_mfma_f32_16x16x32_bf16 v[224:227], v[92:95], v[118:121], 0\n"
        "v_cvt_pk_bf16_f32 v180, v162, v163\n"
        "v_cvt_pk_bf16_f32 v181, v164, v165\n"
        "v_mfma_f32_16x16x32_bf16 v[224:227], v[100:103], v[112:115], v[224:227]\n"
        "v_pk_fma_f16 v184, v188, v142, v184\n"
        "v_pk_fma_f16 v185, v188, v170, v185\n"
        "v_mfma_f32_16x16x32_bf16 v[224:227], v[96:99], v[106:109], v[224:227]\n"
        "ds_read_u16 v255, v19 offset:2048\n"
        "v_pk_fma_f16 v186, v188, v122, v186\n"
        "v_mfma_f32_16x16x32_bf16 v[228:231], v[126:129], v[218:221], 0\n"
        "v_pk_fma_f16 v187, v188, v40, v187\n"
        "v_pk_fma_f16 v182, v189, v175, v182\n"
        "v_mfma_f32_16x16x32_bf16 v[228:231], v[132:135], v[192:195], v[228:231]\n"
        "v_pk_fma_f16 v183, v189, v167, v183\n"
        "v_pk_fma_f16 v184, v189, v143, v184\n"
        "v_mfma_f32_16x16x32_bf16 v[228:231], v[138:141], v[178:181], v[228:231]\n"
        "v_pk_fma_f16 v185, v189, v171, v185\n"
        "v_pk_fma_f16 v186, v189, v123, v186\n"
        "v_mfma_f32_16x16x32_bf16 v[162:165], a[8:11], v[118:121], 0\n"
        "v_pk_fma_f16 v187, v189, v41, v187\n"
        "v_pk_fma_f16 v182, v190, v176, v182\n"
        "v_mfma_f32_16x16x32_bf16 v[162:165], a[4:7], v[112:115], v[162:165]\n"
        "v_pk_fma_f16 v183, v190, v168, v183\n"
        "v_pk_fma_f16 v184, v190, v144, v184\n"
        "v_mfma_f32_16x16x32_bf16 v[162:165], a[0:3], v[106:109], v[162:165]\n"
        "v_pk_fma_f16 v185, v190, v172, v185\n"
        "v_pk_fma_f16 v186, v190, v124, v186\n"
        "v_mfma_f32_16x16x32_bf16 v[122:125], a[12:15], v[106:109], 0\n"
        "s_nop 0\n"
        "v_pk_fma_f16 v187, v190, v42, v187\n"
        "v_cndmask_b32_e64 v254, v254, v205, s[60:61]\n"
        "v_mfma_f32_16x16x32_bf16 v[122:125], a[16:19], v[112:115], v[122:125]\n"
        "s_add_u32 s46, s28, 590016\n"
        "v_cvt_pk_bf16_f32 v40, v162, v163\n"
        "v_cvt_pk_bf16_f32 v41, v164, v165\n"
        "v_mfma_f32_16x16x32_bf16 v[122:125], a[20:23], v[118:121], v[122:125]\n"
        "s_waitcnt lgkmcnt(0)\n"
        "v_cmp_ne_u32_e64 s[24:25], s30, v255\n"
        "v_cndmask_b32_e64 v252, v34, v35, s[56:57]\n"
        "v_mfma_f32_16x16x32_f16 v[210:213], v[80:83], v[182:185], v[210:213]\n"
        "s_min_u32 s46, s46, 200540928\n"
        "v_cndmask_b32_e64 v252, v252, v36, s[58:59]\n"
        "v_cndmask_b32_e64 v252, v252, v37, s[60:61]\n"
        "v_mfma_f32_16x16x32_f16 v[194:197], v[80:83], v[184:187], v[194:197]\n"
        "s_cmp_le_i32 s85, s49\n"
        "ds_read_b128 v[236:239], v23\n"
        "v_cvt_pk_bf16_f32 v42, v122, v123\n"
        "v_mfma_f32_16x16x32_bf16 v[162:165], a[32:35], v[118:121], 0\n"
        "s_cselect_b64 s[24:25], -1, s[24:25]\n"
        "v_cvt_pk_bf16_f32 v174, v210, v211\n"
        "v_cvt_pk_bf16_f32 v175, v212, v213\n"
        "v_mfma_f32_16x16x32_bf16 v[162:165], a[28:31], v[112:115], v[162:165]\n"
        "ds_read_b128 v[170:173], v23 offset:64\n"
        "v_cvt_pk_bf16_f32 v176, v196, v197\n"
        "v_mfma_f32_16x16x32_bf16 v[162:165], a[24:27], v[106:109], v[162:165]\n"
        "s_nop 0\n"
        "ds_read_b128 v[34:37], v23 offset:128\n"
        "ds_read_b64 v[30:31], v24 offset:360\n"
        "v_mfma_f32_16x16x32_bf16 v[142:145], a[36:39], v[106:109], 0\n"
        "s_nop 0\n"
        "v_cvt_pk_bf16_f32 v43, v124, v125\n"
        "v_mfma_f32_16x16x32_bf16 v[142:145], a[40:43], v[112:115], v[142:145]\n"
        "s_nop 0\n"
        "v_cvt_pk_bf16_f32 v166, v162, v163\n"
        "v_cvt_pk_bf16_f32 v167, v164, v165\n"
        "v_mfma_f32_16x16x32_bf16 v[142:145], a[44:47], v[118:121], v[142:145]\n"
        "buffer_store_dwordx3 v[174:176], v17, s[44:47], s26 offen offset:1152 sc1 nt\n"
        "v_mfma_f32_16x16x32_bf16 v[178:181], a[56:59], v[118:121], 0\n"
        "s_cmp_lg_u64 s[24:25], 0\n"
        "v_mfma_f32_16x16x32_bf16 v[178:181], a[52:55], v[112:115], v[178:181]\n"
        "s_cbranch_scc1 SP3_%=\n"
        "BK3_%=:\n"
        "v_mfma_f32_16x16x32_bf16 v[178:181], a[48:51], v[106:109], v[178:181]\n"
        "s_cmp_gt_i32 s85, 0\n"
        "ds_read_b128 v[186:189], v13\n"
        "ds_read_b128 v[210:213], v13 offset:1024\n"
        "v_mfma_f32_16x16x32_bf16 v[122:125], a[60:63], v[106:109], 0\n"
        "s_cselect_b32 s94, s87, 0\n"
        "ds_read_b128 v[214:217], v13 offset:2048\n"
        "ds_read_b128 v[206:209], v13 offset:3072\n"
        "v_mfma_f32_16x16x32_bf16 v[122:125], a[64:67], v[112:115], v[122:125]\n"
        "s_add_u32 s38, s36, s94\n"
        "ds_read_b128 v[198:201], v13 offset:4096\n"
        "ds_read_b128 v[182:185], v13 offset:5120\n"
        "v_mfma_f32_16x16x32_bf16 v[122:125], a[68:71], v[118:121], v[122:125]\n"
        "s_addc_u32 s39, s37, 0\n"
        "ds_read_b128 v[194:197], v13 offset:6144\n"
        "ds_read_b128 v[202:205], v13 offset:7168\n"
        "v_mfma_f32_16x16x32_bf16 v[240:243], v[146:149], v[146:149], 0\n"
        "s_waitcnt lgkmcnt(8)\n"
        "ds_read_b128 v[218:221], v13 offset:8192\n"
        "ds_read_b128 v[190:193], v20\n"
        "v_mfma_f32_16x16x32_bf16 v[240:243], v[152:155], v[152:155], v[240:243]\n"
        "s_sub_u32 s85, s85, 1\n"
        "ds_read_b64 v[234:235], v20 offset:16\n"
        "ds_read_u16 v255, v22\n"
        "v_mfma_f32_16x16x32_bf16 v[240:243], v[158:161], v[158:161], v[240:243]\n"
        "ds_read_b64 v[164:165], v21\n"
        "v_cvt_pk_bf16_f32 v168, v142, v143\n"
        "v_mfma_f32_16x16x32_bf16 v[174:177], v[92:95], v[158:161], 0\n"
        "s_add_u32 m0, s52, 0\n"
        "v_cvt_pk_bf16_f32 v169, v144, v145\n"
        "v_cvt_pk_bf16_f32 v142, v178, v179\n"
        "v_mfma_f32_16x16x32_bf16 v[174:177], v[100:103], v[152:155], v[174:177]\n"
        "v_cvt_pk_bf16_f32 v143, v180, v181\n"
        "v_cvt_pk_bf16_f32 v144, v122, v123\n"
        "v_mfma_f32_16x16x32_bf16 v[174:177], v[96:99], v[146:149], v[174:177]\n"
        "global_load_lds_dwordx4 v0, s[38:39]\n"
        "v_cvt_pk_bf16_f32 v145, v124, v125\n"
        "v_mfma_f32_16x16x32_bf16 v[122:125], v[106:109], v[40:43], 0\n"
        "s_waitcnt lgkmcnt(10)\n"
        "v_readlane_b32 s93, v31, 1\n"
        "v_cndmask_b32_e64 v253, v240, v241, s[56:57]\n"
        "v_mfma_f32_16x16x32_bf16 v[122:125], v[112:115], v[166:169], v[122:125]\n"
        "s_waitcnt lgkmcnt(7)\n"
        "v_cndmask_b32_e64 v253, v253, v242, s[58:59]\n"
        "v_cndmask_b32_e64 v253, v253, v243, s[60:61]\n"
        "v_mfma_f32_16x16x32_bf16 v[122:125], v[118:121], v[142:145], v[122:125]\n"
        "v_mov_b32 v251, s29\n"
        "v_cndmask_b32_e64 v30, 0, v30, s[72:73]\n"
        "v_mfma_f32_16x16x32_bf16 v[40:43], v[236:239], v[186:189], 0\n"
        "ds_write_b16 v22, v251 offset:3584\n"
        "v_mfma_f32_16x16x32_bf16 v[40:43], v[170:173], v[210:213], v[40:43]\n"
        "s_waitcnt lgkmcnt(1)\n"
        "v_cndmask_b32_dpp v252, v253, v252, vcc row_ror:4 row_mask:0xf bank_mask:0xf\n"
        "v_cndmask_b32_dpp v253, v174, v224, vcc row_ror:4 row_mask:0xf bank_mask:0xf\n"
        "v_mfma_f32_16x16x32_bf16 v[40:43], v[34:37], v[214:217], v[40:43]\n"
        "v_cndmask_b32_dpp v254, v252, v254, vcc row_ror:4 row_mask:0xf bank_mask:0xf\n"
        "v_cndmask_b32_dpp v253, v253, v222, vcc row_ror:4 row_mask:0xf bank_mask:0xf\n"
        "v_mfma_f32_16x16x32_bf16 v[166:169], v[34:37], v[182:185], 0\n"
        "v_mul_f32 v252, s80, v253\n"
        "v_fma_f32 v252, -v252, v253, v254\n"
        "v_mfma_f32_16x16x32_bf16 v[166:169], v[170:173], v[198:201], v[166:169]\n"
        "global_load_lds_dwordx4 v1, s[38:39] offset:1024\n"
        "v_max_f32 v252, 0, v252\n"
        "v_mfma_f32_16x16x32_bf16 v[166:169], v[236:239], v[206:209], v[166:169]\n"
        "v_fmaak_f32 v252, v28, v252, 0x3727c5ac\n"
        "v_cndmask_b32_dpp v251, v175, v225, vcc row_ror:4 row_mask:0xf bank_mask:0xf\n"
        "v_mfma_f32_16x16x32_bf16 v[142:145], v[236:239], v[194:197], 0\n"
        "s_nop 0\n"
        "v_rsq_f32 v249, v252\n"
        "v_mfma_f32_16x16x32_bf16 v[142:145], v[170:173], v[202:205], v[142:145]\n"
        "s_nop 0\n"
        "v_cndmask_b32_dpp v251, v251, v223, vcc row_ror:4 row_mask:0xf bank_mask:0xf\n"
        "v_cndmask_b32_e64 v252, v40, v166, s[64:65]\n"
        "v_mfma_f32_16x16x32_bf16 v[142:145], v[34:37], v[218:221], v[142:145]\n"
        "s_nop 0\n"
        "v_cndmask_b32_e64 v254, v41, v167, s[64:65]\n"
        "v_mfma_f32_16x16x32_bf16 v[34:37], a[8:11], v[158:161], 0\n"
        "s_nop 2\n"
        "v_mfma_f32_16x16x32_bf16 v[34:37], a[4:7], v[152:155], v[34:37]\n"
        "s_nop 0\n"
        "v_cndmask_b32_e64 v252, v252, v142, s[70:71]\n"
        "v_cndmask_b32_e64 v254, v254, v143, s[70:71]\n"
        "v_mfma_f32_16x16x32_bf16 v[34:37], a[0:3], v[146:149], v[34:37]\n"
        "s_nop 0\n"
        "v_cvt_pk_f16_f32 v252, v252, v254\n"
        "v_pk_mul_f16 v252, v252, v255 op_sel_hi:[1,0]\n"
        "v_mfma_f32_16x16x32_bf16 v[40:43], a[12:15], v[146:149], 0\n"
        "global_load_lds_dwordx4 v2, s[38:39] offset:2048\n"
        "v_pk_mul_f16 v252, v252, s93 op_sel_hi:[1,0]\n"
        "v_mfma_f32_16x16x32_bf16 v[40:43], a[16:19], v[152:155], v[40:43]\n"
        "s_nop 0\n"
        "v_cvt_pk_bf16_f32 v166, v34, v35\n"
        "v_cvt_pk_bf16_f32 v167, v36, v37\n"
        "v_mfma_f32_16x16x32_bf16 v[40:43], a[20:23], v[158:161], v[40:43]\n"
        "s_nop 0\n"
        "v_exp_f16_sdwa v254, v252 dst_sel:WORD_1 dst_unused:UNUSED_PAD src0_sel:WORD_1\n"
        "v_mfma_f32_16x16x32_bf16 v[34:37], a[32:35], v[158:161], 0\n"
        "s_nop 0\n"
        "v_exp_f16_sdwa v225, v252 dst_sel:WORD_0 dst_unused:UNUSED_PAD src0_sel:WORD_0\n"
        "v_mfma_f32_16x16x32_bf16 v[34:37], a[28:31], v[152:155], v[34:37]\n"
        "s_nop 0\n"
        "v_cndmask_b32_e64 v225, v225, v30, s[68:69]\n"
        "v_cvt_pk_bf16_f32 v168, v40, v41\n"
        "v_mfma_f32_16x16x32_bf16 v[34:37], a[24:27], v[146:149], v[34:37]\n"
        "s_nop 0\n"
        "v_cvt_pk_bf16_f32 v169, v42, v43\n"
        "v_mfma_f32_16x16x32_bf16 v[40:43], a[36:39], v[146:149], 0\n"
        "s_nop 2\n"
        "v_mfma_f32_16x16x32_bf16 v[40:43], a[40:43], v[152:155], v[40:43]\n"
        "s_nop 0\n"
        "v_cvt_pk_bf16_f32 v170, v34, v35\n"
        "v_cvt_pk_bf16_f32 v171, v36, v37\n"
        "v_mfma_f32_16x16x32_bf16 v[40:43], a[44:47], v[158:161], v[40:43]\n"
        "global_load_lds_dwordx4 v3, s[38:39] offset:3072\n"
        "s_nop 0\n"
        "v_mfma_f32_16x16x32_bf16 v[142:145], a[56:59], v[158:161], 0\n"
        "s_nop 2\n"
        "v_mfma_f32_16x16x32_bf16 v[142:145], a[52:55], v[152:155], v[142:145]\n"
        "s_nop 0\n"
        "v_cvt_pk_bf16_f32 v172, v40, v41\n"
        "v_cvt_pk_bf16_f32 v173, v42, v43\n"
        "v_mfma_f32_16x16x32_bf16 v[142:145], a[48:51], v[146:149], v[142:145]\n"
        "s_nop 1\n"
        "v_mfma_f32_16x16x32_bf16 v[40:43], a[60:63], v[146:149], 0\n"
        "s_nop 2\n"
        "v_mfma_f32_16x16x32_bf16 v[40:43], a[64:67], v[152:155], v[40:43]\n"
        "s_nop 0\n"
        "v_cvt_pk_bf16_f32 v34, v142, v143\n"
        "v_cvt_pk_bf16_f32 v35, v144, v145\n"
        "v_mfma_f32_16x16x32_bf16 v[40:43], a[68:71], v[158:161], v[40:43]\n"
        "s_add_u32 m0, s52, 4096\n"
        "s_nop 0\n"
        "v_mfma_f32_16x16x32_bf16 v[142:145], a[152:155], v[214:217], 0\n"
        "s_nop 2\n"
        "v_mfma_f32_16x16x32_bf16 v[142:145], a[148:151], v[210:213], v[142:145]\n"
        "global_load_lds_dwordx4 v4, s[38:39]\n"
        "v_cvt_pk_bf16_f32 v36, v40, v41\n"
        "v_mfma_f32_16x16x32_bf16 v[142:145], a[144:147], v[186:189], v[142:145]\n"
        "s_nop 0\n"
        "v_cvt_pk_bf16_f32 v37, v42, v43\n"
        "v_mfma_f32_16x16x32_bf16 v[40:43], v[146:149], v[166:169], 0\n"
        "s_nop 2\n"
        "v_mfma_f32_16x16x32_bf16 v[40:43], v[152:155], v[170:173], v[40:43]\n"
        "s_nop 0\n"
        "v_cvt_pk_bf16_f32 v170, v142, v143\n"
        "v_cvt_pk_bf16_f32 v171, v144, v145\n"
        "v_mfma_f32_16x16x32_bf16 v[40:43], v[158:161], v[34:37], v[40:43]\n"
        "s_nop 1\n"
        "v_mfma_f32_16x16x32_bf16 v[34:37], a[164:167], v[214:217], 0\n"
        "s_nop 2\n"
        "v_mfma_f32_16x16x32_bf16 v[34:37], a[160:163], v[210:213], v[34:37]\n"
        "s_nop 0\n"
        "v_cndmask_b32_dpp v252, v40, v122, vcc row_ror:4 row_mask:0xf bank_mask:0xf\n"
        "v_cndmask_b32_dpp v247, v41, v123, vcc row_ror:4 row_mask:0xf bank_mask:0xf\n"
        "v_mfma_f32_16x16x32_bf16 v[34:37], a[156:159], v[186:189], v[34:37]\n"
        "v_cndmask_b32_dpp v245, v42, v124, vcc row_ror:4 row_mask:0xf bank_mask:0xf\n"
        "v_cndmask_b32_dpp v250, v43, v125, vcc row_ror:4 row_mask:0xf bank_mask:0xf\n"
        "v_mfma_f32_16x16x32_bf16 v[142:145], a[168:171], v[186:189], 0\n"
        "global_load_lds_dwordx4 v5, s[38:39] offset:1024\n"
        "v_cndmask_b32_dpp v252, v252, v228, vcc row_ror:4 row_mask:0xf bank_mask:0xf\n"
        "v_mfma_f32_16x16x32_bf16 v[142:145], a[172:175], v[210:213], v[142:145]\n"
        "v_cndmask_b32_dpp v247, v247, v229, vcc row_ror:4 row_mask:0xf bank_mask:0xf\n"
        "v_cndmask_b32_dpp v245, v245, v230, vcc row_ror:4 row_mask:0xf bank_mask:0xf\n"
        "v_mfma_f32_16x16x32_bf16 v[142:145], a[176:179], v[214:217], v[142:145]\n"
        "v_cndmask_b32_dpp v250, v250, v231, vcc row_ror:4 row_mask:0xf bank_mask:0xf\n"
        "v_mul_f32_dpp v252, v249, v252 quad_perm:[0,0,0,0] row_mask:0xf bank_mask:0xf\n"
        "v_mfma_f32_16x16x32_bf16 v[40:43], a[188:191], v[214:217], 0\n"
        "v_mul_f32_dpp v247, v249, v247 quad_perm:[1,1,1,1] row_mask:0xf bank_mask:0xf\n"
        "v_mul_f32_dpp v245, v249, v245 quad_perm:[2,2,2,2] row_mask:0xf bank_mask:0xf\n"
        "v_mfma_f32_16x16x32_bf16 v[40:43], a[184:187], v[210:213], v[40:43]\n"
        "v_mul_f32_dpp v250, v249, v250 quad_perm:[3,3,3,3] row_mask:0xf bank_mask:0xf\n"
        "v_max3_f32 v248, v252, v247, v245\n"
        "v_mfma_f32_16x16x32_bf16 v[40:43], a[180:183], v[186:189], v[40:43]\n"
        "v_max_f32 v248, v248, v250\n"
        "v_mul_f32 v248, v248, v249\n"
        "v_mfma_f32_16x16x32_bf16 v[166:169], a[192:195], v[186:189], 0\n"
        "v_fma_f32 v252, v252, v249, -v248\n"
        "v_fma_f32 v247, v247, v249, -v248\n"
        "v_mfma_f32_16x16x32_bf16 v[166:169], a[196:199], v[210:213], v[166:169]\n"
        "v_fma_f32 v245, v245, v249, -v248\n"
        "v_fma_f32 v250, v250, v249, -v248\n"
        "v_mfma_f32_16x16x32_bf16 v[166:169], a[200:203], v[214:217], v[166:169]\n"
        "global_load_lds_dwordx4 v6, s[38:39] offset:2048\n"
        "v_cvt_pk_bf16_f32 v172, v34, v35\n"
        "v_mfma_f32_16x16x32_bf16 v[122:125], a[212:215], v[214:217], 0\n"
        "v_exp_f32 v252, v252\n"
        "v_mfma_f32_16x16x32_bf16 v[122:125], a[208:211], v[210:213], v[122:125]\n"
        "v_cvt_pk_bf16_f32 v173, v36, v37\n"
        "v_cvt_pk_bf16_f32 v34, v142, v143\n"
        "v_mfma_f32_16x16x32_bf16 v[122:125], a[204:207], v[186:189], v[122:125]\n"
        "v_cvt_pk_bf16_f32 v35, v144, v145\n"
        "v_cvt_pk_bf16_f32 v36, v40, v41\n"
        "v_mfma_f32_16x16x32_bf16 v[174:177], v[126:129], a[72:75], 0\n"
        "v_cvt_pk_bf16_f32 v37, v42, v43\n"
        "v_cvt_pk_bf16_f32 v40, v166, v167\n"
        "v_mfma_f32_16x16x32_bf16 v[174:177], v[132:135], a[76:79], v[174:177]\n"
        "s_nop 0\n"
        "v_cvt_pk_bf16_f32 v41, v168, v169\n"
        "v_cvt_pk_bf16_f32 v42, v122, v123\n"
        "v_mfma_f32_16x16x32_bf16 v[174:177], v[138:141], a[80:83], v[174:177]\n"
        "v_cvt_pk_bf16_f32 v43, v124, v125\n"
        "v_mfma_f32_16x16x32_bf16 v[142:145], v[138:141], a[92:95], 0\n"
        "v_exp_f32 v247, v247\n"
        "v_mfma_f32_16x16x32_bf16 v[142:145], v[132:135], a[88:91], v[142:145]\n"
        "global_load_lds_dwordx4 v7, s[38:39] offset:3072\n"
        "v_add_f32 v246, v252, v247\n"
        "v_mfma_f32_16x16x32_bf16 v[142:145], v[126:129], a[84:87], v[142:145]\n"
        "v_exp_f32 v245, v245\n"
        "v_mfma_f32_16x16x32_bf16 v[178:181], v[186:189], v[170:173], 0\n"
        "v_exp_f32 v250, v250\n"
        "v_mfma_f32_16x16x32_bf16 v[178:181], v[210:213], v[34:37], v[178:181]\n"
        "v_add_f32 v248, v245, v250\n"
        "v_add_f32 v246, v246, v248\n"
        "v_mfma_f32_16x16x32_bf16 v[178:181], v[214:217], v[40:43], v[178:181]\n"
        "v_rcp_f32 v246, v246\n"
        "v_mfma_f32_16x16x32_bf16 v[166:169], v[182:185], v[40:43], 0\n"
        "v_mul_f32 v252, v252, v246\n"
        "v_mul_f32 v247, v247, v246\n"
        "v_mfma_f32_16x16x32_bf16 v[166:169], v[198:201], v[34:37], v[166:169]\n"
        "s_add_u32 m0, s52, 8192\n"
        "v_mul_f32 v245, v245, v246\n"
        "v_mul_f32 v250, v250, v246\n"
        "v_mfma_f32_16x16x32_bf16 v[166:169], v[206:209], v[170:173], v[166:169]\n"
        "v_mul_f32_dpp v252, v249, v252 quad_perm:[0,0,0,0] row_mask:0xf bank_mask:0xf\n"
        "v_mul_f32_dpp v247, v249, v247 quad_perm:[1,1,1,1] row_mask:0xf bank_mask:0xf\n"
        "v_mfma_f32_16x16x32_bf16 v[226:229], v[194:197], v[170:173], 0\n"
        "global_load_lds_dwordx4 v8, s[38:39]\n"
        "v_mul_f32_dpp v245, v249, v245 quad_perm:[2,2,2,2] row_mask:0xf bank_mask:0xf\n"
        "v_mfma_f32_16x16x32_bf16 v[226:229], v[202:205], v[34:37], v[226:229]\n"
        "v_mul_f32_dpp v250, v249, v250 quad_perm:[3,3,3,3] row_mask:0xf bank_mask:0xf\n"
        "v_cvt_pk_bf16_f32 v249, v252, v247\n"
        "v_mfma_f32_16x16x32_bf16 v[226:229], v[218:221], v[40:43], v[226:229]\n"
        "v_cvt_pk_bf16_f32 v248, v245, v250\n"
        "v_and_b32 v124, v25, v249\n"
        "v_mfma_f32_16x16x32_bf16 v[170:173], v[138:141], a[104:107], 0\n"
        "v_and_b32 v125, v25, v248\n"
        "v_mov_b64 v[130:131], v[124:125]\n"
        "v_mfma_f32_16x16x32_bf16 v[170:173], v[132:135], a[100:103], v[170:173]\n"
        "v_cvt_pk_bf16_f32 v42, v174, v175\n"
        "v_cvt_pk_bf16_f32 v43, v176, v177\n"
        "v_mfma_f32_16x16x32_bf16 v[170:173], v[126:129], a[96:99], v[170:173]\n"
        "v_cvt_pk_bf16_f32 v46, v142, v143\n"
        "v_cvt_pk_bf16_f32 v47, v144, v145\n"
        "v_mfma_f32_16x16x32_bf16 v[174:177], v[126:129], a[108:111], 0\n"
        "s_nop 0\n"
        "v_mov_b64 v[136:137], v[124:125]\n"
        "v_mov_b64 v[142:143], v[124:125]\n"
        "v_mfma_f32_16x16x32_bf16 v[174:177], v[132:135], a[112:115], v[174:177]\n"
        "s_nop 0\n"
        "v_cvt_pk_bf16_f32 v36, v170, v171\n"
        "v_cvt_pk_bf16_f32 v37, v172, v173\n"
        "v_mfma_f32_16x16x32_bf16 v[174:177], v[138:141], a[116:119], v[174:177]\n"
        "ds_read_u16 v246, v19 offset:2560\n"
        "v_and_b32_dpp v104, v249, v25 row_ror:12 row_mask:0xf bank_mask:0xf\n"
        "v_mfma_f32_16x16x32_bf16 v[236:239], v[42:45], v[124:127], 0\n"
        "s_nop 0\n"
        "v_and_b32_dpp v105, v248, v25 row_ror:12 row_mask:0xf bank_mask:0xf\n"
        "v_mov_b64 v[110:111], v[104:105]\n"
        "v_mfma_f32_16x16x32_bf16 v[122:125], v[44:47], v[128:131], 0\n"
        "s_nop 0\n"
        "v_cvt_pk_bf16_f32 v40, v174, v175\n"
        "v_cvt_pk_bf16_f32 v41, v176, v177\n"
        "v_mfma_f32_16x16x32_bf16 v[230:233], v[138:141], a[128:131], 0\n"
        "v_cvt_pk_bf16_f32 v170, v236, v237\n"
        "v_cvt_pk_bf16_f32 v236, v238, v239\n"
        "v_mfma_f32_16x16x32_bf16 v[230:233], v[132:135], a[124:127], v[230:233]\n"
        "s_nop 0\n"
        "v_cvt_pk_bf16_f32 v174, v122, v123\n"
        "v_cvt_pk_bf16_f32 v171, v124, v125\n"
        "v_mfma_f32_16x16x32_bf16 v[230:233], v[126:129], a[120:123], v[230:233]\n"
        "v_fmac_f32_dpp v253, v251, v252 quad_perm:[0,0,0,0] row_mask:0xf bank_mask:0xf\n"
        "v_and_b32_dpp v144, v249, v25 row_ror:8 row_mask:0xf bank_mask:0xf\n"
        "v_mfma_f32_16x16x32_bf16 v[238:241], v[126:129], a[132:135], 0\n"
        "s_nop 0\n"
        "v_and_b32_dpp v145, v248, v25 row_ror:8 row_mask:0xf bank_mask:0xf\n"
        "v_mov_b64 v[116:117], v[104:105]\n"
        "v_mfma_f32_16x16x32_bf16 v[238:241], v[132:135], a[136:139], v[238:241]\n"
        "s_nop 0\n"
        "v_cvt_pk_bf16_f32 v30, v230, v231\n"
        "v_cvt_pk_bf16_f32 v31, v232, v233\n"
        "v_mfma_f32_16x16x32_bf16 v[238:241], v[138:141], a[140:143], v[238:241]\n"
        "s_waitcnt lgkmcnt(0)\n"
        "v_cmp_ne_u32_e64 s[24:25], s29, v246\n"
        "v_mov_b64 v[122:123], v[104:105]\n"
        "v_mfma_f32_16x16x32_bf16 v[230:233], v[36:39], v[130:133], 0\n"
        "v_mov_b64 v[150:151], v[144:145]\n"
        "v_fmac_f32_dpp v253, v251, v247 quad_perm:[1,1,1,1] row_mask:0xf bank_mask:0xf\n"
        "v_mfma_f32_16x16x32_bf16 v[124:127], v[38:41], v[134:137], 0\n"
        "s_cmp_lg_u64 s[24:25], 0\n"
        "v_cvt_pk_bf16_f32 v34, v238, v239\n"
        "v_cvt_pk_bf16_f32 v35, v240, v241\n"
        "v_mfma_f32_16x16x32_bf16 v[128:131], v[118:121], a[80:83], 0\n"
        "s_cbranch_scc1 SP10_%=\n"
        "BK10_%=:\n"
        "v_mfma_f32_16x16x32_bf16 v[128:131], v[112:115], a[76:79], v[128:131]\n"
        "v_cvt_pk_bf16_f32 v237, v230, v231\n"
        "v_cvt_pk_bf16_f32 v175, v232, v233\n"
        "v_mfma_f32_16x16x32_bf16 v[128:131], v[106:109], a[72:75], v[128:131]\n"
        "v_cvt_pk_bf16_f32 v172, v124, v125\n"
        "v_cvt_pk_bf16_f32 v238, v126, v127\n"
        "v_mfma_f32_16x16x32_bf16 v[132:135], v[106:109], a[84:87], 0\n"
        "s_nop 0\n"
        "v_mov_b64 v[156:157], v[144:145]\n"
        "v_fmac_f32_dpp v253, v251, v245 quad_perm:[2,2,2,2] row_mask:0xf bank_mask:0xf\n"
        "v_mfma_f32_16x16x32_bf16 v[132:135], v[112:115], a[88:91], v[132:135]\n"
        "s_nop 0\n"
        "v_cvt_pk_bf16_f32 v42, v128, v129\n"
        "v_cvt_pk_bf16_f32 v43, v130, v131\n"
        "v_mfma_f32_16x16x32_bf16 v[132:135], v[118:121], a[92:95], v[132:135]\n"
        "v_mov_b64 v[162:163], v[144:145]\n"
        "v_fmac_f32_dpp v253, v251, v250 quad_perm:[3,3,3,3] row_mask:0xf bank_mask:0xf\n"
        "v_mfma_f32_16x16x32_bf16 v[128:131], v[30:33], v[136:139], 0\n"
        "s_nop 0\n"
        "v_cvt_pk_f16_f32 v126, v178, v179\n"
        "v_cvt_pk_f16_f32 v127, v180, v181\n"
        "v_mfma_f32_16x16x32_bf16 v[136:139], v[32:35], v[140:143], 0\n"
        "s_nop 0\n"
        "v_cvt_pk_bf16_f32 v46, v132, v133\n"
        "v_cvt_pk_bf16_f32 v47, v134, v135\n"
        "v_mfma_f32_16x16x32_bf16 v[132:135], v[118:121], a[104:107], 0\n"
        "v_cvt_pk_bf16_f32 v173, v130, v131\n"
        "v_cvt_pk_bf16_f32 v176, v128, v129\n"
        "v_mfma_f32_16x16x32_bf16 v[132:135], v[112:115], a[100:103], v[132:135]\n"
        "s_nop 0\n"
        "v_cvt_pk_bf16_f32 v239, v136, v137\n"
        "v_cvt_pk_bf16_f32 v177, v138, v139\n"
        "v_mfma_f32_16x16x32_bf16 v[132:135], v[106:109], a[96:99], v[132:135]\n"
        "s_nop 0\n"
        "ds_write_b128 v12, v[170:173]\n"
        "v_mfma_f32_16x16x32_bf16 v[138:141], v[106:109], a[108:111], 0\n"
        "s_nop 0\n"
        "v_pk_mul_f16 v126, v126, v164 op_sel_hi:[1,0]\n"
        "v_pk_mul_f16 v127, v127, v164 op_sel_hi:[1,0]\n"
        "v_mfma_f32_16x16x32_bf16 v[138:141], v[112:115], a[112:115], v[138:141]\n"
        "s_nop 0\n"
        "v_cvt_pk_bf16_f32 v36, v132, v133\n"
        "v_cvt_pk_bf16_f32 v37, v134, v135\n"
        "v_mfma_f32_16x16x32_bf16 v[138:141], v[118:121], a[116:119], v[138:141]\n"
        "s_nop 0\n"
        "ds_write_b128 v12, v[236:239] offset:1024\n"
        "v_mfma_f32_16x16x32_bf16 v[128:131], v[42:45], v[104:107], 0\n"
        "s_nop 0\n"
        "ds_write_b128 v12, v[174:177] offset:2048\n"
        "v_mfma_f32_16x16x32_bf16 v[178:181], v[44:47], v[108:111], 0\n"
        "s_nop 0\n"
        "v_pk_mul_f16 v126, v126, v190\n"
        "v_cvt_pk_bf16_f32 v40, v138, v139\n"
        "v_mfma_f32_16x16x32_bf16 v[136:139], v[170:173], v[170:173], 0\n"
        "v_cvt_pk_bf16_f32 v41, v140, v141\n"
        "v_pk_mul_f16 v127, v127, v191\n"
        "v_mfma_f32_16x16x32_bf16 v[136:139], v[236:239], v[236:239], v[136:139]\n"
        "v_exp_f16_sdwa v126, v126 dst_sel:WORD_0 dst_unused:UNUSED_PRESERVE src0_sel:WORD_0\n"
        "v_mfma_f32_16x16x32_bf16 v[136:139], v[174:177], v[174:177], v[136:139]\n"
        "v_exp_f16_sdwa v127, v127 dst_sel:WORD_0 dst_unused:UNUSED_PRESERVE src0_sel:WORD_0\n"
        "v_mfma_f32_16x16x32_bf16 v[132:135], v[118:121], a[128:131], 0\n"
        "v_exp_f16_sdwa v126, v126 dst_sel:WORD_1 dst_unused:UNUSED_PRESERVE src0_sel:WORD_1\n"
        "v_mfma_f32_16x16x32_bf16 v[132:135], v[112:115], a[124:127], v[132:135]\n"
        "v_cvt_pk_bf16_f32 v170, v128, v129\n"
        "v_cvt_pk_bf16_f32 v140, v130, v131\n"
        "v_mfma_f32_16x16x32_bf16 v[132:135], v[106:109], a[120:123], v[132:135]\n"
        "s_nop 0\n"
        "v_cvt_pk_bf16_f32 v130, v178, v179\n"
        "v_cvt_pk_bf16_f32 v171, v180, v181\n"
        "v_mfma_f32_16x16x32_bf16 v[172:175], v[106:109], a[132:135], 0\n"
        "s_nop 0\n"
        "v_exp_f16_sdwa v127, v127 dst_sel:WORD_1 dst_unused:UNUSED_PRESERVE src0_sel:WORD_1\n"
        "v_mfma_f32_16x16x32_bf16 v[172:175], v[112:115], a[136:139], v[172:175]\n"
        "s_nop 0\n"
        "v_cvt_pk_bf16_f32 v30, v132, v133\n"
        "v_cvt_pk_bf16_f32 v31, v134, v135\n"
        "v_mfma_f32_16x16x32_bf16 v[172:175], v[118:121], a[140:143], v[172:175]\n"
        "v_mul_f32 v252, s80, v253\n"
        "v_pk_add_f16 v222, v126, v127\n"
        "v_mfma_f32_16x16x32_bf16 v[132:135], v[36:39], v[110:113], 0\n"
        "s_nop 0\n"
        "v_cvt_pk_f16_f32 v128, v166, v167\n"
        "v_cvt_pk_f16_f32 v129, v168, v169\n"
        "v_mfma_f32_16x16x32_bf16 v[104:107], v[38:41], v[114:117], 0\n"
        "s_nop 0\n"
        "v_cvt_pk_bf16_f32 v34, v172, v173\n"
        "v_cvt_pk_bf16_f32 v35, v174, v175\n"
        "v_mfma_f32_16x16x32_bf16 v[108:111], v[158:161], a[80:83], 0\n"
        "v_cvt_pk_bf16_f32 v141, v132, v133\n"
        "v_cvt_pk_bf16_f32 v131, v134, v135\n"
        "v_mfma_f32_16x16x32_bf16 v[108:111], v[152:155], a[76:79], v[108:111]\n"
        "s_nop 0\n"
        "v_cvt_pk_bf16_f32 v172, v104, v105\n"
        "v_cvt_pk_bf16_f32 v142, v106, v107\n"
        "v_mfma_f32_16x16x32_bf16 v[108:111], v[146:149], a[72:75], v[108:111]\n"
        "v_pk_mul_f16 v128, v128, v164 op_sel_hi:[1,0]\n"
        "v_pk_mul_f16 v129, v129, v164 op_sel_hi:[1,0]\n"
        "v_mfma_f32_16x16x32_bf16 v[104:107], v[146:149], a[84:87], 0\n"
        "s_nop 0\n"
        "v_pk_mul_f16 v128, v128, v192\n"
        "v_pk_mul_f16 v129, v129, v193\n"
        "v_mfma_f32_16x16x32_bf16 v[104:107], v[152:155], a[88:91], v[104:107]\n"
        "s_nop 0\n"
        "v_cvt_pk_bf16_f32 v42, v108, v109\n"
        "v_cvt_pk_bf16_f32 v43, v110, v111\n"
        "v_mfma_f32_16x16x32_bf16 v[104:107], v[158:161], a[92:95], v[104:107]\n"
        "v_exp_f16_sdwa v128, v128 dst_sel:WORD_0 dst_unused:UNUSED_PRESERVE src0_sel:WORD_0\n"
        "v_mfma_f32_16x16x32_bf16 v[112:115], v[30:33], v[116:119], 0\n"
        "v_exp_f16_sdwa v129, v129 dst_sel:WORD_0 dst_unused:UNUSED_PRESERVE src0_sel:WORD_0\n"
        "v_mfma_f32_16x16x32_bf16 v[166:169], v[32:35], v[120:123], 0\n"
        "s_nop 0\n"
        "v_exp_f16_sdwa v128, v128 dst_sel:WORD_1 dst_unused:UNUSED_PRESERVE src0_sel:WORD_1\n"
        "v_mfma_f32_16x16x32_bf16 v[108:111], v[158:161], a[104:107], 0\n"
        "s_nop 0\n"
        "v_cvt_pk_bf16_f32 v46, v104, v105\n"
        "v_cvt_pk_bf16_f32 v47, v106, v107\n"
        "v_mfma_f32_16x16x32_bf16 v[108:111], v[152:155], a[100:103], v[108:111]\n"
        "v_cvt_pk_bf16_f32 v173, v114, v115\n"
        "v_cvt_pk_bf16_f32 v132, v112, v113\n"
        "v_mfma_f32_16x16x32_bf16 v[108:111], v[146:149], a[96:99], v[108:111]\n"
        "s_nop 0\n"
        "ds_write_b128 v12, v[170:173] offset:3072\n"
        "v_mfma_f32_16x16x32_bf16 v[114:117], v[146:149], a[108:111], 0\n"
        "s_nop 0\n"
        "v_cvt_pk_bf16_f32 v143, v166, v167\n"
        "v_cvt_pk_bf16_f32 v133, v168, v169\n"
        "v_mfma_f32_16x16x32_bf16 v[114:117], v[152:155], a[112:115], v[114:117]\n"
        "s_nop 0\n"
        "v_cvt_pk_bf16_f32 v36, v108, v109\n"
        "v_cvt_pk_bf16_f32 v37, v110, v111\n"
        "v_mfma_f32_16x16x32_bf16 v[114:117], v[158:161], a[116:119], v[114:117]\n"
        "ds_write_b128 v12, v[140:143] offset:4096\n"
        "v_mfma_f32_16x16x32_bf16 v[106:109], v[42:45], v[144:147], 0\n"
        "ds_write_b128 v12, v[130:133] offset:5120\n"
        "v_mfma_f32_16x16x32_bf16 v[110:113], v[44:47], v[148:151], 0\n"
        "s_nop 0\n"
        "v_exp_f16_sdwa v129, v129 dst_sel:WORD_1 dst_unused:UNUSED_PRESERVE src0_sel:WORD_1\n"
        "v_mfma_f32_16x16x32_bf16 v[118:121], v[170:173], v[170:173], 0\n"
        "s_nop 0\n"
        "v_cvt_pk_bf16_f32 v40, v114, v115\n"
        "v_cvt_pk_bf16_f32 v41, v116, v117\n"
        "v_mfma_f32_16x16x32_bf16 v[118:121], v[140:143], v[140:143], v[118:121]\n"
        "v_pk_add_f16 v222, v222, v128\n"
        "v_pk_add_f16 v222, v222, v129\n"
        "v_mfma_f32_16x16x32_bf16 v[118:121], v[130:133], v[130:133], v[118:121]\n"
        "v_cvt_pk_f16_f32 v104, v226, v227\n"
        "v_cvt_pk_bf16_f32 v114, v106, v107\n"
        "v_mfma_f32_16x16x32_bf16 v[122:125], v[158:161], a[128:131], 0\n"
        "v_cvt_pk_bf16_f32 v130, v108, v109\n"
        "v_cvt_pk_bf16_f32 v106, v110, v111\n"
        "v_mfma_f32_16x16x32_bf16 v[122:125], v[152:155], a[124:127], v[122:125]\n"
        "v_cvt_pk_bf16_f32 v115, v112, v113\n"
        "v_cvt_pk_f16_f32 v105, v228, v229\n"
        "v_mfma_f32_16x16x32_bf16 v[122:125], v[146:149], a[120:123], v[122:125]\n"
        "v_pk_mul_f16 v104, v104, v164 op_sel_hi:[1,0]\n"
        "v_pk_mul_f16 v105, v105, v164 op_sel_hi:[1,0]\n"
        "v_mfma_f32_16x16x32_bf16 v[140:143], v[146:149], a[132:135], 0\n"
        "s_nop 0\n"
        "v_pk_mul_f16 v104, v104, v234\n"
        "v_pk_mul_f16 v105, v105, v235\n"
        "v_mfma_f32_16x16x32_bf16 v[140:143], v[152:155], a[136:139], v[140:143]\n"
        "s_nop 0\n"
        "v_cvt_pk_bf16_f32 v30, v122, v123\n"
        "v_cvt_pk_bf16_f32 v31, v124, v125\n"
        "v_mfma_f32_16x16x32_bf16 v[140:143], v[158:161], a[140:143], v[140:143]\n"
        "v_cndmask_b32_e64 v250, v136, v137, s[56:57]\n"
        "v_cndmask_b32_e64 v250, v250, v138, s[58:59]\n"
        "v_mfma_f32_16x16x32_bf16 v[132:135], v[36:39], v[150:153], 0\n"
        "s_nop 0\n"
        "v_cndmask_b32_e64 v250, v250, v139, s[60:61]\n"
        "v_cndmask_b32_e64 v251, v118, v119, s[56:57]\n"
        "v_mfma_f32_16x16x32_bf16 v[108:111], v[38:41], v[154:157], 0\n"
        "s_nop 0\n"
        "v_cvt_pk_bf16_f32 v34, v140, v141\n"
        "v_cvt_pk_bf16_f32 v35, v142, v143\n"
        "v_mfma_f32_16x16x32_bf16 v[122:125], a[152:155], v[182:185], 0\n"
        "v_cvt_pk_bf16_f32 v131, v132, v133\n"
        "v_cvt_pk_bf16_f32 v107, v134, v135\n"
        "v_mfma_f32_16x16x32_bf16 v[122:125], a[148:151], v[198:201], v[122:125]\n"
        "s_nop 0\n"
        "v_cvt_pk_bf16_f32 v116, v108, v109\n"
        "v_cvt_pk_bf16_f32 v132, v110, v111\n"
        "v_mfma_f32_16x16x32_bf16 v[122:125], a[144:147], v[206:209], v[122:125]\n"
        "s_nop 0\n"
        "v_cndmask_b32_e64 v251, v251, v120, s[58:59]\n"
        "v_cndmask_b32_e64 v251, v251, v121, s[60:61]\n"
        "v_mfma_f32_16x16x32_bf16 v[110:113], v[30:33], v[156:159], 0\n"
        "s_nop 0\n"
        "v_exp_f16_sdwa v104, v104 dst_sel:WORD_0 dst_unused:UNUSED_PRESERVE src0_sel:WORD_0\n"
        "v_mfma_f32_16x16x32_bf16 v[40:43], v[32:35], v[160:163], 0\n"
        "s_nop 0\n"
        "v_cvt_pk_bf16_f32 v118, v122, v123\n"
        "v_cvt_pk_bf16_f32 v119, v124, v125\n"
        "v_mfma_f32_16x16x32_bf16 v[34:37], a[156:159], v[206:209], 0\n"
        "s_nop 0\n"
        "v_cvt_pk_bf16_f32 v117, v112, v113\n"
        "v_cvt_pk_bf16_f32 v108, v110, v111\n"
        "v_mfma_f32_16x16x32_bf16 v[34:37], a[160:163], v[198:201], v[34:37]\n"
        "v_cvt_pk_bf16_f32 v133, v40, v41\n"
        "v_cvt_pk_bf16_f32 v109, v42, v43\n"
        "v_mfma_f32_16x16x32_bf16 v[34:37], a[164:167], v[182:185], v[34:37]\n"
        "s_nop 0\n"
        "ds_write_b128 v12, v[114:117] offset:6144\n"
        "v_mfma_f32_16x16x32_bf16 v[40:43], a[176:179], v[182:185], 0\n"
        "s_waitcnt vmcnt(9)\n"
        "v_readfirstlane_b32 s50, v26\n"
        "s_nop 0\n"
        "v_mfma_f32_16x16x32_bf16 v[40:43], a[172:175], v[198:201], v[40:43]\n"
        "s_nop 0\n"
        "v_cvt_pk_bf16_f32 v120, v34, v35\n"
        "v_cvt_pk_bf16_f32 v121, v36, v37\n"
        "v_mfma_f32_16x16x32_bf16 v[40:43], a[168:171], v[206:209], v[40:43]\n"
        "s_sub_u32 s50, s50, s20\n"
        "ds_write_b128 v12, v[130:133] offset:7168\n"
        "v_mfma_f32_16x16x32_bf16 v[122:125], v[114:117], v[114:117], 0\n"
        "s_min_u32 s50, s50, 32\n"
        "v_exp_f16_sdwa v105, v105 dst_sel:WORD_0 dst_unused:UNUSED_PRESERVE src0_sel:WORD_0\n"
        "v_mfma_f32_16x16x32_bf16 v[122:125], v[130:133], v[130:133], v[122:125]\n"
        "s_lshl_b32 s50, s50, 8\n"
        "v_exp_f16_sdwa v104, v104 dst_sel:WORD_1 dst_unused:UNUSED_PRESERVE src0_sel:WORD_1\n"
        "v_mfma_f32_16x16x32_bf16 v[122:125], v[106:109], v[106:109], v[122:125]\n"
        "s_cmp_lt_i32 s85, s49\n"
        "ds_write_b128 v12, v[106:109] offset:8192\n"
        "v_mfma_f32_16x16x32_bf16 v[34:37], a[188:191], v[182:185], 0\n"
        "s_cselect_b32 s50, s50, 0\n"
        "v_cvt_pk_bf16_f32 v110, v40, v41\n"
        "v_cvt_pk_bf16_f32 v111, v42, v43\n"
        "v_mfma_f32_16x16x32_bf16 v[34:37], a[184:187], v[198:201], v[34:37]\n"
        "s_or_b32 s50, s50, s29\n"
        "v_cndmask_b32_e64 v249, v122, v123, s[56:57]\n"
        "v_cndmask_b32_e64 v249, v249, v124, s[58:59]\n"
        "v_mfma_f32_16x16x32_bf16 v[34:37], a[180:183], v[206:209], v[34:37]\n"
        "s_nop 0\n"
        "v_cndmask_b32_e64 v249, v249, v125, s[60:61]\n"
        "v_pk_add_f16 v222, v222, v104\n"
        "v_mfma_f32_16x16x32_bf16 v[40:43], a[192:195], v[206:209], 0\n"
        "s_nop 0\n"
        "v_cndmask_b32_dpp v251, v249, v251, vcc row_ror:4 row_mask:0xf bank_mask:0xf\n"
        "v_mfma_f32_16x16x32_bf16 v[40:43], a[196:199], v[198:201], v[40:43]\n"
        "s_nop 0\n"
        "v_cvt_pk_bf16_f32 v112, v34, v35\n"
        "v_cvt_pk_bf16_f32 v113, v36, v37\n"
        "v_mfma_f32_16x16x32_bf16 v[40:43], a[200:203], v[182:185], v[40:43]\n"
        "v_cndmask_b32_dpp v250, v251, v250, vcc row_ror:4 row_mask:0xf bank_mask:0xf\n"
        "v_fma_f32 v252, -v252, v253, v250\n"
        "v_mfma_f32_16x16x32_bf16 v[34:37], a[212:215], v[182:185], 0\n"
        "s_nop 0\n"
        "v_max_f32 v252, 0, v252\n"
        "v_sub_f32 v250, 0x47c00000, v250\n"
        "v_mfma_f32_16x16x32_bf16 v[34:37], a[208:211], v[198:201], v[34:37]\n"
        "s_nop 0\n"
        "v_cvt_pk_bf16_f32 v130, v40, v41\n"
        "v_cvt_pk_bf16_f32 v131, v42, v43\n"
        "v_mfma_f32_16x16x32_bf16 v[34:37], a[204:207], v[206:209], v[34:37]\n"
        "s_nop 0\n"
        "v_fmaak_f32 v252, v28, v252, 0x3727c5ac\n"
        "v_bfi_b32 v252, s84, v250, v252\n"
        "v_mfma_f32_16x16x32_bf16 v[40:43], a[144:147], v[194:197], 0\n"
        "s_nop 0\n"
        "v_rsq_f32 v253, v252\n"
        "v_mfma_f32_16x16x32_bf16 v[40:43], a[148:151], v[202:205], v[40:43]\n"
        "s_nop 0\n"
        "v_cvt_pk_bf16_f32 v132, v34, v35\n"
        "v_cvt_pk_bf16_f32 v133, v36, v37\n"
        "v_mfma_f32_16x16x32_bf16 v[40:43], a[152:155], v[218:221], v[40:43]\n"
        "v_cvt_f16_f32 v253, v253\n"
        "v_mfma_f32_16x16x32_bf16 v[114:117], v[186:189], v[118:121], 0\n"
        "ds_write_b16 v18, v253\n"
        "v_mfma_f32_16x16x32_bf16 v[114:117], v[210:213], v[110:113], v[114:117]\n"
        "ds_write_b16 v19, v253\n"
        "v_mfma_f32_16x16x32_bf16 v[114:117], v[214:217], v[130:133], v[114:117]\n"
        "s_nop 0\n"
        "v_exp_f16_sdwa v105, v105 dst_sel:WORD_1 dst_unused:UNUSED_PRESERVE src0_sel:WORD_1\n"
        "v_mfma_f32_16x16x32_bf16 v[122:125], v[182:185], v[130:133], 0\n"
        "s_nop 0\n"
        "v_pk_add_f16 v222, v222, v105\n"
        "v_cvt_pk_bf16_f32 v34, v40, v41\n"
        "v_mfma_f32_16x16x32_bf16 v[122:125], v[198:201], v[110:113], v[122:125]\n"
        "s_nop 0\n"
        "v_cvt_pk_f16_f32 v108, v114, v115\n"
        "v_cvt_pk_f16_f32 v109, v116, v117\n"
        "v_mfma_f32_16x16x32_bf16 v[122:125], v[206:209], v[118:121], v[122:125]\n"
        "v_pk_mul_f16 v108, v108, v164 op_sel:[0,1]\n"
        "v_pk_mul_f16 v109, v109, v164 op_sel:[0,1]\n"
        "v_mfma_f32_16x16x32_bf16 v[114:117], v[194:197], v[118:121], 0\n"
        "v_pk_mul_f16 v108, v108, v190\n"
        "v_pk_mul_f16 v109, v109, v191\n"
        "v_mfma_f32_16x16x32_bf16 v[114:117], v[202:205], v[110:113], v[114:117]\n"
        "v_mov_b32 v253, s50\n"
        "v_cvt_pk_bf16_f32 v35, v42, v43\n"
        "v_mfma_f32_16x16x32_bf16 v[114:117], v[218:221], v[130:133], v[114:117]\n"
        "ds_write_b16 v22, v253 offset:3072\n"
        "v_mfma_f32_16x16x32_bf16 v[40:43], a[164:167], v[218:221], 0\n"
        "v_exp_f16_sdwa v108, v108 dst_sel:WORD_0 dst_unused:UNUSED_PRESERVE src0_sel:WORD_0\n"
        "v_mfma_f32_16x16x32_bf16 v[40:43], a[160:163], v[202:205], v[40:43]\n"
        "v_exp_f16_sdwa v109, v109 dst_sel:WORD_0 dst_unused:UNUSED_PRESERVE src0_sel:WORD_0\n"
        "v_mfma_f32_16x16x32_bf16 v[40:43], a[156:159], v[194:197], v[40:43]\n"
        "v_exp_f16_sdwa v108, v108 dst_sel:WORD_1 dst_unused:UNUSED_PRESERVE src0_sel:WORD_1\n"
        "v_mfma_f32_16x16x32_bf16 v[134:137], a[168:171], v[194:197], 0\n"
        "v_exp_f16_sdwa v109, v109 dst_sel:WORD_1 dst_unused:UNUSED_PRESERVE src0_sel:WORD_1\n"
        "v_mfma_f32_16x16x32_bf16 v[134:137], a[172:175], v[202:205], v[134:137]\n"
        "v_pk_add_f16 v223, v108, v109\n"
        "v_cvt_pk_f16_f32 v110, v122, v123\n"
        "v_mfma_f32_16x16x32_bf16 v[134:137], a[176:179], v[218:221], v[134:137]\n"
        "v_cvt_pk_f16_f32 v111, v124, v125\n"
        "v_pk_mul_f16 v110, v110, v164 op_sel:[0,1]\n"
        "v_mfma_f32_16x16x32_bf16 v[130:133], a[188:191], v[218:221], 0\n"
        "v_pk_mul_f16 v111, v111, v164 op_sel:[0,1]\n"
        "v_pk_mul_f16 v110, v110, v192\n"
        "v_mfma_f32_16x16x32_bf16 v[130:133], a[184:187], v[202:205], v[130:133]\n"
        "v_pk_mul_f16 v111, v111, v193\n"
        "v_cvt_pk_bf16_f32 v36, v40, v41\n"
        "v_mfma_f32_16x16x32_bf16 v[130:133], a[180:183], v[194:197], v[130:133]\n"
        "v_exp_f16_sdwa v110, v110 dst_sel:WORD_0 dst_unused:UNUSED_PRESERVE src0_sel:WORD_0\n"
        "v_mfma_f32_16x16x32_bf16 v[118:121], a[192:195], v[194:197], 0\n"
        "v_exp_f16_sdwa v111, v111 dst_sel:WORD_0 dst_unused:UNUSED_PRESERVE src0_sel:WORD_0\n"
        "v_mfma_f32_16x16x32_bf16 v[118:121], a[196:199], v[202:205], v[118:121]\n"
        "v_exp_f16_sdwa v110, v110 dst_sel:WORD_1 dst_unused:UNUSED_PRESERVE src0_sel:WORD_1\n"
        "v_mfma_f32_16x16x32_bf16 v[118:121], a[200:203], v[218:221], v[118:121]\n"
        "v_cvt_pk_bf16_f32 v37, v42, v43\n"
        "v_cvt_pk_bf16_f32 v122, v134, v135\n"
        "v_mfma_f32_16x16x32_bf16 v[40:43], a[212:215], v[218:221], 0\n"
        "v_cvt_pk_bf16_f32 v123, v136, v137\n"
        "v_cvt_pk_bf16_f32 v124, v130, v131\n"
        "v_mfma_f32_16x16x32_bf16 v[40:43], a[208:211], v[202:205], v[40:43]\n"
        "s_nop 0\n"
        "v_cvt_pk_bf16_f32 v125, v132, v133\n"
        "v_cvt_pk_bf16_f32 v130, v118, v119\n"
        "v_mfma_f32_16x16x32_bf16 v[40:43], a[204:207], v[194:197], v[40:43]\n"
        "s_nop 0\n"
        "v_cvt_pk_bf16_f32 v131, v120, v121\n"
        "v_pk_add_f16 v223, v223, v110\n"
        "v_mfma_f32_16x16x32_bf16 v[118:121], v[186:189], a[216:219], 0\n"
        "s_nop 0\n"
        "v_exp_f16_sdwa v111, v111 dst_sel:WORD_1 dst_unused:UNUSED_PRESERVE src0_sel:WORD_1\n"
        "v_mfma_f32_16x16x32_bf16 v[118:121], v[210:213], a[220:223], v[118:121]\n"
        "s_nop 0\n"
        "v_cvt_pk_bf16_f32 v132, v40, v41\n"
        "v_cvt_pk_bf16_f32 v133, v42, v43\n"
        "v_mfma_f32_16x16x32_bf16 v[118:121], v[214:217], a[224:227], v[118:121]\n"
        "v_pk_add_f16 v223, v223, v111\n"
        "v_cvt_pk_f16_f32 v112, v114, v115\n"
        "v_mfma_f32_16x16x32_bf16 v[134:137], v[182:185], a[224:227], 0\n"
        "v_cvt_pk_f16_f32 v113, v116, v117\n"
        "v_pk_mul_f16 v112, v112, v164 op_sel:[0,1]\n"
        "v_mfma_f32_16x16x32_bf16 v[134:137], v[198:201], a[220:223], v[134:137]\n"
        "v_pk_mul_f16 v113, v113, v164 op_sel:[0,1]\n"
        "v_pk_mul_f16 v112, v112, v234\n"
        "v_mfma_f32_16x16x32_bf16 v[134:137], v[206:209], a[216:219], v[134:137]\n"
        "v_pk_mul_f16 v113, v113, v235\n"
        "v_cvt_pk_f16_f32 v178, v118, v119\n"
        "v_mfma_f32_16x16x32_bf16 v[40:43], v[194:197], a[216:219], 0\n"
        "v_exp_f16_sdwa v112, v112 dst_sel:WORD_0 dst_unused:UNUSED_PRESERVE src0_sel:WORD_0\n"
        "v_mfma_f32_16x16x32_bf16 v[40:43], v[202:205], a[220:223], v[40:43]\n"
        "v_exp_f16_sdwa v113, v113 dst_sel:WORD_0 dst_unused:UNUSED_PRESERVE src0_sel:WORD_0\n"
        "v_mfma_f32_16x16x32_bf16 v[40:43], v[218:221], a[224:227], v[40:43]\n"
        "v_exp_f16_sdwa v112, v112 dst_sel:WORD_1 dst_unused:UNUSED_PRESERVE src0_sel:WORD_1\n"
        "v_mfma_f32_16x16x32_bf16 v[114:117], v[186:189], v[34:37], 0\n"
        "v_exp_f16_sdwa v113, v113 dst_sel:WORD_1 dst_unused:UNUSED_PRESERVE src0_sel:WORD_1\n"
        "v_mfma_f32_16x16x32_bf16 v[114:117], v[210:213], v[122:125], v[114:117]\n"
        "v_pk_add_f16 v223, v223, v112\n"
        "v_pk_add_f16 v223, v223, v113\n"
        "v_mfma_f32_16x16x32_bf16 v[114:117], v[214:217], v[130:133], v[114:117]\n"
        "v_cvt_pk_f16_f32 v179, v120, v121\n"
        "v_cvt_pk_f16_f32 v180, v134, v135\n"
        "v_mfma_f32_16x16x32_bf16 v[138:141], v[182:185], v[130:133], 0\n"
        "s_nop 0\n"
        "v_cvt_pk_f16_f32 v181, v136, v137\n"
        "v_cvt_pk_f16_f32 v174, v40, v41\n"
        "v_mfma_f32_16x16x32_bf16 v[138:141], v[198:201], v[122:125], v[138:141]\n"
        "s_nop 0\n"
        "v_cvt_pk_f16_f32 v226, v114, v115\n"
        "v_cvt_pk_f16_f32 v227, v116, v117\n"
        "v_mfma_f32_16x16x32_bf16 v[138:141], v[206:209], v[34:37], v[138:141]\n"
        "v_pk_mul_f16 v226, v226, v165 op_sel_hi:[1,0]\n"
        "v_pk_mul_f16 v227, v227, v165 op_sel_hi:[1,0]\n"
        "v_mfma_f32_16x16x32_bf16 v[116:119], v[194:197], v[34:37], 0\n"
        "v_pk_mul_f16 v226, v226, v190\n"
        "v_pk_mul_f16 v227, v227, v191\n"
        "v_mfma_f32_16x16x32_bf16 v[116:119], v[202:205], v[122:125], v[116:119]\n"
        "v_exp_f16_sdwa v226, v226 dst_sel:WORD_0 dst_unused:UNUSED_PRESERVE src0_sel:WORD_0\n"
        "v_mfma_f32_16x16x32_bf16 v[116:119], v[218:221], v[130:133], v[116:119]\n"
        "v_exp_f16_sdwa v227, v227 dst_sel:WORD_0 dst_unused:UNUSED_PRESERVE src0_sel:WORD_0\n"
        "v_mfma_f32_16x16x32_bf16 v[34:37], v[218:221], a[236:239], 0\n"
        "v_exp_f16_sdwa v226, v226 dst_sel:WORD_1 dst_unused:UNUSED_PRESERVE src0_sel:WORD_1\n"
        "v_mfma_f32_16x16x32_bf16 v[34:37], v[202:205], a[232:235], v[34:37]\n"
        "v_exp_f16_sdwa v227, v227 dst_sel:WORD_1 dst_unused:UNUSED_PRESERVE src0_sel:WORD_1\n"
        "v_mfma_f32_16x16x32_bf16 v[34:37], v[194:197], a[228:231], v[34:37]\n"
        "v_pk_add_f16 v224, v226, v227\n"
        "v_cvt_pk_f16_f32 v228, v138, v139\n"
        "v_mfma_f32_16x16x32_bf16 v[134:137], v[206:209], a[228:231], 0\n"
        "v_cndmask_b32_e64 v106, 0, v254, s[62:63]\n"
        "v_cndmask_b32_e64 v114, 0, v254, s[64:65]\n"
        "v_mfma_f32_16x16x32_bf16 v[134:137], v[198:201], a[232:235], v[134:137]\n"
        "v_cndmask_b32_e64 v232, 0, v254, s[66:67]\n"
        "v_pk_add_f16 v222, v222, v106\n"
        "v_mfma_f32_16x16x32_bf16 v[134:137], v[182:185], a[236:239], v[134:137]\n"
        "v_pk_add_f16 v223, v223, v114\n"
        "v_cvt_pk_f16_f32 v229, v140, v141\n"
        "v_mfma_f32_16x16x32_bf16 v[120:123], v[214:217], a[236:239], 0\n"
        "v_pk_mul_f16 v228, v228, v165 op_sel_hi:[1,0]\n"
        "v_pk_mul_f16 v229, v229, v165 op_sel_hi:[1,0]\n"
        "v_mfma_f32_16x16x32_bf16 v[120:123], v[210:213], a[232:235], v[120:123]\n"
        "v_pk_mul_f16 v228, v228, v192\n"
        "v_pk_mul_f16 v229, v229, v193\n"
        "v_mfma_f32_16x16x32_bf16 v[120:123], v[186:189], a[228:231], v[120:123]\n"
        "v_exp_f16_sdwa v228, v228 dst_sel:WORD_0 dst_unused:UNUSED_PRESERVE src0_sel:WORD_0\n"
        "v_mfma_f32_16x16x32_bf16 v[154:157], v[186:189], a[240:243], 0\n"
        "v_exp_f16_sdwa v229, v229 dst_sel:WORD_0 dst_unused:UNUSED_PRESERVE src0_sel:WORD_0\n"
        "v_mfma_f32_16x16x32_bf16 v[154:157], v[210:213], a[244:247], v[154:157]\n"
        "v_exp_f16_sdwa v228, v228 dst_sel:WORD_1 dst_unused:UNUSED_PRESERVE src0_sel:WORD_1\n"
        "v_mfma_f32_16x16x32_bf16 v[154:157], v[214:217], a[248:251], v[154:157]\n"
        "v_exp_f16_sdwa v229, v229 dst_sel:WORD_1 dst_unused:UNUSED_PRESERVE src0_sel:WORD_1\n"
        "v_mfma_f32_16x16x32_bf16 v[130:133], v[182:185], a[248:251], 0\n"
        "v_pk_add_f16 v224, v224, v228\n"
        "v_pk_add_f16 v224, v224, v229\n"
        "v_mfma_f32_16x16x32_bf16 v[130:133], v[198:201], a[244:247], v[130:133]\n"
        "v_cvt_pk_f16_f32 v230, v116, v117\n"
        "v_cvt_pk_f16_f32 v231, v118, v119\n"
        "v_mfma_f32_16x16x32_bf16 v[130:133], v[206:209], a[240:243], v[130:133]\n"
        "v_pk_mul_f16 v230, v230, v165 op_sel_hi:[1,0]\n"
        "v_pk_mul_f16 v231, v231, v165 op_sel_hi:[1,0]\n"
        "v_mfma_f32_16x16x32_bf16 v[150:153], v[194:197], a[240:243], 0\n"
        "v_pk_mul_f16 v230, v230, v234\n"
        "v_pk_mul_f16 v231, v231, v235\n"
        "v_mfma_f32_16x16x32_bf16 v[150:153], v[202:205], a[244:247], v[150:153]\n"
        "v_exp_f16_sdwa v230, v230 dst_sel:WORD_0 dst_unused:UNUSED_PRESERVE src0_sel:WORD_0\n"
        "v_mfma_f32_16x16x32_bf16 v[150:153], v[218:221], a[248:251], v[150:153]\n"
        "v_exp_f16_sdwa v231, v231 dst_sel:WORD_0 dst_unused:UNUSED_PRESERVE src0_sel:WORD_0\n"
        "v_mfma_f32_16x16x32_bf16 v[116:119], v[218:221], v[64:67], 0\n"
        "v_exp_f16_sdwa v230, v230 dst_sel:WORD_1 dst_unused:UNUSED_PRESERVE src0_sel:WORD_1\n"
        "v_mfma_f32_16x16x32_bf16 v[116:119], v[202:205], v[72:75], v[116:119]\n"
        "v_exp_f16_sdwa v231, v231 dst_sel:WORD_1 dst_unused:UNUSED_PRESERVE src0_sel:WORD_1\n"
        "v_mfma_f32_16x16x32_bf16 v[116:119], v[194:197], a[252:255], v[116:119]\n"
        "v_pk_add_f16 v224, v224, v230\n"
        "v_pk_add_f16 v224, v224, v231\n"
        "v_mfma_f32_16x16x32_bf16 v[146:149], v[206:209], a[252:255], 0\n"
        "v_pk_add_f16 v224, v224, v232\n"
        "v_cvt_pk_f16_f32 v175, v42, v43\n"
        "v_mfma_f32_16x16x32_bf16 v[146:149], v[198:201], v[72:75], v[146:149]\n"
        "ds_read2_b32 v[176:177], v24 offset0:0 offset1:0\n"
        "ds_read2_b32 v[168:169], v24 offset0:1 offset1:1\n"
        "v_mfma_f32_16x16x32_bf16 v[146:149], v[182:185], v[64:67], v[146:149]\n"
        "ds_read2_b32 v[144:145], v24 offset0:2 offset1:2\n"
        "ds_read2_b32 v[172:173], v24 offset0:3 offset1:3\n"
        "v_mfma_f32_16x16x32_bf16 v[138:141], v[214:217], v[64:67], 0\n"
        "ds_read2_b32 v[124:125], v24 offset0:4 offset1:4\n"
        "v_mov_b32 v107, 0\n"
        "v_mfma_f32_16x16x32_bf16 v[138:141], v[210:213], v[72:75], v[138:141]\n"
        "ds_read2_b32 v[42:43], v24 offset0:5 offset1:5\n"
        "v_cvt_pk_f16_f32 v166, v34, v35\n"
        "v_mfma_f32_16x16x32_bf16 v[138:141], v[186:189], a[252:255], v[138:141]\n"
        "v_cvt_pk_f16_f32 v167, v36, v37\n"
        "v_cvt_pk_f16_f32 v36, v134, v135\n"
        "v_mfma_f32_16x16x32_f16 v[248:251], v[80:83], v[222:225], 0\n"
        "v_cvt_pk_f16_f32 v37, v136, v137\n"
        "v_cvt_pk_f16_f32 v34, v120, v121\n"
        "v_mfma_f32_16x16x32_bf16 v[158:161], v[186:189], v[68:71], 0\n"
        "v_cvt_pk_f16_f32 v35, v122, v123\n"
        "v_cvt_pk_f16_f32 v162, v154, v155\n"
        "v_mfma_f32_16x16x32_bf16 v[158:161], v[210:213], v[60:63], v[158:161]\n"
        "v_cvt_pk_f16_f32 v163, v156, v157\n"
        "v_cvt_pk_f16_f32 v164, v130, v131\n"
        "v_mfma_f32_16x16x32_bf16 v[158:161], v[214:217], v[56:59], v[158:161]\n"
        "v_cvt_pk_f16_f32 v165, v132, v133\n"
        "v_cvt_pk_f16_f32 v142, v150, v151\n"
        "v_mfma_f32_16x16x32_bf16 v[120:123], v[182:185], v[56:59], 0\n"
        "v_cvt_pk_f16_f32 v143, v152, v153\n"
        "v_cvt_pk_f16_f32 v170, v116, v117\n"
        "v_mfma_f32_16x16x32_bf16 v[120:123], v[198:201], v[60:63], v[120:123]\n"
        "v_cvt_pk_f16_f32 v171, v118, v119\n"
        "v_cvt_pk_f16_f32 v246, v146, v147\n"
        "v_mfma_f32_16x16x32_bf16 v[120:123], v[206:209], v[68:71], v[120:123]\n"
        "s_waitcnt lgkmcnt(0)\n"
        "v_rcp_f32 v248, v248\n"
        "v_mfma_f32_16x16x32_bf16 v[134:137], v[194:197], v[68:71], 0\n"
        "v_rcp_f32 v249, v249\n"
        "v_mfma_f32_16x16x32_bf16 v[134:137], v[202:205], v[60:63], v[134:137]\n"
        "v_cvt_pk_f16_f32 v254, v248, v249\n"
        "v_pk_mul_f16 v126, v126, v254 op_sel_hi:[1,0]\n"
        "v_mfma_f32_16x16x32_bf16 v[134:137], v[218:221], v[56:59], v[134:137]\n"
        "v_pk_mul_f16 v127, v127, v254 op_sel_hi:[1,0]\n"
        "v_pk_mul_f16 v128, v128, v254 op_sel_hi:[1,0]\n"
        "v_mfma_f32_16x16x32_bf16 v[116:119], v[218:221], v[84:87], 0\n"
        "v_pk_mul_f16 v129, v129, v254 op_sel_hi:[1,0]\n"
        "v_pk_mul_f16 v104, v104, v254 op_sel_hi:[1,0]\n"
        "v_mfma_f32_16x16x32_bf16 v[116:119], v[202:205], v[76:79], v[116:119]\n"
        "v_pk_mul_f16 v105, v105, v254 op_sel_hi:[1,0]\n"
        "v_pk_mul_f16 v106, v106, v254 op_sel_hi:[1,0]\n"
        "v_mfma_f32_16x16x32_bf16 v[116:119], v[194:197], v[52:55], v[116:119]\n"
        "v_pk_mul_f16 v126, v126, v190\n"
        "v_pk_mul_f16 v127, v127, v191\n"
        "v_mfma_f32_16x16x32_bf16 v[130:133], v[206:209], v[52:55], 0\n"
        "v_pk_mul_f16 v128, v128, v192\n"
        "v_pk_mul_f16 v129, v129, v193\n"
        "v_mfma_f32_16x16x32_bf16 v[130:133], v[198:201], v[76:79], v[130:133]\n"
        "v_pk_mul_f16 v104, v104, v234\n"
        "v_pk_mul_f16 v105, v105, v235\n"
        "v_mfma_f32_16x16x32_bf16 v[130:133], v[182:185], v[84:87], v[130:133]\n"
        "v_mov_b32 v115, 0\n"
        "v_cvt_pk_f16_f32 v247, v148, v149\n"
        "v_mfma_f32_16x16x32_bf16 v[146:149], v[214:217], v[84:87], 0\n"
        "v_cvt_pk_f16_f32 v244, v138, v139\n"
        "v_cvt_pk_f16_f32 v245, v140, v141\n"
        "v_mfma_f32_16x16x32_bf16 v[146:149], v[210:213], v[76:79], v[146:149]\n"
        "v_add_f32_dpp v251, v251, v251 row_ror:8 row_mask:0xf bank_mask:0xf\n"
        "v_pk_mul_f16 v108, v108, v254 op_sel:[0,1]\n"
        "v_mfma_f32_16x16x32_bf16 v[146:149], v[186:189], v[52:55], v[146:149]\n"
        "v_rcp_f32 v250, v250\n"
        "v_mfma_f32_16x16x32_f16 v[150:153], v[126:129], v[178:181], 0\n"
        "v_add_f32_dpp v251, v251, v251 row_ror:4 row_mask:0xf bank_mask:0xf\n"
        "v_pk_mul_f16 v109, v109, v254 op_sel:[0,1]\n"
        "v_mfma_f32_16x16x32_f16 v[150:153], v[104:107], v[174:177], v[150:153]\n"
        "v_pk_mul_f16 v110, v110, v254 op_sel:[0,1]\n"
        "v_pk_mul_f16 v111, v111, v254 op_sel:[0,1]\n"
        "v_mfma_f32_16x16x32_bf16 v[150:153], v[186:189], v[88:91], v[150:153]\n"
        "v_pk_mul_f16 v112, v112, v254 op_sel:[0,1]\n"
        "v_pk_mul_f16 v113, v113, v254 op_sel:[0,1]\n"
        "v_mfma_f32_16x16x32_bf16 v[138:141], v[186:189], v[48:51], 0\n"
        "v_pk_mul_f16 v114, v114, v254 op_sel:[0,1]\n"
        "v_pk_sub_u16 v253, v254, s83\n"
        "v_mfma_f32_16x16x32_f16 v[138:141], v[104:107], v[166:169], v[138:141]\n"
        "v_pk_mul_f16 v108, v108, v190\n"
        "v_pk_mul_f16 v109, v109, v191\n"
        "v_mfma_f32_16x16x32_f16 v[138:141], v[126:129], v[34:37], v[138:141]\n"
        "v_pk_mul_f16 v110, v110, v192\n"
        "v_pk_mul_f16 v111, v111, v193\n"
        "v_mfma_f32_16x16x32_f16 v[154:157], v[126:129], v[162:165], 0\n"
        "v_cvt_pk_f16_f32 v240, v158, v159\n"
        "v_cvt_pk_f16_f32 v241, v160, v161\n"
        "v_mfma_f32_16x16x32_f16 v[154:157], v[104:107], v[142:145], v[154:157]\n"
        "v_cvt_pk_f16_f32 v242, v120, v121\n"
        "v_cvt_pk_f16_f32 v243, v122, v123\n"
        "v_mfma_f32_16x16x32_bf16 v[154:157], v[210:213], v[88:91], v[154:157]\n"
        "v_cvt_pk_f16_f32 v122, v134, v135\n"
        "v_cvt_pk_f16_f32 v123, v136, v137\n"
        "v_mfma_f32_16x16x32_bf16 v[134:137], v[210:213], v[48:51], 0\n"
        "v_pk_mul_f16 v112, v112, v234\n"
        "v_add_f32_dpp v251, v251, v251 row_ror:2 row_mask:0xf bank_mask:0xf\n"
        "v_mfma_f32_16x16x32_f16 v[134:137], v[104:107], v[170:173], v[134:137]\n"
        "v_cvt_pk_f16_f32 v40, v116, v117\n"
        "v_cvt_pk_f16_f32 v41, v118, v119\n"
        "v_mfma_f32_16x16x32_f16 v[134:137], v[126:129], v[244:247], v[134:137]\n"
        "v_cvt_pk_f16_f32 v238, v130, v131\n"
        "v_cvt_pk_f16_f32 v239, v132, v133\n"
        "v_mfma_f32_16x16x32_f16 v[116:119], v[126:129], v[240:243], 0\n"
        "v_cvt_pk_f16_f32 v236, v146, v147\n"
        "v_cvt_pk_f16_f32 v237, v148, v149\n"
        "v_mfma_f32_16x16x32_f16 v[116:119], v[104:107], v[122:125], v[116:119]\n"
        "v_pk_mul_f16 v113, v113, v235\n"
        "v_add_f32_dpp v251, v251, v251 row_ror:1 row_mask:0xf bank_mask:0xf\n"
        "v_mfma_f32_16x16x32_bf16 v[116:119], v[214:217], v[88:91], v[116:119]\n"
        "v_rcp_f32 v251, v251\n"
        "v_mfma_f32_16x16x32_bf16 v[130:133], v[214:217], v[48:51], 0\n"
        "v_cvt_pk_f16_f32 v254, v251, v250\n"
        "v_mul_f16 v225, v225, v254\n"
        "v_mfma_f32_16x16x32_f16 v[130:133], v[104:107], v[40:43], v[130:133]\n"
        "v_pk_sub_u16 v252, v254, s83\n"
        "v_mul_f16 v225, v225, v255\n"
        "v_mfma_f32_16x16x32_f16 v[130:133], v[126:129], v[236:239], v[130:133]\n"
        "s_add_u32 s90, s89, 0\n"
        "ds_write_b16 v22, v225 offset:512\n"
        "v_mfma_f32_16x16x32_f16 v[104:107], v[108:111], v[178:181], 0\n"
        "v_bitop3_b32 v253, v253, v252, s82 bitop3:0xa8\n"
        "v_cmp_ne_u32_e64 s[74:75], 0, v253\n"
        "v_mfma_f32_16x16x32_f16 v[104:107], v[112:115], v[174:177], v[104:107]\n"
        "v_cvt_pk_bf16_f32 v158, v150, v138\n"
        "v_cvt_pk_bf16_f32 v159, v154, v134\n"
        "v_mfma_f32_16x16x32_bf16 v[104:107], v[206:209], v[88:91], v[104:107]\n"
        "s_cmp_lg_u64 s[74:75], 0\n"
        "v_cvt_pk_bf16_f32 v160, v116, v130\n"
        "v_cvt_pk_bf16_f32 v222, v151, v139\n"
        "v_mfma_f32_16x16x32_bf16 v[126:129], v[206:209], v[48:51], 0\n"
        "s_cbranch_scc1 SLOW_%=\n"
        "v_mfma_f32_16x16x32_f16 v[126:129], v[112:115], v[166:169], v[126:129]\n"
        "v_cvt_pk_bf16_f32 v223, v155, v135\n"
        "v_cvt_pk_bf16_f32 v224, v117, v131\n"
        "v_mfma_f32_16x16x32_f16 v[126:129], v[108:111], v[34:37], v[126:129]\n"
        "s_add_u32 s91, s89, 98112\n"
        "v_mov_b32 v233, 0\n"
        "v_cvt_pk_bf16_f32 v150, v152, v140\n"
        "v_mfma_f32_16x16x32_f16 v[146:149], v[108:111], v[162:165], 0\n"
        "v_cvt_pk_bf16_f32 v151, v156, v136\n"
        "v_cvt_pk_bf16_f32 v152, v118, v132\n"
        "v_mfma_f32_16x16x32_f16 v[146:149], v[112:115], v[142:145], v[146:149]\n"
        "v_pk_mul_f16 v226, v226, v254 op_sel:[0,1]\n"
        "v_cvt_pk_bf16_f32 v138, v153, v141\n"
        "v_mfma_f32_16x16x32_bf16 v[146:149], v[198:201], v[88:91], v[146:149]\n"
        "buffer_store_dwordx3 v[158:160], v14, s[40:43], s90 offen sc1 nt\n"
        "v_cvt_pk_bf16_f32 v139, v157, v137\n"
        "v_cvt_pk_bf16_f32 v140, v119, v133\n"
        "v_pk_mul_f16 v227, v227, v254 op_sel:[0,1]\n"
        "v_mfma_f32_16x16x32_bf16 v[130:133], v[198:201], v[48:51], 0\n"
        "v_pk_mul_f16 v228, v228, v254 op_sel:[0,1]\n"
        "v_pk_mul_f16 v229, v229, v254 op_sel:[0,1]\n"
        "v_mfma_f32_16x16x32_f16 v[130:133], v[112:115], v[170:173], v[130:133]\n"
        "v_pk_mul_f16 v230, v230, v254 op_sel:[0,1]\n"
        "v_pk_mul_f16 v231, v231, v254 op_sel:[0,1]\n"
        "v_mfma_f32_16x16x32_f16 v[130:133], v[108:111], v[244:247], v[130:133]\n"
        "buffer_store_dwordx3 v[222:224], v15, s[40:43], s90 offen offset:768 sc1 nt\n"
        "v_pk_mul_f16 v232, v232, v254 op_sel:[0,1]\n"
        "v_pk_mul_f16 v226, v226, v190\n"
        "v_pk_mul_f16 v227, v227, v191\n"
        "v_mfma_f32_16x16x32_f16 v[134:137], v[108:111], v[240:243], 0\n"
        "v_pk_mul_f16 v228, v228, v192\n"
        "v_pk_mul_f16 v229, v229, v193\n"
        "v_mfma_f32_16x16x32_f16 v[134:137], v[112:115], v[122:125], v[134:137]\n"
        "v_pk_mul_f16 v230, v230, v234\n"
        "v_pk_mul_f16 v231, v231, v235\n"
        "v_mfma_f32_16x16x32_bf16 v[134:137], v[182:185], v[88:91], v[134:137]\n"
        "buffer_store_dwordx3 v[150:152], v14, s[40:43], s91 offen offset:192 sc1 nt\n"
        "v_cvt_pk_bf16_f32 v154, v104, v126\n"
        "v_cvt_pk_bf16_f32 v190, v105, v127\n"
        "v_cvt_pk_bf16_f32 v208, v106, v128\n"
        "v_mfma_f32_16x16x32_bf16 v[116:119], v[182:185], v[48:51], 0\n"
        "v_cvt_pk_bf16_f32 v155, v146, v130\n"
        "v_cvt_pk_bf16_f32 v191, v147, v131\n"
        "v_mfma_f32_16x16x32_f16 v[116:119], v[112:115], v[40:43], v[116:119]\n"
        "v_cvt_pk_bf16_f32 v209, v148, v132\n"
        "v_cvt_pk_bf16_f32 v198, v107, v129\n"
        "v_mfma_f32_16x16x32_f16 v[116:119], v[108:111], v[236:239], v[116:119]\n"
        "buffer_store_dwordx3 v[138:140], v16, s[40:43], s91 offen offset:960 sc1 nt\n"
        "v_cvt_pk_bf16_f32 v199, v149, v133\n"
        "v_mfma_f32_16x16x32_f16 v[146:149], v[226:229], v[178:181], 0\n"
        "s_nop 2\n"
        "v_mfma_f32_16x16x32_f16 v[146:149], v[230:233], v[174:177], v[146:149]\n"
        "s_waitcnt vmcnt(4)\n"
        "v_cvt_pk_bf16_f32 v156, v134, v116\n"
        "v_cvt_pk_bf16_f32 v192, v135, v117\n"
        "v_mfma_f32_16x16x32_bf16 v[146:149], v[194:197], v[88:91], v[146:149]\n"
        "s_add_u32 s90, s89, 196224\n"
        "ds_read_b128 v[126:129], v9\n"
        "v_cvt_pk_bf16_f32 v210, v136, v118\n"
        "v_mfma_f32_16x16x32_bf16 v[150:153], v[194:197], v[48:51], 0\n"
        "ds_read_b128 v[132:135], v10\n"
        "v_cvt_pk_bf16_f32 v200, v137, v119\n"
        "v_mfma_f32_16x16x32_f16 v[150:153], v[230:233], v[166:169], v[150:153]\n"
        "ds_read_b128 v[138:141], v11\n"
        "v_mfma_f32_16x16x32_f16 v[150:153], v[226:229], v[34:37], v[150:153]\n"
        "s_add_u32 s91, s89, 294336\n"
        "ds_read_b128 v[106:109], v9 offset:3072\n"
        "v_mfma_f32_16x16x32_f16 v[182:185], v[226:229], v[162:165], 0\n"
        "s_nop 0\n"
        "ds_read_b128 v[112:115], v10 offset:3072\n"
        "v_mfma_f32_16x16x32_f16 v[182:185], v[230:233], v[142:145], v[182:185]\n"
        "s_nop 0\n"
        "ds_read_b128 v[118:121], v11 offset:3072\n"
        "v_cvt_pk_bf16_f32 v196, v146, v150\n"
        "v_mfma_f32_16x16x32_bf16 v[182:185], v[202:205], v[88:91], v[182:185]\n"
        "buffer_store_dwordx3 v[154:156], v14, s[40:43], s90 offen offset:384 sc1 nt\n"
        "v_cvt_pk_bf16_f32 v214, v147, v151\n"
        "v_cvt_pk_bf16_f32 v206, v148, v152\n"
        "v_cvt_pk_bf16_f32 v248, v149, v153\n"
        "v_mfma_f32_16x16x32_bf16 v[186:189], v[202:205], v[48:51], 0\n"
        "ds_read_b128 v[146:149], v9 offset:6144\n"
        "ds_read_b128 v[152:155], v10 offset:6144\n"
        "v_mfma_f32_16x16x32_f16 v[186:189], v[230:233], v[170:173], v[186:189]\n"
        "ds_read_b128 v[158:161], v11 offset:6144\n"
        "v_mfma_f32_16x16x32_f16 v[186:189], v[226:229], v[244:247], v[186:189]\n"
        "buffer_store_dwordx3 v[190:192], v17, s[40:43], s90 offen offset:1152 sc1 nt\n"
        "s_add_u32 s90, s89, 392448\n"
        "s_nop 0\n"
        "v_mfma_f32_16x16x32_f16 v[192:195], v[226:229], v[240:243], 0\n"
        "s_waitcnt lgkmcnt(6)\n"
        "s_nop 0\n"
        "v_mfma_f32_16x16x32_f16 v[192:195], v[230:233], v[122:125], v[192:195]\n"
        "s_cmp_ge_i32 s85, 0\n"
        "v_cvt_pk_bf16_f32 v197, v182, v186\n"
        "v_cvt_pk_bf16_f32 v215, v183, v187\n"
        "v_mfma_f32_16x16x32_bf16 v[192:195], v[218:221], v[88:91], v[192:195]\n"
        "buffer_store_dwordx3 v[208:210], v14, s[40:43], s91 offen offset:576 sc1 nt\n"
        "v_cvt_pk_bf16_f32 v207, v184, v188\n"
        "v_cvt_pk_bf16_f32 v249, v185, v189\n"
        "v_mfma_f32_16x16x32_bf16 v[182:185], v[218:221], v[48:51], 0\n"
        "v_mfma_f32_16x16x32_f16 v[182:185], v[230:233], v[40:43], v[182:185]\n"
        "v_mfma_f32_16x16x32_f16 v[182:185], v[226:229], v[236:239], v[182:185]\n"
        "buffer_store_dwordx3 v[198:200], v14, s[40:43], s90 offen sc1 nt\n"
        "s_cbranch_scc1 TOP_%=\n"
        "s_branch LAST_%=\n"
        "SP3_%=:\n"
        "v_cmp_ne_u32_sdwa s[24:25], v255, s30 src0_sel:BYTE_0 src1_sel:DWORD\n"
        "s_nop 4\n"
        "s_cmp_lg_u64 s[24:25], 0\n"
        "s_cbranch_scc1 SW3_%=\n"
        "v_readfirstlane_b32 s50, v255\n"
        "s_cmp_gt_i32 s85, 0\n"
        "s_cbranch_scc1 DY1B_%=\n"
        "s_nop 1\n"
        "s_lshr_b32 s50, s50, 8\n"
        "s_lshl_b32 s92, s50, 5\n"
        "s_add_u32 s92, s92, s48\n"
        "s_cmp_ge_u32 s92, 5329\n"
        "s_cbranch_scc1 DY1F_%=\n"
        "s_mul_i32 s87, s92, 1344\n"
        "s_mul_i32 s92, s92, 57457\n"
        "s_lshr_b32 s92, s92, 22\n"
        "s_mul_i32 s92, s92, 588672\n"
        "s_add_u32 s87, s87, s92\n"
        "s_lshl_b32 s92, s50, 2\n"
        "v_mov_b32 v27, 1\n"
        "s_mov_b64 exec, 1\n"
        "buffer_atomic_add v27, off, s[16:19], s92\n"
        "s_mov_b64 exec, -1\n"
        "s_add_u32 s85, s85, 1\n"
        "s_sub_u32 s23, s50, s53\n"
        "s_mul_i32 s23, s23, 1584\n"
        "s_mov_b32 s53, s50\n"
        "DY1B_%=:\n"
        "s_cmp_gt_i32 s85, 1\n"
        "s_cbranch_scc1 BK3_%=\n"
        "v_mov_b32 v26, 1\n"
        "s_mov_b64 exec, 1\n"
        "buffer_atomic_add v26, off, s[16:19], 0 sc0\n"
        "s_mov_b64 exec, -1\n"
        "s_branch BK3_%=\n"
        "DY1F_%=:\n"
        "s_mov_b32 s49, -100\n"
        "DY1G_%=:\n"
        "s_cmp_ge_u32 s92, 5521\n"
        "s_cbranch_scc1 BK3_%=\n"
        "s_sleep 8\n"
        "s_add_u32 s92, s92, 32\n"
        "s_branch DY1G_%=\n"
        "s_branch BK3_%=\n"
        "SW3_%=:\n"
        "s_sleep 1\n"
        "ds_read_u16 v255, v19 offset:2048\n"
        "s_waitcnt lgkmcnt(0)\n"
        "s_sub_u32 s31, s31, 1\n"
        "s_cbranch_scc0 SP3_%=\n"
        "s_endpgm\n"
        "SP10_%=:\n"
        "v_cmp_ne_u32_sdwa s[24:25], v246, s29 src0_sel:BYTE_0 src1_sel:DWORD\n"
        "s_nop 4\n"
        "s_cmp_lg_u64 s[24:25], 0\n"
        "s_cbranch_scc1 SW10_%=\n"
        "s_branch BK10_%=\n"
        "SW10_%=:\n"
        "s_sleep 1\n"
        "ds_read_u16 v246, v19 offset:2560\n"
        "s_waitcnt lgkmcnt(0)\n"
        "s_sub_u32 s31, s31, 1\n"
        "s_cbranch_scc0 SP10_%=\n"
        "s_endpgm\n"
        "LAST_%=:\n"
        "s_waitcnt lgkmcnt(0)\n"
        "s_nop 7\n"
        "ds_read_b128 v[116:119], v20 offset:512\n"
        "ds_read_b128 v[138:141], v20 offset:528\n"
        "ds_read_u16 v255, v19 offset:2048\n"
        "v_mov_b32 v26, 0\n"
        "s_mov_b64 exec, 1\n"
        "buffer_atomic_add v26, off, s[16:19], s15 offset:4 sc0\n"
        "s_mov_b64 exec, -1\n"
        "v_cvt_pk_bf16_f32 v198, v192, v182\n"
        "v_cvt_pk_bf16_f32 v216, v193, v183\n"
        "v_cvt_pk_bf16_f32 v208, v194, v184\n"
        "v_cvt_pk_bf16_f32 v250, v195, v185\n"
        "s_mov_b32 s28, s89\n"
        "s_add_u32 s26, s28, 392448\n"
        "buffer_store_dwordx3 v[196:198], v15, s[40:43], s26 offen offset:768 sc1 nt\n"
        "s_add_u32 s27, s28, 490560\n"
        "buffer_store_dwordx3 v[214:216], v14, s[40:43], s27 offen offset:192 sc1 nt\n"
        "buffer_store_dwordx3 v[206:208], v16, s[40:43], s27 offen offset:960 sc1 nt\n"
        "s_add_u32 s26, s28, 588672\n"
        "buffer_store_dwordx3 v[248:250], v14, s[40:43], s26 offen offset:384 sc1 nt\n"
        "s_mov_b32 s30, s29\n"
        "s_waitcnt lgkmcnt(0)\n"
        "v_cmp_ne_u32_e64 s[24:25], s30, v255\n"
        "s_nop 3\n"
        "s_cmp_lg_u64 s[24:25], 0\n"
        "s_cbranch_scc1 SP20_%=\n"
        "BK20_%=:\n"
        "ds_read_b128 v[186:189], v13\n"
        "ds_read_b128 v[210:213], v13 offset:1024\n"
        "ds_read_b128 v[214:217], v13 offset:2048\n"
        "ds_read_b128 v[206:209], v13 offset:3072\n"
        "ds_read_b128 v[198:201], v13 offset:4096\n"
        "ds_read_b128 v[182:185], v13 offset:5120\n"
        "ds_read_b128 v[194:197], v13 offset:6144\n"
        "ds_read_b128 v[202:205], v13 offset:7168\n"
        "ds_read_b128 v[218:221], v13 offset:8192\n"
        "ds_read_b128 v[104:107], v20\n"
        "ds_read_b64 v[30:31], v20 offset:16\n"
        "ds_read_u16 v255, v22\n"
        "ds_read_b64 v[46:47], v21\n"
        "s_waitcnt lgkmcnt(13)\n"
        "v_pk_mul_f16 v110, v116, v178\n"
        "v_pk_fma_f16 v110, v117, v179, v110\n"
        "v_pk_fma_f16 v110, v118, v180, v110\n"
        "v_pk_fma_f16 v110, v119, v181, v110\n"
        "v_pk_fma_f16 v110, v138, v174, v110\n"
        "v_pk_fma_f16 v110, v139, v175, v110\n"
        "v_pk_fma_f16 v110, v140, v176, v110\n"
        "v_lshlrev_b32 v134, 16, v176\n"
        "v_pk_mul_f16 v111, v116, v34\n"
        "v_pk_fma_f16 v111, v117, v35, v111\n"
        "v_pk_fma_f16 v111, v118, v36, v111\n"
        "v_pk_fma_f16 v111, v119, v37, v111\n"
        "v_pk_fma_f16 v111, v138, v166, v111\n"
        "v_pk_fma_f16 v111, v139, v167, v111\n"
        "v_pk_fma_f16 v111, v140, v168, v111\n"
        "v_lshlrev_b32 v135, 16, v168\n"
        "s_waitcnt lgkmcnt(10)\n"
        "v_mfma_f32_16x16x32_bf16 v[112:115], a[152:155], v[214:217], 0\n"
        "s_add_u32 s89, s86, s51\n"
        "v_add_u32 v23, s22, v23\n"
        "v_add_u32 v24, s22, v24\n"
        "v_mfma_f32_16x16x32_bf16 v[112:115], a[148:151], v[210:213], v[112:115]\n"
        "s_mov_b32 s22, s23\n"
        "ds_read_b128 v[150:153], v23\n"
        "v_lshlrev_b32 v136, 16, v144\n"
        "v_mfma_f32_16x16x32_bf16 v[112:115], a[144:147], v[186:189], v[112:115]\n"
        "s_add_u32 s29, s29, 1\n"
        "ds_read_b128 v[146:149], v23 offset:64\n"
        "v_lshlrev_b32 v137, 16, v172\n"
        "v_mfma_f32_16x16x32_bf16 v[34:37], a[164:167], v[214:217], 0\n"
        "s_movk_i32 s23, 1584\n"
        "ds_read_b128 v[126:129], v23 offset:128\n"
        "v_lshlrev_b32 v120, 16, v124\n"
        "v_mfma_f32_16x16x32_bf16 v[34:37], a[160:163], v[210:213], v[34:37]\n"
        "s_mov_b32 s86, s87\n"
        "ds_read_b64 v[108:109], v24 offset:360\n"
        "v_lshlrev_b32 v121, 16, v42\n"
        "v_mfma_f32_16x16x32_bf16 v[34:37], a[156:159], v[186:189], v[34:37]\n"
        "s_add_u32 s88, s88, 32\n"
        "v_cvt_pk_bf16_f32 v130, v112, v113\n"
        "v_cvt_pk_bf16_f32 v131, v114, v115\n"
        "v_mfma_f32_16x16x32_bf16 v[154:157], a[168:171], v[186:189], 0\n"
        "s_add_u32 s87, s87, 43008\n"
        "v_pk_mul_f16 v112, v116, v162\n"
        "v_pk_mul_f16 v113, v116, v244\n"
        "v_mfma_f32_16x16x32_bf16 v[154:157], a[172:175], v[210:213], v[154:157]\n"
        "s_cmp_ge_u32 s88, 73\n"
        "v_cvt_pk_bf16_f32 v132, v34, v35\n"
        "v_cvt_pk_bf16_f32 v133, v36, v37\n"
        "v_mfma_f32_16x16x32_bf16 v[154:157], a[176:179], v[214:217], v[154:157]\n"
        "s_cselect_b32 s92, 588672, 0\n"
        "v_pk_mul_f16 v114, v116, v240\n"
        "v_pk_mul_f16 v115, v116, v236\n"
        "v_mfma_f32_16x16x32_bf16 v[166:169], a[188:191], v[214:217], 0\n"
        "s_cselect_b32 s94, 73, 0\n"
        "v_pk_fma_f16 v112, v117, v163, v112\n"
        "v_pk_fma_f16 v113, v117, v245, v113\n"
        "v_mfma_f32_16x16x32_bf16 v[166:169], a[184:187], v[210:213], v[166:169]\n"
        "s_sub_u32 s88, s88, s94\n"
        "v_pk_fma_f16 v114, v117, v241, v114\n"
        "v_pk_fma_f16 v115, v117, v237, v115\n"
        "v_mfma_f32_16x16x32_bf16 v[166:169], a[180:183], v[186:189], v[166:169]\n"
        "s_add_u32 s87, s87, s92\n"
        "v_pk_fma_f16 v112, v118, v164, v112\n"
        "v_pk_fma_f16 v113, v118, v246, v113\n"
        "v_mfma_f32_16x16x32_bf16 v[34:37], a[192:195], v[186:189], 0\n"
        "s_waitcnt lgkmcnt(0)\n"
        "v_readlane_b32 s93, v109, 1\n"
        "v_cndmask_b32_e64 v108, 0, v108, s[72:73]\n"
        "v_mfma_f32_16x16x32_bf16 v[34:37], a[196:199], v[210:213], v[34:37]\n"
        "s_cmp_gt_i32 s85, 0\n"
        "v_pk_fma_f16 v114, v118, v242, v114\n"
        "v_pk_fma_f16 v115, v118, v238, v115\n"
        "v_mfma_f32_16x16x32_bf16 v[34:37], a[200:203], v[214:217], v[34:37]\n"
        "s_cselect_b32 s94, s87, 0\n"
        "v_pk_fma_f16 v112, v119, v165, v112\n"
        "v_pk_fma_f16 v113, v119, v247, v113\n"
        "v_mfma_f32_16x16x32_bf16 v[158:161], a[212:215], v[214:217], 0\n"
        "s_sub_u32 s85, s85, 1\n"
        "v_pk_fma_f16 v114, v119, v243, v114\n"
        "v_pk_fma_f16 v115, v119, v239, v115\n"
        "v_mfma_f32_16x16x32_bf16 v[158:161], a[208:211], v[210:213], v[158:161]\n"
        "s_waitcnt lgkmcnt(0)\n"
        "v_pk_fma_f16 v112, v138, v142, v112\n"
        "v_pk_fma_f16 v113, v138, v170, v113\n"
        "v_mfma_f32_16x16x32_bf16 v[158:161], a[204:207], v[186:189], v[158:161]\n"
        "s_waitcnt lgkmcnt(0)\n"
        "v_pk_fma_f16 v114, v138, v122, v114\n"
        "v_pk_fma_f16 v115, v138, v40, v115\n"
        "v_mfma_f32_16x16x32_bf16 v[162:165], v[150:153], v[186:189], 0\n"
        "v_pk_fma_f16 v112, v139, v143, v112\n"
        "v_pk_fma_f16 v113, v139, v171, v113\n"
        "v_mfma_f32_16x16x32_bf16 v[162:165], v[146:149], v[210:213], v[162:165]\n"
        "v_pk_fma_f16 v114, v139, v123, v114\n"
        "v_pk_fma_f16 v115, v139, v41, v115\n"
        "v_mfma_f32_16x16x32_bf16 v[162:165], v[126:129], v[214:217], v[162:165]\n"
        "v_pk_fma_f16 v112, v140, v144, v112\n"
        "v_pk_fma_f16 v113, v140, v172, v113\n"
        "v_mfma_f32_16x16x32_bf16 v[142:145], v[126:129], v[182:185], 0\n"
        "v_pk_fma_f16 v114, v140, v124, v114\n"
        "v_pk_fma_f16 v115, v140, v42, v115\n"
        "v_mfma_f32_16x16x32_bf16 v[142:145], v[146:149], v[198:201], v[142:145]\n"
        "v_cvt_pk_bf16_f32 v116, v154, v155\n"
        "v_cvt_pk_bf16_f32 v117, v156, v157\n"
        "v_mfma_f32_16x16x32_bf16 v[142:145], v[150:153], v[206:209], v[142:145]\n"
        "v_cvt_pk_bf16_f32 v118, v166, v167\n"
        "v_cvt_pk_bf16_f32 v119, v168, v169\n"
        "v_mfma_f32_16x16x32_bf16 v[40:43], v[150:153], v[194:197], 0\n"
        "v_cvt_pk_bf16_f32 v122, v34, v35\n"
        "v_cvt_pk_bf16_f32 v123, v36, v37\n"
        "v_mfma_f32_16x16x32_bf16 v[40:43], v[146:149], v[202:205], v[40:43]\n"
        "v_cvt_pk_bf16_f32 v124, v158, v159\n"
        "v_cvt_pk_bf16_f32 v125, v160, v161\n"
        "v_mfma_f32_16x16x32_bf16 v[40:43], v[126:129], v[218:221], v[40:43]\n"
        "s_nop 0\n"
        "v_cndmask_b32_e64 v253, v162, v142, s[64:65]\n"
        "v_cndmask_b32_e64 v254, v163, v143, s[64:65]\n"
        "v_mfma_f32_16x16x32_bf16 v[34:37], v[186:189], v[130:133], 0\n"
        "s_nop 1\n"
        "v_mfma_f32_16x16x32_bf16 v[34:37], v[210:213], v[116:119], v[34:37]\n"
        "s_nop 0\n"
        "v_cndmask_b32_e64 v253, v253, v40, s[70:71]\n"
        "v_cndmask_b32_e64 v254, v254, v41, s[70:71]\n"
        "v_mfma_f32_16x16x32_bf16 v[34:37], v[214:217], v[122:125], v[34:37]\n"
        "v_cvt_pk_f16_f32 v253, v253, v254\n"
        "v_pk_mul_f16 v253, v253, v255 op_sel_hi:[1,0]\n"
        "v_mfma_f32_16x16x32_bf16 v[126:129], v[182:185], v[122:125], 0\n"
        "v_pk_mul_f16 v253, v253, s93 op_sel_hi:[1,0]\n"
        "v_mfma_f32_16x16x32_bf16 v[126:129], v[198:201], v[116:119], v[126:129]\n"
        "v_exp_f16_sdwa v254, v253 dst_sel:WORD_1 dst_unused:UNUSED_PAD src0_sel:WORD_1\n"
        "v_mfma_f32_16x16x32_bf16 v[126:129], v[206:209], v[130:133], v[126:129]\n"
        "v_exp_f16_sdwa v43, v253 dst_sel:WORD_0 dst_unused:UNUSED_PAD src0_sel:WORD_0\n"
        "v_mfma_f32_16x16x32_bf16 v[138:141], v[194:197], v[130:133], 0\n"
        "v_cndmask_b32_e64 v43, v43, v108, s[68:69]\n"
        "v_cvt_pk_f16_f32 v108, v34, v35\n"
        "v_mfma_f32_16x16x32_bf16 v[138:141], v[202:205], v[116:119], v[138:141]\n"
        "s_add_u32 s46, s28, 590016\n"
        "v_cvt_pk_f16_f32 v109, v36, v37\n"
        "v_pk_mul_f16 v108, v108, v46 op_sel_hi:[1,0]\n"
        "v_mfma_f32_16x16x32_bf16 v[138:141], v[218:221], v[122:125], v[138:141]\n"
        "v_pk_mul_f16 v109, v109, v46 op_sel_hi:[1,0]\n"
        "v_pk_mul_f16 v108, v108, v104\n"
        "v_mfma_f32_16x16x32_f16 v[134:137], v[80:83], v[110:113], v[134:137]\n"
        "s_min_u32 s46, s46, 200540928\n"
        "v_pk_mul_f16 v109, v109, v105\n"
        "v_cvt_pk_f16_f32 v110, v126, v127\n"
        "v_mfma_f32_16x16x32_f16 v[118:121], v[80:83], v[112:115], v[118:121]\n"
        "v_exp_f16_sdwa v108, v108 dst_sel:WORD_0 dst_unused:UNUSED_PRESERVE src0_sel:WORD_0\n"
        "v_mfma_f32_16x16x32_bf16 v[130:133], a[152:155], v[182:185], 0\n"
        "v_exp_f16_sdwa v109, v109 dst_sel:WORD_0 dst_unused:UNUSED_PRESERVE src0_sel:WORD_0\n"
        "v_mfma_f32_16x16x32_bf16 v[130:133], a[148:151], v[198:201], v[130:133]\n"
        "v_exp_f16_sdwa v108, v108 dst_sel:WORD_1 dst_unused:UNUSED_PRESERVE src0_sel:WORD_1\n"
        "v_mfma_f32_16x16x32_bf16 v[130:133], a[144:147], v[206:209], v[130:133]\n"
        "v_exp_f16_sdwa v109, v109 dst_sel:WORD_1 dst_unused:UNUSED_PRESERVE src0_sel:WORD_1\n"
        "v_mfma_f32_16x16x32_bf16 v[112:115], a[156:159], v[206:209], 0\n"
        "v_pk_add_f16 v40, v108, v109\n"
        "v_cvt_pk_f16_f32 v111, v128, v129\n"
        "v_mfma_f32_16x16x32_bf16 v[112:115], a[160:163], v[198:201], v[112:115]\n"
        "v_pk_mul_f16 v110, v110, v46 op_sel_hi:[1,0]\n"
        "v_pk_mul_f16 v111, v111, v46 op_sel_hi:[1,0]\n"
        "v_mfma_f32_16x16x32_bf16 v[112:115], a[164:167], v[182:185], v[112:115]\n"
        "v_cvt_pk_bf16_f32 v116, v134, v135\n"
        "v_cvt_pk_bf16_f32 v117, v136, v137\n"
        "v_mfma_f32_16x16x32_bf16 v[122:125], a[176:179], v[182:185], 0\n"
        "v_cvt_pk_bf16_f32 v118, v120, v121\n"
        "v_pk_mul_f16 v110, v110, v106\n"
        "v_mfma_f32_16x16x32_bf16 v[122:125], a[172:175], v[198:201], v[122:125]\n"
        "v_pk_mul_f16 v111, v111, v107\n"
        "v_cvt_pk_f16_f32 v34, v138, v139\n"
        "v_mfma_f32_16x16x32_bf16 v[122:125], a[168:171], v[206:209], v[122:125]\n"
        "buffer_store_dwordx3 v[116:118], v17, s[44:47], s26 offen offset:1152 sc1 nt\n"
        "v_exp_f16_sdwa v110, v110 dst_sel:WORD_0 dst_unused:UNUSED_PRESERVE src0_sel:WORD_0\n"
        "v_cvt_pk_f16_f32 v35, v140, v141\n"
        "v_mfma_f32_16x16x32_bf16 v[116:119], a[188:191], v[182:185], 0\n"
        "v_exp_f16_sdwa v111, v111 dst_sel:WORD_0 dst_unused:UNUSED_PRESERVE src0_sel:WORD_0\n"
        "v_mfma_f32_16x16x32_bf16 v[116:119], a[184:187], v[198:201], v[116:119]\n"
        "v_exp_f16_sdwa v110, v110 dst_sel:WORD_1 dst_unused:UNUSED_PRESERVE src0_sel:WORD_1\n"
        "v_mfma_f32_16x16x32_bf16 v[116:119], a[180:183], v[206:209], v[116:119]\n"
        "v_exp_f16_sdwa v111, v111 dst_sel:WORD_1 dst_unused:UNUSED_PRESERVE src0_sel:WORD_1\n"
        "v_mfma_f32_16x16x32_bf16 v[138:141], a[192:195], v[206:209], 0\n"
        "v_pk_add_f16 v40, v40, v110\n"
        "v_pk_add_f16 v40, v40, v111\n"
        "v_mfma_f32_16x16x32_bf16 v[138:141], a[196:199], v[198:201], v[138:141]\n"
        "v_pk_mul_f16 v34, v34, v46 op_sel_hi:[1,0]\n"
        "v_pk_mul_f16 v35, v35, v46 op_sel_hi:[1,0]\n"
        "v_mfma_f32_16x16x32_bf16 v[138:141], a[200:203], v[182:185], v[138:141]\n"
        "v_pk_mul_f16 v34, v34, v30\n"
        "v_pk_mul_f16 v35, v35, v31\n"
        "v_mfma_f32_16x16x32_bf16 v[134:137], a[212:215], v[182:185], 0\n"
        "v_exp_f16_sdwa v34, v34 dst_sel:WORD_0 dst_unused:UNUSED_PRESERVE src0_sel:WORD_0\n"
        "v_mfma_f32_16x16x32_bf16 v[134:137], a[208:211], v[198:201], v[134:137]\n"
        "v_exp_f16_sdwa v35, v35 dst_sel:WORD_0 dst_unused:UNUSED_PRESERVE src0_sel:WORD_0\n"
        "v_mfma_f32_16x16x32_bf16 v[134:137], a[204:207], v[206:209], v[134:137]\n"
        "v_exp_f16_sdwa v34, v34 dst_sel:WORD_1 dst_unused:UNUSED_PRESERVE src0_sel:WORD_1\n"
        "v_mfma_f32_16x16x32_bf16 v[142:145], v[186:189], a[216:219], 0\n"
        "v_exp_f16_sdwa v35, v35 dst_sel:WORD_1 dst_unused:UNUSED_PRESERVE src0_sel:WORD_1\n"
        "v_mfma_f32_16x16x32_bf16 v[142:145], v[210:213], a[220:223], v[142:145]\n"
        "v_pk_add_f16 v40, v40, v34\n"
        "v_pk_add_f16 v40, v40, v35\n"
        "v_mfma_f32_16x16x32_bf16 v[142:145], v[214:217], a[224:227], v[142:145]\n"
        "v_cvt_pk_bf16_f32 v126, v130, v131\n"
        "v_cvt_pk_bf16_f32 v127, v132, v133\n"
        "v_mfma_f32_16x16x32_bf16 v[130:133], v[182:185], a[224:227], 0\n"
        "v_cvt_pk_bf16_f32 v128, v112, v113\n"
        "v_cvt_pk_bf16_f32 v129, v114, v115\n"
        "v_mfma_f32_16x16x32_bf16 v[130:133], v[198:201], a[220:223], v[130:133]\n"
        "v_cvt_pk_bf16_f32 v146, v122, v123\n"
        "v_cvt_pk_bf16_f32 v147, v124, v125\n"
        "v_mfma_f32_16x16x32_bf16 v[130:133], v[206:209], a[216:219], v[130:133]\n"
        "v_cvt_pk_bf16_f32 v148, v116, v117\n"
        "v_cvt_pk_bf16_f32 v149, v118, v119\n"
        "v_mfma_f32_16x16x32_bf16 v[116:119], v[194:197], a[216:219], 0\n"
        "v_cvt_pk_bf16_f32 v122, v138, v139\n"
        "v_cvt_pk_bf16_f32 v123, v140, v141\n"
        "v_mfma_f32_16x16x32_bf16 v[116:119], v[202:205], a[220:223], v[116:119]\n"
        "v_cvt_pk_bf16_f32 v124, v134, v135\n"
        "v_cvt_pk_bf16_f32 v125, v136, v137\n"
        "v_mfma_f32_16x16x32_bf16 v[116:119], v[218:221], a[224:227], v[116:119]\n"
        "v_cvt_pk_f16_f32 v112, v142, v143\n"
        "v_cvt_pk_f16_f32 v113, v144, v145\n"
        "v_mfma_f32_16x16x32_bf16 v[134:137], v[186:189], v[126:129], 0\n"
        "s_nop 0\n"
        "v_cvt_pk_f16_f32 v114, v130, v131\n"
        "v_cvt_pk_f16_f32 v115, v132, v133\n"
        "v_mfma_f32_16x16x32_bf16 v[134:137], v[210:213], v[146:149], v[134:137]\n"
        "s_nop 0\n"
        "v_cvt_pk_f16_f32 v120, v116, v117\n"
        "v_cvt_pk_f16_f32 v121, v118, v119\n"
        "v_mfma_f32_16x16x32_bf16 v[134:137], v[214:217], v[122:125], v[134:137]\n"
        "s_nop 1\n"
        "v_mfma_f32_16x16x32_bf16 v[138:141], v[182:185], v[122:125], 0\n"
        "s_nop 2\n"
        "v_mfma_f32_16x16x32_bf16 v[138:141], v[198:201], v[146:149], v[138:141]\n"
        "s_nop 0\n"
        "v_cvt_pk_f16_f32 v116, v134, v135\n"
        "v_cvt_pk_f16_f32 v117, v136, v137\n"
        "v_mfma_f32_16x16x32_bf16 v[138:141], v[206:209], v[126:129], v[138:141]\n"
        "v_pk_mul_f16 v116, v116, v46 op_sel:[0,1]\n"
        "v_pk_mul_f16 v117, v117, v46 op_sel:[0,1]\n"
        "v_mfma_f32_16x16x32_bf16 v[134:137], v[194:197], v[126:129], 0\n"
        "v_pk_mul_f16 v116, v116, v104\n"
        "v_pk_mul_f16 v117, v117, v105\n"
        "v_mfma_f32_16x16x32_bf16 v[134:137], v[202:205], v[146:149], v[134:137]\n"
        "v_exp_f16_sdwa v116, v116 dst_sel:WORD_0 dst_unused:UNUSED_PRESERVE src0_sel:WORD_0\n"
        "v_mfma_f32_16x16x32_bf16 v[134:137], v[218:221], v[122:125], v[134:137]\n"
        "v_exp_f16_sdwa v117, v117 dst_sel:WORD_0 dst_unused:UNUSED_PRESERVE src0_sel:WORD_0\n"
        "v_mfma_f32_16x16x32_bf16 v[130:133], a[144:147], v[194:197], 0\n"
        "v_exp_f16_sdwa v116, v116 dst_sel:WORD_1 dst_unused:UNUSED_PRESERVE src0_sel:WORD_1\n"
        "v_mfma_f32_16x16x32_bf16 v[130:133], a[148:151], v[202:205], v[130:133]\n"
        "v_exp_f16_sdwa v117, v117 dst_sel:WORD_1 dst_unused:UNUSED_PRESERVE src0_sel:WORD_1\n"
        "v_mfma_f32_16x16x32_bf16 v[130:133], a[152:155], v[218:221], v[130:133]\n"
        "v_pk_add_f16 v41, v116, v117\n"
        "v_cvt_pk_f16_f32 v118, v138, v139\n"
        "v_mfma_f32_16x16x32_bf16 v[126:129], a[164:167], v[218:221], 0\n"
        "v_cvt_pk_f16_f32 v119, v140, v141\n"
        "v_pk_mul_f16 v118, v118, v46 op_sel:[0,1]\n"
        "v_mfma_f32_16x16x32_bf16 v[126:129], a[160:163], v[202:205], v[126:129]\n"
        "v_pk_mul_f16 v119, v119, v46 op_sel:[0,1]\n"
        "v_pk_mul_f16 v118, v118, v106\n"
        "v_mfma_f32_16x16x32_bf16 v[126:129], a[156:159], v[194:197], v[126:129]\n"
        "v_pk_mul_f16 v119, v119, v107\n"
        "v_cvt_pk_f16_f32 v124, v134, v135\n"
        "v_mfma_f32_16x16x32_bf16 v[138:141], a[168:171], v[194:197], 0\n"
        "v_exp_f16_sdwa v118, v118 dst_sel:WORD_0 dst_unused:UNUSED_PRESERVE src0_sel:WORD_0\n"
        "v_mfma_f32_16x16x32_bf16 v[138:141], a[172:175], v[202:205], v[138:141]\n"
        "v_exp_f16_sdwa v119, v119 dst_sel:WORD_0 dst_unused:UNUSED_PRESERVE src0_sel:WORD_0\n"
        "v_mfma_f32_16x16x32_bf16 v[138:141], a[176:179], v[218:221], v[138:141]\n"
        "v_exp_f16_sdwa v118, v118 dst_sel:WORD_1 dst_unused:UNUSED_PRESERVE src0_sel:WORD_1\n"
        "v_mfma_f32_16x16x32_bf16 v[146:149], a[188:191], v[218:221], 0\n"
        "v_exp_f16_sdwa v119, v119 dst_sel:WORD_1 dst_unused:UNUSED_PRESERVE src0_sel:WORD_1\n"
        "v_mfma_f32_16x16x32_bf16 v[146:149], a[184:187], v[202:205], v[146:149]\n"
        "v_pk_add_f16 v41, v41, v118\n"
        "v_pk_add_f16 v41, v41, v119\n"
        "v_mfma_f32_16x16x32_bf16 v[146:149], a[180:183], v[194:197], v[146:149]\n"
        "v_cvt_pk_f16_f32 v125, v136, v137\n"
        "v_pk_mul_f16 v124, v124, v46 op_sel:[0,1]\n"
        "v_mfma_f32_16x16x32_bf16 v[154:157], a[192:195], v[194:197], 0\n"
        "v_pk_mul_f16 v125, v125, v46 op_sel:[0,1]\n"
        "v_pk_mul_f16 v124, v124, v30\n"
        "v_mfma_f32_16x16x32_bf16 v[154:157], a[196:199], v[202:205], v[154:157]\n"
        "v_pk_mul_f16 v125, v125, v31\n"
        "v_cvt_pk_bf16_f32 v142, v130, v131\n"
        "v_mfma_f32_16x16x32_bf16 v[154:157], a[200:203], v[218:221], v[154:157]\n"
        "v_exp_f16_sdwa v124, v124 dst_sel:WORD_0 dst_unused:UNUSED_PRESERVE src0_sel:WORD_0\n"
        "v_mfma_f32_16x16x32_bf16 v[134:137], a[212:215], v[218:221], 0\n"
        "v_exp_f16_sdwa v125, v125 dst_sel:WORD_0 dst_unused:UNUSED_PRESERVE src0_sel:WORD_0\n"
        "v_mfma_f32_16x16x32_bf16 v[134:137], a[208:211], v[202:205], v[134:137]\n"
        "v_exp_f16_sdwa v124, v124 dst_sel:WORD_1 dst_unused:UNUSED_PRESERVE src0_sel:WORD_1\n"
        "v_mfma_f32_16x16x32_bf16 v[134:137], a[204:207], v[194:197], v[134:137]\n"
        "v_exp_f16_sdwa v125, v125 dst_sel:WORD_1 dst_unused:UNUSED_PRESERVE src0_sel:WORD_1\n"
        "v_mfma_f32_16x16x32_bf16 v[150:153], v[218:221], a[236:239], 0\n"
        "v_pk_add_f16 v41, v41, v124\n"
        "v_pk_add_f16 v41, v41, v125\n"
        "v_mfma_f32_16x16x32_bf16 v[150:153], v[202:205], a[232:235], v[150:153]\n"
        "v_cvt_pk_bf16_f32 v143, v132, v133\n"
        "v_cvt_pk_bf16_f32 v144, v126, v127\n"
        "v_mfma_f32_16x16x32_bf16 v[150:153], v[194:197], a[228:231], v[150:153]\n"
        "v_cvt_pk_bf16_f32 v145, v128, v129\n"
        "v_cvt_pk_bf16_f32 v158, v138, v139\n"
        "v_mfma_f32_16x16x32_bf16 v[126:129], v[206:209], a[228:231], 0\n"
        "v_cvt_pk_bf16_f32 v159, v140, v141\n"
        "v_cvt_pk_bf16_f32 v160, v146, v147\n"
        "v_mfma_f32_16x16x32_bf16 v[126:129], v[198:201], a[232:235], v[126:129]\n"
        "v_cvt_pk_bf16_f32 v161, v148, v149\n"
        "v_cvt_pk_bf16_f32 v138, v154, v155\n"
        "v_mfma_f32_16x16x32_bf16 v[126:129], v[182:185], a[236:239], v[126:129]\n"
        "v_cvt_pk_bf16_f32 v139, v156, v157\n"
        "v_cvt_pk_bf16_f32 v140, v134, v135\n"
        "v_mfma_f32_16x16x32_bf16 v[154:157], v[214:217], a[236:239], 0\n"
        "v_cvt_pk_bf16_f32 v141, v136, v137\n"
        "v_cvt_pk_f16_f32 v132, v150, v151\n"
        "v_mfma_f32_16x16x32_bf16 v[154:157], v[210:213], a[232:235], v[154:157]\n"
        "s_nop 0\n"
        "v_cvt_pk_f16_f32 v133, v152, v153\n"
        "v_cvt_pk_f16_f32 v130, v126, v127\n"
        "v_mfma_f32_16x16x32_bf16 v[154:157], v[186:189], a[228:231], v[154:157]\n"
        "s_nop 0\n"
        "v_cvt_pk_f16_f32 v131, v128, v129\n"
        "v_mfma_f32_16x16x32_bf16 v[146:149], v[186:189], v[142:145], 0\n"
        "s_nop 2\n"
        "v_mfma_f32_16x16x32_bf16 v[146:149], v[210:213], v[158:161], v[146:149]\n"
        "s_nop 0\n"
        "v_cvt_pk_f16_f32 v128, v154, v155\n"
        "v_cvt_pk_f16_f32 v129, v156, v157\n"
        "v_mfma_f32_16x16x32_bf16 v[146:149], v[214:217], v[138:141], v[146:149]\n"
        "s_nop 1\n"
        "v_mfma_f32_16x16x32_bf16 v[150:153], v[182:185], v[138:141], 0\n"
        "s_nop 2\n"
        "v_mfma_f32_16x16x32_bf16 v[150:153], v[198:201], v[158:161], v[150:153]\n"
        "s_nop 0\n"
        "v_cvt_pk_f16_f32 v136, v146, v147\n"
        "v_cvt_pk_f16_f32 v137, v148, v149\n"
        "v_mfma_f32_16x16x32_bf16 v[150:153], v[206:209], v[142:145], v[150:153]\n"
        "v_pk_mul_f16 v136, v136, v47 op_sel_hi:[1,0]\n"
        "v_pk_mul_f16 v137, v137, v47 op_sel_hi:[1,0]\n"
        "v_mfma_f32_16x16x32_bf16 v[162:165], v[194:197], v[142:145], 0\n"
        "v_pk_mul_f16 v136, v136, v104\n"
        "v_pk_mul_f16 v137, v137, v105\n"
        "v_mfma_f32_16x16x32_bf16 v[162:165], v[202:205], v[158:161], v[162:165]\n"
        "v_exp_f16_sdwa v136, v136 dst_sel:WORD_0 dst_unused:UNUSED_PRESERVE src0_sel:WORD_0\n"
        "v_mfma_f32_16x16x32_bf16 v[162:165], v[218:221], v[138:141], v[162:165]\n"
        "v_exp_f16_sdwa v137, v137 dst_sel:WORD_0 dst_unused:UNUSED_PRESERVE src0_sel:WORD_0\n"
        "v_mfma_f32_16x16x32_bf16 v[158:161], v[186:189], a[240:243], 0\n"
        "v_exp_f16_sdwa v136, v136 dst_sel:WORD_1 dst_unused:UNUSED_PRESERVE src0_sel:WORD_1\n"
        "v_mfma_f32_16x16x32_bf16 v[158:161], v[210:213], a[244:247], v[158:161]\n"
        "v_exp_f16_sdwa v137, v137 dst_sel:WORD_1 dst_unused:UNUSED_PRESERVE src0_sel:WORD_1\n"
        "v_mfma_f32_16x16x32_bf16 v[158:161], v[214:217], a[248:251], v[158:161]\n"
        "v_pk_add_f16 v42, v136, v137\n"
        "v_cvt_pk_f16_f32 v138, v150, v151\n"
        "v_mfma_f32_16x16x32_bf16 v[178:181], v[182:185], a[248:251], 0\n"
        "v_cndmask_b32_e64 v36, 0, v254, s[62:63]\n"
        "v_cndmask_b32_e64 v126, 0, v254, s[64:65]\n"
        "v_mfma_f32_16x16x32_bf16 v[178:181], v[198:201], a[244:247], v[178:181]\n"
        "v_cndmask_b32_e64 v142, 0, v254, s[66:67]\n"
        "v_pk_add_f16 v40, v40, v36\n"
        "v_mfma_f32_16x16x32_bf16 v[178:181], v[206:209], a[240:243], v[178:181]\n"
        "v_pk_add_f16 v41, v41, v126\n"
        "v_cvt_pk_f16_f32 v139, v152, v153\n"
        "v_mfma_f32_16x16x32_bf16 v[226:229], v[194:197], a[240:243], 0\n"
        "v_pk_mul_f16 v138, v138, v47 op_sel_hi:[1,0]\n"
        "v_pk_mul_f16 v139, v139, v47 op_sel_hi:[1,0]\n"
        "v_mfma_f32_16x16x32_bf16 v[226:229], v[202:205], a[244:247], v[226:229]\n"
        "v_pk_mul_f16 v138, v138, v106\n"
        "v_pk_mul_f16 v139, v139, v107\n"
        "v_mfma_f32_16x16x32_bf16 v[226:229], v[218:221], a[248:251], v[226:229]\n"
        "v_exp_f16_sdwa v138, v138 dst_sel:WORD_0 dst_unused:UNUSED_PRESERVE src0_sel:WORD_0\n"
        "v_mfma_f32_16x16x32_bf16 v[174:177], v[218:221], v[64:67], 0\n"
        "v_exp_f16_sdwa v139, v139 dst_sel:WORD_0 dst_unused:UNUSED_PRESERVE src0_sel:WORD_0\n"
        "v_mfma_f32_16x16x32_bf16 v[174:177], v[202:205], v[72:75], v[174:177]\n"
        "s_waitcnt vmcnt(5)\n"
        "v_exp_f16_sdwa v138, v138 dst_sel:WORD_1 dst_unused:UNUSED_PRESERVE src0_sel:WORD_1\n"
        "v_mfma_f32_16x16x32_bf16 v[174:177], v[194:197], a[252:255], v[174:177]\n"
        "v_exp_f16_sdwa v139, v139 dst_sel:WORD_1 dst_unused:UNUSED_PRESERVE src0_sel:WORD_1\n"
        "v_mfma_f32_16x16x32_bf16 v[190:193], v[206:209], a[252:255], 0\n"
        "v_pk_add_f16 v42, v42, v138\n"
        "v_pk_add_f16 v42, v42, v139\n"
        "v_mfma_f32_16x16x32_bf16 v[190:193], v[198:201], v[72:75], v[190:193]\n"
        "ds_read2_b32 v[122:123], v24 offset0:0 offset1:0\n"
        "ds_read2_b32 v[134:135], v24 offset0:1 offset1:1\n"
        "v_mfma_f32_16x16x32_bf16 v[190:193], v[182:185], v[64:67], v[190:193]\n"
        "ds_read2_b32 v[154:155], v24 offset0:2 offset1:2\n"
        "ds_read2_b32 v[146:147], v24 offset0:3 offset1:3\n"
        "v_mfma_f32_16x16x32_bf16 v[238:241], v[214:217], v[64:67], 0\n"
        "ds_read2_b32 v[150:151], v24 offset0:4 offset1:4\n"
        "v_mov_b32 v37, 0\n"
        "v_mfma_f32_16x16x32_bf16 v[238:241], v[210:213], v[72:75], v[238:241]\n"
        "ds_read2_b32 v[170:171], v24 offset0:5 offset1:5\n"
        "v_cvt_pk_f16_f32 v140, v162, v163\n"
        "v_mfma_f32_16x16x32_bf16 v[238:241], v[186:189], a[252:255], v[238:241]\n"
        "v_cvt_pk_f16_f32 v141, v164, v165\n"
        "v_pk_mul_f16 v140, v140, v47 op_sel_hi:[1,0]\n"
        "v_mfma_f32_16x16x32_bf16 v[230:233], v[186:189], v[68:71], 0\n"
        "v_pk_mul_f16 v141, v141, v47 op_sel_hi:[1,0]\n"
        "v_pk_mul_f16 v140, v140, v30\n"
        "v_mfma_f32_16x16x32_bf16 v[230:233], v[210:213], v[60:63], v[230:233]\n"
        "v_pk_mul_f16 v141, v141, v31\n"
        "v_readfirstlane_b32 s50, v26\n"
        "v_mfma_f32_16x16x32_bf16 v[230:233], v[214:217], v[56:59], v[230:233]\n"
        "s_nop 0\n"
        "v_exp_f16_sdwa v140, v140 dst_sel:WORD_0 dst_unused:UNUSED_PRESERVE src0_sel:WORD_0\n"
        "v_mfma_f32_16x16x32_bf16 v[222:225], v[182:185], v[56:59], 0\n"
        "s_cmp_eq_u32 s50, s21\n"
        "v_exp_f16_sdwa v141, v141 dst_sel:WORD_0 dst_unused:UNUSED_PRESERVE src0_sel:WORD_0\n"
        "v_mfma_f32_16x16x32_bf16 v[222:225], v[198:201], v[60:63], v[222:225]\n"
        "s_cselect_b32 s50, 0x100, 0\n"
        "v_exp_f16_sdwa v140, v140 dst_sel:WORD_1 dst_unused:UNUSED_PRESERVE src0_sel:WORD_1\n"
        "v_mfma_f32_16x16x32_bf16 v[222:225], v[206:209], v[68:71], v[222:225]\n"
        "s_or_b32 s50, s50, s29\n"
        "v_exp_f16_sdwa v141, v141 dst_sel:WORD_1 dst_unused:UNUSED_PRESERVE src0_sel:WORD_1\n"
        "v_mfma_f32_16x16x32_bf16 v[234:237], v[194:197], v[68:71], 0\n"
        "s_waitcnt lgkmcnt(0)\n"
        "v_lshlrev_b32 v164, 16, v122\n"
        "v_lshlrev_b32 v165, 16, v134\n"
        "v_mfma_f32_16x16x32_bf16 v[234:237], v[202:205], v[60:63], v[234:237]\n"
        "v_lshlrev_b32 v166, 16, v154\n"
        "v_lshlrev_b32 v167, 16, v146\n"
        "v_mfma_f32_16x16x32_bf16 v[234:237], v[218:221], v[56:59], v[234:237]\n"
        "v_lshlrev_b32 v172, 16, v150\n"
        "v_lshlrev_b32 v173, 16, v170\n"
        "v_pk_add_f16 v42, v42, v140\n"
        "v_pk_add_f16 v42, v42, v141\n"
        "v_pk_add_f16 v42, v42, v142\n"
        "s_nop 1\n"
        "v_mfma_f32_16x16x32_f16 v[246:249], v[80:83], v[40:43], 0\n"
        "v_cvt_pk_f16_f32 v156, v158, v159\n"
        "v_cvt_pk_f16_f32 v157, v160, v161\n"
        "v_mfma_f32_16x16x32_bf16 v[242:245], v[218:221], v[84:87], 0\n"
        "v_cvt_pk_f16_f32 v158, v178, v179\n"
        "v_cvt_pk_f16_f32 v159, v180, v181\n"
        "v_mfma_f32_16x16x32_bf16 v[242:245], v[202:205], v[76:79], v[242:245]\n"
        "v_cvt_pk_f16_f32 v152, v226, v227\n"
        "v_cvt_pk_f16_f32 v153, v228, v229\n"
        "v_mfma_f32_16x16x32_bf16 v[242:245], v[194:197], v[52:55], v[242:245]\n"
        "v_mov_b32 v254, s50\n"
        "v_cvt_pk_f16_f32 v144, v174, v175\n"
        "v_mfma_f32_16x16x32_bf16 v[178:181], v[206:209], v[52:55], 0\n"
        "ds_write_b16 v22, v254 offset:3584\n"
        "v_mfma_f32_16x16x32_bf16 v[178:181], v[198:201], v[76:79], v[178:181]\n"
        "s_mov_b32 s50, 0\n"
        "v_cvt_pk_f16_f32 v145, v176, v177\n"
        "v_cvt_pk_f16_f32 v162, v190, v191\n"
        "v_mfma_f32_16x16x32_bf16 v[178:181], v[182:185], v[84:87], v[178:181]\n"
        "v_mov_b32 v127, 0\n"
        "v_cvt_pk_f16_f32 v163, v192, v193\n"
        "v_mfma_f32_16x16x32_bf16 v[226:229], v[214:217], v[84:87], 0\n"
        "v_cvt_pk_f16_f32 v160, v238, v239\n"
        "v_cvt_pk_f16_f32 v161, v240, v241\n"
        "v_mfma_f32_16x16x32_bf16 v[226:229], v[210:213], v[76:79], v[226:229]\n"
        "v_cvt_pk_f16_f32 v174, v230, v231\n"
        "v_cvt_pk_f16_f32 v175, v232, v233\n"
        "v_mfma_f32_16x16x32_bf16 v[226:229], v[186:189], v[52:55], v[226:229]\n"
        "v_cvt_pk_f16_f32 v176, v222, v223\n"
        "v_cvt_pk_f16_f32 v177, v224, v225\n"
        "v_rcp_f32 v246, v246\n"
        "v_rcp_f32 v247, v247\n"
        "s_nop 0\n"
        "v_cvt_pk_f16_f32 v254, v246, v247\n"
        "v_pk_mul_f16 v108, v108, v254 op_sel_hi:[1,0]\n"
        "v_pk_mul_f16 v108, v108, v104\n"
        "v_pk_mul_f16 v109, v109, v254 op_sel_hi:[1,0]\n"
        "v_pk_mul_f16 v109, v109, v105\n"
        "v_pk_mul_f16 v110, v110, v254 op_sel_hi:[1,0]\n"
        "v_pk_mul_f16 v110, v110, v106\n"
        "v_pk_mul_f16 v111, v111, v254 op_sel_hi:[1,0]\n"
        "v_pk_mul_f16 v111, v111, v107\n"
        "s_nop 1\n"
        "v_mfma_f32_16x16x32_f16 v[190:193], v[108:111], v[112:115], 0\n"
        "v_cvt_pk_f16_f32 v148, v234, v235\n"
        "v_cvt_pk_f16_f32 v149, v236, v237\n"
        "v_pk_mul_f16 v36, v36, v254 op_sel_hi:[1,0]\n"
        "v_pk_mul_f16 v34, v34, v254 op_sel_hi:[1,0]\n"
        "v_pk_mul_f16 v34, v34, v30\n"
        "v_pk_mul_f16 v35, v35, v254 op_sel_hi:[1,0]\n"
        "v_pk_mul_f16 v35, v35, v31\n"
        "s_nop 1\n"
        "v_mfma_f32_16x16x32_f16 v[190:193], v[34:37], v[120:123], v[190:193]\n"
        "v_add_f32_dpp v249, v249, v249 row_ror:8 row_mask:0xf bank_mask:0xf\n"
        "v_pk_mul_f16 v116, v116, v254 op_sel:[0,1]\n"
        "v_mfma_f32_16x16x32_bf16 v[190:193], v[186:189], v[88:91], v[190:193]\n"
        "v_rcp_f32 v248, v248\n"
        "v_mfma_f32_16x16x32_bf16 v[230:233], v[186:189], v[48:51], 0\n"
        "v_add_f32_dpp v249, v249, v249 row_ror:4 row_mask:0xf bank_mask:0xf\n"
        "v_pk_mul_f16 v117, v117, v254 op_sel:[0,1]\n"
        "v_mfma_f32_16x16x32_f16 v[230:233], v[34:37], v[132:135], v[230:233]\n"
        "v_pk_mul_f16 v118, v118, v254 op_sel:[0,1]\n"
        "v_pk_mul_f16 v119, v119, v254 op_sel:[0,1]\n"
        "v_mfma_f32_16x16x32_f16 v[230:233], v[108:111], v[128:131], v[230:233]\n"
        "v_pk_mul_f16 v124, v124, v254 op_sel:[0,1]\n"
        "v_pk_mul_f16 v125, v125, v254 op_sel:[0,1]\n"
        "v_mfma_f32_16x16x32_f16 v[222:225], v[108:111], v[156:159], 0\n"
        "v_pk_mul_f16 v126, v126, v254 op_sel:[0,1]\n"
        "v_pk_sub_u16 v253, v254, s83\n"
        "v_mfma_f32_16x16x32_f16 v[222:225], v[34:37], v[152:155], v[222:225]\n"
        "v_pk_mul_f16 v116, v116, v104\n"
        "v_pk_mul_f16 v117, v117, v105\n"
        "v_mfma_f32_16x16x32_bf16 v[222:225], v[210:213], v[88:91], v[222:225]\n"
        "v_pk_mul_f16 v118, v118, v106\n"
        "v_pk_mul_f16 v119, v119, v107\n"
        "v_mfma_f32_16x16x32_bf16 v[238:241], v[210:213], v[48:51], 0\n"
        "v_add_f32_dpp v249, v249, v249 row_ror:2 row_mask:0xf bank_mask:0xf\n"
        "v_pk_mul_f16 v124, v124, v30\n"
        "v_mfma_f32_16x16x32_f16 v[238:241], v[34:37], v[144:147], v[238:241]\n"
        "v_add_f32_dpp v249, v249, v249 row_ror:1 row_mask:0xf bank_mask:0xf\n"
        "v_pk_mul_f16 v125, v125, v31\n"
        "v_mfma_f32_16x16x32_f16 v[238:241], v[108:111], v[160:163], v[238:241]\n"
        "v_rcp_f32 v249, v249\n"
        "v_mfma_f32_16x16x32_f16 v[234:237], v[108:111], v[174:177], 0\n"
        "v_cvt_pk_f16_f32 v254, v249, v248\n"
        "v_cvt_pk_f16_f32 v168, v242, v243\n"
        "v_mfma_f32_16x16x32_f16 v[234:237], v[34:37], v[148:151], v[234:237]\n"
        "v_cvt_pk_f16_f32 v169, v244, v245\n"
        "v_pk_mul_f16 v136, v136, v254 op_sel:[0,1]\n"
        "v_mfma_f32_16x16x32_bf16 v[234:237], v[214:217], v[88:91], v[234:237]\n"
        "v_mov_b32 v143, 0\n"
        "v_pk_mul_f16 v137, v137, v254 op_sel:[0,1]\n"
        "v_mfma_f32_16x16x32_bf16 v[242:245], v[214:217], v[48:51], 0\n"
        "v_pk_mul_f16 v138, v138, v254 op_sel:[0,1]\n"
        "v_pk_mul_f16 v139, v139, v254 op_sel:[0,1]\n"
        "v_mfma_f32_16x16x32_f16 v[242:245], v[34:37], v[168:171], v[242:245]\n"
        "v_pk_mul_f16 v140, v140, v254 op_sel:[0,1]\n"
        "v_pk_mul_f16 v141, v141, v254 op_sel:[0,1]\n"
        "v_cvt_pk_f16_f32 v36, v178, v179\n"
        "v_cvt_pk_f16_f32 v37, v180, v181\n"
        "v_cvt_pk_f16_f32 v34, v226, v227\n"
        "v_cvt_pk_f16_f32 v35, v228, v229\n"
        "s_nop 1\n"
        "v_mfma_f32_16x16x32_f16 v[242:245], v[108:111], v[34:37], v[242:245]\n"
        "v_mul_f16 v43, v43, v254\n"
        "v_mul_f16 v43, v43, v255\n"
        "v_mfma_f32_16x16x32_f16 v[108:111], v[116:119], v[112:115], 0\n"
        "ds_write_b16 v22, v43 offset:512\n"
        "v_mfma_f32_16x16x32_f16 v[108:111], v[124:127], v[120:123], v[108:111]\n"
        "s_add_u32 s90, s89, 0\n"
        "v_pk_mul_f16 v142, v142, v254 op_sel:[0,1]\n"
        "v_pk_sub_u16 v255, v254, s83\n"
        "v_mfma_f32_16x16x32_bf16 v[108:111], v[206:209], v[88:91], v[108:111]\n"
        "v_bitop3_b32 v253, v253, v255, s82 bitop3:0xa8\n"
        "v_cmp_ne_u32_e64 s[74:75], 0, v253\n"
        "v_mfma_f32_16x16x32_bf16 v[226:229], v[206:209], v[48:51], 0\n"
        "v_pk_mul_f16 v136, v136, v104\n"
        "v_pk_mul_f16 v137, v137, v105\n"
        "v_mfma_f32_16x16x32_f16 v[226:229], v[124:127], v[132:135], v[226:229]\n"
        "s_cmp_lg_u64 s[74:75], 0\n"
        "v_pk_mul_f16 v138, v138, v106\n"
        "v_pk_mul_f16 v139, v139, v107\n"
        "v_mfma_f32_16x16x32_f16 v[226:229], v[116:119], v[128:131], v[226:229]\n"
        "s_cbranch_scc1 SLOW_%=\n"
        "v_mfma_f32_16x16x32_f16 v[178:181], v[116:119], v[156:159], 0\n"
        "s_add_u32 s91, s89, 98112\n"
        "v_pk_mul_f16 v140, v140, v30\n"
        "v_pk_mul_f16 v141, v141, v31\n"
        "v_mfma_f32_16x16x32_f16 v[178:181], v[124:127], v[152:155], v[178:181]\n"
        "ds_read_u16 v255, v19 offset:2560\n"
        "v_cvt_pk_bf16_f32 v104, v190, v230\n"
        "v_mfma_f32_16x16x32_bf16 v[178:181], v[198:201], v[88:91], v[178:181]\n"
        "ds_read_b128 v[40:43], v20 offset:512\n"
        "ds_read_b128 v[186:189], v20 offset:528\n"
        "v_mfma_f32_16x16x32_bf16 v[206:209], v[198:201], v[48:51], 0\n"
        "v_cvt_pk_bf16_f32 v105, v222, v238\n"
        "v_cvt_pk_bf16_f32 v106, v234, v242\n"
        "v_mfma_f32_16x16x32_f16 v[206:209], v[124:127], v[144:147], v[206:209]\n"
        "v_cvt_pk_bf16_f32 v246, v191, v231\n"
        "v_cvt_pk_bf16_f32 v247, v223, v239\n"
        "v_mfma_f32_16x16x32_f16 v[206:209], v[116:119], v[160:163], v[206:209]\n"
        "buffer_store_dwordx3 v[104:106], v14, s[40:43], s90 offen sc1 nt\n"
        "v_cvt_pk_bf16_f32 v248, v235, v243\n"
        "v_cvt_pk_bf16_f32 v210, v192, v232\n"
        "v_cvt_pk_bf16_f32 v211, v224, v240\n"
        "v_mfma_f32_16x16x32_f16 v[198:201], v[116:119], v[174:177], 0\n"
        "v_cvt_pk_bf16_f32 v212, v236, v244\n"
        "v_cvt_pk_bf16_f32 v214, v193, v233\n"
        "v_mfma_f32_16x16x32_f16 v[198:201], v[124:127], v[148:151], v[198:201]\n"
        "v_cvt_pk_bf16_f32 v215, v225, v241\n"
        "v_cvt_pk_bf16_f32 v216, v237, v245\n"
        "v_mfma_f32_16x16x32_bf16 v[198:201], v[182:185], v[88:91], v[198:201]\n"
        "buffer_store_dwordx3 v[246:248], v15, s[40:43], s90 offen offset:768 sc1 nt\n"
        "v_cvt_pk_bf16_f32 v222, v108, v226\n"
        "v_cvt_pk_bf16_f32 v223, v178, v206\n"
        "v_cvt_pk_bf16_f32 v230, v109, v227\n"
        "v_mfma_f32_16x16x32_bf16 v[190:193], v[182:185], v[48:51], 0\n"
        "s_waitcnt lgkmcnt(0)\n"
        "v_pk_mul_f16 v104, v40, v112\n"
        "v_pk_mul_f16 v105, v40, v128\n"
        "v_mfma_f32_16x16x32_f16 v[190:193], v[124:127], v[168:171], v[190:193]\n"
        "s_waitcnt lgkmcnt(0)\n"
        "v_pk_mul_f16 v106, v40, v156\n"
        "v_pk_mul_f16 v107, v40, v160\n"
        "v_mfma_f32_16x16x32_f16 v[190:193], v[116:119], v[34:37], v[190:193]\n"
        "buffer_store_dwordx3 v[210:212], v14, s[40:43], s91 offen offset:192 sc1 nt\n"
        "v_pk_mul_f16 v108, v40, v174\n"
        "v_pk_mul_f16 v109, v40, v34\n"
        "v_pk_fma_f16 v104, v41, v113, v104\n"
        "v_mfma_f32_16x16x32_f16 v[116:119], v[136:139], v[112:115], 0\n"
        "v_pk_fma_f16 v105, v41, v129, v105\n"
        "v_pk_fma_f16 v106, v41, v157, v106\n"
        "v_mfma_f32_16x16x32_f16 v[116:119], v[140:143], v[120:123], v[116:119]\n"
        "s_add_u32 s90, s89, 196224\n"
        "v_pk_fma_f16 v107, v41, v161, v107\n"
        "v_pk_fma_f16 v108, v41, v175, v108\n"
        "v_mfma_f32_16x16x32_bf16 v[116:119], v[194:197], v[88:91], v[116:119]\n"
        "buffer_store_dwordx3 v[214:216], v16, s[40:43], s91 offen offset:960 sc1 nt\n"
        "v_pk_fma_f16 v109, v41, v35, v109\n"
        "v_pk_fma_f16 v104, v42, v114, v104\n"
        "v_pk_fma_f16 v105, v42, v130, v105\n"
        "v_mfma_f32_16x16x32_bf16 v[124:127], v[194:197], v[48:51], 0\n"
        "v_pk_fma_f16 v106, v42, v158, v106\n"
        "v_pk_fma_f16 v107, v42, v162, v107\n"
        "v_mfma_f32_16x16x32_f16 v[124:127], v[140:143], v[132:135], v[124:127]\n"
        "v_pk_fma_f16 v108, v42, v176, v108\n"
        "v_pk_fma_f16 v109, v42, v36, v109\n"
        "v_mfma_f32_16x16x32_f16 v[124:127], v[136:139], v[128:131], v[124:127]\n"
        "s_add_u32 s91, s89, 294336\n"
        "v_pk_fma_f16 v104, v43, v115, v104\n"
        "v_pk_fma_f16 v105, v43, v131, v105\n"
        "v_mfma_f32_16x16x32_f16 v[112:115], v[136:139], v[156:159], 0\n"
        "v_pk_fma_f16 v106, v43, v159, v106\n"
        "v_pk_fma_f16 v107, v43, v163, v107\n"
        "v_mfma_f32_16x16x32_f16 v[112:115], v[140:143], v[152:155], v[112:115]\n"
        "v_pk_fma_f16 v108, v43, v177, v108\n"
        "v_pk_fma_f16 v109, v43, v37, v109\n"
        "v_mfma_f32_16x16x32_bf16 v[112:115], v[202:205], v[88:91], v[112:115]\n"
        "v_pk_fma_f16 v104, v186, v120, v104\n"
        "v_pk_fma_f16 v105, v186, v132, v105\n"
        "v_mfma_f32_16x16x32_bf16 v[40:43], v[202:205], v[48:51], 0\n"
        "v_pk_fma_f16 v106, v186, v152, v106\n"
        "v_pk_fma_f16 v107, v186, v144, v107\n"
        "v_mfma_f32_16x16x32_f16 v[40:43], v[140:143], v[144:147], v[40:43]\n"
        "v_cvt_pk_bf16_f32 v224, v198, v190\n"
        "v_pk_fma_f16 v108, v186, v148, v108\n"
        "v_mfma_f32_16x16x32_f16 v[40:43], v[136:139], v[160:163], v[40:43]\n"
        "buffer_store_dwordx3 v[222:224], v14, s[40:43], s90 offen offset:384 sc1 nt\n"
        "v_cvt_pk_bf16_f32 v231, v179, v207\n"
        "v_cvt_pk_bf16_f32 v232, v199, v191\n"
        "v_pk_fma_f16 v109, v186, v168, v109\n"
        "v_mfma_f32_16x16x32_f16 v[156:159], v[136:139], v[174:177], 0\n"
        "v_cvt_pk_bf16_f32 v160, v110, v228\n"
        "v_cvt_pk_bf16_f32 v161, v180, v208\n"
        "v_mfma_f32_16x16x32_f16 v[156:159], v[140:143], v[148:151], v[156:159]\n"
        "v_cvt_pk_bf16_f32 v162, v200, v192\n"
        "v_cmp_ne_u32_e64 s[24:25], s29, v255\n"
        "v_mfma_f32_16x16x32_bf16 v[156:159], v[218:221], v[88:91], v[156:159]\n"
        "buffer_store_dwordx3 v[230:232], v17, s[40:43], s90 offen offset:1152 sc1 nt\n"
        "v_cvt_pk_bf16_f32 v174, v111, v229\n"
        "v_cvt_pk_bf16_f32 v175, v181, v209\n"
        "s_add_u32 s90, s89, 392448\n"
        "v_mfma_f32_16x16x32_bf16 v[128:131], v[218:221], v[48:51], 0\n"
        "s_and_b64 s[24:25], s[24:25], s[76:77]\n"
        "v_cvt_pk_bf16_f32 v176, v201, v193\n"
        "v_pk_fma_f16 v104, v187, v121, v104\n"
        "v_mfma_f32_16x16x32_f16 v[128:131], v[140:143], v[168:171], v[128:131]\n"
        "s_cbranch_scc1 SP26_%=\n"
        "BK26_%=:\n"
        "v_mfma_f32_16x16x32_f16 v[128:131], v[136:139], v[34:37], v[128:131]\n"
        "buffer_store_dwordx3 v[160:162], v14, s[40:43], s91 offen offset:576 sc1 nt\n"
        "v_pk_fma_f16 v105, v187, v133, v105\n"
        "v_pk_fma_f16 v106, v187, v153, v106\n"
        "v_pk_fma_f16 v107, v187, v145, v107\n"
        "v_pk_fma_f16 v104, v188, v122, v104\n"
        "v_pk_fma_f16 v105, v188, v134, v105\n"
        "v_pk_fma_f16 v106, v188, v154, v106\n"
        "v_pk_fma_f16 v107, v188, v146, v107\n"
        "s_nop 1\n"
        "v_mfma_f32_16x16x32_f16 v[164:167], v[80:83], v[104:107], v[164:167]\n"
        "buffer_store_dwordx3 v[174:176], v14, s[40:43], s90 offen sc1 nt\n"
        "v_pk_fma_f16 v108, v187, v149, v108\n"
        "v_pk_fma_f16 v109, v187, v169, v109\n"
        "v_pk_fma_f16 v108, v188, v150, v108\n"
        "v_pk_fma_f16 v109, v188, v170, v109\n"
        "s_nop 1\n"
        "v_mfma_f32_16x16x32_f16 v[170:173], v[80:83], v[106:109], v[170:173]\n"
        "s_add_u32 s46, s89, 590016\n"
        "v_cvt_pk_bf16_f32 v106, v116, v124\n"
        "v_cvt_pk_bf16_f32 v34, v117, v125\n"
        "v_cvt_pk_bf16_f32 v110, v118, v126\n"
        "v_cvt_pk_bf16_f32 v104, v119, v127\n"
        "v_cvt_pk_bf16_f32 v107, v112, v40\n"
        "v_cvt_pk_bf16_f32 v35, v113, v41\n"
        "v_cvt_pk_bf16_f32 v108, v156, v128\n"
        "buffer_store_dwordx3 v[106:108], v15, s[40:43], s90 offen offset:768 sc1 nt\n"
        "v_cvt_pk_bf16_f32 v111, v114, v42\n"
        "v_cvt_pk_bf16_f32 v105, v115, v43\n"
        "v_cvt_pk_bf16_f32 v36, v157, v129\n"
        "s_add_u32 s91, s89, 490560\n"
        "v_cvt_pk_bf16_f32 v112, v158, v130\n"
        "buffer_store_dwordx3 v[34:36], v14, s[40:43], s91 offen offset:192 sc1 nt\n"
        "s_min_u32 s46, s46, 200540928\n"
        "v_cvt_pk_bf16_f32 v106, v159, v131\n"
        "v_cvt_pk_bf16_f32 v34, v164, v165\n"
        "v_cvt_pk_bf16_f32 v35, v166, v167\n"
        "v_cvt_pk_bf16_f32 v36, v172, v173\n"
        "buffer_store_dwordx3 v[110:112], v16, s[40:43], s91 offen offset:960 sc1 nt\n"
        "s_add_u32 s90, s89, 588672\n"
        "buffer_store_dwordx3 v[104:106], v14, s[40:43], s90 offen offset:384 sc1 nt\n"
        "buffer_store_dwordx3 v[34:36], v17, s[44:47], s90 offen offset:1152 sc1 nt\n"
        "FIN_%=:\n"
        "s_bitcmp1_b32 s50, 8\n"
        "s_cbranch_scc1 POST_%=\n"
        "DONE_%=:\n"
        ".Lswin_fin:\n"
        "s_endpgm\n"
        "POST_%=:\n"
        "s_lshl_b32 s50, s15, 3\n"
        "s_add_u32 s50, s50, s48\n"
        "s_add_u32 s50, s50, 32\n"
        "s_cmp_ge_u32 s50, 5329\n"
        "s_cbranch_scc1 DONE_%=\n"
        "s_waitcnt lgkmcnt(0)\n"
        "s_barrier\n"
        "v_mov_b32 v27, s29\n"
        "ds_write_b16 v22, v27 offset:3072\n"
        "s_mov_b32 s49, -100\n"
        "s_mul_i32 s87, s50, 1344\n"
        "s_mul_i32 s92, s50, 57457\n"
        "s_lshr_b32 s92, s92, 22\n"
        "s_mul_i32 s92, s92, 588672\n"
        "s_add_u32 s87, s87, s92\n"
        "s_lshr_b32 s23, s15, 2\n"
        "s_add_u32 s23, s23, 1\n"
        "s_sub_u32 s23, s23, s53\n"
        "s_mul_i32 s23, s23, 1584\n"
        "s_mov_b32 s22, 0\n"
        "s_mov_b32 s48, 0x10000\n"
        "s_mov_b32 s18, 0\n"
        "s_add_u32 s38, s36, s87\n"
        "s_addc_u32 s39, s37, 0\n"
        "s_add_u32 m0, s52, 0\n"
        "s_nop 0\n"
        "global_load_lds_dwordx4 v0, s[38:39]\n"
        "global_load_lds_dwordx4 v1, s[38:39] offset:1024\n"
        "global_load_lds_dwordx4 v2, s[38:39] offset:2048\n"
        "global_load_lds_dwordx4 v3, s[38:39] offset:3072\n"
        "s_add_u32 m0, s52, 4096\n"
        "s_nop 0\n"
        "global_load_lds_dwordx4 v4, s[38:39]\n"
        "global_load_lds_dwordx4 v5, s[38:39] offset:1024\n"
        "global_load_lds_dwordx4 v6, s[38:39] offset:2048\n"
        "global_load_lds_dwordx4 v7, s[38:39] offset:3072\n"
        "s_add_u32 m0, s52, 8192\n"
        "s_nop 0\n"
        "global_load_lds_dwordx4 v8, s[38:39]\n"
        "s_waitcnt vmcnt(0)\n"
        "ds_read_b128 v[126:129], v9\n"
        "ds_read_b128 v[132:135], v10\n"
        "ds_read_b128 v[138:141], v11\n"
        "ds_read_b128 v[106:109], v9 offset:3072\n"
        "ds_read_b128 v[112:115], v10 offset:3072\n"
        "ds_read_b128 v[118:121], v11 offset:3072\n"
        "ds_read_b128 v[146:149], v9 offset:6144\n"
        "ds_read_b128 v[152:155], v10 offset:6144\n"
        "ds_read_b128 v[158:161], v11 offset:6144\n"
        "s_mov_b32 s86, 0x80000000\n"
        "s_mov_b32 s89, 0x80000000\n"
        "s_mov_b32 s85, 0\n"
        "s_waitcnt lgkmcnt(0)\n"
        "s_nop 7\n"
        "s_branch TOP_%=\n"
        "SLOW_%=:\n"
        "s_waitcnt lgkmcnt(0)\n"
        "s_nop 15\n"
        "v_mbcnt_lo_u32_b32 v255, -1, 0\n"
        "v_mbcnt_hi_u32_b32 v255, -1, v255\n"
        "v_and_b32 v254, 15, v255\n"
        "v_mul_u32_u24 v252, 24, v254\n"
        "v_sub_u32 v253, v24, v252\n"
        "v_lshrrev_b32 v250, 4, v255\n"
        "v_mul_u32_u24 v252, 96, v250\n"
        "v_add_u32 v253, v253, v252\n"
        "ds_read_b64 v[132:133], v253\n"
        "ds_read_b64 v[30:31], v253 offset:24\n"
        "ds_read_b64 v[34:35], v253 offset:48\n"
        "ds_read_b64 v[46:47], v253 offset:72\n"
        "ds_read_b64 v[40:41], v253 offset:8\n"
        "ds_read_b64 v[136:137], v253 offset:32\n"
        "ds_read_b64 v[104:105], v253 offset:56\n"
        "ds_read_b64 v[144:145], v253 offset:80\n"
        "ds_read_b64 v[124:125], v253 offset:16\n"
        "ds_read_b64 v[116:117], v253 offset:40\n"
        "ds_read_b64 v[108:109], v253 offset:64\n"
        "ds_read_b64 v[140:141], v253 offset:88\n"
        "v_mfma_f32_16x16x32_bf16 v[120:123], v[186:189], v[186:189], 0\n"
        "v_mov_b32 v253, 0x05040100\n"
        "v_subrev_u32 v248, 159328, v20\n"
        "v_mfma_f32_16x16x32_bf16 v[120:123], v[210:213], v[210:213], v[120:123]\n"
        "v_mov_b32 v249, 161888\n"
        "v_mov_b32 v166, 0x3f803f80\n"
        "v_mfma_f32_16x16x32_bf16 v[120:123], v[214:217], v[214:217], v[120:123]\n"
        "v_mov_b32 v167, 0x3f803f80\n"
        "v_mov_b32 v168, 0x3f803f80\n"
        "v_mov_b32 v169, 0x3f803f80\n"
        "s_nop 1\n"
        "v_mfma_f32_16x16x32_bf16 v[112:115], v[166:169], v[214:217], 0\n"
        "v_mov_b32 v255, 0xff800000\n"
        "v_mov_b32 v139, 0\n"
        "v_mfma_f32_16x16x32_bf16 v[112:115], v[166:169], v[210:213], v[112:115]\n"
        "s_waitcnt lgkmcnt(11)\n"
        "v_mov_b32 v43, 0\n"
        "v_mov_b32 v143, 0\n"
        "v_mfma_f32_16x16x32_bf16 v[112:115], v[166:169], v[186:189], v[112:115]\n"
        "s_waitcnt lgkmcnt(10)\n"
        "v_mov_b32 v119, 0\n"
        "v_mov_b32 v107, 0\n"
        "v_mfma_f32_16x16x32_bf16 v[128:131], v[206:209], v[206:209], 0\n"
        "s_waitcnt lgkmcnt(9)\n"
        "v_mov_b32 v37, 0\n"
        "v_mov_b32 v111, 0\n"
        "v_mfma_f32_16x16x32_bf16 v[128:131], v[198:201], v[198:201], v[128:131]\n"
        "s_waitcnt lgkmcnt(8)\n"
        "v_mov_b32 v147, 0\n"
        "v_mov_b32 v135, 0\n"
        "v_mfma_f32_16x16x32_bf16 v[128:131], v[182:185], v[182:185], v[128:131]\n"
        "s_waitcnt lgkmcnt(7)\n"
        "v_mov_b32 v127, 0\n"
        "v_and_b32 v248, 384, v248\n"
        "v_mfma_f32_16x16x32_bf16 v[162:165], v[166:169], v[182:185], 0\n"
        "s_waitcnt lgkmcnt(6)\n"
        "v_lshlrev_b32 v248, 1, v248\n"
        "v_add_u32 v248, 160864, v248\n"
        "v_mfma_f32_16x16x32_bf16 v[162:165], v[166:169], v[198:201], v[162:165]\n"
        "s_waitcnt lgkmcnt(5)\n"
        "v_lshlrev_b32 v251, 2, v254\n"
        "v_lshlrev_b32 v252, 4, v250\n"
        "v_mfma_f32_16x16x32_bf16 v[162:165], v[166:169], v[206:209], v[162:165]\n"
        "s_waitcnt lgkmcnt(4)\n"
        "v_add_u32 v251, v251, v248\n"
        "v_add_u32 v252, v252, v248\n"
        "v_mfma_f32_16x16x32_bf16 v[152:155], v[194:197], v[194:197], 0\n"
        "s_waitcnt lgkmcnt(3)\n"
        "v_cndmask_b32 v249, v249, v251, vcc\n"
        "v_cndmask_b32_e64 v254, v120, v121, s[56:57]\n"
        "v_mfma_f32_16x16x32_bf16 v[152:155], v[202:205], v[202:205], v[152:155]\n"
        "s_waitcnt lgkmcnt(2)\n"
        "v_cndmask_b32_e64 v254, v254, v122, s[58:59]\n"
        "v_cndmask_b32_e64 v254, v254, v123, s[60:61]\n"
        "v_mfma_f32_16x16x32_bf16 v[152:155], v[218:221], v[218:221], v[152:155]\n"
        "s_waitcnt lgkmcnt(1)\n"
        "v_mul_f32 v254, s80, v254\n"
        "v_mul_f32 v250, s80, v112\n"
        "v_mfma_f32_16x16x32_bf16 v[156:159], v[166:169], v[218:221], 0\n"
        "s_waitcnt lgkmcnt(0)\n"
        "v_fma_f32 v250, -v250, v250, v254\n"
        "v_max_f32 v250, 0, v250\n"
        "v_mfma_f32_16x16x32_bf16 v[156:159], v[166:169], v[202:205], v[156:159]\n"
        "v_add_f32 v250, s81, v250\n"
        "v_perm_b32 v120, v133, v132, v253\n"
        "v_mfma_f32_16x16x32_bf16 v[156:159], v[166:169], v[194:197], v[156:159]\n"
        "v_rsq_f32 v250, v250\n"
        "v_perm_b32 v121, v31, v30, v253\n"
        "v_perm_b32 v122, v35, v34, v253\n"
        "v_perm_b32 v123, v47, v46, v253\n"
        "s_nop 1\n"
        "v_mfma_f32_16x16x32_bf16 v[170:173], v[120:123], v[120:123], 0\n"
        "ds_write_b32 v249, v250\n"
        "v_perm_b32 v112, v41, v40, v253\n"
        "v_perm_b32 v113, v137, v136, v253\n"
        "v_perm_b32 v114, v105, v104, v253\n"
        "v_perm_b32 v115, v145, v144, v253\n"
        "s_nop 1\n"
        "v_mfma_f32_16x16x32_bf16 v[170:173], v[112:115], v[112:115], v[170:173]\n"
        "v_perm_b32 v148, v125, v124, v253\n"
        "v_perm_b32 v149, v117, v116, v253\n"
        "v_perm_b32 v150, v109, v108, v253\n"
        "v_perm_b32 v151, v141, v140, v253\n"
        "s_nop 1\n"
        "v_mfma_f32_16x16x32_bf16 v[170:173], v[148:151], v[148:151], v[170:173]\n"
        "v_cndmask_b32_e64 v254, v128, v129, s[56:57]\n"
        "v_cndmask_b32_e64 v254, v254, v130, s[58:59]\n"
        "v_mfma_f32_16x16x32_bf16 v[158:161], v[166:169], v[148:151], 0\n"
        "v_cndmask_b32_e64 v254, v254, v131, s[60:61]\n"
        "v_mul_f32 v254, s80, v254\n"
        "v_mfma_f32_16x16x32_bf16 v[158:161], v[166:169], v[112:115], v[158:161]\n"
        "v_mul_f32 v253, s80, v162\n"
        "v_fma_f32 v253, -v253, v253, v254\n"
        "v_mfma_f32_16x16x32_bf16 v[158:161], v[166:169], v[120:123], v[158:161]\n"
        "v_max_f32 v253, 0, v253\n"
        "v_add_f32 v253, s81, v253\n"
        "v_mfma_f32_16x16x32_bf16 v[130:133], v[186:189], a[216:219], 0\n"
        "v_rsq_f32 v253, v253\n"
        "v_mfma_f32_16x16x32_bf16 v[130:133], v[210:213], a[220:223], v[130:133]\n"
        "ds_write_b32 v249, v253 offset:64\n"
        "v_mfma_f32_16x16x32_bf16 v[130:133], v[214:217], a[224:227], v[130:133]\n"
        "v_cndmask_b32_e64 v254, v152, v153, s[56:57]\n"
        "v_cndmask_b32_e64 v254, v254, v154, s[58:59]\n"
        "v_mfma_f32_16x16x32_bf16 v[164:167], v[182:185], a[224:227], 0\n"
        "v_cndmask_b32_e64 v254, v254, v155, s[60:61]\n"
        "v_mul_f32 v254, s80, v254\n"
        "v_mfma_f32_16x16x32_bf16 v[164:167], v[198:201], a[220:223], v[164:167]\n"
        "v_mul_f32 v253, s80, v156\n"
        "v_fma_f32 v253, -v253, v253, v254\n"
        "v_mfma_f32_16x16x32_bf16 v[164:167], v[206:209], a[216:219], v[164:167]\n"
        "v_max_f32 v253, 0, v253\n"
        "v_add_f32 v253, s81, v253\n"
        "v_mfma_f32_16x16x32_bf16 v[160:163], v[194:197], a[216:219], 0\n"
        "v_rsq_f32 v253, v253\n"
        "v_mfma_f32_16x16x32_bf16 v[160:163], v[202:205], a[220:223], v[160:163]\n"
        "ds_write_b32 v249, v253 offset:128\n"
        "v_mfma_f32_16x16x32_bf16 v[160:163], v[218:221], a[224:227], v[160:163]\n"
        "v_cndmask_b32_e64 v253, v170, v171, s[56:57]\n"
        "v_cndmask_b32_e64 v253, v253, v172, s[58:59]\n"
        "v_mfma_f32_16x16x32_bf16 v[152:155], v[148:151], a[224:227], 0\n"
        "v_cndmask_b32_e64 v253, v253, v173, s[60:61]\n"
        "v_mul_f32 v253, s80, v253\n"
        "v_mfma_f32_16x16x32_bf16 v[152:155], v[112:115], a[220:223], v[152:155]\n"
        "v_mul_f32 v254, s80, v158\n"
        "v_fma_f32 v254, -v254, v254, v253\n"
        "v_mfma_f32_16x16x32_bf16 v[152:155], v[120:123], a[216:219], v[152:155]\n"
        "v_max_f32 v254, 0, v254\n"
        "v_add_f32 v254, s81, v254\n"
        "v_mfma_f32_16x16x32_bf16 v[156:159], v[194:197], a[228:231], 0\n"
        "v_rsq_f32 v254, v254\n"
        "v_mfma_f32_16x16x32_bf16 v[156:159], v[202:205], a[232:235], v[156:159]\n"
        "ds_write_b32 v249, v254 offset:192\n"
        "v_mfma_f32_16x16x32_bf16 v[156:159], v[218:221], a[236:239], v[156:159]\n"
        "v_cvt_pk_bf16_f32 v128, v130, v131\n"
        "v_cvt_pk_bf16_f32 v129, v132, v133\n"
        "v_mfma_f32_16x16x32_bf16 v[168:171], v[182:185], a[236:239], 0\n"
        "v_cvt_pk_bf16_f32 v130, v164, v165\n"
        "v_cvt_pk_bf16_f32 v131, v166, v167\n"
        "v_mfma_f32_16x16x32_bf16 v[168:171], v[198:201], a[232:235], v[168:171]\n"
        "v_cvt_pk_bf16_f32 v136, v160, v161\n"
        "v_cvt_pk_bf16_f32 v137, v162, v163\n"
        "v_mfma_f32_16x16x32_bf16 v[168:171], v[206:209], a[228:231], v[168:171]\n"
        "s_nop 0\n"
        "v_cvt_pk_bf16_f32 v138, v152, 0\n"
        "v_cvt_pk_bf16_f32 v40, v156, v157\n"
        "v_mfma_f32_16x16x32_bf16 v[160:163], v[186:189], a[228:231], 0\n"
        "s_nop 0\n"
        "v_cvt_pk_bf16_f32 v41, v158, v159\n"
        "v_mfma_f32_16x16x32_bf16 v[160:163], v[210:213], a[232:235], v[160:163]\n"
        "s_nop 0\n"
        "v_cvt_pk_bf16_f32 v154, v168, v169\n"
        "v_cvt_pk_bf16_f32 v155, v170, v171\n"
        "v_mfma_f32_16x16x32_bf16 v[160:163], v[214:217], a[236:239], v[160:163]\n"
        "s_nop 1\n"
        "v_mfma_f32_16x16x32_bf16 v[156:159], v[148:151], a[236:239], 0\n"
        "s_nop 2\n"
        "v_mfma_f32_16x16x32_bf16 v[156:159], v[112:115], a[232:235], v[156:159]\n"
        "s_nop 0\n"
        "v_cvt_pk_bf16_f32 v152, v160, v161\n"
        "v_cvt_pk_bf16_f32 v153, v162, v163\n"
        "v_mfma_f32_16x16x32_bf16 v[156:159], v[120:123], a[228:231], v[156:159]\n"
        "s_nop 1\n"
        "v_mfma_f32_16x16x32_bf16 v[162:165], v[186:189], a[240:243], 0\n"
        "s_nop 2\n"
        "v_mfma_f32_16x16x32_bf16 v[162:165], v[210:213], a[244:247], v[162:165]\n"
        "s_nop 0\n"
        "v_cvt_pk_bf16_f32 v42, v156, 0\n"
        "v_mfma_f32_16x16x32_bf16 v[162:165], v[214:217], a[248:251], v[162:165]\n"
        "s_nop 1\n"
        "v_mfma_f32_16x16x32_bf16 v[166:169], v[182:185], a[248:251], 0\n"
        "s_nop 2\n"
        "v_mfma_f32_16x16x32_bf16 v[166:169], v[198:201], a[244:247], v[166:169]\n"
        "s_nop 0\n"
        "v_cvt_pk_bf16_f32 v160, v162, v163\n"
        "v_cvt_pk_bf16_f32 v161, v164, v165\n"
        "v_mfma_f32_16x16x32_bf16 v[166:169], v[206:209], a[240:243], v[166:169]\n"
        "s_nop 1\n"
        "v_mfma_f32_16x16x32_bf16 v[156:159], v[194:197], a[240:243], 0\n"
        "s_nop 2\n"
        "v_mfma_f32_16x16x32_bf16 v[156:159], v[202:205], a[244:247], v[156:159]\n"
        "s_nop 0\n"
        "v_cvt_pk_bf16_f32 v162, v166, v167\n"
        "v_cvt_pk_bf16_f32 v163, v168, v169\n"
        "v_mfma_f32_16x16x32_bf16 v[156:159], v[218:221], a[248:251], v[156:159]\n"
        "s_nop 1\n"
        "v_mfma_f32_16x16x32_bf16 v[164:167], v[148:151], a[248:251], 0\n"
        "s_nop 2\n"
        "v_mfma_f32_16x16x32_bf16 v[164:167], v[112:115], a[244:247], v[164:167]\n"
        "s_nop 0\n"
        "v_cvt_pk_bf16_f32 v140, v156, v157\n"
        "v_cvt_pk_bf16_f32 v141, v158, v159\n"
        "v_mfma_f32_16x16x32_bf16 v[164:167], v[120:123], a[240:243], v[164:167]\n"
        "s_nop 1\n"
        "v_mfma_f32_16x16x32_bf16 v[156:159], v[194:197], a[252:255], 0\n"
        "s_nop 2\n"
        "v_mfma_f32_16x16x32_bf16 v[156:159], v[202:205], v[72:75], v[156:159]\n"
        "s_nop 0\n"
        "v_cvt_pk_bf16_f32 v142, v164, 0\n"
        "v_mfma_f32_16x16x32_bf16 v[156:159], v[218:221], v[64:67], v[156:159]\n"
        "s_nop 1\n"
        "v_mfma_f32_16x16x32_bf16 v[164:167], v[182:185], v[64:67], 0\n"
        "s_nop 2\n"
        "v_mfma_f32_16x16x32_bf16 v[164:167], v[198:201], v[72:75], v[164:167]\n"
        "s_nop 0\n"
        "v_cvt_pk_bf16_f32 v116, v156, v157\n"
        "v_cvt_pk_bf16_f32 v117, v158, v159\n"
        "v_mfma_f32_16x16x32_bf16 v[164:167], v[206:209], a[252:255], v[164:167]\n"
        "s_nop 1\n"
        "v_mfma_f32_16x16x32_bf16 v[168:171], v[186:189], a[252:255], 0\n"
        "s_nop 2\n"
        "v_mfma_f32_16x16x32_bf16 v[168:171], v[210:213], v[72:75], v[168:171]\n"
        "s_nop 0\n"
        "v_cvt_pk_bf16_f32 v158, v164, v165\n"
        "v_cvt_pk_bf16_f32 v159, v166, v167\n"
        "v_mfma_f32_16x16x32_bf16 v[168:171], v[214:217], v[64:67], v[168:171]\n"
        "s_nop 1\n"
        "v_mfma_f32_16x16x32_bf16 v[164:167], v[148:151], v[64:67], 0\n"
        "s_nop 2\n"
        "v_mfma_f32_16x16x32_bf16 v[164:167], v[112:115], v[72:75], v[164:167]\n"
        "s_nop 0\n"
        "v_cvt_pk_bf16_f32 v156, v168, v169\n"
        "v_cvt_pk_bf16_f32 v157, v170, v171\n"
        "v_mfma_f32_16x16x32_bf16 v[164:167], v[120:123], a[252:255], v[164:167]\n"
        "s_nop 1\n"
        "v_mfma_f32_16x16x32_bf16 v[168:171], v[186:189], v[68:71], 0\n"
        "s_nop 2\n"
        "v_mfma_f32_16x16x32_bf16 v[168:171], v[210:213], v[60:63], v[168:171]\n"
        "s_nop 0\n"
        "v_cvt_pk_bf16_f32 v118, v164, 0\n"
        "v_mfma_f32_16x16x32_bf16 v[168:171], v[214:217], v[56:59], v[168:171]\n"
        "s_nop 1\n"
        "v_mfma_f32_16x16x32_bf16 v[176:179], v[182:185], v[56:59], 0\n"
        "s_nop 2\n"
        "v_mfma_f32_16x16x32_bf16 v[176:179], v[198:201], v[60:63], v[176:179]\n"
        "s_nop 0\n"
        "v_cvt_pk_bf16_f32 v164, v168, v169\n"
        "v_cvt_pk_bf16_f32 v165, v170, v171\n"
        "v_mfma_f32_16x16x32_bf16 v[176:179], v[206:209], v[68:71], v[176:179]\n"
        "s_nop 1\n"
        "v_mfma_f32_16x16x32_bf16 v[172:175], v[194:197], v[68:71], 0\n"
        "s_nop 2\n"
        "v_mfma_f32_16x16x32_bf16 v[172:175], v[202:205], v[60:63], v[172:175]\n"
        "s_nop 0\n"
        "v_cvt_pk_bf16_f32 v166, v176, v177\n"
        "v_cvt_pk_bf16_f32 v167, v178, v179\n"
        "v_mfma_f32_16x16x32_bf16 v[172:175], v[218:221], v[56:59], v[172:175]\n"
        "s_nop 1\n"
        "v_mfma_f32_16x16x32_bf16 v[168:171], v[148:151], v[56:59], 0\n"
        "s_nop 2\n"
        "v_mfma_f32_16x16x32_bf16 v[168:171], v[112:115], v[60:63], v[168:171]\n"
        "s_nop 0\n"
        "v_cvt_pk_bf16_f32 v104, v172, v173\n"
        "v_cvt_pk_bf16_f32 v105, v174, v175\n"
        "v_mfma_f32_16x16x32_bf16 v[168:171], v[120:123], v[68:71], v[168:171]\n"
        "s_nop 1\n"
        "v_mfma_f32_16x16x32_bf16 v[172:175], v[194:197], v[52:55], 0\n"
        "s_nop 2\n"
        "v_mfma_f32_16x16x32_bf16 v[172:175], v[202:205], v[76:79], v[172:175]\n"
        "s_nop 0\n"
        "v_cvt_pk_bf16_f32 v106, v168, 0\n"
        "v_mfma_f32_16x16x32_bf16 v[172:175], v[218:221], v[84:87], v[172:175]\n"
        "v_mfma_f32_16x16x32_bf16 v[230:233], v[182:185], v[84:87], 0\n"
        "v_mfma_f32_16x16x32_bf16 v[230:233], v[198:201], v[76:79], v[230:233]\n"
        "ds_read_b128 v[168:171], v252\n"
        "ds_read_b128 v[176:179], v252 offset:64\n"
        "v_mfma_f32_16x16x32_bf16 v[230:233], v[206:209], v[52:55], v[230:233]\n"
        "ds_read_b128 v[190:193], v252 offset:128\n"
        "ds_read_b32 v250, v251\n"
        "v_mfma_f32_16x16x32_bf16 v[226:229], v[186:189], v[52:55], 0\n"
        "ds_read_b32 v252, v251 offset:64\n"
        "ds_read_b32 v254, v251 offset:128\n"
        "v_mfma_f32_16x16x32_bf16 v[226:229], v[210:213], v[76:79], v[226:229]\n"
        "ds_read_b32 v253, v251 offset:192\n"
        "v_cvt_pk_bf16_f32 v34, v172, v173\n"
        "v_mfma_f32_16x16x32_bf16 v[226:229], v[214:217], v[84:87], v[226:229]\n"
        "s_nop 0\n"
        "v_cvt_pk_bf16_f32 v35, v174, v175\n"
        "v_cvt_pk_bf16_f32 v174, v230, v231\n"
        "v_mfma_f32_16x16x32_bf16 v[222:225], v[148:151], v[84:87], 0\n"
        "s_nop 0\n"
        "v_cvt_pk_bf16_f32 v175, v232, v233\n"
        "v_mfma_f32_16x16x32_bf16 v[222:225], v[112:115], v[76:79], v[222:225]\n"
        "s_nop 0\n"
        "v_cvt_pk_bf16_f32 v172, v226, v227\n"
        "v_cvt_pk_bf16_f32 v173, v228, v229\n"
        "v_mfma_f32_16x16x32_bf16 v[222:225], v[120:123], v[52:55], v[222:225]\n"
        "s_nop 1\n"
        "v_mfma_f32_16x16x32_bf16 v[226:229], a[144:147], v[186:189], 0\n"
        "s_nop 2\n"
        "v_mfma_f32_16x16x32_bf16 v[226:229], a[148:151], v[210:213], v[226:229]\n"
        "s_nop 0\n"
        "v_cvt_pk_bf16_f32 v36, v222, 0\n"
        "v_mfma_f32_16x16x32_bf16 v[226:229], a[152:155], v[214:217], v[226:229]\n"
        "s_nop 1\n"
        "v_mfma_f32_16x16x32_bf16 v[222:225], a[164:167], v[214:217], 0\n"
        "s_waitcnt lgkmcnt(0)\n"
        "s_nop 1\n"
        "v_mfma_f32_16x16x32_bf16 v[222:225], a[160:163], v[210:213], v[222:225]\n"
        "s_nop 0\n"
        "v_cvt_pk_bf16_f32 v238, v226, v227\n"
        "v_cvt_pk_bf16_f32 v239, v228, v229\n"
        "v_mfma_f32_16x16x32_bf16 v[222:225], a[156:159], v[186:189], v[222:225]\n"
        "s_nop 1\n"
        "v_mfma_f32_16x16x32_bf16 v[226:229], a[168:171], v[186:189], 0\n"
        "s_nop 2\n"
        "v_mfma_f32_16x16x32_bf16 v[226:229], a[172:175], v[210:213], v[226:229]\n"
        "s_nop 0\n"
        "v_cvt_pk_bf16_f32 v240, v222, v223\n"
        "v_cvt_pk_bf16_f32 v241, v224, v225\n"
        "v_mfma_f32_16x16x32_bf16 v[226:229], a[176:179], v[214:217], v[226:229]\n"
        "s_nop 1\n"
        "v_mfma_f32_16x16x32_bf16 v[222:225], a[188:191], v[214:217], 0\n"
        "s_nop 2\n"
        "v_mfma_f32_16x16x32_bf16 v[222:225], a[184:187], v[210:213], v[222:225]\n"
        "s_nop 0\n"
        "v_cvt_pk_bf16_f32 v234, v226, v227\n"
        "v_cvt_pk_bf16_f32 v235, v228, v229\n"
        "v_mfma_f32_16x16x32_bf16 v[222:225], a[180:183], v[186:189], v[222:225]\n"
        "s_nop 1\n"
        "v_mfma_f32_16x16x32_bf16 v[226:229], a[192:195], v[186:189], 0\n"
        "s_nop 2\n"
        "v_mfma_f32_16x16x32_bf16 v[226:229], a[196:199], v[210:213], v[226:229]\n"
        "s_nop 0\n"
        "v_cvt_pk_bf16_f32 v236, v222, v223\n"
        "v_cvt_pk_bf16_f32 v237, v224, v225\n"
        "v_mfma_f32_16x16x32_bf16 v[226:229], a[200:203], v[214:217], v[226:229]\n"
        "s_nop 1\n"
        "v_mfma_f32_16x16x32_bf16 v[230:233], a[212:215], v[214:217], 0\n"
        "s_nop 2\n"
        "v_mfma_f32_16x16x32_bf16 v[230:233], a[208:211], v[210:213], v[230:233]\n"
        "s_nop 0\n"
        "v_cvt_pk_bf16_f32 v222, v226, v227\n"
        "v_cvt_pk_bf16_f32 v223, v228, v229\n"
        "v_mfma_f32_16x16x32_bf16 v[230:233], a[204:207], v[186:189], v[230:233]\n"
        "s_nop 1\n"
        "v_mfma_f32_16x16x32_bf16 v[226:229], v[186:189], v[238:241], 0\n"
        "s_nop 2\n"
        "v_mfma_f32_16x16x32_bf16 v[226:229], v[210:213], v[234:237], v[226:229]\n"
        "s_nop 0\n"
        "v_cvt_pk_bf16_f32 v224, v230, v231\n"
        "v_cvt_pk_bf16_f32 v225, v232, v233\n"
        "s_nop 1\n"
        "v_mfma_f32_16x16x32_bf16 v[226:229], v[214:217], v[222:225], v[226:229]\n"
        "s_nop 1\n"
        "v_mfma_f32_16x16x32_bf16 v[230:233], v[182:185], v[222:225], 0\n"
        "s_nop 2\n"
        "v_mfma_f32_16x16x32_bf16 v[230:233], v[198:201], v[234:237], v[230:233]\n"
        "s_nop 0\n"
        "v_mul_f32 v226, v226, v168\n"
        "v_mul_f32 v227, v227, v169\n"
        "v_mfma_f32_16x16x32_bf16 v[230:233], v[206:209], v[238:241], v[230:233]\n"
        "s_nop 0\n"
        "v_mul_f32 v228, v228, v170\n"
        "v_mul_f32 v229, v229, v171\n"
        "v_mfma_f32_16x16x32_bf16 v[242:245], v[194:197], v[238:241], 0\n"
        "s_nop 0\n"
        "v_max3_f32 v251, v226, v227, v228\n"
        "v_mfma_f32_16x16x32_bf16 v[242:245], v[202:205], v[234:237], v[242:245]\n"
        "s_nop 0\n"
        "v_mul_f32 v230, v230, v176\n"
        "v_mul_f32 v231, v231, v177\n"
        "v_mfma_f32_16x16x32_bf16 v[242:245], v[218:221], v[222:225], v[242:245]\n"
        "v_mul_f32 v232, v232, v178\n"
        "v_mul_f32 v233, v233, v179\n"
        "v_mfma_f32_16x16x32_bf16 v[246:249], v[148:151], v[222:225], 0\n"
        "s_nop 0\n"
        "v_max3_f32 v251, v251, v229, v230\n"
        "v_max3_f32 v251, v251, v231, v232\n"
        "v_mfma_f32_16x16x32_bf16 v[246:249], v[112:115], v[234:237], v[246:249]\n"
        "s_nop 0\n"
        "v_mul_f32 v242, v242, v190\n"
        "v_mul_f32 v243, v243, v191\n"
        "v_mfma_f32_16x16x32_bf16 v[246:249], v[120:123], v[238:241], v[246:249]\n"
        "s_nop 0\n"
        "v_mul_f32 v244, v244, v192\n"
        "v_mul_f32 v245, v245, v193\n"
        "v_max3_f32 v251, v251, v233, v242\n"
        "v_max3_f32 v251, v251, v243, v244\n"
        "s_nop 2\n"
        "v_mul_f32 v246, v246, v253\n"
        "v_cndmask_b32_e64 v246, v255, v246, s[62:63]\n"
        "v_max3_f32 v251, v251, v245, v246\n"
        "v_mov_b32 v249, v251\n"
        "s_nop 1\n"
        "v_permlane32_swap_b32 v251, v249\n"
        "v_max_f32 v251, v251, v249\n"
        "v_mov_b32 v249, v251\n"
        "s_nop 1\n"
        "v_permlane16_swap_b32 v251, v249\n"
        "v_max_f32 v251, v251, v249\n"
        "v_mul_f32 v251, v251, v250\n"
        "v_fma_f32 v226, v226, v250, -v251\n"
        "v_exp_f32 v226, v226\n"
        "v_fma_f32 v227, v227, v250, -v251\n"
        "v_exp_f32 v227, v227\n"
        "s_nop 0\n"
        "v_add_f32 v249, v226, v227\n"
        "v_mul_f32 v226, v226, v168\n"
        "v_fma_f32 v246, v246, v250, -v251\n"
        "v_exp_f32 v246, v246\n"
        "v_fma_f32 v245, v245, v250, -v251\n"
        "v_exp_f32 v245, v245\n"
        "v_fma_f32 v244, v244, v250, -v251\n"
        "v_exp_f32 v244, v244\n"
        "v_fma_f32 v243, v243, v250, -v251\n"
        "v_exp_f32 v243, v243\n"
        "v_fma_f32 v242, v242, v250, -v251\n"
        "v_exp_f32 v242, v242\n"
        "v_fma_f32 v233, v233, v250, -v251\n"
        "v_exp_f32 v233, v233\n"
        "v_fma_f32 v232, v232, v250, -v251\n"
        "v_exp_f32 v232, v232\n"
        "v_fma_f32 v231, v231, v250, -v251\n"
        "v_exp_f32 v231, v231\n"
        "v_fma_f32 v230, v230, v250, -v251\n"
        "v_exp_f32 v230, v230\n"
        "v_fma_f32 v229, v229, v250, -v251\n"
        "v_exp_f32 v229, v229\n"
        "v_fma_f32 v228, v228, v250, -v251\n"
        "v_exp_f32 v228, v228\n"
        "s_nop 0\n"
        "v_add_f32 v249, v249, v228\n"
        "v_add_f32 v249, v249, v229\n"
        "v_add_f32 v249, v249, v230\n"
        "v_add_f32 v249, v249, v231\n"
        "v_add_f32 v249, v249, v232\n"
        "v_add_f32 v249, v249, v233\n"
        "v_add_f32 v249, v249, v242\n"
        "v_add_f32 v249, v249, v243\n"
        "v_add_f32 v249, v249, v244\n"
        "v_add_f32 v249, v249, v245\n"
        "v_add_f32 v249, v249, v246\n"
        "v_mov_b32 v251, v249\n"
        "s_nop 1\n"
        "v_permlane32_swap_b32 v249, v251\n"
        "v_add_f32 v249, v249, v251\n"
        "v_mov_b32 v251, v249\n"
        "s_nop 1\n"
        "v_permlane16_swap_b32 v249, v251\n"
        "v_add_f32 v249, v249, v251\n"
        "v_rcp_f32 v249, v249\n"
        "s_nop 0\n"
        "v_mul_f32 v226, v226, v249\n"
        "v_mul_f32 v227, v227, v169\n"
        "v_mul_f32 v227, v227, v249\n"
        "v_cvt_pk_bf16_f32 v234, v226, v227\n"
        "v_mul_f32 v228, v228, v170\n"
        "v_mul_f32 v228, v228, v249\n"
        "v_mul_f32 v229, v229, v171\n"
        "v_mul_f32 v229, v229, v249\n"
        "v_cvt_pk_bf16_f32 v235, v228, v229\n"
        "v_mul_f32 v230, v230, v176\n"
        "v_mul_f32 v230, v230, v249\n"
        "v_mul_f32 v231, v231, v177\n"
        "v_mul_f32 v231, v231, v249\n"
        "v_cvt_pk_bf16_f32 v236, v230, v231\n"
        "v_mul_f32 v232, v232, v178\n"
        "v_mul_f32 v232, v232, v249\n"
        "v_mul_f32 v233, v233, v179\n"
        "v_mul_f32 v233, v233, v249\n"
        "v_cvt_pk_bf16_f32 v237, v232, v233\n"
        "s_nop 1\n"
        "v_mfma_f32_16x16x32_bf16 v[222:225], v[234:237], v[128:131], 0\n"
        "v_mul_f32 v242, v242, v190\n"
        "v_mul_f32 v243, v243, v191\n"
        "v_mul_f32 v242, v242, v249\n"
        "v_mul_f32 v243, v243, v249\n"
        "v_cvt_pk_bf16_f32 v108, v242, v243\n"
        "v_mul_f32 v244, v244, v192\n"
        "v_mul_f32 v244, v244, v249\n"
        "v_mul_f32 v245, v245, v193\n"
        "v_mul_f32 v245, v245, v249\n"
        "v_cvt_pk_bf16_f32 v109, v244, v245\n"
        "v_mul_f32 v246, v246, v253\n"
        "v_mul_f32 v246, v246, v249\n"
        "v_cvt_pk_bf16_f32 v110, v246, 0\n"
        "s_nop 1\n"
        "v_mfma_f32_16x16x32_bf16 v[222:225], v[108:111], v[136:139], v[222:225]\n"
        "v_mfma_f32_16x16x32_bf16 v[222:225], v[186:189], v[88:91], v[222:225]\n"
        "v_mfma_f32_16x16x32_bf16 v[226:229], v[186:189], v[48:51], 0\n"
        "v_mfma_f32_16x16x32_bf16 v[226:229], v[108:111], v[40:43], v[226:229]\n"
        "v_mfma_f32_16x16x32_bf16 v[226:229], v[234:237], v[152:155], v[226:229]\n"
        "s_add_u32 s90, s89, 0\n"
        "s_nop 0\n"
        "v_mfma_f32_16x16x32_bf16 v[230:233], v[234:237], v[160:163], 0\n"
        "s_nop 2\n"
        "v_mfma_f32_16x16x32_bf16 v[230:233], v[108:111], v[140:143], v[230:233]\n"
        "s_nop 0\n"
        "v_cvt_pk_bf16_f32 v124, v222, v226\n"
        "v_cvt_pk_bf16_f32 v238, v223, v227\n"
        "v_mfma_f32_16x16x32_bf16 v[230:233], v[210:213], v[88:91], v[230:233]\n"
        "v_cvt_pk_bf16_f32 v132, v224, v228\n"
        "v_cvt_pk_bf16_f32 v144, v225, v229\n"
        "v_mfma_f32_16x16x32_bf16 v[222:225], v[210:213], v[48:51], 0\n"
        "s_add_u32 s91, s89, 98112\n"
        "v_mfma_f32_16x16x32_bf16 v[222:225], v[108:111], v[116:119], v[222:225]\n"
        "v_mfma_f32_16x16x32_bf16 v[222:225], v[234:237], v[156:159], v[222:225]\n"
        "s_nop 1\n"
        "v_mfma_f32_16x16x32_bf16 v[226:229], v[234:237], v[164:167], 0\n"
        "s_nop 2\n"
        "v_mfma_f32_16x16x32_bf16 v[226:229], v[108:111], v[104:107], v[226:229]\n"
        "s_nop 0\n"
        "v_cvt_pk_bf16_f32 v125, v230, v222\n"
        "v_cvt_pk_bf16_f32 v239, v231, v223\n"
        "v_mfma_f32_16x16x32_bf16 v[226:229], v[214:217], v[88:91], v[226:229]\n"
        "v_cvt_pk_bf16_f32 v133, v232, v224\n"
        "v_cvt_pk_bf16_f32 v145, v233, v225\n"
        "v_mfma_f32_16x16x32_bf16 v[222:225], v[214:217], v[48:51], 0\n"
        "v_mfma_f32_16x16x32_bf16 v[222:225], v[108:111], v[34:37], v[222:225]\n"
        "v_mfma_f32_16x16x32_bf16 v[222:225], v[234:237], v[172:175], v[222:225]\n"
        "s_nop 1\n"
        "v_mfma_f32_16x16x32_bf16 v[108:111], a[144:147], v[206:209], 0\n"
        "s_nop 2\n"
        "v_mfma_f32_16x16x32_bf16 v[108:111], a[148:151], v[198:201], v[108:111]\n"
        "s_nop 0\n"
        "v_cvt_pk_bf16_f32 v126, v226, v222\n"
        "v_cvt_pk_bf16_f32 v240, v227, v223\n"
        "v_mfma_f32_16x16x32_bf16 v[108:111], a[152:155], v[182:185], v[108:111]\n"
        "s_nop 0\n"
        "v_cvt_pk_bf16_f32 v134, v228, v224\n"
        "v_cvt_pk_bf16_f32 v146, v229, v225\n"
        "v_mfma_f32_16x16x32_bf16 v[226:229], a[164:167], v[182:185], 0\n"
        "s_nop 1\n"
        "v_mfma_f32_16x16x32_bf16 v[226:229], a[160:163], v[198:201], v[226:229]\n"
        "s_nop 0\n"
        "v_cvt_pk_bf16_f32 v222, v108, v109\n"
        "v_cvt_pk_bf16_f32 v223, v110, v111\n"
        "v_mfma_f32_16x16x32_bf16 v[226:229], a[156:159], v[206:209], v[226:229]\n"
        "s_nop 0\n"
        "v_mfma_f32_16x16x32_bf16 v[108:111], a[168:171], v[206:209], 0\n"
        "buffer_store_dwordx3 v[124:126], v14, s[40:43], s90 offen sc1 nt\n"
        "s_nop 2\n"
        "v_mfma_f32_16x16x32_bf16 v[108:111], a[172:175], v[198:201], v[108:111]\n"
        "s_nop 0\n"
        "v_cvt_pk_bf16_f32 v224, v226, v227\n"
        "v_cvt_pk_bf16_f32 v225, v228, v229\n"
        "v_mfma_f32_16x16x32_bf16 v[108:111], a[176:179], v[182:185], v[108:111]\n"
        "buffer_store_dwordx3 v[238:240], v15, s[40:43], s90 offen offset:768 sc1 nt\n"
        "s_nop 0\n"
        "v_mfma_f32_16x16x32_bf16 v[234:237], a[188:191], v[182:185], 0\n"
        "s_nop 2\n"
        "v_mfma_f32_16x16x32_bf16 v[234:237], a[184:187], v[198:201], v[234:237]\n"
        "buffer_store_dwordx3 v[132:134], v14, s[40:43], s91 offen offset:192 sc1 nt\n"
        "v_cvt_pk_bf16_f32 v226, v108, v109\n"
        "v_cvt_pk_bf16_f32 v227, v110, v111\n"
        "v_mfma_f32_16x16x32_bf16 v[234:237], a[180:183], v[206:209], v[234:237]\n"
        "s_nop 0\n"
        "v_mfma_f32_16x16x32_bf16 v[230:233], a[192:195], v[206:209], 0\n"
        "buffer_store_dwordx3 v[144:146], v16, s[40:43], s91 offen offset:960 sc1 nt\n"
        "s_nop 2\n"
        "v_mfma_f32_16x16x32_bf16 v[230:233], a[196:199], v[198:201], v[230:233]\n"
        "s_nop 0\n"
        "v_cvt_pk_bf16_f32 v228, v234, v235\n"
        "v_cvt_pk_bf16_f32 v229, v236, v237\n"
        "v_mfma_f32_16x16x32_bf16 v[230:233], a[200:203], v[182:185], v[230:233]\n"
        "s_nop 1\n"
        "v_mfma_f32_16x16x32_bf16 v[108:111], a[212:215], v[182:185], 0\n"
        "s_nop 2\n"
        "v_mfma_f32_16x16x32_bf16 v[108:111], a[208:211], v[198:201], v[108:111]\n"
        "s_nop 0\n"
        "v_cvt_pk_bf16_f32 v234, v230, v231\n"
        "v_cvt_pk_bf16_f32 v235, v232, v233\n"
        "v_mfma_f32_16x16x32_bf16 v[108:111], a[204:207], v[206:209], v[108:111]\n"
        "s_nop 1\n"
        "v_mfma_f32_16x16x32_bf16 v[238:241], v[186:189], v[222:225], 0\n"
        "s_nop 2\n"
        "v_mfma_f32_16x16x32_bf16 v[238:241], v[210:213], v[226:229], v[238:241]\n"
        "s_nop 0\n"
        "v_cvt_pk_bf16_f32 v236, v108, v109\n"
        "v_cvt_pk_bf16_f32 v237, v110, v111\n"
        "s_nop 1\n"
        "v_mfma_f32_16x16x32_bf16 v[238:241], v[214:217], v[234:237], v[238:241]\n"
        "s_nop 1\n"
        "v_mfma_f32_16x16x32_bf16 v[108:111], v[182:185], v[234:237], 0\n"
        "s_nop 2\n"
        "v_mfma_f32_16x16x32_bf16 v[108:111], v[198:201], v[226:229], v[108:111]\n"
        "s_nop 0\n"
        "v_mul_f32 v238, v238, v168\n"
        "v_mul_f32 v239, v239, v169\n"
        "v_mfma_f32_16x16x32_bf16 v[108:111], v[206:209], v[222:225], v[108:111]\n"
        "s_nop 0\n"
        "v_mul_f32 v240, v240, v170\n"
        "v_mul_f32 v241, v241, v171\n"
        "v_mfma_f32_16x16x32_bf16 v[230:233], v[194:197], v[222:225], 0\n"
        "s_nop 0\n"
        "v_max3_f32 v250, v238, v239, v240\n"
        "v_mfma_f32_16x16x32_bf16 v[230:233], v[202:205], v[226:229], v[230:233]\n"
        "s_nop 0\n"
        "v_mul_f32 v108, v108, v176\n"
        "v_mul_f32 v109, v109, v177\n"
        "v_mfma_f32_16x16x32_bf16 v[230:233], v[218:221], v[234:237], v[230:233]\n"
        "v_mul_f32 v110, v110, v178\n"
        "v_mul_f32 v111, v111, v179\n"
        "v_mfma_f32_16x16x32_bf16 v[242:245], v[148:151], v[234:237], 0\n"
        "s_nop 0\n"
        "v_max3_f32 v250, v250, v241, v108\n"
        "v_max3_f32 v250, v250, v109, v110\n"
        "v_mfma_f32_16x16x32_bf16 v[242:245], v[112:115], v[226:229], v[242:245]\n"
        "s_nop 0\n"
        "v_mul_f32 v230, v230, v190\n"
        "v_mul_f32 v231, v231, v191\n"
        "v_mfma_f32_16x16x32_bf16 v[242:245], v[120:123], v[222:225], v[242:245]\n"
        "s_nop 0\n"
        "v_mul_f32 v232, v232, v192\n"
        "v_mul_f32 v233, v233, v193\n"
        "v_max3_f32 v250, v250, v111, v230\n"
        "v_max3_f32 v250, v250, v231, v232\n"
        "s_nop 2\n"
        "v_mul_f32 v242, v242, v253\n"
        "v_cndmask_b32_e64 v242, v255, v242, s[62:63]\n"
        "v_max3_f32 v250, v250, v233, v242\n"
        "v_mov_b32 v251, v250\n"
        "s_nop 1\n"
        "v_permlane32_swap_b32 v250, v251\n"
        "v_max_f32 v250, v250, v251\n"
        "v_mov_b32 v251, v250\n"
        "s_nop 1\n"
        "v_permlane16_swap_b32 v250, v251\n"
        "v_max_f32 v250, v250, v251\n"
        "v_mul_f32 v250, v250, v252\n"
        "v_fma_f32 v238, v238, v252, -v250\n"
        "v_exp_f32 v238, v238\n"
        "v_fma_f32 v239, v239, v252, -v250\n"
        "v_exp_f32 v239, v239\n"
        "s_nop 0\n"
        "v_add_f32 v251, v238, v239\n"
        "v_mul_f32 v238, v238, v168\n"
        "v_fma_f32 v242, v242, v252, -v250\n"
        "v_exp_f32 v242, v242\n"
        "v_fma_f32 v233, v233, v252, -v250\n"
        "v_exp_f32 v233, v233\n"
        "v_fma_f32 v232, v232, v252, -v250\n"
        "v_exp_f32 v232, v232\n"
        "v_fma_f32 v231, v231, v252, -v250\n"
        "v_exp_f32 v231, v231\n"
        "v_fma_f32 v230, v230, v252, -v250\n"
        "v_exp_f32 v230, v230\n"
        "v_fma_f32 v111, v111, v252, -v250\n"
        "v_exp_f32 v111, v111\n"
        "v_fma_f32 v110, v110, v252, -v250\n"
        "v_exp_f32 v110, v110\n"
        "v_fma_f32 v109, v109, v252, -v250\n"
        "v_exp_f32 v109, v109\n"
        "v_fma_f32 v108, v108, v252, -v250\n"
        "v_exp_f32 v108, v108\n"
        "v_fma_f32 v241, v241, v252, -v250\n"
        "v_exp_f32 v241, v241\n"
        "v_fma_f32 v240, v240, v252, -v250\n"
        "v_exp_f32 v240, v240\n"
        "s_nop 0\n"
        "v_add_f32 v251, v251, v240\n"
        "v_add_f32 v251, v251, v241\n"
        "v_add_f32 v251, v251, v108\n"
        "v_add_f32 v251, v251, v109\n"
        "v_add_f32 v251, v251, v110\n"
        "v_add_f32 v251, v251, v111\n"
        "v_add_f32 v251, v251, v230\n"
        "v_add_f32 v251, v251, v231\n"
        "v_add_f32 v251, v251, v232\n"
        "v_add_f32 v251, v251, v233\n"
        "v_add_f32 v251, v251, v242\n"
        "v_mov_b32 v252, v251\n"
        "s_nop 1\n"
        "v_permlane32_swap_b32 v251, v252\n"
        "v_add_f32 v251, v251, v252\n"
        "v_mov_b32 v252, v251\n"
        "s_nop 1\n"
        "v_permlane16_swap_b32 v251, v252\n"
        "v_add_f32 v251, v251, v252\n"
        "v_rcp_f32 v251, v251\n"
        "s_nop 0\n"
        "v_mul_f32 v238, v238, v251\n"
        "v_mul_f32 v239, v239, v169\n"
        "v_mul_f32 v239, v239, v251\n"
        "v_cvt_pk_bf16_f32 v226, v238, v239\n"
        "v_mul_f32 v240, v240, v170\n"
        "v_mul_f32 v240, v240, v251\n"
        "v_mul_f32 v241, v241, v171\n"
        "v_mul_f32 v241, v241, v251\n"
        "v_cvt_pk_bf16_f32 v227, v240, v241\n"
        "v_mul_f32 v108, v108, v176\n"
        "v_mul_f32 v108, v108, v251\n"
        "v_mul_f32 v109, v109, v177\n"
        "v_mul_f32 v109, v109, v251\n"
        "v_cvt_pk_bf16_f32 v228, v108, v109\n"
        "v_mul_f32 v110, v110, v178\n"
        "v_mul_f32 v110, v110, v251\n"
        "v_mul_f32 v111, v111, v179\n"
        "v_mul_f32 v111, v111, v251\n"
        "v_cvt_pk_bf16_f32 v229, v110, v111\n"
        "s_nop 1\n"
        "v_mfma_f32_16x16x32_bf16 v[108:111], v[226:229], v[128:131], 0\n"
        "v_mul_f32 v230, v230, v190\n"
        "v_mul_f32 v231, v231, v191\n"
        "v_mul_f32 v230, v230, v251\n"
        "v_mul_f32 v231, v231, v251\n"
        "v_cvt_pk_bf16_f32 v144, v230, v231\n"
        "v_mul_f32 v232, v232, v192\n"
        "v_mul_f32 v232, v232, v251\n"
        "v_mul_f32 v233, v233, v193\n"
        "v_mul_f32 v233, v233, v251\n"
        "v_cvt_pk_bf16_f32 v145, v232, v233\n"
        "v_mul_f32 v242, v242, v253\n"
        "v_mul_f32 v242, v242, v251\n"
        "v_cvt_pk_bf16_f32 v146, v242, 0\n"
        "s_nop 1\n"
        "v_mfma_f32_16x16x32_bf16 v[108:111], v[144:147], v[136:139], v[108:111]\n"
        "v_mfma_f32_16x16x32_bf16 v[108:111], v[206:209], v[88:91], v[108:111]\n"
        "v_mfma_f32_16x16x32_bf16 v[230:233], v[206:209], v[48:51], 0\n"
        "v_mfma_f32_16x16x32_bf16 v[230:233], v[144:147], v[40:43], v[230:233]\n"
        "v_mfma_f32_16x16x32_bf16 v[230:233], v[226:229], v[152:155], v[230:233]\n"
        "s_add_u32 s90, s89, 196224\n"
        "s_nop 0\n"
        "v_mfma_f32_16x16x32_bf16 v[222:225], v[226:229], v[160:163], 0\n"
        "s_nop 2\n"
        "v_mfma_f32_16x16x32_bf16 v[222:225], v[144:147], v[140:143], v[222:225]\n"
        "s_nop 0\n"
        "v_cvt_pk_bf16_f32 v238, v108, v230\n"
        "v_cvt_pk_bf16_f32 v234, v109, v231\n"
        "v_mfma_f32_16x16x32_bf16 v[222:225], v[198:201], v[88:91], v[222:225]\n"
        "v_cvt_pk_bf16_f32 v132, v110, v232\n"
        "v_cvt_pk_bf16_f32 v124, v111, v233\n"
        "v_mfma_f32_16x16x32_bf16 v[230:233], v[198:201], v[48:51], 0\n"
        "s_add_u32 s91, s89, 294336\n"
        "v_mfma_f32_16x16x32_bf16 v[230:233], v[144:147], v[116:119], v[230:233]\n"
        "v_mfma_f32_16x16x32_bf16 v[230:233], v[226:229], v[156:159], v[230:233]\n"
        "s_nop 1\n"
        "v_mfma_f32_16x16x32_bf16 v[108:111], v[226:229], v[164:167], 0\n"
        "s_nop 2\n"
        "v_mfma_f32_16x16x32_bf16 v[108:111], v[144:147], v[104:107], v[108:111]\n"
        "s_nop 0\n"
        "v_cvt_pk_bf16_f32 v239, v222, v230\n"
        "v_cvt_pk_bf16_f32 v235, v223, v231\n"
        "v_mfma_f32_16x16x32_bf16 v[108:111], v[182:185], v[88:91], v[108:111]\n"
        "v_cvt_pk_bf16_f32 v133, v224, v232\n"
        "v_cvt_pk_bf16_f32 v125, v225, v233\n"
        "v_mfma_f32_16x16x32_bf16 v[222:225], v[182:185], v[48:51], 0\n"
        "v_mfma_f32_16x16x32_bf16 v[222:225], v[144:147], v[34:37], v[222:225]\n"
        "v_mfma_f32_16x16x32_bf16 v[222:225], v[226:229], v[172:175], v[222:225]\n"
        "s_nop 1\n"
        "v_mfma_f32_16x16x32_bf16 v[144:147], a[144:147], v[194:197], 0\n"
        "s_nop 2\n"
        "v_mfma_f32_16x16x32_bf16 v[144:147], a[148:151], v[202:205], v[144:147]\n"
        "s_nop 0\n"
        "v_cvt_pk_bf16_f32 v240, v108, v222\n"
        "v_cvt_pk_bf16_f32 v236, v109, v223\n"
        "v_mfma_f32_16x16x32_bf16 v[144:147], a[152:155], v[218:221], v[144:147]\n"
        "s_nop 0\n"
        "v_cvt_pk_bf16_f32 v134, v110, v224\n"
        "v_cvt_pk_bf16_f32 v126, v111, v225\n"
        "v_mfma_f32_16x16x32_bf16 v[222:225], a[164:167], v[218:221], 0\n"
        "s_nop 1\n"
        "v_mfma_f32_16x16x32_bf16 v[222:225], a[160:163], v[202:205], v[222:225]\n"
        "s_nop 0\n"
        "v_cvt_pk_bf16_f32 v226, v144, v145\n"
        "v_cvt_pk_bf16_f32 v227, v146, v147\n"
        "v_mfma_f32_16x16x32_bf16 v[222:225], a[156:159], v[194:197], v[222:225]\n"
        "s_nop 0\n"
        "v_mfma_f32_16x16x32_bf16 v[108:111], a[168:171], v[194:197], 0\n"
        "buffer_store_dwordx3 v[238:240], v14, s[40:43], s90 offen offset:384 sc1 nt\n"
        "s_nop 2\n"
        "v_mfma_f32_16x16x32_bf16 v[108:111], a[172:175], v[202:205], v[108:111]\n"
        "s_nop 0\n"
        "v_cvt_pk_bf16_f32 v228, v222, v223\n"
        "v_cvt_pk_bf16_f32 v229, v224, v225\n"
        "v_mfma_f32_16x16x32_bf16 v[108:111], a[176:179], v[218:221], v[108:111]\n"
        "buffer_store_dwordx3 v[234:236], v17, s[40:43], s90 offen offset:1152 sc1 nt\n"
        "s_add_u32 s90, s89, 392448\n"
        "v_mfma_f32_16x16x32_bf16 v[222:225], a[188:191], v[218:221], 0\n"
        "s_nop 2\n"
        "v_mfma_f32_16x16x32_bf16 v[222:225], a[184:187], v[202:205], v[222:225]\n"
        "buffer_store_dwordx3 v[132:134], v14, s[40:43], s91 offen offset:576 sc1 nt\n"
        "v_cvt_pk_bf16_f32 v144, v108, v109\n"
        "v_cvt_pk_bf16_f32 v145, v110, v111\n"
        "v_mfma_f32_16x16x32_bf16 v[222:225], a[180:183], v[194:197], v[222:225]\n"
        "s_nop 0\n"
        "v_mfma_f32_16x16x32_bf16 v[108:111], a[192:195], v[194:197], 0\n"
        "buffer_store_dwordx3 v[124:126], v14, s[40:43], s90 offen sc1 nt\n"
        "s_nop 2\n"
        "v_mfma_f32_16x16x32_bf16 v[108:111], a[196:199], v[202:205], v[108:111]\n"
        "s_nop 0\n"
        "v_cvt_pk_bf16_f32 v146, v222, v223\n"
        "v_cvt_pk_bf16_f32 v147, v224, v225\n"
        "v_mfma_f32_16x16x32_bf16 v[108:111], a[200:203], v[218:221], v[108:111]\n"
        "s_nop 1\n"
        "v_mfma_f32_16x16x32_bf16 v[222:225], a[212:215], v[218:221], 0\n"
        "s_nop 2\n"
        "v_mfma_f32_16x16x32_bf16 v[222:225], a[208:211], v[202:205], v[222:225]\n"
        "s_nop 0\n"
        "v_cvt_pk_bf16_f32 v230, v108, v109\n"
        "v_cvt_pk_bf16_f32 v231, v110, v111\n"
        "v_mfma_f32_16x16x32_bf16 v[222:225], a[204:207], v[194:197], v[222:225]\n"
        "s_nop 1\n"
        "v_mfma_f32_16x16x32_bf16 v[108:111], v[186:189], v[226:229], 0\n"
        "s_nop 2\n"
        "v_mfma_f32_16x16x32_bf16 v[108:111], v[210:213], v[144:147], v[108:111]\n"
        "s_nop 0\n"
        "v_cvt_pk_bf16_f32 v232, v222, v223\n"
        "v_cvt_pk_bf16_f32 v233, v224, v225\n"
        "s_nop 1\n"
        "v_mfma_f32_16x16x32_bf16 v[108:111], v[214:217], v[230:233], v[108:111]\n"
        "s_nop 1\n"
        "v_mfma_f32_16x16x32_bf16 v[234:237], v[182:185], v[230:233], 0\n"
        "s_nop 2\n"
        "v_mfma_f32_16x16x32_bf16 v[234:237], v[198:201], v[144:147], v[234:237]\n"
        "s_nop 0\n"
        "v_mul_f32 v108, v108, v168\n"
        "v_mul_f32 v109, v109, v169\n"
        "v_mfma_f32_16x16x32_bf16 v[234:237], v[206:209], v[226:229], v[234:237]\n"
        "s_nop 0\n"
        "v_mul_f32 v110, v110, v170\n"
        "v_mul_f32 v111, v111, v171\n"
        "v_mfma_f32_16x16x32_bf16 v[238:241], v[194:197], v[226:229], 0\n"
        "s_nop 0\n"
        "v_max3_f32 v251, v108, v109, v110\n"
        "v_mfma_f32_16x16x32_bf16 v[238:241], v[202:205], v[144:147], v[238:241]\n"
        "s_nop 0\n"
        "v_mul_f32 v234, v234, v176\n"
        "v_mul_f32 v235, v235, v177\n"
        "v_mfma_f32_16x16x32_bf16 v[238:241], v[218:221], v[230:233], v[238:241]\n"
        "v_mul_f32 v236, v236, v178\n"
        "v_mul_f32 v237, v237, v179\n"
        "v_mfma_f32_16x16x32_bf16 v[222:225], v[148:151], v[230:233], 0\n"
        "s_nop 0\n"
        "v_max3_f32 v251, v251, v111, v234\n"
        "v_max3_f32 v251, v251, v235, v236\n"
        "v_mfma_f32_16x16x32_bf16 v[222:225], v[112:115], v[144:147], v[222:225]\n"
        "s_nop 0\n"
        "v_mul_f32 v238, v238, v190\n"
        "v_mul_f32 v239, v239, v191\n"
        "v_mfma_f32_16x16x32_bf16 v[222:225], v[120:123], v[226:229], v[222:225]\n"
        "s_nop 0\n"
        "v_mul_f32 v240, v240, v192\n"
        "v_mul_f32 v241, v241, v193\n"
        "v_max3_f32 v251, v251, v237, v238\n"
        "v_max3_f32 v251, v251, v239, v240\n"
        "s_nop 2\n"
        "v_mul_f32 v222, v222, v253\n"
        "v_cndmask_b32_e64 v222, v255, v222, s[62:63]\n"
        "v_max3_f32 v251, v251, v241, v222\n"
        "v_mov_b32 v252, v251\n"
        "s_nop 1\n"
        "v_permlane32_swap_b32 v251, v252\n"
        "v_max_f32 v251, v251, v252\n"
        "v_mov_b32 v252, v251\n"
        "s_nop 1\n"
        "v_permlane16_swap_b32 v251, v252\n"
        "v_max_f32 v251, v251, v252\n"
        "v_mul_f32 v251, v251, v254\n"
        "v_fma_f32 v108, v108, v254, -v251\n"
        "v_exp_f32 v108, v108\n"
        "v_fma_f32 v109, v109, v254, -v251\n"
        "v_exp_f32 v109, v109\n"
        "s_nop 0\n"
        "v_add_f32 v252, v108, v109\n"
        "v_mul_f32 v108, v108, v168\n"
        "v_fma_f32 v222, v222, v254, -v251\n"
        "v_exp_f32 v222, v222\n"
        "v_fma_f32 v241, v241, v254, -v251\n"
        "v_exp_f32 v241, v241\n"
        "v_fma_f32 v240, v240, v254, -v251\n"
        "v_exp_f32 v240, v240\n"
        "v_fma_f32 v239, v239, v254, -v251\n"
        "v_exp_f32 v239, v239\n"
        "v_fma_f32 v238, v238, v254, -v251\n"
        "v_exp_f32 v238, v238\n"
        "v_fma_f32 v237, v237, v254, -v251\n"
        "v_exp_f32 v237, v237\n"
        "v_fma_f32 v236, v236, v254, -v251\n"
        "v_exp_f32 v236, v236\n"
        "v_fma_f32 v235, v235, v254, -v251\n"
        "v_exp_f32 v235, v235\n"
        "v_fma_f32 v234, v234, v254, -v251\n"
        "v_exp_f32 v234, v234\n"
        "v_fma_f32 v111, v111, v254, -v251\n"
        "v_exp_f32 v111, v111\n"
        "v_fma_f32 v110, v110, v254, -v251\n"
        "v_exp_f32 v110, v110\n"
        "s_nop 0\n"
        "v_add_f32 v252, v252, v110\n"
        "v_add_f32 v252, v252, v111\n"
        "v_add_f32 v252, v252, v234\n"
        "v_add_f32 v252, v252, v235\n"
        "v_add_f32 v252, v252, v236\n"
        "v_add_f32 v252, v252, v237\n"
        "v_add_f32 v252, v252, v238\n"
        "v_add_f32 v252, v252, v239\n"
        "v_add_f32 v252, v252, v240\n"
        "v_add_f32 v252, v252, v241\n"
        "v_add_f32 v252, v252, v222\n"
        "v_mov_b32 v254, v252\n"
        "s_nop 1\n"
        "v_permlane32_swap_b32 v252, v254\n"
        "v_add_f32 v252, v252, v254\n"
        "v_mov_b32 v254, v252\n"
        "s_nop 1\n"
        "v_permlane16_swap_b32 v252, v254\n"
        "v_add_f32 v252, v252, v254\n"
        "v_rcp_f32 v252, v252\n"
        "s_nop 0\n"
        "v_mul_f32 v108, v108, v252\n"
        "v_mul_f32 v109, v109, v169\n"
        "v_mul_f32 v109, v109, v252\n"
        "v_cvt_pk_bf16_f32 v226, v108, v109\n"
        "v_mul_f32 v110, v110, v170\n"
        "v_mul_f32 v110, v110, v252\n"
        "v_mul_f32 v111, v111, v171\n"
        "v_mul_f32 v111, v111, v252\n"
        "v_cvt_pk_bf16_f32 v227, v110, v111\n"
        "v_mul_f32 v234, v234, v176\n"
        "v_mul_f32 v234, v234, v252\n"
        "v_mul_f32 v235, v235, v177\n"
        "v_mul_f32 v235, v235, v252\n"
        "v_cvt_pk_bf16_f32 v228, v234, v235\n"
        "v_mul_f32 v236, v236, v178\n"
        "v_mul_f32 v236, v236, v252\n"
        "v_mul_f32 v237, v237, v179\n"
        "v_mul_f32 v237, v237, v252\n"
        "v_cvt_pk_bf16_f32 v229, v236, v237\n"
        "s_nop 1\n"
        "v_mfma_f32_16x16x32_bf16 v[108:111], v[226:229], v[128:131], 0\n"
        "v_mul_f32 v238, v238, v190\n"
        "v_mul_f32 v239, v239, v191\n"
        "v_mul_f32 v238, v238, v252\n"
        "v_mul_f32 v239, v239, v252\n"
        "v_cvt_pk_bf16_f32 v132, v238, v239\n"
        "v_mul_f32 v240, v240, v192\n"
        "v_mul_f32 v240, v240, v252\n"
        "v_mul_f32 v241, v241, v193\n"
        "v_mul_f32 v241, v241, v252\n"
        "v_cvt_pk_bf16_f32 v133, v240, v241\n"
        "v_mul_f32 v222, v222, v253\n"
        "v_mul_f32 v222, v222, v252\n"
        "v_cvt_pk_bf16_f32 v134, v222, 0\n"
        "s_nop 1\n"
        "v_mfma_f32_16x16x32_bf16 v[108:111], v[132:135], v[136:139], v[108:111]\n"
        "v_mfma_f32_16x16x32_bf16 v[108:111], v[194:197], v[88:91], v[108:111]\n"
        "v_mfma_f32_16x16x32_bf16 v[222:225], v[194:197], v[48:51], 0\n"
        "v_mfma_f32_16x16x32_bf16 v[222:225], v[132:135], v[40:43], v[222:225]\n"
        "v_mfma_f32_16x16x32_bf16 v[222:225], v[226:229], v[152:155], v[222:225]\n"
        "s_nop 1\n"
        "v_mfma_f32_16x16x32_bf16 v[144:147], v[226:229], v[160:163], 0\n"
        "s_nop 2\n"
        "v_mfma_f32_16x16x32_bf16 v[144:147], v[132:135], v[140:143], v[144:147]\n"
        "s_add_u32 s91, s89, 490560\n"
        "v_cvt_pk_bf16_f32 v238, v108, v222\n"
        "v_cvt_pk_bf16_f32 v124, v109, v223\n"
        "v_mfma_f32_16x16x32_bf16 v[144:147], v[202:205], v[88:91], v[144:147]\n"
        "v_cvt_pk_bf16_f32 v230, v110, v224\n"
        "v_cvt_pk_bf16_f32 v234, v111, v225\n"
        "v_mfma_f32_16x16x32_bf16 v[108:111], v[202:205], v[48:51], 0\n"
        "v_mfma_f32_16x16x32_bf16 v[108:111], v[132:135], v[116:119], v[108:111]\n"
        "v_mfma_f32_16x16x32_bf16 v[108:111], v[226:229], v[156:159], v[108:111]\n"
        "s_nop 1\n"
        "v_mfma_f32_16x16x32_bf16 v[222:225], v[226:229], v[164:167], 0\n"
        "s_nop 2\n"
        "v_mfma_f32_16x16x32_bf16 v[222:225], v[132:135], v[104:107], v[222:225]\n"
        "s_nop 0\n"
        "v_cvt_pk_bf16_f32 v239, v144, v108\n"
        "v_cvt_pk_bf16_f32 v125, v145, v109\n"
        "v_mfma_f32_16x16x32_bf16 v[222:225], v[218:221], v[88:91], v[222:225]\n"
        "v_cvt_pk_bf16_f32 v231, v146, v110\n"
        "v_cvt_pk_bf16_f32 v235, v147, v111\n"
        "v_mfma_f32_16x16x32_bf16 v[108:111], v[218:221], v[48:51], 0\n"
        "v_mfma_f32_16x16x32_bf16 v[108:111], v[132:135], v[34:37], v[108:111]\n"
        "v_mfma_f32_16x16x32_bf16 v[108:111], v[226:229], v[172:175], v[108:111]\n"
        "s_nop 1\n"
        "v_mfma_f32_16x16x32_bf16 v[144:147], a[144:147], v[120:123], 0\n"
        "s_nop 2\n"
        "v_mfma_f32_16x16x32_bf16 v[144:147], a[148:151], v[112:115], v[144:147]\n"
        "s_nop 0\n"
        "v_cvt_pk_bf16_f32 v240, v222, v108\n"
        "v_cvt_pk_bf16_f32 v126, v223, v109\n"
        "v_mfma_f32_16x16x32_bf16 v[144:147], a[152:155], v[148:151], v[144:147]\n"
        "s_nop 0\n"
        "v_cvt_pk_bf16_f32 v232, v224, v110\n"
        "v_cvt_pk_bf16_f32 v236, v225, v111\n"
        "v_mfma_f32_16x16x32_bf16 v[132:135], a[164:167], v[148:151], 0\n"
        "s_nop 1\n"
        "v_mfma_f32_16x16x32_bf16 v[132:135], a[160:163], v[112:115], v[132:135]\n"
        "s_nop 0\n"
        "v_cvt_pk_bf16_f32 v108, v144, v145\n"
        "v_cvt_pk_bf16_f32 v109, v146, v147\n"
        "v_mfma_f32_16x16x32_bf16 v[132:135], a[156:159], v[120:123], v[132:135]\n"
        "s_nop 0\n"
        "v_mfma_f32_16x16x32_bf16 v[222:225], a[168:171], v[120:123], 0\n"
        "buffer_store_dwordx3 v[238:240], v15, s[40:43], s90 offen offset:768 sc1 nt\n"
        "s_add_u32 s90, s89, 588672\n"
        "s_nop 1\n"
        "v_mfma_f32_16x16x32_bf16 v[222:225], a[172:175], v[112:115], v[222:225]\n"
        "s_nop 0\n"
        "v_cvt_pk_bf16_f32 v110, v132, v133\n"
        "v_cvt_pk_bf16_f32 v111, v134, v135\n"
        "v_mfma_f32_16x16x32_bf16 v[222:225], a[176:179], v[148:151], v[222:225]\n"
        "buffer_store_dwordx3 v[124:126], v14, s[40:43], s91 offen offset:192 sc1 nt\n"
        "s_nop 0\n"
        "v_mfma_f32_16x16x32_bf16 v[132:135], a[188:191], v[148:151], 0\n"
        "s_nop 2\n"
        "v_mfma_f32_16x16x32_bf16 v[132:135], a[184:187], v[112:115], v[132:135]\n"
        "buffer_store_dwordx3 v[230:232], v16, s[40:43], s91 offen offset:960 sc1 nt\n"
        "v_cvt_pk_bf16_f32 v144, v222, v223\n"
        "v_cvt_pk_bf16_f32 v145, v224, v225\n"
        "v_mfma_f32_16x16x32_bf16 v[132:135], a[180:183], v[120:123], v[132:135]\n"
        "s_nop 0\n"
        "v_mfma_f32_16x16x32_bf16 v[222:225], a[192:195], v[120:123], 0\n"
        "buffer_store_dwordx3 v[234:236], v14, s[40:43], s90 offen offset:384 sc1 nt\n"
        "s_nop 2\n"
        "v_mfma_f32_16x16x32_bf16 v[222:225], a[196:199], v[112:115], v[222:225]\n"
        "s_nop 0\n"
        "v_cvt_pk_bf16_f32 v146, v132, v133\n"
        "v_cvt_pk_bf16_f32 v147, v134, v135\n"
        "v_mfma_f32_16x16x32_bf16 v[222:225], a[200:203], v[148:151], v[222:225]\n"
        "s_nop 1\n"
        "v_mfma_f32_16x16x32_bf16 v[132:135], a[212:215], v[148:151], 0\n"
        "s_nop 2\n"
        "v_mfma_f32_16x16x32_bf16 v[132:135], a[208:211], v[112:115], v[132:135]\n"
        "s_nop 0\n"
        "v_cvt_pk_bf16_f32 v226, v222, v223\n"
        "v_cvt_pk_bf16_f32 v227, v224, v225\n"
        "v_mfma_f32_16x16x32_bf16 v[132:135], a[204:207], v[120:123], v[132:135]\n"
        "s_nop 1\n"
        "v_mfma_f32_16x16x32_bf16 v[222:225], v[186:189], v[108:111], 0\n"
        "s_nop 2\n"
        "v_mfma_f32_16x16x32_bf16 v[222:225], v[210:213], v[144:147], v[222:225]\n"
        "s_nop 0\n"
        "v_cvt_pk_bf16_f32 v228, v132, v133\n"
        "v_cvt_pk_bf16_f32 v229, v134, v135\n"
        "s_nop 1\n"
        "v_mfma_f32_16x16x32_bf16 v[222:225], v[214:217], v[226:229], v[222:225]\n"
        "s_nop 1\n"
        "v_mfma_f32_16x16x32_bf16 v[186:189], v[182:185], v[226:229], 0\n"
        "s_nop 2\n"
        "v_mfma_f32_16x16x32_bf16 v[186:189], v[198:201], v[144:147], v[186:189]\n"
        "s_nop 0\n"
        "v_mul_f32 v222, v222, v168\n"
        "v_mul_f32 v223, v223, v169\n"
        "v_mfma_f32_16x16x32_bf16 v[186:189], v[206:209], v[108:111], v[186:189]\n"
        "s_nop 0\n"
        "v_mul_f32 v224, v224, v170\n"
        "v_mul_f32 v225, v225, v171\n"
        "v_mfma_f32_16x16x32_bf16 v[180:183], v[194:197], v[108:111], 0\n"
        "s_nop 0\n"
        "v_max3_f32 v254, v222, v223, v224\n"
        "v_mfma_f32_16x16x32_bf16 v[180:183], v[202:205], v[144:147], v[180:183]\n"
        "s_nop 0\n"
        "v_mul_f32 v186, v186, v176\n"
        "v_mul_f32 v187, v187, v177\n"
        "v_mfma_f32_16x16x32_bf16 v[180:183], v[218:221], v[226:229], v[180:183]\n"
        "v_mul_f32 v188, v188, v178\n"
        "v_mul_f32 v189, v189, v179\n"
        "v_mfma_f32_16x16x32_bf16 v[132:135], v[148:151], v[226:229], 0\n"
        "s_nop 0\n"
        "v_max3_f32 v254, v254, v225, v186\n"
        "v_max3_f32 v254, v254, v187, v188\n"
        "v_mfma_f32_16x16x32_bf16 v[132:135], v[112:115], v[144:147], v[132:135]\n"
        "s_nop 0\n"
        "v_mul_f32 v180, v180, v190\n"
        "v_mul_f32 v181, v181, v191\n"
        "v_mfma_f32_16x16x32_bf16 v[132:135], v[120:123], v[108:111], v[132:135]\n"
        "s_nop 0\n"
        "v_mul_f32 v182, v182, v192\n"
        "v_mul_f32 v183, v183, v193\n"
        "v_max3_f32 v254, v254, v189, v180\n"
        "v_max3_f32 v254, v254, v181, v182\n"
        "s_nop 2\n"
        "v_mul_f32 v132, v132, v253\n"
        "v_cndmask_b32_e64 v132, v255, v132, s[62:63]\n"
        "v_max3_f32 v254, v254, v183, v132\n"
        "v_mov_b32 v255, v254\n"
        "s_nop 1\n"
        "v_permlane32_swap_b32 v254, v255\n"
        "v_max_f32 v254, v254, v255\n"
        "v_mov_b32 v255, v254\n"
        "s_nop 1\n"
        "v_permlane16_swap_b32 v254, v255\n"
        "v_max_f32 v254, v254, v255\n"
        "v_mul_f32 v254, v254, v253\n"
        "v_fma_f32 v222, v222, v253, -v254\n"
        "v_exp_f32 v222, v222\n"
        "v_fma_f32 v223, v223, v253, -v254\n"
        "v_exp_f32 v223, v223\n"
        "s_nop 0\n"
        "v_add_f32 v255, v222, v223\n"
        "v_mul_f32 v222, v222, v168\n"
        "v_fma_f32 v132, v132, v253, -v254\n"
        "v_exp_f32 v132, v132\n"
        "v_fma_f32 v183, v183, v253, -v254\n"
        "v_exp_f32 v183, v183\n"
        "v_fma_f32 v182, v182, v253, -v254\n"
        "v_exp_f32 v182, v182\n"
        "v_fma_f32 v181, v181, v253, -v254\n"
        "v_exp_f32 v181, v181\n"
        "v_fma_f32 v180, v180, v253, -v254\n"
        "v_exp_f32 v180, v180\n"
        "v_fma_f32 v189, v189, v253, -v254\n"
        "v_exp_f32 v189, v189\n"
        "v_fma_f32 v188, v188, v253, -v254\n"
        "v_exp_f32 v188, v188\n"
        "v_fma_f32 v187, v187, v253, -v254\n"
        "v_exp_f32 v187, v187\n"
        "v_fma_f32 v186, v186, v253, -v254\n"
        "v_exp_f32 v186, v186\n"
        "v_fma_f32 v225, v225, v253, -v254\n"
        "v_exp_f32 v225, v225\n"
        "v_fma_f32 v224, v224, v253, -v254\n"
        "v_exp_f32 v224, v224\n"
        "s_nop 0\n"
        "v_add_f32 v255, v255, v224\n"
        "v_add_f32 v255, v255, v225\n"
        "v_add_f32 v255, v255, v186\n"
        "v_add_f32 v255, v255, v187\n"
        "v_add_f32 v255, v255, v188\n"
        "v_add_f32 v255, v255, v189\n"
        "v_add_f32 v255, v255, v180\n"
        "v_add_f32 v255, v255, v181\n"
        "v_add_f32 v255, v255, v182\n"
        "v_add_f32 v255, v255, v183\n"
        "v_add_f32 v255, v255, v132\n"
        "v_mov_b32 v254, v255\n"
        "s_nop 1\n"
        "v_permlane32_swap_b32 v255, v254\n"
        "v_add_f32 v255, v255, v254\n"
        "v_mov_b32 v254, v255\n"
        "s_nop 1\n"
        "v_permlane16_swap_b32 v255, v254\n"
        "v_add_f32 v255, v255, v254\n"
        "v_rcp_f32 v255, v255\n"
        "s_nop 0\n"
        "v_mul_f32 v222, v222, v255\n"
        "v_mul_f32 v223, v223, v169\n"
        "v_mul_f32 v223, v223, v255\n"
        "v_cvt_pk_bf16_f32 v144, v222, v223\n"
        "v_mul_f32 v224, v224, v170\n"
        "v_mul_f32 v224, v224, v255\n"
        "v_mul_f32 v225, v225, v171\n"
        "v_mul_f32 v225, v225, v255\n"
        "v_cvt_pk_bf16_f32 v145, v224, v225\n"
        "v_mul_f32 v186, v186, v176\n"
        "v_mul_f32 v186, v186, v255\n"
        "v_mul_f32 v187, v187, v177\n"
        "v_mul_f32 v187, v187, v255\n"
        "v_cvt_pk_bf16_f32 v146, v186, v187\n"
        "v_mul_f32 v188, v188, v178\n"
        "v_mul_f32 v188, v188, v255\n"
        "v_mul_f32 v189, v189, v179\n"
        "v_mul_f32 v189, v189, v255\n"
        "v_cvt_pk_bf16_f32 v147, v188, v189\n"
        "s_nop 1\n"
        "v_mfma_f32_16x16x32_bf16 v[108:111], v[144:147], v[128:131], 0\n"
        "v_mul_f32 v180, v180, v190\n"
        "v_mul_f32 v181, v181, v191\n"
        "v_mul_f32 v180, v180, v255\n"
        "v_mul_f32 v181, v181, v255\n"
        "v_cvt_pk_bf16_f32 v124, v180, v181\n"
        "v_mul_f32 v182, v182, v192\n"
        "v_mul_f32 v182, v182, v255\n"
        "v_mul_f32 v183, v183, v193\n"
        "v_mul_f32 v183, v183, v255\n"
        "v_cvt_pk_bf16_f32 v125, v182, v183\n"
        "v_mul_f32 v132, v132, v253\n"
        "v_mul_f32 v132, v132, v255\n"
        "v_cvt_pk_bf16_f32 v126, v132, 0\n"
        "s_nop 1\n"
        "v_mfma_f32_16x16x32_bf16 v[108:111], v[124:127], v[136:139], v[108:111]\n"
        "v_mfma_f32_16x16x32_bf16 v[108:111], v[120:123], v[88:91], v[108:111]\n"
        "v_mfma_f32_16x16x32_bf16 v[128:131], v[120:123], v[48:51], 0\n"
        "v_mfma_f32_16x16x32_bf16 v[128:131], v[124:127], v[40:43], v[128:131]\n"
        "v_mfma_f32_16x16x32_bf16 v[128:131], v[144:147], v[152:155], v[128:131]\n"
        "s_nop 1\n"
        "v_mfma_f32_16x16x32_bf16 v[120:123], v[144:147], v[160:163], 0\n"
        "s_nop 2\n"
        "v_mfma_f32_16x16x32_bf16 v[120:123], v[124:127], v[140:143], v[120:123]\n"
        "s_nop 0\n"
        "v_cvt_pk_bf16_f32 v130, v108, v128\n"
        "v_mfma_f32_16x16x32_bf16 v[120:123], v[112:115], v[88:91], v[120:123]\n"
        "v_mfma_f32_16x16x32_bf16 v[108:111], v[112:115], v[48:51], 0\n"
        "v_mfma_f32_16x16x32_bf16 v[108:111], v[124:127], v[116:119], v[108:111]\n"
        "v_mfma_f32_16x16x32_bf16 v[108:111], v[144:147], v[156:159], v[108:111]\n"
        "s_nop 1\n"
        "v_mfma_f32_16x16x32_bf16 v[40:43], v[144:147], v[164:167], 0\n"
        "s_nop 2\n"
        "v_mfma_f32_16x16x32_bf16 v[40:43], v[124:127], v[104:107], v[40:43]\n"
        "s_nop 0\n"
        "v_cvt_pk_bf16_f32 v131, v120, v108\n"
        "v_mfma_f32_16x16x32_bf16 v[40:43], v[148:151], v[88:91], v[40:43]\n"
        "v_mfma_f32_16x16x32_bf16 v[104:107], v[148:151], v[48:51], 0\n"
        "v_mfma_f32_16x16x32_bf16 v[104:107], v[124:127], v[34:37], v[104:107]\n"
        "s_add_u32 s46, s89, 590016\n"
        "v_mfma_f32_16x16x32_bf16 v[104:107], v[144:147], v[172:175], v[104:107]\n"
        "s_nop 2\n"
        "s_min_u32 s46, s46, 200540928\n"
        "s_nop 3\n"
        "v_cvt_pk_bf16_f32 v132, v40, v104\n"
        "buffer_store_dwordx3 v[130:132], v17, s[44:47], s90 offen offset:1152 sc1 nt\n"
        "s_nop 15\n"
        "s_cmp_lt_i32 s85, -1\n"
        "s_cbranch_scc1 SW0_%=\n"
        "s_mov_b32 s89, 0x80000000\n"
        "s_waitcnt vmcnt(0)\n"
        "ds_read_b128 v[126:129], v9\n"
        "ds_read_b128 v[132:135], v10\n"
        "ds_read_b128 v[138:141], v11\n"
        "ds_read_b128 v[106:109], v9 offset:3072\n"
        "ds_read_b128 v[112:115], v10 offset:3072\n"
        "ds_read_b128 v[118:121], v11 offset:3072\n"
        "ds_read_b128 v[146:149], v9 offset:6144\n"
        "ds_read_b128 v[152:155], v10 offset:6144\n"
        "ds_read_b128 v[158:161], v11 offset:6144\n"
        "s_waitcnt lgkmcnt(0)\n"
        "s_nop 7\n"
        "s_cmp_ge_i32 s85, 0\n"
        "s_cbranch_scc1 TOP_%=\n"
        "s_branch LAST_%=\n"
        "SP0_%=:\n"
        "v_cmp_ne_u32_sdwa s[24:25], v27, s29 src0_sel:BYTE_0 src1_sel:DWORD\n"
        "s_nop 4\n"
        "s_and_b64 s[24:25], s[24:25], s[76:77]\n"
        "s_cbranch_scc1 SW0_%=\n"
        "v_readfirstlane_b32 s50, v27\n"
        "s_nop 3\n"
        "s_branch FIN_%=\n"
        "SW0_%=:\n"
        "s_sleep 1\n"
        "ds_read_u16 v27, v19 offset:2560\n"
        "s_waitcnt lgkmcnt(0)\n"
        "s_sub_u32 s31, s31, 1\n"
        "s_cbranch_scc0 SP0_%=\n"
        "s_endpgm\n"
        "SP20_%=:\n"
        "v_cmp_ne_u32_sdwa s[24:25], v255, s30 src0_sel:BYTE_0 src1_sel:DWORD\n"
        "s_nop 4\n"
        "s_cmp_lg_u64 s[24:25], 0\n"
        "s_cbranch_scc1 SW20_%=\n"
        "s_branch BK20_%=\n"
        "SW20_%=:\n"
        "s_sleep 1\n"
        "ds_read_u16 v255, v19 offset:2048\n"
        "s_waitcnt lgkmcnt(0)\n"
        "s_sub_u32 s31, s31, 1\n"
        "s_cbranch_scc0 SP20_%=\n"
        "s_endpgm\n"
        "SP26_%=:\n"
        "v_cmp_ne_u32_sdwa s[24:25], v255, s29 src0_sel:BYTE_0 src1_sel:DWORD\n"
        "s_nop 4\n"
        "s_and_b64 s[24:25], s[24:25], s[76:77]\n"
        "s_cbranch_scc1 SW26_%=\n"
        "v_readfirstlane_b32 s50, v255\n"
        "s_nop 3\n"
        "s_branch BK26_%=\n"
        "SW26_%=:\n"
        "s_sleep 1\n"
        "ds_read_u16 v255, v19 offset:2560\n"
        "s_waitcnt lgkmcnt(0)\n"
        "s_sub_u32 s31, s31, 1\n"
        "s_cbranch_scc0 SP26_%=\n"
        "s_endpgm\n"
        :: "{v[0:15]}"(c0), "{v[16:23]}"(c1), "{v[24:27]}"(c2), "{v[186:193]}"(sp), "{v[30:45]}"(f0), "{v[46:49]}"(f1), "{s15}"(own4), "{s[16:19]}"(srdl), "{s[20:23]}"(sol), "{s[36:39]}"(srdx), "{s[40:43]}"(srdo), "{s[48:55]}"(si)
        : "v28", "v29", "v50", "v51", "v52", "v53", "v54", "v55", "v56", "v57", "v58", "v59", "v60", "v61", "v62", "v63", "v64", "v65", "v66", "v67", "v68", "v69", "v70", "v71", "v72", "v73", "v74", "v75", "v76", "v77", "v78", "v79", "v80", "v81", "v82", "v83", "v84", "v85", "v86", "v87", "v88", "v89", "v90", "v91", "v92", "v93", "v94", "v95", "v96", "v97", "v98", "v99", "v100", "v101", "v102", "v103", "v104", "v105", "v106", "v107", "v108", "v109", "v110", "v111", "v112", "v113", "v114", "v115", "v116", "v117", "v118", "v119", "v120", "v121", "v122", "v123", "v124", "v125", "v126", "v127", "v128", "v129", "v130", "v131", "v132", "v133", "v134", "v135", "v136", "v137", "v138", "v139", "v140", "v141", "v142", "v143", "v144", "v145", "v146", "v147", "v148", "v149", "v150", "v151", "v152", "v153", "v154", "v155", "v156", "v157", "v158", "v159", "v160", "v161", "v162", "v163", "v164", "v165", "v166", "v167", "v168", "v169", "v170", "v171", "v172", "v173", "v174", "v175", "v176", "v177", "v178", "v179", "v180", "v181", "v182", "v183", "v184", "v185", "v194", "v195", "v196", "v197", "v198", "v199", "v200", "v201", "v202", "v203", "v204", "v205", "v206", "v207", "v208", "v209", "v210", "v211", "v212", "v213", "v214", "v215", "v216", "v217", "v218", "v219", "v220", "v221", "v222", "v223", "v224", "v225", "v226", "v227", "v228", "v229", "v230", "v231", "v232", "v233", "v234", "v235", "v236", "v237", "v238", "v239", "v240", "v241", "v242", "v243", "v244", "v245", "v246", "v247", "v248", "v249", "v250", "v251", "v252", "v253", "v254", "v255", "a0", "a1", "a2", "a3", "a4", "a5", "a6", "a7", "a8", "a9", "a10", "a11", "a12", "a13", "a14", "a15", "a16", "a17", "a18", "a19", "a20", "a21", "a22", "a23", "a24", "a25", "a26", "a27", "a28", "a29", "a30", "a31", "a32", "a33", "a34", "a35", "a36", "a37", "a38", "a39", "a40", "a41", "a42", "a43", "a44", "a45", "a46", "a47", "a48", "a49", "a50", "a51", "a52", "a53", "a54", "a55", "a56", "a57", "a58", "a59", "a60", "a61", "a62", "a63", "a64", "a65", "a66", "a67", "a68", "a69", "a70", "a71", "a72", "a73", "a74", "a75", "a76", "a77", "a78", "a79", "a80", "a81", "a82", "a83", "a84", "a85", "a86", "a87", "a88", "a89", "a90", "a91", "a92", "a93", "a94", "a95", "a96", "a97", "a98", "a99", "a100", "a101", "a102", "a103", "a104", "a105", "a106", "a107", "a108", "a109", "a110", "a111", "a112", "a113", "a114", "a115", "a116", "a117", "a118", "a119", "a120", "a121", "a122", "a123", "a124", "a125", "a126", "a127", "a128", "a129", "a130", "a131", "a132", "a133", "a134", "a135", "a136", "a137", "a138", "a139", "a140", "a141", "a142", "a143", "a144", "a145", "a146", "a147", "a148", "a149", "a150", "a151", "a152", "a153", "a154", "a155", "a156", "a157", "a158", "a159", "a160", "a161", "a162", "a163", "a164", "a165", "a166", "a167", "a168", "a169", "a170", "a171", "a172", "a173", "a174", "a175", "a176", "a177", "a178", "a179", "a180", "a181", "a182", "a183", "a184", "a185", "a186", "a187", "a188", "a189", "a190", "a191", "a192", "a193", "a194", "a195", "a196", "a197", "a198", "a199", "a200", "a201", "a202", "a203", "a204", "a205", "a206", "a207", "a208", "a209", "a210", "a211", "a212", "a213", "a214", "a215", "a216", "a217", "a218", "a219", "a220", "a221", "a222", "a223", "a224", "a225", "a226", "a227", "a228", "a229", "a230", "a231", "a232", "a233", "a234", "a235", "a236", "a237", "a238", "a239", "a240", "a241", "a242", "a243", "a244", "a245", "a246", "a247", "a248", "a249", "a250", "a251", "a252", "a253", "a254", "a255", "s24", "s25", "s26", "s27", "s28", "s29", "s30", "s31", "s44", "s45", "s46", "s47", "s56", "s57", "s58", "s59", "s60", "s61", "s62", "s63", "s64", "s65", "s66", "s67", "s68", "s69", "s70", "s71", "s72", "s73", "s74", "s75", "s76", "s77", "s78", "s79", "s80", "s81", "s82", "s83", "s84", "s85", "s86", "s87", "s88", "s89", "s90", "s91", "s92", "s93", "s94", "s95", "vcc", "scc", "memory");
}

extern "C" void kernel_launch(void* const* d_in, const int* in_sizes, int n_in, void* d_out, int out_size, void* d_ws, size_t ws_size, hipStream_t stream) {
    (void)in_sizes; (void)n_in; (void)out_size; (void)ws_size;
    (void)hipFuncSetAttribute((const void*)swin_kernel, hipFuncAttributeMaxDynamicSharedMemorySize, LDS_BYTES);
    swin_kernel<<<GRID, 256, LDS_BYTES, stream>>>((const char*)d_in[0], (const char*)d_in[1], (const char*)d_in[2], (const char*)d_in[3], (const char*)d_in[4], (char*)d_out, (const char*)d_ws);
}
```
